```python
import torch

def _softmax(x, axis=-1):
    return torch.softmax(x, dim=axis)
def _gelu(x):
    return torch.nn.functional.gelu(x, approximate="tanh")
def _silu(x):
    return torch.nn.functional.silu(x)
def _sigmoid(x):
    return torch.sigmoid(x)
def _layernorm(x, axis=-1, eps=1e-5):
    m = x.mean(dim=axis, keepdim=True)
    v = x.var(dim=axis, keepdim=True, unbiased=False)
    return (x - m) / torch.sqrt(v + eps)
def _scatter_add(tgt, idx, src, axis=0):
    return tgt.clone().index_add_(axis, idx, src)
def _segment_sum(src, seg, n):
    out = torch.zeros((n,) + src.shape[1:], dtype=src.dtype)
    return out.index_add_(0, seg, src)
def _pad(x, pw):
    flat = tuple(v for pair in reversed(pw) for v in pair)
    return torch.nn.functional.pad(x, flat)

kernel_name = 'standalone__hybrid_graph_tf_graphgps__graph'

def setup_inputs():

    _w = (lambda a: (lambda t: t.to(torch.bfloat16) if t.is_floating_point() else t)(torch.as_tensor(a)))
    N, deg, D = 10000, 16, 256
    E = N * deg
    src = (lambda lo, hi, *s: torch.randint(lo, hi, s))(0, N, E).to(torch.int64)
    dst = (lambda lo, hi, *s: torch.randint(lo, hi, s))(0, N, E).to(torch.int64)
    deg_cnt = torch.bincount(dst, minlength=N).to(float) + 1.0
    sc = 1.0 / float(D) ** 0.5
    return {
        "x": _w((lambda *s: torch.randn(*s, dtype=torch.bfloat16) * 0.05)(N, D)),
        "src": _w(src),
        "dst": _w(dst),
        "deg_inv": _w((1.0 / deg_cnt).to(float)),
        "w_msg": _w((lambda *s: torch.randn(*s, dtype=torch.bfloat16) * 0.2)(D, D) * sc),
        "wq": _w((lambda *s: torch.randn(*s, dtype=torch.bfloat16) * 1.6)(D, D) * sc),
        "wk": _w((lambda *s: torch.randn(*s, dtype=torch.bfloat16) * 1.6)(D, D) * sc),
        "wv": _w((lambda *s: torch.randn(*s, dtype=torch.bfloat16) * 0.2)(D, D) * sc),
        "w_out": _w((lambda *s: torch.randn(*s, dtype=torch.bfloat16) * 0.2)(D, D) * sc),
    }

def reference(x, src, dst, deg_inv, w_msg, wq, wk, wv, w_out):
    N, D = x.shape[0], x.shape[1]
    h = _layernorm(x, axis=-1)
    m_e = torch.einsum('nd,de->ne', h, w_msg)
    h_local = _segment_sum(m_e[src], dst, N) * deg_inv[:, None]
    q = torch.einsum('nd,de->ne', h, wq)
    k = torch.einsum('nd,de->ne', h, wk)
    v = torch.einsum('nd,de->ne', h, wv)
    logits = torch.einsum('id,jd->ij', q, k) / (float(D) ** 0.5)
    attn = _softmax(logits, axis=-1)
    h_global = torch.einsum('ij,jd->id', attn, v)
    y = torch.einsum('nd,de->ne', h_local + h_global, w_out)
    return x + y
```

```cpp
#include <hip/hip_runtime.h>
#include <hip/hip_bf16.h>
#include <stdint.h>

typedef unsigned short u16;
typedef __attribute__((ext_vector_type(8))) short bf16x8;
typedef __attribute__((ext_vector_type(4))) short bf16x4;
typedef __attribute__((ext_vector_type(4))) float f32x4;
typedef __attribute__((ext_vector_type(2))) float f32x2;
typedef __attribute__((ext_vector_type(4))) unsigned u32x4;
typedef __attribute__((ext_vector_type(2))) unsigned u32x2;
#define DI __device__ __forceinline__

constexpr int NN = 10000, DD = 256, EE = 160000;
constexpr int NATT = 252, NSP = 4, SPN = 2560;
constexpr int GU = 5, GNB = 6; static_assert(GU * GNB <= 30, "list size");

constexpr unsigned WS_H = 0;
constexpr unsigned WS_AF = WS_H + 10240 * 512;
constexpr unsigned WS_WV = WS_AF + 131072;
constexpr unsigned WS_WM = WS_WV + 135168;
constexpr unsigned WS_XF = WS_WM + 135168;
constexpr unsigned WS_GF = WS_XF + 2304 * 128;
constexpr unsigned WS_HF = WS_GF + 2048;
constexpr unsigned WS_BF = WS_HF + 4096;
constexpr unsigned WS_PF = WS_BF + 32768;
constexpr unsigned WS_NF = WS_PF + 4096;
constexpr unsigned WS_WF = WS_NF + 16384;
constexpr unsigned WS_LF = WS_WF + 256;
constexpr unsigned WS_EP = WS_LF + 512;
constexpr unsigned WS_G = WS_EP + 256 + 1024;
constexpr unsigned WS_QP = WS_G + 10240 * 512;
constexpr unsigned WS_XB = WS_QP + 10240 * 512;
constexpr unsigned WS_LS = WS_XB + NATT * 101376;
constexpr unsigned WS_BL = WS_LS + 524288;
constexpr unsigned WS_DBG = WS_BL + 64 * 260 * 64;
constexpr unsigned WS_END = WS_DBG + 1024 * 128 + 8192;

DI float bf2f(unsigned x) { return __uint_as_float(x << 16); }
DI float bfhi(unsigned x) { return __uint_as_float(x & 0xffff0000u); }
DI unsigned pk2(float lo, float hi) {
  typedef __attribute__((ext_vector_type(2))) __bf16 bf2_t;
  const bf2_t r = __builtin_convertvector((f32x2){lo, hi}, bf2_t);
  return __builtin_bit_cast(unsigned, r);
}
typedef __attribute__((address_space(3))) bf16x4* lds_bf16x4_p;
DI bf16x4 tr_read(unsigned p) { return __builtin_amdgcn_ds_read_tr16_b64_v4i16((lds_bf16x4_p)p); }
DI bf16x8 as_bf(u32x4 v) { return __builtin_bit_cast(bf16x8, v); }
#define SB() __builtin_amdgcn_sched_barrier(0)
#define MFMA16(a, b, c) __builtin_amdgcn_mfma_f32_16x16x32_bf16(a, b, c, 0, 0, 0)

typedef __amdgpu_buffer_rsrc_t srd_t;
DI void drain() { asm volatile("s_waitcnt vmcnt(0)" ::: "memory"); }
DI void st_flag(srd_t w, unsigned off, unsigned tag) { __builtin_amdgcn_raw_buffer_store_b32(tag, w, off, 0, 16); }
DI void wait_flag(srd_t w, unsigned off, unsigned tag) {
#pragma unroll 1
  for (int it = 0; it < (1 << 22); ++it) {
    unsigned v;
    asm volatile("buffer_load_dword %0, %1, %2, 0 offen sc1\n s_waitcnt vmcnt(0)" : "=v"(v) : "v"(off), "s"(w) : "memory");
    if (__ballot(v != tag) == 0) break;
    __builtin_amdgcn_s_sleep(2);
    asm volatile("" ::: "memory");
  }
  asm volatile("" ::: "memory");
}

DI void prod_store(const f32x4 (&acc)[2][2], int mat, int ks, int J, int lane, srd_t wsrd) {
#pragma unroll
  for (int s = 0; s < 2; ++s) {
    u32x4 o = {pk2(acc[0][s][0], acc[0][s][1]), pk2(acc[0][s][2], acc[0][s][3]), pk2(acc[1][s][0], acc[1][s][1]), pk2(acc[1][s][2], acc[1][s][3])};
    if (ks == 7 && lane >= 48) o[3] &= 0xffffu;
    const int T = 2 * J + s;
    const int at = mat * 135168 + ks * 4096 + (T & 3) * 1024;
    __builtin_amdgcn_raw_buffer_store_b128(o, wsrd, WS_WV + (T >> 2) * 33792 + at + lane * 16, 0, 16);
  }
}
DI void corr(unsigned* dl, const unsigned (&lr)[16]) {
  float a0 = 0.f, a1 = 0.f;
#pragma unroll
  for (int r = 0; r < 16; ++r) { a0 += bf2f(lr[r] & 0xffffu); a1 += bfhi(lr[r]); }
  dl[0] = pk2(a0 * -0.0625f, a1 * -0.0625f);
  dl[128] = pk2((bf2f(lr[15] & 0xffffu) - 0.25f * a0) * (1.f / 3.f), (bfhi(lr[15]) - 0.25f * a1) * (1.f / 3.f));
}
DI void product_load(char* lds, int wid, int lane, int job, const u16* __restrict__ wv, const u16* __restrict__ wm, const u16* __restrict__ wo) {
  const unsigned ldsb = (unsigned)(size_t)lds;
  const int r = lane >> 3, sw = ((lane & 7) ^ (r & 6)) * 16 + wid * 128;
  const unsigned vp = (8 * (r >> 2) + (r & 3)) * 512 + sw, vn = r * 512 + sw;
  auto image = [&](int n, const u16* w, int row0, bool nat) {
#pragma unroll
    for (int i = 0; i < 8; ++i) {
      const int a = i & 3;
      asm volatile("s_mov_b32 m0, %2\n s_nop 0\n global_load_lds_dwordx4 %0, %1"
                   :: "v"(nat ? vn : vp), "s"(w + (row0 + (i >> 2) * 32 + (nat ? 8 * a : 16 * (a & 1) + 4 * (a >> 1))) * DD),
                      "s"(ldsb + n * 32768 + (i >> 2) * 16384 + wid * 4096 + a * 1024) : "m0", "memory");
    }
  };
  image(2, wv, (job >> 2) * 64, true);
  image(3, wm, (job >> 2) * 64, false);
#pragma unroll
  for (int k = 0; k < 8; ++k)
    asm volatile("s_mov_b32 m0, %2\n s_nop 0\n global_load_lds_dwordx4 %0, %1"
                 :: "v"((lane >> 3) * 512 + (lane & 7) * 16), "s"(wo + (k * 32 + wid * 8) * DD + (job & 3) * 64), "s"(ldsb + k * 4096 + wid * 1024) : "m0", "memory");
}
DI void product_w(char* lds, int wid, int lane, int job, srd_t wsrd) {
  const int m = lane & 15, g = lane >> 4;
  const int ks = 2 * (job >> 2) + (wid >> 1), J = 2 * (job & 3) + (wid & 1);
  const unsigned ldsb = (unsigned)(size_t)lds;
  unsigned* dl = reinterpret_cast<unsigned*>(lds + 0x20000);
  const bool hi = (m >> 3) != 0;
  const unsigned ra = (m >> 3) * 1024 + (m & 7) * 128 + (g ^ (m & 6)) * 16;
  auto frag = [&](int n, int tl, int t, int k) {
    return *reinterpret_cast<const bf16x8*>(lds + n * 32768 + tl * 16384 + (k >> 1) * 4096 + t * 2048 + (ra ^ ((k & 1) * 64)));
  };
  f32x4 av[2][2] = {}, am[2][2] = {}, ad[2] = {};
  const unsigned tb = ldsb + (8 * g + ((lane >> 2) & 3)) * 128 + (wid & 1) * 64 + (lane & 3) * 16;
  bf16x8 d0[8], d1[8], d2[8], rr[8][2], vf[8][2], mf[8][2];
#pragma unroll
  for (int kk = 0; kk < 9; ++kk) {
    if (kk < 8) {
      d0[kk] = *reinterpret_cast<const bf16x8*>(dl + 512 + 16 * kk + 4 * g); d1[kk] = *reinterpret_cast<const bf16x8*>(dl + 512 + (ks == 7 ? 128 : 0) + 16 * kk + 4 * g);
      d2[kk] = *reinterpret_cast<const bf16x8*>(dl + 768 + (ks == 7 && hi ? 128 : 0) + 16 * kk + 4 * g);
#pragma unroll
      for (int s = 0; s < 2; ++s) {
        rr[kk][s] = __builtin_shufflevector(tr_read(tb + kk * 4096 + s * 8), tr_read(tb + kk * 4096 + s * 8 + 512), 0, 1, 2, 3, 4, 5, 6, 7);
        vf[kk][s] = frag(2, wid >> 1, s, kk); mf[kk][s] = frag(3, wid >> 1, s, kk);
      }
    }
    SB();
    if (kk) {
      const int k = kk - 1;
#pragma unroll
      for (int s = 0; s < 2; ++s) {
        av[0][s] = MFMA16(d0[k], rr[k][s], av[0][s]); av[1][s] = MFMA16(d1[k], rr[k][s], av[1][s]); ad[s] = MFMA16(d2[k], rr[k][s], ad[s]);
#pragma unroll
        for (int t = 0; t < 2; ++t) { av[t][s] = MFMA16(vf[k][t], rr[k][s], av[t][s]); am[t][s] = MFMA16(mf[k][t], rr[k][s], am[t][s]); }
      }
    }
    SB();
  }
#pragma unroll
  for (int t = 0; t < 2; ++t)
#pragma unroll
    for (int s = 0; s < 2; ++s) am[t][s] += ad[s];
  prod_store(av, 0, ks, J, lane, wsrd);
  prod_store(am, 1, ks, J, lane, wsrd);
}

DI void helper(char* lds, int j, int wid, const int* __restrict__ src, const int* __restrict__ dst,
               const u16* __restrict__ wv, const u16* __restrict__ wm, const u16* __restrict__ wo, char* ws) {
  unsigned* img = reinterpret_cast<unsigned*>(lds);
  char* cntb = lds + 0x20000;
  int* cnt = reinterpret_cast<int*>(cntb);
  unsigned* ub = reinterpret_cast<unsigned*>(lds + 0x22c00);
  int* red = reinterpret_cast<int*>(ub + 256);
  u16* dg = reinterpret_cast<u16*>(lds + 0x23400);
  u16* tmp = reinterpret_cast<u16*>(lds + 0x25000) + wid * 512;
  int lane; asm volatile("v_mbcnt_lo_u32_b32 %0, -1, 0\n\tv_mbcnt_hi_u32_b32 %0, -1, %0" : "=v"(lane));
  const int tid = wid * 64 + lane, lo = j * SPN, nn_ = min(SPN, NN - lo);
  const srd_t wsrd = __builtin_amdgcn_make_buffer_rsrc(ws, 0, WS_END, 0x00020000);
  const unsigned tag = __builtin_amdgcn_raw_buffer_load_b32(wsrd, WS_EP, 0, 0) + 4u;
  {
    const unsigned at = WS_GF - 32768 + (j * 256 + tid) * 128;
    asm volatile("s_mov_b32 m0, 0x27c00\n s_nop 0\n buffer_load_dword %0, %1, 0 offen sc1 lds" :: "v"(1 ? (at < WS_EP ? at : 0x40000000u) : 0x40000000u), "s"(wsrd) : "m0", "memory");
  }
  for (int i = tid; i < 8192; i += 256) { const unsigned f = (i & 63) >= 60 ? 0u : ~0u; reinterpret_cast<u32x4*>(img)[i] = (u32x4){f, f, f, f}; }
  for (int i = tid; i < SPN + 64; i += 256) cnt[i] = 0;
  ub[tid] = 0u;
  if (tid < 8) red[tid] = 0;
  wait_flag(wsrd, WS_NF + j * 4096 + min(tid, 249) * 16, tag);
  __syncthreads();
  const unsigned dum = (SPN + lane) << 18;
  auto fetch = [&](int k) {
    return __builtin_amdgcn_raw_buffer_load_b128(wsrd, WS_BL + (k < 48 ? tid * 16 + (k & 7) * 4096 : (tid >> 2) * 128 + (tid & 3) * 16 + (k & 3) * 8192 + (k & 4) * 16), (j * 8 + (k >> 3)) * 33280, 16);
  };
  auto clean = [&](u32x4& e, int k) {
#pragma unroll
    for (int r = 0; r < 4; ++r) e[r] = (k < 48 ? (k & 7) == 7 && tid >= 208 : (k & 3) == 3 && tid >= 232) ? dum : min(e[r], dum);
  };
  auto slot = [&](unsigned e) { return reinterpret_cast<int*>(cntb + (e >> 16)); };
  constexpr int NL = 52;
  u32x4 ent[NL];
#pragma unroll
  for (int k = 0; k < NL; ++k) ent[k] = fetch(k);
  SB();
#pragma unroll
  for (int k = 0; k < NL; ++k) {
    clean(ent[k], k);
    int* p[4];
#pragma unroll
    for (int r = 0; r < 4; ++r) p[r] = slot(ent[k][r]);
    SB();
#pragma unroll
    for (int r = 0; r < 4; ++r) atomicAdd(p[r], 2);
    SB();
  }
  unsigned use = 0;
  const unsigned dum3 = (tid & 3) == 3 ? dum : 0;
#pragma unroll
  for (int k = 0; k < 4; ++k) use |= __ballot(ent[NL - 4 + k][3] < dum3) ? 1u << k : 0u;
  if (use)
#pragma unroll 1
    for (int k = NL; k < 64; ++k)
      if (use >> (k - NL) & 1) {
        u32x4 e = fetch(k); clean(e, k);
#pragma unroll
        for (int r = 0; r < 4; ++r) atomicAdd(slot(e[r]), 2);
        if (__ballot(e[3] < dum3)) { use |= 16u << (k - NL); if (k >= 60) red[7] = 1; }
      }
  __syncthreads();
  int inc = 0, nu[10], cn[10], cm = 0;
  auto units = [&](int c) { return max(1, (c + 6) >> 3); };
#pragma unroll
  for (int i = 0; i < 10; i += 2) { const u32x2 v = *reinterpret_cast<u32x2*>(cnt + 10 * tid + i); cn[i] = v[0]; cn[i + 1] = v[1]; }
  const int nup = units(cnt[max(10 * tid - 1, 0)]);
  const bool act = 10 * tid < nn_;
#pragma unroll
  for (int i = 0; i < 10; ++i) { cm = max(cm, cn[i]); nu[i] = act ? units(cn[i]) : 0; inc += nu[i]; }
  const int own = inc;
  inc += __builtin_amdgcn_update_dpp(0, inc, 0x111, 15, 15, true); inc += __builtin_amdgcn_update_dpp(0, inc, 0x112, 15, 15, true);
  inc += __builtin_amdgcn_update_dpp(0, inc, 0x114, 15, 15, true); inc += __builtin_amdgcn_update_dpp(0, inc, 0x118, 15, 15, true);
  inc += __builtin_amdgcn_update_dpp(0, inc, 0x142, 10, 15, false); inc += __builtin_amdgcn_update_dpp(0, inc, 0x143, 12, 15, false);
  if (lane == 63) red[wid] = inc;
  const bool srt = __ballot(cm > 64);
  __syncthreads();
  const u32x4 rd = *reinterpret_cast<u32x4*>(red);
  const float rs_ = 64.f / (float)(rd[0] + rd[1] + rd[2] + rd[3]);
  inc += (wid > 0 ? rd[0] : 0) + (wid > 1 ? rd[1] : 0) + (wid > 2 ? rd[2] : 0) - own;
  int wv_[10];
  {
    int o = inc, wp = tid ? min(127, (int)((float)(2 * o - nup) * rs_)) : 128;
#pragma unroll
    for (int i = 0; i < 10; ++i) {
      wv_[i] = min(127, (int)((float)(2 * o + nu[i]) * rs_));
      ub[act && wv_[i] != wp ? wv_[i] : 128 + lane] = (unsigned)o << 12 | (unsigned)(10 * tid + i);
      wp = wv_[i]; o += nu[i];
    }
  }
  __syncthreads();
  {
    int u0[10], top = 0;
#pragma unroll
    for (int i = 0; i < 10; ++i) u0[i] = ub[wv_[i]] >> 12;
#pragma unroll
    for (int i = 0; i < 10; ++i) {
      const int lu = inc - u0[i], e = lu + nu[i] - 1 + GU, q = min(e / (4 * GU), GNB);
      u0[i] = wv_[i] * 1024 + lu * 8; top = max(top, q);
      atomicOr(img + wv_[i] * 256 + 244 + q, act ? 1u << (e - q * 4 * GU) : 0u);
      inc += nu[i];
    }
#pragma unroll
    for (int i = 0; i < 10; i += 2) *reinterpret_cast<u32x2*>(cnt + 10 * tid + i) = (u32x2){(unsigned)u0[i], (unsigned)u0[i + 1]};
    if (tid < 64) cnt[SPN + tid] = 0x30000;
    if (act && top == GNB) red[7] = 1;
  }
  __syncthreads();
  const bool big_ = red[7];
  if (tid < 128) {
    unsigned nb = 0;
#pragma unroll
    for (int q = 0; q < GNB; ++q) nb = img[tid * 256 + 244 + q] ? q + 1 : nb;
    *reinterpret_cast<u32x4*>(img + tid * 256 + 240) = (u32x4){big_ ? 0u : nb, WS_G + (lo + (ub[tid] & 4095u)) * 512, big_ ? 0x40000000u : WS_GF + j * 512 + tid * 4, 0u};
  }
  if (!big_) {
    const unsigned dmp = 0x27000 + lane * 4;
    typedef __attribute__((address_space(3))) u16* lds_u16_p;
    unsigned p[2][8];
#pragma unroll
    for (int k = 0; k < NL + 2; k += 2) {
      if (k < NL) {
        int* q[8];
#pragma unroll
        for (int r = 0; r < 8; ++r) q[r] = slot(ent[k + (r >> 2)][r & 3]);
        SB();
#pragma unroll
        for (int r = 0; r < 8; ++r) p[k >> 1 & 1][r] = atomicAdd(q[r], 2);
        SB();
      }
      if (k) {
#pragma unroll
        for (int r = 0; r < 8; ++r) p[~k >> 1 & 1][r] = min(p[~k >> 1 & 1][r], dmp);
        SB();
#pragma unroll
        for (int r = 0; r < 8; ++r) *(lds_u16_p)p[~k >> 1 & 1][r] = ent[k - 2 + (r >> 2)][r & 3];
        SB();
      }
    }
    if (use)
#pragma unroll 1
      for (int k = NL; k < 64; ++k)
        if (use >> (k - NL) & 1) {
          u32x4 e = fetch(k); clean(e, k);
#pragma unroll
          for (int r = 0; r < 4; ++r) *(lds_u16_p)min((unsigned)atomicAdd(slot(e[r]), 2), dmp) = e[r];
        }
    __syncthreads();
    if (srt) {
#pragma unroll
      for (int i = 0; i < 10; ++i) dg[10 * tid + i] = cn[i] >> 1;
      for (unsigned long long big = __ballot(cm > 64); big; big &= big - 1)
      for (int n = 10 * (wid * 64 + __builtin_ctzll(big)), n1 = n + 10; n < n1; ++n) {
        const int d = __builtin_amdgcn_readfirstlane(dg[n]);
        if (d <= 32) continue;
        u16* f = reinterpret_cast<u16*>(lds + cnt[n]) - d;
        for (int i = lane; i < d + 8; i += 64) tmp[i] = i < d ? f[i] : 0xffff;
        __builtin_amdgcn_wave_barrier();
        for (int i = lane; i < d; i += 64) {
          const unsigned key = tmp[i] << 9 | i; int p_ = 0;
          for (int q = 0; q < d; q += 8) {
            const u32x4 v = *reinterpret_cast<u32x4*>(tmp + q);
#pragma unroll
            for (int e = 0; e < 4; ++e) p_ += (((v[e] & 0xffffu) << 9 | (q + 2 * e)) < key) + (((v[e] >> 16) << 9 | (q + 2 * e + 1)) < key);
          }
          f[p_] = key >> 9;
        }
        __builtin_amdgcn_wave_barrier();
      }
    }
  }
  __syncthreads();
#pragma unroll 8
  for (int k = 0; k < 32; ++k)
    __builtin_amdgcn_raw_buffer_store_b128(reinterpret_cast<u32x4*>(img)[k * 256 + tid], wsrd, WS_LS + j * 131072 + (k * 256 + tid) * 16, 0, 16);
  unsigned lr[16];
#pragma unroll
  for (int r = 0; r < 16; ++r) lr[r] = reinterpret_cast<const unsigned*>((wid >= 2 ? wm : wv) + (240 + r) * DD)[tid & 127];
  wait_flag(wsrd, WS_HF + min(tid, NATT - 1) * 16, tag);
  drain();
  __syncthreads();
  if (tid == 0) st_flag(wsrd, WS_LF + j * 128, tag);
  corr(reinterpret_cast<unsigned*>(lds + 0x20000) + 512 + (wid >> 1) * 256 + (tid & 127), lr);
#pragma unroll 1
  for (int job = 4 * j; job < 4 * j + 4; ++job) {
    product_load(lds, wid, lane, job, wv, wm, wo);
    drain();
    __syncthreads();
    product_w(lds, wid, lane, job, wsrd);
    __syncthreads();
  }
  drain();
  __syncthreads();
  if (tid == 0) st_flag(wsrd, WS_WF + j * 4, tag);
  if (big_) {
    const srd_t msrd = __builtin_amdgcn_make_buffer_rsrc(ws + WS_H, 0, 10240 * 512, 0x00020000);
    bf16x8 b1;
#pragma unroll
    for (int e = 0; e < 8; ++e) b1[e] = (lane & 15) == e + ((lane >> 1) & 8) ? 0x3f80 : 0;
    for (int n = wid; n < nn_; n += 4) {
      f32x4 acc = {};
      for (int e0 = 0; e0 < EE; e0 += 64) {
        unsigned long long m = __ballot(dst[e0 + lane] == lo + n);
        while (m) {
          const int e = e0 + __builtin_ctzll(m); m &= m - 1;
          const unsigned vo = lane < 32 ? (unsigned)src[e] * 512 + lane * 16 : 0x7fffff00u;
          acc = MFMA16(b1, as_bf(__builtin_amdgcn_raw_buffer_load_b128(msrd, vo, 0, 16)), acc);
        }
      }
      __builtin_amdgcn_raw_buffer_store_b64((u32x2){pk2(acc[0], acc[1]), pk2(acc[2], acc[3])}, wsrd,
                                            WS_G + (lo + n) * 512 + (lane >> 5) * 256 + (lane & 15) * 16 + (lane & 16) / 2, 0, 16);
    }
    drain();
    __syncthreads();
    if (tid < 128) st_flag(wsrd, WS_GF + j * 512 + tid * 4, tag);
  }
}

constexpr unsigned CODE_BYTES = 72000;
DI void code_touch(unsigned i, unsigned ldsdst) {
  unsigned long long a; int o_;
  asm volatile("s_getpc_b64 %0\n s_mov_b32 %1, _Z11attn_kernelPcPKiS1_PKtS3_PtS3_S3_S3_S3_S3_@rel32@lo+4" : "=&s"(a), "=&s"(o_));
  a += (long long)o_;
  asm volatile("s_mov_b32 m0, %2\n s_nop 0\n global_load_lds_dword %0, %1" :: "v"(min(i * 128u, CODE_BYTES - 4u)), "s"(a), "s"(ldsdst) : "m0", "memory");
}
__global__ __launch_bounds__(256, 1) void attn_kernel(char* ws, const int* __restrict__ src, const int* __restrict__ dst,
                                                      const u16* __restrict__ deg_inv, const u16* __restrict__ x, u16* out,
                                                      const u16* __restrict__ wq, const u16* __restrict__ wk, const u16* __restrict__ wv,
                                                      const u16* __restrict__ wm, const u16* __restrict__ wo) {
  extern __shared__ __attribute__((aligned(16))) char lds[];
  asm volatile("s_load_dword s99, %0, 0" :: "s"(__builtin_amdgcn_kernarg_segment_ptr()) : "s99");
  const int b = blockIdx.x, slot = b >> 3;
  code_touch((slot & 3) * 256 + threadIdx.x, (unsigned)(size_t)lds + 0x27f00u);
  const int wid = __builtin_amdgcn_readfirstlane(threadIdx.x >> 6);
  if (__builtin_expect((b & 231) == 231, 0)) { asm volatile("s_waitcnt lgkmcnt(0)" ::: "s99", "memory"); helper(lds, slot - 28, wid, src, dst, wv, wm, wo, ws); return; }
  static_assert(WS_END == 43761664 && WS_EP == 5999360, "workspace layout of the generated code");
  asm volatile(
    "s_load_dwordx2 s[20:21], %0, 0x30\n"
    "s_load_dwordx2 s[24:25], %0, 0x38\n"
    "s_load_dwordx2 s[12:13], %0, 0x20\n"
    "s_load_dwordx2 s[8:9], %0, 0x0\n"
    "s_load_dwordx2 s[16:17], %0, 0x28\n"
    "s_load_dwordx2 s[28:29], %0, 0x18\n"
    "s_load_dwordx4 s[84:87], %0, 0x8\n"
    "v_mbcnt_lo_u32_b32 v24, -1, 0\n"
    "v_mbcnt_hi_u32_b32 v24, -1, v24\n"
    "s_and_b32 s50, %2, 1\n"
    "s_lshr_b32 s51, %2, 1\n"
    "v_lshrrev_b32_e32 v25, 3, v24\n"
    "v_and_b32_e32 v26, 6, v25\n"
    "v_and_b32_e32 v27, 7, v24\n"
    "v_xor_b32_e32 v26, v26, v27\n"
    "v_lshlrev_b32_e32 v26, 4, v26\n"
    "v_and_b32_e32 v27, 4, v25\n"
    "v_add_u32_e32 v27, v27, v25\n"
    "v_lshl_add_u32 v251, v27, 9, v26\n"
    "v_add_u32_e32 v252, 0x1c00, v251\n"
    "s_mov_b32 s22, 0x20000\n"
    "s_mov_b32 s23, 0x20000\n"
    "s_mov_b32 s26, 0x20000\n"
    "s_mov_b32 s27, 0x20000\n"
    "s_lshr_b32 s36, %1, 7\n"
    "s_mul_i32 s36, s36, 3\n"
    "s_add_u32 s36, s36, %1\n"
    "s_and_b32 s36, s36, 127\n"
    "s_lshl_b32 s45, %2, 7\n"
    "s_lshl_b32 s46, %2, 12\n"
    "s_and_b32 s47, s36, 7\n"
    "s_lshl_b32 s47, s47, 14\n"
    "s_mov_b32 m0, s46\n"
    "s_add_u32 s47, s47, s45\n"
    "s_and_b32 s34, %1, 7\n"
    "s_lshl_b32 s34, s34, 5\n"
    "s_lshr_b32 s32, %1, 3\n"
    "s_add_u32 s34, s34, s32\n"
    "s_and_b32 s32, s32, 3\n"
    "s_lshr_b32 s33, s34, 2\n"
    "s_mul_i32 s38, s33, 81920\n"
    "s_lshl_b32 s35, s32, 13\n"
    "s_and_b32 s55, s35, 0x2000\n"
    "s_add_u32 s35, s35, s38\n"
    "s_add_u32 s55, s55, s38\n"
    "s_mov_b32 s14, 5120000\n"
    "s_mov_b32 s15, 0x20000\n"
    "s_waitcnt lgkmcnt(0)\n"
    "buffer_load_dwordx4 v251, s[20:23], s47 offen offset:0 lds\n"
    "s_add_u32 s48, s47, 1024\n"
    "buffer_load_dwordx4 v252, s[20:23], s47 offen offset:3072 lds\n"
    "s_add_u32 s47, s47, 0x1800\n"
    "buffer_load_dwordx4 v251, s[20:23], s48 offen offset:1024 lds\n"
    "buffer_load_dwordx4 v251, s[20:23], s47 offen offset:2048 lds\n"
    "s_lshr_b32 s47, s36, 4\n"
    "s_lshl_b32 s47, s47, 14\n"
    "s_and_b32 s48, s36, 8\n"
    "s_lshl_b32 s48, s48, 8\n"
    "s_add_u32 s47, s47, s48\n"
    "s_add_u32 m0, s46, 0x4000\n"
    "s_add_u32 s47, s47, s45\n"
    "buffer_load_dwordx4 v251, s[24:27], s47 offen offset:0 lds\n"
    "buffer_load_dwordx4 v252, s[24:27], s47 offen offset:1024 lds\n"
    "s_add_u32 s47, s45, 120832\n"
    "buffer_load_dwordx4 v251, s[24:27], s47 offen offset:2048 lds\n"
    "s_add_u32 s47, s47, 1024\n"
    "buffer_load_dwordx4 v251, s[24:27], s47 offen offset:3072 lds\n"
    "s_lshl_b32 s45, %2, 2\n"
    "s_add_u32 s54, s35, 0x8000\n"
    "s_add_u32 s55, s55, 0x10000\n"
    "v_lshlrev_b32_e32 v250, 4, v24\n"
    "v_lshrrev_b32_e32 v28, 4, v24\n"
    "v_add_u32_e32 v28, s45, v28\n"
    "v_and_b32_e32 v104, 0xf0, v250\n"
    "v_lshl_add_u32 v104, v28, 9, v104\n"
    "buffer_load_dwordx4 v[0:3], v104, s[12:15], s35 offen offset:0\n"
    "buffer_load_dwordx4 v[4:7], v104, s[12:15], s35 offen offset:256\n"
    "s_cmp_lt_u32 s32, 2\n"
    "s_cselect_b32 s60, s55, 2.0\n"
    "buffer_load_dwordx4 v[8:11], v104, s[12:15], s54 offen offset:0\n"
    "buffer_load_dwordx4 v[12:15], v104, s[12:15], s54 offen offset:256\n"
    "buffer_load_dwordx4 v[16:19], v104, s[12:15], s55 offen offset:0\n"
    "buffer_load_dwordx4 v[20:23], v104, s[12:15], s55 offen offset:256\n"
    "s_min_u32 s46, s34, 249\n"
    "s_mul_i32 s46, s46, 2560\n"
    "s_lshl_b32 s47, s50, 10\n"
    "s_add_u32 s47, s47, s46\n"
    "v_add_u32_e32 v108, s47, v250\n"
    "s_lshl_b32 s47, s50, 8\n"
    "s_add_u32 s47, s47, s46\n"
    "s_add_u32 s47, s47, 2048\n"
    "v_lshl_add_u32 v109, v24, 2, s47\n"
    "s_cmp_lg_u32 s51, 0\n"
    "s_cselect_b64 exec, -1, 1\n"
    "global_load_dwordx4 v[144:147], v108, s[86:87]\n"
    "global_load_dword v148, v109, s[86:87]\n"
    "global_load_dwordx4 v[152:155], v108, s[84:85]\n"
    "global_load_dword v156, v109, s[84:85]\n"
    "s_mov_b64 exec, -1\n"
    "s_mov_b32 s10, 43761664\n"
    "s_mov_b32 s11, 0x20000\n"
    "s_mov_b32 s38, 5999360\n"
    "s_buffer_load_dword s39, s[8:11], s38\n"
    "s_mov_b32 s49, 0\n"
    "v_and_b32_e32 v27, 1, v25\n"
    "v_lshl_add_u32 v248, v27, 9, v26\n"
    "v_lshrrev_b32_e32 v27, 1, v25\n"
    "v_lshl_add_u32 v248, v27, 12, v248\n"
    "s_mul_i32 s43, s50, 3\n"
    "s_xor_b32 s43, s43, s51\n"
    "s_lshl_b32 s43, s43, 15\n"
    "s_add_u32 s43, s43, 32768\n"
    "s_mul_i32 s42, %2, 5120\n"
    "s_lshl_b32 s47, s50, 8\n"
    "s_mul_i32 s40, s32, 1277952\n"
    "s_lshl_b32 s41, s51, 14\n"
    "s_add_u32 s40, s40, s41\n"
    "s_add_u32 s40, s40, s47\n"
    "s_add_u32 s41, s40, 128\n"
    "v_bfe_u32 v25, v24, 2, 3\n"
    "v_lshlrev_b32_e32 v244, 7, v25\n"
    "v_lshrrev_b32_e32 v26, 5, v24\n"
    "v_lshl_add_u32 v244, v26, 10, v244\n"
    "v_and_b32_e32 v25, 6, v25\n"
    "v_bfe_u32 v26, v24, 1, 1\n"
    "v_xor_b32_e32 v25, v25, v26\n"
    "v_lshl_add_u32 v244, v25, 4, v244\n"
    "v_and_b32_e32 v26, 1, v24\n"
    "v_lshl_add_u32 v244, v26, 3, v244\n"
    "s_lshl_b32 s47, s51, 16\n"
    "s_add_u32 s47, s47, 32768\n"
    "v_add_u32_e32 v244, s47, v244\n"
    "v_xor_b32_e32 v245, 32, v244\n"
    "v_xor_b32_e32 v246, 64, v244\n"
    "v_xor_b32_e32 v247, 0x60, v244\n"
    "v_and_b32_e32 v25, 7, v24\n"
    "v_lshlrev_b32_e32 v27, 7, v25\n"
    "v_bfe_u32 v26, v24, 3, 1\n"
    "v_lshl_add_u32 v27, v26, 10, v27\n"
    "v_and_b32_e32 v25, 6, v24\n"
    "v_lshrrev_b32_e32 v26, 4, v24\n"
    "v_xor_b32_e32 v25, v25, v26\n"
    "v_lshl_add_u32 v27, v25, 4, v27\n"
    "s_lshl_b32 s47, s51, 15\n"
    "s_add_u32 s48, s47, 32768\n"
    "s_sub_u32 s47, 131072, s47\n"
    "v_add_u32_e32 v240, s48, v27\n"
    "v_xor_b32_e32 v241, 64, v240\n"
    "v_add_u32_e32 v242, s47, v27\n"
    "v_xor_b32_e32 v243, 64, v242\n"
    "s_xor_b32 s47, %2, 2\n"
    "s_mul_i32 s47, s47, 5120\n"
    "v_lshl_add_u32 v249, v24, 2, s47\n"
    "v_and_b32_e32 v29, 15, v24\n"
    "s_cmp_ge_u32 s36, 112\n"
    "s_cselect_b32 s47, 8, 16\n"
    "v_cmp_le_u32_e32 vcc, s47, v29\n"
    "v_mov_b32_e32 v64, 0xbd80bd80\n"
    "v_mov_b32_e32 v65, 0xbdabbdab\n"
    "v_mov_b32_e32 v66, 0x392b392b\n"
    "v_cndmask_b32_e32 v64, v64, v65, vcc\n"
    "v_cndmask_b32_e32 v66, 0, v66, vcc\n"
    "s_cselect_b32 s47, 56, 64\n"
    "v_cmp_le_u32_e32 vcc, s47, v24\n"
    "v_mov_b32_e32 v65, 0x3e80bdab\n"
    "v_mov_b32_e32 v67, 0x392b\n"
    "v_cndmask_b32_e32 v65, v64, v65, vcc\n"
    "v_cndmask_b32_e32 v67, v66, v67, vcc\n"
    "s_cmp_ge_u32 s36, 120\n"
    "s_cselect_b32 s47, 15, 16\n"
    "v_cmp_ne_u32_e32 vcc, s47, v29\n"
    "v_mov_b32_e32 v68, 0x3db8aa3b\n"
    "s_lshr_b32 s47, s36, 5\n"
    "v_cndmask_b32_e32 v68, 0, v68, vcc\n"
    "s_lshl_b32 s47, s47, 15\n"
    "s_and_b32 s48, s36, 7\n"
    "s_lshl_b32 s48, s48, 11\n"
    "s_add_u32 s47, s47, s48\n"
    "s_and_b32 s48, s36, 8\n"
    "s_lshl_b32 s48, s48, 7\n"
    "s_add_u32 s47, s47, s48\n"
    "s_and_b32 s48, s36, 16\n"
    "s_lshl_b32 s48, s48, 10\n"
    "s_add_u32 s47, s47, s48\n"
    "s_add_u32 s52, s47, 5242880\n"
    "s_lshl_b32 s48, s50, 9\n"
    "s_add_u32 s52, s52, s48\n"
    "s_lshl_b32 s48, s50, 11\n"
    "v_xor_b32_e32 v75, 64, v27\n"
    "v_add_u32_e32 v72, s48, v27\n"
    "v_add_u32_e32 v73, s48, v75\n"
    "v_and_b32_e32 v74, 0x1f0, v250\n"
    "v_lshrrev_b32_e32 v29, 5, v24\n"
    "v_lshl_add_u32 v74, v29, 3, v74\n"
    "s_lshl1_add_u32 s53, s36, s50\n"
    "s_lshl4_add_u32 s53, s53, 5978112\n"
    "s_cmp_eq_u32 s51, 0\n"
    "s_cselect_b32 s52, s52, 2.0\n"
    "s_cselect_b32 s53, s53, 2.0\n"
    "s_lshl_b32 s47, %2, 10\n"
    "v_add_u32_e32 v108, s47, v250\n"
    "v_lshrrev_b32_e32 v109, 7, v108\n"
    "v_mul_u32_u24_e32 v109, 0x8200, v109\n"
    "v_and_b32_e32 v32, 0x70, v250\n"
    "v_or_b32_e32 v109, v109, v32\n"
    "v_add_u32_e32 v108, 81920, v108\n"
    "v_mov_b64_e32 v[32:33], -1\n"
    "v_mov_b64_e32 v[34:35], -1\n"
    "ds_write_b128 v108, v[32:35] offset:0\n"
    "ds_write_b128 v108, v[32:35] offset:16\n"
    "v_and_b32_e32 v29, 6, v28\n"
    "v_xor_b32_e32 v29, v29, v24\n"
    "v_and_b32_e32 v29, 7, v29\n"
    "v_lshlrev_b32_e32 v105, 7, v28\n"
    "v_lshl_add_u32 v105, v29, 4, v105\n"
    "v_bfe_u32 v29, v24, 3, 1\n"
    "v_subrev_u32_e32 v29, s32, v29\n"
    "v_and_b32_e32 v29, 3, v29\n"
    "v_lshl_add_u32 v105, v29, 11, v105\n"
    "v_add_u32_e32 v105, 32768, v105\n"
    "v_xor_b32_e32 v106, 0x1000, v105\n"
    "v_and_b32_e32 v29, 15, v24\n"
    "v_cmp_eq_u32_e32 vcc, 15, v29\n"
    "v_mov_b32_e32 v107, 0xffff\n"
    "s_mov_b32 s58, 0x3f803f80\n"
    "s_mov_b32 s57, 0x43c00000\n"
    "v_cndmask_b32_e32 v107, -1, v107, vcc\n"
    "s_mov_b32 s56, 0xc3c00000\n"
    "s_mov_b32 s62, 0xc000c000\n"
    "s_mov_b32 s63, 0xc000c000\n"
    "v_add_u32_e32 v96, 32768, v27\n"
    "v_xor_b32_e32 v97, 64, v96\n"
    "s_lshl_b32 s47, %2, 11\n"
    "v_add_u32_e32 v99, s47, v250\n"
    "v_add_u32_e32 v98, 11243520, v99\n"
    "s_lshl_b32 s47, %2, 10\n"
    "s_add_u32 s47, s47, 5978112\n"
    "v_add_u32_e32 v100, s47, v250\n"
    "s_waitcnt vmcnt(10)\n"
    "s_waitcnt lgkmcnt(0)\n"
    "s_barrier\n"
    "s_add_u32 s39, s39, 4\n"
    "s_cmp_lg_u32 s51, 0\n"
    "s_cbranch_scc1 .Lat_bin\n"
    "ds_read_b128 v[112:115], v72 offset:0\n"
    "ds_read_b128 v[208:211], v27 offset:18432\n"
    "ds_read_b128 v[176:179], v27 offset:16384\n"
    "ds_read_b128 v[116:119], v73 offset:0\n"
    "ds_read_b128 v[212:215], v75 offset:18432\n"
    "ds_read_b128 v[180:183], v75 offset:16384\n"
    "ds_read_b128 v[120:123], v72 offset:4096\n"
    "ds_read_b128 v[216:219], v27 offset:22528\n"
    "ds_read_b128 v[184:187], v27 offset:20480\n"
    "ds_read_b128 v[124:127], v73 offset:4096\n"
    "ds_read_b128 v[220:223], v75 offset:22528\n"
    "ds_read_b128 v[188:191], v75 offset:20480\n"
    "ds_read_b128 v[128:131], v72 offset:8192\n"
    "ds_read_b128 v[224:227], v27 offset:26624\n"
    "ds_read_b128 v[192:195], v27 offset:24576\n"
    "s_waitcnt lgkmcnt(12)\n"
    "v_mfma_f32_16x16x32_bf16 v[56:59], v[208:211], v[112:115], 0\n"
    "ds_read_b128 v[132:135], v73 offset:8192\n"
    "ds_read_b128 v[228:231], v75 offset:26624\n"
    "v_mfma_f32_16x16x32_bf16 v[48:51], v[112:115], v[176:179], 0\n"
    "ds_read_b128 v[196:199], v75 offset:24576\n"
    "s_waitcnt lgkmcnt(12)\n"
    "v_mfma_f32_16x16x32_bf16 v[56:59], v[212:215], v[116:119], v[56:59]\n"
    "ds_read_b128 v[136:139], v72 offset:12288\n"
    "ds_read_b128 v[232:235], v27 offset:30720\n"
    "v_mfma_f32_16x16x32_bf16 v[48:51], v[116:119], v[180:183], v[48:51]\n"
    "ds_read_b128 v[200:203], v27 offset:28672\n"
    "s_waitcnt lgkmcnt(12)\n"
    "v_mfma_f32_16x16x32_bf16 v[56:59], v[216:219], v[120:123], v[56:59]\n"
    "ds_read_b128 v[140:143], v73 offset:12288\n"
    "ds_read_b128 v[236:239], v75 offset:30720\n"
    "v_mfma_f32_16x16x32_bf16 v[48:51], v[120:123], v[184:187], v[48:51]\n"
    "ds_read_b128 v[204:207], v75 offset:28672\n"
    "s_waitcnt lgkmcnt(12)\n"
    "v_mfma_f32_16x16x32_bf16 v[56:59], v[220:223], v[124:127], v[56:59]\n"
    "v_mfma_f32_16x16x32_bf16 v[48:51], v[124:127], v[188:191], v[48:51]\n"
    "s_waitcnt lgkmcnt(9)\n"
    "v_mfma_f32_16x16x32_bf16 v[56:59], v[224:227], v[128:131], v[56:59]\n"
    "v_mfma_f32_16x16x32_bf16 v[48:51], v[128:131], v[192:195], v[48:51]\n"
    "s_waitcnt lgkmcnt(6)\n"
    "v_mfma_f32_16x16x32_bf16 v[56:59], v[228:231], v[132:135], v[56:59]\n"
    "v_mfma_f32_16x16x32_bf16 v[48:51], v[132:135], v[196:199], v[48:51]\n"
    "s_waitcnt lgkmcnt(3)\n"
    "v_mfma_f32_16x16x32_bf16 v[56:59], v[232:235], v[136:139], v[56:59]\n"
    "v_mfma_f32_16x16x32_bf16 v[48:51], v[136:139], v[200:203], v[48:51]\n"
    "s_waitcnt lgkmcnt(0)\n"
    "v_mfma_f32_16x16x32_bf16 v[56:59], v[236:239], v[140:143], v[56:59]\n"
    "v_mfma_f32_16x16x32_bf16 v[48:51], v[140:143], v[204:207], v[48:51]\n"
    "s_nop 3\n"
    "v_cvt_pk_bf16_f32 v56, v56, v57\n"
    "v_cvt_pk_bf16_f32 v57, v58, v59\n"
    "s_nop 1\n"
    "v_mfma_f32_16x16x16_bf16 v[48:51], v[56:57], v[64:65], v[48:51]\n"
    "v_mfma_f32_16x16x16_bf16 v[48:51], v[56:57], v[66:67], v[48:51]\n"
    "s_nop 7\n"
    "v_mul_f32_e32 v48, v48, v68\n"
    "v_mul_f32_e32 v49, v49, v68\n"
    "v_mul_f32_e32 v50, v50, v68\n"
    "v_mul_f32_e32 v51, v51, v68\n"
    "v_cvt_pk_bf16_f32 v48, v48, v49\n"
    "v_cvt_pk_bf16_f32 v49, v50, v51\n"
    ".Lat_noj2:\n"
    "buffer_store_dwordx2 v[48:49], v74, s[8:11], s52 offen offset:0 sc1\n"
    "v_mov_b32_e32 v102, s39\n"
    "s_waitcnt vmcnt(5)\n"
    "v_dot2_f32_bf16 v24, v0, s58, 0\n"
    "v_dot2_f32_bf16 v32, v8, s58, 0\n"
    "v_dot2_f32_bf16 v40, v16, s58, 0\n"
    "v_dot2_f32_bf16 v25, v4, s58, 0\n"
    "v_dot2_f32_bf16 v33, v12, s58, 0\n"
    "v_dot2_f32_bf16 v41, v20, s58, 0\n"
    "v_dot2_f32_bf16 v26, v0, v0, 0\n"
    "v_dot2_f32_bf16 v34, v8, v8, 0\n"
    "v_dot2_f32_bf16 v42, v16, v16, 0\n"
    "v_dot2_f32_bf16 v27, v4, v4, 0\n"
    "v_dot2_f32_bf16 v35, v12, v12, 0\n"
    "v_dot2_f32_bf16 v43, v20, v20, 0\n"
    "v_dot2_f32_bf16 v24, v1, s58, v24\n"
    "v_dot2_f32_bf16 v32, v9, s58, v32\n"
    "v_dot2_f32_bf16 v40, v17, s58, v40\n"
    "v_dot2_f32_bf16 v25, v5, s58, v25\n"
    "v_dot2_f32_bf16 v33, v13, s58, v33\n"
    "v_dot2_f32_bf16 v41, v21, s58, v41\n"
    "v_dot2_f32_bf16 v26, v1, v1, v26\n"
    "v_dot2_f32_bf16 v34, v9, v9, v34\n"
    "v_dot2_f32_bf16 v42, v17, v17, v42\n"
    "v_dot2_f32_bf16 v27, v5, v5, v27\n"
    "v_dot2_f32_bf16 v35, v13, v13, v35\n"
    "v_dot2_f32_bf16 v43, v21, v21, v43\n"
    "v_dot2_f32_bf16 v24, v2, s58, v24\n"
    "v_dot2_f32_bf16 v32, v10, s58, v32\n"
    "v_dot2_f32_bf16 v40, v18, s58, v40\n"
    "v_dot2_f32_bf16 v25, v6, s58, v25\n"
    "v_dot2_f32_bf16 v33, v14, s58, v33\n"
    "v_dot2_f32_bf16 v41, v22, s58, v41\n"
    "v_dot2_f32_bf16 v26, v2, v2, v26\n"
    "v_dot2_f32_bf16 v34, v10, v10, v34\n"
    "v_dot2_f32_bf16 v42, v18, v18, v42\n"
    "v_dot2_f32_bf16 v27, v6, v6, v27\n"
    "v_dot2_f32_bf16 v35, v14, v14, v35\n"
    "v_dot2_f32_bf16 v43, v22, v22, v43\n"
    "v_dot2_f32_bf16 v24, v3, s58, v24\n"
    "v_dot2_f32_bf16 v32, v11, s58, v32\n"
    "v_dot2_f32_bf16 v40, v19, s58, v40\n"
    "v_dot2_f32_bf16 v25, v7, s58, v25\n"
    "v_dot2_f32_bf16 v33, v15, s58, v33\n"
    "v_dot2_f32_bf16 v41, v23, s58, v41\n"
    "v_dot2_f32_bf16 v26, v3, v3, v26\n"
    "v_dot2_f32_bf16 v34, v11, v11, v34\n"
    "v_dot2_f32_bf16 v42, v19, v19, v42\n"
    "v_dot2_f32_bf16 v27, v7, v7, v27\n"
    "v_dot2_f32_bf16 v35, v15, v15, v35\n"
    "v_dot2_f32_bf16 v43, v23, v23, v43\n"
    "v_and_b32_e32 v30, 0xffff0000, v7\n"
    "v_and_b32_e32 v38, 0xffff0000, v15\n"
    "v_and_b32_e32 v46, 0xffff0000, v23\n"
    "v_add_f32_e32 v24, v24, v25\n"
    "v_add_f32_e32 v32, v32, v33\n"
    "v_add_f32_e32 v40, v40, v41\n"
    "v_add_f32_e32 v26, v26, v27\n"
    "v_add_f32_e32 v34, v34, v35\n"
    "v_add_f32_e32 v42, v42, v43\n"
    "v_add_f32_dpp v25, v25, v25 quad_perm:[1,0,3,2] row_mask:0xf bank_mask:0xf bound_ctrl:1\n"
    "v_lshlrev_b32_e32 v48, 16, v0\n"
    "v_add_f32_dpp v33, v33, v33 quad_perm:[1,0,3,2] row_mask:0xf bank_mask:0xf bound_ctrl:1\n"
    "v_and_b32_e32 v49, 0xffff0000, v0\n"
    "v_add_f32_dpp v41, v41, v41 quad_perm:[1,0,3,2] row_mask:0xf bank_mask:0xf bound_ctrl:1\n"
    "v_lshlrev_b32_e32 v50, 16, v1\n"
    "v_mov_b32_dpp v30, v30 quad_perm:[3,3,3,3] row_mask:0xf bank_mask:0xf bound_ctrl:1\n"
    "v_and_b32_e32 v51, 0xffff0000, v1\n"
    "v_mov_b32_dpp v38, v38 quad_perm:[3,3,3,3] row_mask:0xf bank_mask:0xf bound_ctrl:1\n"
    "v_lshlrev_b32_e32 v52, 16, v2\n"
    "v_mov_b32_dpp v46, v46 quad_perm:[3,3,3,3] row_mask:0xf bank_mask:0xf bound_ctrl:1\n"
    "v_and_b32_e32 v53, 0xffff0000, v2\n"
    "v_add_f32_dpp v24, v24, v24 quad_perm:[1,0,3,2] row_mask:0xf bank_mask:0xf bound_ctrl:1\n"
    "v_lshlrev_b32_e32 v54, 16, v3\n"
    "v_add_f32_dpp v32, v32, v32 quad_perm:[1,0,3,2] row_mask:0xf bank_mask:0xf bound_ctrl:1\n"
    "v_and_b32_e32 v55, 0xffff0000, v3\n"
    "v_add_f32_dpp v40, v40, v40 quad_perm:[1,0,3,2] row_mask:0xf bank_mask:0xf bound_ctrl:1\n"
    "v_lshlrev_b32_e32 v56, 16, v4\n"
    "v_add_f32_dpp v26, v26, v26 quad_perm:[1,0,3,2] row_mask:0xf bank_mask:0xf bound_ctrl:1\n"
    "v_and_b32_e32 v57, 0xffff0000, v4\n"
    "v_add_f32_dpp v34, v34, v34 quad_perm:[1,0,3,2] row_mask:0xf bank_mask:0xf bound_ctrl:1\n"
    "v_lshlrev_b32_e32 v58, 16, v5\n"
    "s_waitcnt vmcnt(0)\n"
    "buffer_store_dword v102, off, s[8:11], s53 offset:0 sc1\n"
    "v_add_f32_dpp v42, v42, v42 quad_perm:[1,0,3,2] row_mask:0xf bank_mask:0xf bound_ctrl:1\n"
    "v_and_b32_e32 v59, 0xffff0000, v5\n"
    "v_add_f32_dpp v24, v24, v24 quad_perm:[2,3,0,1] row_mask:0xf bank_mask:0xf bound_ctrl:1\n"
    "v_lshlrev_b32_e32 v60, 16, v6\n"
    "v_add_f32_dpp v32, v32, v32 quad_perm:[2,3,0,1] row_mask:0xf bank_mask:0xf bound_ctrl:1\n"
    "v_and_b32_e32 v61, 0xffff0000, v6\n"
    "v_add_f32_dpp v40, v40, v40 quad_perm:[2,3,0,1] row_mask:0xf bank_mask:0xf bound_ctrl:1\n"
    "v_lshlrev_b32_e32 v62, 16, v7\n"
    "v_add_f32_dpp v26, v26, v26 quad_perm:[2,3,0,1] row_mask:0xf bank_mask:0xf bound_ctrl:1\n"
    "v_and_b32_e32 v63, 0xffff0000, v7\n"
    "v_add_f32_dpp v34, v34, v34 quad_perm:[2,3,0,1] row_mask:0xf bank_mask:0xf bound_ctrl:1\n"
    "v_lshlrev_b32_e32 v64, 16, v8\n"
    "v_add_f32_dpp v42, v42, v42 quad_perm:[2,3,0,1] row_mask:0xf bank_mask:0xf bound_ctrl:1\n"
    "v_and_b32_e32 v65, 0xffff0000, v8\n"
    "v_add_f32_dpp v24, v24, v24 row_half_mirror row_mask:0xf bank_mask:0xf bound_ctrl:1\n"
    "v_lshlrev_b32_e32 v66, 16, v9\n"
    "v_add_f32_dpp v32, v32, v32 row_half_mirror row_mask:0xf bank_mask:0xf bound_ctrl:1\n"
    "v_and_b32_e32 v67, 0xffff0000, v9\n"
    "v_add_f32_dpp v40, v40, v40 row_half_mirror row_mask:0xf bank_mask:0xf bound_ctrl:1\n"
    "v_lshlrev_b32_e32 v68, 16, v10\n"
    "v_add_f32_dpp v26, v26, v26 row_half_mirror row_mask:0xf bank_mask:0xf bound_ctrl:1\n"
    "v_and_b32_e32 v69, 0xffff0000, v10\n"
    "v_add_f32_dpp v34, v34, v34 row_half_mirror row_mask:0xf bank_mask:0xf bound_ctrl:1\n"
    "v_lshlrev_b32_e32 v70, 16, v11\n"
    "v_add_f32_dpp v42, v42, v42 row_half_mirror row_mask:0xf bank_mask:0xf bound_ctrl:1\n"
    "v_and_b32_e32 v71, 0xffff0000, v11\n"
    "v_add_f32_dpp v24, v24, v24 row_mirror row_mask:0xf bank_mask:0xf bound_ctrl:1\n"
    "v_lshlrev_b32_e32 v72, 16, v12\n"
    "v_add_f32_dpp v32, v32, v32 row_mirror row_mask:0xf bank_mask:0xf bound_ctrl:1\n"
    "v_and_b32_e32 v73, 0xffff0000, v12\n"
    "v_add_f32_dpp v40, v40, v40 row_mirror row_mask:0xf bank_mask:0xf bound_ctrl:1\n"
    "v_lshlrev_b32_e32 v74, 16, v13\n"
    "v_add_f32_dpp v26, v26, v26 row_mirror row_mask:0xf bank_mask:0xf bound_ctrl:1\n"
    "v_and_b32_e32 v75, 0xffff0000, v13\n"
    "v_add_f32_dpp v34, v34, v34 row_mirror row_mask:0xf bank_mask:0xf bound_ctrl:1\n"
    "v_lshlrev_b32_e32 v76, 16, v14\n"
    "v_add_f32_dpp v42, v42, v42 row_mirror row_mask:0xf bank_mask:0xf bound_ctrl:1\n"
    "v_and_b32_e32 v77, 0xffff0000, v14\n"
    "v_mul_f32_e32 v24, 0x3b800000, v24\n"
    "v_lshlrev_b32_e32 v78, 16, v15\n"
    "v_mul_f32_e32 v32, 0x3b800000, v32\n"
    "v_and_b32_e32 v79, 0xffff0000, v15\n"
    "v_mul_f32_e32 v40, 0x3b800000, v40\n"
    "v_lshlrev_b32_e32 v80, 16, v16\n"
    "v_mul_f32_e32 v25, 0.25, v25\n"
    "v_and_b32_e32 v81, 0xffff0000, v16\n"
    "v_mul_f32_e32 v33, 0.25, v33\n"
    "v_lshlrev_b32_e32 v82, 16, v17\n"
    "v_mul_f32_e32 v41, 0.25, v41\n"
    "v_and_b32_e32 v83, 0xffff0000, v17\n"
    "v_mul_f32_e64 v27, v24, -v24\n"
    "v_lshlrev_b32_e32 v84, 16, v18\n"
    "v_mul_f32_e64 v35, v32, -v32\n"
    "v_and_b32_e32 v85, 0xffff0000, v18\n"
    "v_mul_f32_e64 v43, v40, -v40\n"
    "v_lshlrev_b32_e32 v86, 16, v19\n"
    "v_sub_f32_e32 v25, v25, v30\n"
    "v_and_b32_e32 v87, 0xffff0000, v19\n"
    "v_sub_f32_e32 v33, v33, v38\n"
    "v_lshlrev_b32_e32 v88, 16, v20\n"
    "v_sub_f32_e32 v41, v41, v46\n"
    "v_and_b32_e32 v89, 0xffff0000, v20\n"
    "v_fmac_f32_e32 v27, 0x3b800000, v26\n"
    "v_lshlrev_b32_e32 v90, 16, v21\n"
    "v_fmac_f32_e32 v35, 0x3b800000, v34\n"
    "v_and_b32_e32 v91, 0xffff0000, v21\n"
    "v_fmac_f32_e32 v43, 0x3b800000, v42\n"
    "v_lshlrev_b32_e32 v92, 16, v22\n"
    "v_mul_f32_e32 v25, 0x3eaaaaab, v25\n"
    "v_and_b32_e32 v93, 0xffff0000, v22\n"
    "v_mul_f32_e32 v33, 0x3eaaaaab, v33\n"
    "v_lshlrev_b32_e32 v94, 16, v23\n"
    "v_mul_f32_e32 v41, 0x3eaaaaab, v41\n"
    "v_and_b32_e32 v95, 0xffff0000, v23\n"
    "v_max_f32_e32 v27, 0, v27\n"
    "v_max_f32_e32 v35, 0, v35\n"
    "v_max_f32_e32 v43, 0, v43\n"
    "v_cndmask_b32_e64 v25, v24, v25, s[62:63]\n"
    "v_cndmask_b32_e64 v33, v32, v33, s[62:63]\n"
    "v_cndmask_b32_e64 v41, v40, v41, s[62:63]\n"
    "v_add_f32_e32 v27, 0x3727c5ac, v27\n"
    "v_add_f32_e32 v35, 0x3727c5ac, v35\n"
    "v_add_f32_e32 v43, 0x3727c5ac, v43\n"
    "v_rsq_f32_e32 v28, v27\n"
    "v_rsq_f32_e32 v36, v35\n"
    "v_rsq_f32_e32 v44, v43\n"
    "v_fma_f32 v29, -v24, v28, s57\n"
    "v_fma_f32 v37, -v32, v36, s57\n"
    "v_fma_f32 v45, -v40, v44, s57\n"
    "v_fma_f32 v31, -v25, v28, s57\n"
    "v_fma_f32 v39, -v33, v36, s57\n"
    "v_fma_f32 v47, -v41, v44, s57\n"
    "v_sub_f32_e32 v24, v29, v31\n"
    "v_sub_f32_e32 v32, v37, v39\n"
    "v_sub_f32_e32 v40, v45, v47\n"
    "v_pk_fma_f32 v[48:49], v[48:49], v[28:29], v[28:29] op_sel:[0,0,1] op_sel_hi:[1,0,1]\n"
    "v_pk_fma_f32 v[50:51], v[50:51], v[28:29], v[28:29] op_sel:[0,0,1] op_sel_hi:[1,0,1]\n"
    "v_pk_fma_f32 v[52:53], v[52:53], v[28:29], v[28:29] op_sel:[0,0,1] op_sel_hi:[1,0,1]\n"
    "v_pk_fma_f32 v[54:55], v[54:55], v[28:29], v[28:29] op_sel:[0,0,1] op_sel_hi:[1,0,1]\n"
    "v_pk_fma_f32 v[56:57], v[56:57], v[28:29], v[30:31] op_sel:[0,0,1] op_sel_hi:[1,0,1]\n"
    "v_pk_fma_f32 v[58:59], v[58:59], v[28:29], v[30:31] op_sel:[0,0,1] op_sel_hi:[1,0,1]\n"
    "v_pk_fma_f32 v[60:61], v[60:61], v[28:29], v[30:31] op_sel:[0,0,1] op_sel_hi:[1,0,1]\n"
    "v_pk_fma_f32 v[62:63], v[62:63], v[28:29], v[30:31] op_sel:[0,0,1] op_sel_hi:[1,0,1]\n"
    "v_pk_add_f32 v[48:49], v[48:49], s[56:57] op_sel_hi:[1,0]\n"
    "v_pk_add_f32 v[50:51], v[50:51], s[56:57] op_sel_hi:[1,0]\n"
    "v_pk_add_f32 v[52:53], v[52:53], s[56:57] op_sel_hi:[1,0]\n"
    "v_pk_add_f32 v[54:55], v[54:55], s[56:57] op_sel_hi:[1,0]\n"
    "v_pk_add_f32 v[56:57], v[56:57], s[56:57] op_sel_hi:[1,0]\n"
    "v_pk_add_f32 v[58:59], v[58:59], s[56:57] op_sel_hi:[1,0]\n"
    "v_pk_add_f32 v[60:61], v[60:61], s[56:57] op_sel_hi:[1,0]\n"
    "v_pk_add_f32 v[62:63], v[62:63], s[56:57] op_sel_hi:[1,0]\n"
    "v_cvt_pk_bf16_f32 v0, v48, v49\n"
    "v_cvt_pk_bf16_f32 v1, v50, v51\n"
    "v_cvt_pk_bf16_f32 v2, v52, v53\n"
    "v_cvt_pk_bf16_f32 v3, v54, v55\n"
    "v_cvt_pk_bf16_f32 v4, v56, v57\n"
    "v_cvt_pk_bf16_f32 v5, v58, v59\n"
    "v_cvt_pk_bf16_f32 v6, v60, v61\n"
    "v_cvt_pk_bf16_f32 v7, v62, v63\n"
    "buffer_store_dwordx4 v[0:3], v104, s[8:11], s35 offen offset:0 sc1\n"
    "v_bfi_b32 v7, v107, v7, 1.0\n"
    "ds_write_b128 v105, v[0:3] offset:0\n"
    "buffer_store_dwordx4 v[4:7], v104, s[8:11], s35 offen offset:256 sc1\n"
    "v_pk_fma_f32 v[64:65], v[64:65], v[36:37], v[36:37] op_sel:[0,0,1] op_sel_hi:[1,0,1]\n"
    "v_pk_fma_f32 v[66:67], v[66:67], v[36:37], v[36:37] op_sel:[0,0,1] op_sel_hi:[1,0,1]\n"
    "v_pk_fma_f32 v[68:69], v[68:69], v[36:37], v[36:37] op_sel:[0,0,1] op_sel_hi:[1,0,1]\n"
    "v_pk_fma_f32 v[70:71], v[70:71], v[36:37], v[36:37] op_sel:[0,0,1] op_sel_hi:[1,0,1]\n"
    "v_pk_fma_f32 v[72:73], v[72:73], v[36:37], v[38:39] op_sel:[0,0,1] op_sel_hi:[1,0,1]\n"
    "v_pk_fma_f32 v[74:75], v[74:75], v[36:37], v[38:39] op_sel:[0,0,1] op_sel_hi:[1,0,1]\n"
    "v_pk_fma_f32 v[76:77], v[76:77], v[36:37], v[38:39] op_sel:[0,0,1] op_sel_hi:[1,0,1]\n"
    "v_pk_fma_f32 v[78:79], v[78:79], v[36:37], v[38:39] op_sel:[0,0,1] op_sel_hi:[1,0,1]\n"
    "v_pk_add_f32 v[64:65], v[64:65], s[56:57] op_sel_hi:[1,0]\n"
    "v_pk_add_f32 v[66:67], v[66:67], s[56:57] op_sel_hi:[1,0]\n"
    "v_pk_add_f32 v[68:69], v[68:69], s[56:57] op_sel_hi:[1,0]\n"
    "v_pk_add_f32 v[70:71], v[70:71], s[56:57] op_sel_hi:[1,0]\n"
    "v_pk_add_f32 v[72:73], v[72:73], s[56:57] op_sel_hi:[1,0]\n"
    "v_pk_add_f32 v[74:75], v[74:75], s[56:57] op_sel_hi:[1,0]\n"
    "v_pk_add_f32 v[76:77], v[76:77], s[56:57] op_sel_hi:[1,0]\n"
    "v_pk_add_f32 v[78:79], v[78:79], s[56:57] op_sel_hi:[1,0]\n"
    "v_cvt_pk_bf16_f32 v8, v64, v65\n"
    "v_cvt_pk_bf16_f32 v9, v66, v67\n"
    "v_cvt_pk_bf16_f32 v10, v68, v69\n"
    "v_cvt_pk_bf16_f32 v11, v70, v71\n"
    "v_cvt_pk_bf16_f32 v12, v72, v73\n"
    "v_cvt_pk_bf16_f32 v13, v74, v75\n"
    "v_cvt_pk_bf16_f32 v14, v76, v77\n"
    "v_cvt_pk_bf16_f32 v15, v78, v79\n"
    "buffer_store_dwordx4 v[8:11], v104, s[8:11], s54 offen offset:0 sc1\n"
    "v_bfi_b32 v15, v107, v15, 1.0\n"
    "ds_write_b128 v105, v[8:11] offset:8192\n"
    "buffer_store_dwordx4 v[12:15], v104, s[8:11], s54 offen offset:256 sc1\n"
    "v_pk_fma_f32 v[80:81], v[80:81], v[44:45], v[44:45] op_sel:[0,0,1] op_sel_hi:[1,0,1]\n"
    "v_pk_fma_f32 v[82:83], v[82:83], v[44:45], v[44:45] op_sel:[0,0,1] op_sel_hi:[1,0,1]\n"
    "v_pk_fma_f32 v[84:85], v[84:85], v[44:45], v[44:45] op_sel:[0,0,1] op_sel_hi:[1,0,1]\n"
    "v_pk_fma_f32 v[86:87], v[86:87], v[44:45], v[44:45] op_sel:[0,0,1] op_sel_hi:[1,0,1]\n"
    "v_pk_fma_f32 v[88:89], v[88:89], v[44:45], v[46:47] op_sel:[0,0,1] op_sel_hi:[1,0,1]\n"
    "v_pk_fma_f32 v[90:91], v[90:91], v[44:45], v[46:47] op_sel:[0,0,1] op_sel_hi:[1,0,1]\n"
    "v_pk_fma_f32 v[92:93], v[92:93], v[44:45], v[46:47] op_sel:[0,0,1] op_sel_hi:[1,0,1]\n"
    "v_pk_fma_f32 v[94:95], v[94:95], v[44:45], v[46:47] op_sel:[0,0,1] op_sel_hi:[1,0,1]\n"
    "v_pk_add_f32 v[80:81], v[80:81], s[56:57] op_sel_hi:[1,0]\n"
    "v_pk_add_f32 v[82:83], v[82:83], s[56:57] op_sel_hi:[1,0]\n"
    "v_pk_add_f32 v[84:85], v[84:85], s[56:57] op_sel_hi:[1,0]\n"
    "v_pk_add_f32 v[86:87], v[86:87], s[56:57] op_sel_hi:[1,0]\n"
    "v_pk_add_f32 v[88:89], v[88:89], s[56:57] op_sel_hi:[1,0]\n"
    "v_pk_add_f32 v[90:91], v[90:91], s[56:57] op_sel_hi:[1,0]\n"
    "v_pk_add_f32 v[92:93], v[92:93], s[56:57] op_sel_hi:[1,0]\n"
    "v_pk_add_f32 v[94:95], v[94:95], s[56:57] op_sel_hi:[1,0]\n"
    "v_cvt_pk_bf16_f32 v16, v80, v81\n"
    "v_cvt_pk_bf16_f32 v17, v82, v83\n"
    "v_cvt_pk_bf16_f32 v18, v84, v85\n"
    "v_cvt_pk_bf16_f32 v19, v86, v87\n"
    "v_cvt_pk_bf16_f32 v20, v88, v89\n"
    "v_cvt_pk_bf16_f32 v21, v90, v91\n"
    "v_cvt_pk_bf16_f32 v22, v92, v93\n"
    "v_cvt_pk_bf16_f32 v23, v94, v95\n"
    "buffer_store_dwordx4 v[16:19], v104, s[8:11], s60 offen offset:0 sc1\n"
    "v_bfi_b32 v23, v107, v23, 1.0\n"
    "ds_write_b128 v105, v[16:19] offset:16384\n"
    "buffer_store_dwordx4 v[20:23], v104, s[8:11], s60 offen offset:256 sc1\n"
    "v_pk_add_f32 v[56:57], v[56:57], v[24:25] op_sel_hi:[1,0]\n"
    "v_pk_add_f32 v[58:59], v[58:59], v[24:25] op_sel_hi:[1,0]\n"
    "v_pk_add_f32 v[60:61], v[60:61], v[24:25] op_sel_hi:[1,0]\n"
    "v_pk_add_f32 v[62:63], v[62:63], v[24:25] op_sel_hi:[1,0]\n"
    "v_cvt_pk_bf16_f32 v56, v56, v57\n"
    "v_cvt_pk_bf16_f32 v57, v58, v59\n"
    "v_cvt_pk_bf16_f32 v58, v60, v61\n"
    "v_cvt_pk_bf16_f32 v59, v62, v63\n"
    "ds_write_b128 v106, v[56:59] offset:0\n"
    "v_pk_add_f32 v[72:73], v[72:73], v[32:33] op_sel_hi:[1,0]\n"
    "v_pk_add_f32 v[74:75], v[74:75], v[32:33] op_sel_hi:[1,0]\n"
    "v_pk_add_f32 v[76:77], v[76:77], v[32:33] op_sel_hi:[1,0]\n"
    "v_pk_add_f32 v[78:79], v[78:79], v[32:33] op_sel_hi:[1,0]\n"
    "v_cvt_pk_bf16_f32 v72, v72, v73\n"
    "v_cvt_pk_bf16_f32 v73, v74, v75\n"
    "v_cvt_pk_bf16_f32 v74, v76, v77\n"
    "v_cvt_pk_bf16_f32 v75, v78, v79\n"
    "ds_write_b128 v106, v[72:75] offset:8192\n"
    "v_pk_add_f32 v[88:89], v[88:89], v[40:41] op_sel_hi:[1,0]\n"
    "v_pk_add_f32 v[90:91], v[90:91], v[40:41] op_sel_hi:[1,0]\n"
    "v_pk_add_f32 v[92:93], v[92:93], v[40:41] op_sel_hi:[1,0]\n"
    "v_pk_add_f32 v[94:95], v[94:95], v[40:41] op_sel_hi:[1,0]\n"
    "v_cvt_pk_bf16_f32 v88, v88, v89\n"
    "v_cvt_pk_bf16_f32 v89, v90, v91\n"
    "v_cvt_pk_bf16_f32 v90, v92, v93\n"
    "v_cvt_pk_bf16_f32 v91, v94, v95\n"
    "ds_write_b128 v106, v[88:91] offset:16384\n"
    "s_waitcnt lgkmcnt(0)\n"
    "s_barrier\n"
    "ds_read_b128 a[0:3], v96 offset:0\n"
    "ds_read_b128 a[32:35], v96 offset:8192\n"
    "ds_read_b128 a[64:67], v96 offset:16384\n"
    "ds_read_b128 a[4:7], v97 offset:0\n"
    "ds_read_b128 a[36:39], v97 offset:8192\n"
    "ds_read_b128 a[68:71], v97 offset:16384\n"
    "ds_read_b128 a[8:11], v96 offset:2048\n"
    "ds_read_b128 a[40:43], v96 offset:10240\n"
    "ds_read_b128 a[72:75], v96 offset:18432\n"
    "ds_read_b128 a[12:15], v97 offset:2048\n"
    "ds_read_b128 a[44:47], v97 offset:10240\n"
    "ds_read_b128 a[76:79], v97 offset:18432\n"
    "buffer_load_dword v101, v100, s[8:11], 0 offen offset:0 sc1\n"
    "ds_read_b128 a[16:19], v96 offset:4096\n"
    "ds_read_b128 a[48:51], v96 offset:12288\n"
    "ds_read_b128 a[80:83], v96 offset:20480\n"
    "ds_read_b128 a[20:23], v97 offset:4096\n"
    "ds_read_b128 a[52:55], v97 offset:12288\n"
    "ds_read_b128 a[84:87], v97 offset:20480\n"
    "ds_read_b128 a[24:27], v96 offset:6144\n"
    "ds_read_b128 a[56:59], v96 offset:14336\n"
    "ds_read_b128 a[88:91], v96 offset:22528\n"
    "ds_read_b128 a[28:31], v97 offset:6144\n"
    "ds_read_b128 a[60:63], v97 offset:14336\n"
    "ds_read_b128 a[92:95], v97 offset:22528\n"
    "ds_read_b128 v[40:43], v108 offset:0\n"
    "s_lshl_b32 s52, %2, 15\n"
    "s_add_u32 s52, s52, 5242880\n"
    "s_lshl_b32 s61, s32, 12\n"
    "s_lshl4_add_u32 s46, s34, 5941248\n"
    "s_cmp_eq_u32 %2, 0\n"
    "s_cselect_b32 s46, s46, 2.0\n"
    "s_lshl_b32 s48, s34, 7\n"
    "s_add_u32 s48, s48, 42557440\n"
    "s_lshl_b32 s47, %2, 12\n"
    "s_waitcnt vmcnt(1)\n"
    "s_waitcnt lgkmcnt(0)\n"
    "s_barrier\n"
    "buffer_store_dword v102, off, s[8:11], s46 offset:0 sc1\n"
    "buffer_store_dwordx4 v[40:43], v109, s[8:11], s48 offen offset:0 sc1\n"
    "s_lshl4_add_u32 s46, s34, s47\n"
    "s_add_u32 s46, s46, 5982208\n"
    "s_waitcnt vmcnt(2)\n"
    "v_cmp_ne_u32_e32 vcc, s39, v101\n"
    "s_nop 4\n"
    "s_cbranch_vccnz .Lat_apoll\n"
    ".Lat_apoll_ret:\n"
    "s_add_u32 s47, s52, s61\n"
    "buffer_load_dwordx4 v[112:115], v250, s[8:11], s47 offen offset:0 sc1\n"
    "buffer_load_dwordx4 v[116:119], v250, s[8:11], s47 offen offset:1024 sc1\n"
    "buffer_load_dwordx4 v[128:131], v250, s[8:11], s47 offen offset:2048 sc1\n"
    "s_add_u32 s61, s61, 0x1000\n"
    "buffer_load_dwordx4 v[132:135], v250, s[8:11], s47 offen offset:3072 sc1\n"
    "s_bitset0_b32 s61, 14\n"
    "s_add_u32 s47, s52, s61\n"
    "buffer_load_dwordx4 v[144:147], v250, s[8:11], s47 offen offset:0 sc1\n"
    "buffer_load_dwordx4 v[148:151], v250, s[8:11], s47 offen offset:1024 sc1\n"
    "buffer_load_dwordx4 v[160:163], v250, s[8:11], s47 offen offset:2048 sc1\n"
    "s_add_u32 s61, s61, 0x1000\n"
    "buffer_load_dwordx4 v[164:167], v250, s[8:11], s47 offen offset:3072 sc1\n"
    "s_bitset0_b32 s61, 14\n"
    "s_add_u32 s47, s52, s61\n"
    "buffer_load_dwordx4 v[176:179], v250, s[8:11], s47 offen offset:0 sc1\n"
    "buffer_load_dwordx4 v[180:183], v250, s[8:11], s47 offen offset:1024 sc1\n"
    "buffer_load_dwordx4 v[192:195], v250, s[8:11], s47 offen offset:2048 sc1\n"
    "s_add_u32 s61, s61, 0x1000\n"
    "v_and_b32_e32 v82, 0x30, v250\n"
    "v_add_u32_e32 v82, 0x400, v82\n"
    "s_mul_i32 s59, s32, 999\n"
    "s_andn2_b32 s59, s59, 63\n"
    "s_sub_u32 s48, 3968, s59\n"
    "v_cmp_le_u32_e32 vcc, 0x80, v250\n"
    "v_mov_b32_e32 v80, s48\n"
    "s_add_u32 s59, s59, 5941248\n"
    "v_cndmask_b32_e32 v83, v250, v80, vcc\n"
    "v_bfe_u32 v253, v250, 4, 2\n"
    "v_max_u32_e32 v253, 1, v253\n"
    "v_add_u32_e32 v253, s32, v253\n"
    "v_and_b32_e32 v253, 3, v253\n"
    "v_lshlrev_b32_e32 v253, 7, v253\n"
    "v_mov_b32_e32 v84, s39\n"
    "s_lshl_b32 s58, s34, 7\n"
    "s_add_u32 s58, s58, 5945344\n"
    "s_cmp_eq_u32 %2, 2\n"
    "s_cselect_b32 s58, s58, 2.0\n"
    "s_lshl_b32 s45, s50, 13\n"
    "v_add_u32_e32 v85, s45, v250\n"
    "v_lshrrev_b32_e32 v105, 7, v250\n"
    "v_and_b32_e32 v104, 0x70, v251\n"
    "v_lshl_add_u32 v104, v105, 9, v104\n"
    "v_add_u32_e32 v105, 0xc00, v104\n"
    "v_add_u32_e32 v106, 0x1800, v104\n"
    "v_add_u32_e32 v107, 0x2400, v104\n"
    "s_lshl_b32 s57, s50, 8\n"
    "s_add_u32 s57, s57, 5111808\n"
    "s_waitcnt vmcnt(9)\n"
    "v_mfma_f32_16x16x32_bf16 v[24:27], v[112:115], a[0:3], 0\n"
    "buffer_load_dwordx4 v[196:199], v250, s[8:11], s47 offen offset:3072 sc1\n"
    "s_bitset0_b32 s61, 14\n"
    "v_mfma_f32_16x16x32_bf16 v[28:31], v[116:119], a[0:3], 0\n"
    "v_accvgpr_write_b32 a244, 0\n"
    "v_mfma_f32_16x16x32_bf16 v[48:51], v[112:115], a[32:35], 0\n"
    "v_accvgpr_write_b32 a245, 0\n"
    "v_mfma_f32_16x16x32_bf16 v[52:55], v[116:119], a[32:35], 0\n"
    "s_add_u32 s47, s52, s61\n"
    "buffer_load_dwordx4 v[208:211], v250, s[8:11], s47 offen offset:0 sc1\n"
    "v_mfma_f32_16x16x32_bf16 v[64:67], v[112:115], a[64:67], 0\n"
    "v_bfe_u32 v0, v250, 4, 2\n"
    "v_mfma_f32_16x16x32_bf16 v[68:71], v[116:119], a[64:67], 0\n"
    "s_mov_b32 s18, 5120000\n"
    "v_accvgpr_write_b32 a246, 0\n"
    "s_waitcnt vmcnt(9)\n"
    "v_mfma_f32_16x16x32_bf16 v[24:27], v[128:131], a[4:7], v[24:27]\n"
    "buffer_load_dwordx4 v[212:215], v250, s[8:11], s47 offen offset:1024 sc1\n"
    "s_add_u32 s52, s52, 0x4000\n"
    "v_mfma_f32_16x16x32_bf16 v[28:31], v[132:135], a[4:7], v[28:31]\n"
    "buffer_store_dword v102, off, s[8:11], s46 offset:0 sc1\n"
    "v_accvgpr_write_b32 a247, 0\n"
    "v_mfma_f32_16x16x32_bf16 v[48:51], v[128:131], a[36:39], v[48:51]\n"
    "v_bfe_u32 v1, v250, 6, 2\n"
    "v_accvgpr_write_b32 a248, 0\n"
    "v_mfma_f32_16x16x32_bf16 v[52:55], v[132:135], a[36:39], v[52:55]\n"
    "buffer_load_dwordx4 v[224:227], v250, s[8:11], s47 offen offset:2048 sc1\n"
    "s_add_u32 s61, s61, 0x1000\n"
    "v_mfma_f32_16x16x32_bf16 v[64:67], v[128:131], a[68:71], v[64:67]\n"
    "v_mov_b32_e32 v2, 0x3f80\n"
    "v_accvgpr_write_b32 a249, 0\n"
    "v_mfma_f32_16x16x32_bf16 v[68:71], v[132:135], a[68:71], v[68:71]\n"
    "s_mov_b32 s19, 0x20000\n"
    "s_waitcnt vmcnt(10)\n"
    "v_mfma_f32_16x16x32_bf16 v[24:27], v[144:147], a[8:11], v[24:27]\n"
    "buffer_load_dwordx4 v[228:231], v250, s[8:11], s47 offen offset:3072 sc1\n"
    "s_bitset0_b32 s61, 14\n"
    "v_mfma_f32_16x16x32_bf16 v[28:31], v[148:151], a[8:11], v[28:31]\n"
    "v_accvgpr_write_b32 a250, 0\n"
    "v_mfma_f32_16x16x32_bf16 v[48:51], v[144:147], a[40:43], v[48:51]\n"
    "s_mov_b64 s[20:21], s[8:9]\n"
    "v_accvgpr_write_b32 a251, 0\n"
    "v_mfma_f32_16x16x32_bf16 v[52:55], v[148:151], a[40:43], v[52:55]\n"
    "s_add_u32 s47, s52, s61\n"
    "buffer_load_dwordx4 v[120:123], v250, s[8:11], s47 offen offset:0 sc1\n"
    "v_mfma_f32_16x16x32_bf16 v[64:67], v[144:147], a[72:75], v[64:67]\n"
    "v_cmp_eq_u32_e32 vcc, 3, v0\n"
    "s_nop 1\n"
    "v_cndmask_b32_e64 v2, v2, 1.0, vcc\n"
    "v_mfma_f32_16x16x32_bf16 v[68:71], v[148:151], a[72:75], v[68:71]\n"
    "v_accvgpr_write_b32 a252, 0\n"
    "s_waitcnt vmcnt(10)\n"
    "v_mfma_f32_16x16x32_bf16 v[24:27], v[160:163], a[12:15], v[24:27]\n"
    "buffer_load_dwordx4 v[124:127], v250, s[8:11], s47 offen offset:1024 sc1\n"
    "v_accvgpr_write_b32 a253, 0\n"
    "v_mfma_f32_16x16x32_bf16 v[28:31], v[164:167], a[12:15], v[28:31]\n"
    "s_mov_b32 s22, 5242880\n"
    "v_accvgpr_write_b32 a254, 0\n"
    "v_mfma_f32_16x16x32_bf16 v[48:51], v[160:163], a[44:47], v[48:51]\n"
    "v_cmp_eq_u32_e32 vcc, 0, v1\n"
    "s_nop 1\n"
    "v_cndmask_b32_e32 v3, 0, v2, vcc\n"
    "v_mfma_f32_16x16x32_bf16 v[52:55], v[164:167], a[44:47], v[52:55]\n"
    "buffer_load_dwordx4 v[136:139], v250, s[8:11], s47 offen offset:2048 sc1\n"
    "s_add_u32 s61, s61, 0x1000\n"
    "v_mfma_f32_16x16x32_bf16 v[64:67], v[160:163], a[76:79], v[64:67]\n"
    "v_accvgpr_write_b32 a232, v3\n"
    "v_accvgpr_write_b32 a255, 0\n"
    "v_mfma_f32_16x16x32_bf16 v[68:71], v[164:167], a[76:79], v[68:71]\n"
    "s_mov_b64 s[24:25], s[28:29]\n"
    "v_accvgpr_write_b32 a192, 0\n"
    "s_waitcnt vmcnt(10)\n"
    "v_mfma_f32_16x16x32_bf16 v[24:27], v[176:179], a[16:19], v[24:27]\n"
    "buffer_load_dwordx4 v[140:143], v250, s[8:11], s47 offen offset:3072 sc1\n"
    "s_bitset0_b32 s61, 14\n"
    "v_mfma_f32_16x16x32_bf16 v[28:31], v[180:183], a[16:19], v[28:31]\n"
    "v_accvgpr_write_b32 a193, 0\n"
    "v_mfma_f32_16x16x32_bf16 v[48:51], v[176:179], a[48:51], v[48:51]\n"
    "v_cmp_eq_u32_e32 vcc, 1, v1\n"
    "s_nop 1\n"
    "v_cndmask_b32_e32 v3, 0, v2, vcc\n"
    "v_mfma_f32_16x16x32_bf16 v[52:55], v[180:183], a[48:51], v[52:55]\n"
    "s_add_u32 s47, s52, s61\n"
    "buffer_load_dwordx4 v[152:155], v250, s[8:11], s47 offen offset:0 sc1\n"
    "v_mfma_f32_16x16x32_bf16 v[64:67], v[176:179], a[80:83], v[64:67]\n"
    "buffer_load_dword v80, v83, s[8:11], s59 offen offset:0 sc1\n"
    "v_accvgpr_write_b32 a194, 0\n"
    "v_mfma_f32_16x16x32_bf16 v[68:71], v[180:183], a[80:83], v[68:71]\n"
    "s_movk_i32 s26, 20000\n"
    "v_accvgpr_write_b32 a195, 0\n"
    "s_waitcnt vmcnt(11)\n"
    "v_mfma_f32_16x16x32_bf16 v[24:27], v[192:195], a[20:23], v[24:27]\n"
    "buffer_load_dwordx4 v[156:159], v250, s[8:11], s47 offen offset:1024 sc1\n"
    "v_accvgpr_write_b32 a196, 0\n"
    "v_mfma_f32_16x16x32_bf16 v[28:31], v[196:199], a[20:23], v[28:31]\n"
    "v_accvgpr_write_b32 a233, v3\n"
    "v_mfma_f32_16x16x32_bf16 v[48:51], v[192:195], a[52:55], v[48:51]\n"
    "s_bfe_u32 s88, %1, 0x20001\n"
    "v_accvgpr_write_b32 a197, 0\n"
    "v_mfma_f32_16x16x32_bf16 v[52:55], v[196:199], a[52:55], v[52:55]\n"
    "buffer_load_dwordx4 v[168:171], v250, s[8:11], s47 offen offset:2048 sc1\n"
    "s_add_u32 s61, s61, 0x1000\n"
    "v_mfma_f32_16x16x32_bf16 v[64:67], v[192:195], a[84:87], v[64:67]\n"
    "v_cmp_eq_u32_e32 vcc, 2, v1\n"
    "s_nop 1\n"
    "v_cndmask_b32_e32 v3, 0, v2, vcc\n"
    "v_mfma_f32_16x16x32_bf16 v[68:71], v[196:199], a[84:87], v[68:71]\n"
    "v_accvgpr_write_b32 a234, v3\n"
    "v_accvgpr_write_b32 a198, 0\n"
    "s_waitcnt vmcnt(11)\n"
    "v_mfma_f32_16x16x32_bf16 v[24:27], v[208:211], a[24:27], v[24:27]\n"
    "buffer_load_dwordx4 v[172:175], v250, s[8:11], s47 offen offset:3072 sc1\n"
    "s_bitset0_b32 s61, 14\n"
    "v_mfma_f32_16x16x32_bf16 v[28:31], v[212:215], a[24:27], v[28:31]\n"
    "s_lshl_b32 s82, s88, 7\n"
    "v_accvgpr_write_b32 a199, 0\n"
    "v_mfma_f32_16x16x32_bf16 v[48:51], v[208:211], a[56:59], v[48:51]\n"
    "v_cmp_eq_u32_e32 vcc, 3, v1\n"
    "s_nop 1\n"
    "v_cndmask_b32_e32 v3, 0, v2, vcc\n"
    "v_mfma_f32_16x16x32_bf16 v[52:55], v[212:215], a[56:59], v[52:55]\n"
    "s_add_u32 s47, s52, s61\n"
    "buffer_load_dwordx4 v[184:187], v250, s[8:11], s47 offen offset:0 sc1\n"
    "v_mfma_f32_16x16x32_bf16 v[64:67], v[208:211], a[88:91], v[64:67]\n"
    "v_accvgpr_write_b32 a235, v3\n"
    "v_accvgpr_write_b32 a200, 0\n"
    "v_mfma_f32_16x16x32_bf16 v[68:71], v[212:215], a[88:91], v[68:71]\n"
    "s_add_u32 s82, s82, 5998848\n"
    "v_accvgpr_write_b32 a201, 0\n"
    "s_waitcnt vmcnt(10)\n"
    "v_mfma_f32_16x16x32_bf16 v[24:27], v[224:227], a[28:31], v[24:27]\n"
    "buffer_load_dwordx4 v[188:191], v250, s[8:11], s47 offen offset:1024 sc1\n"
    "v_accvgpr_write_b32 a202, 0\n"
    "v_mfma_f32_16x16x32_bf16 v[28:31], v[228:231], a[28:31], v[28:31]\n"
    "s_lshl_b32 s83, s88, 17\n"
    "v_accvgpr_write_b32 a203, 0\n"
    "v_mfma_f32_16x16x32_bf16 v[48:51], v[224:227], a[60:63], v[48:51]\n"
    "s_and_b32 s89, s34, 31\n"
    "v_accvgpr_write_b32 a204, 0\n"
    "v_mfma_f32_16x16x32_bf16 v[52:55], v[228:231], a[60:63], v[52:55]\n"
    "buffer_load_dwordx4 v[200:203], v250, s[8:11], s47 offen offset:2048 sc1\n"
    "s_add_u32 s61, s61, 0x1000\n"
    "v_mfma_f32_16x16x32_bf16 v[64:67], v[224:227], a[92:95], v[64:67]\n"
    "s_lshl2_add_u32 s89, s89, %2\n"
    "v_accvgpr_write_b32 a205, 0\n"
    "v_mfma_f32_16x16x32_bf16 v[68:71], v[228:231], a[92:95], v[68:71]\n"
    "s_lshl_b32 s89, s89, 10\n"
    "v_accvgpr_write_b32 a206, 0\n"
    "s_waitcnt vmcnt(10)\n"
    "v_mfma_f32_16x16x32_bf16 v[32:35], v[120:123], a[0:3], 0\n"
    "buffer_load_dwordx4 v[204:207], v250, s[8:11], s47 offen offset:3072 sc1\n"
    "s_bitset0_b32 s61, 14\n"
    "v_mfma_f32_16x16x32_bf16 v[36:39], v[124:127], a[0:3], 0\n"
    "v_cvt_pk_bf16_f32 v24, v24, v25\n"
    "v_accvgpr_write_b32 a207, 0\n"
    "v_mfma_f32_16x16x32_bf16 v[56:59], v[120:123], a[32:35], 0\n"
    "v_cvt_pk_bf16_f32 v25, v26, v27\n"
    "v_accvgpr_write_b32 a208, 0\n"
    "v_mfma_f32_16x16x32_bf16 v[60:63], v[124:127], a[32:35], 0\n"
    "s_add_u32 s47, s52, s61\n"
    "buffer_load_dwordx4 v[216:219], v250, s[8:11], s47 offen offset:0 sc1\n"
    "v_mfma_f32_16x16x32_bf16 v[72:75], v[120:123], a[64:67], 0\n"
    "v_cvt_pk_bf16_f32 v26, v28, v29\n"
    "v_cvt_pk_bf16_f32 v27, v30, v31\n"
    "v_mfma_f32_16x16x32_bf16 v[76:79], v[124:127], a[64:67], 0\n"
    "buffer_store_dwordx4 v[24:27], v98, s[8:11], s35 offen offset:0 sc1\n"
    "v_accvgpr_write_b32 a209, 0\n"
    "s_waitcnt vmcnt(11)\n"
    "v_mfma_f32_16x16x32_bf16 v[32:35], v[136:139], a[4:7], v[32:35]\n"
    "buffer_load_dwordx4 v[220:223], v250, s[8:11], s47 offen offset:1024 sc1\n"
    "s_add_u32 s52, s52, 0x4000\n"
    "v_mfma_f32_16x16x32_bf16 v[36:39], v[140:143], a[4:7], v[36:39]\n"
    "v_cvt_pk_bf16_f32 v48, v48, v49\n"
    "v_accvgpr_write_b32 a210, 0\n"
    "v_mfma_f32_16x16x32_bf16 v[56:59], v[136:139], a[36:39], v[56:59]\n"
    "v_cvt_pk_bf16_f32 v49, v50, v51\n"
    "v_cvt_pk_bf16_f32 v50, v52, v53\n"
    "v_mfma_f32_16x16x32_bf16 v[60:63], v[140:143], a[36:39], v[60:63]\n"
    "buffer_load_dwordx4 v[232:235], v250, s[8:11], s47 offen offset:2048 sc1\n"
    "s_add_u32 s61, s61, 0x1000\n"
    "s_waitcnt vmcnt(11)\n"
    "v_cmp_ne_u32_e32 vcc, s39, v80\n"
    "v_mfma_f32_16x16x32_bf16 v[72:75], v[136:139], a[68:71], v[72:75]\n"
    "v_cvt_pk_bf16_f32 v51, v54, v55\n"
    "buffer_store_dwordx4 v[48:51], v98, s[8:11], s54 offen offset:0 sc1\n"
    "v_mfma_f32_16x16x32_bf16 v[76:79], v[140:143], a[68:71], v[76:79]\n"
    "v_cvt_pk_bf16_f32 v64, v64, v65\n"
    "v_cvt_pk_bf16_f32 v65, v66, v67\n"
    "s_waitcnt vmcnt(11)\n"
    "v_mfma_f32_16x16x32_bf16 v[32:35], v[152:155], a[8:11], v[32:35]\n"
    "buffer_load_dwordx4 v[236:239], v250, s[8:11], s47 offen offset:3072 sc1\n"
    "s_bitset0_b32 s61, 14\n"
    "v_mfma_f32_16x16x32_bf16 v[36:39], v[156:159], a[8:11], v[36:39]\n"
    "s_cbranch_vccnz .Lat_h1poll\n"
    ".Lat_h1poll_ret:\n"
    "s_add_u32 m0, s43, 24576\n"
    "v_mfma_f32_16x16x32_bf16 v[56:59], v[152:155], a[40:43], v[56:59]\n"
    "buffer_load_dwordx4 v104, s[8:11], s57 offen offset:0 sc1 lds\n"
    "v_cvt_pk_bf16_f32 v66, v68, v69\n"
    "v_mfma_f32_16x16x32_bf16 v[60:63], v[156:159], a[40:43], v[60:63]\n"
    "v_cvt_pk_bf16_f32 v67, v70, v71\n"
    "buffer_store_dwordx4 v[64:67], v98, s[8:11], s60 offen offset:0 sc1\n"
    "v_mfma_f32_16x16x32_bf16 v[72:75], v[152:155], a[72:75], v[72:75]\n"
    "buffer_load_dwordx4 v105, s[8:11], s57 offen offset:1024 sc1 lds\n"
    "v_accvgpr_write_b32 a211, 0\n"
    "v_mfma_f32_16x16x32_bf16 v[76:79], v[156:159], a[72:75], v[76:79]\n"
    "s_add_u32 m0, s43, 28672\n"
    "s_add_u32 s57, s57, 128\n"
    "s_waitcnt vmcnt(13)\n"
    "v_mfma_f32_16x16x32_bf16 v[32:35], v[168:171], a[12:15], v[32:35]\n"
    "buffer_load_dwordx4 v104, s[8:11], s57 offen offset:0 sc1 lds\n"
    "v_accvgpr_write_b32 a212, 0\n"
    "v_mfma_f32_16x16x32_bf16 v[36:39], v[172:175], a[12:15], v[36:39]\n"
    "ds_write_b128 v99, v[24:27] offset:0\n"
    "v_accvgpr_write_b32 a213, 0\n"
    "v_mfma_f32_16x16x32_bf16 v[56:59], v[168:171], a[44:47], v[56:59]\n"
    "buffer_load_dwordx4 v105, s[8:11], s57 offen offset:1024 sc1 lds\n"
    "v_accvgpr_write_b32 a214, 0\n"
    "v_mfma_f32_16x16x32_bf16 v[60:63], v[172:175], a[44:47], v[60:63]\n"
    "s_add_u32 m0, s43, 0\n"
    "v_accvgpr_write_b32 a215, 0\n"
    "v_mfma_f32_16x16x32_bf16 v[72:75], v[168:171], a[76:79], v[72:75]\n"
    "buffer_load_dwordx4 v104, s[8:11], s40 offen offset:0 sc1 lds\n"
    "v_accvgpr_write_b32 a216, 0\n"
    "v_mfma_f32_16x16x32_bf16 v[76:79], v[172:175], a[76:79], v[76:79]\n"
    "s_add_u32 s83, s83, s89\n"
    "v_accvgpr_write_b32 a217, 0\n"
    "s_waitcnt vmcnt(14)\n"
    "v_mfma_f32_16x16x32_bf16 v[32:35], v[184:187], a[16:19], v[32:35]\n"
    "buffer_load_dwordx4 v105, s[8:11], s40 offen offset:1024 sc1 lds\n"
    "v_accvgpr_write_b32 a218, 0\n"
    "v_mfma_f32_16x16x32_bf16 v[36:39], v[188:191], a[16:19], v[36:39]\n"
    "s_add_u32 s83, s83, 42033152\n"
    "v_accvgpr_write_b32 a219, 0\n"
    "v_mfma_f32_16x16x32_bf16 v[56:59], v[184:187], a[48:51], v[56:59]\n"
    "buffer_load_dwordx4 v106, s[8:11], s40 offen offset:2048 sc1 lds\n"
    "v_mfma_f32_16x16x32_bf16 v[60:63], v[188:191], a[48:51], v[60:63]\n"
    "ds_write_b128 v99, v[48:51] offset:8192\n"
    "v_accvgpr_write_b32 a220, 0\n"
    "v_mfma_f32_16x16x32_bf16 v[72:75], v[184:187], a[80:83], v[72:75]\n"
    "buffer_load_dwordx4 v107, s[8:11], s40 offen offset:3072 sc1 lds\n"
    "v_accvgpr_write_b32 a221, 0\n"
    "v_mfma_f32_16x16x32_bf16 v[76:79], v[188:191], a[80:83], v[76:79]\n"
    "s_add_u32 s40, s40, 0x8000\n"
    "s_add_u32 m0, s43, 4096\n"
    "s_waitcnt vmcnt(15)\n"
    "v_mfma_f32_16x16x32_bf16 v[32:35], v[200:203], a[20:23], v[32:35]\n"
    "buffer_load_dwordx4 v104, s[8:11], s41 offen offset:0 sc1 lds\n"
    "v_accvgpr_write_b32 a222, 0\n"
    "v_mfma_f32_16x16x32_bf16 v[36:39], v[204:207], a[20:23], v[36:39]\n"
    "s_lshl_b32 s30, %2, 10\n"
    "v_mfma_f32_16x16x32_bf16 v[56:59], v[200:203], a[52:55], v[56:59]\n"
    "buffer_load_dwordx4 v105, s[8:11], s41 offen offset:1024 sc1 lds\n"
    "v_accvgpr_write_b32 a223, 0\n"
    "v_mfma_f32_16x16x32_bf16 v[60:63], v[204:207], a[52:55], v[60:63]\n"
    "s_add_u32 s30, s30, 20480\n"
    "v_mfma_f32_16x16x32_bf16 v[72:75], v[200:203], a[84:87], v[72:75]\n"
    "buffer_load_dwordx4 v106, s[8:11], s41 offen offset:2048 sc1 lds\n"
    "v_accvgpr_write_b32 a224, 0\n"
    "v_mfma_f32_16x16x32_bf16 v[76:79], v[204:207], a[84:87], v[76:79]\n"
    "s_bitcmp0_b32 %1, 0\n"
    "s_cselect_b32 s85, 1, 0\n"
    "s_cselect_b32 s80, 6, 0\n"
    "s_waitcnt vmcnt(15)\n"
    "v_mfma_f32_16x16x32_bf16 v[32:35], v[216:219], a[24:27], v[32:35]\n"
    "buffer_load_dwordx4 v107, s[8:11], s41 offen offset:3072 sc1 lds\n"
    "v_accvgpr_write_b32 a225, 0\n"
    "v_mfma_f32_16x16x32_bf16 v[36:39], v[220:223], a[24:27], v[36:39]\n"
    "s_add_u32 s41, s41, 0x8000\n"
    "v_mfma_f32_16x16x32_bf16 v[56:59], v[216:219], a[56:59], v[56:59]\n"
    "ds_write_b128 v99, v[64:67] offset:16384\n"
    "v_mfma_f32_16x16x32_bf16 v[60:63], v[220:223], a[56:59], v[60:63]\n"
    "s_bitcmp0_b32 %1, 0\n"
    "s_cselect_b32 s82, s82, 2.0\n"
    "s_cselect_b32 s83, s83, 2.0\n"
    "v_mfma_f32_16x16x32_bf16 v[72:75], v[216:219], a[88:91], v[72:75]\n"
    "v_accvgpr_write_b32 a226, 0\n"
    "v_mfma_f32_16x16x32_bf16 v[76:79], v[220:223], a[88:91], v[76:79]\n"
    "s_mov_b32 s84, 2.0\n"
    "s_waitcnt vmcnt(13)\n"
    "v_mfma_f32_16x16x32_bf16 v[32:35], v[232:235], a[28:31], v[32:35]\n"
    "v_accvgpr_write_b32 a227, 0\n"
    "v_mfma_f32_16x16x32_bf16 v[36:39], v[236:239], a[28:31], v[36:39]\n"
    "v_accvgpr_write_b32 a228, 0\n"
    "v_mfma_f32_16x16x32_bf16 v[56:59], v[232:235], a[60:63], v[56:59]\n"
    "v_accvgpr_write_b32 a229, 0\n"
    "v_mfma_f32_16x16x32_bf16 v[60:63], v[236:239], a[60:63], v[60:63]\n"
    "v_accvgpr_write_b32 a230, 0\n"
    "v_mfma_f32_16x16x32_bf16 v[72:75], v[232:235], a[92:95], v[72:75]\n"
    "s_movk_i32 s90, 512\n"
    "v_mfma_f32_16x16x32_bf16 v[76:79], v[236:239], a[92:95], v[76:79]\n"
    "v_accvgpr_write_b32 a231, 0\n"
    "v_cvt_pk_bf16_f32 v32, v32, v33\n"
    "v_cvt_pk_bf16_f32 v33, v34, v35\n"
    "v_cvt_pk_bf16_f32 v34, v36, v37\n"
    "v_cvt_pk_bf16_f32 v35, v38, v39\n"
    "ds_write_b128 v99, v[32:35] offset:1024\n"
    "buffer_store_dwordx4 v[32:35], v98, s[8:11], s35 offen offset:1024 sc1\n"
    "v_cvt_pk_bf16_f32 v56, v56, v57\n"
    "v_cvt_pk_bf16_f32 v57, v58, v59\n"
    "v_cvt_pk_bf16_f32 v58, v60, v61\n"
    "v_cvt_pk_bf16_f32 v59, v62, v63\n"
    "ds_write_b128 v99, v[56:59] offset:9216\n"
    "buffer_store_dwordx4 v[56:59], v98, s[8:11], s54 offen offset:1024 sc1\n"
    "v_cvt_pk_bf16_f32 v72, v72, v73\n"
    "v_cvt_pk_bf16_f32 v73, v74, v75\n"
    "v_cvt_pk_bf16_f32 v74, v76, v77\n"
    "v_cvt_pk_bf16_f32 v75, v78, v79\n"
    "ds_write_b128 v99, v[72:75] offset:17408\n"
    "buffer_store_dwordx4 v[72:75], v98, s[8:11], s60 offen offset:1024 sc1\n"
    "v_mov_b64_e32 v[8:9], 0\n"
    "v_mov_b64_e32 v[10:11], 0\n"
    "v_mov_b64_e32 v[12:13], 0\n"
    "s_add_u32 m0, s43, 8192\n"
    "v_mov_b64_e32 v[14:15], 0\n"
    "v_mov_b64_e32 v[16:17], 0\n"
    "v_mov_b64_e32 v[18:19], 0\n"
    "buffer_load_dwordx4 v104, s[8:11], s40 offen offset:0 sc1 lds\n"
    "v_mov_b64_e32 v[28:29], 0\n"
    "v_mov_b64_e32 v[30:31], 0\n"
    "v_mov_b64_e32 v[32:33], 0\n"
    "buffer_load_dwordx4 v105, s[8:11], s40 offen offset:1024 sc1 lds\n"
    "v_mov_b64_e32 v[34:35], 0\n"
    "v_mov_b64_e32 v[36:37], 0\n"
    "v_mov_b64_e32 v[38:39], 0\n"
    "buffer_load_dwordx4 v106, s[8:11], s40 offen offset:2048 sc1 lds\n"
    "v_mov_b64_e32 v[48:49], 0\n"
    "v_mov_b64_e32 v[50:51], 0\n"
    "v_mov_b64_e32 v[52:53], 0\n"
    "buffer_load_dwordx4 v107, s[8:11], s40 offen offset:3072 sc1 lds\n"
    "v_mov_b64_e32 v[54:55], 0\n"
    "v_mov_b64_e32 v[56:57], 0\n"
    "v_mov_b64_e32 v[58:59], 0\n"
    "s_add_u32 s40, s40, 0x8000\n"
    "v_mov_b64_e32 v[68:69], 0\n"
    "v_mov_b64_e32 v[70:71], 0\n"
    "v_mov_b64_e32 v[72:73], 0\n"
    "s_add_u32 m0, s43, 12288\n"
    "v_mov_b64_e32 v[74:75], 0\n"
    "v_mov_b64_e32 v[76:77], 0\n"
    "v_mov_b64_e32 v[78:79], 0\n"
    "buffer_load_dwordx4 v104, s[8:11], s41 offen offset:0 sc1 lds\n"
    "v_mov_b64_e32 v[88:89], 0\n"
    "v_mov_b64_e32 v[90:91], 0\n"
    "v_mov_b64_e32 v[92:93], 0\n"
    "buffer_load_dwordx4 v105, s[8:11], s41 offen offset:1024 sc1 lds\n"
    "v_mov_b64_e32 v[94:95], 0\n"
    "v_mov_b64_e32 v[96:97], 0\n"
    "v_mov_b64_e32 v[98:99], 0\n"
    "buffer_load_dwordx4 v106, s[8:11], s41 offen offset:2048 sc1 lds\n"
    "v_mov_b64_e32 v[108:109], 0\n"
    "v_mov_b64_e32 v[110:111], 0\n"
    "v_mov_b64_e32 v[112:113], 0\n"
    "buffer_load_dwordx4 v107, s[8:11], s41 offen offset:3072 sc1 lds\n"
    "v_mov_b64_e32 v[114:115], 0\n"
    "v_mov_b64_e32 v[116:117], 0\n"
    "v_mov_b64_e32 v[118:119], 0\n"
    "s_add_u32 s41, s41, 0x8000\n"
    "v_mov_b64_e32 v[128:129], 0\n"
    "v_mov_b64_e32 v[130:131], 0\n"
    "v_mov_b64_e32 v[132:133], 0\n"
    "v_mov_b64_e32 v[134:135], 0\n"
    "v_mov_b64_e32 v[136:137], 0\n"
    "v_mov_b64_e32 v[138:139], 0\n"
    "buffer_load_dword v80, v250, s[8:11], s59 offen offset:0 sc1\n"
    "buffer_load_dword v81, v82, s[8:11], s59 offen offset:0 sc1\n"
    ".Lat_p2:\n"
    "v_mov_b32_e32 v220, 0xc2980000\n"
    "v_mov_b32_e32 v221, 0xc2980000\n"
    "v_mov_b64_e32 v[222:223], v[220:221]\n"
    "v_mov_b64_e32 v[224:225], v[220:221]\n"
    "v_mov_b64_e32 v[226:227], v[220:221]\n"
    "v_mov_b64_e32 v[228:229], v[220:221]\n"
    "v_mov_b64_e32 v[230:231], v[220:221]\n"
    "v_mov_b64_e32 v[232:233], v[220:221]\n"
    "v_mov_b64_e32 v[234:235], v[220:221]\n"
    "v_mov_b64_e32 v[236:237], v[220:221]\n"
    "v_mov_b64_e32 v[238:239], v[220:221]\n"
    "s_lshl_b32 s47, s33, 9\n"
    "s_add_u32 s47, s47, 5945344\n"
    "s_mov_b32 s44, 9\n"
    "s_waitcnt vmcnt(10)\n"
    "s_waitcnt lgkmcnt(0)\n"
    "s_barrier\n"
    "buffer_store_dword v84, off, s[8:11], s58 offset:0 sc1\n"
    "s_cmp_lg_u32 s49, 0\n"
    "s_cbranch_scc1 .Lat_maxpass\n"
    "ds_read_b128 a[0:3], v85 offset:0\n"
    "ds_read_b128 a[4:7], v85 offset:1024\n"
    "ds_read_b128 a[8:11], v85 offset:2048\n"
    "ds_read_b128 a[12:15], v85 offset:3072\n"
    "ds_read_b128 a[16:19], v85 offset:4096\n"
    "ds_read_b128 a[20:23], v85 offset:5120\n"
    "ds_read_b128 a[24:27], v85 offset:6144\n"
    "ds_read_b128 a[28:31], v85 offset:7168\n"
    "ds_read_b128 a[32:35], v250 offset:16384\n"
    "ds_read_b128 a[36:39], v250 offset:17408\n"
    "ds_read_b128 a[40:43], v250 offset:18432\n"
    "ds_read_b128 a[44:47], v250 offset:19456\n"
    "ds_read_b128 a[48:51], v250 offset:20480\n"
    "ds_read_b128 a[52:55], v250 offset:21504\n"
    "ds_read_b128 a[56:59], v250 offset:22528\n"
    "ds_read_b128 a[60:63], v250 offset:23552\n"
    ".Lat_maxpass_ret:\n"
    "ds_read_b128 a[160:163], v240 offset:24576\n"
    "ds_read_b128 a[164:167], v241 offset:24576\n"
    "ds_read_b128 a[168:171], v240 offset:28672\n"
    "ds_read_b128 a[172:175], v241 offset:28672\n"
    "ds_read_b128 a[176:179], v242 offset:24576\n"
    "ds_read_b128 a[180:183], v243 offset:24576\n"
    "ds_read_b128 a[184:187], v242 offset:28672\n"
    "ds_read_b128 a[188:191], v243 offset:28672\n"
    "s_waitcnt lgkmcnt(4)\n"
    "v_mfma_f32_16x16x32_bf16 v[212:215], a[160:163], a[0:3], v[220:223]\n"
    "ds_read_b64_tr_b16 v[140:141], v244 offset:24576\n"
    "v_mfma_f32_16x16x32_bf16 v[216:219], a[160:163], a[32:35], v[224:227]\n"
    "ds_read_b64_tr_b16 v[142:143], v245 offset:24576\n"
    "v_mfma_f32_16x16x32_bf16 v[212:215], a[164:167], a[4:7], v[212:215]\n"
    "ds_read_b64_tr_b16 v[144:145], v246 offset:24576\n"
    "v_mfma_f32_16x16x32_bf16 v[216:219], a[164:167], a[36:39], v[216:219]\n"
    "ds_read_b64_tr_b16 v[146:147], v247 offset:24576\n"
    "v_mfma_f32_16x16x32_bf16 v[212:215], a[168:171], a[8:11], v[212:215]\n"
    "ds_read_b64_tr_b16 v[148:149], v244 offset:28672\n"
    "v_mfma_f32_16x16x32_bf16 v[216:219], a[168:171], a[40:43], v[216:219]\n"
    "ds_read_b64_tr_b16 v[150:151], v245 offset:28672\n"
    "v_mfma_f32_16x16x32_bf16 v[212:215], a[172:175], a[12:15], v[212:215]\n"
    "buffer_load_dword v254, v253, s[8:11], s47 offen offset:0 sc1\n"
    "v_mfma_f32_16x16x32_bf16 v[216:219], a[172:175], a[44:47], v[216:219]\n"
    "ds_read_b64_tr_b16 v[152:153], v246 offset:28672\n"
    "s_waitcnt lgkmcnt(7)\n"
    "v_mfma_f32_16x16x32_bf16 v[212:215], a[176:179], a[16:19], v[212:215]\n"
    "ds_read_b64_tr_b16 v[154:155], v247 offset:28672\n"
    "v_mfma_f32_16x16x32_bf16 v[216:219], a[176:179], a[48:51], v[216:219]\n"
    "ds_read_b128 v[180:183], v240 offset:2048\n"
    "v_mfma_f32_16x16x32_bf16 v[212:215], a[180:183], a[20:23], v[212:215]\n"
    "ds_read_b128 v[184:187], v241 offset:2048\n"
    "v_mfma_f32_16x16x32_bf16 v[216:219], a[180:183], a[52:55], v[216:219]\n"
    "ds_read_b128 v[188:191], v240 offset:6144\n"
    "v_mfma_f32_16x16x32_bf16 v[212:215], a[184:187], a[24:27], v[212:215]\n"
    "ds_read_b128 v[192:195], v241 offset:6144\n"
    "v_mfma_f32_16x16x32_bf16 v[216:219], a[184:187], a[56:59], v[216:219]\n"
    "ds_read_b128 v[196:199], v242 offset:2048\n"
    "v_mfma_f32_16x16x32_bf16 v[212:215], a[188:191], a[28:31], v[212:215]\n"
    "ds_read_b128 v[200:203], v243 offset:2048\n"
    "v_mfma_f32_16x16x32_bf16 v[216:219], a[188:191], a[60:63], v[216:219]\n"
    "ds_read_b128 v[204:207], v242 offset:6144\n"
    "ds_read_b128 v[208:211], v243 offset:6144\n"
    "s_lshr_b32 s61, s32, 1\n"
    "s_or_b32 s61, s61, s50\n"
    "s_sub_u32 s61, s61, 1\n"
    "ds_read_b128 a[160:163], v240 offset:0\n"
    "ds_read_b128 a[164:167], v241 offset:0\n"
    "ds_read_b128 a[168:171], v240 offset:4096\n"
    "ds_read_b128 a[172:175], v241 offset:4096\n"
    "ds_read_b128 a[176:179], v242 offset:0\n"
    "ds_read_b128 a[180:183], v243 offset:0\n"
    "ds_read_b128 a[184:187], v242 offset:4096\n"
    "ds_read_b128 a[188:191], v243 offset:4096\n"
    "s_waitcnt vmcnt(0)\n"
    "v_cmp_ne_u32_e32 vcc, s39, v254\n"
    "s_mul_i32 s48, s33, 81920\n"
    "s_add_u32 s48, s48, 11243520\n"
    "v_exp_f32_e32 v212, v212\n"
    "v_exp_f32_e32 v213, v213\n"
    "v_exp_f32_e32 v214, v214\n"
    "v_exp_f32_e32 v215, v215\n"
    "s_cbranch_vccnz .Lat_qpoll\n"
    ".Lat_qpoll_ret:\n"
    "s_cmp_lg_u32 s50, 0\n"
    "s_cbranch_scc1 .Lat_qx\n"
    ".Lat_qx_ret:\n"
    "v_xor_b32_e32 v81, s39, v81\n"
    "v_xor_b32_e32 v80, s39, v80\n"
    "v_or_b32_e32 v80, v80, v81\n"
    "v_cmp_ne_u32_e32 vcc, 0, v80\n"
    "s_add_u32 s45, s32, 1\n"
    "s_and_b32 s45, s45, 3\n"
    "s_lshl2_add_u32 s45, s50, s45\n"
    "s_lshl_b32 s45, s45, 13\n"
    "s_add_u32 s45, s45, s48\n"
    "s_add_u32 s46, s45, 4096\n"
    "buffer_load_dwordx4 a[64:67], v250, s[8:11], s45 offen offset:0 sc1\n"
    "buffer_load_dwordx4 a[68:71], v250, s[8:11], s45 offen offset:1024 sc1\n"
    "v_exp_f32_e32 v216, v216\n"
    "v_exp_f32_e32 v217, v217\n"
    "v_exp_f32_e32 v218, v218\n"
    "v_exp_f32_e32 v219, v219\n"
    "v_cvt_pk_bf16_f32 v212, v212, v213\n"
    "v_cvt_pk_bf16_f32 v213, v214, v215\n"
    "v_cvt_pk_bf16_f32 v216, v216, v217\n"
    "v_cvt_pk_bf16_f32 v217, v218, v219\n"
    "v_and_b32_e32 v216, s61, v216\n"
    "v_and_b32_e32 v217, s61, v217\n"
    "s_cbranch_vccnz .Lat_hpoll\n"
    ".Lat_hpoll_ret:\n"
    "s_waitcnt lgkmcnt(15)\n"
    "v_mfma_f32_16x16x16_bf16 a[236:239], v[140:141], v[212:213], 0\n"
    "buffer_load_dwordx4 a[72:75], v250, s[8:11], s45 offen offset:2048 sc1\n"
    "v_mfma_f32_16x16x16_bf16 v[0:3], v[142:143], v[212:213], 0\n"
    "v_mfma_f32_16x16x16_bf16 v[20:23], v[144:145], v[212:213], 0\n"
    "buffer_load_dwordx4 a[76:79], v250, s[8:11], s45 offen offset:3072 sc1\n"
    "v_mfma_f32_16x16x16_bf16 v[40:43], v[146:147], v[212:213], 0\n"
    "v_mfma_f32_16x16x16_bf16 v[60:63], v[148:149], v[212:213], 0\n"
    "buffer_load_dwordx4 a[80:83], v250, s[8:11], s46 offen offset:0 sc1\n"
    "v_mfma_f32_16x16x16_bf16 v[80:83], v[150:151], v[212:213], 0\n"
    "v_mfma_f32_16x16x16_bf16 v[100:103], v[152:153], v[212:213], 0\n"
    "buffer_load_dwordx4 a[84:87], v250, s[8:11], s46 offen offset:1024 sc1\n"
    "v_mfma_f32_16x16x16_bf16 v[120:123], v[154:155], v[212:213], 0\n"
    "v_mfma_f32_16x16x16_bf16 a[240:243], v[140:141], v[216:217], 0\n"
    "buffer_load_dwordx4 a[88:91], v250, s[8:11], s46 offen offset:2048 sc1\n"
    "v_mfma_f32_16x16x16_bf16 v[4:7], v[142:143], v[216:217], 0\n"
    "v_mfma_f32_16x16x16_bf16 v[24:27], v[144:145], v[216:217], 0\n"
    "buffer_load_dwordx4 a[92:95], v250, s[8:11], s46 offen offset:3072 sc1\n"
    "v_mfma_f32_16x16x16_bf16 v[44:47], v[146:147], v[216:217], 0\n"
    "s_add_u32 s45, s32, 2\n"
    "v_mfma_f32_16x16x16_bf16 v[64:67], v[148:149], v[216:217], 0\n"
    "s_and_b32 s45, s45, 3\n"
    "v_mfma_f32_16x16x16_bf16 v[84:87], v[150:151], v[216:217], 0\n"
    "s_lshl2_add_u32 s45, s50, s45\n"
    "v_mfma_f32_16x16x16_bf16 v[104:107], v[152:153], v[216:217], 0\n"
    "s_lshl_b32 s45, s45, 13\n"
    "v_mfma_f32_16x16x16_bf16 v[124:127], v[154:155], v[216:217], 0\n"
    "s_waitcnt lgkmcnt(0)\n"
    "v_mfma_f32_16x16x32_bf16 v[140:143], a[160:163], a[0:3], v[220:223]\n"
    "s_add_u32 s45, s45, s48\n"
    "v_mfma_f32_16x16x32_bf16 v[140:143], a[164:167], a[4:7], v[140:143]\n"
    "s_add_u32 s46, s45, 4096\n"
    "v_mfma_f32_16x16x32_bf16 v[140:143], a[168:171], a[8:11], v[140:143]\n"
    "buffer_load_dwordx4 a[96:99], v250, s[8:11], s45 offen offset:0 sc1\n"
    "v_mfma_f32_16x16x32_bf16 v[140:143], a[172:175], a[12:15], v[140:143]\n"
    "v_mfma_f32_16x16x32_bf16 v[140:143], a[176:179], a[16:19], v[140:143]\n"
    "buffer_load_dwordx4 a[100:103], v250, s[8:11], s45 offen offset:1024 sc1\n"
    "v_mfma_f32_16x16x32_bf16 v[140:143], a[180:183], a[20:23], v[140:143]\n"
    "v_mfma_f32_16x16x32_bf16 v[140:143], a[184:187], a[24:27], v[140:143]\n"
    "buffer_load_dwordx4 a[104:107], v250, s[8:11], s45 offen offset:2048 sc1\n"
    "v_mfma_f32_16x16x32_bf16 v[140:143], a[188:191], a[28:31], v[140:143]\n"
    "v_mfma_f32_16x16x32_bf16 v[144:147], v[180:183], a[0:3], v[220:223]\n"
    "buffer_load_dwordx4 a[108:111], v250, s[8:11], s45 offen offset:3072 sc1\n"
    "v_mfma_f32_16x16x32_bf16 v[144:147], v[184:187], a[4:7], v[144:147]\n"
    "v_mfma_f32_16x16x32_bf16 v[144:147], v[188:191], a[8:11], v[144:147]\n"
    "buffer_load_dwordx4 a[112:115], v250, s[8:11], s46 offen offset:0 sc1\n"
    "v_mfma_f32_16x16x32_bf16 v[144:147], v[192:195], a[12:15], v[144:147]\n"
    "v_exp_f32_e32 v140, v140\n"
    "v_mfma_f32_16x16x32_bf16 v[144:147], v[196:199], a[16:19], v[144:147]\n"
    "buffer_load_dwordx4 a[116:119], v250, s[8:11], s46 offen offset:1024 sc1\n"
    "v_mfma_f32_16x16x32_bf16 v[144:147], v[200:203], a[20:23], v[144:147]\n"
    "v_exp_f32_e32 v141, v141\n"
    "v_mfma_f32_16x16x32_bf16 v[144:147], v[204:207], a[24:27], v[144:147]\n"
    "buffer_load_dwordx4 a[120:123], v250, s[8:11], s46 offen offset:2048 sc1\n"
    "v_mfma_f32_16x16x32_bf16 v[144:147], v[208:211], a[28:31], v[144:147]\n"
    "s_waitcnt vmcnt(21)\n"
    "v_exp_f32_e32 v142, v142\n"
    "v_mfma_f32_16x16x32_bf16 v[148:151], a[160:163], a[32:35], v[224:227]\n"
    "buffer_load_dwordx4 a[124:127], v250, s[8:11], s46 offen offset:3072 sc1\n"
    "v_mfma_f32_16x16x32_bf16 v[148:151], a[164:167], a[36:39], v[148:151]\n"
    "s_waitcnt vmcnt(20)\n"
    "v_exp_f32_e32 v143, v143\n"
    "v_mfma_f32_16x16x32_bf16 v[148:151], a[168:171], a[40:43], v[148:151]\n"
    "s_add_u32 s45, s32, 3\n"
    "v_cvt_pk_bf16_f32 v140, v140, v141\n"
    "v_mfma_f32_16x16x32_bf16 v[148:151], a[172:175], a[44:47], v[148:151]\n"
    "s_waitcnt vmcnt(18)\n"
    "v_cvt_pk_bf16_f32 v141, v142, v143\n"
    "v_mfma_f32_16x16x32_bf16 v[148:151], a[176:179], a[48:51], v[148:151]\n"
    "s_and_b32 s45, s45, 3\n"
    "v_exp_f32_e32 v144, v144\n"
    "v_mfma_f32_16x16x32_bf16 v[148:151], a[180:183], a[52:55], v[148:151]\n"
    "s_waitcnt vmcnt(16)\n"
    "v_exp_f32_e32 v145, v145\n"
    "v_mfma_f32_16x16x32_bf16 v[148:151], a[184:187], a[56:59], v[148:151]\n"
    "s_lshl2_add_u32 s45, s50, s45\n"
    "v_exp_f32_e32 v146, v146\n"
    "v_mfma_f32_16x16x32_bf16 v[148:151], a[188:191], a[60:63], v[148:151]\n"
    "s_lshl_b32 s45, s45, 13\n"
    "v_exp_f32_e32 v147, v147\n"
    "v_mfma_f32_16x16x32_bf16 v[152:155], v[180:183], a[32:35], v[224:227]\n"
    "s_add_u32 s45, s45, s48\n"
    "v_cvt_pk_bf16_f32 v142, v144, v145\n"
    "v_mfma_f32_16x16x32_bf16 v[152:155], v[184:187], a[36:39], v[152:155]\n"
    "s_add_u32 s46, s45, 4096\n"
    "v_cvt_pk_bf16_f32 v143, v146, v147\n"
    "v_mfma_f32_16x16x32_bf16 v[152:155], v[188:191], a[40:43], v[152:155]\n"
    "buffer_load_dwordx4 a[128:131], v250, s[8:11], s45 offen offset:0 sc1\n"
    "v_mfma_f32_16x16x32_bf16 v[152:155], v[192:195], a[44:47], v[152:155]\n"
    "v_exp_f32_e32 v148, v148\n"
    "v_mfma_f32_16x16x32_bf16 v[152:155], v[196:199], a[48:51], v[152:155]\n"
    "buffer_load_dwordx4 a[132:135], v250, s[8:11], s45 offen offset:1024 sc1\n"
    "v_mfma_f32_16x16x32_bf16 v[152:155], v[200:203], a[52:55], v[152:155]\n"
    "v_exp_f32_e32 v149, v149\n"
    "v_mfma_f32_16x16x32_bf16 v[152:155], v[204:207], a[56:59], v[152:155]\n"
    "buffer_load_dwordx4 a[136:139], v250, s[8:11], s45 offen offset:2048 sc1\n"
    "v_mfma_f32_16x16x32_bf16 v[152:155], v[208:211], a[60:63], v[152:155]\n"
    "s_waitcnt vmcnt(17)\n"
    "v_exp_f32_e32 v150, v150\n"
    "v_mfma_f32_16x16x32_bf16 v[156:159], a[160:163], a[64:67], v[228:231]\n"
    "buffer_load_dwordx4 a[140:143], v250, s[8:11], s45 offen offset:3072 sc1\n"
    "v_mfma_f32_16x16x32_bf16 v[156:159], a[164:167], a[68:71], v[156:159]\n"
    "s_waitcnt vmcnt(16)\n"
    "v_exp_f32_e32 v151, v151\n"
    "v_mfma_f32_16x16x32_bf16 v[156:159], a[168:171], a[72:75], v[156:159]\n"
    "buffer_load_dwordx4 a[144:147], v250, s[8:11], s46 offen offset:0 sc1\n"
    "v_mfma_f32_16x16x32_bf16 v[156:159], a[172:175], a[76:79], v[156:159]\n"
    "s_waitcnt vmcnt(15)\n"
    "v_cvt_pk_bf16_f32 v148, v148, v149\n"
    "v_mfma_f32_16x16x32_bf16 v[156:159], a[176:179], a[80:83], v[156:159]\n"
    "buffer_load_dwordx4 a[148:151], v250, s[8:11], s46 offen offset:1024 sc1\n"
    "v_cvt_pk_bf16_f32 v149, v150, v151\n"
    "v_mfma_f32_16x16x32_bf16 v[156:159], a[180:183], a[84:87], v[156:159]\n"
    "s_waitcnt vmcnt(14)\n"
    "v_exp_f32_e32 v152, v152\n"
    "v_mfma_f32_16x16x32_bf16 v[156:159], a[184:187], a[88:91], v[156:159]\n"
    "buffer_load_dwordx4 a[152:155], v250, s[8:11], s46 offen offset:2048 sc1\n"
    "v_mfma_f32_16x16x32_bf16 v[156:159], a[188:191], a[92:95], v[156:159]\n"
    "v_exp_f32_e32 v153, v153\n"
    "v_mfma_f32_16x16x32_bf16 v[160:163], v[180:183], a[64:67], v[228:231]\n"
    "buffer_load_dwordx4 a[156:159], v250, s[8:11], s46 offen offset:3072 sc1\n"
    "v_mfma_f32_16x16x32_bf16 v[160:163], v[184:187], a[68:71], v[160:163]\n"
    "s_add_u32 m0, s43, 16384\n"
    "v_exp_f32_e32 v154, v154\n"
    "v_mfma_f32_16x16x32_bf16 v[160:163], v[188:191], a[72:75], v[160:163]\n"
    "buffer_load_dwordx4 v248, s[8:11], s40 offen offset:0 sc1 lds\n"
    "v_mfma_f32_16x16x32_bf16 v[160:163], v[192:195], a[76:79], v[160:163]\n"
    "v_exp_f32_e32 v155, v155\n"
    "v_mfma_f32_16x16x32_bf16 v[160:163], v[196:199], a[80:83], v[160:163]\n"
    "buffer_load_dwordx4 v248, s[8:11], s40 offen offset:1024 sc1 lds\n"
    "v_mfma_f32_16x16x32_bf16 v[160:163], v[200:203], a[84:87], v[160:163]\n"
    "v_cvt_pk_bf16_f32 v150, v152, v153\n"
    "v_mfma_f32_16x16x32_bf16 v[160:163], v[204:207], a[88:91], v[160:163]\n"
    "buffer_load_dwordx4 v248, s[8:11], s40 offen offset:2048 sc1 lds\n"
    "v_cvt_pk_bf16_f32 v151, v154, v155\n"
    "v_mfma_f32_16x16x32_bf16 v[160:163], v[208:211], a[92:95], v[160:163]\n"
    "s_waitcnt vmcnt(17)\n"
    "v_exp_f32_e32 v156, v156\n"
    "v_mfma_f32_16x16x32_bf16 v[164:167], a[160:163], a[96:99], v[232:235]\n"
    "buffer_load_dwordx4 v248, s[8:11], s40 offen offset:3072 sc1 lds\n"
    "v_mfma_f32_16x16x32_bf16 v[164:167], a[164:167], a[100:103], v[164:167]\n"
    "s_waitcnt vmcnt(16)\n"
    "v_exp_f32_e32 v157, v157\n"
    "v_mfma_f32_16x16x32_bf16 v[164:167], a[168:171], a[104:107], v[164:167]\n"
    "s_add_u32 s40, s40, 0x8000\n"
    "v_exp_f32_e32 v158, v158\n"
    "v_mfma_f32_16x16x32_bf16 v[164:167], a[172:175], a[108:111], v[164:167]\n"
    "s_waitcnt vmcnt(14)\n"
    "v_exp_f32_e32 v159, v159\n"
    "v_mfma_f32_16x16x32_bf16 v[164:167], a[176:179], a[112:115], v[164:167]\n"
    "s_add_u32 m0, s43, 20480\n"
    "v_cvt_pk_bf16_f32 v156, v156, v157\n"
    "v_mfma_f32_16x16x32_bf16 v[164:167], a[180:183], a[116:119], v[164:167]\n"
    "s_waitcnt vmcnt(12)\n"
    "buffer_load_dwordx4 v248, s[8:11], s41 offen offset:0 sc1 lds\n"
    "v_cvt_pk_bf16_f32 v157, v158, v159\n"
    "v_mfma_f32_16x16x32_bf16 v[164:167], a[184:187], a[120:123], v[164:167]\n"
    "v_exp_f32_e32 v160, v160\n"
    "v_mfma_f32_16x16x32_bf16 v[164:167], a[188:191], a[124:127], v[164:167]\n"
    "buffer_load_dwordx4 v248, s[8:11], s41 offen offset:1024 sc1 lds\n"
    "v_mfma_f32_16x16x32_bf16 v[168:171], v[180:183], a[96:99], v[232:235]\n"
    "v_exp_f32_e32 v161, v161\n"
    "v_mfma_f32_16x16x32_bf16 v[168:171], v[184:187], a[100:103], v[168:171]\n"
    "buffer_load_dwordx4 v248, s[8:11], s41 offen offset:2048 sc1 lds\n"
    "v_mfma_f32_16x16x32_bf16 v[168:171], v[188:191], a[104:107], v[168:171]\n"
    "v_exp_f32_e32 v162, v162\n"
    "v_mfma_f32_16x16x32_bf16 v[168:171], v[192:195], a[108:111], v[168:171]\n"
    "buffer_load_dwordx4 v248, s[8:11], s41 offen offset:3072 sc1 lds\n"
    "v_mfma_f32_16x16x32_bf16 v[168:171], v[196:199], a[112:115], v[168:171]\n"
    "s_add_u32 s41, s41, 0x8000\n"
    "v_exp_f32_e32 v163, v163\n"
    "v_mfma_f32_16x16x32_bf16 v[168:171], v[200:203], a[116:119], v[168:171]\n"
    "s_barrier\n"
    "v_cvt_pk_bf16_f32 v158, v160, v161\n"
    "v_cvt_pk_bf16_f32 v159, v162, v163\n"
    "v_mfma_f32_16x16x32_bf16 v[168:171], v[204:207], a[120:123], v[168:171]\n"
    "s_mov_b32 m0, s42\n"
    "v_exp_f32_e32 v164, v164\n"
    "v_mfma_f32_16x16x32_bf16 v[168:171], v[208:211], a[124:127], v[168:171]\n"
    "s_waitcnt vmcnt(14)\n"
    "v_exp_f32_e32 v165, v165\n"
    "v_mfma_f32_16x16x32_bf16 v[172:175], a[160:163], a[128:131], v[236:239]\n"
    "v_exp_f32_e32 v166, v166\n"
    "v_mfma_f32_16x16x32_bf16 v[172:175], a[164:167], a[132:135], v[172:175]\n"
    "s_waitcnt vmcnt(12)\n"
    "ds_read_b128 a[160:163], v240 offset:8192\n"
    "ds_write_addtid_b32 v140 offset:0\n"
    "v_mfma_f32_16x16x32_bf16 v[172:175], a[168:171], a[136:139], v[172:175]\n"
    "ds_read_b128 a[164:167], v241 offset:8192\n"
    "ds_write_addtid_b32 v141 offset:256\n"
    "v_mfma_f32_16x16x32_bf16 v[172:175], a[172:175], a[140:143], v[172:175]\n"
    "s_waitcnt vmcnt(10)\n"
    "ds_read_b128 a[168:171], v240 offset:12288\n"
    "ds_write_addtid_b32 v142 offset:512\n"
    "v_mfma_f32_16x16x32_bf16 v[172:175], a[176:179], a[144:147], v[172:175]\n"
    "ds_read_b128 a[172:175], v241 offset:12288\n"
    "ds_write_addtid_b32 v148 offset:1024\n"
    "v_mfma_f32_16x16x32_bf16 v[172:175], a[180:183], a[148:151], v[172:175]\n"
    "s_waitcnt vmcnt(8)\n"
    "ds_read_b128 a[176:179], v242 offset:8192\n"
    "ds_write_addtid_b32 v149 offset:1280\n"
    "v_mfma_f32_16x16x32_bf16 v[172:175], a[184:187], a[152:155], v[172:175]\n"
    "ds_read_b128 a[180:183], v243 offset:8192\n"
    "ds_write_addtid_b32 v156 offset:2048\n"
    "v_mfma_f32_16x16x32_bf16 v[172:175], a[188:191], a[156:159], v[172:175]\n"
    "v_exp_f32_e32 v167, v167\n"
    "v_mfma_f32_16x16x32_bf16 v[176:179], v[180:183], a[128:131], v[236:239]\n"
    "v_cvt_pk_bf16_f32 v164, v164, v165\n"
    "v_cvt_pk_bf16_f32 v165, v166, v167\n"
    "v_mfma_f32_16x16x32_bf16 v[176:179], v[184:187], a[132:135], v[176:179]\n"
    "v_exp_f32_e32 v168, v168\n"
    "v_mfma_f32_16x16x32_bf16 v[176:179], v[188:191], a[136:139], v[176:179]\n"
    "v_exp_f32_e32 v169, v169\n"
    "v_mfma_f32_16x16x32_bf16 v[176:179], v[192:195], a[140:143], v[176:179]\n"
    "ds_write_addtid_b32 v157 offset:2304\n"
    "v_exp_f32_e32 v172, v172\n"
    "v_mfma_f32_16x16x32_bf16 v[176:179], v[196:199], a[144:147], v[176:179]\n"
    "v_exp_f32_e32 v173, v173\n"
    "v_mfma_f32_16x16x32_bf16 v[176:179], v[200:203], a[148:151], v[176:179]\n"
    "ds_write_addtid_b32 v164 offset:3072\n"
    "v_exp_f32_e32 v174, v174\n"
    "v_mfma_f32_16x16x32_bf16 v[176:179], v[204:207], a[152:155], v[176:179]\n"
    "v_exp_f32_e32 v175, v175\n"
    "v_mfma_f32_16x16x32_bf16 v[176:179], v[208:211], a[156:159], v[176:179]\n"
    "ds_write_addtid_b32 v165 offset:3328\n"
    "v_cvt_pk_bf16_f32 v172, v172, v173\n"
    "v_cvt_pk_bf16_f32 v173, v174, v175\n"
    "ds_write_addtid_b32 v172 offset:4096\n"
    "ds_write_addtid_b32 v173 offset:4352\n"
    "s_waitcnt lgkmcnt(14)\n"
    "s_branch .Lat_in\n"
    ".p2align 6\n"
    ".Lat_loop:\n"
    "v_mfma_f32_16x16x32_bf16 v[172:175], a[160:163], a[128:131], v[236:239]\n"
    "v_mfma_f32_16x16x32_bf16 v[172:175], a[164:167], a[132:135], v[172:175]\n"
    "v_exp_f32_e32 v210, v210\n"
    "v_mfma_f32_16x16x32_bf16 v[164:167], a[164:167], a[100:103], v[232:235]\n"
    "ds_write_addtid_b32 v183 offset:768\n"
    "v_mfma_f32_16x16x32_bf16 v[164:167], a[160:163], a[96:99], v[164:167]\n"
    "ds_read_b128 a[184:187], v242 offset:4096\n"
    "v_mfma_f32_16x16x32_bf16 v[156:159], a[160:163], a[64:67], v[228:231]\n"
    "v_exp_f32_e32 v211, v211\n"
    "v_mfma_f32_16x16x32_bf16 v[156:159], a[164:167], a[68:71], v[156:159]\n"
    "ds_write_addtid_b32 v190 offset:1536\n"
    "v_mfma_f32_16x16x32_bf16 v[148:151], a[164:167], a[36:39], v[224:227]\n"
    "v_cvt_pk_bf16_f32 v206, v208, v209\n"
    "v_mfma_f32_16x16x32_bf16 v[148:151], a[160:163], a[32:35], v[148:151]\n"
    "v_mfma_f32_16x16x32_bf16 v[140:143], a[160:163], a[0:3], v[220:223]\n"
    "ds_write_addtid_b32 v191 offset:1792\n"
    "v_mfma_f32_16x16x32_bf16 v[140:143], a[164:167], a[4:7], v[140:143]\n"
    "s_waitcnt lgkmcnt(10)\n"
    "v_exp_f32_e32 v216, v216\n"
    "v_mfma_f32_16x16x32_bf16 v[140:143], a[168:171], a[8:11], v[140:143]\n"
    "ds_read_b128 a[188:191], v243 offset:4096\n"
    "v_mfma_f32_16x16x32_bf16 v[140:143], a[172:175], a[12:15], v[140:143]\n"
    "v_exp_f32_e32 v217, v217\n"
    "v_mfma_f32_16x16x32_bf16 v[148:151], a[172:175], a[44:47], v[148:151]\n"
    "ds_write_addtid_b32 v198 offset:2560\n"
    "v_mfma_f32_16x16x32_bf16 v[148:151], a[168:171], a[40:43], v[148:151]\n"
    "ds_read_b128 a[160:163], v240 offset:2048\n"
    "v_mfma_f32_16x16x32_bf16 v[156:159], a[168:171], a[72:75], v[156:159]\n"
    "v_cvt_pk_bf16_f32 v207, v210, v211\n"
    "v_mfma_f32_16x16x32_bf16 v[156:159], a[172:175], a[76:79], v[156:159]\n"
    "ds_write_addtid_b32 v199 offset:2816\n"
    "v_mfma_f32_16x16x32_bf16 v[164:167], a[172:175], a[108:111], v[164:167]\n"
    "v_exp_f32_e32 v218, v218\n"
    "v_mfma_f32_16x16x32_bf16 v[164:167], a[168:171], a[104:107], v[164:167]\n"
    "ds_write_addtid_b32 v206 offset:3584\n"
    "v_mfma_f32_16x16x32_bf16 v[172:175], a[168:171], a[136:139], v[172:175]\n"
    "v_mfma_f32_16x16x32_bf16 v[172:175], a[172:175], a[140:143], v[172:175]\n"
    "s_waitcnt lgkmcnt(10)\n"
    "v_exp_f32_e32 v219, v219\n"
    "v_mfma_f32_16x16x32_bf16 v[172:175], a[176:179], a[144:147], v[172:175]\n"
    "ds_read_b128 a[164:167], v241 offset:2048\n"
    "v_mfma_f32_16x16x32_bf16 v[172:175], a[180:183], a[148:151], v[172:175]\n"
    "v_cvt_pk_bf16_f32 v214, v216, v217\n"
    "v_mfma_f32_16x16x32_bf16 v[164:167], a[180:183], a[116:119], v[164:167]\n"
    "ds_write_addtid_b32 v207 offset:3840\n"
    "v_mfma_f32_16x16x32_bf16 v[164:167], a[176:179], a[112:115], v[164:167]\n"
    "ds_read_b128 a[168:171], v240 offset:6144\n"
    "v_mfma_f32_16x16x32_bf16 v[156:159], a[176:179], a[80:83], v[156:159]\n"
    "v_cvt_pk_bf16_f32 v215, v218, v219\n"
    "v_mfma_f32_16x16x32_bf16 v[156:159], a[180:183], a[84:87], v[156:159]\n"
    "ds_write_addtid_b32 v214 offset:4608\n"
    "v_mfma_f32_16x16x32_bf16 v[148:151], a[180:183], a[52:55], v[148:151]\n"
    "ds_write_addtid_b32 v215 offset:4864\n"
    "v_mfma_f32_16x16x32_bf16 v[148:151], a[176:179], a[48:51], v[148:151]\n"
    "v_mfma_f32_16x16x32_bf16 v[140:143], a[176:179], a[16:19], v[140:143]\n"
    "ds_read_b128 a[172:175], v241 offset:6144\n"
    "v_mfma_f32_16x16x32_bf16 v[140:143], a[180:183], a[20:23], v[140:143]\n"
    "s_waitcnt lgkmcnt(10)\n"
    "s_waitcnt lgkmcnt(1)\n"
    "v_mfma_f32_16x16x32_bf16 v[140:143], a[184:187], a[24:27], v[140:143]\n"
    "s_barrier\n"
    "v_mfma_f32_16x16x32_bf16 v[140:143], a[188:191], a[28:31], v[140:143]\n"
    "s_add_u32 m0, s43, 16384\n"
    "v_mfma_f32_16x16x32_bf16 v[148:151], a[188:191], a[60:63], v[148:151]\n"
    "ds_read_b128 a[176:179], v242 offset:2048\n"
    "v_mfma_f32_16x16x32_bf16 v[148:151], a[184:187], a[56:59], v[148:151]\n"
    "buffer_load_dwordx4 v248, s[8:11], s40 offen offset:0 sc1 lds\n"
    "v_mfma_f32_16x16x32_bf16 v[156:159], a[184:187], a[88:91], v[156:159]\n"
    "ds_read2st64_b32 v[184:185], v249 offset0:0 offset1:1\n"
    "v_mfma_f32_16x16x32_bf16 v[156:159], a[188:191], a[92:95], v[156:159]\n"
    "v_mfma_f32_16x16x32_bf16 v[164:167], a[188:191], a[124:127], v[164:167]\n"
    "v_mfma_f32_16x16x32_bf16 v[164:167], a[184:187], a[120:123], v[164:167]\n"
    "ds_read2st64_b32 v[186:187], v249 offset0:2 offset1:3\n"
    "v_mfma_f32_16x16x32_bf16 v[172:175], a[184:187], a[152:155], v[172:175]\n"
    "buffer_load_dwordx4 v248, s[8:11], s40 offen offset:1024 sc1 lds\n"
    "v_mfma_f32_16x16x32_bf16 v[172:175], a[188:191], a[156:159], v[172:175]\n"
    "v_mfma_f32_16x16x32_bf16 v[176:179], a[160:163], a[128:131], v[236:239]\n"
    "ds_read_b128 a[180:183], v243 offset:2048\n"
    "v_mfma_f32_16x16x32_bf16 v[176:179], a[164:167], a[132:135], v[176:179]\n"
    "ds_read2st64_b32 v[192:193], v249 offset0:4 offset1:5\n"
    "v_mfma_f32_16x16x32_bf16 v[168:171], a[164:167], a[100:103], v[232:235]\n"
    "ds_read_b128 a[184:187], v242 offset:6144\n"
    "v_mfma_f32_16x16x32_bf16 v[168:171], a[160:163], a[96:99], v[168:171]\n"
    "buffer_load_dwordx4 v248, s[8:11], s40 offen offset:2048 sc1 lds\n"
    "v_mfma_f32_16x16x32_bf16 v[160:163], a[160:163], a[64:67], v[228:231]\n"
    "v_exp_f32_e32 v140, v140\n"
    "v_mfma_f32_16x16x32_bf16 v[160:163], a[164:167], a[68:71], v[160:163]\n"
    "ds_read2st64_b32 v[194:195], v249 offset0:6 offset1:7\n"
    "v_mfma_f32_16x16x32_bf16 v[152:155], a[164:167], a[36:39], v[224:227]\n"
    "v_exp_f32_e32 v141, v141\n"
    "v_mfma_f32_16x16x32_bf16 v[152:155], a[160:163], a[32:35], v[152:155]\n"
    "v_exp_f32_e32 v142, v142\n"
    "v_mfma_f32_16x16x32_bf16 v[144:147], a[160:163], a[0:3], v[220:223]\n"
    "buffer_load_dwordx4 v248, s[8:11], s40 offen offset:3072 sc1 lds\n"
    "v_mfma_f32_16x16x32_bf16 v[144:147], a[164:167], a[4:7], v[144:147]\n"
    "s_waitcnt lgkmcnt(7)\n"
    "ds_read2st64_b32 v[200:201], v249 offset0:8 offset1:9\n"
    "v_mfma_f32_16x16x32_bf16 v[144:147], a[168:171], a[8:11], v[144:147]\n"
    "s_add_u32 s40, s40, 0x8000\n"
    "ds_read_b128 a[188:191], v243 offset:6144\n"
    "v_mfma_f32_16x16x32_bf16 v[144:147], a[172:175], a[12:15], v[144:147]\n"
    "s_add_u32 m0, s43, 20480\n"
    "ds_read_b64_tr_b16 a[160:161], v244 offset:24576\n"
    "v_mfma_f32_16x16x32_bf16 v[152:155], a[172:175], a[44:47], v[152:155]\n"
    "ds_read2st64_b32 v[202:203], v249 offset0:10 offset1:11\n"
    "v_mfma_f32_16x16x32_bf16 v[152:155], a[168:171], a[40:43], v[152:155]\n"
    "buffer_load_dwordx4 v248, s[8:11], s41 offen offset:0 sc1 lds\n"
    "v_mfma_f32_16x16x32_bf16 v[160:163], a[168:171], a[72:75], v[160:163]\n"
    "ds_read_b64_tr_b16 a[162:163], v244 offset:26624\n"
    "v_mfma_f32_16x16x32_bf16 v[160:163], a[172:175], a[76:79], v[160:163]\n"
    "v_exp_f32_e32 v143, v143\n"
    "v_mfma_f32_16x16x32_bf16 v[168:171], a[172:175], a[108:111], v[168:171]\n"
    "ds_read2st64_b32 v[208:209], v249 offset0:12 offset1:13\n"
    "v_cvt_pk_bf16_f32 v140, v140, v141\n"
    "v_mfma_f32_16x16x32_bf16 v[168:171], a[168:171], a[104:107], v[168:171]\n"
    "ds_read_b64_tr_b16 a[164:165], v244 offset:57344\n"
    "v_mfma_f32_16x16x32_bf16 v[176:179], a[168:171], a[136:139], v[176:179]\n"
    "buffer_load_dwordx4 v248, s[8:11], s41 offen offset:1024 sc1 lds\n"
    "v_mfma_f32_16x16x32_bf16 v[176:179], a[172:175], a[140:143], v[176:179]\n"
    "s_waitcnt lgkmcnt(10)\n"
    "ds_read2st64_b32 v[210:211], v249 offset0:14 offset1:15\n"
    "v_mfma_f32_16x16x32_bf16 v[176:179], a[176:179], a[144:147], v[176:179]\n"
    "ds_read_b64_tr_b16 a[168:169], v245 offset:24576\n"
    "v_mfma_f32_16x16x32_bf16 v[176:179], a[180:183], a[148:151], v[176:179]\n"
    "ds_read_b64_tr_b16 a[166:167], v244 offset:59392\n"
    "v_mfma_f32_16x16x32_bf16 v[168:171], a[180:183], a[116:119], v[168:171]\n"
    "v_exp_f32_e32 v148, v148\n"
    "v_mfma_f32_16x16x32_bf16 v[168:171], a[176:179], a[112:115], v[168:171]\n"
    "buffer_load_dwordx4 v248, s[8:11], s41 offen offset:2048 sc1 lds\n"
    "v_mfma_f32_16x16x32_bf16 v[160:163], a[176:179], a[80:83], v[160:163]\n"
    "ds_read_b64_tr_b16 a[170:171], v245 offset:26624\n"
    "v_mfma_f32_16x16x32_bf16 v[160:163], a[180:183], a[84:87], v[160:163]\n"
    "ds_read2st64_b32 v[216:217], v249 offset0:16 offset1:17\n"
    "v_mfma_f32_16x16x32_bf16 v[152:155], a[180:183], a[52:55], v[152:155]\n"
    "v_exp_f32_e32 v149, v149\n"
    "v_mfma_f32_16x16x32_bf16 v[152:155], a[176:179], a[48:51], v[152:155]\n"
    "ds_read_b64_tr_b16 a[172:173], v245 offset:57344\n"
    "v_mfma_f32_16x16x32_bf16 v[144:147], a[176:179], a[16:19], v[144:147]\n"
    "buffer_load_dwordx4 v248, s[8:11], s41 offen offset:3072 sc1 lds\n"
    "v_mfma_f32_16x16x32_bf16 v[144:147], a[180:183], a[20:23], v[144:147]\n"
    "s_waitcnt lgkmcnt(11)\n"
    "ds_read2st64_b32 v[218:219], v249 offset0:18 offset1:19\n"
    "v_mfma_f32_16x16x32_bf16 v[144:147], a[184:187], a[24:27], v[144:147]\n"
    "s_add_u32 s41, s41, 0x8000\n"
    "ds_read_b64_tr_b16 a[176:177], v246 offset:24576\n"
    "v_mfma_f32_16x16x32_bf16 v[144:147], a[188:191], a[28:31], v[144:147]\n"
    "v_mfma_f32_16x16x32_bf16 v[152:155], a[188:191], a[60:63], v[152:155]\n"
    "ds_read_b64_tr_b16 a[174:175], v245 offset:59392\n"
    "v_mfma_f32_16x16x32_bf16 v[152:155], a[184:187], a[56:59], v[152:155]\n"
    "v_cvt_pk_bf16_f32 v141, v142, v143\n"
    "v_mfma_f32_16x16x32_bf16 v[160:163], a[184:187], a[88:91], v[160:163]\n"
    "ds_read_b64_tr_b16 a[178:179], v246 offset:26624\n"
    "v_mfma_f32_16x16x32_bf16 v[160:163], a[188:191], a[92:95], v[160:163]\n"
    "v_exp_f32_e32 v150, v150\n"
    "v_mfma_f32_16x16x32_bf16 v[168:171], a[188:191], a[124:127], v[168:171]\n"
    "v_exp_f32_e32 v151, v151\n"
    "v_mfma_f32_16x16x32_bf16 v[168:171], a[184:187], a[120:123], v[168:171]\n"
    "v_cvt_pk_bf16_f32 v148, v148, v149\n"
    "v_mfma_f32_16x16x32_bf16 v[176:179], a[184:187], a[152:155], v[176:179]\n"
    "ds_read_b64_tr_b16 a[180:181], v246 offset:57344\n"
    "v_mfma_f32_16x16x32_bf16 v[176:179], a[188:191], a[156:159], v[176:179]\n"
    "s_waitcnt lgkmcnt(4)\n"
    "v_exp_f32_e32 v156, v156\n"
    "v_mfma_f32_16x16x32_bf16 a[236:239], a[160:163], v[180:183], a[236:239]\n"
    "ds_read_b64_tr_b16 a[184:185], v247 offset:24576\n"
    "v_mfma_f32_16x16x32_bf16 a[236:239], a[164:167], v[184:187], a[236:239]\n"
    "ds_read_b64_tr_b16 a[182:183], v246 offset:59392\n"
    "v_mfma_f32_16x16x32_bf16 a[240:243], a[164:167], v[192:195], a[240:243]\n"
    "v_exp_f32_e32 v157, v157\n"
    "v_mfma_f32_16x16x32_bf16 a[240:243], a[160:163], v[188:191], a[240:243]\n"
    "v_cvt_pk_bf16_f32 v149, v150, v151\n"
    "v_mfma_f32_16x16x32_bf16 a[244:247], a[160:163], v[196:199], a[244:247]\n"
    "ds_read_b64_tr_b16 a[186:187], v247 offset:26624\n"
    "v_mfma_f32_16x16x32_bf16 a[244:247], a[164:167], v[200:203], a[244:247]\n"
    "v_exp_f32_e32 v158, v158\n"
    "v_mfma_f32_16x16x32_bf16 a[248:251], a[164:167], v[208:211], a[248:251]\n"
    "v_exp_f32_e32 v159, v159\n"
    "v_mfma_f32_16x16x32_bf16 a[248:251], a[160:163], v[204:207], a[248:251]\n"
    "v_cvt_pk_bf16_f32 v156, v156, v157\n"
    "v_mfma_f32_16x16x32_bf16 a[252:255], a[160:163], v[212:215], a[252:255]\n"
    "ds_read_b64_tr_b16 a[188:189], v247 offset:57344\n"
    "v_mfma_f32_16x16x32_bf16 a[252:255], a[164:167], v[216:219], a[252:255]\n"
    "s_waitcnt lgkmcnt(6)\n"
    "v_exp_f32_e32 v164, v164\n"
    "v_mfma_f32_16x16x32_bf16 v[16:19], a[172:175], v[216:219], v[16:19]\n"
    "ds_read_b64_tr_b16 a[160:161], v244 offset:28672\n"
    "v_mfma_f32_16x16x32_bf16 v[16:19], a[168:171], v[212:215], v[16:19]\n"
    "ds_read_b64_tr_b16 a[190:191], v247 offset:59392\n"
    "v_mfma_f32_16x16x32_bf16 v[12:15], a[168:171], v[204:207], v[12:15]\n"
    "v_exp_f32_e32 v165, v165\n"
    "v_mfma_f32_16x16x32_bf16 v[12:15], a[172:175], v[208:211], v[12:15]\n"
    "v_cvt_pk_bf16_f32 v157, v158, v159\n"
    "v_mfma_f32_16x16x32_bf16 v[8:11], a[172:175], v[200:203], v[8:11]\n"
    "ds_read_b64_tr_b16 a[162:163], v244 offset:30720\n"
    "v_mfma_f32_16x16x32_bf16 v[8:11], a[168:171], v[196:199], v[8:11]\n"
    "v_exp_f32_e32 v166, v166\n"
    "v_mfma_f32_16x16x32_bf16 v[4:7], a[168:171], v[188:191], v[4:7]\n"
    "v_exp_f32_e32 v167, v167\n"
    "v_mfma_f32_16x16x32_bf16 v[4:7], a[172:175], v[192:195], v[4:7]\n"
    "v_cvt_pk_bf16_f32 v164, v164, v165\n"
    "v_mfma_f32_16x16x32_bf16 v[0:3], a[172:175], v[184:187], v[0:3]\n"
    "ds_read_b64_tr_b16 a[164:165], v244 offset:61440\n"
    "v_mfma_f32_16x16x32_bf16 v[0:3], a[168:171], v[180:183], v[0:3]\n"
    "s_waitcnt lgkmcnt(6)\n"
    "v_exp_f32_e32 v172, v172\n"
    "v_mfma_f32_16x16x32_bf16 v[20:23], a[176:179], v[180:183], v[20:23]\n"
    "ds_read_b64_tr_b16 a[168:169], v245 offset:28672\n"
    "v_mfma_f32_16x16x32_bf16 v[20:23], a[180:183], v[184:187], v[20:23]\n"
    "ds_read_b64_tr_b16 a[166:167], v244 offset:63488\n"
    "v_mfma_f32_16x16x32_bf16 v[24:27], a[180:183], v[192:195], v[24:27]\n"
    "v_exp_f32_e32 v173, v173\n"
    "v_mfma_f32_16x16x32_bf16 v[24:27], a[176:179], v[188:191], v[24:27]\n"
    "v_cvt_pk_bf16_f32 v165, v166, v167\n"
    "v_mfma_f32_16x16x32_bf16 v[28:31], a[176:179], v[196:199], v[28:31]\n"
    "ds_read_b64_tr_b16 a[170:171], v245 offset:30720\n"
    "v_mfma_f32_16x16x32_bf16 v[28:31], a[180:183], v[200:203], v[28:31]\n"
    "v_exp_f32_e32 v174, v174\n"
    "v_mfma_f32_16x16x32_bf16 v[32:35], a[180:183], v[208:211], v[32:35]\n"
    "v_mfma_f32_16x16x32_bf16 v[32:35], a[176:179], v[204:207], v[32:35]\n"
    "v_exp_f32_e32 v175, v175\n"
    "v_mfma_f32_16x16x32_bf16 v[36:39], a[176:179], v[212:215], v[36:39]\n"
    "ds_read_b64_tr_b16 a[172:173], v245 offset:61440\n"
    "v_mfma_f32_16x16x32_bf16 v[36:39], a[180:183], v[216:219], v[36:39]\n"
    "s_waitcnt lgkmcnt(6)\n"
    "v_cvt_pk_bf16_f32 v172, v172, v173\n"
    "v_mfma_f32_16x16x32_bf16 v[56:59], a[188:191], v[216:219], v[56:59]\n"
    "ds_read_b64_tr_b16 a[176:177], v246 offset:28672\n"
    "v_mfma_f32_16x16x32_bf16 v[56:59], a[184:187], v[212:215], v[56:59]\n"
    "ds_read_b64_tr_b16 a[174:175], v245 offset:63488\n"
    "v_mfma_f32_16x16x32_bf16 v[52:55], a[184:187], v[204:207], v[52:55]\n"
    "v_cvt_pk_bf16_f32 v173, v174, v175\n"
    "v_mfma_f32_16x16x32_bf16 v[52:55], a[188:191], v[208:211], v[52:55]\n"
    "v_exp_f32_e32 v144, v144\n"
    "v_mfma_f32_16x16x32_bf16 v[48:51], a[188:191], v[200:203], v[48:51]\n"
    "ds_read_b64_tr_b16 a[178:179], v246 offset:30720\n"
    "v_mfma_f32_16x16x32_bf16 v[48:51], a[184:187], v[196:199], v[48:51]\n"
    "v_exp_f32_e32 v145, v145\n"
    "v_mfma_f32_16x16x32_bf16 v[44:47], a[184:187], v[188:191], v[44:47]\n"
    "v_mfma_f32_16x16x32_bf16 v[44:47], a[188:191], v[192:195], v[44:47]\n"
    "v_mfma_f32_16x16x32_bf16 v[40:43], a[188:191], v[184:187], v[40:43]\n"
    "ds_read_b64_tr_b16 a[180:181], v246 offset:61440\n"
    "v_mfma_f32_16x16x32_bf16 v[40:43], a[184:187], v[180:183], v[40:43]\n"
    "s_waitcnt lgkmcnt(6)\n"
    "v_exp_f32_e32 v146, v146\n"
    "v_mfma_f32_16x16x32_bf16 v[60:63], a[160:163], v[180:183], v[60:63]\n"
    "ds_read_b64_tr_b16 a[184:185], v247 offset:28672\n"
    "v_mfma_f32_16x16x32_bf16 v[60:63], a[164:167], v[184:187], v[60:63]\n"
    "ds_read_b64_tr_b16 a[182:183], v246 offset:63488\n"
    "v_mfma_f32_16x16x32_bf16 v[64:67], a[164:167], v[192:195], v[64:67]\n"
    "ds_read_b64_tr_b16 a[186:187], v247 offset:30720\n"
    "v_mfma_f32_16x16x32_bf16 v[64:67], a[160:163], v[188:191], v[64:67]\n"
    "v_exp_f32_e32 v147, v147\n"
    "v_mfma_f32_16x16x32_bf16 v[68:71], a[160:163], v[196:199], v[68:71]\n"
    "v_cvt_pk_bf16_f32 v142, v144, v145\n"
    "v_mfma_f32_16x16x32_bf16 v[68:71], a[164:167], v[200:203], v[68:71]\n"
    "ds_read_b64_tr_b16 a[188:189], v247 offset:61440\n"
    "v_mfma_f32_16x16x32_bf16 v[72:75], a[164:167], v[208:211], v[72:75]\n"
    "ds_read_b64_tr_b16 a[190:191], v247 offset:63488\n"
    "v_mfma_f32_16x16x32_bf16 v[72:75], a[160:163], v[204:207], v[72:75]\n"
    "s_waitcnt vmcnt(10)\n"
    "v_mfma_f32_16x16x32_bf16 v[76:79], a[160:163], v[212:215], v[76:79]\n"
    "s_barrier\n"
    "v_mfma_f32_16x16x32_bf16 v[76:79], a[164:167], v[216:219], v[76:79]\n"
    "s_waitcnt lgkmcnt(7)\n"
    "s_mov_b32 m0, s42\n"
    "v_mfma_f32_16x16x32_bf16 v[96:99], a[172:175], v[216:219], v[96:99]\n"
    "ds_write_addtid_b32 v140 offset:0\n"
    "v_mfma_f32_16x16x32_bf16 v[96:99], a[168:171], v[212:215], v[96:99]\n"
    "v_mfma_f32_16x16x32_bf16 v[92:95], a[168:171], v[204:207], v[92:95]\n"
    "v_exp_f32_e32 v152, v152\n"
    "v_mfma_f32_16x16x32_bf16 v[92:95], a[172:175], v[208:211], v[92:95]\n"
    "ds_read_b128 a[160:163], v240 offset:8192\n"
    "v_mfma_f32_16x16x32_bf16 v[88:91], a[172:175], v[200:203], v[88:91]\n"
    "v_exp_f32_e32 v153, v153\n"
    "v_mfma_f32_16x16x32_bf16 v[88:91], a[168:171], v[196:199], v[88:91]\n"
    "ds_write_addtid_b32 v141 offset:256\n"
    "v_cvt_pk_bf16_f32 v143, v146, v147\n"
    "v_mfma_f32_16x16x32_bf16 v[84:87], a[168:171], v[188:191], v[84:87]\n"
    "v_exp_f32_e32 v154, v154\n"
    "v_mfma_f32_16x16x32_bf16 v[84:87], a[172:175], v[192:195], v[84:87]\n"
    "v_exp_f32_e32 v155, v155\n"
    "v_mfma_f32_16x16x32_bf16 v[80:83], a[172:175], v[184:187], v[80:83]\n"
    "ds_write_addtid_b32 v148 offset:1024\n"
    "v_cvt_pk_bf16_f32 v150, v152, v153\n"
    "v_mfma_f32_16x16x32_bf16 v[80:83], a[168:171], v[180:183], v[80:83]\n"
    "s_waitcnt lgkmcnt(7)\n"
    "v_exp_f32_e32 v160, v160\n"
    "v_mfma_f32_16x16x32_bf16 v[100:103], a[176:179], v[180:183], v[100:103]\n"
    "ds_read_b128 a[164:167], v241 offset:8192\n"
    "v_mfma_f32_16x16x32_bf16 v[100:103], a[180:183], v[184:187], v[100:103]\n"
    "ds_write_addtid_b32 v149 offset:1280\n"
    "v_mfma_f32_16x16x32_bf16 v[104:107], a[180:183], v[192:195], v[104:107]\n"
    "v_exp_f32_e32 v161, v161\n"
    "v_mfma_f32_16x16x32_bf16 v[104:107], a[176:179], v[188:191], v[104:107]\n"
    "ds_read_b128 a[168:171], v240 offset:12288\n"
    "v_mfma_f32_16x16x32_bf16 v[108:111], a[176:179], v[196:199], v[108:111]\n"
    "ds_write_addtid_b32 v156 offset:2048\n"
    "v_mfma_f32_16x16x32_bf16 v[108:111], a[180:183], v[200:203], v[108:111]\n"
    "v_cvt_pk_bf16_f32 v151, v154, v155\n"
    "v_mfma_f32_16x16x32_bf16 v[112:115], a[180:183], v[208:211], v[112:115]\n"
    "ds_write_addtid_b32 v157 offset:2304\n"
    "v_mfma_f32_16x16x32_bf16 v[112:115], a[176:179], v[204:207], v[112:115]\n"
    "v_exp_f32_e32 v162, v162\n"
    "v_mfma_f32_16x16x32_bf16 v[116:119], a[176:179], v[212:215], v[116:119]\n"
    "ds_write_addtid_b32 v164 offset:3072\n"
    "v_mfma_f32_16x16x32_bf16 v[116:119], a[180:183], v[216:219], v[116:119]\n"
    "s_waitcnt lgkmcnt(10)\n"
    "v_exp_f32_e32 v163, v163\n"
    "v_mfma_f32_16x16x32_bf16 v[136:139], a[188:191], v[216:219], v[136:139]\n"
    "ds_read_b128 a[172:175], v241 offset:12288\n"
    "v_mfma_f32_16x16x32_bf16 v[136:139], a[184:187], v[212:215], v[136:139]\n"
    "ds_write_addtid_b32 v165 offset:3328\n"
    "v_cvt_pk_bf16_f32 v158, v160, v161\n"
    "v_mfma_f32_16x16x32_bf16 v[132:135], a[184:187], v[204:207], v[132:135]\n"
    "ds_read_b128 a[176:179], v242 offset:8192\n"
    "v_mfma_f32_16x16x32_bf16 v[132:135], a[188:191], v[208:211], v[132:135]\n"
    "ds_write_addtid_b32 v172 offset:4096\n"
    "v_mfma_f32_16x16x32_bf16 v[128:131], a[188:191], v[200:203], v[128:131]\n"
    "v_exp_f32_e32 v168, v168\n"
    "v_mfma_f32_16x16x32_bf16 v[128:131], a[184:187], v[196:199], v[128:131]\n"
    "ds_write_addtid_b32 v173 offset:4352\n"
    "v_mfma_f32_16x16x32_bf16 v[124:127], a[184:187], v[188:191], v[124:127]\n"
    "v_exp_f32_e32 v169, v169\n"
    "v_mfma_f32_16x16x32_bf16 v[124:127], a[188:191], v[192:195], v[124:127]\n"
    "ds_read_b128 a[180:183], v243 offset:8192\n"
    "v_mfma_f32_16x16x32_bf16 v[120:123], a[188:191], v[184:187], v[120:123]\n"
    "ds_write_addtid_b32 v142 offset:512\n"
    "v_mfma_f32_16x16x32_bf16 v[120:123], a[184:187], v[180:183], v[120:123]\n"
    "s_waitcnt lgkmcnt(12)\n"
    "v_cvt_pk_bf16_f32 v159, v162, v163\n"
    ".Lat_in:\n"
    "v_mfma_f32_16x16x32_bf16 v[212:215], a[160:163], a[128:131], v[236:239]\n"
    "v_mfma_f32_16x16x32_bf16 v[212:215], a[164:167], a[132:135], v[212:215]\n"
    "v_exp_f32_e32 v170, v170\n"
    "v_mfma_f32_16x16x32_bf16 v[204:207], a[164:167], a[100:103], v[232:235]\n"
    "ds_write_addtid_b32 v143 offset:768\n"
    "v_mfma_f32_16x16x32_bf16 v[204:207], a[160:163], a[96:99], v[204:207]\n"
    "ds_read_b128 a[184:187], v242 offset:12288\n"
    "v_mfma_f32_16x16x32_bf16 v[196:199], a[160:163], a[64:67], v[228:231]\n"
    "v_exp_f32_e32 v171, v171\n"
    "v_mfma_f32_16x16x32_bf16 v[196:199], a[164:167], a[68:71], v[196:199]\n"
    "ds_write_addtid_b32 v150 offset:1536\n"
    "v_mfma_f32_16x16x32_bf16 v[188:191], a[164:167], a[36:39], v[224:227]\n"
    "v_cvt_pk_bf16_f32 v166, v168, v169\n"
    "v_mfma_f32_16x16x32_bf16 v[188:191], a[160:163], a[32:35], v[188:191]\n"
    "v_mfma_f32_16x16x32_bf16 v[180:183], a[160:163], a[0:3], v[220:223]\n"
    "ds_write_addtid_b32 v151 offset:1792\n"
    "v_mfma_f32_16x16x32_bf16 v[180:183], a[164:167], a[4:7], v[180:183]\n"
    "s_waitcnt lgkmcnt(10)\n"
    "v_exp_f32_e32 v176, v176\n"
    "v_mfma_f32_16x16x32_bf16 v[180:183], a[168:171], a[8:11], v[180:183]\n"
    "ds_read_b128 a[188:191], v243 offset:12288\n"
    "v_mfma_f32_16x16x32_bf16 v[180:183], a[172:175], a[12:15], v[180:183]\n"
    "v_exp_f32_e32 v177, v177\n"
    "v_mfma_f32_16x16x32_bf16 v[188:191], a[172:175], a[44:47], v[188:191]\n"
    "ds_write_addtid_b32 v158 offset:2560\n"
    "v_mfma_f32_16x16x32_bf16 v[188:191], a[168:171], a[40:43], v[188:191]\n"
    "ds_read_b128 a[160:163], v240 offset:10240\n"
    "v_mfma_f32_16x16x32_bf16 v[196:199], a[168:171], a[72:75], v[196:199]\n"
    "v_cvt_pk_bf16_f32 v167, v170, v171\n"
    "v_mfma_f32_16x16x32_bf16 v[196:199], a[172:175], a[76:79], v[196:199]\n"
    "ds_write_addtid_b32 v159 offset:2816\n"
    "v_mfma_f32_16x16x32_bf16 v[204:207], a[172:175], a[108:111], v[204:207]\n"
    "v_exp_f32_e32 v178, v178\n"
    "v_mfma_f32_16x16x32_bf16 v[204:207], a[168:171], a[104:107], v[204:207]\n"
    "ds_write_addtid_b32 v166 offset:3584\n"
    "v_mfma_f32_16x16x32_bf16 v[212:215], a[168:171], a[136:139], v[212:215]\n"
    "v_mfma_f32_16x16x32_bf16 v[212:215], a[172:175], a[140:143], v[212:215]\n"
    "s_waitcnt lgkmcnt(10)\n"
    "v_exp_f32_e32 v179, v179\n"
    "v_mfma_f32_16x16x32_bf16 v[212:215], a[176:179], a[144:147], v[212:215]\n"
    "ds_read_b128 a[164:167], v241 offset:10240\n"
    "v_mfma_f32_16x16x32_bf16 v[212:215], a[180:183], a[148:151], v[212:215]\n"
    "v_cvt_pk_bf16_f32 v174, v176, v177\n"
    "v_mfma_f32_16x16x32_bf16 v[204:207], a[180:183], a[116:119], v[204:207]\n"
    "ds_write_addtid_b32 v167 offset:3840\n"
    "v_mfma_f32_16x16x32_bf16 v[204:207], a[176:179], a[112:115], v[204:207]\n"
    "ds_read_b128 a[168:171], v240 offset:14336\n"
    "v_mfma_f32_16x16x32_bf16 v[196:199], a[176:179], a[80:83], v[196:199]\n"
    "v_cvt_pk_bf16_f32 v175, v178, v179\n"
    "v_mfma_f32_16x16x32_bf16 v[196:199], a[180:183], a[84:87], v[196:199]\n"
    "ds_write_addtid_b32 v174 offset:4608\n"
    "v_mfma_f32_16x16x32_bf16 v[188:191], a[180:183], a[52:55], v[188:191]\n"
    "ds_write_addtid_b32 v175 offset:4864\n"
    "v_mfma_f32_16x16x32_bf16 v[188:191], a[176:179], a[48:51], v[188:191]\n"
    "v_mfma_f32_16x16x32_bf16 v[180:183], a[176:179], a[16:19], v[180:183]\n"
    "ds_read_b128 a[172:175], v241 offset:14336\n"
    "v_mfma_f32_16x16x32_bf16 v[180:183], a[180:183], a[20:23], v[180:183]\n"
    "s_waitcnt lgkmcnt(10)\n"
    "s_waitcnt lgkmcnt(1)\n"
    "v_mfma_f32_16x16x32_bf16 v[180:183], a[184:187], a[24:27], v[180:183]\n"
    "s_barrier\n"
    "v_mfma_f32_16x16x32_bf16 v[180:183], a[188:191], a[28:31], v[180:183]\n"
    "s_add_u32 m0, s43, 24576\n"
    "v_mfma_f32_16x16x32_bf16 v[188:191], a[188:191], a[60:63], v[188:191]\n"
    "ds_read_b128 a[176:179], v242 offset:10240\n"
    "v_mfma_f32_16x16x32_bf16 v[188:191], a[184:187], a[56:59], v[188:191]\n"
    "buffer_load_dwordx4 v248, s[8:11], s40 offen offset:0 sc1 lds\n"
    "v_mfma_f32_16x16x32_bf16 v[196:199], a[184:187], a[88:91], v[196:199]\n"
    "ds_read2st64_b32 v[144:145], v249 offset0:0 offset1:1\n"
    "v_mfma_f32_16x16x32_bf16 v[196:199], a[188:191], a[92:95], v[196:199]\n"
    "v_mfma_f32_16x16x32_bf16 v[204:207], a[188:191], a[124:127], v[204:207]\n"
    "v_mfma_f32_16x16x32_bf16 v[204:207], a[184:187], a[120:123], v[204:207]\n"
    "ds_read2st64_b32 v[146:147], v249 offset0:2 offset1:3\n"
    "v_mfma_f32_16x16x32_bf16 v[212:215], a[184:187], a[152:155], v[212:215]\n"
    "buffer_load_dwordx4 v248, s[8:11], s40 offen offset:1024 sc1 lds\n"
    "v_mfma_f32_16x16x32_bf16 v[212:215], a[188:191], a[156:159], v[212:215]\n"
    "v_mfma_f32_16x16x32_bf16 v[216:219], a[160:163], a[128:131], v[236:239]\n"
    "ds_read_b128 a[180:183], v243 offset:10240\n"
    "v_mfma_f32_16x16x32_bf16 v[216:219], a[164:167], a[132:135], v[216:219]\n"
    "ds_read2st64_b32 v[152:153], v249 offset0:4 offset1:5\n"
    "v_mfma_f32_16x16x32_bf16 v[208:211], a[164:167], a[100:103], v[232:235]\n"
    "ds_read_b128 a[184:187], v242 offset:14336\n"
    "v_mfma_f32_16x16x32_bf16 v[208:211], a[160:163], a[96:99], v[208:211]\n"
    "buffer_load_dwordx4 v248, s[8:11], s40 offen offset:2048 sc1 lds\n"
    "v_mfma_f32_16x16x32_bf16 v[200:203], a[160:163], a[64:67], v[228:231]\n"
    "v_exp_f32_e32 v180, v180\n"
    "v_mfma_f32_16x16x32_bf16 v[200:203], a[164:167], a[68:71], v[200:203]\n"
    "ds_read2st64_b32 v[154:155], v249 offset0:6 offset1:7\n"
    "v_mfma_f32_16x16x32_bf16 v[192:195], a[164:167], a[36:39], v[224:227]\n"
    "v_exp_f32_e32 v181, v181\n"
    "v_mfma_f32_16x16x32_bf16 v[192:195], a[160:163], a[32:35], v[192:195]\n"
    "v_exp_f32_e32 v182, v182\n"
    "v_mfma_f32_16x16x32_bf16 v[184:187], a[160:163], a[0:3], v[220:223]\n"
    "buffer_load_dwordx4 v248, s[8:11], s40 offen offset:3072 sc1 lds\n"
    "v_mfma_f32_16x16x32_bf16 v[184:187], a[164:167], a[4:7], v[184:187]\n"
    "s_waitcnt lgkmcnt(7)\n"
    "ds_read2st64_b32 v[160:161], v249 offset0:8 offset1:9\n"
    "v_mfma_f32_16x16x32_bf16 v[184:187], a[168:171], a[8:11], v[184:187]\n"
    "s_add_u32 s40, s40, 0x8000\n"
    "ds_read_b128 a[188:191], v243 offset:14336\n"
    "v_mfma_f32_16x16x32_bf16 v[184:187], a[172:175], a[12:15], v[184:187]\n"
    "s_add_u32 m0, s43, 28672\n"
    "ds_read_b64_tr_b16 a[160:161], v244 offset:0\n"
    "v_mfma_f32_16x16x32_bf16 v[192:195], a[172:175], a[44:47], v[192:195]\n"
    "ds_read2st64_b32 v[162:163], v249 offset0:10 offset1:11\n"
    "v_mfma_f32_16x16x32_bf16 v[192:195], a[168:171], a[40:43], v[192:195]\n"
    "buffer_load_dwordx4 v248, s[8:11], s41 offen offset:0 sc1 lds\n"
    "v_mfma_f32_16x16x32_bf16 v[200:203], a[168:171], a[72:75], v[200:203]\n"
    "ds_read_b64_tr_b16 a[162:163], v244 offset:2048\n"
    "v_mfma_f32_16x16x32_bf16 v[200:203], a[172:175], a[76:79], v[200:203]\n"
    "v_exp_f32_e32 v183, v183\n"
    "v_mfma_f32_16x16x32_bf16 v[208:211], a[172:175], a[108:111], v[208:211]\n"
    "ds_read2st64_b32 v[168:169], v249 offset0:12 offset1:13\n"
    "v_cvt_pk_bf16_f32 v180, v180, v181\n"
    "v_mfma_f32_16x16x32_bf16 v[208:211], a[168:171], a[104:107], v[208:211]\n"
    "ds_read_b64_tr_b16 a[164:165], v244 offset:32768\n"
    "v_mfma_f32_16x16x32_bf16 v[216:219], a[168:171], a[136:139], v[216:219]\n"
    "buffer_load_dwordx4 v248, s[8:11], s41 offen offset:1024 sc1 lds\n"
    "v_mfma_f32_16x16x32_bf16 v[216:219], a[172:175], a[140:143], v[216:219]\n"
    "s_waitcnt lgkmcnt(10)\n"
    "ds_read2st64_b32 v[170:171], v249 offset0:14 offset1:15\n"
    "v_mfma_f32_16x16x32_bf16 v[216:219], a[176:179], a[144:147], v[216:219]\n"
    "ds_read_b64_tr_b16 a[168:169], v245 offset:0\n"
    "v_mfma_f32_16x16x32_bf16 v[216:219], a[180:183], a[148:151], v[216:219]\n"
    "ds_read_b64_tr_b16 a[166:167], v244 offset:34816\n"
    "v_mfma_f32_16x16x32_bf16 v[208:211], a[180:183], a[116:119], v[208:211]\n"
    "v_exp_f32_e32 v188, v188\n"
    "v_mfma_f32_16x16x32_bf16 v[208:211], a[176:179], a[112:115], v[208:211]\n"
    "buffer_load_dwordx4 v248, s[8:11], s41 offen offset:2048 sc1 lds\n"
    "v_mfma_f32_16x16x32_bf16 v[200:203], a[176:179], a[80:83], v[200:203]\n"
    "ds_read_b64_tr_b16 a[170:171], v245 offset:2048\n"
    "v_mfma_f32_16x16x32_bf16 v[200:203], a[180:183], a[84:87], v[200:203]\n"
    "ds_read2st64_b32 v[176:177], v249 offset0:16 offset1:17\n"
    "v_mfma_f32_16x16x32_bf16 v[192:195], a[180:183], a[52:55], v[192:195]\n"
    "v_exp_f32_e32 v189, v189\n"
    "v_mfma_f32_16x16x32_bf16 v[192:195], a[176:179], a[48:51], v[192:195]\n"
    "ds_read_b64_tr_b16 a[172:173], v245 offset:32768\n"
    "v_mfma_f32_16x16x32_bf16 v[184:187], a[176:179], a[16:19], v[184:187]\n"
    "buffer_load_dwordx4 v248, s[8:11], s41 offen offset:3072 sc1 lds\n"
    "v_mfma_f32_16x16x32_bf16 v[184:187], a[180:183], a[20:23], v[184:187]\n"
    "s_waitcnt lgkmcnt(11)\n"
    "ds_read2st64_b32 v[178:179], v249 offset0:18 offset1:19\n"
    "v_mfma_f32_16x16x32_bf16 v[184:187], a[184:187], a[24:27], v[184:187]\n"
    "s_add_u32 s41, s41, 0x8000\n"
    "ds_read_b64_tr_b16 a[176:177], v246 offset:0\n"
    "v_mfma_f32_16x16x32_bf16 v[184:187], a[188:191], a[28:31], v[184:187]\n"
    "v_mfma_f32_16x16x32_bf16 v[192:195], a[188:191], a[60:63], v[192:195]\n"
    "ds_read_b64_tr_b16 a[174:175], v245 offset:34816\n"
    "v_mfma_f32_16x16x32_bf16 v[192:195], a[184:187], a[56:59], v[192:195]\n"
    "v_cvt_pk_bf16_f32 v181, v182, v183\n"
    "v_mfma_f32_16x16x32_bf16 v[200:203], a[184:187], a[88:91], v[200:203]\n"
    "ds_read_b64_tr_b16 a[178:179], v246 offset:2048\n"
    "v_mfma_f32_16x16x32_bf16 v[200:203], a[188:191], a[92:95], v[200:203]\n"
    "v_exp_f32_e32 v190, v190\n"
    "v_mfma_f32_16x16x32_bf16 v[208:211], a[188:191], a[124:127], v[208:211]\n"
    "v_exp_f32_e32 v191, v191\n"
    "v_mfma_f32_16x16x32_bf16 v[208:211], a[184:187], a[120:123], v[208:211]\n"
    "v_cvt_pk_bf16_f32 v188, v188, v189\n"
    "v_mfma_f32_16x16x32_bf16 v[216:219], a[184:187], a[152:155], v[216:219]\n"
    "ds_read_b64_tr_b16 a[180:181], v246 offset:32768\n"
    "v_mfma_f32_16x16x32_bf16 v[216:219], a[188:191], a[156:159], v[216:219]\n"
    "s_waitcnt lgkmcnt(4)\n"
    "v_exp_f32_e32 v196, v196\n"
    "v_mfma_f32_16x16x32_bf16 a[236:239], a[160:163], v[140:143], a[236:239]\n"
    "ds_read_b64_tr_b16 a[184:185], v247 offset:0\n"
    "v_mfma_f32_16x16x32_bf16 a[236:239], a[164:167], v[144:147], a[236:239]\n"
    "ds_read_b64_tr_b16 a[182:183], v246 offset:34816\n"
    "v_mfma_f32_16x16x32_bf16 a[240:243], a[164:167], v[152:155], a[240:243]\n"
    "v_exp_f32_e32 v197, v197\n"
    "v_mfma_f32_16x16x32_bf16 a[240:243], a[160:163], v[148:151], a[240:243]\n"
    "v_cvt_pk_bf16_f32 v189, v190, v191\n"
    "v_mfma_f32_16x16x32_bf16 a[244:247], a[160:163], v[156:159], a[244:247]\n"
    "ds_read_b64_tr_b16 a[186:187], v247 offset:2048\n"
    "v_mfma_f32_16x16x32_bf16 a[244:247], a[164:167], v[160:163], a[244:247]\n"
    "v_exp_f32_e32 v198, v198\n"
    "v_mfma_f32_16x16x32_bf16 a[248:251], a[164:167], v[168:171], a[248:251]\n"
    "v_exp_f32_e32 v199, v199\n"
    "v_mfma_f32_16x16x32_bf16 a[248:251], a[160:163], v[164:167], a[248:251]\n"
    "v_cvt_pk_bf16_f32 v196, v196, v197\n"
    "v_mfma_f32_16x16x32_bf16 a[252:255], a[160:163], v[172:175], a[252:255]\n"
    "ds_read_b64_tr_b16 a[188:189], v247 offset:32768\n"
    "v_mfma_f32_16x16x32_bf16 a[252:255], a[164:167], v[176:179], a[252:255]\n"
    "s_waitcnt lgkmcnt(6)\n"
    "v_exp_f32_e32 v204, v204\n"
    "v_mfma_f32_16x16x32_bf16 v[16:19], a[172:175], v[176:179], v[16:19]\n"
    "ds_read_b64_tr_b16 a[160:161], v244 offset:4096\n"
    "v_mfma_f32_16x16x32_bf16 v[16:19], a[168:171], v[172:175], v[16:19]\n"
    "ds_read_b64_tr_b16 a[190:191], v247 offset:34816\n"
    "v_mfma_f32_16x16x32_bf16 v[12:15], a[168:171], v[164:167], v[12:15]\n"
    "v_exp_f32_e32 v205, v205\n"
    "v_mfma_f32_16x16x32_bf16 v[12:15], a[172:175], v[168:171], v[12:15]\n"
    "v_cvt_pk_bf16_f32 v197, v198, v199\n"
    "v_mfma_f32_16x16x32_bf16 v[8:11], a[172:175], v[160:163], v[8:11]\n"
    "ds_read_b64_tr_b16 a[162:163], v244 offset:6144\n"
    "v_mfma_f32_16x16x32_bf16 v[8:11], a[168:171], v[156:159], v[8:11]\n"
    "v_exp_f32_e32 v206, v206\n"
    "v_mfma_f32_16x16x32_bf16 v[4:7], a[168:171], v[148:151], v[4:7]\n"
    "v_exp_f32_e32 v207, v207\n"
    "v_mfma_f32_16x16x32_bf16 v[4:7], a[172:175], v[152:155], v[4:7]\n"
    "v_cvt_pk_bf16_f32 v204, v204, v205\n"
    "v_mfma_f32_16x16x32_bf16 v[0:3], a[172:175], v[144:147], v[0:3]\n"
    "ds_read_b64_tr_b16 a[164:165], v244 offset:36864\n"
    "v_mfma_f32_16x16x32_bf16 v[0:3], a[168:171], v[140:143], v[0:3]\n"
    "s_waitcnt lgkmcnt(6)\n"
    "v_exp_f32_e32 v212, v212\n"
    "v_mfma_f32_16x16x32_bf16 v[20:23], a[176:179], v[140:143], v[20:23]\n"
    "ds_read_b64_tr_b16 a[168:169], v245 offset:4096\n"
    "v_mfma_f32_16x16x32_bf16 v[20:23], a[180:183], v[144:147], v[20:23]\n"
    "ds_read_b64_tr_b16 a[166:167], v244 offset:38912\n"
    "v_mfma_f32_16x16x32_bf16 v[24:27], a[180:183], v[152:155], v[24:27]\n"
    "v_exp_f32_e32 v213, v213\n"
    "v_mfma_f32_16x16x32_bf16 v[24:27], a[176:179], v[148:151], v[24:27]\n"
    "v_cvt_pk_bf16_f32 v205, v206, v207\n"
    "v_mfma_f32_16x16x32_bf16 v[28:31], a[176:179], v[156:159], v[28:31]\n"
    "ds_read_b64_tr_b16 a[170:171], v245 offset:6144\n"
    "v_mfma_f32_16x16x32_bf16 v[28:31], a[180:183], v[160:163], v[28:31]\n"
    "v_exp_f32_e32 v214, v214\n"
    "v_mfma_f32_16x16x32_bf16 v[32:35], a[180:183], v[168:171], v[32:35]\n"
    "v_mfma_f32_16x16x32_bf16 v[32:35], a[176:179], v[164:167], v[32:35]\n"
    "v_exp_f32_e32 v215, v215\n"
    "v_mfma_f32_16x16x32_bf16 v[36:39], a[176:179], v[172:175], v[36:39]\n"
    "ds_read_b64_tr_b16 a[172:173], v245 offset:36864\n"
    "v_mfma_f32_16x16x32_bf16 v[36:39], a[180:183], v[176:179], v[36:39]\n"
    "s_waitcnt lgkmcnt(6)\n"
    "v_cvt_pk_bf16_f32 v212, v212, v213\n"
    "v_mfma_f32_16x16x32_bf16 v[56:59], a[188:191], v[176:179], v[56:59]\n"
    "ds_read_b64_tr_b16 a[176:177], v246 offset:4096\n"
    "v_mfma_f32_16x16x32_bf16 v[56:59], a[184:187], v[172:175], v[56:59]\n"
    "ds_read_b64_tr_b16 a[174:175], v245 offset:38912\n"
    "v_mfma_f32_16x16x32_bf16 v[52:55], a[184:187], v[164:167], v[52:55]\n"
    "v_cvt_pk_bf16_f32 v213, v214, v215\n"
    "v_mfma_f32_16x16x32_bf16 v[52:55], a[188:191], v[168:171], v[52:55]\n"
    "v_exp_f32_e32 v184, v184\n"
    "v_mfma_f32_16x16x32_bf16 v[48:51], a[188:191], v[160:163], v[48:51]\n"
    "ds_read_b64_tr_b16 a[178:179], v246 offset:6144\n"
    "v_mfma_f32_16x16x32_bf16 v[48:51], a[184:187], v[156:159], v[48:51]\n"
    "v_exp_f32_e32 v185, v185\n"
    "v_mfma_f32_16x16x32_bf16 v[44:47], a[184:187], v[148:151], v[44:47]\n"
    "v_mfma_f32_16x16x32_bf16 v[44:47], a[188:191], v[152:155], v[44:47]\n"
    "v_mfma_f32_16x16x32_bf16 v[40:43], a[188:191], v[144:147], v[40:43]\n"
    "ds_read_b64_tr_b16 a[180:181], v246 offset:36864\n"
    "v_mfma_f32_16x16x32_bf16 v[40:43], a[184:187], v[140:143], v[40:43]\n"
    "s_waitcnt lgkmcnt(6)\n"
    "v_exp_f32_e32 v186, v186\n"
    "v_mfma_f32_16x16x32_bf16 v[60:63], a[160:163], v[140:143], v[60:63]\n"
    "ds_read_b64_tr_b16 a[184:185], v247 offset:4096\n"
    "v_mfma_f32_16x16x32_bf16 v[60:63], a[164:167], v[144:147], v[60:63]\n"
    "ds_read_b64_tr_b16 a[182:183], v246 offset:38912\n"
    "v_mfma_f32_16x16x32_bf16 v[64:67], a[164:167], v[152:155], v[64:67]\n"
    "ds_read_b64_tr_b16 a[186:187], v247 offset:6144\n"
    "v_mfma_f32_16x16x32_bf16 v[64:67], a[160:163], v[148:151], v[64:67]\n"
    "v_exp_f32_e32 v187, v187\n"
    "v_mfma_f32_16x16x32_bf16 v[68:71], a[160:163], v[156:159], v[68:71]\n"
    "v_cvt_pk_bf16_f32 v182, v184, v185\n"
    "v_mfma_f32_16x16x32_bf16 v[68:71], a[164:167], v[160:163], v[68:71]\n"
    "ds_read_b64_tr_b16 a[188:189], v247 offset:36864\n"
    "v_mfma_f32_16x16x32_bf16 v[72:75], a[164:167], v[168:171], v[72:75]\n"
    "ds_read_b64_tr_b16 a[190:191], v247 offset:38912\n"
    "v_mfma_f32_16x16x32_bf16 v[72:75], a[160:163], v[164:167], v[72:75]\n"
    "s_waitcnt vmcnt(8)\n"
    "v_mfma_f32_16x16x32_bf16 v[76:79], a[160:163], v[172:175], v[76:79]\n"
    "s_barrier\n"
    "v_mfma_f32_16x16x32_bf16 v[76:79], a[164:167], v[176:179], v[76:79]\n"
    "s_waitcnt lgkmcnt(7)\n"
    "s_mov_b32 m0, s42\n"
    "v_mfma_f32_16x16x32_bf16 v[96:99], a[172:175], v[176:179], v[96:99]\n"
    "ds_write_addtid_b32 v180 offset:0\n"
    "v_mov_b32_e32 v253, s39\n"
    "v_mfma_f32_16x16x32_bf16 v[96:99], a[168:171], v[172:175], v[96:99]\n"
    "buffer_store_dword v253, off, s[8:11], s84 sc1\n"
    "v_mfma_f32_16x16x32_bf16 v[92:95], a[168:171], v[164:167], v[92:95]\n"
    "s_mov_b32 s84, 2.0\n"
    "v_exp_f32_e32 v192, v192\n"
    "v_mfma_f32_16x16x32_bf16 v[92:95], a[172:175], v[168:171], v[92:95]\n"
    "ds_read_b128 a[160:163], v240 offset:16384\n"
    "v_mfma_f32_16x16x32_bf16 v[88:91], a[172:175], v[160:163], v[88:91]\n"
    "v_exp_f32_e32 v193, v193\n"
    "v_mfma_f32_16x16x32_bf16 v[88:91], a[168:171], v[156:159], v[88:91]\n"
    "ds_write_addtid_b32 v181 offset:256\n"
    "v_cvt_pk_bf16_f32 v183, v186, v187\n"
    "v_mfma_f32_16x16x32_bf16 v[84:87], a[168:171], v[148:151], v[84:87]\n"
    "v_exp_f32_e32 v194, v194\n"
    "v_mfma_f32_16x16x32_bf16 v[84:87], a[172:175], v[152:155], v[84:87]\n"
    "v_exp_f32_e32 v195, v195\n"
    "v_mfma_f32_16x16x32_bf16 v[80:83], a[172:175], v[144:147], v[80:83]\n"
    "ds_write_addtid_b32 v188 offset:1024\n"
    "v_cvt_pk_bf16_f32 v190, v192, v193\n"
    "v_mfma_f32_16x16x32_bf16 v[80:83], a[168:171], v[140:143], v[80:83]\n"
    "s_waitcnt lgkmcnt(7)\n"
    "v_exp_f32_e32 v200, v200\n"
    "v_mfma_f32_16x16x32_bf16 v[100:103], a[176:179], v[140:143], v[100:103]\n"
    "ds_read_b128 a[164:167], v241 offset:16384\n"
    "v_mfma_f32_16x16x32_bf16 v[100:103], a[180:183], v[144:147], v[100:103]\n"
    "ds_write_addtid_b32 v189 offset:1280\n"
    "v_mfma_f32_16x16x32_bf16 v[104:107], a[180:183], v[152:155], v[104:107]\n"
    "v_exp_f32_e32 v201, v201\n"
    "v_mfma_f32_16x16x32_bf16 v[104:107], a[176:179], v[148:151], v[104:107]\n"
    "ds_read_b128 a[168:171], v240 offset:20480\n"
    "v_mfma_f32_16x16x32_bf16 v[108:111], a[176:179], v[156:159], v[108:111]\n"
    "ds_write_addtid_b32 v196 offset:2048\n"
    "v_mfma_f32_16x16x32_bf16 v[108:111], a[180:183], v[160:163], v[108:111]\n"
    "v_cvt_pk_bf16_f32 v191, v194, v195\n"
    "v_mfma_f32_16x16x32_bf16 v[112:115], a[180:183], v[168:171], v[112:115]\n"
    "ds_write_addtid_b32 v197 offset:2304\n"
    "v_mfma_f32_16x16x32_bf16 v[112:115], a[176:179], v[164:167], v[112:115]\n"
    "v_exp_f32_e32 v202, v202\n"
    "v_mfma_f32_16x16x32_bf16 v[116:119], a[176:179], v[172:175], v[116:119]\n"
    "ds_write_addtid_b32 v204 offset:3072\n"
    "v_mfma_f32_16x16x32_bf16 v[116:119], a[180:183], v[176:179], v[116:119]\n"
    "s_waitcnt lgkmcnt(10)\n"
    "v_exp_f32_e32 v203, v203\n"
    "v_mfma_f32_16x16x32_bf16 v[136:139], a[188:191], v[176:179], v[136:139]\n"
    "ds_read_b128 a[172:175], v241 offset:20480\n"
    "v_mfma_f32_16x16x32_bf16 v[136:139], a[184:187], v[172:175], v[136:139]\n"
    "ds_write_addtid_b32 v205 offset:3328\n"
    "v_cvt_pk_bf16_f32 v198, v200, v201\n"
    "v_mfma_f32_16x16x32_bf16 v[132:135], a[184:187], v[164:167], v[132:135]\n"
    "ds_read_b128 a[176:179], v242 offset:16384\n"
    "v_mfma_f32_16x16x32_bf16 v[132:135], a[188:191], v[168:171], v[132:135]\n"
    "ds_write_addtid_b32 v212 offset:4096\n"
    "v_mfma_f32_16x16x32_bf16 v[128:131], a[188:191], v[160:163], v[128:131]\n"
    "v_exp_f32_e32 v208, v208\n"
    "v_mfma_f32_16x16x32_bf16 v[128:131], a[184:187], v[156:159], v[128:131]\n"
    "ds_write_addtid_b32 v213 offset:4352\n"
    "v_mfma_f32_16x16x32_bf16 v[124:127], a[184:187], v[148:151], v[124:127]\n"
    "v_exp_f32_e32 v209, v209\n"
    "v_mfma_f32_16x16x32_bf16 v[124:127], a[188:191], v[152:155], v[124:127]\n"
    "ds_read_b128 a[180:183], v243 offset:16384\n"
    "v_mfma_f32_16x16x32_bf16 v[120:123], a[188:191], v[144:147], v[120:123]\n"
    "ds_write_addtid_b32 v182 offset:512\n"
    "v_mfma_f32_16x16x32_bf16 v[120:123], a[184:187], v[140:143], v[120:123]\n"
    "s_waitcnt lgkmcnt(12)\n"
    "v_cvt_pk_bf16_f32 v199, v202, v203\n"
    "v_mfma_f32_16x16x32_bf16 v[172:175], a[160:163], a[128:131], v[236:239]\n"
    "v_mfma_f32_16x16x32_bf16 v[172:175], a[164:167], a[132:135], v[172:175]\n"
    "v_exp_f32_e32 v210, v210\n"
    "v_mfma_f32_16x16x32_bf16 v[164:167], a[164:167], a[100:103], v[232:235]\n"
    "ds_write_addtid_b32 v183 offset:768\n"
    "v_mfma_f32_16x16x32_bf16 v[164:167], a[160:163], a[96:99], v[164:167]\n"
    "ds_read_b128 a[184:187], v242 offset:20480\n"
    "v_mfma_f32_16x16x32_bf16 v[156:159], a[160:163], a[64:67], v[228:231]\n"
    "v_exp_f32_e32 v211, v211\n"
    "v_mfma_f32_16x16x32_bf16 v[156:159], a[164:167], a[68:71], v[156:159]\n"
    "ds_write_addtid_b32 v190 offset:1536\n"
    "v_mfma_f32_16x16x32_bf16 v[148:151], a[164:167], a[36:39], v[224:227]\n"
    "v_cvt_pk_bf16_f32 v206, v208, v209\n"
    "v_mfma_f32_16x16x32_bf16 v[148:151], a[160:163], a[32:35], v[148:151]\n"
    "v_mfma_f32_16x16x32_bf16 v[140:143], a[160:163], a[0:3], v[220:223]\n"
    "ds_write_addtid_b32 v191 offset:1792\n"
    "v_mfma_f32_16x16x32_bf16 v[140:143], a[164:167], a[4:7], v[140:143]\n"
    "s_waitcnt lgkmcnt(10)\n"
    "v_exp_f32_e32 v216, v216\n"
    "v_mfma_f32_16x16x32_bf16 v[140:143], a[168:171], a[8:11], v[140:143]\n"
    "ds_read_b128 a[188:191], v243 offset:20480\n"
    "v_mfma_f32_16x16x32_bf16 v[140:143], a[172:175], a[12:15], v[140:143]\n"
    "v_exp_f32_e32 v217, v217\n"
    "v_mfma_f32_16x16x32_bf16 v[148:151], a[172:175], a[44:47], v[148:151]\n"
    "ds_write_addtid_b32 v198 offset:2560\n"
    "v_mfma_f32_16x16x32_bf16 v[148:151], a[168:171], a[40:43], v[148:151]\n"
    "ds_read_b128 a[160:163], v240 offset:18432\n"
    "v_mfma_f32_16x16x32_bf16 v[156:159], a[168:171], a[72:75], v[156:159]\n"
    "v_cvt_pk_bf16_f32 v207, v210, v211\n"
    "v_mfma_f32_16x16x32_bf16 v[156:159], a[172:175], a[76:79], v[156:159]\n"
    "ds_write_addtid_b32 v199 offset:2816\n"
    "v_mfma_f32_16x16x32_bf16 v[164:167], a[172:175], a[108:111], v[164:167]\n"
    "v_exp_f32_e32 v218, v218\n"
    "v_mfma_f32_16x16x32_bf16 v[164:167], a[168:171], a[104:107], v[164:167]\n"
    "ds_write_addtid_b32 v206 offset:3584\n"
    "v_mfma_f32_16x16x32_bf16 v[172:175], a[168:171], a[136:139], v[172:175]\n"
    "v_mfma_f32_16x16x32_bf16 v[172:175], a[172:175], a[140:143], v[172:175]\n"
    "s_waitcnt lgkmcnt(10)\n"
    "v_exp_f32_e32 v219, v219\n"
    "v_mfma_f32_16x16x32_bf16 v[172:175], a[176:179], a[144:147], v[172:175]\n"
    "ds_read_b128 a[164:167], v241 offset:18432\n"
    "v_mfma_f32_16x16x32_bf16 v[172:175], a[180:183], a[148:151], v[172:175]\n"
    "v_cvt_pk_bf16_f32 v214, v216, v217\n"
    "v_mfma_f32_16x16x32_bf16 v[164:167], a[180:183], a[116:119], v[164:167]\n"
    "ds_write_addtid_b32 v207 offset:3840\n"
    "v_mfma_f32_16x16x32_bf16 v[164:167], a[176:179], a[112:115], v[164:167]\n"
    "ds_read_b128 a[168:171], v240 offset:22528\n"
    "v_mfma_f32_16x16x32_bf16 v[156:159], a[176:179], a[80:83], v[156:159]\n"
    "v_cvt_pk_bf16_f32 v215, v218, v219\n"
    "v_mfma_f32_16x16x32_bf16 v[156:159], a[180:183], a[84:87], v[156:159]\n"
    "ds_write_addtid_b32 v214 offset:4608\n"
    "v_mfma_f32_16x16x32_bf16 v[148:151], a[180:183], a[52:55], v[148:151]\n"
    "ds_write_addtid_b32 v215 offset:4864\n"
    "v_mfma_f32_16x16x32_bf16 v[148:151], a[176:179], a[48:51], v[148:151]\n"
    "v_mfma_f32_16x16x32_bf16 v[140:143], a[176:179], a[16:19], v[140:143]\n"
    "ds_read_b128 a[172:175], v241 offset:22528\n"
    "v_mfma_f32_16x16x32_bf16 v[140:143], a[180:183], a[20:23], v[140:143]\n"
    "s_waitcnt lgkmcnt(10)\n"
    "s_waitcnt lgkmcnt(1)\n"
    "v_mfma_f32_16x16x32_bf16 v[140:143], a[184:187], a[24:27], v[140:143]\n"
    "s_barrier\n"
    "v_mfma_f32_16x16x32_bf16 v[140:143], a[188:191], a[28:31], v[140:143]\n"
    "s_add_u32 m0, s43, 0\n"
    "v_mfma_f32_16x16x32_bf16 v[148:151], a[188:191], a[60:63], v[148:151]\n"
    "ds_read_b128 a[176:179], v242 offset:18432\n"
    "v_mfma_f32_16x16x32_bf16 v[148:151], a[184:187], a[56:59], v[148:151]\n"
    "buffer_load_dwordx4 v248, s[8:11], s40 offen offset:0 sc1 lds\n"
    "v_mfma_f32_16x16x32_bf16 v[156:159], a[184:187], a[88:91], v[156:159]\n"
    "ds_read2st64_b32 v[184:185], v249 offset0:0 offset1:1\n"
    "v_mfma_f32_16x16x32_bf16 v[156:159], a[188:191], a[92:95], v[156:159]\n"
    "v_mfma_f32_16x16x32_bf16 v[164:167], a[188:191], a[124:127], v[164:167]\n"
    "v_mfma_f32_16x16x32_bf16 v[164:167], a[184:187], a[120:123], v[164:167]\n"
    "ds_read2st64_b32 v[186:187], v249 offset0:2 offset1:3\n"
    "v_mfma_f32_16x16x32_bf16 v[172:175], a[184:187], a[152:155], v[172:175]\n"
    "buffer_load_dwordx4 v248, s[8:11], s40 offen offset:1024 sc1 lds\n"
    "v_mfma_f32_16x16x32_bf16 v[172:175], a[188:191], a[156:159], v[172:175]\n"
    "v_mfma_f32_16x16x32_bf16 v[176:179], a[160:163], a[128:131], v[236:239]\n"
    "ds_read_b128 a[180:183], v243 offset:18432\n"
    "v_mfma_f32_16x16x32_bf16 v[176:179], a[164:167], a[132:135], v[176:179]\n"
    "ds_read2st64_b32 v[192:193], v249 offset0:4 offset1:5\n"
    "v_mfma_f32_16x16x32_bf16 v[168:171], a[164:167], a[100:103], v[232:235]\n"
    "ds_read_b128 a[184:187], v242 offset:22528\n"
    "v_mfma_f32_16x16x32_bf16 v[168:171], a[160:163], a[96:99], v[168:171]\n"
    "buffer_load_dwordx4 v248, s[8:11], s40 offen offset:2048 sc1 lds\n"
    "v_mfma_f32_16x16x32_bf16 v[160:163], a[160:163], a[64:67], v[228:231]\n"
    "v_exp_f32_e32 v140, v140\n"
    "v_mfma_f32_16x16x32_bf16 v[160:163], a[164:167], a[68:71], v[160:163]\n"
    "ds_read2st64_b32 v[194:195], v249 offset0:6 offset1:7\n"
    "v_mfma_f32_16x16x32_bf16 v[152:155], a[164:167], a[36:39], v[224:227]\n"
    "v_exp_f32_e32 v141, v141\n"
    "v_mfma_f32_16x16x32_bf16 v[152:155], a[160:163], a[32:35], v[152:155]\n"
    "v_exp_f32_e32 v142, v142\n"
    "v_mfma_f32_16x16x32_bf16 v[144:147], a[160:163], a[0:3], v[220:223]\n"
    "buffer_load_dwordx4 v248, s[8:11], s40 offen offset:3072 sc1 lds\n"
    "v_mfma_f32_16x16x32_bf16 v[144:147], a[164:167], a[4:7], v[144:147]\n"
    "s_waitcnt lgkmcnt(7)\n"
    "ds_read2st64_b32 v[200:201], v249 offset0:8 offset1:9\n"
    "v_mfma_f32_16x16x32_bf16 v[144:147], a[168:171], a[8:11], v[144:147]\n"
    "s_add_u32 s40, s40, 0x8000\n"
    "ds_read_b128 a[188:191], v243 offset:22528\n"
    "v_mfma_f32_16x16x32_bf16 v[144:147], a[172:175], a[12:15], v[144:147]\n"
    "s_add_u32 m0, s43, 4096\n"
    "ds_read_b64_tr_b16 a[160:161], v244 offset:8192\n"
    "v_mfma_f32_16x16x32_bf16 v[152:155], a[172:175], a[44:47], v[152:155]\n"
    "ds_read2st64_b32 v[202:203], v249 offset0:10 offset1:11\n"
    "v_mfma_f32_16x16x32_bf16 v[152:155], a[168:171], a[40:43], v[152:155]\n"
    "buffer_load_dwordx4 v248, s[8:11], s41 offen offset:0 sc1 lds\n"
    "v_mfma_f32_16x16x32_bf16 v[160:163], a[168:171], a[72:75], v[160:163]\n"
    "ds_read_b64_tr_b16 a[162:163], v244 offset:10240\n"
    "v_mfma_f32_16x16x32_bf16 v[160:163], a[172:175], a[76:79], v[160:163]\n"
    "v_exp_f32_e32 v143, v143\n"
    "v_mfma_f32_16x16x32_bf16 v[168:171], a[172:175], a[108:111], v[168:171]\n"
    "ds_read2st64_b32 v[208:209], v249 offset0:12 offset1:13\n"
    "v_cvt_pk_bf16_f32 v140, v140, v141\n"
    "v_mfma_f32_16x16x32_bf16 v[168:171], a[168:171], a[104:107], v[168:171]\n"
    "ds_read_b64_tr_b16 a[164:165], v244 offset:40960\n"
    "v_mfma_f32_16x16x32_bf16 v[176:179], a[168:171], a[136:139], v[176:179]\n"
    "buffer_load_dwordx4 v248, s[8:11], s41 offen offset:1024 sc1 lds\n"
    "v_mfma_f32_16x16x32_bf16 v[176:179], a[172:175], a[140:143], v[176:179]\n"
    "s_waitcnt lgkmcnt(10)\n"
    "ds_read2st64_b32 v[210:211], v249 offset0:14 offset1:15\n"
    "v_mfma_f32_16x16x32_bf16 v[176:179], a[176:179], a[144:147], v[176:179]\n"
    "ds_read_b64_tr_b16 a[168:169], v245 offset:8192\n"
    "v_mfma_f32_16x16x32_bf16 v[176:179], a[180:183], a[148:151], v[176:179]\n"
    "ds_read_b64_tr_b16 a[166:167], v244 offset:43008\n"
    "v_mfma_f32_16x16x32_bf16 v[168:171], a[180:183], a[116:119], v[168:171]\n"
    "v_exp_f32_e32 v148, v148\n"
    "v_mfma_f32_16x16x32_bf16 v[168:171], a[176:179], a[112:115], v[168:171]\n"
    "buffer_load_dwordx4 v248, s[8:11], s41 offen offset:2048 sc1 lds\n"
    "v_mfma_f32_16x16x32_bf16 v[160:163], a[176:179], a[80:83], v[160:163]\n"
    "ds_read_b64_tr_b16 a[170:171], v245 offset:10240\n"
    "v_mfma_f32_16x16x32_bf16 v[160:163], a[180:183], a[84:87], v[160:163]\n"
    "ds_read2st64_b32 v[216:217], v249 offset0:16 offset1:17\n"
    "v_mfma_f32_16x16x32_bf16 v[152:155], a[180:183], a[52:55], v[152:155]\n"
    "v_exp_f32_e32 v149, v149\n"
    "v_mfma_f32_16x16x32_bf16 v[152:155], a[176:179], a[48:51], v[152:155]\n"
    "ds_read_b64_tr_b16 a[172:173], v245 offset:40960\n"
    "v_mfma_f32_16x16x32_bf16 v[144:147], a[176:179], a[16:19], v[144:147]\n"
    "buffer_load_dwordx4 v248, s[8:11], s41 offen offset:3072 sc1 lds\n"
    "v_mfma_f32_16x16x32_bf16 v[144:147], a[180:183], a[20:23], v[144:147]\n"
    "s_waitcnt lgkmcnt(11)\n"
    "ds_read2st64_b32 v[218:219], v249 offset0:18 offset1:19\n"
    "v_mfma_f32_16x16x32_bf16 v[144:147], a[184:187], a[24:27], v[144:147]\n"
    "s_add_u32 s41, s41, 0x8000\n"
    "ds_read_b64_tr_b16 a[176:177], v246 offset:8192\n"
    "v_mfma_f32_16x16x32_bf16 v[144:147], a[188:191], a[28:31], v[144:147]\n"
    "v_mfma_f32_16x16x32_bf16 v[152:155], a[188:191], a[60:63], v[152:155]\n"
    "ds_read_b64_tr_b16 a[174:175], v245 offset:43008\n"
    "v_mfma_f32_16x16x32_bf16 v[152:155], a[184:187], a[56:59], v[152:155]\n"
    "v_cvt_pk_bf16_f32 v141, v142, v143\n"
    "v_mfma_f32_16x16x32_bf16 v[160:163], a[184:187], a[88:91], v[160:163]\n"
    "ds_read_b64_tr_b16 a[178:179], v246 offset:10240\n"
    "v_mfma_f32_16x16x32_bf16 v[160:163], a[188:191], a[92:95], v[160:163]\n"
    "v_exp_f32_e32 v150, v150\n"
    "v_mfma_f32_16x16x32_bf16 v[168:171], a[188:191], a[124:127], v[168:171]\n"
    "v_exp_f32_e32 v151, v151\n"
    "v_mfma_f32_16x16x32_bf16 v[168:171], a[184:187], a[120:123], v[168:171]\n"
    "v_cvt_pk_bf16_f32 v148, v148, v149\n"
    "v_mfma_f32_16x16x32_bf16 v[176:179], a[184:187], a[152:155], v[176:179]\n"
    "ds_read_b64_tr_b16 a[180:181], v246 offset:40960\n"
    "v_mfma_f32_16x16x32_bf16 v[176:179], a[188:191], a[156:159], v[176:179]\n"
    "s_waitcnt lgkmcnt(4)\n"
    "v_exp_f32_e32 v156, v156\n"
    "v_mfma_f32_16x16x32_bf16 a[236:239], a[160:163], v[180:183], a[236:239]\n"
    "ds_read_b64_tr_b16 a[184:185], v247 offset:8192\n"
    "v_mfma_f32_16x16x32_bf16 a[236:239], a[164:167], v[184:187], a[236:239]\n"
    "ds_read_b64_tr_b16 a[182:183], v246 offset:43008\n"
    "v_mfma_f32_16x16x32_bf16 a[240:243], a[164:167], v[192:195], a[240:243]\n"
    "v_exp_f32_e32 v157, v157\n"
    "v_mfma_f32_16x16x32_bf16 a[240:243], a[160:163], v[188:191], a[240:243]\n"
    "v_cvt_pk_bf16_f32 v149, v150, v151\n"
    "v_mfma_f32_16x16x32_bf16 a[244:247], a[160:163], v[196:199], a[244:247]\n"
    "ds_read_b64_tr_b16 a[186:187], v247 offset:10240\n"
    "v_mfma_f32_16x16x32_bf16 a[244:247], a[164:167], v[200:203], a[244:247]\n"
    "v_exp_f32_e32 v158, v158\n"
    "v_mfma_f32_16x16x32_bf16 a[248:251], a[164:167], v[208:211], a[248:251]\n"
    "v_exp_f32_e32 v159, v159\n"
    "v_mfma_f32_16x16x32_bf16 a[248:251], a[160:163], v[204:207], a[248:251]\n"
    "v_cvt_pk_bf16_f32 v156, v156, v157\n"
    "v_mfma_f32_16x16x32_bf16 a[252:255], a[160:163], v[212:215], a[252:255]\n"
    "ds_read_b64_tr_b16 a[188:189], v247 offset:40960\n"
    "v_mfma_f32_16x16x32_bf16 a[252:255], a[164:167], v[216:219], a[252:255]\n"
    "s_waitcnt lgkmcnt(6)\n"
    "v_exp_f32_e32 v164, v164\n"
    "v_mfma_f32_16x16x32_bf16 v[16:19], a[172:175], v[216:219], v[16:19]\n"
    "ds_read_b64_tr_b16 a[160:161], v244 offset:12288\n"
    "v_mfma_f32_16x16x32_bf16 v[16:19], a[168:171], v[212:215], v[16:19]\n"
    "ds_read_b64_tr_b16 a[190:191], v247 offset:43008\n"
    "v_mfma_f32_16x16x32_bf16 v[12:15], a[168:171], v[204:207], v[12:15]\n"
    "v_exp_f32_e32 v165, v165\n"
    "v_mfma_f32_16x16x32_bf16 v[12:15], a[172:175], v[208:211], v[12:15]\n"
    "v_cvt_pk_bf16_f32 v157, v158, v159\n"
    "v_mfma_f32_16x16x32_bf16 v[8:11], a[172:175], v[200:203], v[8:11]\n"
    "ds_read_b64_tr_b16 a[162:163], v244 offset:14336\n"
    "v_mfma_f32_16x16x32_bf16 v[8:11], a[168:171], v[196:199], v[8:11]\n"
    "v_exp_f32_e32 v166, v166\n"
    "v_mfma_f32_16x16x32_bf16 v[4:7], a[168:171], v[188:191], v[4:7]\n"
    "v_exp_f32_e32 v167, v167\n"
    "v_mfma_f32_16x16x32_bf16 v[4:7], a[172:175], v[192:195], v[4:7]\n"
    "v_cvt_pk_bf16_f32 v164, v164, v165\n"
    "v_mfma_f32_16x16x32_bf16 v[0:3], a[172:175], v[184:187], v[0:3]\n"
    "ds_read_b64_tr_b16 a[164:165], v244 offset:45056\n"
    "v_mfma_f32_16x16x32_bf16 v[0:3], a[168:171], v[180:183], v[0:3]\n"
    "s_waitcnt lgkmcnt(6)\n"
    "v_exp_f32_e32 v172, v172\n"
    "v_mfma_f32_16x16x32_bf16 v[20:23], a[176:179], v[180:183], v[20:23]\n"
    "ds_read_b64_tr_b16 a[168:169], v245 offset:12288\n"
    "v_mfma_f32_16x16x32_bf16 v[20:23], a[180:183], v[184:187], v[20:23]\n"
    "ds_read_b64_tr_b16 a[166:167], v244 offset:47104\n"
    "v_mfma_f32_16x16x32_bf16 v[24:27], a[180:183], v[192:195], v[24:27]\n"
    "v_exp_f32_e32 v173, v173\n"
    "v_mfma_f32_16x16x32_bf16 v[24:27], a[176:179], v[188:191], v[24:27]\n"
    "v_cvt_pk_bf16_f32 v165, v166, v167\n"
    "v_mfma_f32_16x16x32_bf16 v[28:31], a[176:179], v[196:199], v[28:31]\n"
    "ds_read_b64_tr_b16 a[170:171], v245 offset:14336\n"
    "v_mfma_f32_16x16x32_bf16 v[28:31], a[180:183], v[200:203], v[28:31]\n"
    "v_exp_f32_e32 v174, v174\n"
    "v_mfma_f32_16x16x32_bf16 v[32:35], a[180:183], v[208:211], v[32:35]\n"
    "v_mfma_f32_16x16x32_bf16 v[32:35], a[176:179], v[204:207], v[32:35]\n"
    "v_exp_f32_e32 v175, v175\n"
    "v_mfma_f32_16x16x32_bf16 v[36:39], a[176:179], v[212:215], v[36:39]\n"
    "ds_read_b64_tr_b16 a[172:173], v245 offset:45056\n"
    "v_mfma_f32_16x16x32_bf16 v[36:39], a[180:183], v[216:219], v[36:39]\n"
    "s_waitcnt lgkmcnt(6)\n"
    "v_cvt_pk_bf16_f32 v172, v172, v173\n"
    "v_mfma_f32_16x16x32_bf16 v[56:59], a[188:191], v[216:219], v[56:59]\n"
    "ds_read_b64_tr_b16 a[176:177], v246 offset:12288\n"
    "v_mfma_f32_16x16x32_bf16 v[56:59], a[184:187], v[212:215], v[56:59]\n"
    "ds_read_b64_tr_b16 a[174:175], v245 offset:47104\n"
    "v_mfma_f32_16x16x32_bf16 v[52:55], a[184:187], v[204:207], v[52:55]\n"
    "v_cvt_pk_bf16_f32 v173, v174, v175\n"
    "v_mfma_f32_16x16x32_bf16 v[52:55], a[188:191], v[208:211], v[52:55]\n"
    "v_exp_f32_e32 v144, v144\n"
    "v_mfma_f32_16x16x32_bf16 v[48:51], a[188:191], v[200:203], v[48:51]\n"
    "ds_read_b64_tr_b16 a[178:179], v246 offset:14336\n"
    "v_mfma_f32_16x16x32_bf16 v[48:51], a[184:187], v[196:199], v[48:51]\n"
    "v_exp_f32_e32 v145, v145\n"
    "v_mfma_f32_16x16x32_bf16 v[44:47], a[184:187], v[188:191], v[44:47]\n"
    "v_mfma_f32_16x16x32_bf16 v[44:47], a[188:191], v[192:195], v[44:47]\n"
    "v_mfma_f32_16x16x32_bf16 v[40:43], a[188:191], v[184:187], v[40:43]\n"
    "ds_read_b64_tr_b16 a[180:181], v246 offset:45056\n"
    "v_mfma_f32_16x16x32_bf16 v[40:43], a[184:187], v[180:183], v[40:43]\n"
    "s_waitcnt lgkmcnt(6)\n"
    "v_exp_f32_e32 v146, v146\n"
    "v_mfma_f32_16x16x32_bf16 v[60:63], a[160:163], v[180:183], v[60:63]\n"
    "ds_read_b64_tr_b16 a[184:185], v247 offset:12288\n"
    "v_mfma_f32_16x16x32_bf16 v[60:63], a[164:167], v[184:187], v[60:63]\n"
    "ds_read_b64_tr_b16 a[182:183], v246 offset:47104\n"
    "v_mfma_f32_16x16x32_bf16 v[64:67], a[164:167], v[192:195], v[64:67]\n"
    "ds_read_b64_tr_b16 a[186:187], v247 offset:14336\n"
    "v_mfma_f32_16x16x32_bf16 v[64:67], a[160:163], v[188:191], v[64:67]\n"
    "v_exp_f32_e32 v147, v147\n"
    "v_mfma_f32_16x16x32_bf16 v[68:71], a[160:163], v[196:199], v[68:71]\n"
    "v_cvt_pk_bf16_f32 v142, v144, v145\n"
    "v_mfma_f32_16x16x32_bf16 v[68:71], a[164:167], v[200:203], v[68:71]\n"
    "ds_read_b64_tr_b16 a[188:189], v247 offset:45056\n"
    "v_mfma_f32_16x16x32_bf16 v[72:75], a[164:167], v[208:211], v[72:75]\n"
    "ds_read_b64_tr_b16 a[190:191], v247 offset:47104\n"
    "v_mfma_f32_16x16x32_bf16 v[72:75], a[160:163], v[204:207], v[72:75]\n"
    "s_waitcnt vmcnt(9)\n"
    "v_mfma_f32_16x16x32_bf16 v[76:79], a[160:163], v[212:215], v[76:79]\n"
    "s_barrier\n"
    "v_mfma_f32_16x16x32_bf16 v[76:79], a[164:167], v[216:219], v[76:79]\n"
    "s_waitcnt lgkmcnt(7)\n"
    "s_mov_b32 m0, s42\n"
    "v_mfma_f32_16x16x32_bf16 v[96:99], a[172:175], v[216:219], v[96:99]\n"
    "ds_write_addtid_b32 v140 offset:0\n"
    "v_mfma_f32_16x16x32_bf16 v[96:99], a[168:171], v[212:215], v[96:99]\n"
    "v_mfma_f32_16x16x32_bf16 v[92:95], a[168:171], v[204:207], v[92:95]\n"
    "v_exp_f32_e32 v152, v152\n"
    "v_mfma_f32_16x16x32_bf16 v[92:95], a[172:175], v[208:211], v[92:95]\n"
    "ds_read_b128 a[160:163], v240 offset:24576\n"
    "v_mfma_f32_16x16x32_bf16 v[88:91], a[172:175], v[200:203], v[88:91]\n"
    "v_exp_f32_e32 v153, v153\n"
    "v_mfma_f32_16x16x32_bf16 v[88:91], a[168:171], v[196:199], v[88:91]\n"
    "ds_write_addtid_b32 v141 offset:256\n"
    "v_cvt_pk_bf16_f32 v143, v146, v147\n"
    "v_mfma_f32_16x16x32_bf16 v[84:87], a[168:171], v[188:191], v[84:87]\n"
    "v_exp_f32_e32 v154, v154\n"
    "v_mfma_f32_16x16x32_bf16 v[84:87], a[172:175], v[192:195], v[84:87]\n"
    "v_exp_f32_e32 v155, v155\n"
    "v_mfma_f32_16x16x32_bf16 v[80:83], a[172:175], v[184:187], v[80:83]\n"
    "ds_write_addtid_b32 v148 offset:1024\n"
    "v_cvt_pk_bf16_f32 v150, v152, v153\n"
    "v_mfma_f32_16x16x32_bf16 v[80:83], a[168:171], v[180:183], v[80:83]\n"
    "s_waitcnt lgkmcnt(7)\n"
    "v_exp_f32_e32 v160, v160\n"
    "v_mfma_f32_16x16x32_bf16 v[100:103], a[176:179], v[180:183], v[100:103]\n"
    "ds_read_b128 a[164:167], v241 offset:24576\n"
    "v_mfma_f32_16x16x32_bf16 v[100:103], a[180:183], v[184:187], v[100:103]\n"
    "ds_write_addtid_b32 v149 offset:1280\n"
    "v_mfma_f32_16x16x32_bf16 v[104:107], a[180:183], v[192:195], v[104:107]\n"
    "v_exp_f32_e32 v161, v161\n"
    "v_mfma_f32_16x16x32_bf16 v[104:107], a[176:179], v[188:191], v[104:107]\n"
    "ds_read_b128 a[168:171], v240 offset:28672\n"
    "v_mfma_f32_16x16x32_bf16 v[108:111], a[176:179], v[196:199], v[108:111]\n"
    "ds_write_addtid_b32 v156 offset:2048\n"
    "v_mfma_f32_16x16x32_bf16 v[108:111], a[180:183], v[200:203], v[108:111]\n"
    "v_cvt_pk_bf16_f32 v151, v154, v155\n"
    "v_mfma_f32_16x16x32_bf16 v[112:115], a[180:183], v[208:211], v[112:115]\n"
    "ds_write_addtid_b32 v157 offset:2304\n"
    "v_mfma_f32_16x16x32_bf16 v[112:115], a[176:179], v[204:207], v[112:115]\n"
    "v_exp_f32_e32 v162, v162\n"
    "v_mfma_f32_16x16x32_bf16 v[116:119], a[176:179], v[212:215], v[116:119]\n"
    "ds_write_addtid_b32 v164 offset:3072\n"
    "v_mfma_f32_16x16x32_bf16 v[116:119], a[180:183], v[216:219], v[116:119]\n"
    "s_waitcnt lgkmcnt(10)\n"
    "v_exp_f32_e32 v163, v163\n"
    "v_mfma_f32_16x16x32_bf16 v[136:139], a[188:191], v[216:219], v[136:139]\n"
    "ds_read_b128 a[172:175], v241 offset:28672\n"
    "v_mfma_f32_16x16x32_bf16 v[136:139], a[184:187], v[212:215], v[136:139]\n"
    "ds_write_addtid_b32 v165 offset:3328\n"
    "v_cvt_pk_bf16_f32 v158, v160, v161\n"
    "v_mfma_f32_16x16x32_bf16 v[132:135], a[184:187], v[204:207], v[132:135]\n"
    "ds_read_b128 a[176:179], v242 offset:24576\n"
    "v_mfma_f32_16x16x32_bf16 v[132:135], a[188:191], v[208:211], v[132:135]\n"
    "ds_write_addtid_b32 v172 offset:4096\n"
    "v_mfma_f32_16x16x32_bf16 v[128:131], a[188:191], v[200:203], v[128:131]\n"
    "v_exp_f32_e32 v168, v168\n"
    "v_mfma_f32_16x16x32_bf16 v[128:131], a[184:187], v[196:199], v[128:131]\n"
    "ds_write_addtid_b32 v173 offset:4352\n"
    "v_mfma_f32_16x16x32_bf16 v[124:127], a[184:187], v[188:191], v[124:127]\n"
    "v_exp_f32_e32 v169, v169\n"
    "v_mfma_f32_16x16x32_bf16 v[124:127], a[188:191], v[192:195], v[124:127]\n"
    "ds_read_b128 a[180:183], v243 offset:24576\n"
    "v_mfma_f32_16x16x32_bf16 v[120:123], a[188:191], v[184:187], v[120:123]\n"
    "ds_write_addtid_b32 v142 offset:512\n"
    "v_mfma_f32_16x16x32_bf16 v[120:123], a[184:187], v[180:183], v[120:123]\n"
    "s_waitcnt lgkmcnt(12)\n"
    "v_cvt_pk_bf16_f32 v159, v162, v163\n"
    "v_mfma_f32_16x16x32_bf16 v[212:215], a[160:163], a[128:131], v[236:239]\n"
    "v_mfma_f32_16x16x32_bf16 v[212:215], a[164:167], a[132:135], v[212:215]\n"
    "v_exp_f32_e32 v170, v170\n"
    "v_mfma_f32_16x16x32_bf16 v[204:207], a[164:167], a[100:103], v[232:235]\n"
    "ds_write_addtid_b32 v143 offset:768\n"
    "v_mfma_f32_16x16x32_bf16 v[204:207], a[160:163], a[96:99], v[204:207]\n"
    "ds_read_b128 a[184:187], v242 offset:28672\n"
    "v_mfma_f32_16x16x32_bf16 v[196:199], a[160:163], a[64:67], v[228:231]\n"
    "v_exp_f32_e32 v171, v171\n"
    "v_mfma_f32_16x16x32_bf16 v[196:199], a[164:167], a[68:71], v[196:199]\n"
    "ds_write_addtid_b32 v150 offset:1536\n"
    "v_mfma_f32_16x16x32_bf16 v[188:191], a[164:167], a[36:39], v[224:227]\n"
    "v_cvt_pk_bf16_f32 v166, v168, v169\n"
    "v_mfma_f32_16x16x32_bf16 v[188:191], a[160:163], a[32:35], v[188:191]\n"
    "v_mfma_f32_16x16x32_bf16 v[180:183], a[160:163], a[0:3], v[220:223]\n"
    "ds_write_addtid_b32 v151 offset:1792\n"
    "v_mfma_f32_16x16x32_bf16 v[180:183], a[164:167], a[4:7], v[180:183]\n"
    "s_waitcnt lgkmcnt(10)\n"
    "v_exp_f32_e32 v176, v176\n"
    "v_mfma_f32_16x16x32_bf16 v[180:183], a[168:171], a[8:11], v[180:183]\n"
    "ds_read_b128 a[188:191], v243 offset:28672\n"
    "v_mfma_f32_16x16x32_bf16 v[180:183], a[172:175], a[12:15], v[180:183]\n"
    "v_exp_f32_e32 v177, v177\n"
    "v_mfma_f32_16x16x32_bf16 v[188:191], a[172:175], a[44:47], v[188:191]\n"
    "ds_write_addtid_b32 v158 offset:2560\n"
    "v_mfma_f32_16x16x32_bf16 v[188:191], a[168:171], a[40:43], v[188:191]\n"
    "ds_read_b128 a[160:163], v240 offset:26624\n"
    "v_mfma_f32_16x16x32_bf16 v[196:199], a[168:171], a[72:75], v[196:199]\n"
    "v_cvt_pk_bf16_f32 v167, v170, v171\n"
    "v_mfma_f32_16x16x32_bf16 v[196:199], a[172:175], a[76:79], v[196:199]\n"
    "ds_write_addtid_b32 v159 offset:2816\n"
    "v_mfma_f32_16x16x32_bf16 v[204:207], a[172:175], a[108:111], v[204:207]\n"
    "v_exp_f32_e32 v178, v178\n"
    "v_mfma_f32_16x16x32_bf16 v[204:207], a[168:171], a[104:107], v[204:207]\n"
    "ds_write_addtid_b32 v166 offset:3584\n"
    "v_mfma_f32_16x16x32_bf16 v[212:215], a[168:171], a[136:139], v[212:215]\n"
    "v_mfma_f32_16x16x32_bf16 v[212:215], a[172:175], a[140:143], v[212:215]\n"
    "s_waitcnt lgkmcnt(10)\n"
    "v_exp_f32_e32 v179, v179\n"
    "v_mfma_f32_16x16x32_bf16 v[212:215], a[176:179], a[144:147], v[212:215]\n"
    "ds_read_b128 a[164:167], v241 offset:26624\n"
    "v_mfma_f32_16x16x32_bf16 v[212:215], a[180:183], a[148:151], v[212:215]\n"
    "v_cvt_pk_bf16_f32 v174, v176, v177\n"
    "v_mfma_f32_16x16x32_bf16 v[204:207], a[180:183], a[116:119], v[204:207]\n"
    "ds_write_addtid_b32 v167 offset:3840\n"
    "v_mfma_f32_16x16x32_bf16 v[204:207], a[176:179], a[112:115], v[204:207]\n"
    "ds_read_b128 a[168:171], v240 offset:30720\n"
    "v_mfma_f32_16x16x32_bf16 v[196:199], a[176:179], a[80:83], v[196:199]\n"
    "v_cvt_pk_bf16_f32 v175, v178, v179\n"
    "v_mfma_f32_16x16x32_bf16 v[196:199], a[180:183], a[84:87], v[196:199]\n"
    "ds_write_addtid_b32 v174 offset:4608\n"
    "v_mfma_f32_16x16x32_bf16 v[188:191], a[180:183], a[52:55], v[188:191]\n"
    "ds_write_addtid_b32 v175 offset:4864\n"
    "v_mfma_f32_16x16x32_bf16 v[188:191], a[176:179], a[48:51], v[188:191]\n"
    "v_mfma_f32_16x16x32_bf16 v[180:183], a[176:179], a[16:19], v[180:183]\n"
    "ds_read_b128 a[172:175], v241 offset:30720\n"
    "v_mfma_f32_16x16x32_bf16 v[180:183], a[180:183], a[20:23], v[180:183]\n"
    "s_waitcnt lgkmcnt(10)\n"
    "s_waitcnt lgkmcnt(1)\n"
    "v_mfma_f32_16x16x32_bf16 v[180:183], a[184:187], a[24:27], v[180:183]\n"
    "s_barrier\n"
    "v_mfma_f32_16x16x32_bf16 v[180:183], a[188:191], a[28:31], v[180:183]\n"
    "s_add_u32 m0, s43, 8192\n"
    "v_mfma_f32_16x16x32_bf16 v[188:191], a[188:191], a[60:63], v[188:191]\n"
    "ds_read_b128 a[176:179], v242 offset:26624\n"
    "v_mfma_f32_16x16x32_bf16 v[188:191], a[184:187], a[56:59], v[188:191]\n"
    "buffer_load_dwordx4 v248, s[8:11], s40 offen offset:0 sc1 lds\n"
    "v_mfma_f32_16x16x32_bf16 v[196:199], a[184:187], a[88:91], v[196:199]\n"
    "ds_read2st64_b32 v[144:145], v249 offset0:0 offset1:1\n"
    "v_mfma_f32_16x16x32_bf16 v[196:199], a[188:191], a[92:95], v[196:199]\n"
    "v_mfma_f32_16x16x32_bf16 v[204:207], a[188:191], a[124:127], v[204:207]\n"
    "v_mfma_f32_16x16x32_bf16 v[204:207], a[184:187], a[120:123], v[204:207]\n"
    "ds_read2st64_b32 v[146:147], v249 offset0:2 offset1:3\n"
    "v_mfma_f32_16x16x32_bf16 v[212:215], a[184:187], a[152:155], v[212:215]\n"
    "buffer_load_dwordx4 v248, s[8:11], s40 offen offset:1024 sc1 lds\n"
    "v_mfma_f32_16x16x32_bf16 v[212:215], a[188:191], a[156:159], v[212:215]\n"
    "v_mfma_f32_16x16x32_bf16 v[216:219], a[160:163], a[128:131], v[236:239]\n"
    "ds_read_b128 a[180:183], v243 offset:26624\n"
    "v_mfma_f32_16x16x32_bf16 v[216:219], a[164:167], a[132:135], v[216:219]\n"
    "ds_read2st64_b32 v[152:153], v249 offset0:4 offset1:5\n"
    "v_mfma_f32_16x16x32_bf16 v[208:211], a[164:167], a[100:103], v[232:235]\n"
    "ds_read_b128 a[184:187], v242 offset:30720\n"
    "v_mfma_f32_16x16x32_bf16 v[208:211], a[160:163], a[96:99], v[208:211]\n"
    "buffer_load_dwordx4 v248, s[8:11], s40 offen offset:2048 sc1 lds\n"
    "v_mfma_f32_16x16x32_bf16 v[200:203], a[160:163], a[64:67], v[228:231]\n"
    "v_exp_f32_e32 v180, v180\n"
    "v_mfma_f32_16x16x32_bf16 v[200:203], a[164:167], a[68:71], v[200:203]\n"
    "ds_read2st64_b32 v[154:155], v249 offset0:6 offset1:7\n"
    "v_mfma_f32_16x16x32_bf16 v[192:195], a[164:167], a[36:39], v[224:227]\n"
    "v_exp_f32_e32 v181, v181\n"
    "v_mfma_f32_16x16x32_bf16 v[192:195], a[160:163], a[32:35], v[192:195]\n"
    "v_exp_f32_e32 v182, v182\n"
    "v_mfma_f32_16x16x32_bf16 v[184:187], a[160:163], a[0:3], v[220:223]\n"
    "buffer_load_dwordx4 v248, s[8:11], s40 offen offset:3072 sc1 lds\n"
    "v_mfma_f32_16x16x32_bf16 v[184:187], a[164:167], a[4:7], v[184:187]\n"
    "s_waitcnt lgkmcnt(7)\n"
    "ds_read2st64_b32 v[160:161], v249 offset0:8 offset1:9\n"
    "v_mfma_f32_16x16x32_bf16 v[184:187], a[168:171], a[8:11], v[184:187]\n"
    "s_add_u32 s40, s40, 0x8000\n"
    "ds_read_b128 a[188:191], v243 offset:30720\n"
    "v_mfma_f32_16x16x32_bf16 v[184:187], a[172:175], a[12:15], v[184:187]\n"
    "s_add_u32 m0, s43, 12288\n"
    "ds_read_b64_tr_b16 a[160:161], v244 offset:16384\n"
    "v_mfma_f32_16x16x32_bf16 v[192:195], a[172:175], a[44:47], v[192:195]\n"
    "ds_read2st64_b32 v[162:163], v249 offset0:10 offset1:11\n"
    "v_mfma_f32_16x16x32_bf16 v[192:195], a[168:171], a[40:43], v[192:195]\n"
    "buffer_load_dwordx4 v248, s[8:11], s41 offen offset:0 sc1 lds\n"
    "v_mfma_f32_16x16x32_bf16 v[200:203], a[168:171], a[72:75], v[200:203]\n"
    "ds_read_b64_tr_b16 a[162:163], v244 offset:18432\n"
    "v_mfma_f32_16x16x32_bf16 v[200:203], a[172:175], a[76:79], v[200:203]\n"
    "v_exp_f32_e32 v183, v183\n"
    "v_mfma_f32_16x16x32_bf16 v[208:211], a[172:175], a[108:111], v[208:211]\n"
    "ds_read2st64_b32 v[168:169], v249 offset0:12 offset1:13\n"
    "v_cvt_pk_bf16_f32 v180, v180, v181\n"
    "v_mfma_f32_16x16x32_bf16 v[208:211], a[168:171], a[104:107], v[208:211]\n"
    "ds_read_b64_tr_b16 a[164:165], v244 offset:49152\n"
    "v_mfma_f32_16x16x32_bf16 v[216:219], a[168:171], a[136:139], v[216:219]\n"
    "buffer_load_dwordx4 v248, s[8:11], s41 offen offset:1024 sc1 lds\n"
    "v_mfma_f32_16x16x32_bf16 v[216:219], a[172:175], a[140:143], v[216:219]\n"
    "s_waitcnt lgkmcnt(10)\n"
    "ds_read2st64_b32 v[170:171], v249 offset0:14 offset1:15\n"
    "v_mfma_f32_16x16x32_bf16 v[216:219], a[176:179], a[144:147], v[216:219]\n"
    "ds_read_b64_tr_b16 a[168:169], v245 offset:16384\n"
    "v_mfma_f32_16x16x32_bf16 v[216:219], a[180:183], a[148:151], v[216:219]\n"
    "ds_read_b64_tr_b16 a[166:167], v244 offset:51200\n"
    "v_mfma_f32_16x16x32_bf16 v[208:211], a[180:183], a[116:119], v[208:211]\n"
    "v_exp_f32_e32 v188, v188\n"
    "v_mfma_f32_16x16x32_bf16 v[208:211], a[176:179], a[112:115], v[208:211]\n"
    "buffer_load_dwordx4 v248, s[8:11], s41 offen offset:2048 sc1 lds\n"
    "v_mfma_f32_16x16x32_bf16 v[200:203], a[176:179], a[80:83], v[200:203]\n"
    "ds_read_b64_tr_b16 a[170:171], v245 offset:18432\n"
    "v_mfma_f32_16x16x32_bf16 v[200:203], a[180:183], a[84:87], v[200:203]\n"
    "ds_read2st64_b32 v[176:177], v249 offset0:16 offset1:17\n"
    "v_mfma_f32_16x16x32_bf16 v[192:195], a[180:183], a[52:55], v[192:195]\n"
    "v_exp_f32_e32 v189, v189\n"
    "v_mfma_f32_16x16x32_bf16 v[192:195], a[176:179], a[48:51], v[192:195]\n"
    "ds_read_b64_tr_b16 a[172:173], v245 offset:49152\n"
    "v_mfma_f32_16x16x32_bf16 v[184:187], a[176:179], a[16:19], v[184:187]\n"
    "buffer_load_dwordx4 v248, s[8:11], s41 offen offset:3072 sc1 lds\n"
    "v_mfma_f32_16x16x32_bf16 v[184:187], a[180:183], a[20:23], v[184:187]\n"
    "s_waitcnt lgkmcnt(11)\n"
    "ds_read2st64_b32 v[178:179], v249 offset0:18 offset1:19\n"
    "v_mfma_f32_16x16x32_bf16 v[184:187], a[184:187], a[24:27], v[184:187]\n"
    "s_add_u32 s41, s41, 0x8000\n"
    "ds_read_b64_tr_b16 a[176:177], v246 offset:16384\n"
    "v_mfma_f32_16x16x32_bf16 v[184:187], a[188:191], a[28:31], v[184:187]\n"
    "buffer_load_dword v253, off, s[8:11], s82 sc1\n"
    "v_mfma_f32_16x16x32_bf16 v[192:195], a[188:191], a[60:63], v[192:195]\n"
    "ds_read_b64_tr_b16 a[174:175], v245 offset:51200\n"
    "v_mfma_f32_16x16x32_bf16 v[192:195], a[184:187], a[56:59], v[192:195]\n"
    "v_cvt_pk_bf16_f32 v181, v182, v183\n"
    "v_mfma_f32_16x16x32_bf16 v[200:203], a[184:187], a[88:91], v[200:203]\n"
    "ds_read_b64_tr_b16 a[178:179], v246 offset:18432\n"
    "v_mfma_f32_16x16x32_bf16 v[200:203], a[188:191], a[92:95], v[200:203]\n"
    "v_exp_f32_e32 v190, v190\n"
    "v_mfma_f32_16x16x32_bf16 v[208:211], a[188:191], a[124:127], v[208:211]\n"
    "v_exp_f32_e32 v191, v191\n"
    "v_mfma_f32_16x16x32_bf16 v[208:211], a[184:187], a[120:123], v[208:211]\n"
    "v_cvt_pk_bf16_f32 v188, v188, v189\n"
    "v_mfma_f32_16x16x32_bf16 v[216:219], a[184:187], a[152:155], v[216:219]\n"
    "ds_read_b64_tr_b16 a[180:181], v246 offset:49152\n"
    "v_mfma_f32_16x16x32_bf16 v[216:219], a[188:191], a[156:159], v[216:219]\n"
    "s_waitcnt lgkmcnt(4)\n"
    "v_exp_f32_e32 v196, v196\n"
    "v_mfma_f32_16x16x32_bf16 a[236:239], a[160:163], v[140:143], a[236:239]\n"
    "ds_read_b64_tr_b16 a[184:185], v247 offset:16384\n"
    "v_mfma_f32_16x16x32_bf16 a[236:239], a[164:167], v[144:147], a[236:239]\n"
    "ds_read_b64_tr_b16 a[182:183], v246 offset:51200\n"
    "v_mfma_f32_16x16x32_bf16 a[240:243], a[164:167], v[152:155], a[240:243]\n"
    "v_exp_f32_e32 v197, v197\n"
    "v_mfma_f32_16x16x32_bf16 a[240:243], a[160:163], v[148:151], a[240:243]\n"
    "v_cvt_pk_bf16_f32 v189, v190, v191\n"
    "v_mfma_f32_16x16x32_bf16 a[244:247], a[160:163], v[156:159], a[244:247]\n"
    "ds_read_b64_tr_b16 a[186:187], v247 offset:18432\n"
    "v_mfma_f32_16x16x32_bf16 a[244:247], a[164:167], v[160:163], a[244:247]\n"
    "v_exp_f32_e32 v198, v198\n"
    "v_mfma_f32_16x16x32_bf16 a[248:251], a[164:167], v[168:171], a[248:251]\n"
    "v_exp_f32_e32 v199, v199\n"
    "v_mfma_f32_16x16x32_bf16 a[248:251], a[160:163], v[164:167], a[248:251]\n"
    "v_cvt_pk_bf16_f32 v196, v196, v197\n"
    "v_mfma_f32_16x16x32_bf16 a[252:255], a[160:163], v[172:175], a[252:255]\n"
    "ds_read_b64_tr_b16 a[188:189], v247 offset:49152\n"
    "v_mfma_f32_16x16x32_bf16 a[252:255], a[164:167], v[176:179], a[252:255]\n"
    "s_waitcnt lgkmcnt(6)\n"
    "v_exp_f32_e32 v204, v204\n"
    "v_mfma_f32_16x16x32_bf16 v[16:19], a[172:175], v[176:179], v[16:19]\n"
    "ds_read_b64_tr_b16 a[160:161], v244 offset:20480\n"
    "v_mfma_f32_16x16x32_bf16 v[16:19], a[168:171], v[172:175], v[16:19]\n"
    "ds_read_b64_tr_b16 a[190:191], v247 offset:51200\n"
    "v_mfma_f32_16x16x32_bf16 v[12:15], a[168:171], v[164:167], v[12:15]\n"
    "v_exp_f32_e32 v205, v205\n"
    "v_mfma_f32_16x16x32_bf16 v[12:15], a[172:175], v[168:171], v[12:15]\n"
    "v_cvt_pk_bf16_f32 v197, v198, v199\n"
    "v_mfma_f32_16x16x32_bf16 v[8:11], a[172:175], v[160:163], v[8:11]\n"
    "ds_read_b64_tr_b16 a[162:163], v244 offset:22528\n"
    "v_mfma_f32_16x16x32_bf16 v[8:11], a[168:171], v[156:159], v[8:11]\n"
    "v_exp_f32_e32 v206, v206\n"
    "v_mfma_f32_16x16x32_bf16 v[4:7], a[168:171], v[148:151], v[4:7]\n"
    "v_exp_f32_e32 v207, v207\n"
    "v_mfma_f32_16x16x32_bf16 v[4:7], a[172:175], v[152:155], v[4:7]\n"
    "v_cvt_pk_bf16_f32 v204, v204, v205\n"
    "v_mfma_f32_16x16x32_bf16 v[0:3], a[172:175], v[144:147], v[0:3]\n"
    "ds_read_b64_tr_b16 a[164:165], v244 offset:53248\n"
    "v_mfma_f32_16x16x32_bf16 v[0:3], a[168:171], v[140:143], v[0:3]\n"
    "s_waitcnt lgkmcnt(6)\n"
    "v_exp_f32_e32 v212, v212\n"
    "v_mfma_f32_16x16x32_bf16 v[20:23], a[176:179], v[140:143], v[20:23]\n"
    "ds_read_b64_tr_b16 a[168:169], v245 offset:20480\n"
    "v_mfma_f32_16x16x32_bf16 v[20:23], a[180:183], v[144:147], v[20:23]\n"
    "ds_read_b64_tr_b16 a[166:167], v244 offset:55296\n"
    "v_mfma_f32_16x16x32_bf16 v[24:27], a[180:183], v[152:155], v[24:27]\n"
    "v_exp_f32_e32 v213, v213\n"
    "v_mfma_f32_16x16x32_bf16 v[24:27], a[176:179], v[148:151], v[24:27]\n"
    "v_cvt_pk_bf16_f32 v205, v206, v207\n"
    "v_mfma_f32_16x16x32_bf16 v[28:31], a[176:179], v[156:159], v[28:31]\n"
    "ds_read_b64_tr_b16 a[170:171], v245 offset:22528\n"
    "v_mfma_f32_16x16x32_bf16 v[28:31], a[180:183], v[160:163], v[28:31]\n"
    "v_exp_f32_e32 v214, v214\n"
    "v_mfma_f32_16x16x32_bf16 v[32:35], a[180:183], v[168:171], v[32:35]\n"
    "v_mfma_f32_16x16x32_bf16 v[32:35], a[176:179], v[164:167], v[32:35]\n"
    "v_exp_f32_e32 v215, v215\n"
    "v_mfma_f32_16x16x32_bf16 v[36:39], a[176:179], v[172:175], v[36:39]\n"
    "ds_read_b64_tr_b16 a[172:173], v245 offset:53248\n"
    "v_mfma_f32_16x16x32_bf16 v[36:39], a[180:183], v[176:179], v[36:39]\n"
    "s_waitcnt lgkmcnt(6)\n"
    "v_cvt_pk_bf16_f32 v212, v212, v213\n"
    "v_mfma_f32_16x16x32_bf16 v[56:59], a[188:191], v[176:179], v[56:59]\n"
    "ds_read_b64_tr_b16 a[176:177], v246 offset:20480\n"
    "v_mfma_f32_16x16x32_bf16 v[56:59], a[184:187], v[172:175], v[56:59]\n"
    "ds_read_b64_tr_b16 a[174:175], v245 offset:55296\n"
    "v_mfma_f32_16x16x32_bf16 v[52:55], a[184:187], v[164:167], v[52:55]\n"
    "v_cvt_pk_bf16_f32 v213, v214, v215\n"
    "v_mfma_f32_16x16x32_bf16 v[52:55], a[188:191], v[168:171], v[52:55]\n"
    "v_exp_f32_e32 v184, v184\n"
    "v_mfma_f32_16x16x32_bf16 v[48:51], a[188:191], v[160:163], v[48:51]\n"
    "s_waitcnt vmcnt(0)\n"
    "ds_read_b64_tr_b16 a[178:179], v246 offset:22528\n"
    "v_mfma_f32_16x16x32_bf16 v[48:51], a[184:187], v[156:159], v[48:51]\n"
    "s_mov_b32 m0, s30\n"
    "v_exp_f32_e32 v185, v185\n"
    "v_mfma_f32_16x16x32_bf16 v[44:47], a[184:187], v[148:151], v[44:47]\n"
    "buffer_load_dwordx4 v250, s[8:11], s83 offen offset:0 sc1 lds\n"
    "v_mfma_f32_16x16x32_bf16 v[44:47], a[188:191], v[152:155], v[44:47]\n"
    "v_readfirstlane_b32 s81, v253\n"
    "v_mfma_f32_16x16x32_bf16 v[40:43], a[188:191], v[144:147], v[40:43]\n"
    "ds_read_b64_tr_b16 a[180:181], v246 offset:53248\n"
    "v_mfma_f32_16x16x32_bf16 v[40:43], a[184:187], v[140:143], v[40:43]\n"
    "s_waitcnt lgkmcnt(6)\n"
    "v_exp_f32_e32 v186, v186\n"
    "v_mfma_f32_16x16x32_bf16 v[60:63], a[160:163], v[140:143], v[60:63]\n"
    "ds_read_b64_tr_b16 a[184:185], v247 offset:20480\n"
    "v_mfma_f32_16x16x32_bf16 v[60:63], a[164:167], v[144:147], v[60:63]\n"
    "ds_read_b64_tr_b16 a[182:183], v246 offset:55296\n"
    "v_mfma_f32_16x16x32_bf16 v[64:67], a[164:167], v[152:155], v[64:67]\n"
    "ds_read_b64_tr_b16 a[186:187], v247 offset:22528\n"
    "v_mfma_f32_16x16x32_bf16 v[64:67], a[160:163], v[148:151], v[64:67]\n"
    "v_exp_f32_e32 v187, v187\n"
    "v_mfma_f32_16x16x32_bf16 v[68:71], a[160:163], v[156:159], v[68:71]\n"
    "v_cvt_pk_bf16_f32 v182, v184, v185\n"
    "v_mfma_f32_16x16x32_bf16 v[68:71], a[164:167], v[160:163], v[68:71]\n"
    "ds_read_b64_tr_b16 a[188:189], v247 offset:53248\n"
    "v_mfma_f32_16x16x32_bf16 v[72:75], a[164:167], v[168:171], v[72:75]\n"
    "ds_read_b64_tr_b16 a[190:191], v247 offset:55296\n"
    "v_mfma_f32_16x16x32_bf16 v[72:75], a[160:163], v[164:167], v[72:75]\n"
    "s_waitcnt vmcnt(10)\n"
    "v_mfma_f32_16x16x32_bf16 v[76:79], a[160:163], v[172:175], v[76:79]\n"
    "s_barrier\n"
    "v_mfma_f32_16x16x32_bf16 v[76:79], a[164:167], v[176:179], v[76:79]\n"
    "s_waitcnt lgkmcnt(7)\n"
    "s_mov_b32 m0, s42\n"
    "v_mfma_f32_16x16x32_bf16 v[96:99], a[172:175], v[176:179], v[96:99]\n"
    "ds_write_addtid_b32 v180 offset:0\n"
    "v_mfma_f32_16x16x32_bf16 v[96:99], a[168:171], v[172:175], v[96:99]\n"
    "v_mfma_f32_16x16x32_bf16 v[92:95], a[168:171], v[164:167], v[92:95]\n"
    "v_exp_f32_e32 v192, v192\n"
    "v_mfma_f32_16x16x32_bf16 v[92:95], a[172:175], v[168:171], v[92:95]\n"
    "ds_read_b128 a[160:163], v240 offset:0\n"
    "v_mfma_f32_16x16x32_bf16 v[88:91], a[172:175], v[160:163], v[88:91]\n"
    "v_exp_f32_e32 v193, v193\n"
    "v_mfma_f32_16x16x32_bf16 v[88:91], a[168:171], v[156:159], v[88:91]\n"
    "ds_write_addtid_b32 v181 offset:256\n"
    "v_cvt_pk_bf16_f32 v183, v186, v187\n"
    "v_mfma_f32_16x16x32_bf16 v[84:87], a[168:171], v[148:151], v[84:87]\n"
    "v_exp_f32_e32 v194, v194\n"
    "v_mfma_f32_16x16x32_bf16 v[84:87], a[172:175], v[152:155], v[84:87]\n"
    "v_exp_f32_e32 v195, v195\n"
    "v_mfma_f32_16x16x32_bf16 v[80:83], a[172:175], v[144:147], v[80:83]\n"
    "ds_write_addtid_b32 v188 offset:1024\n"
    "v_cvt_pk_bf16_f32 v190, v192, v193\n"
    "v_mfma_f32_16x16x32_bf16 v[80:83], a[168:171], v[140:143], v[80:83]\n"
    "s_waitcnt lgkmcnt(7)\n"
    "v_exp_f32_e32 v200, v200\n"
    "v_mfma_f32_16x16x32_bf16 v[100:103], a[176:179], v[140:143], v[100:103]\n"
    "ds_read_b128 a[164:167], v241 offset:0\n"
    "v_mfma_f32_16x16x32_bf16 v[100:103], a[180:183], v[144:147], v[100:103]\n"
    "ds_write_addtid_b32 v189 offset:1280\n"
    "v_mfma_f32_16x16x32_bf16 v[104:107], a[180:183], v[152:155], v[104:107]\n"
    "v_exp_f32_e32 v201, v201\n"
    "v_mfma_f32_16x16x32_bf16 v[104:107], a[176:179], v[148:151], v[104:107]\n"
    "ds_read_b128 a[168:171], v240 offset:4096\n"
    "v_mfma_f32_16x16x32_bf16 v[108:111], a[176:179], v[156:159], v[108:111]\n"
    "ds_write_addtid_b32 v196 offset:2048\n"
    "v_mfma_f32_16x16x32_bf16 v[108:111], a[180:183], v[160:163], v[108:111]\n"
    "v_cvt_pk_bf16_f32 v191, v194, v195\n"
    "v_mfma_f32_16x16x32_bf16 v[112:115], a[180:183], v[168:171], v[112:115]\n"
    "ds_write_addtid_b32 v197 offset:2304\n"
    "v_mfma_f32_16x16x32_bf16 v[112:115], a[176:179], v[164:167], v[112:115]\n"
    "v_exp_f32_e32 v202, v202\n"
    "v_mfma_f32_16x16x32_bf16 v[116:119], a[176:179], v[172:175], v[116:119]\n"
    "ds_write_addtid_b32 v204 offset:3072\n"
    "v_mfma_f32_16x16x32_bf16 v[116:119], a[180:183], v[176:179], v[116:119]\n"
    "s_waitcnt lgkmcnt(10)\n"
    "v_exp_f32_e32 v203, v203\n"
    "v_mfma_f32_16x16x32_bf16 v[136:139], a[188:191], v[176:179], v[136:139]\n"
    "ds_read_b128 a[172:175], v241 offset:4096\n"
    "v_mfma_f32_16x16x32_bf16 v[136:139], a[184:187], v[172:175], v[136:139]\n"
    "ds_write_addtid_b32 v205 offset:3328\n"
    "v_cvt_pk_bf16_f32 v198, v200, v201\n"
    "v_mfma_f32_16x16x32_bf16 v[132:135], a[184:187], v[164:167], v[132:135]\n"
    "ds_read_b128 a[176:179], v242 offset:0\n"
    "v_mfma_f32_16x16x32_bf16 v[132:135], a[188:191], v[168:171], v[132:135]\n"
    "ds_write_addtid_b32 v212 offset:4096\n"
    "v_mfma_f32_16x16x32_bf16 v[128:131], a[188:191], v[160:163], v[128:131]\n"
    "v_exp_f32_e32 v208, v208\n"
    "v_mfma_f32_16x16x32_bf16 v[128:131], a[184:187], v[156:159], v[128:131]\n"
    "s_sub_u32 s44, s44, 1\n"
    "ds_write_addtid_b32 v213 offset:4352\n"
    "v_mfma_f32_16x16x32_bf16 v[124:127], a[184:187], v[148:151], v[124:127]\n"
    "s_cmp_eq_u32 s44, s80\n"
    "v_exp_f32_e32 v209, v209\n"
    "v_mfma_f32_16x16x32_bf16 v[124:127], a[188:191], v[152:155], v[124:127]\n"
    "s_cselect_b32 s88, s39, s81\n"
    "ds_read_b128 a[180:183], v243 offset:0\n"
    "v_mfma_f32_16x16x32_bf16 v[120:123], a[188:191], v[144:147], v[120:123]\n"
    "s_cmp_lg_u32 s88, s39\n"
    "ds_write_addtid_b32 v182 offset:512\n"
    "v_mfma_f32_16x16x32_bf16 v[120:123], a[184:187], v[140:143], v[120:123]\n"
    "s_waitcnt lgkmcnt(12)\n"
    "v_cvt_pk_bf16_f32 v199, v202, v203\n"
    "s_cbranch_scc1 .Lat_loop\n"
    "s_cmp_lg_u32 s44, 0\n"
    "s_cbranch_scc1 .Lat_g1\n"
    ".Lat_tail:\n"
    "v_mfma_f32_16x16x32_bf16 v[172:175], a[160:163], a[128:131], v[236:239]\n"
    "ds_write_addtid_b32 v183 offset:768\n"
    "v_and_b32_e32 v148, 0x1f0, v250\n"
    "v_mfma_f32_16x16x32_bf16 v[172:175], a[164:167], a[132:135], v[172:175]\n"
    "v_exp_f32_e32 v210, v210\n"
    "v_lshrrev_b32_e32 v253, 4, v250\n"
    "v_mfma_f32_16x16x32_bf16 v[164:167], a[164:167], a[100:103], v[232:235]\n"
    "s_mov_b32 s52, 5998592\n"
    "ds_read_b128 a[184:187], v242 offset:4096\n"
    "v_bfe_u32 v251, v253, 4, 2\n"
    "v_mfma_f32_16x16x32_bf16 v[164:167], a[160:163], a[96:99], v[164:167]\n"
    "s_lshr_b32 s81, s34, 6\n"
    "v_exp_f32_e32 v211, v211\n"
    "v_lshlrev_b32_e32 v251, 1, v251\n"
    "v_mfma_f32_16x16x32_bf16 v[156:159], a[160:163], a[64:67], v[228:231]\n"
    "s_lshl_b32 s81, s81, 9\n"
    "ds_write_addtid_b32 v190 offset:1536\n"
    "v_xor_b32_e32 v251, v251, v253\n"
    "v_mfma_f32_16x16x32_bf16 v[156:159], a[164:167], a[68:71], v[156:159]\n"
    "s_waitcnt lgkmcnt(9)\n"
    "ds_write_addtid_b32 v191 offset:1792\n"
    "v_cvt_pk_bf16_f32 v206, v208, v209\n"
    "v_mfma_f32_16x16x32_bf16 v[156:159], a[168:171], a[72:75], v[156:159]\n"
    "s_add_u32 s81, s81, 5939200\n"
    "ds_read_b128 a[188:191], v243 offset:4096\n"
    "v_and_b32_e32 v251, 7, v251\n"
    "v_mfma_f32_16x16x32_bf16 v[156:159], a[172:175], a[76:79], v[156:159]\n"
    "s_lshl_b32 s63, %2, 13\n"
    "v_exp_f32_e32 v216, v216\n"
    "v_lshlrev_b32_e32 v251, 4, v251\n"
    "v_mfma_f32_16x16x32_bf16 v[164:167], a[172:175], a[108:111], v[164:167]\n"
    "s_xor_b32 s45, s63, 0x4000\n"
    "ds_read_b128 a[160:163], v240 offset:2048\n"
    "v_lshrrev_b32_e32 v253, 3, v253\n"
    "v_mfma_f32_16x16x32_bf16 v[164:167], a[168:171], a[104:107], v[164:167]\n"
    "s_mul_i32 s58, %2, 33792\n"
    "v_exp_f32_e32 v217, v217\n"
    "v_mfma_f32_16x16x32_bf16 v[172:175], a[168:171], a[136:139], v[172:175]\n"
    "s_add_u32 s57, s58, 5509120\n"
    "ds_write_addtid_b32 v198 offset:2560\n"
    "v_cvt_pk_bf16_f32 v207, v210, v211\n"
    "v_lshl_add_u32 v251, v253, 9, v251\n"
    "v_mfma_f32_16x16x32_bf16 v[172:175], a[172:175], a[140:143], v[172:175]\n"
    "s_waitcnt lgkmcnt(8)\n"
    "v_exp_f32_e32 v218, v218\n"
    "v_bfe_u32 v255, v250, 2, 4\n"
    "v_mfma_f32_16x16x32_bf16 v[172:175], a[176:179], a[144:147], v[172:175]\n"
    "s_add_u32 s58, s58, 5373952\n"
    "ds_read_b128 a[168:171], v240 offset:6144\n"
    "v_mov_b32_e32 v254, s39\n"
    "v_mfma_f32_16x16x32_bf16 v[172:175], a[180:183], a[148:151], v[172:175]\n"
    "s_add_u32 s36, s35, 0x8000\n"
    "ds_read_b128 a[164:167], v241 offset:2048\n"
    "v_lshrrev_b32_e32 v252, 2, v250\n"
    "v_mfma_f32_16x16x32_bf16 v[164:167], a[180:183], a[116:119], v[164:167]\n"
    "s_add_u32 s37, s35, 0x10000\n"
    "buffer_load_dword v253, v255, s[8:11], s52 offen offset:0 sc1\n"
    "v_mfma_f32_16x16x32_bf16 v[164:167], a[176:179], a[112:115], v[164:167]\n"
    "s_cmp_lt_u32 s32, 2\n"
    "s_cselect_b32 s37, s37, 2.0\n"
    "ds_write_addtid_b32 v199 offset:2816\n"
    "v_mfma_f32_16x16x32_bf16 v[156:159], a[176:179], a[80:83], v[156:159]\n"
    "v_exp_f32_e32 v219, v219\n"
    "v_mfma_f32_16x16x32_bf16 v[156:159], a[180:183], a[84:87], v[156:159]\n"
    "s_waitcnt lgkmcnt(5)\n"
    "ds_write_addtid_b32 v206 offset:3584\n"
    "v_cvt_pk_bf16_f32 v214, v216, v217\n"
    "v_mfma_f32_16x16x32_bf16 v[156:159], a[184:187], a[88:91], v[156:159]\n"
    "buffer_load_dwordx4 v[144:147], v148, s[8:11], s81 offen offset:0 sc1\n"
    "v_mfma_f32_16x16x32_bf16 v[156:159], a[188:191], a[92:95], v[156:159]\n"
    "v_cvt_pk_bf16_f32 v215, v218, v219\n"
    "v_mfma_f32_16x16x32_bf16 v[164:167], a[188:191], a[124:127], v[164:167]\n"
    "ds_read_b128 a[172:175], v241 offset:6144\n"
    "v_mfma_f32_16x16x32_bf16 v[164:167], a[184:187], a[120:123], v[164:167]\n"
    "ds_read_b128 a[176:179], v242 offset:2048\n"
    "v_mfma_f32_16x16x32_bf16 v[172:175], a[184:187], a[152:155], v[172:175]\n"
    "ds_write_addtid_b32 v207 offset:3840\n"
    "v_mfma_f32_16x16x32_bf16 v[172:175], a[188:191], a[156:159], v[172:175]\n"
    "s_waitcnt lgkmcnt(5)\n"
    "ds_write_addtid_b32 v214 offset:4608\n"
    "v_mfma_f32_16x16x32_bf16 v[176:179], a[160:163], a[128:131], v[236:239]\n"
    "ds_write_addtid_b32 v215 offset:4864\n"
    "v_mfma_f32_16x16x32_bf16 v[176:179], a[164:167], a[132:135], v[176:179]\n"
    "ds_read_b128 a[184:187], v242 offset:6144\n"
    "v_mfma_f32_16x16x32_bf16 v[168:171], a[164:167], a[100:103], v[232:235]\n"
    "ds_read_b128 a[180:183], v243 offset:2048\n"
    "v_mfma_f32_16x16x32_bf16 v[168:171], a[160:163], a[96:99], v[168:171]\n"
    "s_waitcnt lgkmcnt(2)\n"
    "v_mfma_f32_16x16x32_bf16 v[160:163], a[160:163], a[64:67], v[228:231]\n"
    "s_barrier\n"
    "v_mfma_f32_16x16x32_bf16 v[160:163], a[164:167], a[68:71], v[160:163]\n"
    "ds_read2st64_b32 v[218:219], v249 offset0:18 offset1:19\n"
    "v_mfma_f32_16x16x32_bf16 v[160:163], a[168:171], a[72:75], v[160:163]\n"
    "s_add_u32 m0, s43, 16384\n"
    "ds_read_b64_tr_b16 a[160:161], v244 offset:24576\n"
    "ds_read2st64_b32 v[216:217], v249 offset0:16 offset1:17\n"
    "v_mfma_f32_16x16x32_bf16 v[160:163], a[172:175], a[76:79], v[160:163]\n"
    "buffer_load_dwordx4 v248, s[8:11], s40 offen offset:0 sc1 lds\n"
    "v_mfma_f32_16x16x32_bf16 v[168:171], a[172:175], a[108:111], v[168:171]\n"
    "ds_read_b128 a[188:191], v243 offset:6144\n"
    "ds_read2st64_b32 v[210:211], v249 offset0:14 offset1:15\n"
    "v_mfma_f32_16x16x32_bf16 v[168:171], a[168:171], a[104:107], v[168:171]\n"
    "ds_read2st64_b32 v[208:209], v249 offset0:12 offset1:13\n"
    "v_mfma_f32_16x16x32_bf16 v[176:179], a[168:171], a[136:139], v[176:179]\n"
    "ds_read_b64_tr_b16 a[162:163], v244 offset:26624\n"
    "v_mfma_f32_16x16x32_bf16 v[176:179], a[172:175], a[140:143], v[176:179]\n"
    "s_waitcnt lgkmcnt(7)\n"
    "ds_read2st64_b32 v[202:203], v249 offset0:10 offset1:11\n"
    "ds_read2st64_b32 v[200:201], v249 offset0:8 offset1:9\n"
    "v_mfma_f32_16x16x32_bf16 v[176:179], a[176:179], a[144:147], v[176:179]\n"
    "buffer_load_dwordx4 v248, s[8:11], s40 offen offset:1024 sc1 lds\n"
    "v_mfma_f32_16x16x32_bf16 v[176:179], a[180:183], a[148:151], v[176:179]\n"
    "ds_read_b64_tr_b16 a[164:165], v244 offset:57344\n"
    "ds_read2st64_b32 v[194:195], v249 offset0:6 offset1:7\n"
    "v_mfma_f32_16x16x32_bf16 v[168:171], a[180:183], a[116:119], v[168:171]\n"
    "ds_read_b64_tr_b16 a[168:169], v245 offset:24576\n"
    "ds_read2st64_b32 v[192:193], v249 offset0:4 offset1:5\n"
    "v_mfma_f32_16x16x32_bf16 v[168:171], a[176:179], a[112:115], v[168:171]\n"
    "ds_read_b64_tr_b16 a[170:171], v245 offset:26624\n"
    "ds_read2st64_b32 v[186:187], v249 offset0:2 offset1:3\n"
    "v_mfma_f32_16x16x32_bf16 v[160:163], a[176:179], a[80:83], v[160:163]\n"
    "ds_read_b64_tr_b16 a[166:167], v244 offset:59392\n"
    "ds_read2st64_b32 v[184:185], v249 offset0:0 offset1:1\n"
    "v_mfma_f32_16x16x32_bf16 v[160:163], a[180:183], a[84:87], v[160:163]\n"
    "s_waitcnt lgkmcnt(13)\n"
    "buffer_load_dwordx4 v248, s[8:11], s40 offen offset:2048 sc1 lds\n"
    "v_mfma_f32_16x16x32_bf16 v[160:163], a[184:187], a[88:91], v[160:163]\n"
    "ds_read_b64_tr_b16 a[176:177], v246 offset:24576\n"
    "v_mfma_f32_16x16x32_bf16 v[160:163], a[188:191], a[92:95], v[160:163]\n"
    "ds_read_b64_tr_b16 a[172:173], v245 offset:57344\n"
    "v_mfma_f32_16x16x32_bf16 v[168:171], a[188:191], a[124:127], v[168:171]\n"
    "ds_read_b64_tr_b16 a[178:179], v246 offset:26624\n"
    "v_mfma_f32_16x16x32_bf16 v[168:171], a[184:187], a[120:123], v[168:171]\n"
    "ds_read_b64_tr_b16 a[174:175], v245 offset:59392\n"
    "v_mfma_f32_16x16x32_bf16 v[176:179], a[184:187], a[152:155], v[176:179]\n"
    "buffer_load_dwordx4 v248, s[8:11], s40 offen offset:3072 sc1 lds\n"
    "v_mfma_f32_16x16x32_bf16 v[176:179], a[188:191], a[156:159], v[176:179]\n"
    "s_waitcnt lgkmcnt(4)\n"
    "ds_read_b64_tr_b16 a[184:185], v247 offset:24576\n"
    "v_mfma_f32_16x16x32_bf16 a[252:255], a[160:163], v[212:215], a[252:255]\n"
    "s_add_u32 m0, s43, 20480\n"
    "ds_read_b64_tr_b16 a[180:181], v246 offset:57344\n"
    "v_mfma_f32_16x16x32_bf16 a[252:255], a[164:167], v[216:219], a[252:255]\n"
    "s_waitcnt vmcnt(8)\n"
    "v_mfma_f32_16x16x32_bf16 a[248:251], a[164:167], v[208:211], a[248:251]\n"
    "s_barrier\n"
    "v_mfma_f32_16x16x32_bf16 a[248:251], a[160:163], v[204:207], a[248:251]\n"
    "buffer_load_dwordx4 v248, s[8:11], s41 offen offset:0 sc1 lds\n"
    "v_add_u32_e32 v249, s45, v252\n"
    "v_add_u32_e32 v252, s63, v252\n"
    "v_mfma_f32_16x16x32_bf16 a[244:247], a[160:163], v[196:199], a[244:247]\n"
    "s_mul_i32 s45, s33, 405504\n"
    "ds_read_b64_tr_b16 a[182:183], v246 offset:59392\n"
    "v_mfma_f32_16x16x32_bf16 a[244:247], a[164:167], v[200:203], a[244:247]\n"
    "s_add_u32 s45, s45, 16486400\n"
    "ds_read_b64_tr_b16 a[186:187], v247 offset:26624\n"
    "v_mfma_f32_16x16x32_bf16 a[240:243], a[164:167], v[192:195], a[240:243]\n"
    "s_lshl_b32 s46, s51, 12\n"
    "v_exp_f32_e32 v156, v156\n"
    "v_mfma_f32_16x16x32_bf16 a[240:243], a[160:163], v[188:191], a[240:243]\n"
    "s_lshl_b32 s48, s50, 15\n"
    "ds_read_b64_tr_b16 a[188:189], v247 offset:57344\n"
    "v_mfma_f32_16x16x32_bf16 a[236:239], a[160:163], v[180:183], a[236:239]\n"
    "s_add_u32 s46, s46, s48\n"
    "buffer_load_dwordx4 v248, s[8:11], s41 offen offset:1024 sc1 lds\n"
    "v_mfma_f32_16x16x32_bf16 a[236:239], a[164:167], v[184:187], a[236:239]\n"
    "s_waitcnt lgkmcnt(5)\n"
    "ds_read_b64_tr_b16 a[190:191], v247 offset:59392\n"
    "v_mfma_f32_16x16x32_bf16 v[0:3], a[168:171], v[180:183], v[0:3]\n"
    "s_cmp_eq_u32 s51, 0\n"
    "s_cselect_b32 s47, 2.0, 0\n"
    "v_mfma_f32_16x16x32_bf16 v[0:3], a[172:175], v[184:187], v[0:3]\n"
    "s_add_u32 s47, s47, 98304\n"
    "ds_read_b64_tr_b16 a[160:161], v244 offset:28672\n"
    "v_mfma_f32_16x16x32_bf16 v[4:7], a[172:175], v[192:195], v[4:7]\n"
    "s_lshl_b32 s61, s50, 9\n"
    "v_exp_f32_e32 v157, v157\n"
    "v_mfma_f32_16x16x32_bf16 v[4:7], a[168:171], v[188:191], v[4:7]\n"
    "s_add_u32 s61, s61, s47\n"
    "buffer_load_dwordx4 v248, s[8:11], s41 offen offset:2048 sc1 lds\n"
    "v_mfma_f32_16x16x32_bf16 v[8:11], a[168:171], v[196:199], v[8:11]\n"
    "s_add_u32 s48, s32, 3\n"
    "ds_read_b64_tr_b16 a[162:163], v244 offset:30720\n"
    "v_mfma_f32_16x16x32_bf16 v[8:11], a[172:175], v[200:203], v[8:11]\n"
    "s_and_b32 s48, s48, 3\n"
    "v_exp_f32_e32 v158, v158\n"
    "v_mfma_f32_16x16x32_bf16 v[12:15], a[172:175], v[208:211], v[12:15]\n"
    "s_mul_i32 s48, s48, 101376\n"
    "v_exp_f32_e32 v159, v159\n"
    "v_mfma_f32_16x16x32_bf16 v[12:15], a[168:171], v[204:207], v[12:15]\n"
    "s_add_u32 s48, s48, s45\n"
    "ds_read_b64_tr_b16 a[164:165], v244 offset:61440\n"
    "v_mfma_f32_16x16x32_bf16 v[16:19], a[168:171], v[212:215], v[16:19]\n"
    "s_add_u32 s29, s48, s46\n"
    "buffer_load_dwordx4 v248, s[8:11], s41 offen offset:3072 sc1 lds\n"
    "v_mfma_f32_16x16x32_bf16 v[16:19], a[172:175], v[216:219], v[16:19]\n"
    "s_waitcnt lgkmcnt(6)\n"
    "s_waitcnt vmcnt(9)\n"
    "v_cmp_ne_u32_e32 vcc, s39, v253\n"
    "v_mfma_f32_16x16x32_bf16 v[36:39], a[176:179], v[212:215], v[36:39]\n"
    "s_mov_b32 m0, s63\n"
    "ds_read_b64_tr_b16 a[166:167], v244 offset:63488\n"
    "v_mfma_f32_16x16x32_bf16 v[36:39], a[180:183], v[216:219], v[36:39]\n"
    "s_add_u32 s29, s29, 8192\n"
    "ds_read_b64_tr_b16 a[168:169], v245 offset:28672\n"
    "v_mfma_f32_16x16x32_bf16 v[32:35], a[180:183], v[208:211], v[32:35]\n"
    "s_cbranch_vccnz .Lat_wpoll\n"
    ".Lat_wpoll_ret:\n"
    "v_mfma_f32_16x16x32_bf16 v[32:35], a[176:179], v[204:207], v[32:35]\n"
    "s_add_u32 s89, s48, s61\n"
    "ds_read_b64_tr_b16 a[170:171], v245 offset:30720\n"
    "v_mfma_f32_16x16x32_bf16 v[28:31], a[176:179], v[196:199], v[28:31]\n"
    "s_add_u32 s89, s89, 128\n"
    "buffer_store_dword v254, off, s[8:11], s84 offset:0 sc1\n"
    "v_mfma_f32_16x16x32_bf16 v[28:31], a[180:183], v[200:203], v[28:31]\n"
    "s_add_u32 s62, s57, 0\n"
    "v_cvt_pk_bf16_f32 v156, v156, v157\n"
    "v_mfma_f32_16x16x32_bf16 v[24:27], a[180:183], v[192:195], v[24:27]\n"
    "s_add_u32 s48, s32, 2\n"
    "v_exp_f32_e32 v164, v164\n"
    "v_mfma_f32_16x16x32_bf16 v[24:27], a[176:179], v[188:191], v[24:27]\n"
    "s_and_b32 s48, s48, 3\n"
    "ds_read_b64_tr_b16 a[172:173], v245 offset:61440\n"
    "v_mfma_f32_16x16x32_bf16 v[20:23], a[176:179], v[180:183], v[20:23]\n"
    "s_mul_i32 s48, s48, 101376\n"
    "v_exp_f32_e32 v165, v165\n"
    "v_mfma_f32_16x16x32_bf16 v[20:23], a[180:183], v[184:187], v[20:23]\n"
    "s_waitcnt lgkmcnt(7)\n"
    "ds_read_b64_tr_b16 a[174:175], v245 offset:63488\n"
    "v_mfma_f32_16x16x32_bf16 v[40:43], a[184:187], v[180:183], v[40:43]\n"
    "s_waitcnt vmcnt(9)\n"
    "buffer_load_dwordx4 a[216:219], v250, s[8:11], s62 offen offset:0 sc1\n"
    "v_xor_b32_e32 v144, s39, v144\n"
    "v_mfma_f32_16x16x32_bf16 v[40:43], a[188:191], v[184:187], v[40:43]\n"
    "s_add_u32 s48, s48, s45\n"
    "ds_read_b64_tr_b16 a[176:177], v246 offset:28672\n"
    "v_xor_b32_e32 v145, s39, v145\n"
    "v_mfma_f32_16x16x32_bf16 v[44:47], a[188:191], v[192:195], v[44:47]\n"
    "s_add_u32 s28, s48, s46\n"
    "ds_write_addtid_b32 v156 offset:0\n"
    "v_xor_b32_e32 v146, s39, v146\n"
    "v_mfma_f32_16x16x32_bf16 v[44:47], a[184:187], v[188:191], v[44:47]\n"
    "s_add_u32 s28, s28, 16384\n"
    "v_xor_b32_e32 v147, s39, v147\n"
    "v_mfma_f32_16x16x32_bf16 v[48:51], a[184:187], v[196:199], v[48:51]\n"
    "s_add_u32 s88, s48, s61\n"
    "v_or3_b32 v144, v144, v145, v146\n"
    "v_mfma_f32_16x16x32_bf16 v[48:51], a[188:191], v[200:203], v[48:51]\n"
    "s_add_u32 s88, s88, 256\n"
    "v_or_b32_e32 v144, v144, v147\n"
    "v_mfma_f32_16x16x32_bf16 v[52:55], a[188:191], v[208:211], v[52:55]\n"
    "s_add_u32 s48, s32, 1\n"
    "buffer_load_dwordx4 a[220:223], v250, s[8:11], s62 offen offset:1024 sc1\n"
    "v_cmp_ne_u32_e32 vcc, 0, v144\n"
    "v_mfma_f32_16x16x32_bf16 v[52:55], a[184:187], v[204:207], v[52:55]\n"
    "s_and_b32 s48, s48, 3\n"
    "ds_read_b64_tr_b16 a[178:179], v246 offset:30720\n"
    "v_mfma_f32_16x16x32_bf16 v[56:59], a[184:187], v[212:215], v[56:59]\n"
    "s_mul_i32 s48, s48, 101376\n"
    "ds_read_b64_tr_b16 a[180:181], v246 offset:61440\n"
    "v_mfma_f32_16x16x32_bf16 v[56:59], a[188:191], v[216:219], v[56:59]\n"
    "s_waitcnt lgkmcnt(8)\n"
    "v_cvt_pk_bf16_f32 v157, v158, v159\n"
    "v_mfma_f32_16x16x32_bf16 v[76:79], a[160:163], v[212:215], v[76:79]\n"
    "s_add_u32 s48, s48, s45\n"
    "ds_read_b64_tr_b16 a[182:183], v246 offset:63488\n"
    "v_mfma_f32_16x16x32_bf16 v[76:79], a[164:167], v[216:219], v[76:79]\n"
    "s_add_u32 s41, s48, s46\n"
    "ds_read_b64_tr_b16 a[184:185], v247 offset:28672\n"
    "v_mfma_f32_16x16x32_bf16 v[72:75], a[164:167], v[208:211], v[72:75]\n"
    "s_add_u32 s41, s41, 24576\n"
    "buffer_load_dwordx4 a[224:227], v250, s[8:11], s62 offen offset:2048 sc1\n"
    "v_mfma_f32_16x16x32_bf16 v[72:75], a[160:163], v[204:207], v[72:75]\n"
    "s_add_u32 s87, s48, s61\n"
    "v_exp_f32_e32 v166, v166\n"
    "v_mfma_f32_16x16x32_bf16 v[68:71], a[160:163], v[196:199], v[68:71]\n"
    "s_add_u32 s87, s87, 384\n"
    "ds_read_b64_tr_b16 a[186:187], v247 offset:30720\n"
    "v_mfma_f32_16x16x32_bf16 v[68:71], a[164:167], v[200:203], v[68:71]\n"
    "s_mul_i32 s48, s32, 101376\n"
    "v_exp_f32_e32 v167, v167\n"
    "v_mfma_f32_16x16x32_bf16 v[64:67], a[164:167], v[192:195], v[64:67]\n"
    "s_add_u32 s48, s48, s45\n"
    "ds_write_addtid_b32 v157 offset:256\n"
    "v_cvt_pk_bf16_f32 v164, v164, v165\n"
    "v_mfma_f32_16x16x32_bf16 v[64:67], a[160:163], v[188:191], v[64:67]\n"
    "s_add_u32 s59, s48, s46\n"
    "ds_read_b64_tr_b16 a[188:189], v247 offset:61440\n"
    "v_mfma_f32_16x16x32_bf16 v[60:63], a[160:163], v[180:183], v[60:63]\n"
    "s_add_u32 s60, s48, s61\n"
    "buffer_load_dwordx4 a[228:231], v250, s[8:11], s62 offen offset:3072 sc1\n"
    "v_mfma_f32_16x16x32_bf16 v[60:63], a[164:167], v[184:187], v[60:63]\n"
    "s_waitcnt lgkmcnt(9)\n"
    "v_exp_f32_e32 v172, v172\n"
    "v_mfma_f32_16x16x32_bf16 v[80:83], a[168:171], v[180:183], v[80:83]\n"
    "s_add_u32 s62, s57, 4096\n"
    "ds_read_b64_tr_b16 a[190:191], v247 offset:63488\n"
    "v_mfma_f32_16x16x32_bf16 v[80:83], a[172:175], v[184:187], v[80:83]\n"
    "s_and_b32 s48, s32, 1\n"
    "v_exp_f32_e32 v173, v173\n"
    "v_mfma_f32_16x16x32_bf16 v[84:87], a[172:175], v[192:195], v[84:87]\n"
    "s_xor_b32 s48, s48, s50\n"
    "ds_read_b128 a[160:163], v240 offset:8192\n"
    "v_mfma_f32_16x16x32_bf16 v[84:87], a[168:171], v[188:191], v[84:87]\n"
    "s_sub_u32 s61, s32, s48\n"
    "ds_write_addtid_b32 v164 offset:1024\n"
    "v_cvt_pk_bf16_f32 v165, v166, v167\n"
    "v_mfma_f32_16x16x32_bf16 v[88:91], a[168:171], v[196:199], v[88:91]\n"
    "s_and_b32 s61, s61, 3\n"
    "buffer_load_dwordx4 a[232:235], v250, s[8:11], s62 offen offset:0 sc1\n"
    "v_mfma_f32_16x16x32_bf16 v[88:91], a[172:175], v[200:203], v[88:91]\n"
    "s_add_u32 s61, s61, 8\n"
    "v_exp_f32_e32 v174, v174\n"
    "v_mfma_f32_16x16x32_bf16 v[92:95], a[172:175], v[208:211], v[92:95]\n"
    "s_mul_i32 s48, s48, 101376\n"
    "v_exp_f32_e32 v175, v175\n"
    "v_mfma_f32_16x16x32_bf16 v[92:95], a[168:171], v[204:207], v[92:95]\n"
    "s_add_u32 s48, s48, s45\n"
    "ds_write_addtid_b32 v165 offset:1280\n"
    "v_cvt_pk_bf16_f32 v172, v172, v173\n"
    "v_mfma_f32_16x16x32_bf16 v[96:99], a[168:171], v[212:215], v[96:99]\n"
    "s_lshl_b32 s40, s61, 13\n"
    "ds_read_b128 a[164:167], v241 offset:8192\n"
    "v_mfma_f32_16x16x32_bf16 v[96:99], a[172:175], v[216:219], v[96:99]\n"
    "s_waitcnt lgkmcnt(9)\n"
    "v_exp_f32_e32 v160, v160\n"
    "v_mfma_f32_16x16x32_bf16 v[116:119], a[176:179], v[212:215], v[116:119]\n"
    "s_add_u32 s40, s40, s48\n"
    "v_exp_f32_e32 v161, v161\n"
    "v_mfma_f32_16x16x32_bf16 v[116:119], a[180:183], v[216:219], v[116:119]\n"
    "s_lshl_b32 s46, s51, 12\n"
    "v_cvt_pk_bf16_f32 v173, v174, v175\n"
    "v_mfma_f32_16x16x32_bf16 v[112:115], a[180:183], v[208:211], v[112:115]\n"
    "s_add_u32 s40, s40, s46\n"
    "ds_read_b128 a[168:171], v240 offset:12288\n"
    "v_mfma_f32_16x16x32_bf16 v[112:115], a[176:179], v[204:207], v[112:115]\n"
    "s_lshl_b32 s86, s61, 7\n"
    "v_exp_f32_e32 v162, v162\n"
    "v_mfma_f32_16x16x32_bf16 v[108:111], a[176:179], v[196:199], v[108:111]\n"
    "s_add_u32 s86, s86, s48\n"
    "v_exp_f32_e32 v163, v163\n"
    "v_mfma_f32_16x16x32_bf16 v[108:111], a[180:183], v[200:203], v[108:111]\n"
    "s_add_u32 s86, s86, s47\n"
    "v_cvt_pk_bf16_f32 v158, v160, v161\n"
    "v_mfma_f32_16x16x32_bf16 v[104:107], a[180:183], v[192:195], v[104:107]\n"
    "s_lshl_b32 s46, %2, 7\n"
    "v_exp_f32_e32 v168, v168\n"
    "v_mfma_f32_16x16x32_bf16 v[104:107], a[176:179], v[188:191], v[104:107]\n"
    "s_lshl_b32 s55, %2, 15\n"
    "v_exp_f32_e32 v169, v169\n"
    "v_mfma_f32_16x16x32_bf16 v[100:103], a[176:179], v[180:183], v[100:103]\n"
    "s_sub_u32 s54, s55, 24576\n"
    "ds_read_b128 a[172:175], v241 offset:12288\n"
    "v_mfma_f32_16x16x32_bf16 v[100:103], a[180:183], v[184:187], v[100:103]\n"
    "s_waitcnt lgkmcnt(6)\n"
    "v_cvt_pk_bf16_f32 v159, v162, v163\n"
    "v_mfma_f32_16x16x32_bf16 v[120:123], a[184:187], v[180:183], v[120:123]\n"
    "s_add_u32 s55, s55, 32768\n"
    "v_exp_f32_e32 v170, v170\n"
    "v_mfma_f32_16x16x32_bf16 v[120:123], a[188:191], v[184:187], v[120:123]\n"
    "s_lshl2_add_u32 s38, s34, %2\n"
    "v_exp_f32_e32 v171, v171\n"
    "v_mfma_f32_16x16x32_bf16 v[124:127], a[188:191], v[192:195], v[124:127]\n"
    "s_lshl1_add_u32 s38, s38, s49\n"
    "ds_read_b128 a[176:179], v242 offset:8192\n"
    "v_mfma_f32_16x16x32_bf16 v[124:127], a[184:187], v[188:191], v[124:127]\n"
    "s_lshl_b32 s38, s38, 7\n"
    "v_cvt_pk_bf16_f32 v166, v168, v169\n"
    "v_mfma_f32_16x16x32_bf16 v[128:131], a[184:187], v[196:199], v[128:131]\n"
    "s_add_u32 s38, s38, 5644288\n"
    "v_exp_f32_e32 v176, v176\n"
    "v_mfma_f32_16x16x32_bf16 v[128:131], a[188:191], v[200:203], v[128:131]\n"
    "s_lshl3_add_u32 s90, s34, s49\n"
    "ds_write_addtid_b32 v172 offset:2048\n"
    "v_mfma_f32_16x16x32_bf16 v[132:135], a[188:191], v[208:211], v[132:135]\n"
    "s_lshl2_add_u32 s90, s90, %2\n"
    "v_exp_f32_e32 v177, v177\n"
    "v_mfma_f32_16x16x32_bf16 v[132:135], a[184:187], v[204:207], v[132:135]\n"
    "s_lshl2_add_u32 s90, s90, 5906432\n"
    "ds_write_addtid_b32 v173 offset:2304\n"
    "v_mfma_f32_16x16x32_bf16 v[136:139], a[184:187], v[212:215], v[136:139]\n"
    "s_lshl_b32 s45, s51, 15\n"
    "ds_read_b128 a[180:183], v243 offset:8192\n"
    "v_mfma_f32_16x16x32_bf16 v[136:139], a[188:191], v[216:219], v[136:139]\n"
    "s_waitcnt lgkmcnt(6)\n"
    "v_cvt_pk_bf16_f32 v167, v170, v171\n"
    "v_mfma_f32_16x16x32_bf16 v[212:215], a[160:163], a[128:131], v[236:239]\n"
    "s_sub_u32 s45, 24576, s45\n"
    "ds_write_addtid_b32 v158 offset:512\n"
    "v_mfma_f32_16x16x32_bf16 v[212:215], a[164:167], a[132:135], v[212:215]\n"
    "s_add_u32 s47, s46, 6000640\n"
    "ds_write_addtid_b32 v159 offset:768\n"
    "v_mfma_f32_16x16x32_bf16 v[204:207], a[164:167], a[100:103], v[232:235]\n"
    "s_andn2_b32 s48, s38, 0xc00\n"
    "ds_read_b128 a[184:187], v242 offset:12288\n"
    "v_mfma_f32_16x16x32_bf16 v[204:207], a[160:163], a[96:99], v[204:207]\n"
    "s_andn2_b32 s61, s90, 0x18c\n"
    "v_exp_f32_e32 v178, v178\n"
    "v_mfma_f32_16x16x32_bf16 v[196:199], a[160:163], a[64:67], v[228:231]\n"
    "s_cmp_eq_u32 %2, 1\n"
    "s_cselect_b32 s31, s36, s35\n"
    "v_mfma_f32_16x16x32_bf16 v[196:199], a[164:167], a[68:71], v[196:199]\n"
    "s_waitcnt lgkmcnt(7)\n"
    "v_exp_f32_e32 v179, v179\n"
    "v_mfma_f32_16x16x32_bf16 v[196:199], a[168:171], a[72:75], v[196:199]\n"
    "s_cmp_eq_u32 %2, 2\n"
    "s_cselect_b32 s31, s37, s31\n"
    "v_mfma_f32_16x16x32_bf16 v[196:199], a[172:175], a[76:79], v[196:199]\n"
    "v_lshlrev_b32_e32 v188, 3, v250\n"
    "v_mfma_f32_16x16x32_bf16 v[204:207], a[172:175], a[108:111], v[204:207]\n"
    "buffer_load_dword v189, v188, s[12:15], s31 offen offset:0\n"
    "v_mfma_f32_16x16x32_bf16 v[204:207], a[168:171], a[104:107], v[204:207]\n"
    "s_cmp_eq_u64 vcc, 0\n"
    "s_cselect_b32 s56, s47, 0x20000000\n"
    "s_cselect_b32 s44, 0, 1\n"
    "v_mfma_f32_16x16x32_bf16 v[212:215], a[168:171], a[136:139], v[212:215]\n"
    "ds_read_b128 a[188:191], v243 offset:12288\n"
    "v_mfma_f32_16x16x32_bf16 v[212:215], a[172:175], a[140:143], v[212:215]\n"
    "s_waitcnt lgkmcnt(4)\n"
    "ds_read_b128 a[160:163], v240 offset:10240\n"
    "v_mfma_f32_16x16x32_bf16 v[212:215], a[176:179], a[144:147], v[212:215]\n"
    "v_cvt_pk_bf16_f32 v174, v176, v177\n"
    "v_mfma_f32_16x16x32_bf16 v[212:215], a[180:183], a[148:151], v[212:215]\n"
    "v_cvt_pk_bf16_f32 v175, v178, v179\n"
    "v_mfma_f32_16x16x32_bf16 v[204:207], a[180:183], a[116:119], v[204:207]\n"
    "ds_read_b128 a[164:167], v241 offset:10240\n"
    "v_mfma_f32_16x16x32_bf16 v[204:207], a[176:179], a[112:115], v[204:207]\n"
    "ds_read_b128 a[168:171], v240 offset:14336\n"
    "v_mfma_f32_16x16x32_bf16 v[196:199], a[176:179], a[80:83], v[196:199]\n"
    "ds_write_addtid_b32 v166 offset:1536\n"
    "v_mfma_f32_16x16x32_bf16 v[196:199], a[180:183], a[84:87], v[196:199]\n"
    "s_waitcnt lgkmcnt(4)\n"
    "ds_write_addtid_b32 v167 offset:1792\n"
    "v_mfma_f32_16x16x32_bf16 v[196:199], a[184:187], a[88:91], v[196:199]\n"
    "ds_write_addtid_b32 v174 offset:2560\n"
    "v_mfma_f32_16x16x32_bf16 v[196:199], a[188:191], a[92:95], v[196:199]\n"
    "ds_write_addtid_b32 v175 offset:2816\n"
    "v_mfma_f32_16x16x32_bf16 v[204:207], a[188:191], a[124:127], v[204:207]\n"
    "ds_read_b128 a[172:175], v241 offset:14336\n"
    "v_mfma_f32_16x16x32_bf16 v[204:207], a[184:187], a[120:123], v[204:207]\n"
    "ds_read_b128 a[176:179], v242 offset:10240\n"
    "v_mfma_f32_16x16x32_bf16 v[212:215], a[184:187], a[152:155], v[212:215]\n"
    "v_exp_f32_e32 v196, v196\n"
    "v_mfma_f32_16x16x32_bf16 v[212:215], a[188:191], a[156:159], v[212:215]\n"
    "s_waitcnt lgkmcnt(7)\n"
    "v_exp_f32_e32 v197, v197\n"
    "v_mfma_f32_16x16x32_bf16 v[216:219], a[160:163], a[128:131], v[236:239]\n"
    "ds_read_b128 a[184:187], v242 offset:14336\n"
    "v_mfma_f32_16x16x32_bf16 v[216:219], a[164:167], a[132:135], v[216:219]\n"
    "s_waitcnt lgkmcnt(3)\n"
    "s_waitcnt vmcnt(7)\n"
    "v_mfma_f32_16x16x32_bf16 v[208:211], a[164:167], a[100:103], v[232:235]\n"
    "s_barrier\n"
    "v_mfma_f32_16x16x32_bf16 v[208:211], a[160:163], a[96:99], v[208:211]\n"
    "ds_read_b128 a[180:183], v243 offset:10240\n"
    "ds_read2st64_b32 a[196:197], v249 offset0:0 offset1:1\n"
    "v_mfma_f32_16x16x32_bf16 v[200:203], a[160:163], a[64:67], v[228:231]\n"
    "s_add_u32 s31, s56, s35\n"
    "v_exp_f32_e32 v198, v198\n"
    "v_mfma_f32_16x16x32_bf16 v[200:203], a[164:167], a[68:71], v[200:203]\n"
    "s_waitcnt lgkmcnt(4)\n"
    "v_exp_f32_e32 v199, v199\n"
    "v_mfma_f32_16x16x32_bf16 v[200:203], a[168:171], a[72:75], v[200:203]\n"
    "s_add_u32 m0, s55, 24576\n"
    "ds_read2st64_b32 a[198:199], v249 offset0:2 offset1:3\n"
    "v_cvt_pk_bf16_f32 v196, v196, v197\n"
    "v_mfma_f32_16x16x32_bf16 v[200:203], a[172:175], a[76:79], v[200:203]\n"
    "buffer_load_dwordx4 v251, s[8:11], s31 offen offset:0 sc1 lds\n"
    "v_mfma_f32_16x16x32_bf16 v[208:211], a[172:175], a[108:111], v[208:211]\n"
    "s_mov_b32 m0, s63\n"
    "ds_read_b128 a[188:191], v243 offset:14336\n"
    "ds_read2st64_b32 a[192:193], v252 offset0:0 offset1:1\n"
    "v_mfma_f32_16x16x32_bf16 v[208:211], a[168:171], a[104:107], v[208:211]\n"
    "ds_read_b128 a[160:163], v240 offset:16384\n"
    "ds_read2st64_b32 a[194:195], v252 offset0:2 offset1:3\n"
    "v_mfma_f32_16x16x32_bf16 v[216:219], a[168:171], a[136:139], v[216:219]\n"
    "v_exp_f32_e32 v204, v204\n"
    "v_mfma_f32_16x16x32_bf16 v[216:219], a[172:175], a[140:143], v[216:219]\n"
    "s_waitcnt lgkmcnt(6)\n"
    "v_exp_f32_e32 v205, v205\n"
    "v_mfma_f32_16x16x32_bf16 v[216:219], a[176:179], a[144:147], v[216:219]\n"
    "s_add_u32 s31, s31, 0x1000\n"
    "ds_write_addtid_b32 v196 offset:3072\n"
    "v_cvt_pk_bf16_f32 v197, v198, v199\n"
    "v_mfma_f32_16x16x32_bf16 v[216:219], a[180:183], a[148:151], v[216:219]\n"
    "s_add_u32 m0, s55, 25600\n"
    "ds_read_b128 a[168:171], v240 offset:20480\n"
    "ds_read2st64_b32 a[204:205], v249 offset0:4 offset1:5\n"
    "v_mfma_f32_16x16x32_bf16 v[208:211], a[180:183], a[116:119], v[208:211]\n"
    "buffer_load_dwordx4 v251, s[8:11], s31 offen offset:0 sc1 lds\n"
    "v_mfma_f32_16x16x32_bf16 v[208:211], a[176:179], a[112:115], v[208:211]\n"
    "s_mov_b32 m0, s63\n"
    "ds_read_b128 a[164:167], v241 offset:16384\n"
    "ds_read2st64_b32 a[206:207], v249 offset0:6 offset1:7\n"
    "v_mfma_f32_16x16x32_bf16 v[200:203], a[176:179], a[80:83], v[200:203]\n"
    "v_exp_f32_e32 v206, v206\n"
    "v_mfma_f32_16x16x32_bf16 v[200:203], a[180:183], a[84:87], v[200:203]\n"
    "s_waitcnt lgkmcnt(8)\n"
    "v_exp_f32_e32 v207, v207\n"
    "v_mfma_f32_16x16x32_bf16 v[200:203], a[184:187], a[88:91], v[200:203]\n"
    "s_add_u32 s31, s56, s36\n"
    "ds_write_addtid_b32 v197 offset:3328\n"
    "v_cvt_pk_bf16_f32 v204, v204, v205\n"
    "v_mfma_f32_16x16x32_bf16 v[200:203], a[188:191], a[92:95], v[200:203]\n"
    "s_add_u32 m0, s55, 26624\n"
    "ds_read_b128 a[176:179], v242 offset:16384\n"
    "ds_read2st64_b32 a[200:201], v252 offset0:4 offset1:5\n"
    "v_mfma_f32_16x16x32_bf16 v[208:211], a[188:191], a[124:127], v[208:211]\n"
    "buffer_load_dwordx4 v251, s[8:11], s31 offen offset:0 sc1 lds\n"
    "v_mfma_f32_16x16x32_bf16 v[208:211], a[184:187], a[120:123], v[208:211]\n"
    "s_mov_b32 m0, s63\n"
    "ds_read_b128 a[172:175], v241 offset:20480\n"
    "ds_read2st64_b32 a[202:203], v252 offset0:6 offset1:7\n"
    "v_mfma_f32_16x16x32_bf16 v[216:219], a[184:187], a[152:155], v[216:219]\n"
    "ds_write_addtid_b32 v204 offset:4096\n"
    "v_exp_f32_e32 v212, v212\n"
    "v_mfma_f32_16x16x32_bf16 v[216:219], a[188:191], a[156:159], v[216:219]\n"
    "s_waitcnt lgkmcnt(7)\n"
    "ds_read2st64_b32 a[212:213], v249 offset0:8 offset1:9\n"
    "v_exp_f32_e32 v213, v213\n"
    "v_mfma_f32_16x16x32_bf16 v[172:175], a[160:163], a[128:131], v[236:239]\n"
    "s_add_u32 s31, s31, 0x1000\n"
    "v_cvt_pk_bf16_f32 v205, v206, v207\n"
    "v_exp_f32_e32 v214, v214\n"
    "v_mfma_f32_16x16x32_bf16 v[172:175], a[164:167], a[132:135], v[172:175]\n"
    "s_add_u32 m0, s55, 27648\n"
    "ds_read_b128 a[184:187], v242 offset:20480\n"
    "v_exp_f32_e32 v215, v215\n"
    "v_mfma_f32_16x16x32_bf16 v[164:167], a[164:167], a[100:103], v[232:235]\n"
    "buffer_load_dwordx4 v251, s[8:11], s31 offen offset:0 sc1 lds\n"
    "ds_read2st64_b32 a[214:215], v249 offset0:10 offset1:11\n"
    "v_mfma_f32_16x16x32_bf16 v[164:167], a[160:163], a[96:99], v[164:167]\n"
    "s_mov_b32 m0, s63\n"
    "ds_read_b128 a[180:183], v243 offset:16384\n"
    "v_cvt_pk_bf16_f32 v212, v212, v213\n"
    "v_mfma_f32_16x16x32_bf16 v[156:159], a[160:163], a[64:67], v[228:231]\n"
    "ds_write_addtid_b32 v205 offset:4352\n"
    "v_exp_f32_e32 v200, v200\n"
    "v_mfma_f32_16x16x32_bf16 v[156:159], a[164:167], a[68:71], v[156:159]\n"
    "s_waitcnt lgkmcnt(7)\n"
    "ds_write_addtid_b32 v212 offset:5120\n"
    "v_exp_f32_e32 v201, v201\n"
    "v_mfma_f32_16x16x32_bf16 v[156:159], a[168:171], a[72:75], v[156:159]\n"
    "s_add_u32 s31, s56, s37\n"
    "v_cvt_pk_bf16_f32 v213, v214, v215\n"
    "v_exp_f32_e32 v202, v202\n"
    "v_mfma_f32_16x16x32_bf16 v[156:159], a[172:175], a[76:79], v[156:159]\n"
    "s_add_u32 m0, s55, 28672\n"
    "ds_read_b128 a[160:163], v240 offset:18432\n"
    "v_exp_f32_e32 v203, v203\n"
    "v_mfma_f32_16x16x32_bf16 v[164:167], a[172:175], a[108:111], v[164:167]\n"
    "buffer_load_dwordx4 v251, s[8:11], s31 offen offset:0 sc1 lds\n"
    "ds_read2st64_b32 a[208:209], v252 offset0:8 offset1:9\n"
    "v_mfma_f32_16x16x32_bf16 v[164:167], a[168:171], a[104:107], v[164:167]\n"
    "s_mov_b32 m0, s63\n"
    "ds_read_b128 a[188:191], v243 offset:20480\n"
    "v_cvt_pk_bf16_f32 v198, v200, v201\n"
    "v_mfma_f32_16x16x32_bf16 v[172:175], a[168:171], a[136:139], v[172:175]\n"
    "ds_write_addtid_b32 v213 offset:5376\n"
    "v_exp_f32_e32 v208, v208\n"
    "v_mfma_f32_16x16x32_bf16 v[172:175], a[172:175], a[140:143], v[172:175]\n"
    "s_waitcnt lgkmcnt(6)\n"
    "ds_write_addtid_b32 v198 offset:3584\n"
    "v_exp_f32_e32 v209, v209\n"
    "v_mfma_f32_16x16x32_bf16 v[172:175], a[176:179], a[144:147], v[172:175]\n"
    "s_add_u32 s31, s31, 0x1000\n"
    "v_cvt_pk_bf16_f32 v199, v202, v203\n"
    "v_exp_f32_e32 v210, v210\n"
    "v_mfma_f32_16x16x32_bf16 v[172:175], a[180:183], a[148:151], v[172:175]\n"
    "s_add_u32 m0, s55, 29696\n"
    "ds_read_b128 a[168:171], v240 offset:22528\n"
    "v_exp_f32_e32 v211, v211\n"
    "v_mfma_f32_16x16x32_bf16 v[164:167], a[180:183], a[116:119], v[164:167]\n"
    "buffer_load_dwordx4 v251, s[8:11], s31 offen offset:0 sc1 lds\n"
    "ds_read2st64_b32 a[210:211], v252 offset0:10 offset1:11\n"
    "v_mfma_f32_16x16x32_bf16 v[164:167], a[176:179], a[112:115], v[164:167]\n"
    "s_mov_b32 m0, s63\n"
    "ds_read_b128 a[164:167], v241 offset:18432\n"
    "v_cvt_pk_bf16_f32 v206, v208, v209\n"
    "v_mfma_f32_16x16x32_bf16 v[156:159], a[176:179], a[80:83], v[156:159]\n"
    "ds_write_addtid_b32 v199 offset:3840\n"
    "v_exp_f32_e32 v216, v216\n"
    "v_mfma_f32_16x16x32_bf16 v[156:159], a[180:183], a[84:87], v[156:159]\n"
    "s_waitcnt lgkmcnt(6)\n"
    "ds_write_addtid_b32 v206 offset:4608\n"
    "v_exp_f32_e32 v217, v217\n"
    "v_mfma_f32_16x16x32_bf16 v[156:159], a[184:187], a[88:91], v[156:159]\n"
    "ds_read_b128 a[176:179], v242 offset:18432\n"
    "v_cvt_pk_bf16_f32 v207, v210, v211\n"
    "v_mfma_f32_16x16x32_bf16 v[156:159], a[188:191], a[92:95], v[156:159]\n"
    "v_mfma_f32_16x16x32_bf16 v[164:167], a[188:191], a[124:127], v[164:167]\n"
    "s_barrier\n"
    "v_mfma_f32_16x16x32_bf16 v[164:167], a[184:187], a[120:123], v[164:167]\n"
    "ds_read_b128 a[172:175], v241 offset:22528\n"
    "v_exp_f32_e32 v218, v218\n"
    "v_mfma_f32_16x16x32_bf16 v[172:175], a[184:187], a[152:155], v[172:175]\n"
    "v_exp_f32_e32 v156, v156\n"
    "v_mfma_f32_16x16x32_bf16 v[172:175], a[188:191], a[156:159], v[172:175]\n"
    "s_waitcnt lgkmcnt(4)\n"
    "v_exp_f32_e32 v157, v157\n"
    "v_mfma_f32_16x16x32_bf16 v[176:179], a[160:163], a[128:131], v[236:239]\n"
    "v_exp_f32_e32 v158, v158\n"
    "v_mfma_f32_16x16x32_bf16 v[176:179], a[164:167], a[132:135], v[176:179]\n"
    "ds_write_addtid_b32 v207 offset:4864\n"
    "v_exp_f32_e32 v159, v159\n"
    "v_mfma_f32_16x16x32_bf16 v[168:171], a[164:167], a[100:103], v[232:235]\n"
    "ds_read_b128 a[180:183], v243 offset:18432\n"
    "v_exp_f32_e32 v219, v219\n"
    "v_mfma_f32_16x16x32_bf16 v[168:171], a[160:163], a[96:99], v[168:171]\n"
    "ds_read_b128 a[184:187], v242 offset:22528\n"
    "v_cvt_pk_bf16_f32 v214, v216, v217\n"
    "v_mfma_f32_16x16x32_bf16 v[160:163], a[160:163], a[64:67], v[228:231]\n"
    "v_cvt_pk_bf16_f32 v156, v156, v157\n"
    "v_cvt_pk_bf16_f32 v215, v218, v219\n"
    "v_mfma_f32_16x16x32_bf16 v[160:163], a[164:167], a[68:71], v[160:163]\n"
    "s_waitcnt lgkmcnt(3)\n"
    "v_exp_f32_e32 v164, v164\n"
    "v_mfma_f32_16x16x32_bf16 v[160:163], a[168:171], a[72:75], v[160:163]\n"
    "v_exp_f32_e32 v165, v165\n"
    "v_mfma_f32_16x16x32_bf16 v[160:163], a[172:175], a[76:79], v[160:163]\n"
    "ds_read_b64_tr_b16 a[160:161], v244 offset:0\n"
    "ds_write_addtid_b32 v156 offset:0\n"
    "v_mfma_f32_16x16x32_bf16 v[168:171], a[172:175], a[108:111], v[168:171]\n"
    "v_and_b32_e32 v248, 0xf0, v250\n"
    "v_lshrrev_b32_e32 v231, 3, v248\n"
    "v_mfma_f32_16x16x32_bf16 v[168:171], a[168:171], a[104:107], v[168:171]\n"
    "ds_read_b128 a[188:191], v243 offset:22528\n"
    "v_cvt_pk_bf16_f32 v157, v158, v159\n"
    "v_mfma_f32_16x16x32_bf16 v[176:179], a[168:171], a[136:139], v[176:179]\n"
    "s_lshr_b32 s31, s35, 8\n"
    "ds_read_b64_tr_b16 a[162:163], v244 offset:2048\n"
    "v_exp_f32_e32 v166, v166\n"
    "v_mfma_f32_16x16x32_bf16 v[176:179], a[172:175], a[140:143], v[176:179]\n"
    "s_waitcnt lgkmcnt(5)\n"
    "buffer_load_ushort v228, v231, s[24:27], s31 offen\n"
    "v_mfma_f32_16x16x32_bf16 v[176:179], a[176:179], a[144:147], v[176:179]\n"
    "s_lshr_b32 s31, s36, 8\n"
    "ds_read_b64_tr_b16 a[168:169], v245 offset:0\n"
    "v_exp_f32_e32 v167, v167\n"
    "v_mfma_f32_16x16x32_bf16 v[176:179], a[180:183], a[148:151], v[176:179]\n"
    "ds_read_b64_tr_b16 a[164:165], v244 offset:32768\n"
    "ds_write_addtid_b32 v157 offset:256\n"
    "v_mfma_f32_16x16x32_bf16 v[168:171], a[180:183], a[116:119], v[168:171]\n"
    "ds_read_b64_tr_b16 a[170:171], v245 offset:2048\n"
    "v_cvt_pk_bf16_f32 v164, v164, v165\n"
    "v_mfma_f32_16x16x32_bf16 v[168:171], a[176:179], a[112:115], v[168:171]\n"
    "buffer_load_ushort v229, v231, s[24:27], s31 offen\n"
    "v_mfma_f32_16x16x32_bf16 v[160:163], a[176:179], a[80:83], v[160:163]\n"
    "s_lshr_b32 s31, s37, 8\n"
    "ds_read_b64_tr_b16 a[166:167], v244 offset:34816\n"
    "v_exp_f32_e32 v172, v172\n"
    "v_mfma_f32_16x16x32_bf16 v[160:163], a[180:183], a[84:87], v[160:163]\n"
    "s_waitcnt lgkmcnt(6)\n"
    "ds_write_addtid_b32 v214 offset:5632\n"
    "v_exp_f32_e32 v173, v173\n"
    "v_mfma_f32_16x16x32_bf16 v[160:163], a[184:187], a[88:91], v[160:163]\n"
    "ds_read_b64_tr_b16 a[172:173], v245 offset:32768\n"
    "v_cvt_pk_bf16_f32 v165, v166, v167\n"
    "v_mfma_f32_16x16x32_bf16 v[160:163], a[188:191], a[92:95], v[160:163]\n"
    "buffer_load_ushort v230, v231, s[24:27], s31 offen\n"
    "v_mfma_f32_16x16x32_bf16 v[168:171], a[188:191], a[124:127], v[168:171]\n"
    "ds_read_b64_tr_b16 a[176:177], v246 offset:0\n"
    "v_exp_f32_e32 v174, v174\n"
    "v_mfma_f32_16x16x32_bf16 v[168:171], a[184:187], a[120:123], v[168:171]\n"
    "ds_read_b64_tr_b16 a[178:179], v246 offset:2048\n"
    "ds_write_addtid_b32 v164 offset:1024\n"
    "v_mfma_f32_16x16x32_bf16 v[176:179], a[184:187], a[152:155], v[176:179]\n"
    "ds_read_b64_tr_b16 a[174:175], v245 offset:34816\n"
    "ds_write_addtid_b32 v165 offset:1280\n"
    "v_mfma_f32_16x16x32_bf16 v[176:179], a[188:191], a[156:159], v[176:179]\n"
    "s_waitcnt lgkmcnt(7)\n"
    "v_exp_f32_e32 v175, v175\n"
    "v_mfma_f32_16x16x32_bf16 a[252:255], a[160:163], a[208:211], a[252:255]\n"
    "s_add_u32 s62, s57, 4096\n"
    "ds_read_b64_tr_b16 a[180:181], v246 offset:32768\n"
    "v_cvt_pk_bf16_f32 v172, v172, v173\n"
    "v_mfma_f32_16x16x32_bf16 a[252:255], a[164:167], a[212:215], a[252:255]\n"
    "buffer_load_dwordx4 a[64:67], v250, s[8:11], s62 offen offset:1024 sc1\n"
    "ds_write_addtid_b32 v172 offset:2048\n"
    "v_mfma_f32_16x16x32_bf16 a[248:251], a[164:167], a[204:207], a[248:251]\n"
    "ds_read_b64_tr_b16 a[184:185], v247 offset:0\n"
    "v_exp_f32_e32 v160, v160\n"
    "v_mfma_f32_16x16x32_bf16 a[248:251], a[160:163], a[200:203], a[248:251]\n"
    "ds_read_b64_tr_b16 a[186:187], v247 offset:2048\n"
    "v_exp_f32_e32 v161, v161\n"
    "v_mfma_f32_16x16x32_bf16 a[244:247], a[160:163], a[192:195], a[244:247]\n"
    "ds_read_b64_tr_b16 a[182:183], v246 offset:34816\n"
    "v_cvt_pk_bf16_f32 v173, v174, v175\n"
    "v_mfma_f32_16x16x32_bf16 a[244:247], a[164:167], a[196:199], a[244:247]\n"
    "s_waitcnt lgkmcnt(6)\n"
    "v_exp_f32_e32 v162, v162\n"
    "v_mfma_f32_16x16x32_bf16 v[8:11], a[168:171], a[192:195], v[8:11]\n"
    "buffer_load_dwordx4 a[68:71], v250, s[8:11], s62 offen offset:2048 sc1\n"
    "ds_write_addtid_b32 v215 offset:5888\n"
    "v_mfma_f32_16x16x32_bf16 v[8:11], a[172:175], a[196:199], v[8:11]\n"
    "ds_read_b64_tr_b16 a[188:189], v247 offset:32768\n"
    "ds_write_addtid_b32 v173 offset:2304\n"
    "v_mfma_f32_16x16x32_bf16 v[12:15], a[172:175], a[204:207], v[12:15]\n"
    "ds_read_b64_tr_b16 a[160:161], v244 offset:4096\n"
    "v_exp_f32_e32 v163, v163\n"
    "v_mfma_f32_16x16x32_bf16 v[12:15], a[168:171], a[200:203], v[12:15]\n"
    "ds_read_b64_tr_b16 a[162:163], v244 offset:6144\n"
    "v_cvt_pk_bf16_f32 v158, v160, v161\n"
    "v_mfma_f32_16x16x32_bf16 v[16:19], a[168:171], a[208:211], v[16:19]\n"
    "ds_read_b64_tr_b16 a[190:191], v247 offset:34816\n"
    "v_exp_f32_e32 v168, v168\n"
    "v_mfma_f32_16x16x32_bf16 v[16:19], a[172:175], a[212:215], v[16:19]\n"
    "s_waitcnt lgkmcnt(6)\n"
    "buffer_load_dwordx4 a[72:75], v250, s[8:11], s62 offen offset:3072 sc1\n"
    "ds_write_addtid_b32 v158 offset:512\n"
    "v_mfma_f32_16x16x32_bf16 v[36:39], a[176:179], a[208:211], v[36:39]\n"
    "s_add_u32 s62, s57, 8192\n"
    "ds_read_b64_tr_b16 a[168:169], v244 offset:8192\n"
    "v_exp_f32_e32 v169, v169\n"
    "v_mfma_f32_16x16x32_bf16 v[36:39], a[180:183], a[212:215], v[36:39]\n"
    "ds_read_b64_tr_b16 a[164:165], v244 offset:36864\n"
    "v_cvt_pk_bf16_f32 v159, v162, v163\n"
    "v_mfma_f32_16x16x32_bf16 v[32:35], a[180:183], a[204:207], v[32:35]\n"
    "ds_read_b64_tr_b16 a[166:167], v244 offset:38912\n"
    "v_exp_f32_e32 v170, v170\n"
    "v_mfma_f32_16x16x32_bf16 v[32:35], a[176:179], a[200:203], v[32:35]\n"
    "s_waitcnt lgkmcnt(9)\n"
    "v_mfma_f32_16x16x32_bf16 v[28:31], a[176:179], a[192:195], v[28:31]\n"
    "s_barrier\n"
    "v_mfma_f32_16x16x32_bf16 v[28:31], a[180:183], a[196:199], v[28:31]\n"
    "s_waitcnt lgkmcnt(4)\n"
    "buffer_load_dwordx4 a[76:79], v250, s[8:11], s62 offen offset:0 sc1\n"
    "ds_read2st64_b32 v[200:201], v249 offset0:12 offset1:13\n"
    "v_mfma_f32_16x16x32_bf16 v[48:51], a[184:187], a[192:195], v[48:51]\n"
    "ds_read_b64_tr_b16 a[170:171], v244 offset:10240\n"
    "v_exp_f32_e32 v171, v171\n"
    "v_mfma_f32_16x16x32_bf16 v[48:51], a[188:191], a[196:199], v[48:51]\n"
    "ds_read_b64_tr_b16 a[172:173], v244 offset:40960\n"
    "ds_read2st64_b32 v[202:203], v249 offset0:14 offset1:15\n"
    "v_cvt_pk_bf16_f32 v166, v168, v169\n"
    "v_mfma_f32_16x16x32_bf16 v[52:55], a[188:191], a[204:207], v[52:55]\n"
    "ds_read_b64_tr_b16 a[176:177], v245 offset:8192\n"
    "ds_write_addtid_b32 v159 offset:768\n"
    "ds_read2st64_b32 v[208:209], v249 offset0:16 offset1:17\n"
    "v_mfma_f32_16x16x32_bf16 v[52:55], a[184:187], a[200:203], v[52:55]\n"
    "ds_read_b64_tr_b16 a[174:175], v244 offset:43008\n"
    "ds_read2st64_b32 v[210:211], v249 offset0:18 offset1:19\n"
    "v_exp_f32_e32 v176, v176\n"
    "v_mfma_f32_16x16x32_bf16 v[56:59], a[184:187], a[208:211], v[56:59]\n"
    "buffer_load_dwordx4 a[80:83], v250, s[8:11], s62 offen offset:1024 sc1\n"
    "ds_read2st64_b32 v[216:217], v249 offset0:20 offset1:21\n"
    "v_mfma_f32_16x16x32_bf16 v[56:59], a[188:191], a[212:215], v[56:59]\n"
    "s_waitcnt lgkmcnt(10)\n"
    "ds_read_b64_tr_b16 a[178:179], v245 offset:10240\n"
    "ds_read2st64_b32 v[218:219], v249 offset0:22 offset1:23\n"
    "v_exp_f32_e32 v177, v177\n"
    "v_mfma_f32_16x16x32_bf16 v[76:79], a[160:163], a[208:211], v[76:79]\n"
    "ds_read_b64_tr_b16 a[184:185], v246 offset:8192\n"
    "v_cvt_pk_bf16_f32 v167, v170, v171\n"
    "v_mfma_f32_16x16x32_bf16 v[76:79], a[164:167], a[212:215], v[76:79]\n"
    "ds_read_b64_tr_b16 a[180:181], v245 offset:40960\n"
    "v_exp_f32_e32 v178, v178\n"
    "v_mfma_f32_16x16x32_bf16 v[72:75], a[164:167], a[204:207], v[72:75]\n"
    "ds_read_b64_tr_b16 a[186:187], v246 offset:10240\n"
    "v_exp_f32_e32 v179, v179\n"
    "v_mfma_f32_16x16x32_bf16 v[72:75], a[160:163], a[200:203], v[72:75]\n"
    "buffer_load_dwordx4 a[84:87], v250, s[8:11], s62 offen offset:2048 sc1\n"
    "v_mfma_f32_16x16x32_bf16 v[68:71], a[160:163], a[192:195], v[68:71]\n"
    "ds_read_b64_tr_b16 a[182:183], v245 offset:43008\n"
    "ds_write_addtid_b32 v166 offset:1536\n"
    "v_mfma_f32_16x16x32_bf16 v[68:71], a[164:167], a[196:199], v[68:71]\n"
    "s_waitcnt lgkmcnt(5)\n"
    "ds_write_addtid_b32 v167 offset:1792\n"
    "v_cvt_pk_bf16_f32 v174, v176, v177\n"
    "v_mfma_f32_16x16x32_bf16 a[244:247], a[168:171], v[196:199], a[244:247]\n"
    "ds_read_b64_tr_b16 a[160:161], v244 offset:16384\n"
    "v_cvt_pk_bf16_f32 v175, v178, v179\n"
    "v_mfma_f32_16x16x32_bf16 a[244:247], a[172:175], v[200:203], a[244:247]\n"
    "ds_read_b64_tr_b16 a[188:189], v246 offset:40960\n"
    "ds_write_addtid_b32 v174 offset:2560\n"
    "v_mfma_f32_16x16x32_bf16 a[248:251], a[172:175], v[208:211], a[248:251]\n"
    "buffer_load_dwordx4 a[88:91], v250, s[8:11], s62 offen offset:3072 sc1\n"
    "ds_write_addtid_b32 v175 offset:2816\n"
    "v_mfma_f32_16x16x32_bf16 a[248:251], a[168:171], v[204:207], a[248:251]\n"
    "s_add_u32 s62, s57, 12288\n"
    "ds_read_b64_tr_b16 a[162:163], v244 offset:18432\n"
    "v_lshrrev_b32_e32 v231, 2, v250\n"
    "v_mfma_f32_16x16x32_bf16 a[252:255], a[168:171], v[212:215], a[252:255]\n"
    "ds_read_b64_tr_b16 a[190:191], v246 offset:43008\n"
    "v_subrev_u32_e32 v231, 0x80, v231\n"
    "v_mfma_f32_16x16x32_bf16 a[252:255], a[172:175], v[216:219], a[252:255]\n"
    "s_waitcnt lgkmcnt(8)\n"
    "s_waitcnt lgkmcnt(2)\n"
    "v_mfma_f32_16x16x32_bf16 v[16:19], a[176:179], v[212:215], v[16:19]\n"
    "s_barrier\n"
    "v_mfma_f32_16x16x32_bf16 v[16:19], a[180:183], v[216:219], v[16:19]\n"
    "buffer_load_dwordx4 a[92:95], v250, s[8:11], s62 offen offset:0 sc1\n"
    "v_mfma_f32_16x16x32_bf16 v[12:15], a[180:183], v[208:211], v[12:15]\n"
    "ds_read_b64_tr_b16 a[164:165], v244 offset:49152\n"
    "ds_read2st64_b32 v[160:161], v249 offset0:0 offset1:1\n"
    "v_mfma_f32_16x16x32_bf16 v[12:15], a[176:179], v[204:207], v[12:15]\n"
    "ds_read_b64_tr_b16 a[168:169], v245 offset:16384\n"
    "ds_read2st64_b32 v[162:163], v249 offset0:2 offset1:3\n"
    "v_mfma_f32_16x16x32_bf16 v[8:11], a[176:179], v[196:199], v[8:11]\n"
    "ds_read_b64_tr_b16 a[166:167], v244 offset:51200\n"
    "ds_read2st64_b32 v[168:169], v249 offset0:4 offset1:5\n"
    "v_mfma_f32_16x16x32_bf16 v[8:11], a[180:183], v[200:203], v[8:11]\n"
    "s_waitcnt lgkmcnt(6)\n"
    "ds_read_b64_tr_b16 a[170:171], v245 offset:18432\n"
    "ds_read2st64_b32 v[170:171], v249 offset0:6 offset1:7\n"
    "v_mfma_f32_16x16x32_bf16 v[28:31], a[184:187], v[196:199], v[28:31]\n"
    "buffer_load_dwordx4 a[96:99], v250, s[8:11], s62 offen offset:1024 sc1\n"
    "ds_read2st64_b32 v[176:177], v249 offset0:8 offset1:9\n"
    "v_mfma_f32_16x16x32_bf16 v[28:31], a[188:191], v[200:203], v[28:31]\n"
    "ds_read_b64_tr_b16 a[172:173], v245 offset:49152\n"
    "ds_read2st64_b32 v[178:179], v249 offset0:10 offset1:11\n"
    "v_mfma_f32_16x16x32_bf16 v[32:35], a[188:191], v[208:211], v[32:35]\n"
    "ds_read_b64_tr_b16 a[176:177], v247 offset:8192\n"
    "v_min_u32_e32 v231, 2.0, v231\n"
    "v_mfma_f32_16x16x32_bf16 v[32:35], a[184:187], v[204:207], v[32:35]\n"
    "ds_read_b64_tr_b16 a[178:179], v247 offset:10240\n"
    "v_lshrrev_b32_e32 v252, 4, v250\n"
    "v_mfma_f32_16x16x32_bf16 v[36:39], a[184:187], v[212:215], v[36:39]\n"
    "ds_read_b64_tr_b16 a[174:175], v245 offset:51200\n"
    "v_lshlrev_b32_e32 v239, 5, v248\n"
    "v_mfma_f32_16x16x32_bf16 v[36:39], a[188:191], v[216:219], v[36:39]\n"
    "s_waitcnt lgkmcnt(3)\n"
    "buffer_load_dwordx4 a[100:103], v250, s[8:11], s62 offen offset:2048 sc1\n"
    "v_mfma_f32_16x16x32_bf16 a[252:255], a[160:163], v[172:175], a[252:255]\n"
    "ds_read_b64_tr_b16 a[184:185], v246 offset:16384\n"
    "v_and_or_b32 v239, v252, 48, v239\n"
    "v_mfma_f32_16x16x32_bf16 a[252:255], a[164:167], v[176:179], a[252:255]\n"
    "ds_read_b64_tr_b16 a[180:181], v247 offset:40960\n"
    "v_lshrrev_b32_e32 v248, 2, v248\n"
    "v_mfma_f32_16x16x32_bf16 a[248:251], a[164:167], v[168:171], a[248:251]\n"
    "ds_read_b64_tr_b16 a[186:187], v246 offset:18432\n"
    "v_lshrrev_b32_e32 v253, 4, v252\n"
    "v_mfma_f32_16x16x32_bf16 a[248:251], a[160:163], v[164:167], a[248:251]\n"
    "ds_read_b64_tr_b16 a[182:183], v247 offset:43008\n"
    "v_lshl_add_u32 v248, v253, 7, v248\n"
    "v_mfma_f32_16x16x32_bf16 a[244:247], a[160:163], v[156:159], a[244:247]\n"
    "buffer_load_dwordx4 a[104:107], v250, s[8:11], s62 offen offset:3072 sc1\n"
    "v_mfma_f32_16x16x32_bf16 a[244:247], a[164:167], v[160:163], a[244:247]\n"
    "s_waitcnt lgkmcnt(4)\n"
    "v_accvgpr_read_b32 v144, a248\n"
    "v_accvgpr_read_b32 v148, a252\n"
    "v_mfma_f32_16x16x32_bf16 v[8:11], a[168:171], v[156:159], v[8:11]\n"
    "s_add_u32 s62, s57, 16384\n"
    "ds_read_b64_tr_b16 a[160:161], v247 offset:16384\n"
    "v_accvgpr_read_b32 v145, a249\n"
    "v_mfma_f32_16x16x32_bf16 v[8:11], a[172:175], v[160:163], v[8:11]\n"
    "ds_read_b64_tr_b16 a[188:189], v246 offset:49152\n"
    "v_accvgpr_read_b32 v140, a244\n"
    "v_mfma_f32_16x16x32_bf16 v[12:15], a[172:175], v[168:171], v[12:15]\n"
    "v_accvgpr_read_b32 v141, a245\n"
    "v_accvgpr_read_b32 v142, a246\n"
    "v_accvgpr_read_b32 v143, a247\n"
    "v_mfma_f32_16x16x32_bf16 v[12:15], a[168:171], v[164:167], v[12:15]\n"
    "ds_read_b64_tr_b16 a[162:163], v247 offset:18432\n"
    "v_cvt_pk_bf16_f32 v140, v140, v141\n"
    "v_cvt_pk_bf16_f32 v141, v142, v143\n"
    "v_mfma_f32_16x16x32_bf16 v[16:19], a[168:171], v[172:175], v[16:19]\n"
    "ds_read_b64_tr_b16 a[190:191], v246 offset:51200\n"
    "v_cvt_pk_bf16_f32 v142, v8, v9\n"
    "v_cvt_pk_bf16_f32 v143, v10, v11\n"
    "v_mfma_f32_16x16x32_bf16 v[16:19], a[172:175], v[176:179], v[16:19]\n"
    "s_waitcnt lgkmcnt(4)\n"
    "buffer_store_dwordx4 v[140:143], v250, s[8:11], s41 offen offset:0 sc1\n"
    "v_mfma_f32_16x16x32_bf16 v[56:59], a[176:179], v[212:215], v[56:59]\n"
    "ds_read_b64_tr_b16 a[168:169], v244 offset:12288\n"
    "v_accvgpr_read_b32 v146, a250\n"
    "v_mfma_f32_16x16x32_bf16 v[56:59], a[180:183], v[216:219], v[56:59]\n"
    "ds_read_b64_tr_b16 a[164:165], v247 offset:49152\n"
    "v_accvgpr_read_b32 v149, a253\n"
    "v_mfma_f32_16x16x32_bf16 v[52:55], a[180:183], v[208:211], v[52:55]\n"
    "buffer_load_dwordx4 a[108:111], v250, s[8:11], s62 offen offset:0 sc1\n"
    "v_mfma_f32_16x16x32_bf16 v[52:55], a[176:179], v[204:207], v[52:55]\n"
    "ds_read_b64_tr_b16 a[170:171], v244 offset:14336\n"
    "v_accvgpr_read_b32 v147, a251\n"
    "v_mfma_f32_16x16x32_bf16 v[48:51], a[176:179], v[196:199], v[48:51]\n"
    "ds_read_b64_tr_b16 a[166:167], v247 offset:51200\n"
    "v_accvgpr_read_b32 v150, a254\n"
    "v_mfma_f32_16x16x32_bf16 v[48:51], a[180:183], v[200:203], v[48:51]\n"
    "s_waitcnt lgkmcnt(4)\n"
    "v_cvt_pk_bf16_f32 v144, v144, v145\n"
    "v_bfe_u32 v255, v252, 2, 2\n"
    "v_mfma_f32_16x16x32_bf16 v[28:31], a[184:187], v[156:159], v[28:31]\n"
    "ds_read_b64_tr_b16 a[172:173], v244 offset:45056\n"
    "v_max_u32_e32 v238, 1, v255\n"
    "v_mfma_f32_16x16x32_bf16 v[28:31], a[188:191], v[160:163], v[28:31]\n"
    "buffer_load_dwordx4 a[112:115], v250, s[8:11], s62 offen offset:1024 sc1\n"
    "v_mfma_f32_16x16x32_bf16 v[32:35], a[188:191], v[168:171], v[32:35]\n"
    "ds_read_b64_tr_b16 a[176:177], v244 offset:20480\n"
    "v_cvt_pk_bf16_f32 v145, v146, v147\n"
    "v_mfma_f32_16x16x32_bf16 v[32:35], a[184:187], v[164:167], v[32:35]\n"
    "ds_read_b64_tr_b16 a[178:179], v244 offset:22528\n"
    "v_accvgpr_read_b32 v151, a255\n"
    "v_mfma_f32_16x16x32_bf16 v[36:39], a[184:187], v[172:175], v[36:39]\n"
    "ds_read_b64_tr_b16 a[174:175], v244 offset:47104\n"
    "v_add_u32_e32 v238, s32, v238\n"
    "v_mfma_f32_16x16x32_bf16 v[36:39], a[188:191], v[176:179], v[36:39]\n"
    "s_waitcnt lgkmcnt(4)\n"
    "v_cvt_pk_bf16_f32 v146, v12, v13\n"
    "v_and_b32_e32 v238, 3, v238\n"
    "v_mfma_f32_16x16x32_bf16 v[56:59], a[160:163], v[172:175], v[56:59]\n"
    "buffer_load_dwordx4 a[116:119], v250, s[8:11], s62 offen offset:2048 sc1\n"
    "v_mfma_f32_16x16x32_bf16 v[56:59], a[164:167], v[176:179], v[56:59]\n"
    "ds_read_b64_tr_b16 a[180:181], v244 offset:53248\n"
    "v_cvt_pk_bf16_f32 v148, v148, v149\n"
    "v_mfma_f32_16x16x32_bf16 v[52:55], a[164:167], v[168:171], v[52:55]\n"
    "ds_read_b64_tr_b16 a[184:185], v245 offset:4096\n"
    "v_cvt_pk_bf16_f32 v147, v14, v15\n"
    "v_mfma_f32_16x16x32_bf16 v[52:55], a[160:163], v[164:167], v[52:55]\n"
    "buffer_store_dwordx4 v[144:147], v250, s[8:11], s28 offen offset:0 sc1\n"
    "v_mfma_f32_16x16x32_bf16 v[48:51], a[160:163], v[156:159], v[48:51]\n"
    "s_add_u32 s62, s57, 16384\n"
    "ds_read_b64_tr_b16 a[182:183], v244 offset:55296\n"
    "v_cvt_pk_bf16_f32 v28, v28, v29\n"
    "v_mfma_f32_16x16x32_bf16 v[48:51], a[164:167], v[160:163], v[48:51]\n"
    "s_waitcnt lgkmcnt(3)\n"
    "ds_read_b64_tr_b16 a[186:187], v245 offset:6144\n"
    "v_cvt_pk_bf16_f32 v32, v32, v33\n"
    "v_mfma_f32_16x16x32_bf16 v[68:71], a[168:171], v[196:199], v[68:71]\n"
    "buffer_load_dwordx4 a[120:123], v250, s[8:11], s62 offen offset:3072 sc1\n"
    "v_mfma_f32_16x16x32_bf16 v[68:71], a[172:175], v[200:203], v[68:71]\n"
    "s_add_u32 s62, s57, 20480\n"
    "ds_read_b64_tr_b16 a[188:189], v245 offset:36864\n"
    "v_cvt_pk_bf16_f32 v149, v150, v151\n"
    "v_mfma_f32_16x16x32_bf16 v[72:75], a[172:175], v[208:211], v[72:75]\n"
    "ds_read_b64_tr_b16 a[160:161], v245 offset:12288\n"
    "v_lshlrev_b32_e32 v238, 10, v238\n"
    "v_mfma_f32_16x16x32_bf16 v[72:75], a[168:171], v[204:207], v[72:75]\n"
    "ds_read_b64_tr_b16 a[162:163], v245 offset:14336\n"
    "v_and_b32_e32 v252, 3, v252\n"
    "v_mfma_f32_16x16x32_bf16 v[76:79], a[168:171], v[212:215], v[76:79]\n"
    "ds_read_b64_tr_b16 a[190:191], v245 offset:38912\n"
    "v_xor_b32_e32 v253, v253, v255\n"
    "v_mfma_f32_16x16x32_bf16 v[76:79], a[172:175], v[216:219], v[76:79]\n"
    "s_waitcnt lgkmcnt(5)\n"
    "v_cvt_pk_bf16_f32 v150, v16, v17\n"
    "v_cvt_pk_bf16_f32 v29, v30, v31\n"
    "v_mfma_f32_16x16x32_bf16 v[76:79], a[176:179], v[172:175], v[76:79]\n"
    "ds_read_b64_tr_b16 a[164:165], v245 offset:45056\n"
    "v_lshl_or_b32 v252, v253, 2, v252\n"
    "v_mfma_f32_16x16x32_bf16 v[76:79], a[180:183], v[176:179], v[76:79]\n"
    "buffer_load_dwordx4 a[124:127], v250, s[8:11], s62 offen offset:0 sc1\n"
    "v_mfma_f32_16x16x32_bf16 v[72:75], a[180:183], v[168:171], v[72:75]\n"
    "ds_read_b64_tr_b16 a[168:169], v245 offset:20480\n"
    "v_mfma_f32_16x16x32_bf16 v[72:75], a[176:179], v[164:167], v[72:75]\n"
    "ds_read_b64_tr_b16 a[170:171], v245 offset:22528\n"
    "v_mfma_f32_16x16x32_bf16 v[68:71], a[176:179], v[156:159], v[68:71]\n"
    "ds_read_b64_tr_b16 a[166:167], v245 offset:47104\n"
    "v_mov_b32_e32 v253, 0x3f80\n"
    "v_mfma_f32_16x16x32_bf16 v[68:71], a[180:183], v[160:163], v[68:71]\n"
    "s_waitcnt lgkmcnt(4)\n"
    "ds_read_b64_tr_b16 a[176:177], v246 offset:4096\n"
    "v_cvt_pk_bf16_f32 v151, v18, v19\n"
    "v_mfma_f32_16x16x32_bf16 v[88:91], a[184:187], a[192:195], v[88:91]\n"
    "ds_read_b64_tr_b16 a[172:173], v245 offset:53248\n"
    "v_cvt_pk_bf16_f32 v33, v34, v35\n"
    "v_mfma_f32_16x16x32_bf16 v[88:91], a[188:191], a[196:199], v[88:91]\n"
    "buffer_store_dwordx4 v[148:151], v250, s[8:11], s29 offen offset:0 sc1\n"
    "v_mfma_f32_16x16x32_bf16 v[92:95], a[188:191], a[204:207], v[92:95]\n"
    "v_cvt_pk_bf16_f32 v30, v48, v49\n"
    "v_mfma_f32_16x16x32_bf16 v[92:95], a[184:187], a[200:203], v[92:95]\n"
    "ds_read_b64_tr_b16 a[174:175], v245 offset:55296\n"
    "v_mov_b64_e32 v[232:233], 0\n"
    "v_mfma_f32_16x16x32_bf16 v[96:99], a[184:187], a[208:211], v[96:99]\n"
    "buffer_load_dwordx4 a[128:131], v250, s[8:11], s62 offen offset:1024 sc1\n"
    "v_mfma_f32_16x16x32_bf16 v[96:99], a[188:191], a[212:215], v[96:99]\n"
    "s_waitcnt lgkmcnt(3)\n"
    "ds_read_b64_tr_b16 a[178:179], v246 offset:6144\n"
    "v_mfma_f32_16x16x32_bf16 v[96:99], a[160:163], v[212:215], v[96:99]\n"
    "ds_read_b64_tr_b16 a[184:185], v246 offset:12288\n"
    "v_mfma_f32_16x16x32_bf16 v[96:99], a[164:167], v[216:219], v[96:99]\n"
    "ds_read_b64_tr_b16 a[180:181], v246 offset:36864\n"
    "v_mfma_f32_16x16x32_bf16 v[92:95], a[164:167], v[208:211], v[92:95]\n"
    "v_cvt_pk_bf16_f32 v31, v50, v51\n"
    "v_mov_b64_e32 v[236:237], 0\n"
    "v_mfma_f32_16x16x32_bf16 v[92:95], a[160:163], v[204:207], v[92:95]\n"
    "ds_read_b64_tr_b16 a[186:187], v246 offset:14336\n"
    "v_cvt_pk_bf16_f32 v72, v72, v73\n"
    "v_mfma_f32_16x16x32_bf16 v[88:91], a[160:163], v[196:199], v[88:91]\n"
    "ds_read_b64_tr_b16 a[182:183], v246 offset:38912\n"
    "v_cvt_pk_bf16_f32 v76, v76, v77\n"
    "v_mfma_f32_16x16x32_bf16 v[88:91], a[164:167], v[200:203], v[88:91]\n"
    "s_waitcnt lgkmcnt(5)\n"
    "buffer_load_dwordx4 a[132:135], v250, s[8:11], s62 offen offset:2048 sc1\n"
    "v_mfma_f32_16x16x32_bf16 v[88:91], a[168:171], v[156:159], v[88:91]\n"
    "ds_read_b64_tr_b16 a[160:161], v246 offset:20480\n"
    "v_mfma_f32_16x16x32_bf16 v[88:91], a[172:175], v[160:163], v[88:91]\n"
    "ds_read_b64_tr_b16 a[188:189], v246 offset:45056\n"
    "v_cmp_eq_u32_e32 vcc, 0, v252\n"
    "v_mfma_f32_16x16x32_bf16 v[92:95], a[172:175], v[168:171], v[92:95]\n"
    "ds_read_b64_tr_b16 a[190:191], v246 offset:47104\n"
    "v_cvt_pk_bf16_f32 v34, v52, v53\n"
    "v_mfma_f32_16x16x32_bf16 v[92:95], a[168:171], v[164:167], v[92:95]\n"
    "v_mfma_f32_16x16x32_bf16 v[96:99], a[168:171], v[172:175], v[96:99]\n"
    "s_barrier\n"
    "v_mfma_f32_16x16x32_bf16 v[96:99], a[172:175], v[176:179], v[96:99]\n"
    "s_waitcnt lgkmcnt(3)\n"
    "buffer_store_dwordx4 v[28:31], v250, s[8:11], s41 offen offset:1024 sc1\n"
    "v_mfma_f32_16x16x32_bf16 v[116:119], a[176:179], a[208:211], v[116:119]\n"
    "ds_read_b64_tr_b16 a[162:163], v246 offset:22528\n"
    "v_mfma_f32_16x16x32_bf16 v[116:119], a[180:183], a[212:215], v[116:119]\n"
    "ds_read_b64_tr_b16 a[164:165], v246 offset:53248\n"
    "v_cndmask_b32_e32 v234, 0, v253, vcc\n"
    "v_mfma_f32_16x16x32_bf16 v[112:115], a[180:183], a[204:207], v[112:115]\n"
    "buffer_load_dwordx4 a[136:139], v250, s[8:11], s62 offen offset:3072 sc1\n"
    "v_mfma_f32_16x16x32_bf16 v[112:115], a[176:179], a[200:203], v[112:115]\n"
    "s_add_u32 s62, s57, 24576\n"
    "ds_read_b64_tr_b16 a[168:169], v247 offset:4096\n"
    "v_mfma_f32_16x16x32_bf16 v[108:111], a[176:179], a[192:195], v[108:111]\n"
    "ds_read_b64_tr_b16 a[166:167], v246 offset:55296\n"
    "v_mfma_f32_16x16x32_bf16 v[108:111], a[180:183], a[196:199], v[108:111]\n"
    "s_waitcnt lgkmcnt(4)\n"
    "ds_read_b64_tr_b16 a[170:171], v247 offset:6144\n"
    "v_mfma_f32_16x16x32_bf16 v[108:111], a[184:187], v[196:199], v[108:111]\n"
    "ds_read_b64_tr_b16 a[176:177], v247 offset:12288\n"
    "v_cmp_eq_u32_e32 vcc, 1, v252\n"
    "v_mfma_f32_16x16x32_bf16 v[108:111], a[188:191], v[200:203], v[108:111]\n"
    "ds_read_b64_tr_b16 a[172:173], v247 offset:36864\n"
    "v_mfma_f32_16x16x32_bf16 v[112:115], a[188:191], v[208:211], v[112:115]\n"
    "ds_read_b64_tr_b16 a[178:179], v247 offset:14336\n"
    "v_mfma_f32_16x16x32_bf16 v[112:115], a[184:187], v[204:207], v[112:115]\n"
    "buffer_load_dwordx4 a[140:143], v250, s[8:11], s62 offen offset:0 sc1\n"
    "v_mfma_f32_16x16x32_bf16 v[116:119], a[184:187], v[212:215], v[116:119]\n"
    "ds_read_b64_tr_b16 a[174:175], v247 offset:38912\n"
    "v_mfma_f32_16x16x32_bf16 v[116:119], a[188:191], v[216:219], v[116:119]\n"
    "s_waitcnt lgkmcnt(5)\n"
    "v_cvt_pk_bf16_f32 v35, v54, v55\n"
    "v_cndmask_b32_e64 v234, v234, 1.0, vcc\n"
    "v_mfma_f32_16x16x32_bf16 v[116:119], a[160:163], v[172:175], v[116:119]\n"
    "ds_read_b64_tr_b16 a[184:185], v247 offset:20480\n"
    "v_mfma_f32_16x16x32_bf16 v[116:119], a[164:167], v[176:179], v[116:119]\n"
    "ds_read_b64_tr_b16 a[180:181], v247 offset:45056\n"
    "v_mfma_f32_16x16x32_bf16 v[112:115], a[164:167], v[168:171], v[112:115]\n"
    "ds_read_b64_tr_b16 a[186:187], v247 offset:22528\n"
    "v_mfma_f32_16x16x32_bf16 v[112:115], a[160:163], v[164:167], v[112:115]\n"
    "buffer_store_dwordx4 v[32:35], v250, s[8:11], s28 offen offset:1024 sc1\n"
    "v_mfma_f32_16x16x32_bf16 v[108:111], a[160:163], v[156:159], v[108:111]\n"
    "ds_read_b64_tr_b16 a[182:183], v247 offset:47104\n"
    "v_cmp_eq_u32_e32 vcc, 2, v252\n"
    "v_mfma_f32_16x16x32_bf16 v[108:111], a[164:167], v[160:163], v[108:111]\n"
    "s_waitcnt lgkmcnt(4)\n"
    "v_cvt_pk_bf16_f32 v36, v36, v37\n"
    "v_cvt_pk_bf16_f32 v77, v78, v79\n"
    "v_mfma_f32_16x16x32_bf16 v[128:131], a[168:171], a[192:195], v[128:131]\n"
    "buffer_load_dwordx4 a[144:147], v250, s[8:11], s62 offen offset:1024 sc1\n"
    "v_mfma_f32_16x16x32_bf16 v[128:131], a[172:175], a[196:199], v[128:131]\n"
    "ds_read_b64_tr_b16 a[188:189], v247 offset:53248\n"
    "v_mfma_f32_16x16x32_bf16 v[132:135], a[172:175], a[204:207], v[132:135]\n"
    "ds_read_b128 a[160:163], v240 offset:0\n"
    "v_mfma_f32_16x16x32_bf16 v[132:135], a[168:171], a[200:203], v[132:135]\n"
    "v_cvt_pk_bf16_f32 v37, v38, v39\n"
    "v_mfma_f32_16x16x32_bf16 v[136:139], a[168:171], a[208:211], v[136:139]\n"
    "ds_read_b64_tr_b16 a[190:191], v247 offset:55296\n"
    "v_cndmask_b32_e32 v235, 0, v253, vcc\n"
    "v_mfma_f32_16x16x32_bf16 v[136:139], a[172:175], a[212:215], v[136:139]\n"
    "s_waitcnt lgkmcnt(3)\n"
    "v_cvt_pk_bf16_f32 v38, v56, v57\n"
    "v_mfma_f32_16x16x32_bf16 v[136:139], a[176:179], v[212:215], v[136:139]\n"
    "v_cvt_pk_bf16_f32 v39, v58, v59\n"
    "v_mfma_f32_16x16x32_bf16 v[136:139], a[180:183], v[216:219], v[136:139]\n"
    "buffer_load_dwordx4 a[148:151], v250, s[8:11], s62 offen offset:2048 sc1\n"
    "v_mfma_f32_16x16x32_bf16 v[132:135], a[180:183], v[208:211], v[132:135]\n"
    "ds_read_b128 a[164:167], v241 offset:0\n"
    "v_mfma_f32_16x16x32_bf16 v[132:135], a[176:179], v[204:207], v[132:135]\n"
    "ds_read_b128 a[168:171], v240 offset:4096\n"
    "v_cmp_eq_u32_e32 vcc, 3, v252\n"
    "v_mfma_f32_16x16x32_bf16 v[128:131], a[176:179], v[196:199], v[128:131]\n"
    "v_cvt_pk_bf16_f32 v68, v68, v69\n"
    "v_cvt_pk_bf16_f32 v112, v112, v113\n"
    "v_mfma_f32_16x16x32_bf16 v[128:131], a[180:183], v[200:203], v[128:131]\n"
    "s_waitcnt lgkmcnt(2)\n"
    "v_cvt_pk_bf16_f32 v69, v70, v71\n"
    "v_cvt_pk_bf16_f32 v108, v108, v109\n"
    "v_mfma_f32_16x16x32_bf16 v[128:131], a[184:187], v[156:159], v[128:131]\n"
    "ds_read_b128 a[172:175], v241 offset:4096\n"
    "v_cvt_pk_bf16_f32 v116, v116, v117\n"
    "v_mfma_f32_16x16x32_bf16 v[128:131], a[188:191], v[160:163], v[128:131]\n"
    "buffer_store_dwordx4 v[36:39], v250, s[8:11], s29 offen offset:1024 sc1\n"
    "v_mfma_f32_16x16x32_bf16 v[132:135], a[188:191], v[168:171], v[132:135]\n"
    "ds_read_b128 a[176:179], v242 offset:0\n"
    "v_cndmask_b32_e64 v235, v235, 1.0, vcc\n"
    "v_mfma_f32_16x16x32_bf16 v[132:135], a[184:187], v[164:167], v[132:135]\n"
    "v_cvt_pk_bf16_f32 v70, v88, v89\n"
    "v_mfma_f32_16x16x32_bf16 v[136:139], a[184:187], v[172:175], v[136:139]\n"
    "buffer_load_dwordx4 a[152:155], v250, s[8:11], s62 offen offset:3072 sc1\n"
    "v_mfma_f32_16x16x32_bf16 v[136:139], a[188:191], v[176:179], v[136:139]\n"
    "s_waitcnt lgkmcnt(3)\n"
    "ds_read_b128 a[180:183], v243 offset:0\n"
    "v_mfma_f32_16x16x32_bf16 v[148:151], a[160:163], a[32:35], v[224:227]\n"
    "s_add_u32 s62, s57, 28672\n"
    "v_cvt_pk_bf16_f32 v73, v74, v75\n"
    "v_mfma_f32_16x16x32_bf16 v[148:151], a[164:167], a[36:39], v[148:151]\n"
    "ds_read_b128 a[184:187], v242 offset:4096\n"
    "v_mfma_f32_16x16x32_bf16 v[140:143], a[164:167], a[4:7], v[220:223]\n"
    "v_cvt_pk_bf16_f32 v71, v90, v91\n"
    "v_add_f32_e32 v212, v131, v135\n"
    "v_mfma_f32_16x16x32_bf16 v[140:143], a[160:163], a[0:3], v[140:143]\n"
    "s_waitcnt lgkmcnt(3)\n"
    "buffer_store_dword v131, v231, s[8:11], s87 offen offset:0 sc1\n"
    "v_mfma_f32_16x16x32_bf16 v[140:143], a[168:171], a[8:11], v[140:143]\n"
    "ds_read_b128 a[188:191], v243 offset:4096\n"
    "v_cvt_pk_bf16_f32 v117, v118, v119\n"
    "v_mfma_f32_16x16x32_bf16 v[140:143], a[172:175], a[12:15], v[140:143]\n"
    "ds_read_b128 a[160:163], v240 offset:2048\n"
    "v_cvt_pk_bf16_f32 v118, v136, v137\n"
    "v_mfma_f32_16x16x32_bf16 v[148:151], a[172:175], a[44:47], v[148:151]\n"
    "v_cvt_pk_bf16_f32 v74, v92, v93\n"
    "v_cvt_pk_bf16_f32 v78, v96, v97\n"
    "v_mfma_f32_16x16x32_bf16 v[148:151], a[168:171], a[40:43], v[148:151]\n"
    "s_waitcnt lgkmcnt(3)\n"
    "ds_read_b128 a[164:167], v241 offset:2048\n"
    "v_cvt_pk_bf16_f32 v119, v138, v139\n"
    "v_mfma_f32_16x16x32_bf16 v[148:151], a[176:179], a[48:51], v[148:151]\n"
    "buffer_store_dword v135, v231, s[8:11], s88 offen offset:0 sc1\n"
    "v_min3_f32 v213, v131, v135, v139\n"
    "v_mfma_f32_16x16x32_bf16 v[148:151], a[180:183], a[52:55], v[148:151]\n"
    "ds_read_b128 a[168:171], v240 offset:6144\n"
    "v_cvt_pk_bf16_f32 v113, v114, v115\n"
    "v_mfma_f32_16x16x32_bf16 v[140:143], a[180:183], a[20:23], v[140:143]\n"
    "v_cvt_pk_bf16_f32 v75, v94, v95\n"
    "v_cvt_pk_bf16_f32 v114, v132, v133\n"
    "v_mfma_f32_16x16x32_bf16 v[140:143], a[176:179], a[16:19], v[140:143]\n"
    "s_waitcnt lgkmcnt(3)\n"
    "buffer_store_dwordx4 v[68:71], v250, s[8:11], s41 offen offset:2048 sc1\n"
    "v_mfma_f32_16x16x32_bf16 v[140:143], a[184:187], a[24:27], v[140:143]\n"
    "ds_read_b128 a[172:175], v241 offset:6144\n"
    "v_cvt_pk_bf16_f32 v109, v110, v111\n"
    "v_add_f32_e32 v212, v212, v139\n"
    "v_mfma_f32_16x16x32_bf16 v[140:143], a[188:191], a[28:31], v[140:143]\n"
    "ds_read_b128 a[176:179], v242 offset:2048\n"
    "v_cvt_pk_bf16_f32 v115, v134, v135\n"
    "v_mfma_f32_16x16x32_bf16 v[148:151], a[188:191], a[60:63], v[148:151]\n"
    "buffer_store_dword v139, v231, s[8:11], s89 offen offset:0 sc1\n"
    "v_mfma_f32_16x16x32_bf16 v[148:151], a[184:187], a[56:59], v[148:151]\n"
    "s_waitcnt lgkmcnt(3)\n"
    "v_exp_f32_e32 v140, v140\n"
    "v_mfma_f32_16x16x32_bf16 v[152:155], a[160:163], a[32:35], v[224:227]\n"
    "s_mov_b32 s56, 0\n"
    "ds_read_b128 a[180:183], v243 offset:2048\n"
    "v_cvt_pk_bf16_f32 v110, v128, v129\n"
    "v_mfma_f32_16x16x32_bf16 v[152:155], a[164:167], a[36:39], v[152:155]\n"
    "buffer_load_dwordx4 a[156:159], v250, s[8:11], s62 offen offset:0 sc1\n"
    "v_mfma_f32_16x16x32_bf16 v[144:147], a[164:167], a[4:7], v[220:223]\n"
    "ds_read_b128 a[184:187], v242 offset:6144\n"
    "v_exp_f32_e32 v141, v141\n"
    "v_mfma_f32_16x16x32_bf16 v[144:147], a[160:163], a[0:3], v[144:147]\n"
    "s_waitcnt lgkmcnt(3)\n"
    "v_exp_f32_e32 v142, v142\n"
    "v_mfma_f32_16x16x32_bf16 v[144:147], a[168:171], a[8:11], v[144:147]\n"
    "ds_read_b128 a[188:191], v243 offset:6144\n"
    "v_cvt_pk_bf16_f32 v111, v130, v131\n"
    "v_mfma_f32_16x16x32_bf16 v[144:147], a[172:175], a[12:15], v[144:147]\n"
    "ds_read_b128 a[160:163], v240 offset:8192\n"
    "v_cvt_pk_bf16_f32 v79, v98, v99\n"
    "v_mfma_f32_16x16x32_bf16 v[152:155], a[172:175], a[44:47], v[152:155]\n"
    "v_exp_f32_e32 v143, v143\n"
    "v_mfma_f32_16x16x32_bf16 v[152:155], a[168:171], a[40:43], v[152:155]\n"
    "s_waitcnt lgkmcnt(3)\n"
    "ds_read_b128 a[164:167], v241 offset:8192\n"
    "v_cvt_pk_bf16_f32 v140, v140, v141\n"
    "v_mfma_f32_16x16x32_bf16 v[152:155], a[176:179], a[48:51], v[152:155]\n"
    "buffer_load_dwordx4 a[192:195], v250, s[8:11], s62 offen offset:1024 sc1\n"
    "ds_write_addtid_b32 v140 offset:6144\n"
    "v_mfma_f32_16x16x32_bf16 v[152:155], a[180:183], a[52:55], v[152:155]\n"
    "ds_read_b128 a[168:171], v240 offset:12288\n"
    "v_exp_f32_e32 v148, v148\n"
    "v_mfma_f32_16x16x32_bf16 v[144:147], a[180:183], a[20:23], v[144:147]\n"
    "v_exp_f32_e32 v149, v149\n"
    "v_mfma_f32_16x16x32_bf16 v[144:147], a[176:179], a[16:19], v[144:147]\n"
    "s_waitcnt lgkmcnt(4)\n"
    "buffer_store_dwordx4 v[72:75], v250, s[8:11], s28 offen offset:2048 sc1\n"
    "v_mfma_f32_16x16x32_bf16 v[144:147], a[184:187], a[24:27], v[144:147]\n"
    "s_waitcnt vmcnt(39)\n"
    "ds_read_b128 a[172:175], v241 offset:12288\n"
    "v_cvt_pk_bf16_f32 v141, v142, v143\n"
    "v_mfma_f32_16x16x32_bf16 v[144:147], a[188:191], a[28:31], v[144:147]\n"
    "ds_read_b128 a[176:179], v242 offset:8192\n"
    "v_exp_f32_e32 v150, v150\n"
    "v_mfma_f32_16x16x32_bf16 v[152:155], a[188:191], a[60:63], v[152:155]\n"
    "ds_write_addtid_b32 v141 offset:6400\n"
    "v_exp_f32_e32 v151, v151\n"
    "v_mfma_f32_16x16x32_bf16 v[152:155], a[184:187], a[56:59], v[152:155]\n"
    "s_waitcnt lgkmcnt(5)\n"
    "buffer_load_dwordx4 a[196:199], v250, s[8:11], s62 offen offset:2048 sc1\n"
    "v_mfma_f32_16x16x32_bf16 v[188:191], a[160:163], a[32:35], v[224:227]\n"
    "ds_read_b128 a[180:183], v243 offset:8192\n"
    "v_cvt_pk_bf16_f32 v148, v148, v149\n"
    "v_mfma_f32_16x16x32_bf16 v[188:191], a[164:167], a[36:39], v[188:191]\n"
    "ds_read_b128 a[184:187], v242 offset:12288\n"
    "v_exp_f32_e32 v144, v144\n"
    "v_mfma_f32_16x16x32_bf16 v[180:183], a[164:167], a[4:7], v[220:223]\n"
    "ds_write_addtid_b32 v148 offset:7168\n"
    "v_exp_f32_e32 v145, v145\n"
    "v_mfma_f32_16x16x32_bf16 v[180:183], a[160:163], a[0:3], v[180:183]\n"
    "s_waitcnt lgkmcnt(5)\n"
    "v_cvt_pk_bf16_f32 v149, v150, v151\n"
    "v_exp_f32_e32 v146, v146\n"
    "v_mfma_f32_16x16x32_bf16 v[180:183], a[168:171], a[8:11], v[180:183]\n"
    "ds_read_b128 a[188:191], v243 offset:12288\n"
    "v_exp_f32_e32 v147, v147\n"
    "v_mfma_f32_16x16x32_bf16 v[180:183], a[172:175], a[12:15], v[180:183]\n"
    "ds_read_b128 a[160:163], v240 offset:10240\n"
    "v_cvt_pk_bf16_f32 v142, v144, v145\n"
    "v_mfma_f32_16x16x32_bf16 v[188:191], a[172:175], a[44:47], v[188:191]\n"
    "buffer_load_dwordx4 a[200:203], v250, s[8:11], s62 offen offset:3072 sc1\n"
    "ds_write_addtid_b32 v149 offset:7424\n"
    "v_mfma_f32_16x16x32_bf16 v[188:191], a[168:171], a[40:43], v[188:191]\n"
    "s_waitcnt lgkmcnt(5)\n"
    "ds_write_addtid_b32 v142 offset:6656\n"
    "v_exp_f32_e32 v152, v152\n"
    "v_mfma_f32_16x16x32_bf16 v[188:191], a[176:179], a[48:51], v[188:191]\n"
    "s_add_u32 s62, s58, 0\n"
    "ds_read_b128 a[164:167], v241 offset:10240\n"
    "v_exp_f32_e32 v153, v153\n"
    "v_mfma_f32_16x16x32_bf16 v[188:191], a[180:183], a[52:55], v[188:191]\n"
    "ds_read_b128 a[168:171], v240 offset:14336\n"
    "v_cvt_pk_bf16_f32 v143, v146, v147\n"
    "v_mfma_f32_16x16x32_bf16 v[180:183], a[180:183], a[20:23], v[180:183]\n"
    "v_exp_f32_e32 v154, v154\n"
    "v_mfma_f32_16x16x32_bf16 v[180:183], a[176:179], a[16:19], v[180:183]\n"
    "s_waitcnt lgkmcnt(5)\n"
    "buffer_store_dwordx4 v[76:79], v250, s[8:11], s29 offen offset:2048 sc1\n"
    "v_mfma_f32_16x16x32_bf16 v[180:183], a[184:187], a[24:27], v[180:183]\n"
    "ds_read_b128 a[172:175], v241 offset:14336\n"
    "v_exp_f32_e32 v155, v155\n"
    "v_mfma_f32_16x16x32_bf16 v[180:183], a[188:191], a[28:31], v[180:183]\n"
    "ds_read_b128 a[176:179], v242 offset:10240\n"
    "v_cvt_pk_bf16_f32 v150, v152, v153\n"
    "v_mfma_f32_16x16x32_bf16 v[188:191], a[188:191], a[60:63], v[188:191]\n"
    "buffer_load_dwordx4 a[244:247], v250, s[8:11], s62 offen offset:0 sc1\n"
    "ds_write_addtid_b32 v143 offset:6912\n"
    "v_mfma_f32_16x16x32_bf16 v[188:191], a[184:187], a[56:59], v[188:191]\n"
    "s_waitcnt lgkmcnt(4)\n"
    "v_exp_f32_e32 v180, v180\n"
    "v_mfma_f32_16x16x32_bf16 v[192:195], a[160:163], a[32:35], v[224:227]\n"
    "ds_read_b128 a[180:183], v243 offset:10240\n"
    "v_cvt_pk_bf16_f32 v151, v154, v155\n"
    "v_mfma_f32_16x16x32_bf16 v[192:195], a[164:167], a[36:39], v[192:195]\n"
    "ds_read_b128 a[184:187], v242 offset:14336\n"
    "v_exp_f32_e32 v181, v181\n"
    "v_mfma_f32_16x16x32_bf16 v[184:187], a[164:167], a[4:7], v[220:223]\n"
    "s_waitcnt vmcnt(12)\n"
    "buffer_store_dword v254, v231, s[8:11], s38 offen offset:0 sc1\n"
    "v_mfma_f32_16x16x32_bf16 v[184:187], a[160:163], a[0:3], v[184:187]\n"
    "s_waitcnt lgkmcnt(4)\n"
    "v_add_u32_e32 v254, 1, v254\n"
    "v_exp_f32_e32 v182, v182\n"
    "v_mfma_f32_16x16x32_bf16 v[184:187], a[168:171], a[8:11], v[184:187]\n"
    "ds_read_b128 a[188:191], v243 offset:14336\n"
    "v_exp_f32_e32 v183, v183\n"
    "v_mfma_f32_16x16x32_bf16 v[184:187], a[172:175], a[12:15], v[184:187]\n"
    "buffer_load_dwordx4 v[8:11], v250, s[8:11], s62 offen offset:1024 sc1\n"
    "v_mfma_f32_16x16x32_bf16 v[192:195], a[172:175], a[44:47], v[192:195]\n"
    "ds_read_b128 a[160:163], v240 offset:16384\n"
    "v_cvt_pk_bf16_f32 v180, v180, v181\n"
    "v_mfma_f32_16x16x32_bf16 v[192:195], a[168:171], a[40:43], v[192:195]\n"
    "s_waitcnt lgkmcnt(3)\n"
    "ds_write_addtid_b32 v180 offset:0\n"
    "v_exp_f32_e32 v188, v188\n"
    "v_mfma_f32_16x16x32_bf16 v[192:195], a[176:179], a[48:51], v[192:195]\n"
    "ds_read_b128 a[164:167], v241 offset:16384\n"
    "v_exp_f32_e32 v189, v189\n"
    "v_mfma_f32_16x16x32_bf16 v[192:195], a[180:183], a[52:55], v[192:195]\n"
    "ds_read_b128 a[168:171], v240 offset:20480\n"
    "v_cvt_pk_bf16_f32 v181, v182, v183\n"
    "v_mfma_f32_16x16x32_bf16 v[184:187], a[180:183], a[20:23], v[184:187]\n"
    "ds_write_addtid_b32 v181 offset:256\n"
    "v_exp_f32_e32 v190, v190\n"
    "v_mfma_f32_16x16x32_bf16 v[184:187], a[176:179], a[16:19], v[184:187]\n"
    "s_waitcnt lgkmcnt(5)\n"
    "buffer_store_dwordx4 v[108:111], v250, s[8:11], s41 offen offset:3072 sc1\n"
    "v_mfma_f32_16x16x32_bf16 v[184:187], a[184:187], a[24:27], v[184:187]\n"
    "ds_read_b128 a[172:175], v241 offset:20480\n"
    "v_exp_f32_e32 v191, v191\n"
    "v_mfma_f32_16x16x32_bf16 v[184:187], a[188:191], a[28:31], v[184:187]\n"
    "ds_read_b128 a[176:179], v242 offset:16384\n"
    "v_cvt_pk_bf16_f32 v188, v188, v189\n"
    "v_mfma_f32_16x16x32_bf16 v[192:195], a[188:191], a[60:63], v[192:195]\n"
    "buffer_load_dwordx4 a[248:251], v250, s[8:11], s62 offen offset:2048 sc1\n"
    "ds_write_addtid_b32 v188 offset:1024\n"
    "v_mfma_f32_16x16x32_bf16 v[192:195], a[184:187], a[56:59], v[192:195]\n"
    "s_waitcnt lgkmcnt(5)\n"
    "ds_write_addtid_b32 v150 offset:7680\n"
    "v_exp_f32_e32 v184, v184\n"
    "v_mfma_f32_16x16x32_bf16 v[204:207], a[160:163], a[32:35], v[224:227]\n"
    "ds_read_b128 a[180:183], v243 offset:16384\n"
    "v_exp_f32_e32 v185, v185\n"
    "v_mfma_f32_16x16x32_bf16 v[204:207], a[164:167], a[36:39], v[204:207]\n"
    "ds_read_b128 a[184:187], v242 offset:20480\n"
    "v_cvt_pk_bf16_f32 v189, v190, v191\n"
    "v_mfma_f32_16x16x32_bf16 v[196:199], a[164:167], a[4:7], v[220:223]\n"
    "v_exp_f32_e32 v186, v186\n"
    "v_mfma_f32_16x16x32_bf16 v[196:199], a[160:163], a[0:3], v[196:199]\n"
    "s_waitcnt lgkmcnt(5)\n"
    "v_exp_f32_e32 v187, v187\n"
    "v_mfma_f32_16x16x32_bf16 v[196:199], a[168:171], a[8:11], v[196:199]\n"
    "ds_read_b128 a[188:191], v243 offset:20480\n"
    "ds_write_addtid_b32 v189 offset:1280\n"
    "v_mfma_f32_16x16x32_bf16 v[196:199], a[172:175], a[12:15], v[196:199]\n"
    "buffer_load_dwordx4 v[12:15], v250, s[8:11], s62 offen offset:3072 sc1\n"
    "v_mfma_f32_16x16x32_bf16 v[204:207], a[172:175], a[44:47], v[204:207]\n"
    "s_add_u32 s62, s58, 4096\n"
    "ds_read_b128 a[160:163], v240 offset:18432\n"
    "v_cvt_pk_bf16_f32 v182, v184, v185\n"
    "v_mfma_f32_16x16x32_bf16 v[204:207], a[168:171], a[40:43], v[204:207]\n"
    "s_waitcnt lgkmcnt(4)\n"
    "ds_write_addtid_b32 v182 offset:512\n"
    "v_exp_f32_e32 v192, v192\n"
    "v_mfma_f32_16x16x32_bf16 v[204:207], a[176:179], a[48:51], v[204:207]\n"
    "ds_read_b128 a[164:167], v241 offset:18432\n"
    "v_exp_f32_e32 v193, v193\n"
    "v_mfma_f32_16x16x32_bf16 v[204:207], a[180:183], a[52:55], v[204:207]\n"
    "ds_read_b128 a[168:171], v240 offset:22528\n"
    "v_cvt_pk_bf16_f32 v183, v186, v187\n"
    "v_mfma_f32_16x16x32_bf16 v[196:199], a[180:183], a[20:23], v[196:199]\n"
    "ds_write_addtid_b32 v183 offset:768\n"
    "v_exp_f32_e32 v194, v194\n"
    "v_mfma_f32_16x16x32_bf16 v[196:199], a[176:179], a[16:19], v[196:199]\n"
    "s_waitcnt lgkmcnt(6)\n"
    "buffer_store_dwordx4 v[112:115], v250, s[8:11], s28 offen offset:3072 sc1\n"
    "v_mfma_f32_16x16x32_bf16 v[196:199], a[184:187], a[24:27], v[196:199]\n"
    "ds_read_b128 a[172:175], v241 offset:22528\n"
    "v_exp_f32_e32 v195, v195\n"
    "v_mfma_f32_16x16x32_bf16 v[196:199], a[188:191], a[28:31], v[196:199]\n"
    "ds_read_b128 a[176:179], v242 offset:18432\n"
    "v_cvt_pk_bf16_f32 v190, v192, v193\n"
    "v_mfma_f32_16x16x32_bf16 v[204:207], a[188:191], a[60:63], v[204:207]\n"
    "buffer_load_dwordx4 a[252:255], v250, s[8:11], s62 offen offset:0 sc1\n"
    "ds_write_addtid_b32 v151 offset:7936\n"
    "v_mfma_f32_16x16x32_bf16 v[204:207], a[184:187], a[56:59], v[204:207]\n"
    "s_waitcnt lgkmcnt(5)\n"
    "ds_write_addtid_b32 v190 offset:1536\n"
    "v_exp_f32_e32 v196, v196\n"
    "v_mfma_f32_16x16x32_bf16 v[208:211], a[160:163], a[32:35], v[224:227]\n"
    "ds_read_b128 a[180:183], v243 offset:18432\n"
    "v_cvt_pk_bf16_f32 v191, v194, v195\n"
    "v_mfma_f32_16x16x32_bf16 v[208:211], a[164:167], a[36:39], v[208:211]\n"
    "ds_read_b128 a[184:187], v242 offset:22528\n"
    "v_exp_f32_e32 v197, v197\n"
    "v_mfma_f32_16x16x32_bf16 v[200:203], a[164:167], a[4:7], v[220:223]\n"
    "ds_read_b128 a[188:191], v243 offset:22528\n"
    "v_exp_f32_e32 v198, v198\n"
    "v_mfma_f32_16x16x32_bf16 v[200:203], a[160:163], a[0:3], v[200:203]\n"
    "s_waitcnt lgkmcnt(6)\n"
    "s_waitcnt lgkmcnt(4)\n"
    "v_mfma_f32_16x16x32_bf16 v[200:203], a[168:171], a[8:11], v[200:203]\n"
    "s_barrier\n"
    "v_mfma_f32_16x16x32_bf16 v[200:203], a[172:175], a[12:15], v[200:203]\n"
    "buffer_load_dwordx4 v[16:19], v250, s[8:11], s62 offen offset:1024 sc1\n"
    "ds_write_addtid_b32 v191 offset:1792\n"
    "v_mfma_f32_16x16x32_bf16 v[208:211], a[172:175], a[44:47], v[208:211]\n"
    "ds_read_b64_tr_b16 a[160:161], v244 offset:0\n"
    "ds_read_b64_tr_b16 a[162:163], v244 offset:2048\n"
    "ds_read2st64_b32 v[144:145], v249 offset0:24 offset1:25\n"
    "v_mfma_f32_16x16x32_bf16 v[208:211], a[168:171], a[40:43], v[208:211]\n"
    "s_waitcnt lgkmcnt(6)\n"
    "ds_read_b64_tr_b16 a[164:165], v244 offset:32768\n"
    "ds_read2st64_b32 v[146:147], v249 offset0:26 offset1:27\n"
    "v_exp_f32_e32 v199, v199\n"
    "v_mfma_f32_16x16x32_bf16 v[208:211], a[176:179], a[48:51], v[208:211]\n"
    "ds_read_b64_tr_b16 a[166:167], v244 offset:34816\n"
    "ds_read_b64_tr_b16 a[168:169], v245 offset:0\n"
    "ds_read2st64_b32 v[152:153], v249 offset0:28 offset1:29\n"
    "v_mfma_f32_16x16x32_bf16 v[208:211], a[180:183], a[52:55], v[208:211]\n"
    "ds_read_b64_tr_b16 a[170:171], v245 offset:2048\n"
    "ds_read2st64_b32 v[154:155], v249 offset0:30 offset1:31\n"
    "v_cvt_pk_bf16_f32 v196, v196, v197\n"
    "v_mfma_f32_16x16x32_bf16 v[200:203], a[180:183], a[20:23], v[200:203]\n"
    "ds_read_b64_tr_b16 a[172:173], v245 offset:32768\n"
    "v_exp_f32_e32 v204, v204\n"
    "v_mfma_f32_16x16x32_bf16 v[200:203], a[176:179], a[16:19], v[200:203]\n"
    "s_waitcnt lgkmcnt(12)\n"
    "buffer_store_dwordx4 v[116:119], v250, s[8:11], s29 offen offset:3072 sc1\n"
    "v_mfma_f32_16x16x32_bf16 v[200:203], a[184:187], a[24:27], v[200:203]\n"
    "ds_read_b64_tr_b16 a[174:175], v245 offset:34816\n"
    "ds_read_b64_tr_b16 a[176:177], v246 offset:0\n"
    "v_mfma_f32_16x16x32_bf16 v[200:203], a[188:191], a[28:31], v[200:203]\n"
    "ds_read_b64_tr_b16 a[178:179], v246 offset:2048\n"
    "v_exp_f32_e32 v205, v205\n"
    "v_mfma_f32_16x16x32_bf16 v[208:211], a[188:191], a[60:63], v[208:211]\n"
    "buffer_load_dwordx4 v[28:31], v250, s[8:11], s62 offen offset:2048 sc1\n"
    "ds_write_addtid_b32 v196 offset:2048\n"
    "v_mfma_f32_16x16x32_bf16 v[208:211], a[184:187], a[56:59], v[208:211]\n"
    "s_waitcnt lgkmcnt(5)\n"
    "ds_read_b64_tr_b16 a[180:181], v246 offset:32768\n"
    "v_cvt_pk_bf16_f32 v197, v198, v199\n"
    "v_mfma_f32_16x16x32_bf16 a[240:243], a[160:163], v[148:151], a[240:243]\n"
    "ds_read_b64_tr_b16 a[182:183], v246 offset:34816\n"
    "ds_read_b64_tr_b16 a[184:185], v247 offset:0\n"
    "v_mfma_f32_16x16x32_bf16 a[240:243], a[164:167], v[152:155], a[240:243]\n"
    "ds_read_b64_tr_b16 a[186:187], v247 offset:2048\n"
    "v_exp_f32_e32 v206, v206\n"
    "v_mfma_f32_16x16x32_bf16 a[236:239], a[164:167], v[144:147], a[236:239]\n"
    "ds_read_b64_tr_b16 a[188:189], v247 offset:32768\n"
    "v_exp_f32_e32 v207, v207\n"
    "v_mfma_f32_16x16x32_bf16 a[236:239], a[160:163], v[140:143], a[236:239]\n"
    "s_waitcnt lgkmcnt(8)\n"
    "ds_read_b64_tr_b16 a[190:191], v247 offset:34816\n"
    "ds_read_b64_tr_b16 a[160:161], v244 offset:4096\n"
    "v_mfma_f32_16x16x32_bf16 v[0:3], a[168:171], v[140:143], v[0:3]\n"
    "buffer_load_dword v253, v238, s[8:11], s48 offen offset:0 sc1\n"
    "ds_write_addtid_b32 v197 offset:2304\n"
    "v_mfma_f32_16x16x32_bf16 v[0:3], a[172:175], v[144:147], v[0:3]\n"
    "ds_read_b64_tr_b16 a[162:163], v244 offset:6144\n"
    "v_cvt_pk_bf16_f32 v204, v204, v205\n"
    "v_mfma_f32_16x16x32_bf16 v[4:7], a[172:175], v[152:155], v[4:7]\n"
    "ds_read_b64_tr_b16 a[164:165], v244 offset:36864\n"
    "v_exp_f32_e32 v200, v200\n"
    "v_mfma_f32_16x16x32_bf16 v[4:7], a[168:171], v[148:151], v[4:7]\n"
    "s_waitcnt lgkmcnt(8)\n"
    "buffer_load_dwordx4 v[48:51], v250, s[8:11], s62 offen offset:3072 sc1\n"
    "ds_write_addtid_b32 v204 offset:3072\n"
    "v_mfma_f32_16x16x32_bf16 v[24:27], a[176:179], v[148:151], v[24:27]\n"
    "s_add_u32 s62, s58, 8192\n"
    "ds_read_b64_tr_b16 a[166:167], v244 offset:38912\n"
    "v_exp_f32_e32 v201, v201\n"
    "v_mfma_f32_16x16x32_bf16 v[24:27], a[180:183], v[152:155], v[24:27]\n"
    "ds_read_b64_tr_b16 a[168:169], v245 offset:4096\n"
    "v_cvt_pk_bf16_f32 v205, v206, v207\n"
    "v_mfma_f32_16x16x32_bf16 v[20:23], a[180:183], v[144:147], v[20:23]\n"
    "ds_read_b64_tr_b16 a[170:171], v245 offset:6144\n"
    "v_exp_f32_e32 v202, v202\n"
    "v_mfma_f32_16x16x32_bf16 v[20:23], a[176:179], v[140:143], v[20:23]\n"
    "s_waitcnt lgkmcnt(8)\n"
    "ds_read_b64_tr_b16 a[172:173], v245 offset:36864\n"
    "v_exp_f32_e32 v203, v203\n"
    "v_mfma_f32_16x16x32_bf16 v[40:43], a[184:187], v[140:143], v[40:43]\n"
    "ds_read_b64_tr_b16 a[174:175], v245 offset:38912\n"
    "ds_read_b64_tr_b16 a[176:177], v246 offset:4096\n"
    "v_mfma_f32_16x16x32_bf16 v[40:43], a[188:191], v[144:147], v[40:43]\n"
    "ds_read_b64_tr_b16 a[178:179], v246 offset:6144\n"
    "v_cvt_pk_bf16_f32 v198, v200, v201\n"
    "v_mfma_f32_16x16x32_bf16 v[44:47], a[188:191], v[152:155], v[44:47]\n"
    "buffer_load_dwordx4 v[32:35], v250, s[8:11], s62 offen offset:0 sc1\n"
    "ds_write_addtid_b32 v205 offset:3328\n"
    "v_mfma_f32_16x16x32_bf16 v[44:47], a[184:187], v[148:151], v[44:47]\n"
    "s_waitcnt lgkmcnt(7)\n"
    "ds_read_b64_tr_b16 a[180:181], v246 offset:36864\n"
    "v_exp_f32_e32 v208, v208\n"
    "v_mfma_f32_16x16x32_bf16 v[64:67], a[160:163], v[148:151], v[64:67]\n"
    "ds_read_b64_tr_b16 a[182:183], v246 offset:38912\n"
    "ds_read_b64_tr_b16 a[184:185], v247 offset:4096\n"
    "v_mfma_f32_16x16x32_bf16 v[64:67], a[164:167], v[152:155], v[64:67]\n"
    "ds_read_b64_tr_b16 a[186:187], v247 offset:6144\n"
    "v_exp_f32_e32 v209, v209\n"
    "v_mfma_f32_16x16x32_bf16 v[60:63], a[164:167], v[144:147], v[60:63]\n"
    "ds_read_b64_tr_b16 a[188:189], v247 offset:36864\n"
    "ds_read_b64_tr_b16 a[190:191], v247 offset:38912\n"
    "v_mfma_f32_16x16x32_bf16 v[60:63], a[160:163], v[140:143], v[60:63]\n"
    "s_waitcnt lgkmcnt(9)\n"
    "s_waitcnt lgkmcnt(15)\n"
    "v_mfma_f32_16x16x32_bf16 v[80:83], a[168:171], v[140:143], v[80:83]\n"
    "s_barrier\n"
    "v_mfma_f32_16x16x32_bf16 v[80:83], a[172:175], v[144:147], v[80:83]\n"
    "buffer_load_dwordx4 v[52:55], v250, s[8:11], s62 offen offset:1024 sc1\n"
    "ds_write_addtid_b32 v198 offset:2560\n"
    "v_mfma_f32_16x16x32_bf16 v[84:87], a[172:175], v[152:155], v[84:87]\n"
    "s_add_u32 s31, s46, s35\n"
    "ds_read_b64_tr_b16 a[160:161], v244 offset:12288\n"
    "ds_read_b64_tr_b16 a[162:163], v244 offset:14336\n"
    "ds_read2st64_b32 v[184:185], v249 offset0:0 offset1:1\n"
    "v_mfma_f32_16x16x32_bf16 v[84:87], a[168:171], v[148:151], v[84:87]\n"
    "s_waitcnt lgkmcnt(8)\n"
    "ds_read_b64_tr_b16 a[164:165], v244 offset:45056\n"
    "ds_read2st64_b32 v[186:187], v249 offset0:2 offset1:3\n"
    "v_cvt_pk_bf16_f32 v199, v202, v203\n"
    "v_mfma_f32_16x16x32_bf16 v[104:107], a[176:179], v[148:151], v[104:107]\n"
    "s_add_u32 m0, s55, 0\n"
    "ds_read_b64_tr_b16 a[166:167], v244 offset:47104\n"
    "ds_read_b64_tr_b16 a[168:169], v245 offset:12288\n"
    "ds_read2st64_b32 v[192:193], v249 offset0:4 offset1:5\n"
    "v_mfma_f32_16x16x32_bf16 v[104:107], a[180:183], v[152:155], v[104:107]\n"
    "buffer_load_dwordx4 v251, s[12:15], s31 offen offset:0 lds\n"
    "ds_read2st64_b32 v[194:195], v249 offset0:6 offset1:7\n"
    "v_mfma_f32_16x16x32_bf16 v[100:103], a[180:183], v[144:147], v[100:103]\n"
    "s_mov_b32 m0, s63\n"
    "ds_read_b64_tr_b16 a[170:171], v245 offset:14336\n"
    "v_exp_f32_e32 v210, v210\n"
    "v_mfma_f32_16x16x32_bf16 v[100:103], a[176:179], v[140:143], v[100:103]\n"
    "s_waitcnt lgkmcnt(11)\n"
    "ds_read_b64_tr_b16 a[172:173], v245 offset:45056\n"
    "ds_read_b64_tr_b16 a[174:175], v245 offset:47104\n"
    "v_mfma_f32_16x16x32_bf16 v[120:123], a[184:187], v[140:143], v[120:123]\n"
    "buffer_load_dwordx4 a[204:207], v250, s[8:11], s62 offen offset:2048 sc1\n"
    "ds_write_addtid_b32 v199 offset:2816\n"
    "v_mfma_f32_16x16x32_bf16 v[120:123], a[188:191], v[144:147], v[120:123]\n"
    "s_add_u32 s31, s31, 0x1000\n"
    "ds_read_b64_tr_b16 a[176:177], v246 offset:12288\n"
    "ds_read_b64_tr_b16 a[178:179], v246 offset:14336\n"
    "v_mfma_f32_16x16x32_bf16 v[124:127], a[188:191], v[152:155], v[124:127]\n"
    "s_add_u32 m0, s55, 1024\n"
    "ds_read_b64_tr_b16 a[180:181], v246 offset:45056\n"
    "v_exp_f32_e32 v211, v211\n"
    "v_mfma_f32_16x16x32_bf16 v[124:127], a[184:187], v[148:151], v[124:127]\n"
    "s_waitcnt lgkmcnt(7)\n"
    "buffer_load_dwordx4 v251, s[12:15], s31 offen offset:0 lds\n"
    "v_mfma_f32_16x16x32_bf16 v[64:67], a[160:163], v[188:191], v[64:67]\n"
    "s_mov_b32 m0, s63\n"
    "ds_read_b64_tr_b16 a[182:183], v246 offset:47104\n"
    "ds_read_b64_tr_b16 a[184:185], v247 offset:12288\n"
    "v_mfma_f32_16x16x32_bf16 v[64:67], a[164:167], v[192:195], v[64:67]\n"
    "ds_read_b64_tr_b16 a[186:187], v247 offset:14336\n"
    "v_cvt_pk_bf16_f32 v206, v208, v209\n"
    "v_mfma_f32_16x16x32_bf16 v[60:63], a[164:167], v[184:187], v[60:63]\n"
    "ds_read_b64_tr_b16 a[188:189], v247 offset:45056\n"
    "ds_write_addtid_b32 v206 offset:3584\n"
    "v_cvt_pk_bf16_f32 v207, v210, v211\n"
    "v_mfma_f32_16x16x32_bf16 v[60:63], a[160:163], v[180:183], v[60:63]\n"
    "s_waitcnt lgkmcnt(9)\n"
    "buffer_load_dwordx4 a[208:211], v250, s[8:11], s62 offen offset:3072 sc1\n"
    "ds_write_addtid_b32 v207 offset:3840\n"
    "v_mfma_f32_16x16x32_bf16 v[80:83], a[168:171], v[180:183], v[80:83]\n"
    "s_add_u32 s31, s46, s36\n"
    "ds_read_b64_tr_b16 a[190:191], v247 offset:47104\n"
    "ds_read_b64_tr_b16 a[160:161], v244 offset:8192\n"
    "v_mfma_f32_16x16x32_bf16 v[80:83], a[172:175], v[184:187], v[80:83]\n"
    "s_add_u32 m0, s55, 2048\n"
    "ds_read_b64_tr_b16 a[162:163], v244 offset:10240\n"
    "ds_read_b64_tr_b16 a[164:165], v244 offset:40960\n"
    "v_mfma_f32_16x16x32_bf16 v[84:87], a[172:175], v[192:195], v[84:87]\n"
    "buffer_load_dwordx4 v251, s[12:15], s31 offen offset:0 lds\n"
    "ds_read_b64_tr_b16 a[166:167], v244 offset:43008\n"
    "v_mfma_f32_16x16x32_bf16 v[84:87], a[168:171], v[188:191], v[84:87]\n"
    "s_waitcnt lgkmcnt(10)\n"
    "s_waitcnt lgkmcnt(5)\n"
    "v_mfma_f32_16x16x32_bf16 v[104:107], a[176:179], v[188:191], v[104:107]\n"
    "s_barrier\n"
    "v_mfma_f32_16x16x32_bf16 v[104:107], a[180:183], v[192:195], v[104:107]\n"
    "s_mov_b32 m0, s63\n"
    "ds_read_b64_tr_b16 a[168:169], v244 offset:16384\n"
    "ds_read2st64_b32 v[200:201], v249 offset0:8 offset1:9\n"
    "v_mfma_f32_16x16x32_bf16 v[100:103], a[180:183], v[184:187], v[100:103]\n"
    "ds_read_b64_tr_b16 a[170:171], v244 offset:18432\n"
    "ds_read2st64_b32 v[202:203], v249 offset0:10 offset1:11\n"
    "v_mfma_f32_16x16x32_bf16 v[100:103], a[176:179], v[180:183], v[100:103]\n"
    "s_waitcnt lgkmcnt(8)\n"
    "ds_read_b64_tr_b16 a[172:173], v244 offset:49152\n"
    "ds_read2st64_b32 v[208:209], v249 offset0:12 offset1:13\n"
    "v_mfma_f32_16x16x32_bf16 v[120:123], a[184:187], v[180:183], v[120:123]\n"
    "s_add_u32 s31, s31, 0x1000\n"
    "ds_read_b64_tr_b16 a[174:175], v244 offset:51200\n"
    "ds_read_b64_tr_b16 a[176:177], v245 offset:8192\n"
    "v_mfma_f32_16x16x32_bf16 v[120:123], a[188:191], v[184:187], v[120:123]\n"
    "s_add_u32 m0, s55, 3072\n"
    "s_waitcnt vmcnt(8)\n"
    "ds_read2st64_b32 v[210:211], v249 offset0:14 offset1:15\n"
    "v_subrev_u32_e32 v253, s39, v253\n"
    "v_subrev_u32_e32 v253, 0, v253\n"
    "v_mfma_f32_16x16x32_bf16 v[124:127], a[188:191], v[192:195], v[124:127]\n"
    "buffer_load_dwordx4 v251, s[12:15], s31 offen offset:0 lds\n"
    "v_cmp_le_u32_e32 vcc, 2, v253\n"
    "v_mfma_f32_16x16x32_bf16 v[124:127], a[184:187], v[188:191], v[124:127]\n"
    "s_waitcnt lgkmcnt(9)\n"
    "ds_read_b64_tr_b16 a[178:179], v245 offset:10240\n"
    "ds_read_b64_tr_b16 a[180:181], v245 offset:40960\n"
    "v_mfma_f32_16x16x32_bf16 a[240:243], a[160:163], v[188:191], a[240:243]\n"
    "s_mov_b32 m0, s63\n"
    "ds_read_b64_tr_b16 a[182:183], v245 offset:43008\n"
    "ds_read_b64_tr_b16 a[184:185], v245 offset:16384\n"
    "v_mfma_f32_16x16x32_bf16 a[240:243], a[164:167], v[192:195], a[240:243]\n"
    "s_cmp_eq_u64 vcc, 0\n"
    "s_cselect_b32 s89, s59, 2.0\n"
    "s_cselect_b32 s56, s56, 1\n"
    "v_mfma_f32_16x16x32_bf16 a[236:239], a[164:167], v[184:187], a[236:239]\n"
    "s_add_u32 s31, s46, s37\n"
    "s_waitcnt vmcnt(11)\n"
    "buffer_store_dword v254, v231, s[8:11], s38 offen offset:0 sc1\n"
    "v_mfma_f32_16x16x32_bf16 a[236:239], a[160:163], v[180:183], a[236:239]\n"
    "s_waitcnt lgkmcnt(4)\n"
    "ds_read_b64_tr_b16 a[186:187], v245 offset:18432\n"
    "ds_read_b64_tr_b16 a[188:189], v245 offset:49152\n"
    "v_mfma_f32_16x16x32_bf16 a[236:239], a[168:171], v[196:199], a[236:239]\n"
    "s_add_u32 m0, s55, 4096\n"
    "ds_read_b64_tr_b16 a[190:191], v245 offset:51200\n"
    "ds_read_b64_tr_b16 a[160:161], v246 offset:8192\n"
    "v_mfma_f32_16x16x32_bf16 a[236:239], a[172:175], v[200:203], a[236:239]\n"
    "s_add_u32 s87, s89, 8192\n"
    "buffer_load_dwordx4 v251, s[12:15], s31 offen offset:0 lds\n"
    "v_mfma_f32_16x16x32_bf16 a[240:243], a[172:175], v[208:211], a[240:243]\n"
    "s_mov_b32 m0, s63\n"
    "ds_read_b64_tr_b16 a[162:163], v246 offset:10240\n"
    "v_mfma_f32_16x16x32_bf16 a[240:243], a[168:171], v[204:207], a[240:243]\n"
    "s_waitcnt lgkmcnt(6)\n"
    "ds_read_b64_tr_b16 a[164:165], v246 offset:40960\n"
    "v_mfma_f32_16x16x32_bf16 v[4:7], a[176:179], v[188:191], v[4:7]\n"
    "s_add_u32 s88, s89, 16384\n"
    "ds_read_b64_tr_b16 a[166:167], v246 offset:43008\n"
    "ds_read_b64_tr_b16 a[168:169], v246 offset:16384\n"
    "v_mfma_f32_16x16x32_bf16 v[4:7], a[180:183], v[192:195], v[4:7]\n"
    "s_add_u32 s31, s31, 0x1000\n"
    "ds_read_b64_tr_b16 a[170:171], v246 offset:18432\n"
    "v_mfma_f32_16x16x32_bf16 v[0:3], a[180:183], v[184:187], v[0:3]\n"
    "s_add_u32 m0, s55, 5120\n"
    "ds_read_b64_tr_b16 a[172:173], v246 offset:49152\n"
    "v_accvgpr_read_b32 v140, a240\n"
    "v_mfma_f32_16x16x32_bf16 v[0:3], a[176:179], v[180:183], v[0:3]\n"
    "s_waitcnt lgkmcnt(7)\n"
    "buffer_load_dwordx4 v251, s[12:15], s31 offen offset:0 lds\n"
    "v_mfma_f32_16x16x32_bf16 v[0:3], a[184:187], v[196:199], v[0:3]\n"
    "s_mov_b32 m0, s63\n"
    "ds_read_b64_tr_b16 a[174:175], v246 offset:51200\n"
    "ds_read_b64_tr_b16 a[176:177], v247 offset:8192\n"
    "v_mfma_f32_16x16x32_bf16 v[0:3], a[188:191], v[200:203], v[0:3]\n"
    "s_add_u32 s89, s89, 24576\n"
    "ds_read_b64_tr_b16 a[178:179], v247 offset:10240\n"
    "v_mfma_f32_16x16x32_bf16 v[4:7], a[188:191], v[208:211], v[4:7]\n"
    "buffer_load_dwordx4 a[0:3], v250, s[8:11], s87 offen offset:0 sc1\n"
    "v_mfma_f32_16x16x32_bf16 v[4:7], a[184:187], v[204:207], v[4:7]\n"
    "s_waitcnt lgkmcnt(6)\n"
    "ds_read_b64_tr_b16 a[180:181], v247 offset:40960\n"
    "v_accvgpr_read_b32 v141, a241\n"
    "v_mfma_f32_16x16x32_bf16 v[24:27], a[160:163], v[188:191], v[24:27]\n"
    "ds_read_b64_tr_b16 a[182:183], v247 offset:43008\n"
    "ds_read_b64_tr_b16 a[184:185], v247 offset:16384\n"
    "v_mfma_f32_16x16x32_bf16 v[24:27], a[164:167], v[192:195], v[24:27]\n"
    "ds_read_b64_tr_b16 a[186:187], v247 offset:18432\n"
    "v_mfma_f32_16x16x32_bf16 v[20:23], a[164:167], v[184:187], v[20:23]\n"
    "buffer_load_dwordx4 a[4:7], v250, s[8:11], s88 offen offset:0 sc1\n"
    "v_mfma_f32_16x16x32_bf16 v[20:23], a[160:163], v[180:183], v[20:23]\n"
    "s_waitcnt lgkmcnt(6)\n"
    "ds_read_b64_tr_b16 a[188:189], v247 offset:49152\n"
    "v_accvgpr_read_b32 v142, a242\n"
    "v_mfma_f32_16x16x32_bf16 v[20:23], a[168:171], v[196:199], v[20:23]\n"
    "ds_read_b64_tr_b16 a[190:191], v247 offset:51200\n"
    "ds_read_b64_tr_b16 a[160:161], v244 offset:20480\n"
    "v_mfma_f32_16x16x32_bf16 v[20:23], a[172:175], v[200:203], v[20:23]\n"
    "ds_read_b64_tr_b16 a[162:163], v244 offset:22528\n"
    "v_mfma_f32_16x16x32_bf16 v[24:27], a[172:175], v[208:211], v[24:27]\n"
    "buffer_load_dwordx4 a[8:11], v250, s[8:11], s89 offen offset:0 sc1\n"
    "v_mfma_f32_16x16x32_bf16 v[24:27], a[168:171], v[204:207], v[24:27]\n"
    "s_waitcnt lgkmcnt(6)\n"
    "ds_read_b64_tr_b16 a[164:165], v244 offset:53248\n"
    "v_accvgpr_read_b32 v143, a243\n"
    "v_mfma_f32_16x16x32_bf16 v[44:47], a[176:179], v[188:191], v[44:47]\n"
    "ds_read_b64_tr_b16 a[166:167], v244 offset:55296\n"
    "ds_read_b64_tr_b16 a[168:169], v245 offset:20480\n"
    "v_mfma_f32_16x16x32_bf16 v[44:47], a[180:183], v[192:195], v[44:47]\n"
    "ds_read_b64_tr_b16 a[170:171], v245 offset:22528\n"
    "v_mfma_f32_16x16x32_bf16 v[40:43], a[180:183], v[184:187], v[40:43]\n"
    "buffer_load_dwordx4 a[12:15], v250, s[8:11], s87 offen offset:1024 sc1\n"
    "v_mfma_f32_16x16x32_bf16 v[40:43], a[176:179], v[180:183], v[40:43]\n"
    "s_waitcnt lgkmcnt(6)\n"
    "ds_read_b64_tr_b16 a[172:173], v245 offset:53248\n"
    "v_cvt_pk_bf16_f32 v140, v140, v141\n"
    "v_mfma_f32_16x16x32_bf16 v[40:43], a[184:187], v[196:199], v[40:43]\n"
    "ds_read_b64_tr_b16 a[174:175], v245 offset:55296\n"
    "ds_read_b64_tr_b16 a[176:177], v246 offset:20480\n"
    "v_mfma_f32_16x16x32_bf16 v[40:43], a[188:191], v[200:203], v[40:43]\n"
    "ds_read_b64_tr_b16 a[178:179], v246 offset:22528\n"
    "v_mfma_f32_16x16x32_bf16 v[44:47], a[188:191], v[208:211], v[44:47]\n"
    "buffer_load_dwordx4 a[16:19], v250, s[8:11], s88 offen offset:1024 sc1\n"
    "v_mfma_f32_16x16x32_bf16 v[44:47], a[184:187], v[204:207], v[44:47]\n"
    "s_waitcnt lgkmcnt(6)\n"
    "ds_read_b64_tr_b16 a[180:181], v246 offset:53248\n"
    "v_cvt_pk_bf16_f32 v141, v142, v143\n"
    "v_mfma_f32_16x16x32_bf16 v[64:67], a[160:163], v[204:207], v[64:67]\n"
    "ds_read_b64_tr_b16 a[182:183], v246 offset:55296\n"
    "ds_read_b64_tr_b16 a[184:185], v247 offset:20480\n"
    "v_mfma_f32_16x16x32_bf16 v[64:67], a[164:167], v[208:211], v[64:67]\n"
    "ds_read_b64_tr_b16 a[186:187], v247 offset:22528\n"
    "v_cvt_pk_bf16_f32 v142, v4, v5\n"
    "v_mfma_f32_16x16x32_bf16 v[60:63], a[164:167], v[200:203], v[60:63]\n"
    "buffer_load_dwordx4 a[20:23], v250, s[8:11], s89 offen offset:1024 sc1\n"
    "v_mfma_f32_16x16x32_bf16 v[60:63], a[160:163], v[196:199], v[60:63]\n"
    "s_waitcnt lgkmcnt(6)\n"
    "ds_read_b64_tr_b16 a[188:189], v247 offset:53248\n"
    "v_cvt_pk_bf16_f32 v143, v6, v7\n"
    "v_mfma_f32_16x16x32_bf16 v[80:83], a[168:171], v[196:199], v[80:83]\n"
    "ds_read_b64_tr_b16 a[190:191], v247 offset:55296\n"
    "buffer_store_dwordx4 v[140:143], v250, s[8:11], s40 offen offset:0 sc1\n"
    "v_mfma_f32_16x16x32_bf16 v[80:83], a[172:175], v[200:203], v[80:83]\n"
    "v_add_u32_e32 v240, s45, v240\n"
    "v_cvt_pk_bf16_f32 v24, v24, v25\n"
    "v_mfma_f32_16x16x32_bf16 v[84:87], a[172:175], v[208:211], v[84:87]\n"
    "v_xor_b32_e32 v241, 64, v240\n"
    "v_cvt_pk_bf16_f32 v25, v26, v27\n"
    "v_mfma_f32_16x16x32_bf16 v[84:87], a[168:171], v[204:207], v[84:87]\n"
    "s_waitcnt lgkmcnt(4)\n"
    "v_add_u32_e32 v242, 0x10000, v240\n"
    "v_cvt_pk_bf16_f32 v26, v44, v45\n"
    "v_mfma_f32_16x16x32_bf16 v[104:107], a[176:179], v[204:207], v[104:107]\n"
    "v_add_u32_e32 v243, 0x10000, v241\n"
    "v_cvt_pk_bf16_f32 v27, v46, v47\n"
    "v_mfma_f32_16x16x32_bf16 v[104:107], a[180:183], v[208:211], v[104:107]\n"
    "buffer_store_dwordx4 v[24:27], v250, s[8:11], s40 offen offset:1024 sc1\n"
    "v_add_u32_e32 v252, s54, v240\n"
    "v_mfma_f32_16x16x32_bf16 v[100:103], a[180:183], v[200:203], v[100:103]\n"
    "v_xor_b32_e32 v255, 64, v252\n"
    "v_cvt_pk_bf16_f32 v64, v64, v65\n"
    "v_mfma_f32_16x16x32_bf16 v[100:103], a[176:179], v[196:199], v[100:103]\n"
    "s_waitcnt lgkmcnt(0)\n"
    "v_lshlrev_b32_e32 v228, 16, v228\n"
    "v_cvt_pk_bf16_f32 v65, v66, v67\n"
    "v_mfma_f32_16x16x32_bf16 v[120:123], a[184:187], v[196:199], v[120:123]\n"
    "v_lshlrev_b32_e32 v229, 16, v229\n"
    "v_cvt_pk_bf16_f32 v66, v84, v85\n"
    "v_mfma_f32_16x16x32_bf16 v[120:123], a[188:191], v[200:203], v[120:123]\n"
    "v_lshlrev_b32_e32 v230, 16, v230\n"
    "v_cvt_pk_bf16_f32 v67, v86, v87\n"
    "v_mfma_f32_16x16x32_bf16 v[124:127], a[188:191], v[208:211], v[124:127]\n"
    "buffer_store_dwordx4 v[64:67], v250, s[8:11], s40 offen offset:2048 sc1\n"
    "v_mfma_f32_16x16x32_bf16 v[124:127], a[184:187], v[204:207], v[124:127]\n"
    "v_cvt_pk_bf16_f32 v104, v104, v105\n"
    "v_cvt_pk_bf16_f32 v105, v106, v107\n"
    "s_lshl1_add_u32 s45, s50, s51\n"
    "s_lshl_b32 s45, s45, 12\n"
    "v_add_u32_e32 v244, s45, v250\n"
    "s_xor_b32 s45, %2, 2\n"
    "s_lshl_b32 s45, s45, 8\n"
    "s_add_u32 s45, s45, 0x6000\n"
    "v_lshrrev_b32_e32 v245, 2, v250\n"
    "v_add_u32_e32 v245, s45, v245\n"
    "s_mov_b32 s45, 0x71800000\n"
    "v_cvt_pk_bf16_f32 v106, v124, v125\n"
    "v_cvt_pk_bf16_f32 v107, v126, v127\n"
    "buffer_store_dwordx4 v[104:107], v250, s[8:11], s40 offen offset:3072 sc1\n"
    "buffer_store_dword v127, v231, s[8:11], s86 offen offset:0 sc1\n"
    "v_add_f32_e32 v225, v123, v127\n"
    "v_min3_f32 v213, v213, v123, v127\n"
    "v_add_f32_e32 v225, v225, v212\n"
    "v_mov_b32_e32 v227, v123\n"
    "v_cmp_nlt_f32_e32 vcc, 0x12800000, v213\n"
    "v_cmp_nlt_f32_e64 s[28:29], v225, s45\n"
    "v_bfe_u32 v226, v250, 2, 4\n"
    "v_bfe_u32 v225, v250, 6, 2\n"
    "v_lshl_or_b32 v226, v225, 7, v226\n"
    "s_or_b32 s45, s29, vcc_hi\n"
    "s_lshr_b32 s45, s45, 16\n"
    "s_cselect_b32 s45, s51, 0\n"
    "s_andn2_b32 s45, s45, s49\n"
    "s_add_u32 s45, s45, s39\n"
    "v_mov_b32_e32 v254, s45\n"
    "s_cmp_lg_u32 s44, 0\n"
    "s_cbranch_scc1 .Lat_glate\n"
    ".Lat_glate_ret:\n"
    "s_waitcnt lgkmcnt(0)\n"
    "s_barrier\n"
    "ds_read_b128 a[48:51], v240 offset:0\n"
    "ds_read_b128 a[52:55], v240 offset:2048\n"
    "ds_read_b128 a[56:59], v240 offset:4096\n"
    "ds_read_b128 a[60:63], v241 offset:0\n"
    "ds_read_b128 a[160:163], v241 offset:2048\n"
    "ds_read_b128 a[164:167], v241 offset:4096\n"
    "s_waitcnt lgkmcnt(3)\n"
    "v_mfma_f32_16x16x32_bf16 v[140:143], a[216:219], a[48:51], 0\n"
    "s_add_u32 s87, s59, 8192\n"
    "ds_read_b128 a[168:171], v240 offset:32768\n"
    "v_mfma_f32_16x16x32_bf16 v[144:147], a[220:223], a[48:51], 0\n"
    "s_add_u32 s88, s59, 16384\n"
    "ds_read_b128 a[172:175], v240 offset:34816\n"
    "v_mfma_f32_16x16x32_bf16 v[148:151], a[224:227], a[48:51], 0\n"
    "s_add_u32 s89, s59, 24576\n"
    "ds_read_b128 a[176:179], v240 offset:36864\n"
    "v_mfma_f32_16x16x32_bf16 v[152:155], a[228:231], a[48:51], 0\n"
    "ds_read_b128 a[180:183], v241 offset:32768\n"
    "v_mfma_f32_16x16x32_bf16 v[156:159], a[216:219], a[52:55], 0\n"
    "ds_read_b128 a[184:187], v241 offset:34816\n"
    "v_mfma_f32_16x16x32_bf16 v[160:163], a[220:223], a[52:55], 0\n"
    "ds_read_b128 a[188:191], v241 offset:36864\n"
    "v_mfma_f32_16x16x32_bf16 v[164:167], a[224:227], a[52:55], 0\n"
    "ds_read_b128 a[48:51], v242 offset:0\n"
    "v_mfma_f32_16x16x32_bf16 v[168:171], a[228:231], a[52:55], 0\n"
    "s_add_u32 s62, s58, 12288\n"
    "buffer_load_dword v253, v238, s[8:11], s48 offen offset:0 sc1\n"
    "v_mfma_f32_16x16x32_bf16 v[180:183], a[216:219], a[56:59], 0\n"
    "v_mfma_f32_16x16x32_bf16 v[184:187], a[220:223], a[56:59], 0\n"
    "buffer_load_dwordx4 a[212:215], v250, s[8:11], s62 offen offset:0 sc1\n"
    "v_mfma_f32_16x16x32_bf16 v[188:191], a[224:227], a[56:59], 0\n"
    "ds_read_b128 a[52:55], v242 offset:2048\n"
    "v_mfma_f32_16x16x32_bf16 v[192:195], a[228:231], a[56:59], 0\n"
    "s_waitcnt lgkmcnt(8)\n"
    "v_mfma_f32_16x16x32_bf16 v[140:143], a[232:235], a[60:63], v[140:143]\n"
    "v_mfma_f32_16x16x32_bf16 v[144:147], a[64:67], a[60:63], v[144:147]\n"
    "buffer_load_dwordx4 v[36:39], v250, s[8:11], s62 offen offset:1024 sc1\n"
    "v_mfma_f32_16x16x32_bf16 v[148:151], a[68:71], a[60:63], v[148:151]\n"
    "ds_read_b128 a[56:59], v242 offset:4096\n"
    "v_mfma_f32_16x16x32_bf16 v[152:155], a[72:75], a[60:63], v[152:155]\n"
    "v_mfma_f32_16x16x32_bf16 v[156:159], a[232:235], a[160:163], v[156:159]\n"
    "v_mfma_f32_16x16x32_bf16 v[160:163], a[64:67], a[160:163], v[160:163]\n"
    "buffer_load_dwordx4 v[56:59], v250, s[8:11], s62 offen offset:2048 sc1\n"
    "v_mfma_f32_16x16x32_bf16 v[164:167], a[68:71], a[160:163], v[164:167]\n"
    "ds_read_b128 a[60:63], v243 offset:0\n"
    "v_mfma_f32_16x16x32_bf16 v[168:171], a[72:75], a[160:163], v[168:171]\n"
    "v_mfma_f32_16x16x32_bf16 v[180:183], a[232:235], a[164:167], v[180:183]\n"
    "v_mfma_f32_16x16x32_bf16 v[184:187], a[64:67], a[164:167], v[184:187]\n"
    "buffer_load_dwordx4 v[172:175], v250, s[8:11], s62 offen offset:3072 sc1\n"
    "v_mfma_f32_16x16x32_bf16 v[188:191], a[68:71], a[164:167], v[188:191]\n"
    "s_add_u32 s62, s58, 16384\n"
    "ds_read_b128 a[160:163], v243 offset:2048\n"
    "v_mfma_f32_16x16x32_bf16 v[192:195], a[72:75], a[164:167], v[192:195]\n"
    "s_waitcnt lgkmcnt(8)\n"
    "v_mfma_f32_16x16x32_bf16 v[140:143], a[76:79], a[168:171], v[140:143]\n"
    "v_mfma_f32_16x16x32_bf16 v[144:147], a[80:83], a[168:171], v[144:147]\n"
    "buffer_load_dwordx4 v[176:179], v250, s[8:11], s62 offen offset:0 sc1\n"
    "v_mfma_f32_16x16x32_bf16 v[148:151], a[84:87], a[168:171], v[148:151]\n"
    "ds_read_b128 a[164:167], v243 offset:4096\n"
    "v_mfma_f32_16x16x32_bf16 v[152:155], a[88:91], a[168:171], v[152:155]\n"
    "v_mfma_f32_16x16x32_bf16 v[156:159], a[76:79], a[172:175], v[156:159]\n"
    "v_mfma_f32_16x16x32_bf16 v[160:163], a[80:83], a[172:175], v[160:163]\n"
    "buffer_load_dwordx4 v[216:219], v250, s[8:11], s62 offen offset:1024 sc1\n"
    "v_mfma_f32_16x16x32_bf16 v[164:167], a[84:87], a[172:175], v[164:167]\n"
    "ds_read_b128 a[168:171], v242 offset:32768\n"
    "v_mfma_f32_16x16x32_bf16 v[168:171], a[88:91], a[172:175], v[168:171]\n"
    "v_mfma_f32_16x16x32_bf16 v[180:183], a[76:79], a[176:179], v[180:183]\n"
    "v_mfma_f32_16x16x32_bf16 v[184:187], a[80:83], a[176:179], v[184:187]\n"
    "buffer_load_dwordx4 v[68:71], v250, s[8:11], s62 offen offset:2048 sc1\n"
    "v_mfma_f32_16x16x32_bf16 v[188:191], a[84:87], a[176:179], v[188:191]\n"
    "ds_read_b128 a[172:175], v242 offset:34816\n"
    "v_mfma_f32_16x16x32_bf16 v[192:195], a[88:91], a[176:179], v[192:195]\n"
    "s_waitcnt lgkmcnt(8)\n"
    "v_mfma_f32_16x16x32_bf16 v[140:143], a[92:95], a[180:183], v[140:143]\n"
    "v_mfma_f32_16x16x32_bf16 v[144:147], a[96:99], a[180:183], v[144:147]\n"
    "buffer_load_dwordx4 v[88:91], v250, s[8:11], s62 offen offset:3072 sc1\n"
    "v_mfma_f32_16x16x32_bf16 v[148:151], a[100:103], a[180:183], v[148:151]\n"
    "ds_read_b128 a[176:179], v242 offset:36864\n"
    "v_mfma_f32_16x16x32_bf16 v[152:155], a[104:107], a[180:183], v[152:155]\n"
    "v_mfma_f32_16x16x32_bf16 v[156:159], a[92:95], a[184:187], v[156:159]\n"
    "v_mfma_f32_16x16x32_bf16 v[160:163], a[96:99], a[184:187], v[160:163]\n"
    "v_mfma_f32_16x16x32_bf16 v[164:167], a[100:103], a[184:187], v[164:167]\n"
    "ds_read_b128 a[180:183], v243 offset:32768\n"
    "v_mfma_f32_16x16x32_bf16 v[168:171], a[104:107], a[184:187], v[168:171]\n"
    "v_mfma_f32_16x16x32_bf16 v[180:183], a[92:95], a[188:191], v[180:183]\n"
    "s_waitcnt vmcnt(8)\n"
    "v_subrev_u32_e32 v253, s39, v253\n"
    "v_subrev_u32_e32 v253, 1, v253\n"
    "v_mfma_f32_16x16x32_bf16 v[184:187], a[96:99], a[188:191], v[184:187]\n"
    "v_cmp_le_u32_e32 vcc, 1, v253\n"
    "v_mfma_f32_16x16x32_bf16 v[188:191], a[100:103], a[188:191], v[188:191]\n"
    "ds_read_b128 a[184:187], v243 offset:34816\n"
    "v_mfma_f32_16x16x32_bf16 v[192:195], a[104:107], a[188:191], v[192:195]\n"
    "s_waitcnt lgkmcnt(8)\n"
    "s_waitcnt vmcnt(9)\n"
    "buffer_store_dword v254, off, s[8:11], s90 offset:0 sc1\n"
    "v_mfma_f32_16x16x32_bf16 v[140:143], a[108:111], a[48:51], v[140:143]\n"
    "s_cbranch_vccnz .Lat_k\n"
    ".Lat_kr:\n"
    "s_cmp_lg_u32 s56, 0\n"
    "s_cbranch_scc1 .Lat_kx\n"
    ".Lat_kxr:\n"
    "v_mfma_f32_16x16x32_bf16 v[144:147], a[112:115], a[48:51], v[144:147]\n"
    "buffer_load_dwordx4 a[24:27], v250, s[8:11], s87 offen offset:2048 sc1\n"
    "v_mfma_f32_16x16x32_bf16 v[148:151], a[116:119], a[48:51], v[148:151]\n"
    "ds_read_b128 a[188:191], v243 offset:36864\n"
    "v_mfma_f32_16x16x32_bf16 v[152:155], a[120:123], a[48:51], v[152:155]\n"
    "v_mfma_f32_16x16x32_bf16 v[156:159], a[108:111], a[52:55], v[156:159]\n"
    "s_waitcnt vmcnt(22)\n"
    "buffer_load_dwordx4 a[28:31], v250, s[8:11], s88 offen offset:2048 sc1\n"
    "v_mfma_f32_16x16x32_bf16 v[160:163], a[112:115], a[52:55], v[160:163]\n"
    "v_mfma_f32_16x16x32_bf16 v[164:167], a[116:119], a[52:55], v[164:167]\n"
    "v_mfma_f32_16x16x32_bf16 v[168:171], a[120:123], a[52:55], v[168:171]\n"
    "buffer_load_dwordx4 a[32:35], v250, s[8:11], s89 offen offset:2048 sc1\n"
    "v_mfma_f32_16x16x32_bf16 v[180:183], a[108:111], a[56:59], v[180:183]\n"
    "v_mfma_f32_16x16x32_bf16 v[184:187], a[112:115], a[56:59], v[184:187]\n"
    "ds_read_b128 a[216:219], v252 offset:0\n"
    "v_mfma_f32_16x16x32_bf16 v[188:191], a[116:119], a[56:59], v[188:191]\n"
    "buffer_load_dwordx4 a[36:39], v250, s[8:11], s87 offen offset:3072 sc1\n"
    "v_mfma_f32_16x16x32_bf16 v[192:195], a[120:123], a[56:59], v[192:195]\n"
    "s_waitcnt lgkmcnt(7)\n"
    "v_mfma_f32_16x16x32_bf16 v[140:143], a[124:127], a[60:63], v[140:143]\n"
    "s_mul_i32 s45, s34, 101376\n"
    "v_mfma_f32_16x16x32_bf16 v[144:147], a[128:131], a[60:63], v[144:147]\n"
    "s_add_u32 s45, s45, 16551936\n"
    "buffer_load_dwordx4 a[40:43], v250, s[8:11], s88 offen offset:3072 sc1\n"
    "v_mfma_f32_16x16x32_bf16 v[148:151], a[132:135], a[60:63], v[148:151]\n"
    "s_lshl_b32 s47, %2, 11\n"
    "v_mfma_f32_16x16x32_bf16 v[152:155], a[136:139], a[60:63], v[152:155]\n"
    "s_add_u32 s54, s47, s45\n"
    "ds_read_b128 a[220:223], v255 offset:0\n"
    "v_mfma_f32_16x16x32_bf16 v[156:159], a[124:127], a[160:163], v[156:159]\n"
    "s_add_u32 s45, s45, 32768\n"
    "buffer_load_dwordx4 a[44:47], v250, s[8:11], s89 offen offset:3072 sc1\n"
    "v_mfma_f32_16x16x32_bf16 v[160:163], a[128:131], a[160:163], v[160:163]\n"
    "s_cmp_lt_u32 s32, 2\n"
    "s_cselect_b32 s54, s54, 2.0\n"
    "v_mfma_f32_16x16x32_bf16 v[164:167], a[132:135], a[160:163], v[164:167]\n"
    "s_add_u32 s40, s35, s46\n"
    "v_mfma_f32_16x16x32_bf16 v[168:171], a[136:139], a[160:163], v[168:171]\n"
    "s_add_u32 s41, s36, s46\n"
    "buffer_load_dword v212, v248, s[8:11], s60 offen offset:192 sc1\n"
    "v_mfma_f32_16x16x32_bf16 v[180:183], a[124:127], a[164:167], v[180:183]\n"
    "s_add_u32 s28, s37, s46\n"
    "v_mfma_f32_16x16x32_bf16 v[184:187], a[128:131], a[164:167], v[184:187]\n"
    "ds_read_b128 a[224:227], v252 offset:2048\n"
    "v_mfma_f32_16x16x32_bf16 v[188:191], a[132:135], a[164:167], v[188:191]\n"
    "s_add_u32 s62, s58, 20480\n"
    "v_mfma_f32_16x16x32_bf16 v[192:195], a[136:139], a[164:167], v[192:195]\n"
    "s_waitcnt lgkmcnt(6)\n"
    "buffer_load_dwordx4 v[72:75], v250, s[8:11], s62 offen offset:0 sc1\n"
    "v_mfma_f32_16x16x32_bf16 v[140:143], a[140:143], a[168:171], v[140:143]\n"
    "v_mfma_f32_16x16x32_bf16 v[144:147], a[144:147], a[168:171], v[144:147]\n"
    "v_mfma_f32_16x16x32_bf16 v[148:151], a[148:151], a[168:171], v[148:151]\n"
    "v_mfma_f32_16x16x32_bf16 v[152:155], a[152:155], a[168:171], v[152:155]\n"
    "buffer_load_dwordx4 v[92:95], v250, s[8:11], s62 offen offset:1024 sc1\n"
    "v_mfma_f32_16x16x32_bf16 v[156:159], a[140:143], a[172:175], v[156:159]\n"
    "ds_read_b128 a[228:231], v255 offset:2048\n"
    "v_mfma_f32_16x16x32_bf16 v[160:163], a[144:147], a[172:175], v[160:163]\n"
    "v_mfma_f32_16x16x32_bf16 v[164:167], a[148:151], a[172:175], v[164:167]\n"
    "v_mfma_f32_16x16x32_bf16 v[168:171], a[152:155], a[172:175], v[168:171]\n"
    "buffer_load_dwordx4 v[76:79], v250, s[8:11], s62 offen offset:2048 sc1\n"
    "v_mfma_f32_16x16x32_bf16 v[180:183], a[140:143], a[176:179], v[180:183]\n"
    "v_mfma_f32_16x16x32_bf16 v[184:187], a[144:147], a[176:179], v[184:187]\n"
    "ds_read_b128 a[232:235], v252 offset:4096\n"
    "v_mfma_f32_16x16x32_bf16 v[188:191], a[148:151], a[176:179], v[188:191]\n"
    "v_mfma_f32_16x16x32_bf16 v[192:195], a[152:155], a[176:179], v[192:195]\n"
    "s_waitcnt lgkmcnt(5)\n"
    "buffer_load_dwordx4 v[96:99], v250, s[8:11], s62 offen offset:3072 sc1\n"
    "v_mfma_f32_16x16x32_bf16 v[140:143], a[156:159], a[180:183], v[140:143]\n"
    "s_add_u32 s62, s58, 24576\n"
    "v_mfma_f32_16x16x32_bf16 v[144:147], a[192:195], a[180:183], v[144:147]\n"
    "v_mfma_f32_16x16x32_bf16 v[148:151], a[196:199], a[180:183], v[148:151]\n"
    "ds_read_b128 a[48:51], v255 offset:4096\n"
    "v_mfma_f32_16x16x32_bf16 v[152:155], a[200:203], a[180:183], v[152:155]\n"
    "buffer_load_dwordx4 v[108:111], v250, s[8:11], s62 offen offset:0 sc1\n"
    "v_mfma_f32_16x16x32_bf16 v[156:159], a[156:159], a[184:187], v[156:159]\n"
    "v_mul_f32_e32 v140, v140, v228\n"
    "v_mfma_f32_16x16x32_bf16 v[160:163], a[192:195], a[184:187], v[160:163]\n"
    "v_mul_f32_e32 v141, v141, v228\n"
    "v_mfma_f32_16x16x32_bf16 v[164:167], a[196:199], a[184:187], v[164:167]\n"
    "v_mul_f32_e32 v142, v142, v228\n"
    "v_mfma_f32_16x16x32_bf16 v[168:171], a[200:203], a[184:187], v[168:171]\n"
    "buffer_load_dwordx4 v[128:131], v250, s[8:11], s62 offen offset:1024 sc1\n"
    "v_mfma_f32_16x16x32_bf16 v[180:183], a[156:159], a[188:191], v[180:183]\n"
    "v_mul_f32_e32 v143, v143, v228\n"
    "v_mul_f32_e32 v144, v144, v228\n"
    "v_mfma_f32_16x16x32_bf16 v[184:187], a[192:195], a[188:191], v[184:187]\n"
    "v_mul_f32_e32 v145, v145, v228\n"
    "v_mul_f32_e32 v156, v156, v229\n"
    "v_mfma_f32_16x16x32_bf16 v[188:191], a[196:199], a[188:191], v[188:191]\n"
    "v_mul_f32_e32 v146, v146, v228\n"
    "v_mul_f32_e32 v157, v157, v229\n"
    "v_mfma_f32_16x16x32_bf16 v[192:195], a[200:203], a[188:191], v[192:195]\n"
    "s_waitcnt vmcnt(6)\n"
    "buffer_load_dwordx4 v[112:115], v250, s[8:11], s62 offen offset:2048 sc1\n"
    "v_mfma_f32_16x16x32_bf16 a[236:239], v[234:237], a[0:3], a[236:239]\n"
    "v_mul_f32_e32 v147, v147, v228\n"
    "v_mul_f32_e32 v148, v148, v228\n"
    "v_mfma_f32_16x16x32_bf16 v[0:3], v[232:235], a[0:3], v[0:3]\n"
    "v_mul_f32_e32 v149, v149, v228\n"
    "v_mul_f32_e32 v158, v158, v229\n"
    "v_mfma_f32_16x16x32_bf16 a[236:239], v[234:237], a[4:7], a[236:239]\n"
    "v_mul_f32_e32 v150, v150, v228\n"
    "v_mul_f32_e32 v151, v151, v228\n"
    "v_mfma_f32_16x16x32_bf16 v[0:3], v[232:235], a[4:7], v[0:3]\n"
    "v_mul_f32_e32 v152, v152, v228\n"
    "v_mul_f32_e32 v153, v153, v228\n"
    "v_mfma_f32_16x16x32_bf16 a[236:239], v[234:237], a[8:11], a[236:239]\n"
    "buffer_load_dword v253, v226, s[8:11], s61 offen offset:0 sc1\n"
    "v_mfma_f32_16x16x32_bf16 v[0:3], v[232:235], a[8:11], v[0:3]\n"
    "v_mul_f32_e32 v154, v154, v228\n"
    "v_mul_f32_e32 v155, v155, v228\n"
    "v_mfma_f32_16x16x32_bf16 v[20:23], v[234:237], a[12:15], v[20:23]\n"
    "buffer_load_dwordx4 v[132:135], v250, s[8:11], s62 offen offset:3072 sc1\n"
    "v_cmp_le_u32_e32 vcc, 0x300, v250\n"
    "v_mfma_f32_16x16x32_bf16 v[40:43], v[232:235], a[12:15], v[40:43]\n"
    "s_add_u32 s62, s58, 28672\n"
    "v_mul_f32_e32 v159, v159, v229\n"
    "v_mul_f32_e32 v160, v160, v229\n"
    "v_mfma_f32_16x16x32_bf16 v[20:23], v[234:237], a[16:19], v[20:23]\n"
    "v_mul_f32_e32 v161, v161, v229\n"
    "v_mul_f32_e32 v162, v162, v229\n"
    "v_accvgpr_read_b32 v64, a236\n"
    "v_mfma_f32_16x16x32_bf16 v[40:43], v[232:235], a[16:19], v[40:43]\n"
    "v_mul_f32_e32 v163, v163, v229\n"
    "v_mul_f32_e32 v164, v164, v229\n"
    "v_accvgpr_read_b32 v65, a237\n"
    "v_mfma_f32_16x16x32_bf16 v[20:23], v[234:237], a[20:23], v[20:23]\n"
    "buffer_load_dwordx4 v[220:223], v250, s[8:11], s62 offen offset:0 sc1\n"
    "v_accvgpr_read_b32 v66, a238\n"
    "v_mfma_f32_16x16x32_bf16 v[40:43], v[232:235], a[20:23], v[40:43]\n"
    "v_mul_f32_e32 v165, v165, v229\n"
    "v_mul_f32_e32 v166, v166, v229\n"
    "v_accvgpr_read_b32 v67, a239\n"
    "v_mfma_f32_16x16x32_bf16 v[60:63], v[234:237], a[24:27], v[60:63]\n"
    "v_mul_f32_e32 v167, v167, v229\n"
    "v_mul_f32_e32 v180, v180, v230\n"
    "v_cvt_pk_bf16_f32 v64, v64, v65\n"
    "v_mfma_f32_16x16x32_bf16 v[80:83], v[232:235], a[24:27], v[80:83]\n"
    "v_mul_f32_e32 v168, v168, v229\n"
    "v_mul_f32_e32 v181, v181, v230\n"
    "v_cvt_pk_bf16_f32 v65, v66, v67\n"
    "v_mfma_f32_16x16x32_bf16 v[60:63], v[234:237], a[28:31], v[60:63]\n"
    "buffer_load_dwordx4 v[116:119], v250, s[8:11], s62 offen offset:1024 sc1\n"
    "v_cvt_pk_bf16_f32 v66, v0, v1\n"
    "v_mfma_f32_16x16x32_bf16 v[80:83], v[232:235], a[28:31], v[80:83]\n"
    "v_mul_f32_e32 v169, v169, v229\n"
    "v_mul_f32_e32 v182, v182, v230\n"
    "v_mfma_f32_16x16x32_bf16 v[60:63], v[234:237], a[32:35], v[60:63]\n"
    "v_mul_f32_e32 v170, v170, v229\n"
    "v_mul_f32_e32 v183, v183, v230\n"
    "v_mfma_f32_16x16x32_bf16 v[80:83], v[232:235], a[32:35], v[80:83]\n"
    "v_mul_f32_e32 v171, v171, v229\n"
    "v_mul_f32_e32 v184, v184, v230\n"
    "v_cndmask_b32_e32 v227, v212, v227, vcc\n"
    "v_mfma_f32_16x16x32_bf16 v[100:103], v[234:237], a[36:39], v[100:103]\n"
    "buffer_load_dwordx4 v[136:139], v250, s[8:11], s62 offen offset:2048 sc1\n"
    "v_mfma_f32_16x16x32_bf16 v[120:123], v[232:235], a[36:39], v[120:123]\n"
    "v_mul_f32_e32 v185, v185, v230\n"
    "v_cvt_pk_bf16_f32 v67, v2, v3\n"
    "v_mfma_f32_16x16x32_bf16 v[100:103], v[234:237], a[40:43], v[100:103]\n"
    "ds_write_b128 v244, v[64:67] offset:0\n"
    "v_mul_f32_e32 v186, v186, v230\n"
    "v_mfma_f32_16x16x32_bf16 v[120:123], v[232:235], a[40:43], v[120:123]\n"
    "v_mul_f32_e32 v187, v187, v230\n"
    "v_cvt_pk_bf16_f32 v20, v20, v21\n"
    "v_mfma_f32_16x16x32_bf16 v[100:103], v[234:237], a[44:47], v[100:103]\n"
    "buffer_load_dwordx4 a[240:243], v250, s[8:11], s62 offen offset:3072 sc1\n"
    "v_mfma_f32_16x16x32_bf16 v[120:123], v[232:235], a[44:47], v[120:123]\n"
    "s_waitcnt lgkmcnt(1)\n"
    "v_mul_f32_e32 v188, v188, v230\n"
    "v_cvt_pk_bf16_f32 v21, v22, v23\n"
    "v_mfma_f32_16x16x32_bf16 v[140:143], v[234:237], a[216:219], v[140:143]\n"
    "v_mul_f32_e32 v189, v189, v230\n"
    "v_cvt_pk_bf16_f32 v22, v40, v41\n"
    "v_mfma_f32_16x16x32_bf16 v[144:147], v[232:235], a[216:219], v[144:147]\n"
    "v_mul_f32_e32 v190, v190, v230\n"
    "v_cvt_pk_bf16_f32 v23, v42, v43\n"
    "v_mfma_f32_16x16x32_bf16 v[148:151], v[234:237], a[220:223], v[148:151]\n"
    "ds_write_b128 v244, v[20:23] offset:1024\n"
    "v_mul_f32_e32 v191, v191, v230\n"
    "v_mfma_f32_16x16x32_bf16 v[152:155], v[232:235], a[220:223], v[152:155]\n"
    "v_mul_f32_e32 v192, v192, v230\n"
    "v_cvt_pk_bf16_f32 v60, v60, v61\n"
    "v_mfma_f32_16x16x32_bf16 v[156:159], v[234:237], a[224:227], v[156:159]\n"
    "ds_write_b32 v245, v227 offset:0\n"
    "v_mul_f32_e32 v193, v193, v230\n"
    "v_cvt_pk_bf16_f32 v61, v62, v63\n"
    "v_mfma_f32_16x16x32_bf16 v[160:163], v[232:235], a[224:227], v[160:163]\n"
    "v_mul_f32_e32 v194, v194, v230\n"
    "v_cvt_pk_bf16_f32 v62, v80, v81\n"
    "v_mfma_f32_16x16x32_bf16 v[164:167], v[234:237], a[228:231], v[164:167]\n"
    "v_mul_f32_e32 v195, v195, v230\n"
    "v_cvt_pk_bf16_f32 v63, v82, v83\n"
    "v_mfma_f32_16x16x32_bf16 v[168:171], v[232:235], a[228:231], v[168:171]\n"
    "ds_write_b128 v244, v[60:63] offset:2048\n"
    "v_mfma_f32_16x16x32_bf16 v[180:183], v[234:237], a[232:235], v[180:183]\n"
    "v_cvt_pk_bf16_f32 v100, v100, v101\n"
    "v_cvt_pk_bf16_f32 v101, v102, v103\n"
    "v_cvt_pk_bf16_f32 v102, v120, v121\n"
    "v_cvt_pk_bf16_f32 v103, v122, v123\n"
    "v_mfma_f32_16x16x32_bf16 v[184:187], v[232:235], a[232:235], v[184:187]\n"
    "ds_write_b128 v244, v[100:103] offset:3072\n"
    "v_mfma_f32_16x16x32_bf16 v[188:191], v[234:237], a[48:51], v[188:191]\n"
    "v_mfma_f32_16x16x32_bf16 v[192:195], v[232:235], a[48:51], v[192:195]\n"
    "s_mov_b32 s53, 0x400000\n"
    "v_and_b32_e32 v225, 0x60ff, v245\n"
    "v_and_b32_e32 v245, 0x603f, v245\n"
    "s_waitcnt vmcnt(5)\n"
    "v_subrev_u32_e32 v253, s39, v253\n"
    "v_cmp_lt_u32_e32 vcc, 1, v253\n"
    "s_nop 1\n"
    "v_add_u32_e32 v244, s47, v250\n"
    "s_cbranch_vccnz .Lat_ppoll\n"
    ".Lat_pok:\n"
    "v_cmp_eq_u32_e32 vcc, 1, v253\n"
    "buffer_load_dwordx4 a[128:131], v250, s[8:11], s54 offen offset:0 sc1\n"
    "buffer_load_dwordx4 a[132:135], v250, s[8:11], s54 offen offset:1024 sc1\n"
    "s_add_u32 s54, s54, 8192\n"
    "buffer_load_dwordx4 a[136:139], v250, s[8:11], s54 offen offset:0 sc1\n"
    "buffer_load_dwordx4 a[140:143], v250, s[8:11], s54 offen offset:1024 sc1\n"
    "s_cbranch_vccnz .Lat_redo\n"
    "s_waitcnt lgkmcnt(0)\n"
    "s_barrier\n"
    "ds_read_b128 a[64:67], v250 offset:0\n"
    "ds_read_b128 a[68:71], v250 offset:1024\n"
    "ds_read_b128 a[72:75], v250 offset:2048\n"
    "ds_read_b128 a[76:79], v250 offset:3072\n"
    "ds_read_b128 a[80:83], v250 offset:4096\n"
    "ds_read_b128 a[84:87], v250 offset:5120\n"
    "ds_read_b128 a[88:91], v250 offset:6144\n"
    "ds_read_b128 a[92:95], v250 offset:7168\n"
    "s_add_u32 s54, s54, 8192\n"
    "buffer_load_dwordx4 a[144:147], v250, s[8:11], s54 offen offset:0 sc1\n"
    "buffer_load_dwordx4 a[148:151], v250, s[8:11], s54 offen offset:1024 sc1\n"
    "s_add_u32 s54, s54, 8192\n"
    "buffer_load_dwordx4 a[152:155], v250, s[8:11], s54 offen offset:0 sc1\n"
    "buffer_load_dwordx4 a[156:159], v250, s[8:11], s54 offen offset:1024 sc1\n"
    "buffer_load_dword v227, v248, s[8:11], s45 offen offset:1088 sc1\n"
    "s_waitcnt lgkmcnt(6)\n"
    "v_mfma_f32_16x16x32_bf16 v[196:199], a[244:247], a[64:67], 0\n"
    "v_mfma_f32_16x16x32_bf16 v[200:203], v[8:11], a[64:67], 0\n"
    "ds_read_b128 a[96:99], v250 offset:8192\n"
    "v_mfma_f32_16x16x32_bf16 v[204:207], a[248:251], a[64:67], 0\n"
    "ds_read_b128 a[100:103], v250 offset:9216\n"
    "v_mfma_f32_16x16x32_bf16 v[4:7], v[12:15], a[64:67], 0\n"
    "ds_read_b128 a[104:107], v250 offset:10240\n"
    "v_mfma_f32_16x16x32_bf16 v[196:199], a[252:255], a[68:71], v[196:199]\n"
    "v_mfma_f32_16x16x32_bf16 v[200:203], v[16:19], a[68:71], v[200:203]\n"
    "ds_read_b128 a[108:111], v250 offset:11264\n"
    "v_mfma_f32_16x16x32_bf16 v[204:207], v[28:31], a[68:71], v[204:207]\n"
    "ds_read_b128 a[112:115], v250 offset:12288\n"
    "v_mfma_f32_16x16x32_bf16 v[4:7], v[48:51], a[68:71], v[4:7]\n"
    "s_waitcnt lgkmcnt(9)\n"
    "ds_read_b128 a[116:119], v250 offset:13312\n"
    "v_mfma_f32_16x16x32_bf16 v[196:199], v[32:35], a[72:75], v[196:199]\n"
    "v_mfma_f32_16x16x32_bf16 v[200:203], v[52:55], a[72:75], v[200:203]\n"
    "ds_read_b128 a[120:123], v250 offset:14336\n"
    "v_mfma_f32_16x16x32_bf16 v[204:207], a[204:207], a[72:75], v[204:207]\n"
    "ds_read_b128 a[124:127], v250 offset:15360\n"
    "v_mfma_f32_16x16x32_bf16 v[4:7], a[208:211], a[72:75], v[4:7]\n"
    "ds_read2_b32 v[212:213], v245 offset0:0 offset1:16\n"
    "v_mfma_f32_16x16x32_bf16 v[196:199], a[212:215], a[76:79], v[196:199]\n"
    "v_mfma_f32_16x16x32_bf16 v[200:203], v[36:39], a[76:79], v[200:203]\n"
    "ds_read2_b32 v[214:215], v245 offset0:32 offset1:48\n"
    "v_mfma_f32_16x16x32_bf16 v[204:207], v[56:59], a[76:79], v[204:207]\n"
    "s_waitcnt vmcnt(9)\n"
    "ds_read2_b32 v[208:209], v245 offset0:64 offset1:80\n"
    "v_mfma_f32_16x16x32_bf16 v[4:7], v[172:175], a[76:79], v[4:7]\n"
    "s_waitcnt lgkmcnt(13)\n"
    "ds_read2_b32 v[210:211], v245 offset0:96 offset1:112\n"
    "v_mfma_f32_16x16x32_bf16 v[196:199], v[176:179], a[80:83], v[196:199]\n"
    "v_mfma_f32_16x16x32_bf16 v[200:203], v[216:219], a[80:83], v[200:203]\n"
    "v_mfma_f32_16x16x32_bf16 v[204:207], v[68:71], a[80:83], v[204:207]\n"
    "v_mfma_f32_16x16x32_bf16 v[4:7], v[88:91], a[80:83], v[4:7]\n"
    "v_mfma_f32_16x16x32_bf16 v[196:199], v[72:75], a[84:87], v[196:199]\n"
    "v_mfma_f32_16x16x32_bf16 v[200:203], v[92:95], a[84:87], v[200:203]\n"
    "v_mfma_f32_16x16x32_bf16 v[204:207], v[76:79], a[84:87], v[204:207]\n"
    "v_mfma_f32_16x16x32_bf16 v[4:7], v[96:99], a[84:87], v[4:7]\n"
    "s_waitcnt lgkmcnt(0)\n"
    "v_mfma_f32_16x16x32_bf16 v[196:199], v[108:111], a[88:91], v[196:199]\n"
    "v_add_f32_e32 v212, v212, v213\n"
    "v_mfma_f32_16x16x32_bf16 v[200:203], v[128:131], a[88:91], v[200:203]\n"
    "v_add_f32_e32 v214, v214, v215\n"
    "v_mfma_f32_16x16x32_bf16 v[204:207], v[112:115], a[88:91], v[204:207]\n"
    "v_add_f32_e32 v212, v212, v214\n"
    "v_mfma_f32_16x16x32_bf16 v[4:7], v[132:135], a[88:91], v[4:7]\n"
    "v_rcp_f32_e32 v212, v212\n"
    "v_mfma_f32_16x16x32_bf16 v[196:199], v[220:223], a[92:95], v[196:199]\n"
    "v_add_f32_e32 v208, v208, v209\n"
    "v_mfma_f32_16x16x32_bf16 v[200:203], v[116:119], a[92:95], v[200:203]\n"
    "v_add_f32_e32 v210, v210, v211\n"
    "v_mfma_f32_16x16x32_bf16 v[204:207], v[136:139], a[92:95], v[204:207]\n"
    "v_add_f32_e32 v208, v208, v210\n"
    "v_mfma_f32_16x16x32_bf16 v[4:7], a[240:243], a[92:95], v[4:7]\n"
    "v_rcp_f32_e32 v208, v208\n"
    "v_mfma_f32_16x16x32_bf16 v[24:27], a[244:247], a[96:99], 0\n"
    "v_mfma_f32_16x16x32_bf16 v[44:47], v[8:11], a[96:99], 0\n"
    "v_fma_f32 v196, v196, v212, v140\n"
    "v_mfma_f32_16x16x32_bf16 v[0:3], a[248:251], a[96:99], 0\n"
    "v_fma_f32 v197, v197, v212, v141\n"
    "v_mfma_f32_16x16x32_bf16 v[20:23], v[12:15], a[96:99], 0\n"
    "v_fma_f32 v198, v198, v212, v142\n"
    "v_mfma_f32_16x16x32_bf16 v[24:27], a[252:255], a[100:103], v[24:27]\n"
    "v_fma_f32 v199, v199, v212, v143\n"
    "v_mfma_f32_16x16x32_bf16 v[44:47], v[16:19], a[100:103], v[44:47]\n"
    "v_fma_f32 v200, v200, v212, v144\n"
    "v_mfma_f32_16x16x32_bf16 v[0:3], v[28:31], a[100:103], v[0:3]\n"
    "v_fma_f32 v201, v201, v212, v145\n"
    "v_fma_f32 v202, v202, v212, v146\n"
    "v_mfma_f32_16x16x32_bf16 v[20:23], v[48:51], a[100:103], v[20:23]\n"
    "v_fma_f32 v203, v203, v212, v147\n"
    "v_mfma_f32_16x16x32_bf16 v[24:27], v[32:35], a[104:107], v[24:27]\n"
    "v_cvt_pk_bf16_f32 v196, v196, v197\n"
    "v_mfma_f32_16x16x32_bf16 v[44:47], v[52:55], a[104:107], v[44:47]\n"
    "v_cvt_pk_bf16_f32 v197, v198, v199\n"
    "v_mfma_f32_16x16x32_bf16 v[0:3], a[204:207], a[104:107], v[0:3]\n"
    "v_cvt_pk_bf16_f32 v198, v200, v201\n"
    "v_mfma_f32_16x16x32_bf16 v[20:23], a[208:211], a[104:107], v[20:23]\n"
    "v_cvt_pk_bf16_f32 v199, v202, v203\n"
    "v_mfma_f32_16x16x32_bf16 v[24:27], a[212:215], a[108:111], v[24:27]\n"
    "buffer_store_dwordx4 v[196:199], v239, s[16:19], s40 offen offset:0 sc0 sc1\n"
    "v_mfma_f32_16x16x32_bf16 v[44:47], v[36:39], a[108:111], v[44:47]\n"
    "v_fma_f32 v204, v204, v212, v148\n"
    "v_fma_f32 v205, v205, v212, v149\n"
    "v_mfma_f32_16x16x32_bf16 v[0:3], v[56:59], a[108:111], v[0:3]\n"
    "v_fma_f32 v206, v206, v212, v150\n"
    "v_mfma_f32_16x16x32_bf16 v[20:23], v[172:175], a[108:111], v[20:23]\n"
    "s_waitcnt vmcnt(1)\n"
    "v_fma_f32 v207, v207, v212, v151\n"
    "v_mfma_f32_16x16x32_bf16 v[64:67], v[234:237], a[128:131], 0\n"
    "v_fma_f32 v4, v4, v212, v152\n"
    "v_mfma_f32_16x16x32_bf16 v[84:87], v[232:235], a[128:131], 0\n"
    "v_fma_f32 v5, v5, v212, v153\n"
    "v_fma_f32 v6, v6, v212, v154\n"
    "v_mfma_f32_16x16x32_bf16 v[104:107], v[234:237], a[132:135], 0\n"
    "ds_write_b32 v225, v227 offset:512\n"
    "v_mfma_f32_16x16x32_bf16 v[124:127], v[232:235], a[132:135], 0\n"
    "v_fma_f32 v7, v7, v212, v155\n"
    "v_cvt_pk_bf16_f32 v204, v204, v205\n"
    "v_mfma_f32_16x16x32_bf16 v[64:67], v[234:237], a[136:139], v[64:67]\n"
    "v_cvt_pk_bf16_f32 v205, v206, v207\n"
    "v_mfma_f32_16x16x32_bf16 v[84:87], v[232:235], a[136:139], v[84:87]\n"
    "v_cvt_pk_bf16_f32 v206, v4, v5\n"
    "v_cvt_pk_bf16_f32 v207, v6, v7\n"
    "v_mfma_f32_16x16x32_bf16 v[104:107], v[234:237], a[140:143], v[104:107]\n"
    "buffer_store_dwordx4 v[204:207], v239, s[16:19], s40 offen offset:64 sc0 sc1\n"
    "v_mfma_f32_16x16x32_bf16 v[124:127], v[232:235], a[140:143], v[124:127]\n"
    "v_mfma_f32_16x16x32_bf16 v[64:67], v[234:237], a[144:147], v[64:67]\n"
    "v_mfma_f32_16x16x32_bf16 v[84:87], v[232:235], a[144:147], v[84:87]\n"
    "v_mfma_f32_16x16x32_bf16 v[104:107], v[234:237], a[148:151], v[104:107]\n"
    "v_mfma_f32_16x16x32_bf16 v[124:127], v[232:235], a[148:151], v[124:127]\n"
    "v_mfma_f32_16x16x32_bf16 v[64:67], v[234:237], a[152:155], v[64:67]\n"
    "v_mfma_f32_16x16x32_bf16 v[84:87], v[232:235], a[152:155], v[84:87]\n"
    "v_mfma_f32_16x16x32_bf16 v[104:107], v[234:237], a[156:159], v[104:107]\n"
    "v_mfma_f32_16x16x32_bf16 v[124:127], v[232:235], a[156:159], v[124:127]\n"
    "v_mfma_f32_16x16x32_bf16 v[24:27], v[176:179], a[112:115], v[24:27]\n"
    "v_mfma_f32_16x16x32_bf16 v[44:47], v[216:219], a[112:115], v[44:47]\n"
    "v_cvt_pk_bf16_f32 v64, v64, v65\n"
    "v_mfma_f32_16x16x32_bf16 v[0:3], v[68:71], a[112:115], v[0:3]\n"
    "v_cvt_pk_bf16_f32 v65, v66, v67\n"
    "v_mfma_f32_16x16x32_bf16 v[20:23], v[88:91], a[112:115], v[20:23]\n"
    "v_cvt_pk_bf16_f32 v66, v84, v85\n"
    "v_mfma_f32_16x16x32_bf16 v[24:27], v[72:75], a[116:119], v[24:27]\n"
    "v_cvt_pk_bf16_f32 v67, v86, v87\n"
    "v_mfma_f32_16x16x32_bf16 v[44:47], v[92:95], a[116:119], v[44:47]\n"
    "ds_write_b128 v244, v[64:67] offset:16384\n"
    "v_mfma_f32_16x16x32_bf16 v[0:3], v[76:79], a[116:119], v[0:3]\n"
    "v_cvt_pk_bf16_f32 v104, v104, v105\n"
    "v_cvt_pk_bf16_f32 v105, v106, v107\n"
    "v_mfma_f32_16x16x32_bf16 v[20:23], v[96:99], a[116:119], v[20:23]\n"
    "v_cvt_pk_bf16_f32 v106, v124, v125\n"
    "v_cvt_pk_bf16_f32 v107, v126, v127\n"
    "v_mfma_f32_16x16x32_bf16 v[24:27], v[108:111], a[120:123], v[24:27]\n"
    "ds_write_b128 v244, v[104:107] offset:17408\n"
    "v_mfma_f32_16x16x32_bf16 v[44:47], v[128:131], a[120:123], v[44:47]\n"
    "s_waitcnt lgkmcnt(0)\n"
    "s_barrier\n"
    "ds_read_b128 a[0:3], v250 offset:16384\n"
    "ds_read_b128 a[4:7], v250 offset:17408\n"
    "v_mfma_f32_16x16x32_bf16 v[0:3], v[112:115], a[120:123], v[0:3]\n"
    "ds_read_b128 a[8:11], v250 offset:18432\n"
    "v_mfma_f32_16x16x32_bf16 v[20:23], v[132:135], a[120:123], v[20:23]\n"
    "ds_read_b128 a[12:15], v250 offset:19456\n"
    "v_mfma_f32_16x16x32_bf16 v[24:27], v[220:223], a[124:127], v[24:27]\n"
    "ds_read_b128 a[16:19], v250 offset:20480\n"
    "v_mfma_f32_16x16x32_bf16 v[44:47], v[116:119], a[124:127], v[44:47]\n"
    "ds_read_b128 a[20:23], v250 offset:21504\n"
    "ds_read_b128 a[24:27], v250 offset:22528\n"
    "v_mfma_f32_16x16x32_bf16 v[0:3], v[136:139], a[124:127], v[0:3]\n"
    "ds_read_b128 a[28:31], v250 offset:23552\n"
    "v_mfma_f32_16x16x32_bf16 v[20:23], a[240:243], a[124:127], v[20:23]\n"
    "ds_read2_b32 v[212:213], v245 offset0:128 offset1:144\n"
    "ds_read2_b32 v[214:215], v245 offset0:160 offset1:176\n"
    "s_waitcnt lgkmcnt(8)\n"
    "v_mfma_f32_16x16x32_bf16 v[40:43], a[244:247], a[0:3], 0\n"
    "v_mfma_f32_16x16x32_bf16 v[60:63], v[8:11], a[0:3], 0\n"
    "v_mfma_f32_16x16x32_bf16 v[80:83], a[248:251], a[0:3], 0\n"
    "v_fma_f32 v24, v24, v208, v156\n"
    "v_fma_f32 v25, v25, v208, v157\n"
    "v_mfma_f32_16x16x32_bf16 v[100:103], v[12:15], a[0:3], 0\n"
    "v_fma_f32 v26, v26, v208, v158\n"
    "v_fma_f32 v27, v27, v208, v159\n"
    "v_mfma_f32_16x16x32_bf16 v[40:43], a[252:255], a[4:7], v[40:43]\n"
    "v_fma_f32 v44, v44, v208, v160\n"
    "v_fma_f32 v45, v45, v208, v161\n"
    "v_mfma_f32_16x16x32_bf16 v[60:63], v[16:19], a[4:7], v[60:63]\n"
    "v_fma_f32 v46, v46, v208, v162\n"
    "v_fma_f32 v47, v47, v208, v163\n"
    "v_mfma_f32_16x16x32_bf16 v[80:83], v[28:31], a[4:7], v[80:83]\n"
    "v_cvt_pk_bf16_f32 v24, v24, v25\n"
    "v_cvt_pk_bf16_f32 v25, v26, v27\n"
    "v_mfma_f32_16x16x32_bf16 v[100:103], v[48:51], a[4:7], v[100:103]\n"
    "v_cvt_pk_bf16_f32 v26, v44, v45\n"
    "v_cvt_pk_bf16_f32 v27, v46, v47\n"
    "s_waitcnt lgkmcnt(0)\n"
    "v_mfma_f32_16x16x32_bf16 v[40:43], v[32:35], a[8:11], v[40:43]\n"
    "buffer_store_dwordx4 v[24:27], v239, s[16:19], s41 offen offset:0 sc0 sc1\n"
    "v_fma_f32 v0, v0, v208, v164\n"
    "v_mfma_f32_16x16x32_bf16 v[60:63], v[52:55], a[8:11], v[60:63]\n"
    "v_fma_f32 v1, v1, v208, v165\n"
    "v_fma_f32 v2, v2, v208, v166\n"
    "v_mfma_f32_16x16x32_bf16 v[80:83], a[204:207], a[8:11], v[80:83]\n"
    "v_fma_f32 v3, v3, v208, v167\n"
    "v_fma_f32 v20, v20, v208, v168\n"
    "v_mfma_f32_16x16x32_bf16 v[100:103], a[208:211], a[8:11], v[100:103]\n"
    "v_fma_f32 v21, v21, v208, v169\n"
    "v_fma_f32 v22, v22, v208, v170\n"
    "v_mfma_f32_16x16x32_bf16 v[40:43], a[212:215], a[12:15], v[40:43]\n"
    "v_fma_f32 v23, v23, v208, v171\n"
    "v_cvt_pk_bf16_f32 v0, v0, v1\n"
    "v_mfma_f32_16x16x32_bf16 v[60:63], v[36:39], a[12:15], v[60:63]\n"
    "v_cvt_pk_bf16_f32 v1, v2, v3\n"
    "v_cvt_pk_bf16_f32 v2, v20, v21\n"
    "v_mfma_f32_16x16x32_bf16 v[80:83], v[56:59], a[12:15], v[80:83]\n"
    "v_cvt_pk_bf16_f32 v3, v22, v23\n"
    "buffer_store_dwordx4 v[0:3], v239, s[16:19], s41 offen offset:64 sc0 sc1\n"
    "v_mfma_f32_16x16x32_bf16 v[100:103], v[172:175], a[12:15], v[100:103]\n"
    "v_add_f32_e32 v212, v212, v213\n"
    "v_add_f32_e32 v214, v214, v215\n"
    "v_mfma_f32_16x16x32_bf16 v[40:43], v[176:179], a[16:19], v[40:43]\n"
    "v_add_f32_e32 v212, v212, v214\n"
    "v_mfma_f32_16x16x32_bf16 v[60:63], v[216:219], a[16:19], v[60:63]\n"
    "v_rcp_f32_e32 v212, v212\n"
    "v_mfma_f32_16x16x32_bf16 v[80:83], v[68:71], a[16:19], v[80:83]\n"
    "s_or_b32 s45, s34, %2\n"
    "s_cselect_b32 s45, 2.0, 5999360\n"
    "v_mfma_f32_16x16x32_bf16 v[100:103], v[88:91], a[16:19], v[100:103]\n"
    "v_mov_b32_e32 v254, s39\n"
    "v_mfma_f32_16x16x32_bf16 v[40:43], v[72:75], a[20:23], v[40:43]\n"
    "v_mfma_f32_16x16x32_bf16 v[60:63], v[92:95], a[20:23], v[60:63]\n"
    "v_mfma_f32_16x16x32_bf16 v[80:83], v[76:79], a[20:23], v[80:83]\n"
    "v_mfma_f32_16x16x32_bf16 v[100:103], v[96:99], a[20:23], v[100:103]\n"
    "v_mfma_f32_16x16x32_bf16 v[40:43], v[108:111], a[24:27], v[40:43]\n"
    "v_mfma_f32_16x16x32_bf16 v[60:63], v[128:131], a[24:27], v[60:63]\n"
    "v_mfma_f32_16x16x32_bf16 v[80:83], v[112:115], a[24:27], v[80:83]\n"
    "v_mfma_f32_16x16x32_bf16 v[100:103], v[132:135], a[24:27], v[100:103]\n"
    "v_mfma_f32_16x16x32_bf16 v[40:43], v[220:223], a[28:31], v[40:43]\n"
    "v_mfma_f32_16x16x32_bf16 v[60:63], v[116:119], a[28:31], v[60:63]\n"
    "v_mfma_f32_16x16x32_bf16 v[80:83], v[136:139], a[28:31], v[80:83]\n"
    "v_mfma_f32_16x16x32_bf16 v[100:103], a[240:243], a[28:31], v[100:103]\n"
    "v_fma_f32 v40, v40, v212, v180\n"
    "v_fma_f32 v41, v41, v212, v181\n"
    "v_fma_f32 v42, v42, v212, v182\n"
    "v_fma_f32 v43, v43, v212, v183\n"
    "v_fma_f32 v60, v60, v212, v184\n"
    "v_fma_f32 v61, v61, v212, v185\n"
    "v_fma_f32 v62, v62, v212, v186\n"
    "v_fma_f32 v63, v63, v212, v187\n"
    "v_cvt_pk_bf16_f32 v40, v40, v41\n"
    "v_cvt_pk_bf16_f32 v41, v42, v43\n"
    "v_cvt_pk_bf16_f32 v42, v60, v61\n"
    "v_cvt_pk_bf16_f32 v43, v62, v63\n"
    "buffer_store_dwordx4 v[40:43], v239, s[16:19], s28 offen offset:0 sc0 sc1\n"
    "v_fma_f32 v80, v80, v212, v188\n"
    "v_fma_f32 v81, v81, v212, v189\n"
    "v_fma_f32 v82, v82, v212, v190\n"
    "v_fma_f32 v83, v83, v212, v191\n"
    "v_fma_f32 v100, v100, v212, v192\n"
    "v_fma_f32 v101, v101, v212, v193\n"
    "v_fma_f32 v102, v102, v212, v194\n"
    "v_fma_f32 v103, v103, v212, v195\n"
    "v_cvt_pk_bf16_f32 v80, v80, v81\n"
    "v_cvt_pk_bf16_f32 v81, v82, v83\n"
    "v_cvt_pk_bf16_f32 v82, v100, v101\n"
    "v_cvt_pk_bf16_f32 v83, v102, v103\n"
    "buffer_store_dwordx4 v[80:83], v239, s[16:19], s28 offen offset:64 sc0 sc1\n"
    "buffer_store_dword v254, off, s[8:11], s45 offset:0 sc1\n"
    "s_endpgm\n"
    ".Lat_g1:\n"
    "s_cmp_eq_u32 s85, 0\n"
    "s_cbranch_scc1 .Lat_loop\n"
    "s_cmp_eq_u32 s81, s39\n"
    "s_cbranch_scc1 .Lat_g1b\n"
    "s_mov_b32 s53, 0x400000\n"
    ".Lat_lp:\n"
    "s_sleep 1\n"
    "buffer_load_dword v253, off, s[8:11], s82 sc1\n"
    "s_waitcnt vmcnt(0)\n"
    "v_readfirstlane_b32 s81, v253\n"
    "s_sub_u32 s53, s53, 1\n"
    "s_cbranch_scc1 .Lat_lpx\n"
    "s_nop 3\n"
    "s_cmp_lg_u32 s81, s39\n"
    "s_cbranch_scc1 .Lat_lp\n"
    ".Lat_lpx:\n"
    "s_mov_b32 m0, s30\n"
    "s_nop 0\n"
    "buffer_load_dwordx4 v250, s[8:11], s83 offen offset:0 sc1 lds\n"
    "s_mov_b32 m0, s42\n"
    ".Lat_g1b:\n"
    "v_lshrrev_b32_e32 v228, 2, v250\n"
    "v_add_u32_e32 v254, s30, v228\n"
    "s_waitcnt vmcnt(0)\n"
    "ds_read_b32 v254, v254 offset:960\n"
    "v_lshrrev_b32_e32 v228, 9, v250\n"
    "v_lshl_add_u32 v251, v228, 2, s30\n"
    "v_and_b32_e32 v252, 0xf0, v250\n"
    "v_lshl_add_u32 v252, v228, 8, v252\n"
    "v_bfe_u32 v229, v250, 8, 1\n"
    "v_lshl_add_u32 v252, v229, 3, v252\n"
    "v_and_b32_e32 v239, 0x1f0, v250\n"
    "v_bfe_u32 v228, v250, 2, 4\n"
    "s_mov_b32 s88, 0xcedc\n"
    "v_bfe_u32 v238, s88, v228, 4\n"
    "v_mul_u32_u24_e32 v238, 0x1111, v238\n"
    "v_mov_b64_e32 v[224:225], 0\n"
    "v_mov_b64_e32 v[226:227], 0\n"
    "s_mov_b32 s85, 0\n"
    "s_mov_b32 s82, 2.0\n"
    "s_mov_b32 s83, 2.0\n"
    "s_mov_b32 s89, 4\n"
    "s_waitcnt lgkmcnt(0)\n"
    "v_readlane_b32 s88, v254, 0\n"
    "v_readlane_b32 s87, v254, 1\n"
    "v_readlane_b32 s84, v254, 2\n"
    "v_readlane_b32 s86, v254, 4\n"
    "s_nop 3\n"
    "s_min_u32 s88, s88, s44\n"
    "s_sub_u32 s80, s44, s88\n"
    "s_cmp_eq_u32 s88, 0\n"
    "s_cbranch_scc1 .Lat_g2\n"
    "s_branch .Lat_gloop\n"
    ".p2align 6\n"
    ".Lat_gloop:\n"
    "v_mfma_f32_16x16x32_bf16 v[172:175], a[160:163], a[128:131], v[220:223]\n"
    "ds_write_addtid_b32 v183 offset:768\n"
    "v_mfma_f32_16x16x32_bf16 v[172:175], a[164:167], a[132:135], v[172:175]\n"
    "v_exp_f32_e32 v210, v210\n"
    "v_mfma_f32_16x16x32_bf16 v[164:167], a[164:167], a[100:103], v[220:223]\n"
    "ds_write_addtid_b32 v190 offset:1536\n"
    "v_mfma_f32_16x16x32_bf16 v[164:167], a[160:163], a[96:99], v[164:167]\n"
    "ds_read_b128 a[184:187], v242 offset:4096\n"
    "v_mfma_f32_16x16x32_bf16 v[156:159], a[160:163], a[64:67], v[220:223]\n"
    "v_exp_f32_e32 v211, v211\n"
    "v_mfma_f32_16x16x32_bf16 v[156:159], a[164:167], a[68:71], v[156:159]\n"
    "ds_write_addtid_b32 v191 offset:1792\n"
    "v_cvt_pk_bf16_f32 v206, v208, v209\n"
    "v_mfma_f32_16x16x32_bf16 v[148:151], a[164:167], a[36:39], v[220:223]\n"
    "v_exp_f32_e32 v216, v216\n"
    "v_mfma_f32_16x16x32_bf16 v[148:151], a[160:163], a[32:35], v[148:151]\n"
    "v_exp_f32_e32 v217, v217\n"
    "v_mfma_f32_16x16x32_bf16 v[140:143], a[160:163], a[0:3], v[220:223]\n"
    "ds_write_addtid_b32 v198 offset:2560\n"
    "v_cvt_pk_bf16_f32 v207, v210, v211\n"
    "v_mfma_f32_16x16x32_bf16 v[140:143], a[164:167], a[4:7], v[140:143]\n"
    "s_waitcnt vmcnt(20)\n"
    "v_exp_f32_e32 v218, v218\n"
    "v_smfmac_f32_16x16x64_bf16 v[224:227], a[232:235], a[192:199], v238\n"
    "s_waitcnt lgkmcnt(10)\n"
    "ds_read_b128 a[188:191], v243 offset:4096\n"
    "v_mfma_f32_16x16x32_bf16 v[140:143], a[168:171], a[8:11], v[140:143]\n"
    "s_bitcmp1_b32 s86, 0\n"
    "ds_read_b128 a[160:163], v240 offset:2048\n"
    "v_mfma_f32_16x16x32_bf16 v[140:143], a[172:175], a[12:15], v[140:143]\n"
    "s_cselect_b64 exec, -1, 0\n"
    "v_cvt_pk_bf16_f32 v236, v224, v225\n"
    "v_cvt_pk_bf16_f32 v237, v226, v227\n"
    "v_mfma_f32_16x16x32_bf16 v[148:151], a[172:175], a[44:47], v[148:151]\n"
    "s_cselect_b32 s88, 512, 0\n"
    "v_mov_b64_e32 v[224:225], 0\n"
    "v_mov_b64_e32 v[226:227], 0\n"
    "v_mfma_f32_16x16x32_bf16 v[148:151], a[168:171], a[40:43], v[148:151]\n"
    "buffer_store_dwordx2 v[236:237], v252, s[8:11], s87 offen sc1\n"
    "v_mfma_f32_16x16x32_bf16 v[156:159], a[168:171], a[72:75], v[156:159]\n"
    "s_mov_b64 exec, -1\n"
    "ds_read2_b32 v[228:229], v251 offset0:0 offset1:2\n"
    "v_mfma_f32_16x16x32_bf16 v[156:159], a[172:175], a[76:79], v[156:159]\n"
    "s_add_u32 s87, s87, s88\n"
    "ds_read2_b32 v[230:231], v251 offset0:4 offset1:6\n"
    "v_mfma_f32_16x16x32_bf16 v[164:167], a[172:175], a[108:111], v[164:167]\n"
    "ds_read_b32 v232, v251 offset:32\n"
    "v_mfma_f32_16x16x32_bf16 v[164:167], a[168:171], a[104:107], v[164:167]\n"
    "v_exp_f32_e32 v219, v219\n"
    "v_mfma_f32_16x16x32_bf16 v[172:175], a[168:171], a[136:139], v[172:175]\n"
    "ds_write_addtid_b32 v199 offset:2816\n"
    "v_cvt_pk_bf16_f32 v214, v216, v217\n"
    "v_mfma_f32_16x16x32_bf16 v[172:175], a[172:175], a[140:143], v[172:175]\n"
    "s_waitcnt lgkmcnt(13)\n"
    "ds_read_b128 a[164:167], v241 offset:2048\n"
    "v_mfma_f32_16x16x32_bf16 v[172:175], a[176:179], a[144:147], v[172:175]\n"
    "ds_write_addtid_b32 v206 offset:3584\n"
    "v_cvt_pk_bf16_f32 v215, v218, v219\n"
    "v_mfma_f32_16x16x32_bf16 v[172:175], a[180:183], a[148:151], v[172:175]\n"
    "ds_read_b128 a[168:171], v240 offset:6144\n"
    "v_mfma_f32_16x16x32_bf16 v[164:167], a[180:183], a[116:119], v[164:167]\n"
    "s_waitcnt lgkmcnt(6)\n"
    "ds_write_addtid_b32 v207 offset:3840\n"
    "v_mad_u32_u16 v234, v228, s90, v239 op_sel:[0,0,0,0]\n"
    "v_mfma_f32_16x16x32_bf16 v[164:167], a[176:179], a[112:115], v[164:167]\n"
    "buffer_load_dwordx4 a[192:195], v234, s[20:23], 0 offen sc1\n"
    "v_mad_u32_u16 v235, v228, s90, v239 op_sel:[1,0,0,0]\n"
    "v_mfma_f32_16x16x32_bf16 v[156:159], a[176:179], a[80:83], v[156:159]\n"
    "ds_write_addtid_b32 v214 offset:4608\n"
    "v_mfma_f32_16x16x32_bf16 v[156:159], a[180:183], a[84:87], v[156:159]\n"
    "ds_write_addtid_b32 v215 offset:4864\n"
    "v_mfma_f32_16x16x32_bf16 v[148:151], a[180:183], a[52:55], v[148:151]\n"
    "buffer_load_dwordx4 a[196:199], v235, s[20:23], 0 offen sc1\n"
    "v_mfma_f32_16x16x32_bf16 v[148:151], a[176:179], a[48:51], v[148:151]\n"
    "v_mfma_f32_16x16x32_bf16 v[140:143], a[176:179], a[16:19], v[140:143]\n"
    "s_waitcnt lgkmcnt(0)\n"
    "v_mfma_f32_16x16x32_bf16 v[140:143], a[180:183], a[20:23], v[140:143]\n"
    "s_barrier\n"
    "v_mfma_f32_16x16x32_bf16 v[140:143], a[184:187], a[24:27], v[140:143]\n"
    "s_add_u32 m0, s43, 16384\n"
    "ds_read_b128 a[172:175], v241 offset:6144\n"
    "v_mfma_f32_16x16x32_bf16 v[140:143], a[188:191], a[28:31], v[140:143]\n"
    "ds_read2st64_b32 v[184:185], v249 offset0:0 offset1:1\n"
    "v_mfma_f32_16x16x32_bf16 v[148:151], a[188:191], a[60:63], v[148:151]\n"
    "buffer_load_dwordx4 v248, s[8:11], s40 offen offset:0 sc1 lds\n"
    "v_mfma_f32_16x16x32_bf16 v[148:151], a[184:187], a[56:59], v[148:151]\n"
    "ds_read_b128 a[176:179], v242 offset:2048\n"
    "v_mfma_f32_16x16x32_bf16 v[156:159], a[184:187], a[88:91], v[156:159]\n"
    "v_mfma_f32_16x16x32_bf16 v[156:159], a[188:191], a[92:95], v[156:159]\n"
    "v_mfma_f32_16x16x32_bf16 v[164:167], a[188:191], a[124:127], v[164:167]\n"
    "ds_read2st64_b32 v[186:187], v249 offset0:2 offset1:3\n"
    "v_mfma_f32_16x16x32_bf16 v[164:167], a[184:187], a[120:123], v[164:167]\n"
    "buffer_load_dwordx4 v248, s[8:11], s40 offen offset:1024 sc1 lds\n"
    "v_mfma_f32_16x16x32_bf16 v[172:175], a[184:187], a[152:155], v[172:175]\n"
    "v_mfma_f32_16x16x32_bf16 v[172:175], a[188:191], a[156:159], v[172:175]\n"
    "v_mfma_f32_16x16x32_bf16 v[176:179], a[160:163], a[128:131], v[220:223]\n"
    "ds_read_b128 a[180:183], v243 offset:2048\n"
    "v_mfma_f32_16x16x32_bf16 v[176:179], a[164:167], a[132:135], v[176:179]\n"
    "ds_read2st64_b32 v[192:193], v249 offset0:4 offset1:5\n"
    "v_mfma_f32_16x16x32_bf16 v[168:171], a[164:167], a[100:103], v[220:223]\n"
    "buffer_load_dwordx4 v248, s[8:11], s40 offen offset:2048 sc1 lds\n"
    "v_mfma_f32_16x16x32_bf16 v[168:171], a[160:163], a[96:99], v[168:171]\n"
    "ds_read_b128 a[184:187], v242 offset:6144\n"
    "v_mfma_f32_16x16x32_bf16 v[160:163], a[160:163], a[64:67], v[220:223]\n"
    "v_exp_f32_e32 v140, v140\n"
    "v_mfma_f32_16x16x32_bf16 v[160:163], a[164:167], a[68:71], v[160:163]\n"
    "ds_read2st64_b32 v[194:195], v249 offset0:6 offset1:7\n"
    "v_mfma_f32_16x16x32_bf16 v[152:155], a[164:167], a[36:39], v[220:223]\n"
    "ds_read2st64_b32 v[200:201], v249 offset0:8 offset1:9\n"
    "v_mfma_f32_16x16x32_bf16 v[152:155], a[160:163], a[32:35], v[152:155]\n"
    "buffer_load_dwordx4 v248, s[8:11], s40 offen offset:3072 sc1 lds\n"
    "v_mfma_f32_16x16x32_bf16 v[144:147], a[160:163], a[0:3], v[220:223]\n"
    "s_add_u32 m0, s43, 20480\n"
    "ds_read2st64_b32 v[202:203], v249 offset0:10 offset1:11\n"
    "v_mfma_f32_16x16x32_bf16 v[144:147], a[164:167], a[4:7], v[144:147]\n"
    "s_waitcnt vmcnt(16)\n"
    "ds_read_b128 a[188:191], v243 offset:6144\n"
    "v_smfmac_f32_16x16x64_bf16 v[224:227], a[232:235], a[200:207], v238\n"
    "s_waitcnt lgkmcnt(10)\n"
    "v_mad_u32_u16 v234, v229, s90, v239 op_sel:[0,0,0,0]\n"
    "v_mfma_f32_16x16x32_bf16 v[144:147], a[168:171], a[8:11], v[144:147]\n"
    "s_bitcmp1_b32 s86, 1\n"
    "buffer_load_dwordx4 a[200:203], v234, s[20:23], 0 offen sc1\n"
    "v_mad_u32_u16 v235, v229, s90, v239 op_sel:[1,0,0,0]\n"
    "v_mfma_f32_16x16x32_bf16 v[144:147], a[172:175], a[12:15], v[144:147]\n"
    "s_cselect_b64 exec, -1, 0\n"
    "v_cvt_pk_bf16_f32 v236, v224, v225\n"
    "v_cvt_pk_bf16_f32 v237, v226, v227\n"
    "v_mfma_f32_16x16x32_bf16 v[152:155], a[172:175], a[44:47], v[152:155]\n"
    "s_cselect_b32 s88, 512, 0\n"
    "v_mov_b64_e32 v[224:225], 0\n"
    "v_mov_b64_e32 v[226:227], 0\n"
    "v_mfma_f32_16x16x32_bf16 v[152:155], a[168:171], a[40:43], v[152:155]\n"
    "buffer_store_dwordx2 v[236:237], v252, s[8:11], s87 offen sc1\n"
    "v_mfma_f32_16x16x32_bf16 v[160:163], a[168:171], a[72:75], v[160:163]\n"
    "s_mov_b64 exec, -1\n"
    "ds_read_b64_tr_b16 a[160:161], v244 offset:24576\n"
    "v_mfma_f32_16x16x32_bf16 v[160:163], a[172:175], a[76:79], v[160:163]\n"
    "s_add_u32 s87, s87, s88\n"
    "ds_read_b64_tr_b16 a[162:163], v244 offset:26624\n"
    "v_mfma_f32_16x16x32_bf16 v[168:171], a[172:175], a[108:111], v[168:171]\n"
    "s_add_u32 s40, s40, 0x8000\n"
    "buffer_load_dwordx4 v248, s[8:11], s41 offen offset:0 sc1 lds\n"
    "v_mfma_f32_16x16x32_bf16 v[168:171], a[168:171], a[104:107], v[168:171]\n"
    "ds_read_b64_tr_b16 a[164:165], v244 offset:57344\n"
    "v_mfma_f32_16x16x32_bf16 v[176:179], a[168:171], a[136:139], v[176:179]\n"
    "ds_read2st64_b32 v[208:209], v249 offset0:12 offset1:13\n"
    "v_mfma_f32_16x16x32_bf16 v[176:179], a[172:175], a[140:143], v[176:179]\n"
    "s_waitcnt lgkmcnt(10)\n"
    "ds_read2st64_b32 v[210:211], v249 offset0:14 offset1:15\n"
    "v_mfma_f32_16x16x32_bf16 v[176:179], a[176:179], a[144:147], v[176:179]\n"
    "ds_read_b64_tr_b16 a[166:167], v244 offset:59392\n"
    "v_mfma_f32_16x16x32_bf16 v[176:179], a[180:183], a[148:151], v[176:179]\n"
    "buffer_load_dwordx4 v248, s[8:11], s41 offen offset:1024 sc1 lds\n"
    "v_mfma_f32_16x16x32_bf16 v[168:171], a[180:183], a[116:119], v[168:171]\n"
    "ds_read_b64_tr_b16 a[168:169], v245 offset:24576\n"
    "v_mfma_f32_16x16x32_bf16 v[168:171], a[176:179], a[112:115], v[168:171]\n"
    "v_exp_f32_e32 v141, v141\n"
    "v_mfma_f32_16x16x32_bf16 v[160:163], a[176:179], a[80:83], v[160:163]\n"
    "ds_read_b64_tr_b16 a[170:171], v245 offset:26624\n"
    "v_mfma_f32_16x16x32_bf16 v[160:163], a[180:183], a[84:87], v[160:163]\n"
    "ds_read2st64_b32 v[216:217], v249 offset0:16 offset1:17\n"
    "v_mfma_f32_16x16x32_bf16 v[152:155], a[180:183], a[52:55], v[152:155]\n"
    "buffer_load_dwordx4 v248, s[8:11], s41 offen offset:2048 sc1 lds\n"
    "v_mfma_f32_16x16x32_bf16 v[152:155], a[176:179], a[48:51], v[152:155]\n"
    "v_exp_f32_e32 v142, v142\n"
    "v_mfma_f32_16x16x32_bf16 v[144:147], a[176:179], a[16:19], v[144:147]\n"
    "ds_read_b64_tr_b16 a[172:173], v245 offset:57344\n"
    "v_mfma_f32_16x16x32_bf16 v[144:147], a[180:183], a[20:23], v[144:147]\n"
    "s_waitcnt lgkmcnt(10)\n"
    "ds_read2st64_b32 v[218:219], v249 offset0:18 offset1:19\n"
    "v_mfma_f32_16x16x32_bf16 v[144:147], a[184:187], a[24:27], v[144:147]\n"
    "ds_read_b64_tr_b16 a[176:177], v246 offset:24576\n"
    "v_mfma_f32_16x16x32_bf16 v[144:147], a[188:191], a[28:31], v[144:147]\n"
    "buffer_load_dwordx4 v248, s[8:11], s41 offen offset:3072 sc1 lds\n"
    "v_mfma_f32_16x16x32_bf16 v[152:155], a[188:191], a[60:63], v[152:155]\n"
    "ds_read_b64_tr_b16 a[174:175], v245 offset:59392\n"
    "v_mfma_f32_16x16x32_bf16 v[152:155], a[184:187], a[56:59], v[152:155]\n"
    "s_add_u32 s41, s41, 0x8000\n"
    "v_exp_f32_e32 v143, v143\n"
    "v_mfma_f32_16x16x32_bf16 v[160:163], a[184:187], a[88:91], v[160:163]\n"
    "buffer_load_dwordx4 a[204:207], v235, s[20:23], 0 offen sc1\n"
    "v_cvt_pk_bf16_f32 v140, v140, v141\n"
    "v_mfma_f32_16x16x32_bf16 v[160:163], a[188:191], a[92:95], v[160:163]\n"
    "ds_read_b64_tr_b16 a[178:179], v246 offset:26624\n"
    "v_mfma_f32_16x16x32_bf16 v[168:171], a[188:191], a[124:127], v[168:171]\n"
    "v_exp_f32_e32 v148, v148\n"
    "v_mfma_f32_16x16x32_bf16 v[168:171], a[184:187], a[120:123], v[168:171]\n"
    "v_exp_f32_e32 v149, v149\n"
    "v_mfma_f32_16x16x32_bf16 v[176:179], a[184:187], a[152:155], v[176:179]\n"
    "ds_read_b64_tr_b16 a[180:181], v246 offset:57344\n"
    "v_cvt_pk_bf16_f32 v141, v142, v143\n"
    "v_mfma_f32_16x16x32_bf16 v[176:179], a[188:191], a[156:159], v[176:179]\n"
    "s_waitcnt vmcnt(20)\n"
    "ds_read_b64_tr_b16 a[182:183], v246 offset:59392\n"
    "v_smfmac_f32_16x16x64_bf16 v[224:227], a[232:235], a[208:215], v238\n"
    "s_waitcnt lgkmcnt(5)\n"
    "v_mad_u32_u16 v234, v230, s90, v239 op_sel:[0,0,0,0]\n"
    "v_mfma_f32_16x16x32_bf16 a[236:239], a[160:163], v[180:183], a[236:239]\n"
    "s_bitcmp1_b32 s86, 2\n"
    "buffer_load_dwordx4 a[208:211], v234, s[20:23], 0 offen sc1\n"
    "v_mad_u32_u16 v235, v230, s90, v239 op_sel:[1,0,0,0]\n"
    "v_mfma_f32_16x16x32_bf16 a[236:239], a[164:167], v[184:187], a[236:239]\n"
    "s_cselect_b64 exec, -1, 0\n"
    "v_cvt_pk_bf16_f32 v236, v224, v225\n"
    "v_cvt_pk_bf16_f32 v237, v226, v227\n"
    "v_mfma_f32_16x16x32_bf16 a[240:243], a[164:167], v[192:195], a[240:243]\n"
    "s_cselect_b32 s88, 512, 0\n"
    "v_mov_b64_e32 v[224:225], 0\n"
    "v_mov_b64_e32 v[226:227], 0\n"
    "v_mfma_f32_16x16x32_bf16 a[240:243], a[160:163], v[188:191], a[240:243]\n"
    "buffer_store_dwordx2 v[236:237], v252, s[8:11], s87 offen sc1\n"
    "v_mfma_f32_16x16x32_bf16 a[244:247], a[160:163], v[196:199], a[244:247]\n"
    "s_mov_b64 exec, -1\n"
    "ds_read_b64_tr_b16 a[184:185], v247 offset:24576\n"
    "v_mfma_f32_16x16x32_bf16 a[244:247], a[164:167], v[200:203], a[244:247]\n"
    "s_add_u32 s87, s87, s88\n"
    "ds_read_b64_tr_b16 a[186:187], v247 offset:26624\n"
    "v_mfma_f32_16x16x32_bf16 a[248:251], a[164:167], v[208:211], a[248:251]\n"
    "buffer_load_dwordx4 a[212:215], v235, s[20:23], 0 offen sc1\n"
    "v_mfma_f32_16x16x32_bf16 a[248:251], a[160:163], v[204:207], a[248:251]\n"
    "ds_read_b64_tr_b16 a[188:189], v247 offset:57344\n"
    "v_mfma_f32_16x16x32_bf16 a[252:255], a[160:163], v[212:215], a[252:255]\n"
    "v_exp_f32_e32 v150, v150\n"
    "v_mfma_f32_16x16x32_bf16 a[252:255], a[164:167], v[216:219], a[252:255]\n"
    "s_waitcnt lgkmcnt(6)\n"
    "v_exp_f32_e32 v151, v151\n"
    "v_mfma_f32_16x16x32_bf16 v[16:19], a[172:175], v[216:219], v[16:19]\n"
    "ds_read_b64_tr_b16 a[190:191], v247 offset:59392\n"
    "v_cvt_pk_bf16_f32 v148, v148, v149\n"
    "v_mfma_f32_16x16x32_bf16 v[16:19], a[168:171], v[212:215], v[16:19]\n"
    "ds_read_b64_tr_b16 a[160:161], v244 offset:28672\n"
    "v_mfma_f32_16x16x32_bf16 v[12:15], a[168:171], v[204:207], v[12:15]\n"
    "v_exp_f32_e32 v156, v156\n"
    "v_mfma_f32_16x16x32_bf16 v[12:15], a[172:175], v[208:211], v[12:15]\n"
    "v_exp_f32_e32 v157, v157\n"
    "v_mfma_f32_16x16x32_bf16 v[8:11], a[172:175], v[200:203], v[8:11]\n"
    "ds_read_b64_tr_b16 a[162:163], v244 offset:30720\n"
    "v_cvt_pk_bf16_f32 v149, v150, v151\n"
    "v_mfma_f32_16x16x32_bf16 v[8:11], a[168:171], v[196:199], v[8:11]\n"
    "v_exp_f32_e32 v158, v158\n"
    "v_mfma_f32_16x16x32_bf16 v[4:7], a[168:171], v[188:191], v[4:7]\n"
    "v_exp_f32_e32 v159, v159\n"
    "v_mfma_f32_16x16x32_bf16 v[4:7], a[172:175], v[192:195], v[4:7]\n"
    "v_cvt_pk_bf16_f32 v156, v156, v157\n"
    "v_mfma_f32_16x16x32_bf16 v[0:3], a[172:175], v[184:187], v[0:3]\n"
    "ds_read_b64_tr_b16 a[164:165], v244 offset:61440\n"
    "v_mfma_f32_16x16x32_bf16 v[0:3], a[168:171], v[180:183], v[0:3]\n"
    "s_waitcnt lgkmcnt(7)\n"
    "v_exp_f32_e32 v164, v164\n"
    "v_mfma_f32_16x16x32_bf16 v[20:23], a[176:179], v[180:183], v[20:23]\n"
    "ds_read_b64_tr_b16 a[168:169], v245 offset:28672\n"
    "v_mfma_f32_16x16x32_bf16 v[20:23], a[180:183], v[184:187], v[20:23]\n"
    "ds_read_b64_tr_b16 a[166:167], v244 offset:63488\n"
    "v_mfma_f32_16x16x32_bf16 v[24:27], a[180:183], v[192:195], v[24:27]\n"
    "v_exp_f32_e32 v165, v165\n"
    "v_mfma_f32_16x16x32_bf16 v[24:27], a[176:179], v[188:191], v[24:27]\n"
    "v_cvt_pk_bf16_f32 v157, v158, v159\n"
    "v_mfma_f32_16x16x32_bf16 v[28:31], a[176:179], v[196:199], v[28:31]\n"
    "ds_read_b64_tr_b16 a[170:171], v245 offset:30720\n"
    "v_mfma_f32_16x16x32_bf16 v[28:31], a[180:183], v[200:203], v[28:31]\n"
    "v_exp_f32_e32 v166, v166\n"
    "v_mfma_f32_16x16x32_bf16 v[32:35], a[180:183], v[208:211], v[32:35]\n"
    "v_exp_f32_e32 v167, v167\n"
    "v_mfma_f32_16x16x32_bf16 v[32:35], a[176:179], v[204:207], v[32:35]\n"
    "v_cvt_pk_bf16_f32 v164, v164, v165\n"
    "v_mfma_f32_16x16x32_bf16 v[36:39], a[176:179], v[212:215], v[36:39]\n"
    "ds_read_b64_tr_b16 a[172:173], v245 offset:61440\n"
    "v_mfma_f32_16x16x32_bf16 v[36:39], a[180:183], v[216:219], v[36:39]\n"
    "s_waitcnt vmcnt(20)\n"
    "ds_read_b64_tr_b16 a[174:175], v245 offset:63488\n"
    "v_smfmac_f32_16x16x64_bf16 v[224:227], a[232:235], a[216:223], v238\n"
    "s_waitcnt lgkmcnt(8)\n"
    "v_mad_u32_u16 v234, v231, s90, v239 op_sel:[0,0,0,0]\n"
    "v_mfma_f32_16x16x32_bf16 v[56:59], a[188:191], v[216:219], v[56:59]\n"
    "s_bitcmp1_b32 s86, 3\n"
    "buffer_load_dwordx4 a[216:219], v234, s[20:23], 0 offen sc1\n"
    "v_mad_u32_u16 v235, v231, s90, v239 op_sel:[1,0,0,0]\n"
    "v_mfma_f32_16x16x32_bf16 v[56:59], a[184:187], v[212:215], v[56:59]\n"
    "s_cselect_b64 exec, -1, 0\n"
    "v_cvt_pk_bf16_f32 v236, v224, v225\n"
    "v_cvt_pk_bf16_f32 v237, v226, v227\n"
    "v_mfma_f32_16x16x32_bf16 v[52:55], a[184:187], v[204:207], v[52:55]\n"
    "s_cselect_b32 s88, 512, 0\n"
    "v_mov_b64_e32 v[224:225], 0\n"
    "v_mov_b64_e32 v[226:227], 0\n"
    "v_mfma_f32_16x16x32_bf16 v[52:55], a[188:191], v[208:211], v[52:55]\n"
    "buffer_store_dwordx2 v[236:237], v252, s[8:11], s87 offen sc1\n"
    "v_mfma_f32_16x16x32_bf16 v[48:51], a[188:191], v[200:203], v[48:51]\n"
    "s_mov_b64 exec, -1\n"
    "ds_read_b64_tr_b16 a[176:177], v246 offset:28672\n"
    "v_mfma_f32_16x16x32_bf16 v[48:51], a[184:187], v[196:199], v[48:51]\n"
    "s_add_u32 s87, s87, s88\n"
    "ds_read_b64_tr_b16 a[178:179], v246 offset:30720\n"
    "v_mfma_f32_16x16x32_bf16 v[44:47], a[184:187], v[188:191], v[44:47]\n"
    "buffer_load_dwordx4 a[220:223], v235, s[20:23], 0 offen sc1\n"
    "v_mfma_f32_16x16x32_bf16 v[44:47], a[188:191], v[192:195], v[44:47]\n"
    "ds_read_b64_tr_b16 a[180:181], v246 offset:61440\n"
    "v_mfma_f32_16x16x32_bf16 v[40:43], a[188:191], v[184:187], v[40:43]\n"
    "v_exp_f32_e32 v172, v172\n"
    "v_mfma_f32_16x16x32_bf16 v[40:43], a[184:187], v[180:183], v[40:43]\n"
    "s_waitcnt lgkmcnt(6)\n"
    "v_exp_f32_e32 v173, v173\n"
    "v_mfma_f32_16x16x32_bf16 v[60:63], a[160:163], v[180:183], v[60:63]\n"
    "ds_read_b64_tr_b16 a[182:183], v246 offset:63488\n"
    "v_cvt_pk_bf16_f32 v165, v166, v167\n"
    "v_mfma_f32_16x16x32_bf16 v[60:63], a[164:167], v[184:187], v[60:63]\n"
    "ds_read_b64_tr_b16 a[184:185], v247 offset:28672\n"
    "v_mfma_f32_16x16x32_bf16 v[64:67], a[164:167], v[192:195], v[64:67]\n"
    "ds_read_b64_tr_b16 a[186:187], v247 offset:30720\n"
    "v_mfma_f32_16x16x32_bf16 v[64:67], a[160:163], v[188:191], v[64:67]\n"
    "v_exp_f32_e32 v174, v174\n"
    "v_mfma_f32_16x16x32_bf16 v[68:71], a[160:163], v[196:199], v[68:71]\n"
    "v_exp_f32_e32 v175, v175\n"
    "v_mfma_f32_16x16x32_bf16 v[68:71], a[164:167], v[200:203], v[68:71]\n"
    "ds_read_b64_tr_b16 a[188:189], v247 offset:61440\n"
    "v_cvt_pk_bf16_f32 v172, v172, v173\n"
    "v_mfma_f32_16x16x32_bf16 v[72:75], a[164:167], v[208:211], v[72:75]\n"
    "ds_read_b64_tr_b16 a[190:191], v247 offset:63488\n"
    "v_cvt_pk_bf16_f32 v173, v174, v175\n"
    "v_mfma_f32_16x16x32_bf16 v[72:75], a[160:163], v[204:207], v[72:75]\n"
    "s_waitcnt vmcnt(30)\n"
    "v_mfma_f32_16x16x32_bf16 v[76:79], a[160:163], v[212:215], v[76:79]\n"
    "s_barrier\n"
    "v_mfma_f32_16x16x32_bf16 v[76:79], a[164:167], v[216:219], v[76:79]\n"
    "s_waitcnt lgkmcnt(8)\n"
    "s_mov_b32 m0, s42\n"
    "v_mfma_f32_16x16x32_bf16 v[96:99], a[172:175], v[216:219], v[96:99]\n"
    "v_exp_f32_e32 v144, v144\n"
    "v_mfma_f32_16x16x32_bf16 v[96:99], a[168:171], v[212:215], v[96:99]\n"
    "v_exp_f32_e32 v145, v145\n"
    "v_mfma_f32_16x16x32_bf16 v[92:95], a[168:171], v[204:207], v[92:95]\n"
    "v_exp_f32_e32 v146, v146\n"
    "v_mfma_f32_16x16x32_bf16 v[92:95], a[172:175], v[208:211], v[92:95]\n"
    "ds_read_b128 a[160:163], v240 offset:8192\n"
    "ds_write_addtid_b32 v140 offset:0\n"
    "v_mfma_f32_16x16x32_bf16 v[88:91], a[172:175], v[200:203], v[88:91]\n"
    "v_exp_f32_e32 v147, v147\n"
    "v_mfma_f32_16x16x32_bf16 v[88:91], a[168:171], v[196:199], v[88:91]\n"
    "ds_write_addtid_b32 v141 offset:256\n"
    "v_cvt_pk_bf16_f32 v142, v144, v145\n"
    "v_mfma_f32_16x16x32_bf16 v[84:87], a[168:171], v[188:191], v[84:87]\n"
    "v_exp_f32_e32 v152, v152\n"
    "v_mfma_f32_16x16x32_bf16 v[84:87], a[172:175], v[192:195], v[84:87]\n"
    "v_exp_f32_e32 v153, v153\n"
    "v_mfma_f32_16x16x32_bf16 v[80:83], a[172:175], v[184:187], v[80:83]\n"
    "ds_write_addtid_b32 v148 offset:1024\n"
    "v_cvt_pk_bf16_f32 v143, v146, v147\n"
    "v_mfma_f32_16x16x32_bf16 v[80:83], a[168:171], v[180:183], v[80:83]\n"
    "s_waitcnt vmcnt(20)\n"
    "ds_read_b128 a[164:167], v241 offset:8192\n"
    "ds_write_addtid_b32 v149 offset:1280\n"
    "v_smfmac_f32_16x16x64_bf16 v[224:227], a[232:235], a[224:231], v238\n"
    "s_waitcnt lgkmcnt(10)\n"
    "ds_write_addtid_b32 v156 offset:2048\n"
    "v_mad_u32_u16 v234, v232, s90, v239 op_sel:[0,0,0,0]\n"
    "v_mfma_f32_16x16x32_bf16 v[100:103], a[176:179], v[180:183], v[100:103]\n"
    "s_bitcmp1_b32 s86, 4\n"
    "buffer_load_dwordx4 a[224:227], v234, s[20:23], 0 offen sc1\n"
    "v_mad_u32_u16 v235, v232, s90, v239 op_sel:[1,0,0,0]\n"
    "v_mfma_f32_16x16x32_bf16 v[100:103], a[180:183], v[184:187], v[100:103]\n"
    "s_cselect_b64 exec, -1, 0\n"
    "v_cvt_pk_bf16_f32 v236, v224, v225\n"
    "v_cvt_pk_bf16_f32 v237, v226, v227\n"
    "v_mfma_f32_16x16x32_bf16 v[104:107], a[180:183], v[192:195], v[104:107]\n"
    "s_cselect_b32 s88, 512, 0\n"
    "v_mov_b64_e32 v[224:225], 0\n"
    "v_mov_b64_e32 v[226:227], 0\n"
    "v_mfma_f32_16x16x32_bf16 v[104:107], a[176:179], v[188:191], v[104:107]\n"
    "buffer_store_dwordx2 v[236:237], v252, s[8:11], s87 offen sc1\n"
    "v_mfma_f32_16x16x32_bf16 v[108:111], a[176:179], v[196:199], v[108:111]\n"
    "s_mov_b64 exec, -1\n"
    "ds_read_b128 a[168:171], v240 offset:12288\n"
    "ds_write_addtid_b32 v157 offset:2304\n"
    "v_mfma_f32_16x16x32_bf16 v[108:111], a[180:183], v[200:203], v[108:111]\n"
    "s_add_u32 s87, s87, s88\n"
    "v_exp_f32_e32 v154, v154\n"
    "v_mfma_f32_16x16x32_bf16 v[112:115], a[180:183], v[208:211], v[112:115]\n"
    "buffer_load_dwordx4 a[228:231], v235, s[20:23], 0 offen sc1\n"
    "ds_write_addtid_b32 v164 offset:3072\n"
    "v_mfma_f32_16x16x32_bf16 v[112:115], a[176:179], v[204:207], v[112:115]\n"
    "ds_write_addtid_b32 v165 offset:3328\n"
    "v_mfma_f32_16x16x32_bf16 v[116:119], a[176:179], v[212:215], v[116:119]\n"
    "v_exp_f32_e32 v155, v155\n"
    "v_mfma_f32_16x16x32_bf16 v[116:119], a[180:183], v[216:219], v[116:119]\n"
    "s_waitcnt lgkmcnt(11)\n"
    "ds_read_b128 a[172:175], v241 offset:12288\n"
    "v_cvt_pk_bf16_f32 v150, v152, v153\n"
    "v_mfma_f32_16x16x32_bf16 v[136:139], a[188:191], v[216:219], v[136:139]\n"
    "v_exp_f32_e32 v160, v160\n"
    "v_mfma_f32_16x16x32_bf16 v[136:139], a[184:187], v[212:215], v[136:139]\n"
    "v_exp_f32_e32 v161, v161\n"
    "v_mfma_f32_16x16x32_bf16 v[132:135], a[184:187], v[204:207], v[132:135]\n"
    "ds_read_b128 a[176:179], v242 offset:8192\n"
    "v_cvt_pk_bf16_f32 v151, v154, v155\n"
    "v_mfma_f32_16x16x32_bf16 v[132:135], a[188:191], v[208:211], v[132:135]\n"
    "v_exp_f32_e32 v162, v162\n"
    "v_mfma_f32_16x16x32_bf16 v[128:131], a[188:191], v[200:203], v[128:131]\n"
    "v_exp_f32_e32 v163, v163\n"
    "v_mfma_f32_16x16x32_bf16 v[128:131], a[184:187], v[196:199], v[128:131]\n"
    "ds_write_addtid_b32 v172 offset:4096\n"
    "v_cvt_pk_bf16_f32 v158, v160, v161\n"
    "v_mfma_f32_16x16x32_bf16 v[124:127], a[184:187], v[188:191], v[124:127]\n"
    "v_exp_f32_e32 v168, v168\n"
    "v_mfma_f32_16x16x32_bf16 v[124:127], a[188:191], v[192:195], v[124:127]\n"
    "ds_read_b128 a[180:183], v243 offset:8192\n"
    "ds_write_addtid_b32 v173 offset:4352\n"
    "v_mfma_f32_16x16x32_bf16 v[120:123], a[188:191], v[184:187], v[120:123]\n"
    "v_exp_f32_e32 v169, v169\n"
    "v_mfma_f32_16x16x32_bf16 v[120:123], a[184:187], v[180:183], v[120:123]\n"
    "s_waitcnt lgkmcnt(11)\n"
    "ds_write_addtid_b32 v142 offset:512\n"
    "v_cvt_pk_bf16_f32 v159, v162, v163\n"
    "v_mfma_f32_16x16x32_bf16 v[212:215], a[160:163], a[128:131], v[220:223]\n"
    "ds_write_addtid_b32 v143 offset:768\n"
    "v_mfma_f32_16x16x32_bf16 v[212:215], a[164:167], a[132:135], v[212:215]\n"
    "v_exp_f32_e32 v170, v170\n"
    "v_mfma_f32_16x16x32_bf16 v[204:207], a[164:167], a[100:103], v[220:223]\n"
    "ds_write_addtid_b32 v150 offset:1536\n"
    "v_mfma_f32_16x16x32_bf16 v[204:207], a[160:163], a[96:99], v[204:207]\n"
    "ds_read_b128 a[184:187], v242 offset:12288\n"
    "v_mfma_f32_16x16x32_bf16 v[196:199], a[160:163], a[64:67], v[220:223]\n"
    "v_exp_f32_e32 v171, v171\n"
    "v_mfma_f32_16x16x32_bf16 v[196:199], a[164:167], a[68:71], v[196:199]\n"
    "ds_write_addtid_b32 v151 offset:1792\n"
    "v_cvt_pk_bf16_f32 v166, v168, v169\n"
    "v_mfma_f32_16x16x32_bf16 v[188:191], a[164:167], a[36:39], v[220:223]\n"
    "v_exp_f32_e32 v176, v176\n"
    "v_mfma_f32_16x16x32_bf16 v[188:191], a[160:163], a[32:35], v[188:191]\n"
    "v_exp_f32_e32 v177, v177\n"
    "v_mfma_f32_16x16x32_bf16 v[180:183], a[160:163], a[0:3], v[220:223]\n"
    "ds_write_addtid_b32 v158 offset:2560\n"
    "v_cvt_pk_bf16_f32 v167, v170, v171\n"
    "v_mfma_f32_16x16x32_bf16 v[180:183], a[164:167], a[4:7], v[180:183]\n"
    "s_waitcnt vmcnt(20)\n"
    "v_exp_f32_e32 v178, v178\n"
    "v_smfmac_f32_16x16x64_bf16 v[224:227], a[232:235], a[192:199], v238\n"
    "s_waitcnt lgkmcnt(10)\n"
    "ds_read_b128 a[188:191], v243 offset:12288\n"
    "v_mfma_f32_16x16x32_bf16 v[180:183], a[168:171], a[8:11], v[180:183]\n"
    "s_bitcmp1_b32 s86, 5\n"
    "ds_read_b128 a[160:163], v240 offset:10240\n"
    "v_mfma_f32_16x16x32_bf16 v[180:183], a[172:175], a[12:15], v[180:183]\n"
    "s_cselect_b64 exec, -1, 0\n"
    "v_cvt_pk_bf16_f32 v236, v224, v225\n"
    "v_cvt_pk_bf16_f32 v237, v226, v227\n"
    "v_mfma_f32_16x16x32_bf16 v[188:191], a[172:175], a[44:47], v[188:191]\n"
    "s_cselect_b32 s88, 512, 0\n"
    "v_mov_b64_e32 v[224:225], 0\n"
    "v_mov_b64_e32 v[226:227], 0\n"
    "v_mfma_f32_16x16x32_bf16 v[188:191], a[168:171], a[40:43], v[188:191]\n"
    "buffer_store_dwordx2 v[236:237], v252, s[8:11], s87 offen sc1\n"
    "v_mfma_f32_16x16x32_bf16 v[196:199], a[168:171], a[72:75], v[196:199]\n"
    "s_mov_b64 exec, -1\n"
    "ds_read2_b32 v[228:229], v251 offset0:10 offset1:12\n"
    "v_mfma_f32_16x16x32_bf16 v[196:199], a[172:175], a[76:79], v[196:199]\n"
    "s_add_u32 s87, s87, s88\n"
    "ds_read2_b32 v[230:231], v251 offset0:14 offset1:16\n"
    "v_mfma_f32_16x16x32_bf16 v[204:207], a[172:175], a[108:111], v[204:207]\n"
    "ds_read_b32 v232, v251 offset:72\n"
    "v_mfma_f32_16x16x32_bf16 v[204:207], a[168:171], a[104:107], v[204:207]\n"
    "v_exp_f32_e32 v179, v179\n"
    "v_mfma_f32_16x16x32_bf16 v[212:215], a[168:171], a[136:139], v[212:215]\n"
    "ds_write_addtid_b32 v159 offset:2816\n"
    "v_cvt_pk_bf16_f32 v174, v176, v177\n"
    "v_mfma_f32_16x16x32_bf16 v[212:215], a[172:175], a[140:143], v[212:215]\n"
    "s_waitcnt lgkmcnt(13)\n"
    "ds_read_b128 a[164:167], v241 offset:10240\n"
    "v_mfma_f32_16x16x32_bf16 v[212:215], a[176:179], a[144:147], v[212:215]\n"
    "ds_write_addtid_b32 v166 offset:3584\n"
    "v_cvt_pk_bf16_f32 v175, v178, v179\n"
    "v_mfma_f32_16x16x32_bf16 v[212:215], a[180:183], a[148:151], v[212:215]\n"
    "ds_read_b128 a[168:171], v240 offset:14336\n"
    "v_mfma_f32_16x16x32_bf16 v[204:207], a[180:183], a[116:119], v[204:207]\n"
    "s_waitcnt lgkmcnt(6)\n"
    "ds_write_addtid_b32 v167 offset:3840\n"
    "v_mad_u32_u16 v234, v228, s90, v239 op_sel:[0,0,0,0]\n"
    "v_mfma_f32_16x16x32_bf16 v[204:207], a[176:179], a[112:115], v[204:207]\n"
    "buffer_load_dwordx4 a[192:195], v234, s[20:23], 0 offen sc1\n"
    "v_mad_u32_u16 v235, v228, s90, v239 op_sel:[1,0,0,0]\n"
    "v_mfma_f32_16x16x32_bf16 v[196:199], a[176:179], a[80:83], v[196:199]\n"
    "ds_write_addtid_b32 v174 offset:4608\n"
    "v_mfma_f32_16x16x32_bf16 v[196:199], a[180:183], a[84:87], v[196:199]\n"
    "ds_write_addtid_b32 v175 offset:4864\n"
    "v_mfma_f32_16x16x32_bf16 v[188:191], a[180:183], a[52:55], v[188:191]\n"
    "buffer_load_dwordx4 a[196:199], v235, s[20:23], 0 offen sc1\n"
    "v_mfma_f32_16x16x32_bf16 v[188:191], a[176:179], a[48:51], v[188:191]\n"
    "v_mfma_f32_16x16x32_bf16 v[180:183], a[176:179], a[16:19], v[180:183]\n"
    "s_waitcnt lgkmcnt(0)\n"
    "v_mfma_f32_16x16x32_bf16 v[180:183], a[180:183], a[20:23], v[180:183]\n"
    "s_barrier\n"
    "v_mfma_f32_16x16x32_bf16 v[180:183], a[184:187], a[24:27], v[180:183]\n"
    "s_add_u32 m0, s43, 24576\n"
    "ds_read_b128 a[172:175], v241 offset:14336\n"
    "v_mfma_f32_16x16x32_bf16 v[180:183], a[188:191], a[28:31], v[180:183]\n"
    "ds_read2st64_b32 v[144:145], v249 offset0:0 offset1:1\n"
    "v_mfma_f32_16x16x32_bf16 v[188:191], a[188:191], a[60:63], v[188:191]\n"
    "buffer_load_dwordx4 v248, s[8:11], s40 offen offset:0 sc1 lds\n"
    "v_mfma_f32_16x16x32_bf16 v[188:191], a[184:187], a[56:59], v[188:191]\n"
    "ds_read_b128 a[176:179], v242 offset:10240\n"
    "v_mfma_f32_16x16x32_bf16 v[196:199], a[184:187], a[88:91], v[196:199]\n"
    "v_mfma_f32_16x16x32_bf16 v[196:199], a[188:191], a[92:95], v[196:199]\n"
    "v_mfma_f32_16x16x32_bf16 v[204:207], a[188:191], a[124:127], v[204:207]\n"
    "ds_read2st64_b32 v[146:147], v249 offset0:2 offset1:3\n"
    "v_mfma_f32_16x16x32_bf16 v[204:207], a[184:187], a[120:123], v[204:207]\n"
    "buffer_load_dwordx4 v248, s[8:11], s40 offen offset:1024 sc1 lds\n"
    "v_mfma_f32_16x16x32_bf16 v[212:215], a[184:187], a[152:155], v[212:215]\n"
    "v_mfma_f32_16x16x32_bf16 v[212:215], a[188:191], a[156:159], v[212:215]\n"
    "v_mfma_f32_16x16x32_bf16 v[216:219], a[160:163], a[128:131], v[220:223]\n"
    "ds_read_b128 a[180:183], v243 offset:10240\n"
    "v_mfma_f32_16x16x32_bf16 v[216:219], a[164:167], a[132:135], v[216:219]\n"
    "ds_read2st64_b32 v[152:153], v249 offset0:4 offset1:5\n"
    "v_mfma_f32_16x16x32_bf16 v[208:211], a[164:167], a[100:103], v[220:223]\n"
    "buffer_load_dwordx4 v248, s[8:11], s40 offen offset:2048 sc1 lds\n"
    "v_mfma_f32_16x16x32_bf16 v[208:211], a[160:163], a[96:99], v[208:211]\n"
    "ds_read_b128 a[184:187], v242 offset:14336\n"
    "v_mfma_f32_16x16x32_bf16 v[200:203], a[160:163], a[64:67], v[220:223]\n"
    "v_exp_f32_e32 v180, v180\n"
    "v_mfma_f32_16x16x32_bf16 v[200:203], a[164:167], a[68:71], v[200:203]\n"
    "ds_read2st64_b32 v[154:155], v249 offset0:6 offset1:7\n"
    "v_mfma_f32_16x16x32_bf16 v[192:195], a[164:167], a[36:39], v[220:223]\n"
    "ds_read2st64_b32 v[160:161], v249 offset0:8 offset1:9\n"
    "v_mfma_f32_16x16x32_bf16 v[192:195], a[160:163], a[32:35], v[192:195]\n"
    "buffer_load_dwordx4 v248, s[8:11], s40 offen offset:3072 sc1 lds\n"
    "v_mfma_f32_16x16x32_bf16 v[184:187], a[160:163], a[0:3], v[220:223]\n"
    "s_add_u32 m0, s43, 28672\n"
    "ds_read2st64_b32 v[162:163], v249 offset0:10 offset1:11\n"
    "v_mfma_f32_16x16x32_bf16 v[184:187], a[164:167], a[4:7], v[184:187]\n"
    "s_waitcnt vmcnt(16)\n"
    "ds_read_b128 a[188:191], v243 offset:14336\n"
    "v_smfmac_f32_16x16x64_bf16 v[224:227], a[232:235], a[200:207], v238\n"
    "s_waitcnt lgkmcnt(10)\n"
    "v_mad_u32_u16 v234, v229, s90, v239 op_sel:[0,0,0,0]\n"
    "v_mfma_f32_16x16x32_bf16 v[184:187], a[168:171], a[8:11], v[184:187]\n"
    "s_bitcmp1_b32 s86, 6\n"
    "buffer_load_dwordx4 a[200:203], v234, s[20:23], 0 offen sc1\n"
    "v_mad_u32_u16 v235, v229, s90, v239 op_sel:[1,0,0,0]\n"
    "v_mfma_f32_16x16x32_bf16 v[184:187], a[172:175], a[12:15], v[184:187]\n"
    "s_cselect_b64 exec, -1, 0\n"
    "v_cvt_pk_bf16_f32 v236, v224, v225\n"
    "v_cvt_pk_bf16_f32 v237, v226, v227\n"
    "v_mfma_f32_16x16x32_bf16 v[192:195], a[172:175], a[44:47], v[192:195]\n"
    "s_cselect_b32 s88, 512, 0\n"
    "v_mov_b64_e32 v[224:225], 0\n"
    "v_mov_b64_e32 v[226:227], 0\n"
    "v_mfma_f32_16x16x32_bf16 v[192:195], a[168:171], a[40:43], v[192:195]\n"
    "buffer_store_dwordx2 v[236:237], v252, s[8:11], s87 offen sc1\n"
    "v_mfma_f32_16x16x32_bf16 v[200:203], a[168:171], a[72:75], v[200:203]\n"
    "s_mov_b64 exec, -1\n"
    "ds_read_b64_tr_b16 a[160:161], v244 offset:0\n"
    "v_mfma_f32_16x16x32_bf16 v[200:203], a[172:175], a[76:79], v[200:203]\n"
    "s_add_u32 s87, s87, s88\n"
    "ds_read_b64_tr_b16 a[162:163], v244 offset:2048\n"
    "v_mfma_f32_16x16x32_bf16 v[208:211], a[172:175], a[108:111], v[208:211]\n"
    "s_add_u32 s40, s40, 0x8000\n"
    "buffer_load_dwordx4 v248, s[8:11], s41 offen offset:0 sc1 lds\n"
    "v_mfma_f32_16x16x32_bf16 v[208:211], a[168:171], a[104:107], v[208:211]\n"
    "ds_read_b64_tr_b16 a[164:165], v244 offset:32768\n"
    "v_mfma_f32_16x16x32_bf16 v[216:219], a[168:171], a[136:139], v[216:219]\n"
    "ds_read2st64_b32 v[168:169], v249 offset0:12 offset1:13\n"
    "v_mfma_f32_16x16x32_bf16 v[216:219], a[172:175], a[140:143], v[216:219]\n"
    "s_waitcnt lgkmcnt(10)\n"
    "ds_read2st64_b32 v[170:171], v249 offset0:14 offset1:15\n"
    "v_mfma_f32_16x16x32_bf16 v[216:219], a[176:179], a[144:147], v[216:219]\n"
    "ds_read_b64_tr_b16 a[166:167], v244 offset:34816\n"
    "v_mfma_f32_16x16x32_bf16 v[216:219], a[180:183], a[148:151], v[216:219]\n"
    "buffer_load_dwordx4 v248, s[8:11], s41 offen offset:1024 sc1 lds\n"
    "v_mfma_f32_16x16x32_bf16 v[208:211], a[180:183], a[116:119], v[208:211]\n"
    "ds_read_b64_tr_b16 a[168:169], v245 offset:0\n"
    "v_mfma_f32_16x16x32_bf16 v[208:211], a[176:179], a[112:115], v[208:211]\n"
    "v_exp_f32_e32 v181, v181\n"
    "v_mfma_f32_16x16x32_bf16 v[200:203], a[176:179], a[80:83], v[200:203]\n"
    "ds_read_b64_tr_b16 a[170:171], v245 offset:2048\n"
    "v_mfma_f32_16x16x32_bf16 v[200:203], a[180:183], a[84:87], v[200:203]\n"
    "ds_read2st64_b32 v[176:177], v249 offset0:16 offset1:17\n"
    "v_mfma_f32_16x16x32_bf16 v[192:195], a[180:183], a[52:55], v[192:195]\n"
    "buffer_load_dwordx4 v248, s[8:11], s41 offen offset:2048 sc1 lds\n"
    "v_mfma_f32_16x16x32_bf16 v[192:195], a[176:179], a[48:51], v[192:195]\n"
    "v_exp_f32_e32 v182, v182\n"
    "v_mfma_f32_16x16x32_bf16 v[184:187], a[176:179], a[16:19], v[184:187]\n"
    "ds_read_b64_tr_b16 a[172:173], v245 offset:32768\n"
    "v_mfma_f32_16x16x32_bf16 v[184:187], a[180:183], a[20:23], v[184:187]\n"
    "s_waitcnt lgkmcnt(10)\n"
    "ds_read2st64_b32 v[178:179], v249 offset0:18 offset1:19\n"
    "v_mfma_f32_16x16x32_bf16 v[184:187], a[184:187], a[24:27], v[184:187]\n"
    "ds_read_b64_tr_b16 a[176:177], v246 offset:0\n"
    "v_mfma_f32_16x16x32_bf16 v[184:187], a[188:191], a[28:31], v[184:187]\n"
    "buffer_load_dwordx4 v248, s[8:11], s41 offen offset:3072 sc1 lds\n"
    "v_mfma_f32_16x16x32_bf16 v[192:195], a[188:191], a[60:63], v[192:195]\n"
    "ds_read_b64_tr_b16 a[174:175], v245 offset:34816\n"
    "v_mfma_f32_16x16x32_bf16 v[192:195], a[184:187], a[56:59], v[192:195]\n"
    "s_add_u32 s41, s41, 0x8000\n"
    "v_exp_f32_e32 v183, v183\n"
    "v_mfma_f32_16x16x32_bf16 v[200:203], a[184:187], a[88:91], v[200:203]\n"
    "buffer_load_dwordx4 a[204:207], v235, s[20:23], 0 offen sc1\n"
    "v_cvt_pk_bf16_f32 v180, v180, v181\n"
    "v_mfma_f32_16x16x32_bf16 v[200:203], a[188:191], a[92:95], v[200:203]\n"
    "ds_read_b64_tr_b16 a[178:179], v246 offset:2048\n"
    "v_mfma_f32_16x16x32_bf16 v[208:211], a[188:191], a[124:127], v[208:211]\n"
    "v_exp_f32_e32 v188, v188\n"
    "v_mfma_f32_16x16x32_bf16 v[208:211], a[184:187], a[120:123], v[208:211]\n"
    "v_exp_f32_e32 v189, v189\n"
    "v_mfma_f32_16x16x32_bf16 v[216:219], a[184:187], a[152:155], v[216:219]\n"
    "ds_read_b64_tr_b16 a[180:181], v246 offset:32768\n"
    "v_cvt_pk_bf16_f32 v181, v182, v183\n"
    "v_mfma_f32_16x16x32_bf16 v[216:219], a[188:191], a[156:159], v[216:219]\n"
    "s_waitcnt vmcnt(20)\n"
    "ds_read_b64_tr_b16 a[182:183], v246 offset:34816\n"
    "v_smfmac_f32_16x16x64_bf16 v[224:227], a[232:235], a[208:215], v238\n"
    "s_waitcnt lgkmcnt(5)\n"
    "v_mad_u32_u16 v234, v230, s90, v239 op_sel:[0,0,0,0]\n"
    "v_mfma_f32_16x16x32_bf16 a[236:239], a[160:163], v[140:143], a[236:239]\n"
    "s_bitcmp1_b32 s86, 7\n"
    "buffer_load_dwordx4 a[208:211], v234, s[20:23], 0 offen sc1\n"
    "v_mad_u32_u16 v235, v230, s90, v239 op_sel:[1,0,0,0]\n"
    "v_mfma_f32_16x16x32_bf16 a[236:239], a[164:167], v[144:147], a[236:239]\n"
    "s_cselect_b64 exec, -1, 0\n"
    "v_cvt_pk_bf16_f32 v236, v224, v225\n"
    "v_cvt_pk_bf16_f32 v237, v226, v227\n"
    "v_mfma_f32_16x16x32_bf16 a[240:243], a[164:167], v[152:155], a[240:243]\n"
    "s_cselect_b32 s88, 512, 0\n"
    "v_mov_b64_e32 v[224:225], 0\n"
    "v_mov_b64_e32 v[226:227], 0\n"
    "v_mfma_f32_16x16x32_bf16 a[240:243], a[160:163], v[148:151], a[240:243]\n"
    "buffer_store_dwordx2 v[236:237], v252, s[8:11], s87 offen sc1\n"
    "v_mfma_f32_16x16x32_bf16 a[244:247], a[160:163], v[156:159], a[244:247]\n"
    "s_mov_b64 exec, -1\n"
    "ds_read_b64_tr_b16 a[184:185], v247 offset:0\n"
    "v_mfma_f32_16x16x32_bf16 a[244:247], a[164:167], v[160:163], a[244:247]\n"
    "s_add_u32 s87, s87, s88\n"
    "ds_read_b64_tr_b16 a[186:187], v247 offset:2048\n"
    "v_mfma_f32_16x16x32_bf16 a[248:251], a[164:167], v[168:171], a[248:251]\n"
    "buffer_load_dwordx4 a[212:215], v235, s[20:23], 0 offen sc1\n"
    "v_mfma_f32_16x16x32_bf16 a[248:251], a[160:163], v[164:167], a[248:251]\n"
    "ds_read_b64_tr_b16 a[188:189], v247 offset:32768\n"
    "v_mfma_f32_16x16x32_bf16 a[252:255], a[160:163], v[172:175], a[252:255]\n"
    "v_exp_f32_e32 v190, v190\n"
    "v_mfma_f32_16x16x32_bf16 a[252:255], a[164:167], v[176:179], a[252:255]\n"
    "s_waitcnt lgkmcnt(6)\n"
    "v_exp_f32_e32 v191, v191\n"
    "v_mfma_f32_16x16x32_bf16 v[16:19], a[172:175], v[176:179], v[16:19]\n"
    "ds_read_b64_tr_b16 a[190:191], v247 offset:34816\n"
    "v_cvt_pk_bf16_f32 v188, v188, v189\n"
    "v_mfma_f32_16x16x32_bf16 v[16:19], a[168:171], v[172:175], v[16:19]\n"
    "ds_read_b64_tr_b16 a[160:161], v244 offset:4096\n"
    "v_mfma_f32_16x16x32_bf16 v[12:15], a[168:171], v[164:167], v[12:15]\n"
    "v_exp_f32_e32 v196, v196\n"
    "v_mfma_f32_16x16x32_bf16 v[12:15], a[172:175], v[168:171], v[12:15]\n"
    "v_exp_f32_e32 v197, v197\n"
    "v_mfma_f32_16x16x32_bf16 v[8:11], a[172:175], v[160:163], v[8:11]\n"
    "ds_read_b64_tr_b16 a[162:163], v244 offset:6144\n"
    "v_cvt_pk_bf16_f32 v189, v190, v191\n"
    "v_mfma_f32_16x16x32_bf16 v[8:11], a[168:171], v[156:159], v[8:11]\n"
    "v_exp_f32_e32 v198, v198\n"
    "v_mfma_f32_16x16x32_bf16 v[4:7], a[168:171], v[148:151], v[4:7]\n"
    "v_exp_f32_e32 v199, v199\n"
    "v_mfma_f32_16x16x32_bf16 v[4:7], a[172:175], v[152:155], v[4:7]\n"
    "v_cvt_pk_bf16_f32 v196, v196, v197\n"
    "v_mfma_f32_16x16x32_bf16 v[0:3], a[172:175], v[144:147], v[0:3]\n"
    "ds_read_b64_tr_b16 a[164:165], v244 offset:36864\n"
    "v_mfma_f32_16x16x32_bf16 v[0:3], a[168:171], v[140:143], v[0:3]\n"
    "s_waitcnt lgkmcnt(7)\n"
    "v_exp_f32_e32 v204, v204\n"
    "v_mfma_f32_16x16x32_bf16 v[20:23], a[176:179], v[140:143], v[20:23]\n"
    "ds_read_b64_tr_b16 a[168:169], v245 offset:4096\n"
    "v_mfma_f32_16x16x32_bf16 v[20:23], a[180:183], v[144:147], v[20:23]\n"
    "ds_read_b64_tr_b16 a[166:167], v244 offset:38912\n"
    "v_mfma_f32_16x16x32_bf16 v[24:27], a[180:183], v[152:155], v[24:27]\n"
    "v_exp_f32_e32 v205, v205\n"
    "v_mfma_f32_16x16x32_bf16 v[24:27], a[176:179], v[148:151], v[24:27]\n"
    "v_cvt_pk_bf16_f32 v197, v198, v199\n"
    "v_mfma_f32_16x16x32_bf16 v[28:31], a[176:179], v[156:159], v[28:31]\n"
    "ds_read_b64_tr_b16 a[170:171], v245 offset:6144\n"
    "v_mfma_f32_16x16x32_bf16 v[28:31], a[180:183], v[160:163], v[28:31]\n"
    "v_exp_f32_e32 v206, v206\n"
    "v_mfma_f32_16x16x32_bf16 v[32:35], a[180:183], v[168:171], v[32:35]\n"
    "v_exp_f32_e32 v207, v207\n"
    "v_mfma_f32_16x16x32_bf16 v[32:35], a[176:179], v[164:167], v[32:35]\n"
    "v_cvt_pk_bf16_f32 v204, v204, v205\n"
    "v_mfma_f32_16x16x32_bf16 v[36:39], a[176:179], v[172:175], v[36:39]\n"
    "ds_read_b64_tr_b16 a[172:173], v245 offset:36864\n"
    "v_mfma_f32_16x16x32_bf16 v[36:39], a[180:183], v[176:179], v[36:39]\n"
    "s_waitcnt vmcnt(20)\n"
    "ds_read_b64_tr_b16 a[174:175], v245 offset:38912\n"
    "v_smfmac_f32_16x16x64_bf16 v[224:227], a[232:235], a[216:223], v238\n"
    "s_waitcnt lgkmcnt(8)\n"
    "v_mad_u32_u16 v234, v231, s90, v239 op_sel:[0,0,0,0]\n"
    "v_mfma_f32_16x16x32_bf16 v[56:59], a[188:191], v[176:179], v[56:59]\n"
    "s_bitcmp1_b32 s86, 8\n"
    "buffer_load_dwordx4 a[216:219], v234, s[20:23], 0 offen sc1\n"
    "v_mad_u32_u16 v235, v231, s90, v239 op_sel:[1,0,0,0]\n"
    "v_mfma_f32_16x16x32_bf16 v[56:59], a[184:187], v[172:175], v[56:59]\n"
    "s_cselect_b64 exec, -1, 0\n"
    "v_cvt_pk_bf16_f32 v236, v224, v225\n"
    "v_cvt_pk_bf16_f32 v237, v226, v227\n"
    "v_mfma_f32_16x16x32_bf16 v[52:55], a[184:187], v[164:167], v[52:55]\n"
    "s_cselect_b32 s88, 512, 0\n"
    "v_mov_b64_e32 v[224:225], 0\n"
    "v_mov_b64_e32 v[226:227], 0\n"
    "v_mfma_f32_16x16x32_bf16 v[52:55], a[188:191], v[168:171], v[52:55]\n"
    "buffer_store_dwordx2 v[236:237], v252, s[8:11], s87 offen sc1\n"
    "v_mfma_f32_16x16x32_bf16 v[48:51], a[188:191], v[160:163], v[48:51]\n"
    "s_mov_b64 exec, -1\n"
    "ds_read_b64_tr_b16 a[176:177], v246 offset:4096\n"
    "v_mfma_f32_16x16x32_bf16 v[48:51], a[184:187], v[156:159], v[48:51]\n"
    "s_add_u32 s87, s87, s88\n"
    "ds_read_b64_tr_b16 a[178:179], v246 offset:6144\n"
    "v_mfma_f32_16x16x32_bf16 v[44:47], a[184:187], v[148:151], v[44:47]\n"
    "buffer_load_dwordx4 a[220:223], v235, s[20:23], 0 offen sc1\n"
    "v_mfma_f32_16x16x32_bf16 v[44:47], a[188:191], v[152:155], v[44:47]\n"
    "ds_read_b64_tr_b16 a[180:181], v246 offset:36864\n"
    "v_mfma_f32_16x16x32_bf16 v[40:43], a[188:191], v[144:147], v[40:43]\n"
    "v_exp_f32_e32 v212, v212\n"
    "v_mfma_f32_16x16x32_bf16 v[40:43], a[184:187], v[140:143], v[40:43]\n"
    "s_waitcnt lgkmcnt(6)\n"
    "v_exp_f32_e32 v213, v213\n"
    "v_mfma_f32_16x16x32_bf16 v[60:63], a[160:163], v[140:143], v[60:63]\n"
    "ds_read_b64_tr_b16 a[182:183], v246 offset:38912\n"
    "v_cvt_pk_bf16_f32 v205, v206, v207\n"
    "v_mfma_f32_16x16x32_bf16 v[60:63], a[164:167], v[144:147], v[60:63]\n"
    "ds_read_b64_tr_b16 a[184:185], v247 offset:4096\n"
    "v_mfma_f32_16x16x32_bf16 v[64:67], a[164:167], v[152:155], v[64:67]\n"
    "ds_read_b64_tr_b16 a[186:187], v247 offset:6144\n"
    "v_mfma_f32_16x16x32_bf16 v[64:67], a[160:163], v[148:151], v[64:67]\n"
    "v_exp_f32_e32 v214, v214\n"
    "v_mfma_f32_16x16x32_bf16 v[68:71], a[160:163], v[156:159], v[68:71]\n"
    "v_exp_f32_e32 v215, v215\n"
    "v_mfma_f32_16x16x32_bf16 v[68:71], a[164:167], v[160:163], v[68:71]\n"
    "ds_read_b64_tr_b16 a[188:189], v247 offset:36864\n"
    "v_cvt_pk_bf16_f32 v212, v212, v213\n"
    "v_mfma_f32_16x16x32_bf16 v[72:75], a[164:167], v[168:171], v[72:75]\n"
    "ds_read_b64_tr_b16 a[190:191], v247 offset:38912\n"
    "v_cvt_pk_bf16_f32 v213, v214, v215\n"
    "v_mfma_f32_16x16x32_bf16 v[72:75], a[160:163], v[164:167], v[72:75]\n"
    "s_waitcnt vmcnt(30)\n"
    "v_mfma_f32_16x16x32_bf16 v[76:79], a[160:163], v[172:175], v[76:79]\n"
    "s_barrier\n"
    "v_mfma_f32_16x16x32_bf16 v[76:79], a[164:167], v[176:179], v[76:79]\n"
    "s_waitcnt lgkmcnt(8)\n"
    "s_mov_b32 m0, s42\n"
    "v_mfma_f32_16x16x32_bf16 v[96:99], a[172:175], v[176:179], v[96:99]\n"
    "v_exp_f32_e32 v184, v184\n"
    "v_mfma_f32_16x16x32_bf16 v[96:99], a[168:171], v[172:175], v[96:99]\n"
    "v_exp_f32_e32 v185, v185\n"
    "v_mfma_f32_16x16x32_bf16 v[92:95], a[168:171], v[164:167], v[92:95]\n"
    "v_exp_f32_e32 v186, v186\n"
    "v_mfma_f32_16x16x32_bf16 v[92:95], a[172:175], v[168:171], v[92:95]\n"
    "ds_read_b128 a[160:163], v240 offset:16384\n"
    "ds_write_addtid_b32 v180 offset:0\n"
    "v_mfma_f32_16x16x32_bf16 v[88:91], a[172:175], v[160:163], v[88:91]\n"
    "v_exp_f32_e32 v187, v187\n"
    "v_mfma_f32_16x16x32_bf16 v[88:91], a[168:171], v[156:159], v[88:91]\n"
    "ds_write_addtid_b32 v181 offset:256\n"
    "v_cvt_pk_bf16_f32 v182, v184, v185\n"
    "v_mfma_f32_16x16x32_bf16 v[84:87], a[168:171], v[148:151], v[84:87]\n"
    "v_exp_f32_e32 v192, v192\n"
    "v_mfma_f32_16x16x32_bf16 v[84:87], a[172:175], v[152:155], v[84:87]\n"
    "v_exp_f32_e32 v193, v193\n"
    "v_mfma_f32_16x16x32_bf16 v[80:83], a[172:175], v[144:147], v[80:83]\n"
    "ds_write_addtid_b32 v188 offset:1024\n"
    "v_cvt_pk_bf16_f32 v183, v186, v187\n"
    "v_mfma_f32_16x16x32_bf16 v[80:83], a[168:171], v[140:143], v[80:83]\n"
    "s_waitcnt vmcnt(20)\n"
    "ds_read_b128 a[164:167], v241 offset:16384\n"
    "ds_write_addtid_b32 v189 offset:1280\n"
    "v_smfmac_f32_16x16x64_bf16 v[224:227], a[232:235], a[224:231], v238\n"
    "s_waitcnt lgkmcnt(10)\n"
    "ds_write_addtid_b32 v196 offset:2048\n"
    "v_mad_u32_u16 v234, v232, s90, v239 op_sel:[0,0,0,0]\n"
    "v_mfma_f32_16x16x32_bf16 v[100:103], a[176:179], v[140:143], v[100:103]\n"
    "s_bitcmp1_b32 s86, 9\n"
    "buffer_load_dwordx4 a[224:227], v234, s[20:23], 0 offen sc1\n"
    "v_mad_u32_u16 v235, v232, s90, v239 op_sel:[1,0,0,0]\n"
    "v_mfma_f32_16x16x32_bf16 v[100:103], a[180:183], v[144:147], v[100:103]\n"
    "s_cselect_b64 exec, -1, 0\n"
    "v_cvt_pk_bf16_f32 v236, v224, v225\n"
    "v_cvt_pk_bf16_f32 v237, v226, v227\n"
    "v_mfma_f32_16x16x32_bf16 v[104:107], a[180:183], v[152:155], v[104:107]\n"
    "s_cselect_b32 s88, 512, 0\n"
    "v_mov_b64_e32 v[224:225], 0\n"
    "v_mov_b64_e32 v[226:227], 0\n"
    "v_mfma_f32_16x16x32_bf16 v[104:107], a[176:179], v[148:151], v[104:107]\n"
    "buffer_store_dwordx2 v[236:237], v252, s[8:11], s87 offen sc1\n"
    "v_mfma_f32_16x16x32_bf16 v[108:111], a[176:179], v[156:159], v[108:111]\n"
    "s_mov_b64 exec, -1\n"
    "ds_read_b128 a[168:171], v240 offset:20480\n"
    "ds_write_addtid_b32 v197 offset:2304\n"
    "v_mfma_f32_16x16x32_bf16 v[108:111], a[180:183], v[160:163], v[108:111]\n"
    "s_add_u32 s87, s87, s88\n"
    "v_exp_f32_e32 v194, v194\n"
    "v_mfma_f32_16x16x32_bf16 v[112:115], a[180:183], v[168:171], v[112:115]\n"
    "buffer_load_dwordx4 a[228:231], v235, s[20:23], 0 offen sc1\n"
    "ds_write_addtid_b32 v204 offset:3072\n"
    "v_mfma_f32_16x16x32_bf16 v[112:115], a[176:179], v[164:167], v[112:115]\n"
    "ds_write_addtid_b32 v205 offset:3328\n"
    "v_mfma_f32_16x16x32_bf16 v[116:119], a[176:179], v[172:175], v[116:119]\n"
    "v_exp_f32_e32 v195, v195\n"
    "v_mfma_f32_16x16x32_bf16 v[116:119], a[180:183], v[176:179], v[116:119]\n"
    "s_waitcnt lgkmcnt(11)\n"
    "ds_read_b128 a[172:175], v241 offset:20480\n"
    "v_cvt_pk_bf16_f32 v190, v192, v193\n"
    "v_mfma_f32_16x16x32_bf16 v[136:139], a[188:191], v[176:179], v[136:139]\n"
    "v_exp_f32_e32 v200, v200\n"
    "v_mfma_f32_16x16x32_bf16 v[136:139], a[184:187], v[172:175], v[136:139]\n"
    "v_exp_f32_e32 v201, v201\n"
    "v_mfma_f32_16x16x32_bf16 v[132:135], a[184:187], v[164:167], v[132:135]\n"
    "ds_read_b128 a[176:179], v242 offset:16384\n"
    "v_cvt_pk_bf16_f32 v191, v194, v195\n"
    "v_mfma_f32_16x16x32_bf16 v[132:135], a[188:191], v[168:171], v[132:135]\n"
    "v_exp_f32_e32 v202, v202\n"
    "v_mfma_f32_16x16x32_bf16 v[128:131], a[188:191], v[160:163], v[128:131]\n"
    "v_exp_f32_e32 v203, v203\n"
    "v_mfma_f32_16x16x32_bf16 v[128:131], a[184:187], v[156:159], v[128:131]\n"
    "ds_write_addtid_b32 v212 offset:4096\n"
    "v_cvt_pk_bf16_f32 v198, v200, v201\n"
    "v_mfma_f32_16x16x32_bf16 v[124:127], a[184:187], v[148:151], v[124:127]\n"
    "v_exp_f32_e32 v208, v208\n"
    "v_mfma_f32_16x16x32_bf16 v[124:127], a[188:191], v[152:155], v[124:127]\n"
    "ds_read_b128 a[180:183], v243 offset:16384\n"
    "ds_write_addtid_b32 v213 offset:4352\n"
    "v_mfma_f32_16x16x32_bf16 v[120:123], a[188:191], v[144:147], v[120:123]\n"
    "v_exp_f32_e32 v209, v209\n"
    "v_mfma_f32_16x16x32_bf16 v[120:123], a[184:187], v[140:143], v[120:123]\n"
    "s_waitcnt lgkmcnt(11)\n"
    "ds_write_addtid_b32 v182 offset:512\n"
    "v_cvt_pk_bf16_f32 v199, v202, v203\n"
    "v_mfma_f32_16x16x32_bf16 v[172:175], a[160:163], a[128:131], v[220:223]\n"
    "ds_write_addtid_b32 v183 offset:768\n"
    "v_mfma_f32_16x16x32_bf16 v[172:175], a[164:167], a[132:135], v[172:175]\n"
    "v_exp_f32_e32 v210, v210\n"
    "v_mfma_f32_16x16x32_bf16 v[164:167], a[164:167], a[100:103], v[220:223]\n"
    "ds_write_addtid_b32 v190 offset:1536\n"
    "v_mfma_f32_16x16x32_bf16 v[164:167], a[160:163], a[96:99], v[164:167]\n"
    "ds_read_b128 a[184:187], v242 offset:20480\n"
    "v_mfma_f32_16x16x32_bf16 v[156:159], a[160:163], a[64:67], v[220:223]\n"
    "v_exp_f32_e32 v211, v211\n"
    "v_mfma_f32_16x16x32_bf16 v[156:159], a[164:167], a[68:71], v[156:159]\n"
    "ds_write_addtid_b32 v191 offset:1792\n"
    "v_cvt_pk_bf16_f32 v206, v208, v209\n"
    "v_mfma_f32_16x16x32_bf16 v[148:151], a[164:167], a[36:39], v[220:223]\n"
    "v_exp_f32_e32 v216, v216\n"
    "v_mfma_f32_16x16x32_bf16 v[148:151], a[160:163], a[32:35], v[148:151]\n"
    "v_exp_f32_e32 v217, v217\n"
    "v_mfma_f32_16x16x32_bf16 v[140:143], a[160:163], a[0:3], v[220:223]\n"
    "ds_write_addtid_b32 v198 offset:2560\n"
    "v_cvt_pk_bf16_f32 v207, v210, v211\n"
    "v_mfma_f32_16x16x32_bf16 v[140:143], a[164:167], a[4:7], v[140:143]\n"
    "s_waitcnt vmcnt(20)\n"
    "v_exp_f32_e32 v218, v218\n"
    "v_smfmac_f32_16x16x64_bf16 v[224:227], a[232:235], a[192:199], v238\n"
    "s_waitcnt lgkmcnt(10)\n"
    "ds_read_b128 a[188:191], v243 offset:20480\n"
    "v_mfma_f32_16x16x32_bf16 v[140:143], a[168:171], a[8:11], v[140:143]\n"
    "s_bitcmp1_b32 s86, 10\n"
    "ds_read_b128 a[160:163], v240 offset:18432\n"
    "v_mfma_f32_16x16x32_bf16 v[140:143], a[172:175], a[12:15], v[140:143]\n"
    "s_cselect_b64 exec, -1, 0\n"
    "v_cvt_pk_bf16_f32 v236, v224, v225\n"
    "v_cvt_pk_bf16_f32 v237, v226, v227\n"
    "v_mfma_f32_16x16x32_bf16 v[148:151], a[172:175], a[44:47], v[148:151]\n"
    "s_cselect_b32 s88, 512, 0\n"
    "v_mov_b64_e32 v[224:225], 0\n"
    "v_mov_b64_e32 v[226:227], 0\n"
    "v_mfma_f32_16x16x32_bf16 v[148:151], a[168:171], a[40:43], v[148:151]\n"
    "buffer_store_dwordx2 v[236:237], v252, s[8:11], s87 offen sc1\n"
    "v_mfma_f32_16x16x32_bf16 v[156:159], a[168:171], a[72:75], v[156:159]\n"
    "s_mov_b64 exec, -1\n"
    "ds_read2_b32 v[228:229], v251 offset0:20 offset1:22\n"
    "v_mfma_f32_16x16x32_bf16 v[156:159], a[172:175], a[76:79], v[156:159]\n"
    "s_add_u32 s87, s87, s88\n"
    "ds_read2_b32 v[230:231], v251 offset0:24 offset1:26\n"
    "v_mfma_f32_16x16x32_bf16 v[164:167], a[172:175], a[108:111], v[164:167]\n"
    "ds_read_b32 v232, v251 offset:112\n"
    "v_mfma_f32_16x16x32_bf16 v[164:167], a[168:171], a[104:107], v[164:167]\n"
    "v_exp_f32_e32 v219, v219\n"
    "v_mfma_f32_16x16x32_bf16 v[172:175], a[168:171], a[136:139], v[172:175]\n"
    "ds_write_addtid_b32 v199 offset:2816\n"
    "v_cvt_pk_bf16_f32 v214, v216, v217\n"
    "v_mfma_f32_16x16x32_bf16 v[172:175], a[172:175], a[140:143], v[172:175]\n"
    "s_waitcnt lgkmcnt(13)\n"
    "ds_read_b128 a[164:167], v241 offset:18432\n"
    "v_mfma_f32_16x16x32_bf16 v[172:175], a[176:179], a[144:147], v[172:175]\n"
    "ds_write_addtid_b32 v206 offset:3584\n"
    "v_cvt_pk_bf16_f32 v215, v218, v219\n"
    "v_mfma_f32_16x16x32_bf16 v[172:175], a[180:183], a[148:151], v[172:175]\n"
    "ds_read_b128 a[168:171], v240 offset:22528\n"
    "v_mfma_f32_16x16x32_bf16 v[164:167], a[180:183], a[116:119], v[164:167]\n"
    "s_waitcnt lgkmcnt(6)\n"
    "ds_write_addtid_b32 v207 offset:3840\n"
    "v_mad_u32_u16 v234, v228, s90, v239 op_sel:[0,0,0,0]\n"
    "v_mfma_f32_16x16x32_bf16 v[164:167], a[176:179], a[112:115], v[164:167]\n"
    "buffer_load_dwordx4 a[192:195], v234, s[20:23], 0 offen sc1\n"
    "v_mad_u32_u16 v235, v228, s90, v239 op_sel:[1,0,0,0]\n"
    "v_mfma_f32_16x16x32_bf16 v[156:159], a[176:179], a[80:83], v[156:159]\n"
    "ds_write_addtid_b32 v214 offset:4608\n"
    "v_mfma_f32_16x16x32_bf16 v[156:159], a[180:183], a[84:87], v[156:159]\n"
    "ds_write_addtid_b32 v215 offset:4864\n"
    "v_mfma_f32_16x16x32_bf16 v[148:151], a[180:183], a[52:55], v[148:151]\n"
    "buffer_load_dwordx4 a[196:199], v235, s[20:23], 0 offen sc1\n"
    "v_mfma_f32_16x16x32_bf16 v[148:151], a[176:179], a[48:51], v[148:151]\n"
    "v_mfma_f32_16x16x32_bf16 v[140:143], a[176:179], a[16:19], v[140:143]\n"
    "s_waitcnt lgkmcnt(0)\n"
    "v_mfma_f32_16x16x32_bf16 v[140:143], a[180:183], a[20:23], v[140:143]\n"
    "s_barrier\n"
    "v_mfma_f32_16x16x32_bf16 v[140:143], a[184:187], a[24:27], v[140:143]\n"
    "s_add_u32 m0, s43, 0\n"
    "ds_read_b128 a[172:175], v241 offset:22528\n"
    "v_mfma_f32_16x16x32_bf16 v[140:143], a[188:191], a[28:31], v[140:143]\n"
    "ds_read2st64_b32 v[184:185], v249 offset0:0 offset1:1\n"
    "v_mfma_f32_16x16x32_bf16 v[148:151], a[188:191], a[60:63], v[148:151]\n"
    "buffer_load_dwordx4 v248, s[8:11], s40 offen offset:0 sc1 lds\n"
    "v_mfma_f32_16x16x32_bf16 v[148:151], a[184:187], a[56:59], v[148:151]\n"
    "ds_read_b128 a[176:179], v242 offset:18432\n"
    "v_mfma_f32_16x16x32_bf16 v[156:159], a[184:187], a[88:91], v[156:159]\n"
    "v_mfma_f32_16x16x32_bf16 v[156:159], a[188:191], a[92:95], v[156:159]\n"
    "v_mfma_f32_16x16x32_bf16 v[164:167], a[188:191], a[124:127], v[164:167]\n"
    "ds_read2st64_b32 v[186:187], v249 offset0:2 offset1:3\n"
    "v_mfma_f32_16x16x32_bf16 v[164:167], a[184:187], a[120:123], v[164:167]\n"
    "buffer_load_dwordx4 v248, s[8:11], s40 offen offset:1024 sc1 lds\n"
    "v_mfma_f32_16x16x32_bf16 v[172:175], a[184:187], a[152:155], v[172:175]\n"
    "v_mfma_f32_16x16x32_bf16 v[172:175], a[188:191], a[156:159], v[172:175]\n"
    "v_mfma_f32_16x16x32_bf16 v[176:179], a[160:163], a[128:131], v[220:223]\n"
    "ds_read_b128 a[180:183], v243 offset:18432\n"
    "v_mfma_f32_16x16x32_bf16 v[176:179], a[164:167], a[132:135], v[176:179]\n"
    "ds_read2st64_b32 v[192:193], v249 offset0:4 offset1:5\n"
    "v_mfma_f32_16x16x32_bf16 v[168:171], a[164:167], a[100:103], v[220:223]\n"
    "buffer_load_dwordx4 v248, s[8:11], s40 offen offset:2048 sc1 lds\n"
    "v_mfma_f32_16x16x32_bf16 v[168:171], a[160:163], a[96:99], v[168:171]\n"
    "ds_read_b128 a[184:187], v242 offset:22528\n"
    "v_mfma_f32_16x16x32_bf16 v[160:163], a[160:163], a[64:67], v[220:223]\n"
    "v_exp_f32_e32 v140, v140\n"
    "v_mfma_f32_16x16x32_bf16 v[160:163], a[164:167], a[68:71], v[160:163]\n"
    "ds_read2st64_b32 v[194:195], v249 offset0:6 offset1:7\n"
    "v_mfma_f32_16x16x32_bf16 v[152:155], a[164:167], a[36:39], v[220:223]\n"
    "ds_read2st64_b32 v[200:201], v249 offset0:8 offset1:9\n"
    "v_mfma_f32_16x16x32_bf16 v[152:155], a[160:163], a[32:35], v[152:155]\n"
    "buffer_load_dwordx4 v248, s[8:11], s40 offen offset:3072 sc1 lds\n"
    "v_mfma_f32_16x16x32_bf16 v[144:147], a[160:163], a[0:3], v[220:223]\n"
    "s_add_u32 m0, s43, 4096\n"
    "ds_read2st64_b32 v[202:203], v249 offset0:10 offset1:11\n"
    "v_mfma_f32_16x16x32_bf16 v[144:147], a[164:167], a[4:7], v[144:147]\n"
    "s_waitcnt vmcnt(16)\n"
    "ds_read_b128 a[188:191], v243 offset:22528\n"
    "v_smfmac_f32_16x16x64_bf16 v[224:227], a[232:235], a[200:207], v238\n"
    "s_waitcnt lgkmcnt(10)\n"
    "v_mad_u32_u16 v234, v229, s90, v239 op_sel:[0,0,0,0]\n"
    "v_mfma_f32_16x16x32_bf16 v[144:147], a[168:171], a[8:11], v[144:147]\n"
    "s_bitcmp1_b32 s86, 11\n"
    "buffer_load_dwordx4 a[200:203], v234, s[20:23], 0 offen sc1\n"
    "v_mad_u32_u16 v235, v229, s90, v239 op_sel:[1,0,0,0]\n"
    "v_mfma_f32_16x16x32_bf16 v[144:147], a[172:175], a[12:15], v[144:147]\n"
    "s_cselect_b64 exec, -1, 0\n"
    "v_cvt_pk_bf16_f32 v236, v224, v225\n"
    "v_cvt_pk_bf16_f32 v237, v226, v227\n"
    "v_mfma_f32_16x16x32_bf16 v[152:155], a[172:175], a[44:47], v[152:155]\n"
    "s_cselect_b32 s88, 512, 0\n"
    "v_mov_b64_e32 v[224:225], 0\n"
    "v_mov_b64_e32 v[226:227], 0\n"
    "v_mfma_f32_16x16x32_bf16 v[152:155], a[168:171], a[40:43], v[152:155]\n"
    "buffer_store_dwordx2 v[236:237], v252, s[8:11], s87 offen sc1\n"
    "v_mfma_f32_16x16x32_bf16 v[160:163], a[168:171], a[72:75], v[160:163]\n"
    "s_mov_b64 exec, -1\n"
    "ds_read_b64_tr_b16 a[160:161], v244 offset:8192\n"
    "v_mfma_f32_16x16x32_bf16 v[160:163], a[172:175], a[76:79], v[160:163]\n"
    "s_add_u32 s87, s87, s88\n"
    "ds_read_b64_tr_b16 a[162:163], v244 offset:10240\n"
    "v_mfma_f32_16x16x32_bf16 v[168:171], a[172:175], a[108:111], v[168:171]\n"
    "s_add_u32 s40, s40, 0x8000\n"
    "buffer_load_dwordx4 v248, s[8:11], s41 offen offset:0 sc1 lds\n"
    "v_mfma_f32_16x16x32_bf16 v[168:171], a[168:171], a[104:107], v[168:171]\n"
    "ds_read_b64_tr_b16 a[164:165], v244 offset:40960\n"
    "v_mfma_f32_16x16x32_bf16 v[176:179], a[168:171], a[136:139], v[176:179]\n"
    "ds_read2st64_b32 v[208:209], v249 offset0:12 offset1:13\n"
    "v_mfma_f32_16x16x32_bf16 v[176:179], a[172:175], a[140:143], v[176:179]\n"
    "s_waitcnt lgkmcnt(10)\n"
    "ds_read2st64_b32 v[210:211], v249 offset0:14 offset1:15\n"
    "v_mfma_f32_16x16x32_bf16 v[176:179], a[176:179], a[144:147], v[176:179]\n"
    "ds_read_b64_tr_b16 a[166:167], v244 offset:43008\n"
    "v_mfma_f32_16x16x32_bf16 v[176:179], a[180:183], a[148:151], v[176:179]\n"
    "buffer_load_dwordx4 v248, s[8:11], s41 offen offset:1024 sc1 lds\n"
    "v_mfma_f32_16x16x32_bf16 v[168:171], a[180:183], a[116:119], v[168:171]\n"
    "ds_read_b64_tr_b16 a[168:169], v245 offset:8192\n"
    "v_mfma_f32_16x16x32_bf16 v[168:171], a[176:179], a[112:115], v[168:171]\n"
    "v_exp_f32_e32 v141, v141\n"
    "v_mfma_f32_16x16x32_bf16 v[160:163], a[176:179], a[80:83], v[160:163]\n"
    "ds_read_b64_tr_b16 a[170:171], v245 offset:10240\n"
    "v_mfma_f32_16x16x32_bf16 v[160:163], a[180:183], a[84:87], v[160:163]\n"
    "ds_read2st64_b32 v[216:217], v249 offset0:16 offset1:17\n"
    "v_mfma_f32_16x16x32_bf16 v[152:155], a[180:183], a[52:55], v[152:155]\n"
    "buffer_load_dwordx4 v248, s[8:11], s41 offen offset:2048 sc1 lds\n"
    "v_mfma_f32_16x16x32_bf16 v[152:155], a[176:179], a[48:51], v[152:155]\n"
    "v_exp_f32_e32 v142, v142\n"
    "v_mfma_f32_16x16x32_bf16 v[144:147], a[176:179], a[16:19], v[144:147]\n"
    "ds_read_b64_tr_b16 a[172:173], v245 offset:40960\n"
    "v_mfma_f32_16x16x32_bf16 v[144:147], a[180:183], a[20:23], v[144:147]\n"
    "s_waitcnt lgkmcnt(10)\n"
    "ds_read2st64_b32 v[218:219], v249 offset0:18 offset1:19\n"
    "v_mfma_f32_16x16x32_bf16 v[144:147], a[184:187], a[24:27], v[144:147]\n"
    "ds_read_b64_tr_b16 a[176:177], v246 offset:8192\n"
    "v_mfma_f32_16x16x32_bf16 v[144:147], a[188:191], a[28:31], v[144:147]\n"
    "buffer_load_dwordx4 v248, s[8:11], s41 offen offset:3072 sc1 lds\n"
    "v_mfma_f32_16x16x32_bf16 v[152:155], a[188:191], a[60:63], v[152:155]\n"
    "ds_read_b64_tr_b16 a[174:175], v245 offset:43008\n"
    "v_mfma_f32_16x16x32_bf16 v[152:155], a[184:187], a[56:59], v[152:155]\n"
    "s_add_u32 s41, s41, 0x8000\n"
    "v_exp_f32_e32 v143, v143\n"
    "v_mfma_f32_16x16x32_bf16 v[160:163], a[184:187], a[88:91], v[160:163]\n"
    "buffer_load_dwordx4 a[204:207], v235, s[20:23], 0 offen sc1\n"
    "v_cvt_pk_bf16_f32 v140, v140, v141\n"
    "v_mfma_f32_16x16x32_bf16 v[160:163], a[188:191], a[92:95], v[160:163]\n"
    "ds_read_b64_tr_b16 a[178:179], v246 offset:10240\n"
    "v_mfma_f32_16x16x32_bf16 v[168:171], a[188:191], a[124:127], v[168:171]\n"
    "v_exp_f32_e32 v148, v148\n"
    "v_mfma_f32_16x16x32_bf16 v[168:171], a[184:187], a[120:123], v[168:171]\n"
    "v_exp_f32_e32 v149, v149\n"
    "v_mfma_f32_16x16x32_bf16 v[176:179], a[184:187], a[152:155], v[176:179]\n"
    "ds_read_b64_tr_b16 a[180:181], v246 offset:40960\n"
    "v_cvt_pk_bf16_f32 v141, v142, v143\n"
    "v_mfma_f32_16x16x32_bf16 v[176:179], a[188:191], a[156:159], v[176:179]\n"
    "s_waitcnt vmcnt(20)\n"
    "ds_read_b64_tr_b16 a[182:183], v246 offset:43008\n"
    "v_smfmac_f32_16x16x64_bf16 v[224:227], a[232:235], a[208:215], v238\n"
    "s_waitcnt lgkmcnt(5)\n"
    "v_mad_u32_u16 v234, v230, s90, v239 op_sel:[0,0,0,0]\n"
    "v_mfma_f32_16x16x32_bf16 a[236:239], a[160:163], v[180:183], a[236:239]\n"
    "s_bitcmp1_b32 s86, 12\n"
    "buffer_load_dwordx4 a[208:211], v234, s[20:23], 0 offen sc1\n"
    "v_mad_u32_u16 v235, v230, s90, v239 op_sel:[1,0,0,0]\n"
    "v_mfma_f32_16x16x32_bf16 a[236:239], a[164:167], v[184:187], a[236:239]\n"
    "s_cselect_b64 exec, -1, 0\n"
    "v_cvt_pk_bf16_f32 v236, v224, v225\n"
    "v_cvt_pk_bf16_f32 v237, v226, v227\n"
    "v_mfma_f32_16x16x32_bf16 a[240:243], a[164:167], v[192:195], a[240:243]\n"
    "s_cselect_b32 s88, 512, 0\n"
    "v_mov_b64_e32 v[224:225], 0\n"
    "v_mov_b64_e32 v[226:227], 0\n"
    "v_mfma_f32_16x16x32_bf16 a[240:243], a[160:163], v[188:191], a[240:243]\n"
    "buffer_store_dwordx2 v[236:237], v252, s[8:11], s87 offen sc1\n"
    "v_mfma_f32_16x16x32_bf16 a[244:247], a[160:163], v[196:199], a[244:247]\n"
    "s_mov_b64 exec, -1\n"
    "ds_read_b64_tr_b16 a[184:185], v247 offset:8192\n"
    "v_mfma_f32_16x16x32_bf16 a[244:247], a[164:167], v[200:203], a[244:247]\n"
    "s_add_u32 s87, s87, s88\n"
    "ds_read_b64_tr_b16 a[186:187], v247 offset:10240\n"
    "v_mfma_f32_16x16x32_bf16 a[248:251], a[164:167], v[208:211], a[248:251]\n"
    "buffer_load_dwordx4 a[212:215], v235, s[20:23], 0 offen sc1\n"
    "v_mfma_f32_16x16x32_bf16 a[248:251], a[160:163], v[204:207], a[248:251]\n"
    "ds_read_b64_tr_b16 a[188:189], v247 offset:40960\n"
    "v_mfma_f32_16x16x32_bf16 a[252:255], a[160:163], v[212:215], a[252:255]\n"
    "v_exp_f32_e32 v150, v150\n"
    "v_mfma_f32_16x16x32_bf16 a[252:255], a[164:167], v[216:219], a[252:255]\n"
    "s_waitcnt lgkmcnt(6)\n"
    "v_exp_f32_e32 v151, v151\n"
    "v_mfma_f32_16x16x32_bf16 v[16:19], a[172:175], v[216:219], v[16:19]\n"
    "ds_read_b64_tr_b16 a[190:191], v247 offset:43008\n"
    "v_cvt_pk_bf16_f32 v148, v148, v149\n"
    "v_mfma_f32_16x16x32_bf16 v[16:19], a[168:171], v[212:215], v[16:19]\n"
    "ds_read_b64_tr_b16 a[160:161], v244 offset:12288\n"
    "v_mfma_f32_16x16x32_bf16 v[12:15], a[168:171], v[204:207], v[12:15]\n"
    "v_exp_f32_e32 v156, v156\n"
    "v_mfma_f32_16x16x32_bf16 v[12:15], a[172:175], v[208:211], v[12:15]\n"
    "v_exp_f32_e32 v157, v157\n"
    "v_mfma_f32_16x16x32_bf16 v[8:11], a[172:175], v[200:203], v[8:11]\n"
    "ds_read_b64_tr_b16 a[162:163], v244 offset:14336\n"
    "v_cvt_pk_bf16_f32 v149, v150, v151\n"
    "v_mfma_f32_16x16x32_bf16 v[8:11], a[168:171], v[196:199], v[8:11]\n"
    "v_exp_f32_e32 v158, v158\n"
    "v_mfma_f32_16x16x32_bf16 v[4:7], a[168:171], v[188:191], v[4:7]\n"
    "v_exp_f32_e32 v159, v159\n"
    "v_mfma_f32_16x16x32_bf16 v[4:7], a[172:175], v[192:195], v[4:7]\n"
    "v_cvt_pk_bf16_f32 v156, v156, v157\n"
    "v_mfma_f32_16x16x32_bf16 v[0:3], a[172:175], v[184:187], v[0:3]\n"
    "ds_read_b64_tr_b16 a[164:165], v244 offset:45056\n"
    "v_mfma_f32_16x16x32_bf16 v[0:3], a[168:171], v[180:183], v[0:3]\n"
    "s_waitcnt lgkmcnt(7)\n"
    "v_exp_f32_e32 v164, v164\n"
    "v_mfma_f32_16x16x32_bf16 v[20:23], a[176:179], v[180:183], v[20:23]\n"
    "ds_read_b64_tr_b16 a[168:169], v245 offset:12288\n"
    "v_mfma_f32_16x16x32_bf16 v[20:23], a[180:183], v[184:187], v[20:23]\n"
    "ds_read_b64_tr_b16 a[166:167], v244 offset:47104\n"
    "v_mfma_f32_16x16x32_bf16 v[24:27], a[180:183], v[192:195], v[24:27]\n"
    "v_exp_f32_e32 v165, v165\n"
    "v_mfma_f32_16x16x32_bf16 v[24:27], a[176:179], v[188:191], v[24:27]\n"
    "v_cvt_pk_bf16_f32 v157, v158, v159\n"
    "v_mfma_f32_16x16x32_bf16 v[28:31], a[176:179], v[196:199], v[28:31]\n"
    "ds_read_b64_tr_b16 a[170:171], v245 offset:14336\n"
    "v_mfma_f32_16x16x32_bf16 v[28:31], a[180:183], v[200:203], v[28:31]\n"
    "v_exp_f32_e32 v166, v166\n"
    "v_mfma_f32_16x16x32_bf16 v[32:35], a[180:183], v[208:211], v[32:35]\n"
    "v_exp_f32_e32 v167, v167\n"
    "v_mfma_f32_16x16x32_bf16 v[32:35], a[176:179], v[204:207], v[32:35]\n"
    "v_cvt_pk_bf16_f32 v164, v164, v165\n"
    "v_mfma_f32_16x16x32_bf16 v[36:39], a[176:179], v[212:215], v[36:39]\n"
    "ds_read_b64_tr_b16 a[172:173], v245 offset:45056\n"
    "v_mfma_f32_16x16x32_bf16 v[36:39], a[180:183], v[216:219], v[36:39]\n"
    "s_waitcnt vmcnt(20)\n"
    "ds_read_b64_tr_b16 a[174:175], v245 offset:47104\n"
    "v_smfmac_f32_16x16x64_bf16 v[224:227], a[232:235], a[216:223], v238\n"
    "s_waitcnt lgkmcnt(8)\n"
    "v_mad_u32_u16 v234, v231, s90, v239 op_sel:[0,0,0,0]\n"
    "v_mfma_f32_16x16x32_bf16 v[56:59], a[188:191], v[216:219], v[56:59]\n"
    "s_bitcmp1_b32 s86, 13\n"
    "buffer_load_dwordx4 a[216:219], v234, s[20:23], 0 offen sc1\n"
    "v_mad_u32_u16 v235, v231, s90, v239 op_sel:[1,0,0,0]\n"
    "v_mfma_f32_16x16x32_bf16 v[56:59], a[184:187], v[212:215], v[56:59]\n"
    "s_cselect_b64 exec, -1, 0\n"
    "v_cvt_pk_bf16_f32 v236, v224, v225\n"
    "v_cvt_pk_bf16_f32 v237, v226, v227\n"
    "v_mfma_f32_16x16x32_bf16 v[52:55], a[184:187], v[204:207], v[52:55]\n"
    "s_cselect_b32 s88, 512, 0\n"
    "v_mov_b64_e32 v[224:225], 0\n"
    "v_mov_b64_e32 v[226:227], 0\n"
    "v_mfma_f32_16x16x32_bf16 v[52:55], a[188:191], v[208:211], v[52:55]\n"
    "buffer_store_dwordx2 v[236:237], v252, s[8:11], s87 offen sc1\n"
    "v_mfma_f32_16x16x32_bf16 v[48:51], a[188:191], v[200:203], v[48:51]\n"
    "s_mov_b64 exec, -1\n"
    "ds_read_b64_tr_b16 a[176:177], v246 offset:12288\n"
    "v_mfma_f32_16x16x32_bf16 v[48:51], a[184:187], v[196:199], v[48:51]\n"
    "s_add_u32 s87, s87, s88\n"
    "ds_read_b64_tr_b16 a[178:179], v246 offset:14336\n"
    "v_mfma_f32_16x16x32_bf16 v[44:47], a[184:187], v[188:191], v[44:47]\n"
    "buffer_load_dwordx4 a[220:223], v235, s[20:23], 0 offen sc1\n"
    "v_mfma_f32_16x16x32_bf16 v[44:47], a[188:191], v[192:195], v[44:47]\n"
    "ds_read_b64_tr_b16 a[180:181], v246 offset:45056\n"
    "v_mfma_f32_16x16x32_bf16 v[40:43], a[188:191], v[184:187], v[40:43]\n"
    "v_exp_f32_e32 v172, v172\n"
    "v_mfma_f32_16x16x32_bf16 v[40:43], a[184:187], v[180:183], v[40:43]\n"
    "s_waitcnt lgkmcnt(6)\n"
    "v_exp_f32_e32 v173, v173\n"
    "v_mfma_f32_16x16x32_bf16 v[60:63], a[160:163], v[180:183], v[60:63]\n"
    "ds_read_b64_tr_b16 a[182:183], v246 offset:47104\n"
    "v_cvt_pk_bf16_f32 v165, v166, v167\n"
    "v_mfma_f32_16x16x32_bf16 v[60:63], a[164:167], v[184:187], v[60:63]\n"
    "ds_read_b64_tr_b16 a[184:185], v247 offset:12288\n"
    "v_mfma_f32_16x16x32_bf16 v[64:67], a[164:167], v[192:195], v[64:67]\n"
    "ds_read_b64_tr_b16 a[186:187], v247 offset:14336\n"
    "v_mfma_f32_16x16x32_bf16 v[64:67], a[160:163], v[188:191], v[64:67]\n"
    "v_exp_f32_e32 v174, v174\n"
    "v_mfma_f32_16x16x32_bf16 v[68:71], a[160:163], v[196:199], v[68:71]\n"
    "v_exp_f32_e32 v175, v175\n"
    "v_mfma_f32_16x16x32_bf16 v[68:71], a[164:167], v[200:203], v[68:71]\n"
    "ds_read_b64_tr_b16 a[188:189], v247 offset:45056\n"
    "v_cvt_pk_bf16_f32 v172, v172, v173\n"
    "v_mfma_f32_16x16x32_bf16 v[72:75], a[164:167], v[208:211], v[72:75]\n"
    "ds_read_b64_tr_b16 a[190:191], v247 offset:47104\n"
    "v_cvt_pk_bf16_f32 v173, v174, v175\n"
    "v_mfma_f32_16x16x32_bf16 v[72:75], a[160:163], v[204:207], v[72:75]\n"
    "s_waitcnt vmcnt(30)\n"
    "v_mfma_f32_16x16x32_bf16 v[76:79], a[160:163], v[212:215], v[76:79]\n"
    "s_barrier\n"
    "v_mfma_f32_16x16x32_bf16 v[76:79], a[164:167], v[216:219], v[76:79]\n"
    "s_waitcnt lgkmcnt(8)\n"
    "s_mov_b32 m0, s42\n"
    "v_mfma_f32_16x16x32_bf16 v[96:99], a[172:175], v[216:219], v[96:99]\n"
    "v_exp_f32_e32 v144, v144\n"
    "v_mfma_f32_16x16x32_bf16 v[96:99], a[168:171], v[212:215], v[96:99]\n"
    "v_exp_f32_e32 v145, v145\n"
    "v_mfma_f32_16x16x32_bf16 v[92:95], a[168:171], v[204:207], v[92:95]\n"
    "v_exp_f32_e32 v146, v146\n"
    "v_mfma_f32_16x16x32_bf16 v[92:95], a[172:175], v[208:211], v[92:95]\n"
    "ds_read_b128 a[160:163], v240 offset:24576\n"
    "ds_write_addtid_b32 v140 offset:0\n"
    "v_mfma_f32_16x16x32_bf16 v[88:91], a[172:175], v[200:203], v[88:91]\n"
    "v_exp_f32_e32 v147, v147\n"
    "v_mfma_f32_16x16x32_bf16 v[88:91], a[168:171], v[196:199], v[88:91]\n"
    "ds_write_addtid_b32 v141 offset:256\n"
    "v_cvt_pk_bf16_f32 v142, v144, v145\n"
    "v_mfma_f32_16x16x32_bf16 v[84:87], a[168:171], v[188:191], v[84:87]\n"
    "v_exp_f32_e32 v152, v152\n"
    "v_mfma_f32_16x16x32_bf16 v[84:87], a[172:175], v[192:195], v[84:87]\n"
    "v_exp_f32_e32 v153, v153\n"
    "v_mfma_f32_16x16x32_bf16 v[80:83], a[172:175], v[184:187], v[80:83]\n"
    "ds_write_addtid_b32 v148 offset:1024\n"
    "v_cvt_pk_bf16_f32 v143, v146, v147\n"
    "v_mfma_f32_16x16x32_bf16 v[80:83], a[168:171], v[180:183], v[80:83]\n"
    "s_waitcnt vmcnt(20)\n"
    "ds_read_b128 a[164:167], v241 offset:24576\n"
    "ds_write_addtid_b32 v149 offset:1280\n"
    "v_smfmac_f32_16x16x64_bf16 v[224:227], a[232:235], a[224:231], v238\n"
    "s_waitcnt lgkmcnt(10)\n"
    "ds_write_addtid_b32 v156 offset:2048\n"
    "v_mad_u32_u16 v234, v232, s90, v239 op_sel:[0,0,0,0]\n"
    "v_mfma_f32_16x16x32_bf16 v[100:103], a[176:179], v[180:183], v[100:103]\n"
    "s_bitcmp1_b32 s86, 14\n"
    "buffer_load_dwordx4 a[224:227], v234, s[20:23], 0 offen sc1\n"
    "v_mad_u32_u16 v235, v232, s90, v239 op_sel:[1,0,0,0]\n"
    "v_mfma_f32_16x16x32_bf16 v[100:103], a[180:183], v[184:187], v[100:103]\n"
    "s_cselect_b64 exec, -1, 0\n"
    "v_cvt_pk_bf16_f32 v236, v224, v225\n"
    "v_cvt_pk_bf16_f32 v237, v226, v227\n"
    "v_mfma_f32_16x16x32_bf16 v[104:107], a[180:183], v[192:195], v[104:107]\n"
    "s_cselect_b32 s88, 512, 0\n"
    "v_mov_b64_e32 v[224:225], 0\n"
    "v_mov_b64_e32 v[226:227], 0\n"
    "v_mfma_f32_16x16x32_bf16 v[104:107], a[176:179], v[188:191], v[104:107]\n"
    "buffer_store_dwordx2 v[236:237], v252, s[8:11], s87 offen sc1\n"
    "v_mfma_f32_16x16x32_bf16 v[108:111], a[176:179], v[196:199], v[108:111]\n"
    "s_mov_b64 exec, -1\n"
    "ds_read_b128 a[168:171], v240 offset:28672\n"
    "ds_write_addtid_b32 v157 offset:2304\n"
    "v_mfma_f32_16x16x32_bf16 v[108:111], a[180:183], v[200:203], v[108:111]\n"
    "s_add_u32 s87, s87, s88\n"
    "v_exp_f32_e32 v154, v154\n"
    "v_mfma_f32_16x16x32_bf16 v[112:115], a[180:183], v[208:211], v[112:115]\n"
    "buffer_load_dwordx4 a[228:231], v235, s[20:23], 0 offen sc1\n"
    "ds_write_addtid_b32 v164 offset:3072\n"
    "v_mfma_f32_16x16x32_bf16 v[112:115], a[176:179], v[204:207], v[112:115]\n"
    "ds_write_addtid_b32 v165 offset:3328\n"
    "v_mfma_f32_16x16x32_bf16 v[116:119], a[176:179], v[212:215], v[116:119]\n"
    "v_exp_f32_e32 v155, v155\n"
    "v_mfma_f32_16x16x32_bf16 v[116:119], a[180:183], v[216:219], v[116:119]\n"
    "s_waitcnt lgkmcnt(11)\n"
    "ds_read_b128 a[172:175], v241 offset:28672\n"
    "v_cvt_pk_bf16_f32 v150, v152, v153\n"
    "v_mfma_f32_16x16x32_bf16 v[136:139], a[188:191], v[216:219], v[136:139]\n"
    "v_exp_f32_e32 v160, v160\n"
    "v_mfma_f32_16x16x32_bf16 v[136:139], a[184:187], v[212:215], v[136:139]\n"
    "v_exp_f32_e32 v161, v161\n"
    "v_mfma_f32_16x16x32_bf16 v[132:135], a[184:187], v[204:207], v[132:135]\n"
    "ds_read_b128 a[176:179], v242 offset:24576\n"
    "v_cvt_pk_bf16_f32 v151, v154, v155\n"
    "v_mfma_f32_16x16x32_bf16 v[132:135], a[188:191], v[208:211], v[132:135]\n"
    "v_exp_f32_e32 v162, v162\n"
    "v_mfma_f32_16x16x32_bf16 v[128:131], a[188:191], v[200:203], v[128:131]\n"
    "v_exp_f32_e32 v163, v163\n"
    "v_mfma_f32_16x16x32_bf16 v[128:131], a[184:187], v[196:199], v[128:131]\n"
    "ds_write_addtid_b32 v172 offset:4096\n"
    "v_cvt_pk_bf16_f32 v158, v160, v161\n"
    "v_mfma_f32_16x16x32_bf16 v[124:127], a[184:187], v[188:191], v[124:127]\n"
    "v_exp_f32_e32 v168, v168\n"
    "v_mfma_f32_16x16x32_bf16 v[124:127], a[188:191], v[192:195], v[124:127]\n"
    "ds_read_b128 a[180:183], v243 offset:24576\n"
    "ds_write_addtid_b32 v173 offset:4352\n"
    "v_mfma_f32_16x16x32_bf16 v[120:123], a[188:191], v[184:187], v[120:123]\n"
    "v_exp_f32_e32 v169, v169\n"
    "v_mfma_f32_16x16x32_bf16 v[120:123], a[184:187], v[180:183], v[120:123]\n"
    "s_waitcnt lgkmcnt(11)\n"
    "ds_write_addtid_b32 v142 offset:512\n"
    "v_cvt_pk_bf16_f32 v159, v162, v163\n"
    "v_mfma_f32_16x16x32_bf16 v[212:215], a[160:163], a[128:131], v[220:223]\n"
    "ds_write_addtid_b32 v143 offset:768\n"
    "v_mfma_f32_16x16x32_bf16 v[212:215], a[164:167], a[132:135], v[212:215]\n"
    "v_exp_f32_e32 v170, v170\n"
    "v_mfma_f32_16x16x32_bf16 v[204:207], a[164:167], a[100:103], v[220:223]\n"
    "ds_write_addtid_b32 v150 offset:1536\n"
    "v_mfma_f32_16x16x32_bf16 v[204:207], a[160:163], a[96:99], v[204:207]\n"
    "ds_read_b128 a[184:187], v242 offset:28672\n"
    "v_mfma_f32_16x16x32_bf16 v[196:199], a[160:163], a[64:67], v[220:223]\n"
    "v_exp_f32_e32 v171, v171\n"
    "v_mfma_f32_16x16x32_bf16 v[196:199], a[164:167], a[68:71], v[196:199]\n"
    "ds_write_addtid_b32 v151 offset:1792\n"
    "v_cvt_pk_bf16_f32 v166, v168, v169\n"
    "v_mfma_f32_16x16x32_bf16 v[188:191], a[164:167], a[36:39], v[220:223]\n"
    "v_exp_f32_e32 v176, v176\n"
    "v_mfma_f32_16x16x32_bf16 v[188:191], a[160:163], a[32:35], v[188:191]\n"
    "v_exp_f32_e32 v177, v177\n"
    "v_mfma_f32_16x16x32_bf16 v[180:183], a[160:163], a[0:3], v[220:223]\n"
    "ds_write_addtid_b32 v158 offset:2560\n"
    "v_cvt_pk_bf16_f32 v167, v170, v171\n"
    "v_mfma_f32_16x16x32_bf16 v[180:183], a[164:167], a[4:7], v[180:183]\n"
    "s_waitcnt vmcnt(20)\n"
    "v_exp_f32_e32 v178, v178\n"
    "v_smfmac_f32_16x16x64_bf16 v[224:227], a[232:235], a[192:199], v238\n"
    "s_waitcnt lgkmcnt(10)\n"
    "ds_read_b128 a[188:191], v243 offset:28672\n"
    "v_mfma_f32_16x16x32_bf16 v[180:183], a[168:171], a[8:11], v[180:183]\n"
    "s_bitcmp1_b32 s86, 15\n"
    "ds_read_b128 a[160:163], v240 offset:26624\n"
    "v_mfma_f32_16x16x32_bf16 v[180:183], a[172:175], a[12:15], v[180:183]\n"
    "s_cselect_b64 exec, -1, 0\n"
    "v_cvt_pk_bf16_f32 v236, v224, v225\n"
    "v_cvt_pk_bf16_f32 v237, v226, v227\n"
    "v_mfma_f32_16x16x32_bf16 v[188:191], a[172:175], a[44:47], v[188:191]\n"
    "s_cselect_b32 s88, 512, 0\n"
    "v_mov_b64_e32 v[224:225], 0\n"
    "v_mov_b64_e32 v[226:227], 0\n"
    "v_mfma_f32_16x16x32_bf16 v[188:191], a[168:171], a[40:43], v[188:191]\n"
    "buffer_store_dwordx2 v[236:237], v252, s[8:11], s87 offen sc1\n"
    "v_mfma_f32_16x16x32_bf16 v[196:199], a[168:171], a[72:75], v[196:199]\n"
    "s_mov_b64 exec, -1\n"
    "ds_read2_b32 v[228:229], v251 offset0:30 offset1:32\n"
    "v_mfma_f32_16x16x32_bf16 v[196:199], a[172:175], a[76:79], v[196:199]\n"
    "s_add_u32 s87, s87, s88\n"
    "ds_read2_b32 v[230:231], v251 offset0:34 offset1:36\n"
    "v_mfma_f32_16x16x32_bf16 v[204:207], a[172:175], a[108:111], v[204:207]\n"
    "ds_read_b32 v232, v251 offset:152\n"
    "v_mfma_f32_16x16x32_bf16 v[204:207], a[168:171], a[104:107], v[204:207]\n"
    "v_exp_f32_e32 v179, v179\n"
    "v_mfma_f32_16x16x32_bf16 v[212:215], a[168:171], a[136:139], v[212:215]\n"
    "ds_write_addtid_b32 v159 offset:2816\n"
    "v_cvt_pk_bf16_f32 v174, v176, v177\n"
    "v_mfma_f32_16x16x32_bf16 v[212:215], a[172:175], a[140:143], v[212:215]\n"
    "s_waitcnt lgkmcnt(13)\n"
    "ds_read_b128 a[164:167], v241 offset:26624\n"
    "v_mfma_f32_16x16x32_bf16 v[212:215], a[176:179], a[144:147], v[212:215]\n"
    "ds_write_addtid_b32 v166 offset:3584\n"
    "v_cvt_pk_bf16_f32 v175, v178, v179\n"
    "v_mfma_f32_16x16x32_bf16 v[212:215], a[180:183], a[148:151], v[212:215]\n"
    "ds_read_b128 a[168:171], v240 offset:30720\n"
    "v_mfma_f32_16x16x32_bf16 v[204:207], a[180:183], a[116:119], v[204:207]\n"
    "s_waitcnt lgkmcnt(6)\n"
    "ds_write_addtid_b32 v167 offset:3840\n"
    "v_mad_u32_u16 v234, v228, s90, v239 op_sel:[0,0,0,0]\n"
    "v_mfma_f32_16x16x32_bf16 v[204:207], a[176:179], a[112:115], v[204:207]\n"
    "buffer_load_dwordx4 a[192:195], v234, s[20:23], 0 offen sc1\n"
    "v_mad_u32_u16 v235, v228, s90, v239 op_sel:[1,0,0,0]\n"
    "v_mfma_f32_16x16x32_bf16 v[196:199], a[176:179], a[80:83], v[196:199]\n"
    "ds_write_addtid_b32 v174 offset:4608\n"
    "v_add_u32_e32 v251, 160, v251\n"
    "v_mfma_f32_16x16x32_bf16 v[196:199], a[180:183], a[84:87], v[196:199]\n"
    "ds_write_addtid_b32 v175 offset:4864\n"
    "v_mfma_f32_16x16x32_bf16 v[188:191], a[180:183], a[52:55], v[188:191]\n"
    "buffer_load_dwordx4 a[196:199], v235, s[20:23], 0 offen sc1\n"
    "v_mfma_f32_16x16x32_bf16 v[188:191], a[176:179], a[48:51], v[188:191]\n"
    "v_mfma_f32_16x16x32_bf16 v[180:183], a[176:179], a[16:19], v[180:183]\n"
    "s_waitcnt lgkmcnt(0)\n"
    "v_mfma_f32_16x16x32_bf16 v[180:183], a[180:183], a[20:23], v[180:183]\n"
    "s_barrier\n"
    "v_mfma_f32_16x16x32_bf16 v[180:183], a[184:187], a[24:27], v[180:183]\n"
    "s_add_u32 m0, s43, 8192\n"
    "ds_read_b128 a[172:175], v241 offset:30720\n"
    "v_mfma_f32_16x16x32_bf16 v[180:183], a[188:191], a[28:31], v[180:183]\n"
    "ds_read2st64_b32 v[144:145], v249 offset0:0 offset1:1\n"
    "v_mfma_f32_16x16x32_bf16 v[188:191], a[188:191], a[60:63], v[188:191]\n"
    "buffer_load_dwordx4 v248, s[8:11], s40 offen offset:0 sc1 lds\n"
    "v_mfma_f32_16x16x32_bf16 v[188:191], a[184:187], a[56:59], v[188:191]\n"
    "ds_read_b128 a[176:179], v242 offset:26624\n"
    "v_mfma_f32_16x16x32_bf16 v[196:199], a[184:187], a[88:91], v[196:199]\n"
    "v_mfma_f32_16x16x32_bf16 v[196:199], a[188:191], a[92:95], v[196:199]\n"
    "v_mfma_f32_16x16x32_bf16 v[204:207], a[188:191], a[124:127], v[204:207]\n"
    "ds_read2st64_b32 v[146:147], v249 offset0:2 offset1:3\n"
    "v_mfma_f32_16x16x32_bf16 v[204:207], a[184:187], a[120:123], v[204:207]\n"
    "buffer_load_dwordx4 v248, s[8:11], s40 offen offset:1024 sc1 lds\n"
    "v_mfma_f32_16x16x32_bf16 v[212:215], a[184:187], a[152:155], v[212:215]\n"
    "v_mfma_f32_16x16x32_bf16 v[212:215], a[188:191], a[156:159], v[212:215]\n"
    "v_mfma_f32_16x16x32_bf16 v[216:219], a[160:163], a[128:131], v[220:223]\n"
    "ds_read_b128 a[180:183], v243 offset:26624\n"
    "v_mfma_f32_16x16x32_bf16 v[216:219], a[164:167], a[132:135], v[216:219]\n"
    "ds_read2st64_b32 v[152:153], v249 offset0:4 offset1:5\n"
    "v_mfma_f32_16x16x32_bf16 v[208:211], a[164:167], a[100:103], v[220:223]\n"
    "buffer_load_dwordx4 v248, s[8:11], s40 offen offset:2048 sc1 lds\n"
    "v_mfma_f32_16x16x32_bf16 v[208:211], a[160:163], a[96:99], v[208:211]\n"
    "ds_read_b128 a[184:187], v242 offset:30720\n"
    "v_mfma_f32_16x16x32_bf16 v[200:203], a[160:163], a[64:67], v[220:223]\n"
    "v_exp_f32_e32 v180, v180\n"
    "v_mfma_f32_16x16x32_bf16 v[200:203], a[164:167], a[68:71], v[200:203]\n"
    "ds_read2st64_b32 v[154:155], v249 offset0:6 offset1:7\n"
    "v_mfma_f32_16x16x32_bf16 v[192:195], a[164:167], a[36:39], v[220:223]\n"
    "ds_read2st64_b32 v[160:161], v249 offset0:8 offset1:9\n"
    "v_mfma_f32_16x16x32_bf16 v[192:195], a[160:163], a[32:35], v[192:195]\n"
    "buffer_load_dwordx4 v248, s[8:11], s40 offen offset:3072 sc1 lds\n"
    "v_mfma_f32_16x16x32_bf16 v[184:187], a[160:163], a[0:3], v[220:223]\n"
    "s_add_u32 m0, s43, 12288\n"
    "ds_read2st64_b32 v[162:163], v249 offset0:10 offset1:11\n"
    "v_mfma_f32_16x16x32_bf16 v[184:187], a[164:167], a[4:7], v[184:187]\n"
    "s_waitcnt vmcnt(16)\n"
    "ds_read_b128 a[188:191], v243 offset:30720\n"
    "v_smfmac_f32_16x16x64_bf16 v[224:227], a[232:235], a[200:207], v238\n"
    "s_waitcnt lgkmcnt(10)\n"
    "v_mad_u32_u16 v234, v229, s90, v239 op_sel:[0,0,0,0]\n"
    "v_mfma_f32_16x16x32_bf16 v[184:187], a[168:171], a[8:11], v[184:187]\n"
    "s_bitcmp1_b32 s86, 16\n"
    "buffer_load_dwordx4 a[200:203], v234, s[20:23], 0 offen sc1\n"
    "v_mad_u32_u16 v235, v229, s90, v239 op_sel:[1,0,0,0]\n"
    "v_mfma_f32_16x16x32_bf16 v[184:187], a[172:175], a[12:15], v[184:187]\n"
    "s_cselect_b64 exec, -1, 0\n"
    "v_cvt_pk_bf16_f32 v236, v224, v225\n"
    "v_cvt_pk_bf16_f32 v237, v226, v227\n"
    "v_mfma_f32_16x16x32_bf16 v[192:195], a[172:175], a[44:47], v[192:195]\n"
    "s_cselect_b32 s88, 512, 0\n"
    "v_mov_b64_e32 v[224:225], 0\n"
    "v_mov_b64_e32 v[226:227], 0\n"
    "v_mfma_f32_16x16x32_bf16 v[192:195], a[168:171], a[40:43], v[192:195]\n"
    "buffer_store_dwordx2 v[236:237], v252, s[8:11], s87 offen sc1\n"
    "v_mfma_f32_16x16x32_bf16 v[200:203], a[168:171], a[72:75], v[200:203]\n"
    "s_mov_b64 exec, -1\n"
    "ds_read_b64_tr_b16 a[160:161], v244 offset:16384\n"
    "v_mfma_f32_16x16x32_bf16 v[200:203], a[172:175], a[76:79], v[200:203]\n"
    "s_add_u32 s87, s87, s88\n"
    "ds_read_b64_tr_b16 a[162:163], v244 offset:18432\n"
    "v_mfma_f32_16x16x32_bf16 v[208:211], a[172:175], a[108:111], v[208:211]\n"
    "s_add_u32 s40, s40, 0x8000\n"
    "buffer_load_dwordx4 v248, s[8:11], s41 offen offset:0 sc1 lds\n"
    "v_mfma_f32_16x16x32_bf16 v[208:211], a[168:171], a[104:107], v[208:211]\n"
    "ds_read_b64_tr_b16 a[164:165], v244 offset:49152\n"
    "v_mfma_f32_16x16x32_bf16 v[216:219], a[168:171], a[136:139], v[216:219]\n"
    "ds_read2st64_b32 v[168:169], v249 offset0:12 offset1:13\n"
    "v_mfma_f32_16x16x32_bf16 v[216:219], a[172:175], a[140:143], v[216:219]\n"
    "s_waitcnt lgkmcnt(10)\n"
    "ds_read2st64_b32 v[170:171], v249 offset0:14 offset1:15\n"
    "v_mfma_f32_16x16x32_bf16 v[216:219], a[176:179], a[144:147], v[216:219]\n"
    "ds_read_b64_tr_b16 a[166:167], v244 offset:51200\n"
    "v_mfma_f32_16x16x32_bf16 v[216:219], a[180:183], a[148:151], v[216:219]\n"
    "buffer_load_dwordx4 v248, s[8:11], s41 offen offset:1024 sc1 lds\n"
    "v_mfma_f32_16x16x32_bf16 v[208:211], a[180:183], a[116:119], v[208:211]\n"
    "ds_read_b64_tr_b16 a[168:169], v245 offset:16384\n"
    "v_mfma_f32_16x16x32_bf16 v[208:211], a[176:179], a[112:115], v[208:211]\n"
    "v_exp_f32_e32 v181, v181\n"
    "v_mfma_f32_16x16x32_bf16 v[200:203], a[176:179], a[80:83], v[200:203]\n"
    "ds_read_b64_tr_b16 a[170:171], v245 offset:18432\n"
    "v_mfma_f32_16x16x32_bf16 v[200:203], a[180:183], a[84:87], v[200:203]\n"
    "ds_read2st64_b32 v[176:177], v249 offset0:16 offset1:17\n"
    "v_mfma_f32_16x16x32_bf16 v[192:195], a[180:183], a[52:55], v[192:195]\n"
    "buffer_load_dwordx4 v248, s[8:11], s41 offen offset:2048 sc1 lds\n"
    "v_mfma_f32_16x16x32_bf16 v[192:195], a[176:179], a[48:51], v[192:195]\n"
    "v_exp_f32_e32 v182, v182\n"
    "v_mfma_f32_16x16x32_bf16 v[184:187], a[176:179], a[16:19], v[184:187]\n"
    "ds_read_b64_tr_b16 a[172:173], v245 offset:49152\n"
    "v_mfma_f32_16x16x32_bf16 v[184:187], a[180:183], a[20:23], v[184:187]\n"
    "s_waitcnt lgkmcnt(10)\n"
    "ds_read2st64_b32 v[178:179], v249 offset0:18 offset1:19\n"
    "v_mfma_f32_16x16x32_bf16 v[184:187], a[184:187], a[24:27], v[184:187]\n"
    "ds_read_b64_tr_b16 a[176:177], v246 offset:16384\n"
    "v_mfma_f32_16x16x32_bf16 v[184:187], a[188:191], a[28:31], v[184:187]\n"
    "buffer_load_dwordx4 v248, s[8:11], s41 offen offset:3072 sc1 lds\n"
    "v_mfma_f32_16x16x32_bf16 v[192:195], a[188:191], a[60:63], v[192:195]\n"
    "ds_read_b64_tr_b16 a[174:175], v245 offset:51200\n"
    "v_mfma_f32_16x16x32_bf16 v[192:195], a[184:187], a[56:59], v[192:195]\n"
    "s_add_u32 s41, s41, 0x8000\n"
    "v_exp_f32_e32 v183, v183\n"
    "v_mfma_f32_16x16x32_bf16 v[200:203], a[184:187], a[88:91], v[200:203]\n"
    "buffer_load_dwordx4 a[204:207], v235, s[20:23], 0 offen sc1\n"
    "v_cvt_pk_bf16_f32 v180, v180, v181\n"
    "v_mfma_f32_16x16x32_bf16 v[200:203], a[188:191], a[92:95], v[200:203]\n"
    "ds_read_b64_tr_b16 a[178:179], v246 offset:18432\n"
    "v_mfma_f32_16x16x32_bf16 v[208:211], a[188:191], a[124:127], v[208:211]\n"
    "v_exp_f32_e32 v188, v188\n"
    "v_mfma_f32_16x16x32_bf16 v[208:211], a[184:187], a[120:123], v[208:211]\n"
    "v_exp_f32_e32 v189, v189\n"
    "v_mfma_f32_16x16x32_bf16 v[216:219], a[184:187], a[152:155], v[216:219]\n"
    "ds_read_b64_tr_b16 a[180:181], v246 offset:49152\n"
    "v_cvt_pk_bf16_f32 v181, v182, v183\n"
    "v_mfma_f32_16x16x32_bf16 v[216:219], a[188:191], a[156:159], v[216:219]\n"
    "s_waitcnt vmcnt(20)\n"
    "ds_read_b64_tr_b16 a[182:183], v246 offset:51200\n"
    "v_smfmac_f32_16x16x64_bf16 v[224:227], a[232:235], a[208:215], v238\n"
    "s_waitcnt lgkmcnt(5)\n"
    "v_mad_u32_u16 v234, v230, s90, v239 op_sel:[0,0,0,0]\n"
    "v_mfma_f32_16x16x32_bf16 a[236:239], a[160:163], v[140:143], a[236:239]\n"
    "s_bitcmp1_b32 s86, 17\n"
    "buffer_load_dwordx4 a[208:211], v234, s[20:23], 0 offen sc1\n"
    "v_mad_u32_u16 v235, v230, s90, v239 op_sel:[1,0,0,0]\n"
    "v_mfma_f32_16x16x32_bf16 a[236:239], a[164:167], v[144:147], a[236:239]\n"
    "s_cselect_b64 exec, -1, 0\n"
    "v_cvt_pk_bf16_f32 v236, v224, v225\n"
    "v_cvt_pk_bf16_f32 v237, v226, v227\n"
    "v_mfma_f32_16x16x32_bf16 a[240:243], a[164:167], v[152:155], a[240:243]\n"
    "s_cselect_b32 s88, 512, 0\n"
    "v_mov_b64_e32 v[224:225], 0\n"
    "v_mov_b64_e32 v[226:227], 0\n"
    "v_mfma_f32_16x16x32_bf16 a[240:243], a[160:163], v[148:151], a[240:243]\n"
    "buffer_store_dwordx2 v[236:237], v252, s[8:11], s87 offen sc1\n"
    "v_mfma_f32_16x16x32_bf16 a[244:247], a[160:163], v[156:159], a[244:247]\n"
    "s_mov_b64 exec, -1\n"
    "ds_read_b64_tr_b16 a[184:185], v247 offset:16384\n"
    "v_mfma_f32_16x16x32_bf16 a[244:247], a[164:167], v[160:163], a[244:247]\n"
    "s_add_u32 s87, s87, s88\n"
    "ds_read_b64_tr_b16 a[186:187], v247 offset:18432\n"
    "v_mfma_f32_16x16x32_bf16 a[248:251], a[164:167], v[168:171], a[248:251]\n"
    "buffer_load_dwordx4 a[212:215], v235, s[20:23], 0 offen sc1\n"
    "v_mfma_f32_16x16x32_bf16 a[248:251], a[160:163], v[164:167], a[248:251]\n"
    "ds_read_b64_tr_b16 a[188:189], v247 offset:49152\n"
    "v_mfma_f32_16x16x32_bf16 a[252:255], a[160:163], v[172:175], a[252:255]\n"
    "v_exp_f32_e32 v190, v190\n"
    "v_mfma_f32_16x16x32_bf16 a[252:255], a[164:167], v[176:179], a[252:255]\n"
    "s_waitcnt lgkmcnt(6)\n"
    "v_exp_f32_e32 v191, v191\n"
    "v_mfma_f32_16x16x32_bf16 v[16:19], a[172:175], v[176:179], v[16:19]\n"
    "ds_read_b64_tr_b16 a[190:191], v247 offset:51200\n"
    "v_cvt_pk_bf16_f32 v188, v188, v189\n"
    "v_mfma_f32_16x16x32_bf16 v[16:19], a[168:171], v[172:175], v[16:19]\n"
    "ds_read_b64_tr_b16 a[160:161], v244 offset:20480\n"
    "v_mfma_f32_16x16x32_bf16 v[12:15], a[168:171], v[164:167], v[12:15]\n"
    "v_exp_f32_e32 v196, v196\n"
    "v_mfma_f32_16x16x32_bf16 v[12:15], a[172:175], v[168:171], v[12:15]\n"
    "v_exp_f32_e32 v197, v197\n"
    "v_mfma_f32_16x16x32_bf16 v[8:11], a[172:175], v[160:163], v[8:11]\n"
    "ds_read_b64_tr_b16 a[162:163], v244 offset:22528\n"
    "v_cvt_pk_bf16_f32 v189, v190, v191\n"
    "v_mfma_f32_16x16x32_bf16 v[8:11], a[168:171], v[156:159], v[8:11]\n"
    "v_exp_f32_e32 v198, v198\n"
    "v_mfma_f32_16x16x32_bf16 v[4:7], a[168:171], v[148:151], v[4:7]\n"
    "v_exp_f32_e32 v199, v199\n"
    "v_mfma_f32_16x16x32_bf16 v[4:7], a[172:175], v[152:155], v[4:7]\n"
    "v_cvt_pk_bf16_f32 v196, v196, v197\n"
    "v_mfma_f32_16x16x32_bf16 v[0:3], a[172:175], v[144:147], v[0:3]\n"
    "ds_read_b64_tr_b16 a[164:165], v244 offset:53248\n"
    "v_mfma_f32_16x16x32_bf16 v[0:3], a[168:171], v[140:143], v[0:3]\n"
    "s_waitcnt lgkmcnt(7)\n"
    "v_exp_f32_e32 v204, v204\n"
    "v_mfma_f32_16x16x32_bf16 v[20:23], a[176:179], v[140:143], v[20:23]\n"
    "ds_read_b64_tr_b16 a[168:169], v245 offset:20480\n"
    "v_mfma_f32_16x16x32_bf16 v[20:23], a[180:183], v[144:147], v[20:23]\n"
    "ds_read_b64_tr_b16 a[166:167], v244 offset:55296\n"
    "v_mfma_f32_16x16x32_bf16 v[24:27], a[180:183], v[152:155], v[24:27]\n"
    "v_exp_f32_e32 v205, v205\n"
    "v_mfma_f32_16x16x32_bf16 v[24:27], a[176:179], v[148:151], v[24:27]\n"
    "v_cvt_pk_bf16_f32 v197, v198, v199\n"
    "v_mfma_f32_16x16x32_bf16 v[28:31], a[176:179], v[156:159], v[28:31]\n"
    "ds_read_b64_tr_b16 a[170:171], v245 offset:22528\n"
    "v_mfma_f32_16x16x32_bf16 v[28:31], a[180:183], v[160:163], v[28:31]\n"
    "v_exp_f32_e32 v206, v206\n"
    "v_mfma_f32_16x16x32_bf16 v[32:35], a[180:183], v[168:171], v[32:35]\n"
    "v_exp_f32_e32 v207, v207\n"
    "v_mfma_f32_16x16x32_bf16 v[32:35], a[176:179], v[164:167], v[32:35]\n"
    "v_cvt_pk_bf16_f32 v204, v204, v205\n"
    "v_mfma_f32_16x16x32_bf16 v[36:39], a[176:179], v[172:175], v[36:39]\n"
    "ds_read_b64_tr_b16 a[172:173], v245 offset:53248\n"
    "v_mfma_f32_16x16x32_bf16 v[36:39], a[180:183], v[176:179], v[36:39]\n"
    "s_waitcnt vmcnt(20)\n"
    "ds_read_b64_tr_b16 a[174:175], v245 offset:55296\n"
    "v_smfmac_f32_16x16x64_bf16 v[224:227], a[232:235], a[216:223], v238\n"
    "s_waitcnt lgkmcnt(8)\n"
    "v_mad_u32_u16 v234, v231, s90, v239 op_sel:[0,0,0,0]\n"
    "v_mfma_f32_16x16x32_bf16 v[56:59], a[188:191], v[176:179], v[56:59]\n"
    "s_bitcmp1_b32 s86, 18\n"
    "buffer_load_dwordx4 a[216:219], v234, s[20:23], 0 offen sc1\n"
    "v_mad_u32_u16 v235, v231, s90, v239 op_sel:[1,0,0,0]\n"
    "v_mfma_f32_16x16x32_bf16 v[56:59], a[184:187], v[172:175], v[56:59]\n"
    "s_cselect_b64 exec, -1, 0\n"
    "v_cvt_pk_bf16_f32 v236, v224, v225\n"
    "v_cvt_pk_bf16_f32 v237, v226, v227\n"
    "v_mfma_f32_16x16x32_bf16 v[52:55], a[184:187], v[164:167], v[52:55]\n"
    "s_cselect_b32 s88, 512, 0\n"
    "v_mov_b64_e32 v[224:225], 0\n"
    "v_mov_b64_e32 v[226:227], 0\n"
    "v_mfma_f32_16x16x32_bf16 v[52:55], a[188:191], v[168:171], v[52:55]\n"
    "buffer_store_dwordx2 v[236:237], v252, s[8:11], s87 offen sc1\n"
    "v_mfma_f32_16x16x32_bf16 v[48:51], a[188:191], v[160:163], v[48:51]\n"
    "s_mov_b64 exec, -1\n"
    "ds_read_b64_tr_b16 a[176:177], v246 offset:20480\n"
    "v_mfma_f32_16x16x32_bf16 v[48:51], a[184:187], v[156:159], v[48:51]\n"
    "s_add_u32 s87, s87, s88\n"
    "ds_read_b64_tr_b16 a[178:179], v246 offset:22528\n"
    "v_mfma_f32_16x16x32_bf16 v[44:47], a[184:187], v[148:151], v[44:47]\n"
    "buffer_load_dwordx4 a[220:223], v235, s[20:23], 0 offen sc1\n"
    "v_mfma_f32_16x16x32_bf16 v[44:47], a[188:191], v[152:155], v[44:47]\n"
    "ds_read_b64_tr_b16 a[180:181], v246 offset:53248\n"
    "v_mfma_f32_16x16x32_bf16 v[40:43], a[188:191], v[144:147], v[40:43]\n"
    "v_exp_f32_e32 v212, v212\n"
    "v_mfma_f32_16x16x32_bf16 v[40:43], a[184:187], v[140:143], v[40:43]\n"
    "s_waitcnt lgkmcnt(6)\n"
    "v_exp_f32_e32 v213, v213\n"
    "v_mfma_f32_16x16x32_bf16 v[60:63], a[160:163], v[140:143], v[60:63]\n"
    "ds_read_b64_tr_b16 a[182:183], v246 offset:55296\n"
    "v_cvt_pk_bf16_f32 v205, v206, v207\n"
    "v_mfma_f32_16x16x32_bf16 v[60:63], a[164:167], v[144:147], v[60:63]\n"
    "ds_read_b64_tr_b16 a[184:185], v247 offset:20480\n"
    "v_mfma_f32_16x16x32_bf16 v[64:67], a[164:167], v[152:155], v[64:67]\n"
    "ds_read_b64_tr_b16 a[186:187], v247 offset:22528\n"
    "v_mfma_f32_16x16x32_bf16 v[64:67], a[160:163], v[148:151], v[64:67]\n"
    "v_exp_f32_e32 v214, v214\n"
    "v_mfma_f32_16x16x32_bf16 v[68:71], a[160:163], v[156:159], v[68:71]\n"
    "v_exp_f32_e32 v215, v215\n"
    "v_mfma_f32_16x16x32_bf16 v[68:71], a[164:167], v[160:163], v[68:71]\n"
    "ds_read_b64_tr_b16 a[188:189], v247 offset:53248\n"
    "v_cvt_pk_bf16_f32 v212, v212, v213\n"
    "v_mfma_f32_16x16x32_bf16 v[72:75], a[164:167], v[168:171], v[72:75]\n"
    "ds_read_b64_tr_b16 a[190:191], v247 offset:55296\n"
    "v_cvt_pk_bf16_f32 v213, v214, v215\n"
    "v_mfma_f32_16x16x32_bf16 v[72:75], a[160:163], v[164:167], v[72:75]\n"
    "s_waitcnt vmcnt(30)\n"
    "v_mfma_f32_16x16x32_bf16 v[76:79], a[160:163], v[172:175], v[76:79]\n"
    "s_barrier\n"
    "v_mfma_f32_16x16x32_bf16 v[76:79], a[164:167], v[176:179], v[76:79]\n"
    "s_waitcnt lgkmcnt(8)\n"
    "s_mov_b32 m0, s42\n"
    "v_mfma_f32_16x16x32_bf16 v[96:99], a[172:175], v[176:179], v[96:99]\n"
    "v_exp_f32_e32 v184, v184\n"
    "v_mfma_f32_16x16x32_bf16 v[96:99], a[168:171], v[172:175], v[96:99]\n"
    "v_exp_f32_e32 v185, v185\n"
    "v_mfma_f32_16x16x32_bf16 v[92:95], a[168:171], v[164:167], v[92:95]\n"
    "v_exp_f32_e32 v186, v186\n"
    "v_mfma_f32_16x16x32_bf16 v[92:95], a[172:175], v[168:171], v[92:95]\n"
    "ds_read_b128 a[160:163], v240 offset:0\n"
    "ds_write_addtid_b32 v180 offset:0\n"
    "v_mfma_f32_16x16x32_bf16 v[88:91], a[172:175], v[160:163], v[88:91]\n"
    "v_exp_f32_e32 v187, v187\n"
    "v_mfma_f32_16x16x32_bf16 v[88:91], a[168:171], v[156:159], v[88:91]\n"
    "ds_write_addtid_b32 v181 offset:256\n"
    "v_cvt_pk_bf16_f32 v182, v184, v185\n"
    "v_mfma_f32_16x16x32_bf16 v[84:87], a[168:171], v[148:151], v[84:87]\n"
    "v_exp_f32_e32 v192, v192\n"
    "v_mfma_f32_16x16x32_bf16 v[84:87], a[172:175], v[152:155], v[84:87]\n"
    "v_exp_f32_e32 v193, v193\n"
    "v_mfma_f32_16x16x32_bf16 v[80:83], a[172:175], v[144:147], v[80:83]\n"
    "ds_write_addtid_b32 v188 offset:1024\n"
    "v_cvt_pk_bf16_f32 v183, v186, v187\n"
    "v_mfma_f32_16x16x32_bf16 v[80:83], a[168:171], v[140:143], v[80:83]\n"
    "s_waitcnt vmcnt(20)\n"
    "ds_read_b128 a[164:167], v241 offset:0\n"
    "ds_write_addtid_b32 v189 offset:1280\n"
    "v_smfmac_f32_16x16x64_bf16 v[224:227], a[232:235], a[224:231], v238\n"
    "s_waitcnt lgkmcnt(10)\n"
    "ds_write_addtid_b32 v196 offset:2048\n"
    "v_mad_u32_u16 v234, v232, s90, v239 op_sel:[0,0,0,0]\n"
    "v_mfma_f32_16x16x32_bf16 v[100:103], a[176:179], v[140:143], v[100:103]\n"
    "s_bitcmp1_b32 s86, 19\n"
    "buffer_load_dwordx4 a[224:227], v234, s[20:23], 0 offen sc1\n"
    "v_mad_u32_u16 v235, v232, s90, v239 op_sel:[1,0,0,0]\n"
    "v_mfma_f32_16x16x32_bf16 v[100:103], a[180:183], v[144:147], v[100:103]\n"
    "s_cselect_b64 exec, -1, 0\n"
    "v_cvt_pk_bf16_f32 v236, v224, v225\n"
    "v_cvt_pk_bf16_f32 v237, v226, v227\n"
    "v_mfma_f32_16x16x32_bf16 v[104:107], a[180:183], v[152:155], v[104:107]\n"
    "s_cselect_b32 s88, 512, 0\n"
    "v_mov_b64_e32 v[224:225], 0\n"
    "v_mov_b64_e32 v[226:227], 0\n"
    "v_mfma_f32_16x16x32_bf16 v[104:107], a[176:179], v[148:151], v[104:107]\n"
    "buffer_store_dwordx2 v[236:237], v252, s[8:11], s87 offen sc1\n"
    "v_mfma_f32_16x16x32_bf16 v[108:111], a[176:179], v[156:159], v[108:111]\n"
    "s_mov_b64 exec, -1\n"
    "ds_read_b128 a[168:171], v240 offset:4096\n"
    "ds_write_addtid_b32 v197 offset:2304\n"
    "v_mfma_f32_16x16x32_bf16 v[108:111], a[180:183], v[160:163], v[108:111]\n"
    "s_add_u32 s87, s87, s88\n"
    "v_exp_f32_e32 v194, v194\n"
    "v_mfma_f32_16x16x32_bf16 v[112:115], a[180:183], v[168:171], v[112:115]\n"
    "s_add_u32 s89, s89, 1\n"
    "buffer_load_dwordx4 a[228:231], v235, s[20:23], 0 offen sc1\n"
    "ds_write_addtid_b32 v204 offset:3072\n"
    "v_mfma_f32_16x16x32_bf16 v[112:115], a[176:179], v[164:167], v[112:115]\n"
    "ds_write_addtid_b32 v205 offset:3328\n"
    "v_readlane_b32 s86, v254, s89\n"
    "v_mfma_f32_16x16x32_bf16 v[116:119], a[176:179], v[172:175], v[116:119]\n"
    "v_exp_f32_e32 v195, v195\n"
    "v_mfma_f32_16x16x32_bf16 v[116:119], a[180:183], v[176:179], v[116:119]\n"
    "s_waitcnt lgkmcnt(11)\n"
    "ds_read_b128 a[172:175], v241 offset:4096\n"
    "v_cvt_pk_bf16_f32 v190, v192, v193\n"
    "v_mfma_f32_16x16x32_bf16 v[136:139], a[188:191], v[176:179], v[136:139]\n"
    "v_exp_f32_e32 v200, v200\n"
    "v_mfma_f32_16x16x32_bf16 v[136:139], a[184:187], v[172:175], v[136:139]\n"
    "v_exp_f32_e32 v201, v201\n"
    "v_mfma_f32_16x16x32_bf16 v[132:135], a[184:187], v[164:167], v[132:135]\n"
    "ds_read_b128 a[176:179], v242 offset:0\n"
    "v_cvt_pk_bf16_f32 v191, v194, v195\n"
    "v_mfma_f32_16x16x32_bf16 v[132:135], a[188:191], v[168:171], v[132:135]\n"
    "v_exp_f32_e32 v202, v202\n"
    "v_mfma_f32_16x16x32_bf16 v[128:131], a[188:191], v[160:163], v[128:131]\n"
    "v_exp_f32_e32 v203, v203\n"
    "v_mfma_f32_16x16x32_bf16 v[128:131], a[184:187], v[156:159], v[128:131]\n"
    "ds_write_addtid_b32 v212 offset:4096\n"
    "v_cvt_pk_bf16_f32 v198, v200, v201\n"
    "v_mfma_f32_16x16x32_bf16 v[124:127], a[184:187], v[148:151], v[124:127]\n"
    "v_exp_f32_e32 v208, v208\n"
    "v_mfma_f32_16x16x32_bf16 v[124:127], a[188:191], v[152:155], v[124:127]\n"
    "s_sub_u32 s44, s44, 1\n"
    "ds_read_b128 a[180:183], v243 offset:0\n"
    "ds_write_addtid_b32 v213 offset:4352\n"
    "v_mfma_f32_16x16x32_bf16 v[120:123], a[188:191], v[144:147], v[120:123]\n"
    "s_cmp_lg_u32 s44, s80\n"
    "v_exp_f32_e32 v209, v209\n"
    "v_mfma_f32_16x16x32_bf16 v[120:123], a[184:187], v[140:143], v[120:123]\n"
    "s_waitcnt lgkmcnt(11)\n"
    "ds_write_addtid_b32 v182 offset:512\n"
    "v_cvt_pk_bf16_f32 v199, v202, v203\n"
    "s_cbranch_scc1 .Lat_gloop\n"
    ".Lat_g2:\n"
    "v_mov_b64_e32 v[224:225], v[220:221]\n"
    "v_mov_b64_e32 v[226:227], v[220:221]\n"
    "v_mov_b64_e32 v[228:229], v[220:221]\n"
    "v_mov_b64_e32 v[230:231], v[220:221]\n"
    "v_mov_b64_e32 v[232:233], v[220:221]\n"
    "v_mov_b64_e32 v[234:235], v[220:221]\n"
    "v_mov_b64_e32 v[236:237], v[220:221]\n"
    "v_mov_b64_e32 v[238:239], v[220:221]\n"
    "s_mov_b32 s80, 0\n"
    "s_waitcnt lgkmcnt(0)\n"
    "s_cmp_lg_u32 s44, 0\n"
    "s_cbranch_scc1 .Lat_loop\n"
    "s_branch .Lat_tail\n"
    ".Lat_wpoll:\n"
    "s_mov_b32 s53, 0x400000\n"
    ".Lat_wpoll_l:\n"
    "s_sleep 1\n"
    "buffer_load_dword v253, v255, s[8:11], s52 offen offset:0 sc1\n"
    "s_waitcnt vmcnt(0)\n"
    "v_cmp_ne_u32_e32 vcc, s39, v253\n"
    "s_sub_u32 s53, s53, 1\n"
    "s_cbranch_scc1 .Lat_wpoll_ret\n"
    "s_nop 2\n"
    "s_cbranch_vccnz .Lat_wpoll_l\n"
    "s_branch .Lat_wpoll_ret\n"
    ".Lat_k:\n"
    "s_mov_b32 s53, 0x400000\n"
    ".Lat_kl:\n"
    "buffer_load_dword v253, v238, s[8:11], s48 offen offset:0 sc1\n"
    "s_waitcnt vmcnt(0)\n"
    "v_subrev_u32_e32 v253, s39, v253\n"
    "v_subrev_u32_e32 v253, 1, v253\n"
    "v_cmp_le_u32_e32 vcc, 1, v253\n"
    "s_sub_u32 s53, s53, 1\n"
    "s_cbranch_scc1 .Lat_kr\n"
    "s_nop 2\n"
    "s_cbranch_vccnz .Lat_kl\n"
    "s_branch .Lat_kr\n"
    ".Lat_kx:\n"
    "buffer_load_dwordx4 a[0:3], v250, s[8:11], s87 offen offset:0 sc1\n"
    "buffer_load_dwordx4 a[4:7], v250, s[8:11], s88 offen offset:0 sc1\n"
    "buffer_load_dwordx4 a[8:11], v250, s[8:11], s89 offen offset:0 sc1\n"
    "buffer_load_dwordx4 a[12:15], v250, s[8:11], s87 offen offset:1024 sc1\n"
    "buffer_load_dwordx4 a[16:19], v250, s[8:11], s88 offen offset:1024 sc1\n"
    "buffer_load_dwordx4 a[20:23], v250, s[8:11], s89 offen offset:1024 sc1\n"
    "s_branch .Lat_kxr\n"
    ".Lat_glate:\n"
    "s_waitcnt vmcnt(0)\n"
    "buffer_store_dword v254, off, s[8:11], s90 offset:0 sc1\n"
    "s_mov_b32 s53, 0x400000\n"
    "v_and_b32_e32 v148, 0x1f0, v250\n"
    ".Lat_gpoll:\n"
    "buffer_load_dwordx4 v[144:147], v148, s[8:11], s81 offen offset:0 sc1\n"
    "s_waitcnt vmcnt(0)\n"
    "v_xor_b32_e32 v144, s39, v144\n"
    "v_xor_b32_e32 v145, s39, v145\n"
    "v_xor_b32_e32 v146, s39, v146\n"
    "v_xor_b32_e32 v147, s39, v147\n"
    "v_or3_b32 v144, v144, v145, v146\n"
    "v_or_b32_e32 v144, v144, v147\n"
    "v_cmp_ne_u32_e32 vcc, 0, v144\n"
    "s_sub_u32 s53, s53, 1\n"
    "s_cbranch_scc1 .Lat_gok\n"
    "s_nop 2\n"
    "s_cbranch_vccz .Lat_gok\n"
    "s_sleep 2\n"
    "s_branch .Lat_gpoll\n"
    ".Lat_gok:\n"
    "s_add_u32 s31, s47, s35\n"
    "s_add_u32 m0, s55, 24576\n"
    "s_nop 0\n"
    "buffer_load_dwordx4 v251, s[8:11], s31 offen offset:0 sc1 lds\n"
    "s_add_u32 s31, s31, 0x1000\n"
    "s_add_u32 m0, s55, 25600\n"
    "s_nop 0\n"
    "buffer_load_dwordx4 v251, s[8:11], s31 offen offset:0 sc1 lds\n"
    "s_add_u32 s31, s47, s36\n"
    "s_add_u32 m0, s55, 26624\n"
    "s_nop 0\n"
    "buffer_load_dwordx4 v251, s[8:11], s31 offen offset:0 sc1 lds\n"
    "s_add_u32 s31, s31, 0x1000\n"
    "s_add_u32 m0, s55, 27648\n"
    "s_nop 0\n"
    "buffer_load_dwordx4 v251, s[8:11], s31 offen offset:0 sc1 lds\n"
    "s_add_u32 s31, s47, s37\n"
    "s_add_u32 m0, s55, 28672\n"
    "s_nop 0\n"
    "buffer_load_dwordx4 v251, s[8:11], s31 offen offset:0 sc1 lds\n"
    "s_add_u32 s31, s31, 0x1000\n"
    "s_add_u32 m0, s55, 29696\n"
    "s_nop 0\n"
    "buffer_load_dwordx4 v251, s[8:11], s31 offen offset:0 sc1 lds\n"
    "s_waitcnt vmcnt(0)\n"
    "s_branch .Lat_glate_ret\n"
    ".Lat_ppoll:\n"
    "buffer_load_dword v253, v226, s[8:11], s61 offen offset:0 sc1\n"
    "s_waitcnt vmcnt(0)\n"
    "v_subrev_u32_e32 v253, s39, v253\n"
    "v_cmp_lt_u32_e32 vcc, 1, v253\n"
    "s_sub_u32 s53, s53, 1\n"
    "s_cbranch_scc1 .Lat_pok\n"
    "s_nop 2\n"
    "s_cbranch_vccnz .Lat_ppoll\n"
    "s_branch .Lat_pok\n"
    ".Lat_bin:\n"
    "s_movk_i32 s45, -2560\n"
    "s_mov_b32 s48, 81920\n"
    "v_mov_b32_e32 v60, 1\n"
    "s_waitcnt vmcnt(2)\n"
    "v_mul_u32_u24_e32 v48, 0x199a, v144\n"
    "v_mul_u32_u24_e32 v49, 0x199a, v145\n"
    "v_mul_u32_u24_e32 v50, 0x199a, v146\n"
    "v_mul_u32_u24_e32 v51, 0x199a, v147\n"
    "v_mul_u32_u24_e32 v52, 0x199a, v148\n"
    "v_bfe_u32 v48, v48, 24, 2\n"
    "v_bfe_u32 v49, v49, 24, 2\n"
    "v_bfe_u32 v50, v50, 24, 2\n"
    "v_bfe_u32 v51, v51, 24, 2\n"
    "v_bfe_u32 v52, v52, 24, 2\n"
    "v_lshl_add_u32 v54, v48, 2, s48\n"
    "v_lshl_add_u32 v55, v49, 2, s48\n"
    "v_lshl_add_u32 v56, v50, 2, s48\n"
    "v_lshl_add_u32 v57, v51, 2, s48\n"
    "v_lshl_add_u32 v58, v52, 2, s48\n"
    "ds_add_rtn_u32 v54, v54, v60 offset:4096\n"
    "ds_add_rtn_u32 v55, v55, v60 offset:4096\n"
    "ds_add_rtn_u32 v56, v56, v60 offset:4096\n"
    "ds_add_rtn_u32 v57, v57, v60 offset:4096\n"
    "ds_add_rtn_u32 v58, v58, v60 offset:4096\n"
    "s_waitcnt vmcnt(0)\n"
    "v_mad_i32_i24 v144, v48, s45, v144\n"
    "v_mad_i32_i24 v145, v49, s45, v145\n"
    "v_mad_i32_i24 v146, v50, s45, v146\n"
    "v_mad_i32_i24 v147, v51, s45, v147\n"
    "v_mad_i32_i24 v148, v52, s45, v148\n"
    "v_lshl_or_b32 v144, v144, 18, v152\n"
    "v_lshl_or_b32 v145, v145, 18, v153\n"
    "v_lshl_or_b32 v146, v146, 18, v154\n"
    "v_lshl_or_b32 v147, v147, 18, v155\n"
    "v_lshl_or_b32 v148, v148, 18, v156\n"
    "v_lshl_add_u32 v48, v48, 10, s48\n"
    "v_lshl_add_u32 v49, v49, 10, s48\n"
    "v_lshl_add_u32 v50, v50, 10, s48\n"
    "v_lshl_add_u32 v51, v51, 10, s48\n"
    "v_lshl_add_u32 v52, v52, 10, s48\n"
    "s_waitcnt lgkmcnt(0)\n"
    "v_min_i32_e32 v54, 254, v54\n"
    "v_min_i32_e32 v55, 254, v55\n"
    "v_min_i32_e32 v56, 254, v56\n"
    "v_min_i32_e32 v57, 254, v57\n"
    "v_min_i32_e32 v58, 254, v58\n"
    "v_lshl_add_u32 v48, v54, 2, v48\n"
    "v_lshl_add_u32 v49, v55, 2, v49\n"
    "v_lshl_add_u32 v50, v56, 2, v50\n"
    "v_lshl_add_u32 v51, v57, 2, v51\n"
    "v_lshl_add_u32 v52, v58, 2, v52\n"
    "ds_write_b32 v48, v144 offset:4\n"
    "ds_write_b32 v49, v145 offset:4\n"
    "ds_write_b32 v50, v146 offset:4\n"
    "ds_write_b32 v51, v147 offset:4\n"
    "ds_write_b32 v52, v148 offset:4\n"
    "s_branch .Lat_noj2\n"
    ".Lat_apoll:\n"
    "s_mov_b32 s53, 0x400000\n"
    ".Lat_apoll_l:\n"
    "s_sleep 1\n"
    "buffer_load_dword v101, v100, s[8:11], 0 offen offset:0 sc1\n"
    "s_waitcnt vmcnt(0)\n"
    "v_cmp_ne_u32_e32 vcc, s39, v101\n"
    "s_sub_u32 s53, s53, 1\n"
    "s_cbranch_scc1 .Lat_apoll_ret\n"
    "s_nop 2\n"
    "s_cbranch_vccnz .Lat_apoll_l\n"
    "s_branch .Lat_apoll_ret\n"
    ".Lat_h1poll:\n"
    "s_mov_b32 s53, 0x400000\n"
    ".Lat_h1poll_l:\n"
    "s_sleep 1\n"
    "buffer_load_dword v80, v83, s[8:11], s59 offen offset:0 sc1\n"
    "s_waitcnt vmcnt(0)\n"
    "v_cmp_ne_u32_e32 vcc, s39, v80\n"
    "s_sub_u32 s53, s53, 1\n"
    "s_cbranch_scc1 .Lat_h1poll_ret\n"
    "s_nop 2\n"
    "s_cbranch_vccnz .Lat_h1poll_l\n"
    "s_branch .Lat_h1poll_ret\n"
    ".Lat_qpoll:\n"
    "s_mov_b32 s53, 0x400000\n"
    ".Lat_qpoll_l:\n"
    "s_sleep 0\n"
    "buffer_load_dword v254, v253, s[8:11], s47 offen offset:0 sc1\n"
    "s_waitcnt vmcnt(0)\n"
    "v_cmp_ne_u32_e32 vcc, s39, v254\n"
    "s_sub_u32 s53, s53, 1\n"
    "s_cbranch_scc1 .Lat_qpoll_ret\n"
    "s_nop 2\n"
    "s_cbranch_vccnz .Lat_qpoll_l\n"
    "s_branch .Lat_qpoll_ret\n"
    ".Lat_qx:\n"
    "s_and_b32 s45, s32, 1\n"
    "s_xor_b32 s45, s45, s50\n"
    "s_add_u32 s45, s45, 8\n"
    "s_lshl_b32 s45, s45, 13\n"
    "s_add_u32 s45, s45, s48\n"
    "s_add_u32 s46, s45, 4096\n"
    "buffer_load_dwordx4 a[32:35], v250, s[8:11], s45 offen offset:0 sc1\n"
    "buffer_load_dwordx4 a[36:39], v250, s[8:11], s45 offen offset:1024 sc1\n"
    "buffer_load_dwordx4 a[40:43], v250, s[8:11], s45 offen offset:2048 sc1\n"
    "buffer_load_dwordx4 a[44:47], v250, s[8:11], s45 offen offset:3072 sc1\n"
    "buffer_load_dwordx4 a[48:51], v250, s[8:11], s46 offen offset:0 sc1\n"
    "buffer_load_dwordx4 a[52:55], v250, s[8:11], s46 offen offset:1024 sc1\n"
    "buffer_load_dwordx4 a[56:59], v250, s[8:11], s46 offen offset:2048 sc1\n"
    "buffer_load_dwordx4 a[60:63], v250, s[8:11], s46 offen offset:3072 sc1\n"
    "s_branch .Lat_qx_ret\n"
    ".Lat_hpoll:\n"
    "s_mov_b32 s53, 0x400000\n"
    ".Lat_hpoll_l:\n"
    "s_sleep 1\n"
    "buffer_load_dword v80, v250, s[8:11], s59 offen offset:0 sc1\n"
    "buffer_load_dword v81, v82, s[8:11], s59 offen offset:0 sc1\n"
    "s_waitcnt vmcnt(0)\n"
    "v_xor_b32_e32 v81, s39, v81\n"
    "v_xor_b32_e32 v80, s39, v80\n"
    "v_or_b32_e32 v80, v80, v81\n"
    "v_cmp_ne_u32_e32 vcc, 0, v80\n"
    "s_sub_u32 s53, s53, 1\n"
    "s_cbranch_scc1 .Lat_hpoll_ret\n"
    "s_nop 2\n"
    "s_cbranch_vccnz .Lat_hpoll_l\n"
    "s_branch .Lat_hpoll_ret\n"
    ".Lat_redo:\n"
    "s_waitcnt vmcnt(0) lgkmcnt(0)\n"
    "s_barrier\n"
    "s_mov_b32 s49, 1\n"
    "v_lshrrev_b32_e32 v224, 4, v250\n"
    "s_and_b32 s50, %2, 1\n"
    "s_lshr_b32 s51, %2, 1\n"
    "v_lshrrev_b32_e32 v225, 3, v224\n"
    "v_and_b32_e32 v226, 6, v225\n"
    "v_and_b32_e32 v227, 7, v224\n"
    "v_xor_b32_e32 v226, v226, v227\n"
    "v_lshlrev_b32_e32 v226, 4, v226\n"
    "v_and_b32_e32 v227, 4, v225\n"
    "v_add_u32_e32 v227, v227, v225\n"
    "v_lshl_add_u32 v251, v227, 9, v226\n"
    "v_add_u32_e32 v252, 0x1c00, v251\n"
    "v_and_b32_e32 v227, 1, v225\n"
    "v_lshl_add_u32 v248, v227, 9, v226\n"
    "v_lshrrev_b32_e32 v227, 1, v225\n"
    "v_lshl_add_u32 v248, v227, 12, v248\n"
    "s_mul_i32 s43, s50, 3\n"
    "s_xor_b32 s43, s43, s51\n"
    "s_lshl_b32 s43, s43, 15\n"
    "s_add_u32 s43, s43, 32768\n"
    "s_mul_i32 s42, %2, 5120\n"
    "s_lshl_b32 s47, s50, 8\n"
    "s_mul_i32 s40, s32, 1277952\n"
    "s_lshl_b32 s41, s51, 14\n"
    "s_add_u32 s40, s40, s41\n"
    "s_add_u32 s40, s40, s47\n"
    "s_add_u32 s41, s40, 128\n"
    "v_bfe_u32 v225, v224, 2, 3\n"
    "v_lshlrev_b32_e32 v244, 7, v225\n"
    "v_lshrrev_b32_e32 v226, 5, v224\n"
    "v_lshl_add_u32 v244, v226, 10, v244\n"
    "v_and_b32_e32 v225, 6, v225\n"
    "v_bfe_u32 v226, v224, 1, 1\n"
    "v_xor_b32_e32 v225, v225, v226\n"
    "v_lshl_add_u32 v244, v225, 4, v244\n"
    "v_and_b32_e32 v226, 1, v224\n"
    "v_lshl_add_u32 v244, v226, 3, v244\n"
    "s_lshl_b32 s47, s51, 16\n"
    "s_add_u32 s47, s47, 32768\n"
    "v_add_u32_e32 v244, s47, v244\n"
    "v_xor_b32_e32 v245, 32, v244\n"
    "v_xor_b32_e32 v246, 64, v244\n"
    "v_xor_b32_e32 v247, 0x60, v244\n"
    "v_and_b32_e32 v225, 7, v224\n"
    "v_lshlrev_b32_e32 v227, 7, v225\n"
    "v_bfe_u32 v226, v224, 3, 1\n"
    "v_lshl_add_u32 v227, v226, 10, v227\n"
    "v_and_b32_e32 v225, 6, v224\n"
    "v_lshrrev_b32_e32 v226, 4, v224\n"
    "v_xor_b32_e32 v225, v225, v226\n"
    "v_lshl_add_u32 v227, v225, 4, v227\n"
    "s_lshl_b32 s47, s51, 15\n"
    "s_add_u32 s48, s47, 32768\n"
    "s_sub_u32 s47, 131072, s47\n"
    "v_add_u32_e32 v240, s48, v227\n"
    "v_xor_b32_e32 v241, 64, v240\n"
    "v_add_u32_e32 v242, s47, v227\n"
    "v_xor_b32_e32 v243, 64, v242\n"
    "s_xor_b32 s47, %2, 2\n"
    "s_mul_i32 s47, s47, 5120\n"
    "v_lshl_add_u32 v249, v224, 2, s47\n"
    "v_bfe_u32 v253, v250, 4, 2\n"
    "v_max_u32_e32 v253, 1, v253\n"
    "v_add_u32_e32 v253, s32, v253\n"
    "v_and_b32_e32 v253, 3, v253\n"
    "v_lshlrev_b32_e32 v253, 7, v253\n"
    "v_mov_b32_e32 v84, s39\n"
    "s_lshl_b32 s58, s34, 7\n"
    "s_add_u32 s58, s58, 5945344\n"
    "s_cmp_eq_u32 %2, 2\n"
    "s_cselect_b32 s58, s58, 2.0\n"
    "s_lshl_b32 s45, s50, 13\n"
    "v_add_u32_e32 v85, s45, v250\n"
    "v_lshrrev_b32_e32 v105, 7, v250\n"
    "v_and_b32_e32 v104, 0x70, v251\n"
    "v_lshl_add_u32 v104, v105, 9, v104\n"
    "v_add_u32_e32 v105, 0xc00, v104\n"
    "v_add_u32_e32 v106, 0x1800, v104\n"
    "v_add_u32_e32 v107, 0x2400, v104\n"
    "s_lshl_b32 s57, s50, 8\n"
    "s_add_u32 s57, s57, 5111808\n"
    "v_accvgpr_write_b32 a244, 0\n"
    "v_accvgpr_write_b32 a245, 0\n"
    "v_accvgpr_write_b32 a246, 0\n"
    "v_accvgpr_write_b32 a247, 0\n"
    "v_accvgpr_write_b32 a248, 0\n"
    "v_accvgpr_write_b32 a249, 0\n"
    "v_accvgpr_write_b32 a250, 0\n"
    "v_accvgpr_write_b32 a251, 0\n"
    "v_accvgpr_write_b32 a252, 0\n"
    "v_accvgpr_write_b32 a253, 0\n"
    "v_accvgpr_write_b32 a254, 0\n"
    "v_accvgpr_write_b32 a255, 0\n"
    "v_mov_b64_e32 v[8:9], 0\n"
    "v_mov_b64_e32 v[10:11], 0\n"
    "v_mov_b64_e32 v[12:13], 0\n"
    "v_mov_b64_e32 v[14:15], 0\n"
    "v_mov_b64_e32 v[16:17], 0\n"
    "v_mov_b64_e32 v[18:19], 0\n"
    "v_mov_b64_e32 v[28:29], 0\n"
    "v_mov_b64_e32 v[30:31], 0\n"
    "v_mov_b64_e32 v[32:33], 0\n"
    "v_mov_b64_e32 v[34:35], 0\n"
    "v_mov_b64_e32 v[36:37], 0\n"
    "v_mov_b64_e32 v[38:39], 0\n"
    "v_mov_b64_e32 v[48:49], 0\n"
    "v_mov_b64_e32 v[50:51], 0\n"
    "v_mov_b64_e32 v[52:53], 0\n"
    "v_mov_b64_e32 v[54:55], 0\n"
    "v_mov_b64_e32 v[56:57], 0\n"
    "v_mov_b64_e32 v[58:59], 0\n"
    "v_mov_b64_e32 v[68:69], 0\n"
    "v_mov_b64_e32 v[70:71], 0\n"
    "v_mov_b64_e32 v[72:73], 0\n"
    "v_mov_b64_e32 v[74:75], 0\n"
    "v_mov_b64_e32 v[76:77], 0\n"
    "v_mov_b64_e32 v[78:79], 0\n"
    "v_mov_b64_e32 v[88:89], 0\n"
    "v_mov_b64_e32 v[90:91], 0\n"
    "v_mov_b64_e32 v[92:93], 0\n"
    "v_mov_b64_e32 v[94:95], 0\n"
    "v_mov_b64_e32 v[96:97], 0\n"
    "v_mov_b64_e32 v[98:99], 0\n"
    "v_mov_b64_e32 v[108:109], 0\n"
    "v_mov_b64_e32 v[110:111], 0\n"
    "v_mov_b64_e32 v[112:113], 0\n"
    "v_mov_b64_e32 v[114:115], 0\n"
    "v_mov_b64_e32 v[116:117], 0\n"
    "v_mov_b64_e32 v[118:119], 0\n"
    "v_mov_b64_e32 v[128:129], 0\n"
    "v_mov_b64_e32 v[130:131], 0\n"
    "v_mov_b64_e32 v[132:133], 0\n"
    "v_mov_b64_e32 v[134:135], 0\n"
    "v_mov_b64_e32 v[136:137], 0\n"
    "v_mov_b64_e32 v[138:139], 0\n"
    "s_add_u32 m0, s43, 24576\n"
    "s_nop 0\n"
    "buffer_load_dwordx4 v104, s[8:11], s57 offen offset:0 sc1 lds\n"
    "buffer_load_dwordx4 v105, s[8:11], s57 offen offset:1024 sc1 lds\n"
    "s_add_u32 m0, s43, 28672\n"
    "s_add_u32 s57, s57, 128\n"
    "buffer_load_dwordx4 v104, s[8:11], s57 offen offset:0 sc1 lds\n"
    "buffer_load_dwordx4 v105, s[8:11], s57 offen offset:1024 sc1 lds\n"
    "s_add_u32 m0, s43, 0\n"
    "s_nop 0\n"
    "buffer_load_dwordx4 v104, s[8:11], s40 offen offset:0 sc1 lds\n"
    "buffer_load_dwordx4 v105, s[8:11], s40 offen offset:1024 sc1 lds\n"
    "buffer_load_dwordx4 v106, s[8:11], s40 offen offset:2048 sc1 lds\n"
    "buffer_load_dwordx4 v107, s[8:11], s40 offen offset:3072 sc1 lds\n"
    "s_add_u32 s40, s40, 0x8000\n"
    "s_add_u32 m0, s43, 4096\n"
    "s_nop 0\n"
    "buffer_load_dwordx4 v104, s[8:11], s41 offen offset:0 sc1 lds\n"
    "buffer_load_dwordx4 v105, s[8:11], s41 offen offset:1024 sc1 lds\n"
    "buffer_load_dwordx4 v106, s[8:11], s41 offen offset:2048 sc1 lds\n"
    "buffer_load_dwordx4 v107, s[8:11], s41 offen offset:3072 sc1 lds\n"
    "s_add_u32 s41, s41, 0x8000\n"
    "s_add_u32 m0, s43, 8192\n"
    "s_nop 0\n"
    "buffer_load_dwordx4 v104, s[8:11], s40 offen offset:0 sc1 lds\n"
    "buffer_load_dwordx4 v105, s[8:11], s40 offen offset:1024 sc1 lds\n"
    "buffer_load_dwordx4 v106, s[8:11], s40 offen offset:2048 sc1 lds\n"
    "buffer_load_dwordx4 v107, s[8:11], s40 offen offset:3072 sc1 lds\n"
    "s_add_u32 s40, s40, 0x8000\n"
    "s_add_u32 m0, s43, 12288\n"
    "s_nop 0\n"
    "buffer_load_dwordx4 v104, s[8:11], s41 offen offset:0 sc1 lds\n"
    "buffer_load_dwordx4 v105, s[8:11], s41 offen offset:1024 sc1 lds\n"
    "buffer_load_dwordx4 v106, s[8:11], s41 offen offset:2048 sc1 lds\n"
    "buffer_load_dwordx4 v107, s[8:11], s41 offen offset:3072 sc1 lds\n"
    "s_add_u32 s41, s41, 0x8000\n"
    "v_mov_b32_e32 v80, s39\n"
    "v_mov_b32_e32 v81, s39\n"
    "s_waitcnt vmcnt(0)\n"
    "s_branch .Lat_p2\n"
    ".Lat_maxpass:\n"
    "s_waitcnt lgkmcnt(0)\n"
    "s_mul_i32 s48, s33, 81920\n"
    "s_add_u32 s48, s48, 11243520\n"
    "s_add_u32 s45, s32, 0\n"
    "s_and_b32 s45, s45, 3\n"
    "s_lshl2_add_u32 s45, s50, s45\n"
    "s_lshl_b32 s45, s45, 13\n"
    "s_add_u32 s45, s45, s48\n"
    "s_add_u32 s46, s45, 4096\n"
    "buffer_load_dwordx4 a[0:3], v250, s[8:11], s45 offen offset:0 sc1\n"
    "buffer_load_dwordx4 a[4:7], v250, s[8:11], s45 offen offset:1024 sc1\n"
    "buffer_load_dwordx4 a[8:11], v250, s[8:11], s45 offen offset:2048 sc1\n"
    "buffer_load_dwordx4 a[12:15], v250, s[8:11], s45 offen offset:3072 sc1\n"
    "buffer_load_dwordx4 a[16:19], v250, s[8:11], s46 offen offset:0 sc1\n"
    "buffer_load_dwordx4 a[20:23], v250, s[8:11], s46 offen offset:1024 sc1\n"
    "buffer_load_dwordx4 a[24:27], v250, s[8:11], s46 offen offset:2048 sc1\n"
    "buffer_load_dwordx4 a[28:31], v250, s[8:11], s46 offen offset:3072 sc1\n"
    "s_and_b32 s45, s32, 1\n"
    "s_xor_b32 s45, s45, s50\n"
    "s_add_u32 s45, s45, 8\n"
    "s_lshl_b32 s45, s45, 13\n"
    "s_add_u32 s45, s45, s48\n"
    "s_add_u32 s46, s45, 4096\n"
    "buffer_load_dwordx4 a[32:35], v250, s[8:11], s45 offen offset:0 sc1\n"
    "buffer_load_dwordx4 a[36:39], v250, s[8:11], s45 offen offset:1024 sc1\n"
    "buffer_load_dwordx4 a[40:43], v250, s[8:11], s45 offen offset:2048 sc1\n"
    "buffer_load_dwordx4 a[44:47], v250, s[8:11], s45 offen offset:3072 sc1\n"
    "buffer_load_dwordx4 a[48:51], v250, s[8:11], s46 offen offset:0 sc1\n"
    "buffer_load_dwordx4 a[52:55], v250, s[8:11], s46 offen offset:1024 sc1\n"
    "buffer_load_dwordx4 a[56:59], v250, s[8:11], s46 offen offset:2048 sc1\n"
    "buffer_load_dwordx4 a[60:63], v250, s[8:11], s46 offen offset:3072 sc1\n"
    "s_add_u32 s45, s32, 1\n"
    "s_and_b32 s45, s45, 3\n"
    "s_lshl2_add_u32 s45, s50, s45\n"
    "s_lshl_b32 s45, s45, 13\n"
    "s_add_u32 s45, s45, s48\n"
    "s_add_u32 s46, s45, 4096\n"
    "buffer_load_dwordx4 a[64:67], v250, s[8:11], s45 offen offset:0 sc1\n"
    "buffer_load_dwordx4 a[68:71], v250, s[8:11], s45 offen offset:1024 sc1\n"
    "buffer_load_dwordx4 a[72:75], v250, s[8:11], s45 offen offset:2048 sc1\n"
    "buffer_load_dwordx4 a[76:79], v250, s[8:11], s45 offen offset:3072 sc1\n"
    "buffer_load_dwordx4 a[80:83], v250, s[8:11], s46 offen offset:0 sc1\n"
    "buffer_load_dwordx4 a[84:87], v250, s[8:11], s46 offen offset:1024 sc1\n"
    "buffer_load_dwordx4 a[88:91], v250, s[8:11], s46 offen offset:2048 sc1\n"
    "buffer_load_dwordx4 a[92:95], v250, s[8:11], s46 offen offset:3072 sc1\n"
    "s_add_u32 s45, s32, 2\n"
    "s_and_b32 s45, s45, 3\n"
    "s_lshl2_add_u32 s45, s50, s45\n"
    "s_lshl_b32 s45, s45, 13\n"
    "s_add_u32 s45, s45, s48\n"
    "s_add_u32 s46, s45, 4096\n"
    "buffer_load_dwordx4 a[96:99], v250, s[8:11], s45 offen offset:0 sc1\n"
    "buffer_load_dwordx4 a[100:103], v250, s[8:11], s45 offen offset:1024 sc1\n"
    "buffer_load_dwordx4 a[104:107], v250, s[8:11], s45 offen offset:2048 sc1\n"
    "buffer_load_dwordx4 a[108:111], v250, s[8:11], s45 offen offset:3072 sc1\n"
    "buffer_load_dwordx4 a[112:115], v250, s[8:11], s46 offen offset:0 sc1\n"
    "buffer_load_dwordx4 a[116:119], v250, s[8:11], s46 offen offset:1024 sc1\n"
    "buffer_load_dwordx4 a[120:123], v250, s[8:11], s46 offen offset:2048 sc1\n"
    "buffer_load_dwordx4 a[124:127], v250, s[8:11], s46 offen offset:3072 sc1\n"
    "s_add_u32 s45, s32, 3\n"
    "s_and_b32 s45, s45, 3\n"
    "s_lshl2_add_u32 s45, s50, s45\n"
    "s_lshl_b32 s45, s45, 13\n"
    "s_add_u32 s45, s45, s48\n"
    "s_add_u32 s46, s45, 4096\n"
    "buffer_load_dwordx4 a[128:131], v250, s[8:11], s45 offen offset:0 sc1\n"
    "buffer_load_dwordx4 a[132:135], v250, s[8:11], s45 offen offset:1024 sc1\n"
    "buffer_load_dwordx4 a[136:139], v250, s[8:11], s45 offen offset:2048 sc1\n"
    "buffer_load_dwordx4 a[140:143], v250, s[8:11], s45 offen offset:3072 sc1\n"
    "buffer_load_dwordx4 a[144:147], v250, s[8:11], s46 offen offset:0 sc1\n"
    "buffer_load_dwordx4 a[148:151], v250, s[8:11], s46 offen offset:1024 sc1\n"
    "buffer_load_dwordx4 a[152:155], v250, s[8:11], s46 offen offset:2048 sc1\n"
    "buffer_load_dwordx4 a[156:159], v250, s[8:11], s46 offen offset:3072 sc1\n"
    "s_waitcnt vmcnt(0)\n"
    "v_mov_b32_e32 v200, 0xff800000\n"
    "v_mov_b32_e32 v201, 0xff800000\n"
    "v_mov_b32_e32 v202, 0xff800000\n"
    "v_mov_b32_e32 v203, 0xff800000\n"
    "v_mov_b32_e32 v204, 0xff800000\n"
    "s_lshl_b32 s45, s51, 14\n"
    "s_lshl_b32 s46, s50, 8\n"
    "s_add_u32 s45, s45, s46\n"
    "s_add_u32 s61, s46, 5103616\n"
    "s_movk_i32 s48, 157\n"
    ".Lat_mp_loop:\n"
    "s_min_u32 s45, s45, s61\n"
    "s_add_u32 s46, s45, 128\n"
    "s_add_u32 m0, s43, 16384\n"
    "s_nop 0\n"
    "buffer_load_dwordx4 v248, s[8:11], s45 offen offset:0 sc1 lds\n"
    "buffer_load_dwordx4 v248, s[8:11], s45 offen offset:1024 sc1 lds\n"
    "buffer_load_dwordx4 v248, s[8:11], s45 offen offset:2048 sc1 lds\n"
    "buffer_load_dwordx4 v248, s[8:11], s45 offen offset:3072 sc1 lds\n"
    "s_add_u32 m0, s43, 20480\n"
    "s_nop 0\n"
    "buffer_load_dwordx4 v248, s[8:11], s46 offen offset:0 sc1 lds\n"
    "buffer_load_dwordx4 v248, s[8:11], s46 offen offset:1024 sc1 lds\n"
    "buffer_load_dwordx4 v248, s[8:11], s46 offen offset:2048 sc1 lds\n"
    "buffer_load_dwordx4 v248, s[8:11], s46 offen offset:3072 sc1 lds\n"
    "s_waitcnt vmcnt(0)\n"
    "s_barrier\n"
    "ds_read_b128 a[160:163], v240 offset:16384\n"
    "ds_read_b128 a[164:167], v241 offset:16384\n"
    "ds_read_b128 a[168:171], v240 offset:20480\n"
    "ds_read_b128 a[172:175], v241 offset:20480\n"
    "ds_read_b128 a[176:179], v242 offset:16384\n"
    "ds_read_b128 a[180:183], v243 offset:16384\n"
    "ds_read_b128 a[184:187], v242 offset:20480\n"
    "ds_read_b128 a[188:191], v243 offset:20480\n"
    "s_waitcnt lgkmcnt(0)\n"
    "v_mfma_f32_16x16x32_bf16 v[180:183], a[160:163], a[0:3], 0\n"
    "v_mfma_f32_16x16x32_bf16 v[184:187], a[160:163], a[32:35], 0\n"
    "v_mfma_f32_16x16x32_bf16 v[188:191], a[160:163], a[64:67], 0\n"
    "v_mfma_f32_16x16x32_bf16 v[192:195], a[160:163], a[96:99], 0\n"
    "v_mfma_f32_16x16x32_bf16 v[196:199], a[160:163], a[128:131], 0\n"
    "v_mfma_f32_16x16x32_bf16 v[180:183], a[164:167], a[4:7], v[180:183]\n"
    "v_mfma_f32_16x16x32_bf16 v[184:187], a[164:167], a[36:39], v[184:187]\n"
    "v_mfma_f32_16x16x32_bf16 v[188:191], a[164:167], a[68:71], v[188:191]\n"
    "v_mfma_f32_16x16x32_bf16 v[192:195], a[164:167], a[100:103], v[192:195]\n"
    "v_mfma_f32_16x16x32_bf16 v[196:199], a[164:167], a[132:135], v[196:199]\n"
    "v_mfma_f32_16x16x32_bf16 v[180:183], a[168:171], a[8:11], v[180:183]\n"
    "v_mfma_f32_16x16x32_bf16 v[184:187], a[168:171], a[40:43], v[184:187]\n"
    "v_mfma_f32_16x16x32_bf16 v[188:191], a[168:171], a[72:75], v[188:191]\n"
    "v_mfma_f32_16x16x32_bf16 v[192:195], a[168:171], a[104:107], v[192:195]\n"
    "v_mfma_f32_16x16x32_bf16 v[196:199], a[168:171], a[136:139], v[196:199]\n"
    "v_mfma_f32_16x16x32_bf16 v[180:183], a[172:175], a[12:15], v[180:183]\n"
    "v_mfma_f32_16x16x32_bf16 v[184:187], a[172:175], a[44:47], v[184:187]\n"
    "v_mfma_f32_16x16x32_bf16 v[188:191], a[172:175], a[76:79], v[188:191]\n"
    "v_mfma_f32_16x16x32_bf16 v[192:195], a[172:175], a[108:111], v[192:195]\n"
    "v_mfma_f32_16x16x32_bf16 v[196:199], a[172:175], a[140:143], v[196:199]\n"
    "v_mfma_f32_16x16x32_bf16 v[180:183], a[176:179], a[16:19], v[180:183]\n"
    "v_mfma_f32_16x16x32_bf16 v[184:187], a[176:179], a[48:51], v[184:187]\n"
    "v_mfma_f32_16x16x32_bf16 v[188:191], a[176:179], a[80:83], v[188:191]\n"
    "v_mfma_f32_16x16x32_bf16 v[192:195], a[176:179], a[112:115], v[192:195]\n"
    "v_mfma_f32_16x16x32_bf16 v[196:199], a[176:179], a[144:147], v[196:199]\n"
    "v_mfma_f32_16x16x32_bf16 v[180:183], a[180:183], a[20:23], v[180:183]\n"
    "v_mfma_f32_16x16x32_bf16 v[184:187], a[180:183], a[52:55], v[184:187]\n"
    "v_mfma_f32_16x16x32_bf16 v[188:191], a[180:183], a[84:87], v[188:191]\n"
    "v_mfma_f32_16x16x32_bf16 v[192:195], a[180:183], a[116:119], v[192:195]\n"
    "v_mfma_f32_16x16x32_bf16 v[196:199], a[180:183], a[148:151], v[196:199]\n"
    "v_mfma_f32_16x16x32_bf16 v[180:183], a[184:187], a[24:27], v[180:183]\n"
    "v_mfma_f32_16x16x32_bf16 v[184:187], a[184:187], a[56:59], v[184:187]\n"
    "v_mfma_f32_16x16x32_bf16 v[188:191], a[184:187], a[88:91], v[188:191]\n"
    "v_mfma_f32_16x16x32_bf16 v[192:195], a[184:187], a[120:123], v[192:195]\n"
    "v_mfma_f32_16x16x32_bf16 v[196:199], a[184:187], a[152:155], v[196:199]\n"
    "v_mfma_f32_16x16x32_bf16 v[180:183], a[188:191], a[28:31], v[180:183]\n"
    "v_mfma_f32_16x16x32_bf16 v[184:187], a[188:191], a[60:63], v[184:187]\n"
    "v_mfma_f32_16x16x32_bf16 v[188:191], a[188:191], a[92:95], v[188:191]\n"
    "v_mfma_f32_16x16x32_bf16 v[192:195], a[188:191], a[124:127], v[192:195]\n"
    "v_mfma_f32_16x16x32_bf16 v[196:199], a[188:191], a[156:159], v[196:199]\n"
    "s_nop 15\n"
    "v_max3_f32 v200, v200, v180, v181\n"
    "v_max3_f32 v200, v200, v182, v183\n"
    "v_max3_f32 v201, v201, v184, v185\n"
    "v_max3_f32 v201, v201, v186, v187\n"
    "v_max3_f32 v202, v202, v188, v189\n"
    "v_max3_f32 v202, v202, v190, v191\n"
    "v_max3_f32 v203, v203, v192, v193\n"
    "v_max3_f32 v203, v203, v194, v195\n"
    "v_max3_f32 v204, v204, v196, v197\n"
    "v_max3_f32 v204, v204, v198, v199\n"
    "s_nop 3\n"
    "ds_read_b128 a[160:163], v240 offset:18432\n"
    "ds_read_b128 a[164:167], v241 offset:18432\n"
    "ds_read_b128 a[168:171], v240 offset:22528\n"
    "ds_read_b128 a[172:175], v241 offset:22528\n"
    "ds_read_b128 a[176:179], v242 offset:18432\n"
    "ds_read_b128 a[180:183], v243 offset:18432\n"
    "ds_read_b128 a[184:187], v242 offset:22528\n"
    "ds_read_b128 a[188:191], v243 offset:22528\n"
    "s_waitcnt lgkmcnt(0)\n"
    "v_mfma_f32_16x16x32_bf16 v[180:183], a[160:163], a[0:3], 0\n"
    "v_mfma_f32_16x16x32_bf16 v[184:187], a[160:163], a[32:35], 0\n"
    "v_mfma_f32_16x16x32_bf16 v[188:191], a[160:163], a[64:67], 0\n"
    "v_mfma_f32_16x16x32_bf16 v[192:195], a[160:163], a[96:99], 0\n"
    "v_mfma_f32_16x16x32_bf16 v[196:199], a[160:163], a[128:131], 0\n"
    "v_mfma_f32_16x16x32_bf16 v[180:183], a[164:167], a[4:7], v[180:183]\n"
    "v_mfma_f32_16x16x32_bf16 v[184:187], a[164:167], a[36:39], v[184:187]\n"
    "v_mfma_f32_16x16x32_bf16 v[188:191], a[164:167], a[68:71], v[188:191]\n"
    "v_mfma_f32_16x16x32_bf16 v[192:195], a[164:167], a[100:103], v[192:195]\n"
    "v_mfma_f32_16x16x32_bf16 v[196:199], a[164:167], a[132:135], v[196:199]\n"
    "v_mfma_f32_16x16x32_bf16 v[180:183], a[168:171], a[8:11], v[180:183]\n"
    "v_mfma_f32_16x16x32_bf16 v[184:187], a[168:171], a[40:43], v[184:187]\n"
    "v_mfma_f32_16x16x32_bf16 v[188:191], a[168:171], a[72:75], v[188:191]\n"
    "v_mfma_f32_16x16x32_bf16 v[192:195], a[168:171], a[104:107], v[192:195]\n"
    "v_mfma_f32_16x16x32_bf16 v[196:199], a[168:171], a[136:139], v[196:199]\n"
    "v_mfma_f32_16x16x32_bf16 v[180:183], a[172:175], a[12:15], v[180:183]\n"
    "v_mfma_f32_16x16x32_bf16 v[184:187], a[172:175], a[44:47], v[184:187]\n"
    "v_mfma_f32_16x16x32_bf16 v[188:191], a[172:175], a[76:79], v[188:191]\n"
    "v_mfma_f32_16x16x32_bf16 v[192:195], a[172:175], a[108:111], v[192:195]\n"
    "v_mfma_f32_16x16x32_bf16 v[196:199], a[172:175], a[140:143], v[196:199]\n"
    "v_mfma_f32_16x16x32_bf16 v[180:183], a[176:179], a[16:19], v[180:183]\n"
    "v_mfma_f32_16x16x32_bf16 v[184:187], a[176:179], a[48:51], v[184:187]\n"
    "v_mfma_f32_16x16x32_bf16 v[188:191], a[176:179], a[80:83], v[188:191]\n"
    "v_mfma_f32_16x16x32_bf16 v[192:195], a[176:179], a[112:115], v[192:195]\n"
    "v_mfma_f32_16x16x32_bf16 v[196:199], a[176:179], a[144:147], v[196:199]\n"
    "v_mfma_f32_16x16x32_bf16 v[180:183], a[180:183], a[20:23], v[180:183]\n"
    "v_mfma_f32_16x16x32_bf16 v[184:187], a[180:183], a[52:55], v[184:187]\n"
    "v_mfma_f32_16x16x32_bf16 v[188:191], a[180:183], a[84:87], v[188:191]\n"
    "v_mfma_f32_16x16x32_bf16 v[192:195], a[180:183], a[116:119], v[192:195]\n"
    "v_mfma_f32_16x16x32_bf16 v[196:199], a[180:183], a[148:151], v[196:199]\n"
    "v_mfma_f32_16x16x32_bf16 v[180:183], a[184:187], a[24:27], v[180:183]\n"
    "v_mfma_f32_16x16x32_bf16 v[184:187], a[184:187], a[56:59], v[184:187]\n"
    "v_mfma_f32_16x16x32_bf16 v[188:191], a[184:187], a[88:91], v[188:191]\n"
    "v_mfma_f32_16x16x32_bf16 v[192:195], a[184:187], a[120:123], v[192:195]\n"
    "v_mfma_f32_16x16x32_bf16 v[196:199], a[184:187], a[152:155], v[196:199]\n"
    "v_mfma_f32_16x16x32_bf16 v[180:183], a[188:191], a[28:31], v[180:183]\n"
    "v_mfma_f32_16x16x32_bf16 v[184:187], a[188:191], a[60:63], v[184:187]\n"
    "v_mfma_f32_16x16x32_bf16 v[188:191], a[188:191], a[92:95], v[188:191]\n"
    "v_mfma_f32_16x16x32_bf16 v[192:195], a[188:191], a[124:127], v[192:195]\n"
    "v_mfma_f32_16x16x32_bf16 v[196:199], a[188:191], a[156:159], v[196:199]\n"
    "s_nop 15\n"
    "v_max3_f32 v200, v200, v180, v181\n"
    "v_max3_f32 v200, v200, v182, v183\n"
    "v_max3_f32 v201, v201, v184, v185\n"
    "v_max3_f32 v201, v201, v186, v187\n"
    "v_max3_f32 v202, v202, v188, v189\n"
    "v_max3_f32 v202, v202, v190, v191\n"
    "v_max3_f32 v203, v203, v192, v193\n"
    "v_max3_f32 v203, v203, v194, v195\n"
    "v_max3_f32 v204, v204, v196, v197\n"
    "v_max3_f32 v204, v204, v198, v199\n"
    "s_nop 3\n"
    "s_barrier\n"
    "s_add_u32 s45, s45, 0x8000\n"
    "s_sub_u32 s48, s48, 1\n"
    "s_cmp_lg_u32 s48, 0\n"
    "s_cbranch_scc1 .Lat_mp_loop\n"
    "s_mov_b32 m0, s42\n"
    "s_nop 1\n"
    "ds_write_addtid_b32 v200 offset:0\n"
    "ds_write_addtid_b32 v201 offset:256\n"
    "ds_write_addtid_b32 v202 offset:512\n"
    "ds_write_addtid_b32 v203 offset:768\n"
    "ds_write_addtid_b32 v204 offset:1024\n"
    "s_waitcnt lgkmcnt(0)\n"
    "s_barrier\n"
    "ds_read_b32 v180, v249 offset:0\n"
    "ds_read_b32 v184, v249 offset:256\n"
    "ds_read_b32 v188, v249 offset:512\n"
    "ds_read_b32 v192, v249 offset:768\n"
    "ds_read_b32 v196, v249 offset:1024\n"
    "s_waitcnt lgkmcnt(0)\n"
    "v_max_f32_e32 v205, v200, v180\n"
    "v_mov_b32_e32 v206, v205\n"
    "s_nop 1\n"
    "v_permlane16_swap_b32_e32 v205, v206\n"
    "s_nop 0\n"
    "v_max_f32_e32 v205, v205, v206\n"
    "v_mov_b32_e32 v206, v205\n"
    "s_nop 1\n"
    "v_permlane32_swap_b32_e32 v205, v206\n"
    "s_nop 0\n"
    "v_max_f32_e32 v205, v205, v206\n"
    "v_add_f32_e32 v205, 0x42980000, v205\n"
    "v_xor_b32_e32 v220, 0x80000000, v205\n"
    "v_xor_b32_e32 v221, 0x80000000, v205\n"
    "v_xor_b32_e32 v222, 0x80000000, v205\n"
    "v_xor_b32_e32 v223, 0x80000000, v205\n"
    "v_max_f32_e32 v205, v201, v184\n"
    "v_mov_b32_e32 v206, v205\n"
    "s_nop 1\n"
    "v_permlane16_swap_b32_e32 v205, v206\n"
    "s_nop 0\n"
    "v_max_f32_e32 v205, v205, v206\n"
    "v_mov_b32_e32 v206, v205\n"
    "s_nop 1\n"
    "v_permlane32_swap_b32_e32 v205, v206\n"
    "s_nop 0\n"
    "v_max_f32_e32 v205, v205, v206\n"
    "v_add_f32_e32 v205, 0x42980000, v205\n"
    "v_xor_b32_e32 v224, 0x80000000, v205\n"
    "v_xor_b32_e32 v225, 0x80000000, v205\n"
    "v_xor_b32_e32 v226, 0x80000000, v205\n"
    "v_xor_b32_e32 v227, 0x80000000, v205\n"
    "v_max_f32_e32 v205, v202, v188\n"
    "v_mov_b32_e32 v206, v205\n"
    "s_nop 1\n"
    "v_permlane16_swap_b32_e32 v205, v206\n"
    "s_nop 0\n"
    "v_max_f32_e32 v205, v205, v206\n"
    "v_mov_b32_e32 v206, v205\n"
    "s_nop 1\n"
    "v_permlane32_swap_b32_e32 v205, v206\n"
    "s_nop 0\n"
    "v_max_f32_e32 v205, v205, v206\n"
    "v_add_f32_e32 v205, 0x42980000, v205\n"
    "v_xor_b32_e32 v228, 0x80000000, v205\n"
    "v_xor_b32_e32 v229, 0x80000000, v205\n"
    "v_xor_b32_e32 v230, 0x80000000, v205\n"
    "v_xor_b32_e32 v231, 0x80000000, v205\n"
    "v_max_f32_e32 v205, v203, v192\n"
    "v_mov_b32_e32 v206, v205\n"
    "s_nop 1\n"
    "v_permlane16_swap_b32_e32 v205, v206\n"
    "s_nop 0\n"
    "v_max_f32_e32 v205, v205, v206\n"
    "v_mov_b32_e32 v206, v205\n"
    "s_nop 1\n"
    "v_permlane32_swap_b32_e32 v205, v206\n"
    "s_nop 0\n"
    "v_max_f32_e32 v205, v205, v206\n"
    "v_add_f32_e32 v205, 0x42980000, v205\n"
    "v_xor_b32_e32 v232, 0x80000000, v205\n"
    "v_xor_b32_e32 v233, 0x80000000, v205\n"
    "v_xor_b32_e32 v234, 0x80000000, v205\n"
    "v_xor_b32_e32 v235, 0x80000000, v205\n"
    "v_max_f32_e32 v205, v204, v196\n"
    "v_mov_b32_e32 v206, v205\n"
    "s_nop 1\n"
    "v_permlane16_swap_b32_e32 v205, v206\n"
    "s_nop 0\n"
    "v_max_f32_e32 v205, v205, v206\n"
    "v_mov_b32_e32 v206, v205\n"
    "s_nop 1\n"
    "v_permlane32_swap_b32_e32 v205, v206\n"
    "s_nop 0\n"
    "v_max_f32_e32 v205, v205, v206\n"
    "v_add_f32_e32 v205, 0x42980000, v205\n"
    "v_xor_b32_e32 v236, 0x80000000, v205\n"
    "v_xor_b32_e32 v237, 0x80000000, v205\n"
    "v_xor_b32_e32 v238, 0x80000000, v205\n"
    "v_xor_b32_e32 v239, 0x80000000, v205\n"
    "s_nop 1\n"
    "s_barrier\n"
    "s_branch .Lat_maxpass_ret\n"
    :: "s"(__builtin_amdgcn_kernarg_segment_ptr()), "s"(b), "s"(wid)
    : "v0", "v1", "v2", "v3", "v4", "v5", "v6", "v7", "v8", "v9", "v10", "v11", "v12", "v13", "v14", "v15", "v16", "v17", "v18", "v19", "v20", "v21", "v22", "v23", "v24", "v25", "v26", "v27", "v28", "v29", "v30", "v31", "v32", "v33", "v34", "v35", "v36", "v37", "v38", "v39", "v40", "v41", "v42", "v43", "v44", "v45", "v46", "v47", "v48", "v49", "v50", "v51", "v52", "v53", "v54", "v55", "v56", "v57", "v58", "v59", "v60", "v61", "v62", "v63", "v64", "v65", "v66", "v67", "v68", "v69", "v70", "v71", "v72", "v73", "v74", "v75", "v76", "v77", "v78", "v79", "v80", "v81", "v82", "v83", "v84", "v85", "v86", "v87", "v88", "v89", "v90", "v91", "v92", "v93", "v94", "v95", "v96", "v97", "v98", "v99", "v100", "v101", "v102", "v103", "v104", "v105", "v106", "v107", "v108", "v109", "v110", "v111", "v112", "v113", "v114", "v115", "v116", "v117", "v118", "v119", "v120", "v121", "v122", "v123", "v124", "v125", "v126", "v127", "v128", "v129", "v130", "v131", "v132", "v133", "v134", "v135", "v136", "v137", "v138", "v139", "v140", "v141", "v142", "v143", "v144", "v145", "v146", "v147", "v148", "v149", "v150", "v151", "v152", "v153", "v154", "v155", "v156", "v157", "v158", "v159", "v160", "v161", "v162", "v163", "v164", "v165", "v166", "v167", "v168", "v169", "v170", "v171", "v172", "v173", "v174", "v175", "v176", "v177", "v178", "v179", "v180", "v181", "v182", "v183", "v184", "v185", "v186", "v187", "v188", "v189", "v190", "v191", "v192", "v193", "v194", "v195", "v196", "v197", "v198", "v199", "v200", "v201", "v202", "v203", "v204", "v205", "v206", "v207", "v208", "v209", "v210", "v211", "v212", "v213", "v214", "v215", "v216", "v217", "v218", "v219", "v220", "v221", "v222", "v223", "v224", "v225", "v226", "v227", "v228", "v229", "v230", "v231", "v232", "v233", "v234", "v235", "v236", "v237", "v238", "v239", "v240", "v241", "v242", "v243", "v244", "v245", "v246", "v247", "v248", "v249", "v250", "v251", "v252", "v253", "v254", "v255", "a0", "a1", "a2", "a3", "a4", "a5", "a6", "a7", "a8", "a9", "a10", "a11", "a12", "a13", "a14", "a15", "a16", "a17", "a18", "a19", "a20", "a21", "a22", "a23", "a24", "a25", "a26", "a27", "a28", "a29", "a30", "a31", "a32", "a33", "a34", "a35", "a36", "a37", "a38", "a39", "a40", "a41", "a42", "a43", "a44", "a45", "a46", "a47", "a48", "a49", "a50", "a51", "a52", "a53", "a54", "a55", "a56", "a57", "a58", "a59", "a60", "a61", "a62", "a63", "a64", "a65", "a66", "a67", "a68", "a69", "a70", "a71", "a72", "a73", "a74", "a75", "a76", "a77", "a78", "a79", "a80", "a81", "a82", "a83", "a84", "a85", "a86", "a87", "a88", "a89", "a90", "a91", "a92", "a93", "a94", "a95", "a96", "a97", "a98", "a99", "a100", "a101", "a102", "a103", "a104", "a105", "a106", "a107", "a108", "a109", "a110", "a111", "a112", "a113", "a114", "a115", "a116", "a117", "a118", "a119", "a120", "a121", "a122", "a123", "a124", "a125", "a126", "a127", "a128", "a129", "a130", "a131", "a132", "a133", "a134", "a135", "a136", "a137", "a138", "a139", "a140", "a141", "a142", "a143", "a144", "a145", "a146", "a147", "a148", "a149", "a150", "a151", "a152", "a153", "a154", "a155", "a156", "a157", "a158", "a159", "a160", "a161", "a162", "a163", "a164", "a165", "a166", "a167", "a168", "a169", "a170", "a171", "a172", "a173", "a174", "a175", "a176", "a177", "a178", "a179", "a180", "a181", "a182", "a183", "a184", "a185", "a186", "a187", "a188", "a189", "a190", "a191", "a192", "a193", "a194", "a195", "a196", "a197", "a198", "a199", "a200", "a201", "a202", "a203", "a204", "a205", "a206", "a207", "a208", "a209", "a210", "a211", "a212", "a213", "a214", "a215", "a216", "a217", "a218", "a219", "a220", "a221", "a222", "a223", "a224", "a225", "a226", "a227", "a228", "a229", "a230", "a231", "a232", "a233", "a234", "a235", "a236", "a237", "a238", "a239", "a240", "a241", "a242", "a243", "a244", "a245", "a246", "a247", "a248", "a249", "a250", "a251", "a252", "a253", "a254", "a255", "s8", "s9", "s10", "s11", "s12", "s13", "s14", "s15", "s16", "s17", "s18", "s19", "s20", "s21", "s22", "s23", "s24", "s25", "s26", "s27", "s28", "s29", "s30", "s31", "s32", "s33", "s34", "s35", "s36", "s37", "s38", "s39", "s40", "s41", "s42", "s43", "s44", "s45", "s46", "s47", "s48", "s49", "s50", "s51", "s52", "s53", "s54", "s55", "s56", "s57", "s58", "s59", "s60", "s61", "s62", "s63", "s80", "s81", "s82", "s83", "s84", "s85", "s86", "s87", "s88", "s89", "s90", "s91", "vcc", "m0", "scc", "memory");
  __builtin_unreachable();
}

extern "C" void kernel_launch(void* const* d_in, const int* in_sizes, int n_in, void* d_out, int out_size, void* d_ws,
                              size_t ws_size, hipStream_t stream) {
  const u16* x = (const u16*)d_in[0];
  const int* src = (const int*)d_in[1];
  const int* dst = (const int*)d_in[2];
  const u16* deg_inv = (const u16*)d_in[3];
  const u16* wmsg = (const u16*)d_in[4];
  const u16* wq = (const u16*)d_in[5];
  const u16* wk = (const u16*)d_in[6];
  const u16* wv = (const u16*)d_in[7];
  const u16* wout = (const u16*)d_in[8];
  char* ws = (char*)d_ws;
  hipFuncSetAttribute((const void*)attn_kernel, hipFuncAttributeMaxDynamicSharedMemorySize, 163840);
  attn_kernel<<<256, 256, 163840, stream>>>(ws, src, dst, deg_inv, x, (u16*)d_out, wq, wk, wv, wmsg, wout);
}
```

```cpp
#include <hip/hip_runtime.h>
#include <hip/hip_bf16.h>
#include <stdint.h>

typedef unsigned short u16;
typedef __attribute__((ext_vector_type(8))) short bf16x8;
typedef __attribute__((ext_vector_type(4))) short bf16x4;
typedef __attribute__((ext_vector_type(4))) float f32x4;
typedef __attribute__((ext_vector_type(2))) float f32x2;
typedef __attribute__((ext_vector_type(4))) unsigned u32x4;
typedef __attribute__((ext_vector_type(2))) unsigned u32x2;
#define DI __device__ __forceinline__

constexpr int NN = 10000, DD = 256, EE = 160000;
constexpr int NATT = 252, NSP = 4, SPN = 2560;
constexpr int GU = 5, GNB = 5; static_assert(GU * GNB <= 30, "list size");

constexpr unsigned WS_H = 0;
constexpr unsigned WS_AF = WS_H + 10240 * 512;
constexpr unsigned WS_WV = WS_AF + 135168;
constexpr unsigned WS_WM = WS_WV + 135168;
constexpr unsigned WS_XF = WS_WM + 135168;
constexpr unsigned WS_GF = WS_XF + 3328 * 128;
constexpr unsigned WS_HF = WS_GF + 2048;
constexpr unsigned WS_PF = WS_HF + 4096;
constexpr unsigned WS_NF = WS_PF + 4096;
constexpr unsigned WS_WF = WS_NF + 16384;
constexpr unsigned WS_LF = WS_WF + 256;
constexpr unsigned WS_EP = WS_LF + 512;
constexpr unsigned WS_G = WS_EP + 256 + 1024;
constexpr unsigned WS_QP = WS_G + 10240 * 512;
constexpr unsigned WS_XB = WS_QP + 10240 * 512;
constexpr unsigned WS_LS = WS_XB + NATT * 101376;
constexpr unsigned WS_BL = WS_LS + 524288;
constexpr unsigned WS_DBG = WS_BL + 64 * 260 * 64;
constexpr unsigned WS_END = WS_DBG + 1024 * 128 + 8192;

DI float bf2f(unsigned x) { return __uint_as_float(x << 16); }
DI float bfhi(unsigned x) { return __uint_as_float(x & 0xffff0000u); }
DI unsigned pk2(float lo, float hi) {
  typedef __attribute__((ext_vector_type(2))) __bf16 bf2_t;
  const bf2_t r = __builtin_convertvector((f32x2){lo, hi}, bf2_t);
  return __builtin_bit_cast(unsigned, r);
}
typedef __attribute__((address_space(3))) bf16x4* lds_bf16x4_p;
DI bf16x4 tr_read(unsigned p) { return __builtin_amdgcn_ds_read_tr16_b64_v4i16((lds_bf16x4_p)p); }
DI bf16x8 as_bf(u32x4 v) { return __builtin_bit_cast(bf16x8, v); }
#define SB() __builtin_amdgcn_sched_barrier(0)
#define MFMA16(a, b, c) __builtin_amdgcn_mfma_f32_16x16x32_bf16(a, b, c, 0, 0, 0)

typedef __amdgpu_buffer_rsrc_t srd_t;
DI void drain() { asm volatile("s_waitcnt vmcnt(0)" ::: "memory"); }
DI void st_flag(srd_t w, unsigned off, unsigned tag) { __builtin_amdgcn_raw_buffer_store_b32(tag, w, off, 0, 16); }
DI void wait_flag(srd_t w, unsigned off, unsigned tag) {
#pragma unroll 1
  for (int it = 0; it < (1 << 22); ++it) {
    unsigned v;
    asm volatile("buffer_load_dword %0, %1, %2, 0 offen sc1\n s_waitcnt vmcnt(0)" : "=v"(v) : "v"(off), "s"(w) : "memory");
    if (__ballot(v != tag) == 0) break;
    __builtin_amdgcn_s_sleep(2);
    asm volatile("" ::: "memory");
  }
  asm volatile("" ::: "memory");
}

DI void prod_store(const f32x4 (&acc)[2][2], int mat, int ks, int J, int lane, srd_t wsrd) {
#pragma unroll
  for (int s = 0; s < 2; ++s) {
    u32x4 o = {pk2(acc[0][s][0], acc[0][s][1]), pk2(acc[0][s][2], acc[0][s][3]), pk2(acc[1][s][0], acc[1][s][1]), pk2(acc[1][s][2], acc[1][s][3])};
    if (ks == 7 && lane >= 48) o[3] &= 0xffffu;
    const int T = 2 * J + s;
    const int at = mat * 135168 + ks * 4096 + (T & 3) * 1024;
    __builtin_amdgcn_raw_buffer_store_b128(o, wsrd, WS_WV + (T >> 2) * 33792 + at + lane * 16, 0, 16);
  }
}
DI void corr(unsigned* dl, const unsigned (&lr)[16]) {
  float a0 = 0.f, a1 = 0.f;
#pragma unroll
  for (int r = 0; r < 16; ++r) { a0 += bf2f(lr[r] & 0xffffu); a1 += bfhi(lr[r]); }
  dl[0] = pk2(a0 * -0.0625f, a1 * -0.0625f);
  dl[128] = pk2((bf2f(lr[15] & 0xffffu) - 0.25f * a0) * (1.f / 3.f), (bfhi(lr[15]) - 0.25f * a1) * (1.f / 3.f));
}
DI void product_load(char* lds, int wid, int lane, int job, const u16* __restrict__ wv, const u16* __restrict__ wm, const u16* __restrict__ wo) {
  const unsigned ldsb = (unsigned)(size_t)lds;
  const int r = lane >> 3, sw = ((lane & 7) ^ (r & 6)) * 16 + wid * 128;
  const unsigned vp = (8 * (r >> 2) + (r & 3)) * 512 + sw, vn = r * 512 + sw;
  auto image = [&](int n, const u16* w, int row0, bool nat) {
#pragma unroll
    for (int i = 0; i < 8; ++i) {
      const int a = i & 3;
      asm volatile("s_mov_b32 m0, %2\n s_nop 0\n global_load_lds_dwordx4 %0, %1"
                   :: "v"(nat ? vn : vp), "s"(w + (row0 + (i >> 2) * 32 + (nat ? 8 * a : 16 * (a & 1) + 4 * (a >> 1))) * DD),
                      "s"(ldsb + n * 32768 + (i >> 2) * 16384 + wid * 4096 + a * 1024) : "m0", "memory");
    }
  };
  image(2, wv, (job >> 2) * 64, true);
  image(3, wm, (job >> 2) * 64, false);
#pragma unroll
  for (int k = 0; k < 8; ++k)
    asm volatile("s_mov_b32 m0, %2\n s_nop 0\n global_load_lds_dwordx4 %0, %1"
                 :: "v"((lane >> 3) * 512 + (lane & 7) * 16), "s"(wo + (k * 32 + wid * 8) * DD + (job & 3) * 64), "s"(ldsb + k * 4096 + wid * 1024) : "m0", "memory");
}
DI void product_w(char* lds, int wid, int lane, int job, srd_t wsrd) {
  const int m = lane & 15, g = lane >> 4;
  const int ks = 2 * (job >> 2) + (wid >> 1), J = 2 * (job & 3) + (wid & 1);
  const unsigned ldsb = (unsigned)(size_t)lds;
  unsigned* dl = reinterpret_cast<unsigned*>(lds + 0x20000);
  const bool hi = (m >> 3) != 0;
  const unsigned ra = (m >> 3) * 1024 + (m & 7) * 128 + (g ^ (m & 6)) * 16;
  auto frag = [&](int n, int tl, int t, int k) {
    return *reinterpret_cast<const bf16x8*>(lds + n * 32768 + tl * 16384 + (k >> 1) * 4096 + t * 2048 + (ra ^ ((k & 1) * 64)));
  };
  f32x4 av[2][2] = {}, am[2][2] = {}, ad[2] = {};
  const unsigned tb = ldsb + (8 * g + ((lane >> 2) & 3)) * 128 + (wid & 1) * 64 + (lane & 3) * 16;
  bf16x8 d0[8], d1[8], d2[8], rr[8][2], vf[8][2], mf[8][2];
#pragma unroll
  for (int kk = 0; kk < 9; ++kk) {
    if (kk < 8) {
      d0[kk] = *reinterpret_cast<const bf16x8*>(dl + 512 + 16 * kk + 4 * g); d1[kk] = *reinterpret_cast<const bf16x8*>(dl + 512 + (ks == 7 ? 128 : 0) + 16 * kk + 4 * g);
      d2[kk] = *reinterpret_cast<const bf16x8*>(dl + 768 + (ks == 7 && hi ? 128 : 0) + 16 * kk + 4 * g);
#pragma unroll
      for (int s = 0; s < 2; ++s) {
        rr[kk][s] = __builtin_shufflevector(tr_read(tb + kk * 4096 + s * 8), tr_read(tb + kk * 4096 + s * 8 + 512), 0, 1, 2, 3, 4, 5, 6, 7);
        vf[kk][s] = frag(2, wid >> 1, s, kk); mf[kk][s] = frag(3, wid >> 1, s, kk);
      }
    }
    SB();
    if (kk) {
      const int k = kk - 1;
#pragma unroll
      for (int s = 0; s < 2; ++s) {
        av[0][s] = MFMA16(d0[k], rr[k][s], av[0][s]); av[1][s] = MFMA16(d1[k], rr[k][s], av[1][s]); ad[s] = MFMA16(d2[k], rr[k][s], ad[s]);
#pragma unroll
        for (int t = 0; t < 2; ++t) { av[t][s] = MFMA16(vf[k][t], rr[k][s], av[t][s]); am[t][s] = MFMA16(mf[k][t], rr[k][s], am[t][s]); }
      }
    }
    SB();
  }
#pragma unroll
  for (int t = 0; t < 2; ++t)
#pragma unroll
    for (int s = 0; s < 2; ++s) am[t][s] += ad[s];
  prod_store(av, 0, ks, J, lane, wsrd);
  prod_store(am, 1, ks, J, lane, wsrd);
}

DI void helper(char* lds, int j, int wid, const int* __restrict__ src, const int* __restrict__ dst,
               const u16* __restrict__ wv, const u16* __restrict__ wm, const u16* __restrict__ wo, char* ws) {
  unsigned* img = reinterpret_cast<unsigned*>(lds);
  char* cntb = lds + 0x20000;
  int* cnt = reinterpret_cast<int*>(cntb);
  unsigned* ub = reinterpret_cast<unsigned*>(lds + 0x22c00);
  int* red = reinterpret_cast<int*>(ub + 256);
  u16* dg = reinterpret_cast<u16*>(lds + 0x23400);
  u16* tmp = reinterpret_cast<u16*>(lds + 0x25000) + wid * 512;
  int lane; asm volatile("v_mbcnt_lo_u32_b32 %0, -1, 0\n\tv_mbcnt_hi_u32_b32 %0, -1, %0" : "=v"(lane));
  const int tid = wid * 64 + lane, lo = j * SPN, nn_ = min(SPN, NN - lo);
  const srd_t wsrd = __builtin_amdgcn_make_buffer_rsrc(ws, 0, WS_END, 0x00020000);
  const unsigned tag = __builtin_amdgcn_raw_buffer_load_b32(wsrd, WS_EP, 0, 0) + 4u;
  auto touch = [&](unsigned at) { asm volatile("s_mov_b32 m0, 0x27c00\n s_nop 0\n buffer_load_dword %0, %1, 0 offen sc1 lds" :: "v"(at), "s"(wsrd) : "m0", "memory"); };
  touch(lane < 16 ? min(WS_GF + (lane * 16 + wid * 4 + j) * 128, WS_EP) : 0x40000000u);
  for (int i = tid; i < 8192; i += 256) { const unsigned f = (i & 63) >= 60 ? 0u : ~0u; reinterpret_cast<u32x4*>(img)[i] = (u32x4){f, f, f, f}; }
  for (int i = tid; i < SPN + 64; i += 256) cnt[i] = 0;
  ub[tid] = 0u;
  if (tid < 8) red[tid] = 0;
  touch(WS_XF + 262144 + (j * 256 + tid) * 128);
  wait_flag(wsrd, WS_NF + j * 4096 + min(tid, 249) * 16, tag);
  __syncthreads();
  const unsigned dum = (SPN + lane) << 18;
  auto fetch = [&](int k) {
    return __builtin_amdgcn_raw_buffer_load_b128(wsrd, WS_BL + (k < 48 ? tid * 16 + (k & 7) * 4096 : (tid >> 2) * 128 + (tid & 3) * 16 + (k & 3) * 8192 + (k & 4) * 16), (j * 8 + (k >> 3)) * 33280, 16);
  };
  auto clean = [&](u32x4& e, int k) {
#pragma unroll
    for (int r = 0; r < 4; ++r) e[r] = (k < 48 ? (k & 7) == 7 && tid >= 208 : (k & 3) == 3 && tid >= 232) ? dum : min(e[r], dum);
  };
  auto slot = [&](unsigned e) { return reinterpret_cast<int*>(cntb + (e >> 16)); };
  constexpr int NL = 52;
  u32x4 ent[NL];
#pragma unroll
  for (int k = 0; k < NL; ++k) ent[k] = fetch(k);
  SB();
#pragma unroll
  for (int k = 0; k < NL; ++k) {
    clean(ent[k], k);
    int* p[4];
#pragma unroll
    for (int r = 0; r < 4; ++r) p[r] = slot(ent[k][r]);
    SB();
#pragma unroll
    for (int r = 0; r < 4; ++r) atomicAdd(p[r], 2);
    SB();
  }
  unsigned use = 0;
  const unsigned dum3 = (tid & 3) == 3 ? dum : 0;
#pragma unroll
  for (int k = 0; k < 4; ++k) use |= __ballot(ent[NL - 4 + k][3] < dum3) ? 1u << k : 0u;
  if (use)
#pragma unroll 1
    for (int k = NL; k < 64; ++k)
      if (use >> (k - NL) & 1) {
        u32x4 e = fetch(k); clean(e, k);
#pragma unroll
        for (int r = 0; r < 4; ++r) atomicAdd(slot(e[r]), 2);
        if (__ballot(e[3] < dum3)) { use |= 16u << (k - NL); if (k >= 60) red[7] = 1; }
      }
  __syncthreads();
  int inc = 0, nu[10], cn[10], cm = 0;
  auto units = [&](int c) { return max(1, (c + 6) >> 3); };
#pragma unroll
  for (int i = 0; i < 10; i += 2) { const u32x2 v = *reinterpret_cast<u32x2*>(cnt + 10 * tid + i); cn[i] = v[0]; cn[i + 1] = v[1]; }
  const int nup = units(cnt[max(10 * tid - 1, 0)]);
  const bool act = 10 * tid < nn_;
#pragma unroll
  for (int i = 0; i < 10; ++i) { cm = max(cm, cn[i]); nu[i] = act ? units(cn[i]) : 0; inc += nu[i]; }
  const int own = inc;
  inc += __builtin_amdgcn_update_dpp(0, inc, 0x111, 15, 15, true); inc += __builtin_amdgcn_update_dpp(0, inc, 0x112, 15, 15, true);
  inc += __builtin_amdgcn_update_dpp(0, inc, 0x114, 15, 15, true); inc += __builtin_amdgcn_update_dpp(0, inc, 0x118, 15, 15, true);
  inc += __builtin_amdgcn_update_dpp(0, inc, 0x142, 10, 15, false); inc += __builtin_amdgcn_update_dpp(0, inc, 0x143, 12, 15, false);
  if (lane == 63) red[wid] = inc;
  const bool srt = __ballot(cm > 64);
  __syncthreads();
  const u32x4 rd = *reinterpret_cast<u32x4*>(red);
  const float rs_ = 64.f / (float)(rd[0] + rd[1] + rd[2] + rd[3]);
  inc += (wid > 0 ? rd[0] : 0) + (wid > 1 ? rd[1] : 0) + (wid > 2 ? rd[2] : 0) - own;
  int wv_[10];
  {
    int o = inc, wp = tid ? min(127, (int)((float)(2 * o - nup) * rs_)) : 128;
#pragma unroll
    for (int i = 0; i < 10; ++i) {
      wv_[i] = min(127, (int)((float)(2 * o + nu[i]) * rs_));
      ub[act && wv_[i] != wp ? wv_[i] : 128 + lane] = (unsigned)o << 12 | (unsigned)(10 * tid + i);
      wp = wv_[i]; o += nu[i];
    }
  }
  __syncthreads();
  {
    int u0[10], top = 0;
#pragma unroll
    for (int i = 0; i < 10; ++i) u0[i] = ub[wv_[i]] >> 12;
#pragma unroll
    for (int i = 0; i < 10; ++i) {
      const int lu = inc - u0[i], e = lu + nu[i] - 1 + GU, q = min(e / (4 * GU), GNB);
      u0[i] = wv_[i] * 1024 + lu * 8; top = max(top, q);
      atomicOr(img + wv_[i] * 256 + 244 + q, act ? 1u << (e - q * 4 * GU) : 0u);
      inc += nu[i];
    }
#pragma unroll
    for (int i = 0; i < 10; i += 2) *reinterpret_cast<u32x2*>(cnt + 10 * tid + i) = (u32x2){(unsigned)u0[i], (unsigned)u0[i + 1]};
    if (tid < 64) cnt[SPN + tid] = 0x30000;
    if (act && top == GNB) red[7] = 1;
  }
  __syncthreads();
  const bool big_ = red[7];
  if (tid < 128) {
    unsigned nb = 0;
#pragma unroll
    for (int q = 0; q < GNB; ++q) nb = img[tid * 256 + 244 + q] ? q + 1 : nb;
    *reinterpret_cast<u32x4*>(img + tid * 256 + 240) = (u32x4){big_ ? 0u : nb, WS_G + (lo + (ub[tid] & 4095u)) * 512, big_ || (tid & 3) ? 0x40000000u : WS_GF + j * 512 + tid * 4, 0u};
  }
  if (!big_) {
    const unsigned dmp = 0x27000 + lane * 4;
    typedef __attribute__((address_space(3))) u16* lds_u16_p;
    unsigned p[2][8];
#pragma unroll
    for (int k = 0; k < NL + 2; k += 2) {
      if (k < NL) {
        int* q[8];
#pragma unroll
        for (int r = 0; r < 8; ++r) q[r] = slot(ent[k + (r >> 2)][r & 3]);
        SB();
#pragma unroll
        for (int r = 0; r < 8; ++r) p[k >> 1 & 1][r] = atomicAdd(q[r], 2);
        SB();
      }
      if (k) {
#pragma unroll
        for (int r = 0; r < 8; ++r) p[~k >> 1 & 1][r] = min(p[~k >> 1 & 1][r], dmp);
        SB();
#pragma unroll
        for (int r = 0; r < 8; ++r) *(lds_u16_p)p[~k >> 1 & 1][r] = ent[k - 2 + (r >> 2)][r & 3];
        SB();
      }
    }
    if (use)
#pragma unroll 1
      for (int k = NL; k < 64; ++k)
        if (use >> (k - NL) & 1) {
          u32x4 e = fetch(k); clean(e, k);
#pragma unroll
          for (int r = 0; r < 4; ++r) *(lds_u16_p)min((unsigned)atomicAdd(slot(e[r]), 2), dmp) = e[r];
        }
    __syncthreads();
    if (srt) {
#pragma unroll
      for (int i = 0; i < 10; ++i) dg[10 * tid + i] = cn[i] >> 1;
      for (unsigned long long big = __ballot(cm > 64); big; big &= big - 1)
      for (int n = 10 * (wid * 64 + __builtin_ctzll(big)), n1 = n + 10; n < n1; ++n) {
        const int d = __builtin_amdgcn_readfirstlane(dg[n]);
        if (d <= 32) continue;
        u16* f = reinterpret_cast<u16*>(lds + cnt[n]) - d;
        for (int i = lane; i < d + 8; i += 64) tmp[i] = i < d ? f[i] : 0xffff;
        __builtin_amdgcn_wave_barrier();
        for (int i = lane; i < d; i += 64) {
          const unsigned key = tmp[i] << 9 | i; int p_ = 0;
          for (int q = 0; q < d; q += 8) {
            const u32x4 v = *reinterpret_cast<u32x4*>(tmp + q);
#pragma unroll
            for (int e = 0; e < 4; ++e) p_ += (((v[e] & 0xffffu) << 9 | (q + 2 * e)) < key) + (((v[e] >> 16) << 9 | (q + 2 * e + 1)) < key);
          }
          f[p_] = key >> 9;
        }
        __builtin_amdgcn_wave_barrier();
      }
    }
  }
  __syncthreads();
#pragma unroll 8
  for (int k = 0; k < 32; ++k)
    __builtin_amdgcn_raw_buffer_store_b128(reinterpret_cast<u32x4*>(img)[k * 256 + tid], wsrd, WS_LS + j * 131072 + (k * 256 + tid) * 16, 0, 16);
  unsigned lr[16];
#pragma unroll
  for (int r = 0; r < 16; ++r) lr[r] = reinterpret_cast<const unsigned*>((wid >= 2 ? wm : wv) + (240 + r) * DD)[tid & 127];
  wait_flag(wsrd, WS_HF + min(tid, NATT - 1) * 16, tag);
  drain();
  __syncthreads();
  if (tid == 0) st_flag(wsrd, WS_LF + j * 128, tag);
  touch(WS_XF + (j * 256 + tid) * 256);
  touch(wid ? 0x40000000u : WS_GF - 32768 + (j * 64 + lane) * 128);
  corr(reinterpret_cast<unsigned*>(lds + 0x20000) + 512 + (wid >> 1) * 256 + (tid & 127), lr);
#pragma unroll 1
  for (int job = 4 * j; job < 4 * j + 4; ++job) {
    product_load(lds, wid, lane, job, wv, wm, wo);
    drain();
    __syncthreads();
    product_w(lds, wid, lane, job, wsrd);
    __syncthreads();
  }
  drain();
  __syncthreads();
  if (tid == 0) st_flag(wsrd, WS_WF + j * 4, tag);
  if (big_) {
    const srd_t msrd = __builtin_amdgcn_make_buffer_rsrc(ws + WS_H, 0, 10240 * 512, 0x00020000);
    bf16x8 b1;
#pragma unroll
    for (int e = 0; e < 8; ++e) b1[e] = (lane & 15) == e + ((lane >> 1) & 8) ? 0x3f80 : 0;
    for (int n = wid; n < nn_; n += 4) {
      f32x4 acc = {};
      for (int e0 = 0; e0 < EE; e0 += 64) {
        unsigned long long m = __ballot(dst[e0 + lane] == lo + n);
        while (m) {
          const int e = e0 + __builtin_ctzll(m); m &= m - 1;
          const unsigned vo = lane < 32 ? (unsigned)src[e] * 512 + lane * 16 : 0x7fffff00u;
          acc = MFMA16(b1, as_bf(__builtin_amdgcn_raw_buffer_load_b128(msrd, vo, 0, 16)), acc);
        }
      }
      __builtin_amdgcn_raw_buffer_store_b64((u32x2){pk2(acc[0], acc[1]), pk2(acc[2], acc[3])}, wsrd,
                                            WS_G + (lo + n) * 512 + (lane >> 5) * 256 + (lane & 15) * 16 + (lane & 16) / 2, 0, 16);
    }
    drain();
    __syncthreads();
    if (tid < 32) st_flag(wsrd, WS_GF + j * 512 + tid * 16, tag);
  }
}

constexpr unsigned CODE_BYTES = 76000;
extern "C" __global__ __launch_bounds__(256, 1) void gps_kernel(const u16* wq, const u16* wk, const int* src, char* ws, const int* dst, const u16* x, u16* out,
                                                                const u16* deg_inv, const u16* wv, const u16* wm, const u16* wo) {
  extern __shared__ __attribute__((aligned(16))) char lds[];
  static_assert(WS_END == 43864064 && WS_EP == 6101760, "workspace layout of the generated code");
  typedef void* const __attribute__((address_space(4)))* arg_p;
  arg_p ka; int b, wid;
  asm volatile(
    ".Lat_0:\n"
    "s_mov_b32 s92, s2\n"
    "s_load_dwordx2 s[2:3], s[0:1], 0x0\n"
    "s_load_dwordx4 s[4:7], s[0:1], 0x8\n"
    "s_load_dwordx8 s[8:15], s[0:1], 0x18\n"
    "s_load_dwordx2 s[28:29], s[0:1], 0x38\n"
    "s_getpc_b64 s[30:31]\n"
    ".Lat_pc:\n"
    "s_bfe_u32 s45, s92, 0x20003\n"
    "v_lshlrev_b32_e32 v1, 7, v0\n"
    "s_lshl_b32 s45, s45, 15\n"
    "s_mov_b32 m0, 0x27f00\n"
    "v_add_u32_e32 v1, s45, v1\n"
    "v_min_u32_e32 v1, 75488, v1\n"
    "v_readfirstlane_b32 s93, v0\n"
    "global_load_lds_dword v1, s[30:31]\n"
    "s_and_b32 s45, s92, 231\n"
    "s_lshr_b32 s93, s93, 6\n"
    "s_cmp_eq_u32 s45, 231\n"
    "s_cbranch_scc1 .Lat_out\n"
    "v_mbcnt_lo_u32_b32 v24, -1, 0\n"
    "v_mbcnt_hi_u32_b32 v24, -1, v24\n"
    "s_and_b32 s50, s93, 1\n"
    "s_lshr_b32 s51, s93, 1\n"
    "v_lshrrev_b32_e32 v25, 3, v24\n"
    "v_and_b32_e32 v26, 6, v25\n"
    "v_and_b32_e32 v27, 7, v24\n"
    "v_xor_b32_e32 v26, v26, v27\n"
    "v_lshlrev_b32_e32 v26, 4, v26\n"
    "v_and_b32_e32 v27, 4, v25\n"
    "v_add_u32_e32 v27, v27, v25\n"
    "v_lshl_add_u32 v251, v27, 9, v26\n"
    "v_add_u32_e32 v252, 0x1c00, v251\n"
    "s_mov_b32 s22, 0x20000\n"
    "s_mov_b32 s23, 0x20000\n"
    "s_mov_b32 s26, 0x20000\n"
    "s_mov_b32 s27, 0x20000\n"
    "s_lshr_b32 s36, s92, 7\n"
    "s_mul_i32 s36, s36, 3\n"
    "s_add_u32 s36, s36, s92\n"
    "s_and_b32 s36, s36, 127\n"
    "s_lshl_b32 s45, s93, 7\n"
    "s_lshl_b32 s46, s93, 12\n"
    "s_and_b32 s47, s36, 7\n"
    "s_lshl_b32 s47, s47, 14\n"
    "s_mov_b32 m0, s46\n"
    "s_add_u32 s47, s47, s45\n"
    "s_and_b32 s34, s92, 7\n"
    "s_lshl_b32 s34, s34, 5\n"
    "s_lshr_b32 s32, s92, 3\n"
    "s_add_u32 s34, s34, s32\n"
    "s_and_b32 s32, s32, 3\n"
    "s_lshr_b32 s33, s34, 2\n"
    "s_or_b32 s38, s33, 16\n"
    "s_cmp_eq_u32 s38, 62\n"
    "s_cselect_b32 s38, 16, 0\n"
    "s_xor_b32 s33, s33, s38\n"
    "s_lshl2_add_u32 s34, s33, s32\n"
    "s_mul_i32 s38, s33, 81920\n"
    "s_lshl_b32 s35, s32, 13\n"
    "s_and_b32 s55, s35, 0x2000\n"
    "s_add_u32 s35, s35, s38\n"
    "s_add_u32 s55, s55, s38\n"
    "s_waitcnt lgkmcnt(0)\n"
    "s_mov_b64 s[20:21], s[2:3]\n"
    "s_mov_b64 s[24:25], s[4:5]\n"
    "buffer_load_dwordx4 v251, s[20:23], s47 offen offset:0 lds\n"
    "s_add_u32 s48, s47, 1024\n"
    "buffer_load_dwordx4 v252, s[20:23], s47 offen offset:3072 lds\n"
    "s_add_u32 s47, s47, 0x1800\n"
    "buffer_load_dwordx4 v251, s[20:23], s48 offen offset:1024 lds\n"
    "buffer_load_dwordx4 v251, s[20:23], s47 offen offset:2048 lds\n"
    "s_lshr_b32 s47, s36, 4\n"
    "s_lshl_b32 s47, s47, 14\n"
    "s_and_b32 s48, s36, 8\n"
    "s_lshl_b32 s48, s48, 8\n"
    "s_add_u32 s47, s47, s48\n"
    "s_add_u32 m0, s46, 0x4000\n"
    "s_add_u32 s47, s47, s45\n"
    "buffer_load_dwordx4 v251, s[24:27], s47 offen offset:0 lds\n"
    "buffer_load_dwordx4 v252, s[24:27], s47 offen offset:1024 lds\n"
    "s_add_u32 s47, s45, 120832\n"
    "buffer_load_dwordx4 v251, s[24:27], s47 offen offset:2048 lds\n"
    "s_add_u32 s47, s47, 1024\n"
    "buffer_load_dwordx4 v251, s[24:27], s47 offen offset:3072 lds\n"
    "s_mov_b64 s[16:17], s[14:15]\n"
    "s_mov_b64 s[86:87], s[10:11]\n"
    "s_mov_b32 s14, 5120000\n"
    "s_mov_b32 s15, 0x20000\n"
    "s_lshl_b32 s45, s93, 2\n"
    "s_add_u32 s54, s35, 0x8000\n"
    "s_add_u32 s55, s55, 0x10000\n"
    "v_lshlrev_b32_e32 v250, 4, v24\n"
    "v_lshrrev_b32_e32 v28, 4, v24\n"
    "v_add_u32_e32 v28, s45, v28\n"
    "v_and_b32_e32 v104, 0xf0, v250\n"
    "v_lshl_add_u32 v104, v28, 9, v104\n"
    "buffer_load_dwordx4 v[0:3], v104, s[12:15], s35 offen offset:0\n"
    "buffer_load_dwordx4 v[4:7], v104, s[12:15], s35 offen offset:256\n"
    "s_cmp_lt_u32 s32, 2\n"
    "s_cselect_b32 s60, s55, 2.0\n"
    "buffer_load_dwordx4 v[8:11], v104, s[12:15], s54 offen offset:0\n"
    "buffer_load_dwordx4 v[12:15], v104, s[12:15], s54 offen offset:256\n"
    "buffer_load_dwordx4 v[16:19], v104, s[12:15], s55 offen offset:0\n"
    "buffer_load_dwordx4 v[20:23], v104, s[12:15], s55 offen offset:256\n"
    "s_min_u32 s46, s34, 249\n"
    "s_mul_i32 s46, s46, 2560\n"
    "s_lshl_b32 s47, s50, 10\n"
    "s_add_u32 s47, s47, s46\n"
    "v_add_u32_e32 v108, s47, v250\n"
    "s_lshl_b32 s47, s50, 8\n"
    "s_add_u32 s47, s47, s46\n"
    "s_add_u32 s47, s47, 2048\n"
    "v_lshl_add_u32 v109, v24, 2, s47\n"
    "s_cmp_lg_u32 s51, 0\n"
    "s_cselect_b64 exec, -1, 1\n"
    "global_load_dwordx4 v[144:147], v108, s[86:87]\n"
    "global_load_dword v148, v109, s[86:87]\n"
    "global_load_dwordx4 v[152:155], v108, s[6:7]\n"
    "global_load_dword v156, v109, s[6:7]\n"
    "s_mov_b64 exec, -1\n"
    "s_mov_b32 s10, 43864064\n"
    "s_mov_b32 s11, 0x20000\n"
    "s_mov_b32 s38, 6101760\n"
    "s_buffer_load_dword s39, s[8:11], s38\n"
    "s_mov_b32 s49, 0\n"
    "v_and_b32_e32 v27, 1, v25\n"
    "v_lshl_add_u32 v248, v27, 9, v26\n"
    "v_lshrrev_b32_e32 v27, 1, v25\n"
    "v_lshl_add_u32 v248, v27, 12, v248\n"
    "s_mul_i32 s43, s50, 3\n"
    "s_xor_b32 s43, s43, s51\n"
    "s_lshl_b32 s43, s43, 15\n"
    "s_add_u32 s43, s43, 32768\n"
    "s_mul_i32 s42, s93, 5120\n"
    "s_lshl_b32 s47, s50, 8\n"
    "s_mul_i32 s40, s32, 1277952\n"
    "s_lshl_b32 s41, s51, 14\n"
    "s_add_u32 s40, s40, s41\n"
    "s_add_u32 s40, s40, s47\n"
    "s_add_u32 s41, s40, 128\n"
    "v_bfe_u32 v25, v24, 2, 3\n"
    "v_lshlrev_b32_e32 v244, 7, v25\n"
    "v_lshrrev_b32_e32 v26, 5, v24\n"
    "v_lshl_add_u32 v244, v26, 10, v244\n"
    "v_and_b32_e32 v25, 6, v25\n"
    "v_bfe_u32 v26, v24, 1, 1\n"
    "v_xor_b32_e32 v25, v25, v26\n"
    "v_lshl_add_u32 v244, v25, 4, v244\n"
    "v_and_b32_e32 v26, 1, v24\n"
    "v_lshl_add_u32 v244, v26, 3, v244\n"
    "s_lshl_b32 s47, s51, 16\n"
    "s_add_u32 s47, s47, 32768\n"
    "v_add_u32_e32 v244, s47, v244\n"
    "v_xor_b32_e32 v245, 32, v244\n"
    "v_xor_b32_e32 v246, 64, v244\n"
    "v_xor_b32_e32 v247, 0x60, v244\n"
    "v_and_b32_e32 v25, 7, v24\n"
    "v_lshlrev_b32_e32 v27, 7, v25\n"
    "v_bfe_u32 v26, v24, 3, 1\n"
    "v_lshl_add_u32 v27, v26, 10, v27\n"
    "v_and_b32_e32 v25, 6, v24\n"
    "v_lshrrev_b32_e32 v26, 4, v24\n"
    "v_xor_b32_e32 v25, v25, v26\n"
    "v_lshl_add_u32 v27, v25, 4, v27\n"
    "s_lshl_b32 s47, s51, 15\n"
    "s_add_u32 s48, s47, 32768\n"
    "s_sub_u32 s47, 131072, s47\n"
    "v_add_u32_e32 v240, s48, v27\n"
    "v_xor_b32_e32 v241, 64, v240\n"
    "v_add_u32_e32 v242, s47, v27\n"
    "v_xor_b32_e32 v243, 64, v242\n"
    "s_xor_b32 s47, s93, 2\n"
    "s_mul_i32 s47, s47, 5120\n"
    "v_lshl_add_u32 v249, v24, 2, s47\n"
    "v_and_b32_e32 v29, 15, v24\n"
    "s_cmp_ge_u32 s36, 112\n"
    "s_cselect_b32 s47, 8, 16\n"
    "v_cmp_le_u32_e32 vcc, s47, v29\n"
    "v_mov_b32_e32 v64, 0xbd80bd80\n"
    "v_mov_b32_e32 v65, 0xbdabbdab\n"
    "v_mov_b32_e32 v66, 0x392b392b\n"
    "v_cndmask_b32_e32 v64, v64, v65, vcc\n"
    "v_cndmask_b32_e32 v66, 0, v66, vcc\n"
    "s_cselect_b32 s47, 56, 64\n"
    "v_cmp_le_u32_e32 vcc, s47, v24\n"
    "v_mov_b32_e32 v65, 0x3e80bdab\n"
    "v_mov_b32_e32 v67, 0x392b\n"
    "v_cndmask_b32_e32 v65, v64, v65, vcc\n"
    "v_cndmask_b32_e32 v67, v66, v67, vcc\n"
    "s_cmp_ge_u32 s36, 120\n"
    "s_cselect_b32 s47, 15, 16\n"
    "v_cmp_ne_u32_e32 vcc, s47, v29\n"
    "v_mov_b32_e32 v68, 0x3db8aa3b\n"
    "s_lshr_b32 s47, s36, 5\n"
    "v_cndmask_b32_e32 v68, 0, v68, vcc\n"
    "s_mul_i32 s47, s47, 33792\n"
    "s_and_b32 s48, s36, 7\n"
    "s_lshl_b32 s48, s48, 11\n"
    "s_add_u32 s47, s47, s48\n"
    "s_and_b32 s48, s36, 8\n"
    "s_lshl_b32 s48, s48, 7\n"
    "s_add_u32 s47, s47, s48\n"
    "s_and_b32 s48, s36, 16\n"
    "s_lshl_b32 s48, s48, 10\n"
    "s_add_u32 s47, s47, s48\n"
    "s_add_u32 s52, s47, 5242880\n"
    "s_lshl_b32 s48, s50, 9\n"
    "s_add_u32 s52, s52, s48\n"
    "s_lshl_b32 s48, s50, 11\n"
    "v_xor_b32_e32 v75, 64, v27\n"
    "v_add_u32_e32 v72, s48, v27\n"
    "v_add_u32_e32 v73, s48, v75\n"
    "v_and_b32_e32 v74, 0x1f0, v250\n"
    "v_lshrrev_b32_e32 v29, 5, v24\n"
    "v_lshl_add_u32 v74, v29, 3, v74\n"
    "s_lshl1_add_u32 s53, s36, s50\n"
    "s_lshl4_add_u32 s53, s53, 6080512\n"
    "s_cmp_eq_u32 s51, 0\n"
    "s_cselect_b32 s52, s52, 2.0\n"
    "s_cselect_b32 s53, s53, 2.0\n"
    "s_lshl_b32 s47, s93, 10\n"
    "v_add_u32_e32 v108, s47, v250\n"
    "v_lshrrev_b32_e32 v109, 7, v108\n"
    "v_mul_u32_u24_e32 v109, 0x8200, v109\n"
    "v_and_b32_e32 v32, 0x70, v250\n"
    "v_or_b32_e32 v109, v109, v32\n"
    "v_add_u32_e32 v108, 81920, v108\n"
    "v_mov_b64_e32 v[32:33], -1\n"
    "v_mov_b64_e32 v[34:35], -1\n"
    "ds_write_b128 v108, v[32:35] offset:0\n"
    "ds_write_b128 v108, v[32:35] offset:16\n"
    "v_and_b32_e32 v29, 6, v28\n"
    "v_xor_b32_e32 v29, v29, v24\n"
    "v_and_b32_e32 v29, 7, v29\n"
    "v_lshlrev_b32_e32 v105, 7, v28\n"
    "v_lshl_add_u32 v105, v29, 4, v105\n"
    "v_bfe_u32 v29, v24, 3, 1\n"
    "v_subrev_u32_e32 v29, s32, v29\n"
    "v_and_b32_e32 v29, 3, v29\n"
    "v_lshl_add_u32 v105, v29, 11, v105\n"
    "v_add_u32_e32 v105, 32768, v105\n"
    "v_xor_b32_e32 v106, 0x1000, v105\n"
    "v_and_b32_e32 v29, 15, v24\n"
    "v_cmp_eq_u32_e32 vcc, 15, v29\n"
    "v_mov_b32_e32 v107, 0xffff\n"
    "s_mov_b32 s58, 0x3f803f80\n"
    "s_mov_b32 s57, 0x43c00000\n"
    "v_cndmask_b32_e32 v107, -1, v107, vcc\n"
    "s_mov_b32 s56, 0xc3c00000\n"
    "s_mov_b32 s62, 0xc000c000\n"
    "s_mov_b32 s63, 0xc000c000\n"
    "v_add_u32_e32 v96, 32768, v27\n"
    "v_xor_b32_e32 v97, 64, v96\n"
    "s_lshl_b32 s47, s93, 11\n"
    "v_add_u32_e32 v99, s47, v250\n"
    "v_add_u32_e32 v98, 11345920, v99\n"
    "s_lshl_b32 s47, s93, 10\n"
    "s_add_u32 s47, s47, 6080512\n"
    "v_add_u32_e32 v100, s47, v250\n"
    "s_waitcnt vmcnt(10)\n"
    "s_waitcnt lgkmcnt(0)\n"
    "s_barrier\n"
    "s_add_u32 s39, s39, 4\n"
    "s_cmp_lg_u32 s51, 0\n"
    "s_cbranch_scc1 .Lat_noj2\n"
    "ds_read_b128 v[112:115], v72 offset:0\n"
    "ds_read_b128 v[208:211], v27 offset:18432\n"
    "ds_read_b128 v[176:179], v27 offset:16384\n"
    "ds_read_b128 v[116:119], v73 offset:0\n"
    "ds_read_b128 v[212:215], v75 offset:18432\n"
    "ds_read_b128 v[180:183], v75 offset:16384\n"
    "ds_read_b128 v[120:123], v72 offset:4096\n"
    "ds_read_b128 v[216:219], v27 offset:22528\n"
    "ds_read_b128 v[184:187], v27 offset:20480\n"
    "ds_read_b128 v[124:127], v73 offset:4096\n"
    "ds_read_b128 v[220:223], v75 offset:22528\n"
    "ds_read_b128 v[188:191], v75 offset:20480\n"
    "ds_read_b128 v[128:131], v72 offset:8192\n"
    "ds_read_b128 v[224:227], v27 offset:26624\n"
    "ds_read_b128 v[192:195], v27 offset:24576\n"
    "s_waitcnt lgkmcnt(12)\n"
    "v_mfma_f32_16x16x32_bf16 v[56:59], v[208:211], v[112:115], 0\n"
    "ds_read_b128 v[132:135], v73 offset:8192\n"
    "ds_read_b128 v[228:231], v75 offset:26624\n"
    "v_mfma_f32_16x16x32_bf16 v[48:51], v[112:115], v[176:179], 0\n"
    "ds_read_b128 v[196:199], v75 offset:24576\n"
    "s_waitcnt lgkmcnt(12)\n"
    "v_mfma_f32_16x16x32_bf16 v[56:59], v[212:215], v[116:119], v[56:59]\n"
    "ds_read_b128 v[136:139], v72 offset:12288\n"
    "ds_read_b128 v[232:235], v27 offset:30720\n"
    "v_mfma_f32_16x16x32_bf16 v[48:51], v[116:119], v[180:183], v[48:51]\n"
    "ds_read_b128 v[200:203], v27 offset:28672\n"
    "s_waitcnt lgkmcnt(12)\n"
    "v_mfma_f32_16x16x32_bf16 v[56:59], v[216:219], v[120:123], v[56:59]\n"
    "ds_read_b128 v[140:143], v73 offset:12288\n"
    "ds_read_b128 v[236:239], v75 offset:30720\n"
    "v_mfma_f32_16x16x32_bf16 v[48:51], v[120:123], v[184:187], v[48:51]\n"
    "ds_read_b128 v[204:207], v75 offset:28672\n"
    "s_waitcnt lgkmcnt(12)\n"
    "v_mfma_f32_16x16x32_bf16 v[56:59], v[220:223], v[124:127], v[56:59]\n"
    "v_mfma_f32_16x16x32_bf16 v[48:51], v[124:127], v[188:191], v[48:51]\n"
    "s_waitcnt lgkmcnt(9)\n"
    "v_mfma_f32_16x16x32_bf16 v[56:59], v[224:227], v[128:131], v[56:59]\n"
    "v_mfma_f32_16x16x32_bf16 v[48:51], v[128:131], v[192:195], v[48:51]\n"
    "s_waitcnt lgkmcnt(6)\n"
    "v_mfma_f32_16x16x32_bf16 v[56:59], v[228:231], v[132:135], v[56:59]\n"
    "v_mfma_f32_16x16x32_bf16 v[48:51], v[132:135], v[196:199], v[48:51]\n"
    "s_waitcnt lgkmcnt(3)\n"
    "v_mfma_f32_16x16x32_bf16 v[56:59], v[232:235], v[136:139], v[56:59]\n"
    "v_mfma_f32_16x16x32_bf16 v[48:51], v[136:139], v[200:203], v[48:51]\n"
    "s_waitcnt lgkmcnt(0)\n"
    "v_mfma_f32_16x16x32_bf16 v[56:59], v[236:239], v[140:143], v[56:59]\n"
    "v_mfma_f32_16x16x32_bf16 v[48:51], v[140:143], v[204:207], v[48:51]\n"
    "s_nop 3\n"
    "v_cvt_pk_bf16_f32 v56, v56, v57\n"
    "v_cvt_pk_bf16_f32 v57, v58, v59\n"
    "s_nop 1\n"
    "v_mfma_f32_16x16x16_bf16 v[48:51], v[56:57], v[64:65], v[48:51]\n"
    "v_mfma_f32_16x16x16_bf16 v[48:51], v[56:57], v[66:67], v[48:51]\n"
    "s_nop 7\n"
    "v_mul_f32_e32 v48, v48, v68\n"
    "v_mul_f32_e32 v49, v49, v68\n"
    "v_mul_f32_e32 v50, v50, v68\n"
    "v_mul_f32_e32 v51, v51, v68\n"
    "v_cvt_pk_bf16_f32 v48, v48, v49\n"
    "v_cvt_pk_bf16_f32 v49, v50, v51\n"
    ".Lat_noj2:\n"
    "buffer_store_dwordx2 v[48:49], v74, s[8:11], s52 offen offset:0 sc1\n"
    "v_mov_b32_e32 v102, s39\n"
    "s_waitcnt vmcnt(5)\n"
    "v_dot2_f32_bf16 v24, v0, s58, 0\n"
    "v_dot2_f32_bf16 v32, v8, s58, 0\n"
    "v_dot2_f32_bf16 v40, v16, s58, 0\n"
    "v_dot2_f32_bf16 v25, v4, s58, 0\n"
    "v_dot2_f32_bf16 v33, v12, s58, 0\n"
    "v_dot2_f32_bf16 v41, v20, s58, 0\n"
    "v_dot2_f32_bf16 v26, v0, v0, 0\n"
    "v_dot2_f32_bf16 v34, v8, v8, 0\n"
    "v_dot2_f32_bf16 v42, v16, v16, 0\n"
    "v_dot2_f32_bf16 v27, v4, v4, 0\n"
    "v_dot2_f32_bf16 v35, v12, v12, 0\n"
    "v_dot2_f32_bf16 v43, v20, v20, 0\n"
    "v_dot2_f32_bf16 v24, v1, s58, v24\n"
    "v_dot2_f32_bf16 v32, v9, s58, v32\n"
    "v_dot2_f32_bf16 v40, v17, s58, v40\n"
    "v_dot2_f32_bf16 v25, v5, s58, v25\n"
    "v_dot2_f32_bf16 v33, v13, s58, v33\n"
    "v_dot2_f32_bf16 v41, v21, s58, v41\n"
    "v_dot2_f32_bf16 v26, v1, v1, v26\n"
    "v_dot2_f32_bf16 v34, v9, v9, v34\n"
    "v_dot2_f32_bf16 v42, v17, v17, v42\n"
    "v_dot2_f32_bf16 v27, v5, v5, v27\n"
    "v_dot2_f32_bf16 v35, v13, v13, v35\n"
    "v_dot2_f32_bf16 v43, v21, v21, v43\n"
    "v_dot2_f32_bf16 v24, v2, s58, v24\n"
    "v_dot2_f32_bf16 v32, v10, s58, v32\n"
    "v_dot2_f32_bf16 v40, v18, s58, v40\n"
    "v_dot2_f32_bf16 v25, v6, s58, v25\n"
    "v_dot2_f32_bf16 v33, v14, s58, v33\n"
    "v_dot2_f32_bf16 v41, v22, s58, v41\n"
    "v_dot2_f32_bf16 v26, v2, v2, v26\n"
    "v_dot2_f32_bf16 v34, v10, v10, v34\n"
    "v_dot2_f32_bf16 v42, v18, v18, v42\n"
    "v_dot2_f32_bf16 v27, v6, v6, v27\n"
    "v_dot2_f32_bf16 v35, v14, v14, v35\n"
    "v_dot2_f32_bf16 v43, v22, v22, v43\n"
    "v_dot2_f32_bf16 v24, v3, s58, v24\n"
    "v_dot2_f32_bf16 v32, v11, s58, v32\n"
    "v_dot2_f32_bf16 v40, v19, s58, v40\n"
    "v_dot2_f32_bf16 v25, v7, s58, v25\n"
    "v_dot2_f32_bf16 v33, v15, s58, v33\n"
    "v_dot2_f32_bf16 v41, v23, s58, v41\n"
    "v_dot2_f32_bf16 v26, v3, v3, v26\n"
    "v_dot2_f32_bf16 v34, v11, v11, v34\n"
    "v_dot2_f32_bf16 v42, v19, v19, v42\n"
    "v_dot2_f32_bf16 v27, v7, v7, v27\n"
    "v_dot2_f32_bf16 v35, v15, v15, v35\n"
    "v_dot2_f32_bf16 v43, v23, v23, v43\n"
    "v_and_b32_e32 v30, 0xffff0000, v7\n"
    "v_and_b32_e32 v38, 0xffff0000, v15\n"
    "v_and_b32_e32 v46, 0xffff0000, v23\n"
    "v_add_f32_e32 v24, v24, v25\n"
    "v_add_f32_e32 v32, v32, v33\n"
    "v_add_f32_e32 v40, v40, v41\n"
    "v_add_f32_e32 v26, v26, v27\n"
    "v_add_f32_e32 v34, v34, v35\n"
    "v_add_f32_e32 v42, v42, v43\n"
    "v_add_f32_dpp v25, v25, v25 quad_perm:[1,0,3,2] row_mask:0xf bank_mask:0xf bound_ctrl:1\n"
    "v_lshlrev_b32_e32 v48, 16, v0\n"
    "v_add_f32_dpp v33, v33, v33 quad_perm:[1,0,3,2] row_mask:0xf bank_mask:0xf bound_ctrl:1\n"
    "v_and_b32_e32 v49, 0xffff0000, v0\n"
    "v_add_f32_dpp v41, v41, v41 quad_perm:[1,0,3,2] row_mask:0xf bank_mask:0xf bound_ctrl:1\n"
    "v_lshlrev_b32_e32 v50, 16, v1\n"
    "v_mov_b32_dpp v30, v30 quad_perm:[3,3,3,3] row_mask:0xf bank_mask:0xf bound_ctrl:1\n"
    "v_and_b32_e32 v51, 0xffff0000, v1\n"
    "v_mov_b32_dpp v38, v38 quad_perm:[3,3,3,3] row_mask:0xf bank_mask:0xf bound_ctrl:1\n"
    "v_lshlrev_b32_e32 v52, 16, v2\n"
    "v_mov_b32_dpp v46, v46 quad_perm:[3,3,3,3] row_mask:0xf bank_mask:0xf bound_ctrl:1\n"
    "v_and_b32_e32 v53, 0xffff0000, v2\n"
    "v_add_f32_dpp v24, v24, v24 quad_perm:[1,0,3,2] row_mask:0xf bank_mask:0xf bound_ctrl:1\n"
    "v_lshlrev_b32_e32 v54, 16, v3\n"
    "v_add_f32_dpp v32, v32, v32 quad_perm:[1,0,3,2] row_mask:0xf bank_mask:0xf bound_ctrl:1\n"
    "v_and_b32_e32 v55, 0xffff0000, v3\n"
    "v_add_f32_dpp v40, v40, v40 quad_perm:[1,0,3,2] row_mask:0xf bank_mask:0xf bound_ctrl:1\n"
    "v_lshlrev_b32_e32 v56, 16, v4\n"
    "v_add_f32_dpp v26, v26, v26 quad_perm:[1,0,3,2] row_mask:0xf bank_mask:0xf bound_ctrl:1\n"
    "v_and_b32_e32 v57, 0xffff0000, v4\n"
    "v_add_f32_dpp v34, v34, v34 quad_perm:[1,0,3,2] row_mask:0xf bank_mask:0xf bound_ctrl:1\n"
    "v_lshlrev_b32_e32 v58, 16, v5\n"
    "s_cbranch_scc1 .Lat_nojf\n"
    "s_waitcnt vmcnt(0)\n"
    "buffer_store_dword v102, off, s[8:11], s53 offset:0 sc1\n"
    ".Lat_nojf:\n"
    "v_add_f32_dpp v42, v42, v42 quad_perm:[1,0,3,2] row_mask:0xf bank_mask:0xf bound_ctrl:1\n"
    "v_and_b32_e32 v59, 0xffff0000, v5\n"
    "v_add_f32_dpp v24, v24, v24 quad_perm:[2,3,0,1] row_mask:0xf bank_mask:0xf bound_ctrl:1\n"
    "v_lshlrev_b32_e32 v60, 16, v6\n"
    "v_add_f32_dpp v32, v32, v32 quad_perm:[2,3,0,1] row_mask:0xf bank_mask:0xf bound_ctrl:1\n"
    "v_and_b32_e32 v61, 0xffff0000, v6\n"
    "v_add_f32_dpp v40, v40, v40 quad_perm:[2,3,0,1] row_mask:0xf bank_mask:0xf bound_ctrl:1\n"
    "v_lshlrev_b32_e32 v62, 16, v7\n"
    "v_add_f32_dpp v26, v26, v26 quad_perm:[2,3,0,1] row_mask:0xf bank_mask:0xf bound_ctrl:1\n"
    "v_and_b32_e32 v63, 0xffff0000, v7\n"
    "v_add_f32_dpp v34, v34, v34 quad_perm:[2,3,0,1] row_mask:0xf bank_mask:0xf bound_ctrl:1\n"
    "v_lshlrev_b32_e32 v64, 16, v8\n"
    "v_add_f32_dpp v42, v42, v42 quad_perm:[2,3,0,1] row_mask:0xf bank_mask:0xf bound_ctrl:1\n"
    "v_and_b32_e32 v65, 0xffff0000, v8\n"
    "v_add_f32_dpp v24, v24, v24 row_half_mirror row_mask:0xf bank_mask:0xf bound_ctrl:1\n"
    "v_lshlrev_b32_e32 v66, 16, v9\n"
    "v_add_f32_dpp v32, v32, v32 row_half_mirror row_mask:0xf bank_mask:0xf bound_ctrl:1\n"
    "v_and_b32_e32 v67, 0xffff0000, v9\n"
    "v_add_f32_dpp v40, v40, v40 row_half_mirror row_mask:0xf bank_mask:0xf bound_ctrl:1\n"
    "v_lshlrev_b32_e32 v68, 16, v10\n"
    "v_add_f32_dpp v26, v26, v26 row_half_mirror row_mask:0xf bank_mask:0xf bound_ctrl:1\n"
    "v_and_b32_e32 v69, 0xffff0000, v10\n"
    "v_add_f32_dpp v34, v34, v34 row_half_mirror row_mask:0xf bank_mask:0xf bound_ctrl:1\n"
    "v_lshlrev_b32_e32 v70, 16, v11\n"
    "v_add_f32_dpp v42, v42, v42 row_half_mirror row_mask:0xf bank_mask:0xf bound_ctrl:1\n"
    "v_and_b32_e32 v71, 0xffff0000, v11\n"
    "v_add_f32_dpp v24, v24, v24 row_mirror row_mask:0xf bank_mask:0xf bound_ctrl:1\n"
    "v_lshlrev_b32_e32 v72, 16, v12\n"
    "v_add_f32_dpp v32, v32, v32 row_mirror row_mask:0xf bank_mask:0xf bound_ctrl:1\n"
    "v_and_b32_e32 v73, 0xffff0000, v12\n"
    "v_add_f32_dpp v40, v40, v40 row_mirror row_mask:0xf bank_mask:0xf bound_ctrl:1\n"
    "v_lshlrev_b32_e32 v74, 16, v13\n"
    "v_add_f32_dpp v26, v26, v26 row_mirror row_mask:0xf bank_mask:0xf bound_ctrl:1\n"
    "v_and_b32_e32 v75, 0xffff0000, v13\n"
    "v_add_f32_dpp v34, v34, v34 row_mirror row_mask:0xf bank_mask:0xf bound_ctrl:1\n"
    "v_lshlrev_b32_e32 v76, 16, v14\n"
    "v_add_f32_dpp v42, v42, v42 row_mirror row_mask:0xf bank_mask:0xf bound_ctrl:1\n"
    "v_and_b32_e32 v77, 0xffff0000, v14\n"
    "v_mul_f32_e32 v24, 0x3b800000, v24\n"
    "v_lshlrev_b32_e32 v78, 16, v15\n"
    "v_mul_f32_e32 v32, 0x3b800000, v32\n"
    "v_and_b32_e32 v79, 0xffff0000, v15\n"
    "v_mul_f32_e32 v40, 0x3b800000, v40\n"
    "v_lshlrev_b32_e32 v80, 16, v16\n"
    "v_mul_f32_e32 v25, 0.25, v25\n"
    "v_and_b32_e32 v81, 0xffff0000, v16\n"
    "v_mul_f32_e32 v33, 0.25, v33\n"
    "v_lshlrev_b32_e32 v82, 16, v17\n"
    "v_mul_f32_e32 v41, 0.25, v41\n"
    "v_and_b32_e32 v83, 0xffff0000, v17\n"
    "v_mul_f32_e64 v27, v24, -v24\n"
    "v_lshlrev_b32_e32 v84, 16, v18\n"
    "v_mul_f32_e64 v35, v32, -v32\n"
    "v_and_b32_e32 v85, 0xffff0000, v18\n"
    "v_mul_f32_e64 v43, v40, -v40\n"
    "v_lshlrev_b32_e32 v86, 16, v19\n"
    "v_sub_f32_e32 v25, v25, v30\n"
    "v_and_b32_e32 v87, 0xffff0000, v19\n"
    "v_sub_f32_e32 v33, v33, v38\n"
    "v_lshlrev_b32_e32 v88, 16, v20\n"
    "v_sub_f32_e32 v41, v41, v46\n"
    "v_and_b32_e32 v89, 0xffff0000, v20\n"
    "v_fmac_f32_e32 v27, 0x3b800000, v26\n"
    "v_lshlrev_b32_e32 v90, 16, v21\n"
    "v_fmac_f32_e32 v35, 0x3b800000, v34\n"
    "v_and_b32_e32 v91, 0xffff0000, v21\n"
    "v_fmac_f32_e32 v43, 0x3b800000, v42\n"
    "v_lshlrev_b32_e32 v92, 16, v22\n"
    "v_mul_f32_e32 v25, 0x3eaaaaab, v25\n"
    "v_and_b32_e32 v93, 0xffff0000, v22\n"
    "v_mul_f32_e32 v33, 0x3eaaaaab, v33\n"
    "v_lshlrev_b32_e32 v94, 16, v23\n"
    "v_mul_f32_e32 v41, 0x3eaaaaab, v41\n"
    "v_and_b32_e32 v95, 0xffff0000, v23\n"
    "v_max_f32_e32 v27, 0, v27\n"
    "v_max_f32_e32 v35, 0, v35\n"
    "v_max_f32_e32 v43, 0, v43\n"
    "s_cbranch_scc1 .Lat_bin\n"
    ".Lat_bin_ret:\n"
    "v_cndmask_b32_e64 v25, v24, v25, s[62:63]\n"
    "v_cndmask_b32_e64 v33, v32, v33, s[62:63]\n"
    "v_cndmask_b32_e64 v41, v40, v41, s[62:63]\n"
    "v_add_f32_e32 v27, 0x3727c5ac, v27\n"
    "v_add_f32_e32 v35, 0x3727c5ac, v35\n"
    "v_add_f32_e32 v43, 0x3727c5ac, v43\n"
    "v_rsq_f32_e32 v28, v27\n"
    "v_rsq_f32_e32 v36, v35\n"
    "v_rsq_f32_e32 v44, v43\n"
    "v_fma_f32 v29, -v24, v28, s57\n"
    "v_fma_f32 v37, -v32, v36, s57\n"
    "v_fma_f32 v45, -v40, v44, s57\n"
    "v_fma_f32 v31, -v25, v28, s57\n"
    "v_fma_f32 v39, -v33, v36, s57\n"
    "v_fma_f32 v47, -v41, v44, s57\n"
    "v_sub_f32_e32 v24, v29, v31\n"
    "v_sub_f32_e32 v32, v37, v39\n"
    "v_sub_f32_e32 v40, v45, v47\n"
    "v_pk_fma_f32 v[48:49], v[48:49], v[28:29], v[28:29] op_sel:[0,0,1] op_sel_hi:[1,0,1]\n"
    "v_pk_fma_f32 v[50:51], v[50:51], v[28:29], v[28:29] op_sel:[0,0,1] op_sel_hi:[1,0,1]\n"
    "v_pk_fma_f32 v[52:53], v[52:53], v[28:29], v[28:29] op_sel:[0,0,1] op_sel_hi:[1,0,1]\n"
    "v_pk_fma_f32 v[54:55], v[54:55], v[28:29], v[28:29] op_sel:[0,0,1] op_sel_hi:[1,0,1]\n"
    "v_pk_fma_f32 v[56:57], v[56:57], v[28:29], v[30:31] op_sel:[0,0,1] op_sel_hi:[1,0,1]\n"
    "v_pk_fma_f32 v[58:59], v[58:59], v[28:29], v[30:31] op_sel:[0,0,1] op_sel_hi:[1,0,1]\n"
    "v_pk_fma_f32 v[60:61], v[60:61], v[28:29], v[30:31] op_sel:[0,0,1] op_sel_hi:[1,0,1]\n"
    "v_pk_fma_f32 v[62:63], v[62:63], v[28:29], v[30:31] op_sel:[0,0,1] op_sel_hi:[1,0,1]\n"
    "v_pk_add_f32 v[48:49], v[48:49], s[56:57] op_sel_hi:[1,0]\n"
    "v_pk_add_f32 v[50:51], v[50:51], s[56:57] op_sel_hi:[1,0]\n"
    "v_pk_add_f32 v[52:53], v[52:53], s[56:57] op_sel_hi:[1,0]\n"
    "v_pk_add_f32 v[54:55], v[54:55], s[56:57] op_sel_hi:[1,0]\n"
    "v_pk_add_f32 v[56:57], v[56:57], s[56:57] op_sel_hi:[1,0]\n"
    "v_pk_add_f32 v[58:59], v[58:59], s[56:57] op_sel_hi:[1,0]\n"
    "v_pk_add_f32 v[60:61], v[60:61], s[56:57] op_sel_hi:[1,0]\n"
    "v_pk_add_f32 v[62:63], v[62:63], s[56:57] op_sel_hi:[1,0]\n"
    "v_cvt_pk_bf16_f32 v0, v48, v49\n"
    "v_cvt_pk_bf16_f32 v1, v50, v51\n"
    "v_cvt_pk_bf16_f32 v2, v52, v53\n"
    "v_cvt_pk_bf16_f32 v3, v54, v55\n"
    "v_cvt_pk_bf16_f32 v4, v56, v57\n"
    "v_cvt_pk_bf16_f32 v5, v58, v59\n"
    "v_cvt_pk_bf16_f32 v6, v60, v61\n"
    "v_cvt_pk_bf16_f32 v7, v62, v63\n"
    "buffer_store_dwordx4 v[0:3], v104, s[8:11], s35 offen offset:0 sc1\n"
    "v_bfi_b32 v7, v107, v7, 1.0\n"
    "ds_write_b128 v105, v[0:3] offset:0\n"
    "buffer_store_dwordx4 v[4:7], v104, s[8:11], s35 offen offset:256 sc1\n"
    "v_pk_fma_f32 v[64:65], v[64:65], v[36:37], v[36:37] op_sel:[0,0,1] op_sel_hi:[1,0,1]\n"
    "v_pk_fma_f32 v[66:67], v[66:67], v[36:37], v[36:37] op_sel:[0,0,1] op_sel_hi:[1,0,1]\n"
    "v_pk_fma_f32 v[68:69], v[68:69], v[36:37], v[36:37] op_sel:[0,0,1] op_sel_hi:[1,0,1]\n"
    "v_pk_fma_f32 v[70:71], v[70:71], v[36:37], v[36:37] op_sel:[0,0,1] op_sel_hi:[1,0,1]\n"
    "v_pk_fma_f32 v[72:73], v[72:73], v[36:37], v[38:39] op_sel:[0,0,1] op_sel_hi:[1,0,1]\n"
    "v_pk_fma_f32 v[74:75], v[74:75], v[36:37], v[38:39] op_sel:[0,0,1] op_sel_hi:[1,0,1]\n"
    "v_pk_fma_f32 v[76:77], v[76:77], v[36:37], v[38:39] op_sel:[0,0,1] op_sel_hi:[1,0,1]\n"
    "v_pk_fma_f32 v[78:79], v[78:79], v[36:37], v[38:39] op_sel:[0,0,1] op_sel_hi:[1,0,1]\n"
    "v_pk_add_f32 v[64:65], v[64:65], s[56:57] op_sel_hi:[1,0]\n"
    "v_pk_add_f32 v[66:67], v[66:67], s[56:57] op_sel_hi:[1,0]\n"
    "v_pk_add_f32 v[68:69], v[68:69], s[56:57] op_sel_hi:[1,0]\n"
    "v_pk_add_f32 v[70:71], v[70:71], s[56:57] op_sel_hi:[1,0]\n"
    "v_pk_add_f32 v[72:73], v[72:73], s[56:57] op_sel_hi:[1,0]\n"
    "v_pk_add_f32 v[74:75], v[74:75], s[56:57] op_sel_hi:[1,0]\n"
    "v_pk_add_f32 v[76:77], v[76:77], s[56:57] op_sel_hi:[1,0]\n"
    "v_pk_add_f32 v[78:79], v[78:79], s[56:57] op_sel_hi:[1,0]\n"
    "v_cvt_pk_bf16_f32 v8, v64, v65\n"
    "v_cvt_pk_bf16_f32 v9, v66, v67\n"
    "v_cvt_pk_bf16_f32 v10, v68, v69\n"
    "v_cvt_pk_bf16_f32 v11, v70, v71\n"
    "v_cvt_pk_bf16_f32 v12, v72, v73\n"
    "v_cvt_pk_bf16_f32 v13, v74, v75\n"
    "v_cvt_pk_bf16_f32 v14, v76, v77\n"
    "v_cvt_pk_bf16_f32 v15, v78, v79\n"
    "buffer_store_dwordx4 v[8:11], v104, s[8:11], s54 offen offset:0 sc1\n"
    "v_bfi_b32 v15, v107, v15, 1.0\n"
    "ds_write_b128 v105, v[8:11] offset:8192\n"
    "buffer_store_dwordx4 v[12:15], v104, s[8:11], s54 offen offset:256 sc1\n"
    "v_pk_fma_f32 v[80:81], v[80:81], v[44:45], v[44:45] op_sel:[0,0,1] op_sel_hi:[1,0,1]\n"
    "v_pk_fma_f32 v[82:83], v[82:83], v[44:45], v[44:45] op_sel:[0,0,1] op_sel_hi:[1,0,1]\n"
    "v_pk_fma_f32 v[84:85], v[84:85], v[44:45], v[44:45] op_sel:[0,0,1] op_sel_hi:[1,0,1]\n"
    "v_pk_fma_f32 v[86:87], v[86:87], v[44:45], v[44:45] op_sel:[0,0,1] op_sel_hi:[1,0,1]\n"
    "v_pk_fma_f32 v[88:89], v[88:89], v[44:45], v[46:47] op_sel:[0,0,1] op_sel_hi:[1,0,1]\n"
    "v_pk_fma_f32 v[90:91], v[90:91], v[44:45], v[46:47] op_sel:[0,0,1] op_sel_hi:[1,0,1]\n"
    "v_pk_fma_f32 v[92:93], v[92:93], v[44:45], v[46:47] op_sel:[0,0,1] op_sel_hi:[1,0,1]\n"
    "v_pk_fma_f32 v[94:95], v[94:95], v[44:45], v[46:47] op_sel:[0,0,1] op_sel_hi:[1,0,1]\n"
    "v_pk_add_f32 v[80:81], v[80:81], s[56:57] op_sel_hi:[1,0]\n"
    "v_pk_add_f32 v[82:83], v[82:83], s[56:57] op_sel_hi:[1,0]\n"
    "v_pk_add_f32 v[84:85], v[84:85], s[56:57] op_sel_hi:[1,0]\n"
    "v_pk_add_f32 v[86:87], v[86:87], s[56:57] op_sel_hi:[1,0]\n"
    "v_pk_add_f32 v[88:89], v[88:89], s[56:57] op_sel_hi:[1,0]\n"
    "v_pk_add_f32 v[90:91], v[90:91], s[56:57] op_sel_hi:[1,0]\n"
    "v_pk_add_f32 v[92:93], v[92:93], s[56:57] op_sel_hi:[1,0]\n"
    "v_pk_add_f32 v[94:95], v[94:95], s[56:57] op_sel_hi:[1,0]\n"
    "v_cvt_pk_bf16_f32 v16, v80, v81\n"
    "v_cvt_pk_bf16_f32 v17, v82, v83\n"
    "v_cvt_pk_bf16_f32 v18, v84, v85\n"
    "v_cvt_pk_bf16_f32 v19, v86, v87\n"
    "v_cvt_pk_bf16_f32 v20, v88, v89\n"
    "v_cvt_pk_bf16_f32 v21, v90, v91\n"
    "v_cvt_pk_bf16_f32 v22, v92, v93\n"
    "v_cvt_pk_bf16_f32 v23, v94, v95\n"
    "buffer_store_dwordx4 v[16:19], v104, s[8:11], s60 offen offset:0 sc1\n"
    "v_bfi_b32 v23, v107, v23, 1.0\n"
    "ds_write_b128 v105, v[16:19] offset:16384\n"
    "buffer_store_dwordx4 v[20:23], v104, s[8:11], s60 offen offset:256 sc1\n"
    "v_pk_add_f32 v[56:57], v[56:57], v[24:25] op_sel_hi:[1,0]\n"
    "v_pk_add_f32 v[58:59], v[58:59], v[24:25] op_sel_hi:[1,0]\n"
    "v_pk_add_f32 v[60:61], v[60:61], v[24:25] op_sel_hi:[1,0]\n"
    "v_pk_add_f32 v[62:63], v[62:63], v[24:25] op_sel_hi:[1,0]\n"
    "v_cvt_pk_bf16_f32 v56, v56, v57\n"
    "v_cvt_pk_bf16_f32 v57, v58, v59\n"
    "v_cvt_pk_bf16_f32 v58, v60, v61\n"
    "v_cvt_pk_bf16_f32 v59, v62, v63\n"
    "ds_write_b128 v106, v[56:59] offset:0\n"
    "v_pk_add_f32 v[72:73], v[72:73], v[32:33] op_sel_hi:[1,0]\n"
    "v_pk_add_f32 v[74:75], v[74:75], v[32:33] op_sel_hi:[1,0]\n"
    "v_pk_add_f32 v[76:77], v[76:77], v[32:33] op_sel_hi:[1,0]\n"
    "v_pk_add_f32 v[78:79], v[78:79], v[32:33] op_sel_hi:[1,0]\n"
    "v_cvt_pk_bf16_f32 v72, v72, v73\n"
    "v_cvt_pk_bf16_f32 v73, v74, v75\n"
    "v_cvt_pk_bf16_f32 v74, v76, v77\n"
    "v_cvt_pk_bf16_f32 v75, v78, v79\n"
    "ds_write_b128 v106, v[72:75] offset:8192\n"
    "v_pk_add_f32 v[88:89], v[88:89], v[40:41] op_sel_hi:[1,0]\n"
    "v_pk_add_f32 v[90:91], v[90:91], v[40:41] op_sel_hi:[1,0]\n"
    "v_pk_add_f32 v[92:93], v[92:93], v[40:41] op_sel_hi:[1,0]\n"
    "v_pk_add_f32 v[94:95], v[94:95], v[40:41] op_sel_hi:[1,0]\n"
    "v_cvt_pk_bf16_f32 v88, v88, v89\n"
    "v_cvt_pk_bf16_f32 v89, v90, v91\n"
    "v_cvt_pk_bf16_f32 v90, v92, v93\n"
    "v_cvt_pk_bf16_f32 v91, v94, v95\n"
    "ds_write_b128 v106, v[88:91] offset:16384\n"
    "s_cbranch_scc1 .Lat_bin2\n"
    ".Lat_bin2_ret:\n"
    "s_waitcnt lgkmcnt(0)\n"
    "s_barrier\n"
    "ds_read_b128 a[0:3], v96 offset:0\n"
    "ds_read_b128 a[32:35], v96 offset:8192\n"
    "ds_read_b128 a[64:67], v96 offset:16384\n"
    "ds_read_b128 a[4:7], v97 offset:0\n"
    "ds_read_b128 a[36:39], v97 offset:8192\n"
    "ds_read_b128 a[68:71], v97 offset:16384\n"
    "ds_read_b128 a[8:11], v96 offset:2048\n"
    "ds_read_b128 a[40:43], v96 offset:10240\n"
    "ds_read_b128 a[72:75], v96 offset:18432\n"
    "ds_read_b128 a[12:15], v97 offset:2048\n"
    "ds_read_b128 a[44:47], v97 offset:10240\n"
    "ds_read_b128 a[76:79], v97 offset:18432\n"
    "buffer_load_dword v101, v100, s[8:11], 0 offen offset:0 sc1\n"
    "ds_read_b128 a[16:19], v96 offset:4096\n"
    "ds_read_b128 a[48:51], v96 offset:12288\n"
    "ds_read_b128 a[80:83], v96 offset:20480\n"
    "ds_read_b128 a[20:23], v97 offset:4096\n"
    "ds_read_b128 a[52:55], v97 offset:12288\n"
    "ds_read_b128 a[84:87], v97 offset:20480\n"
    "ds_read_b128 a[24:27], v96 offset:6144\n"
    "ds_read_b128 a[56:59], v96 offset:14336\n"
    "ds_read_b128 a[88:91], v96 offset:22528\n"
    "ds_read_b128 a[28:31], v97 offset:6144\n"
    "ds_read_b128 a[60:63], v97 offset:14336\n"
    "ds_read_b128 a[92:95], v97 offset:22528\n"
    "ds_read_b128 v[40:43], v108 offset:0\n"
    "s_mul_i32 s52, s93, 33792\n"
    "s_add_u32 s52, s52, 5242880\n"
    "s_lshl_b32 s61, s32, 12\n"
    "s_lshl4_add_u32 s46, s34, 6076416\n"
    "s_cmp_eq_u32 s93, 0\n"
    "s_cselect_b32 s46, s46, 2.0\n"
    "s_lshl_b32 s48, s34, 7\n"
    "s_add_u32 s48, s48, 42659840\n"
    "s_lshl_b32 s47, s93, 12\n"
    "s_waitcnt vmcnt(1)\n"
    "s_waitcnt lgkmcnt(0)\n"
    "s_barrier\n"
    "buffer_store_dword v102, off, s[8:11], s46 offset:0 sc1\n"
    "buffer_store_dwordx4 v[40:43], v109, s[8:11], s48 offen offset:0 sc1\n"
    "s_lshl4_add_u32 s46, s34, s47\n"
    "s_add_u32 s46, s46, 6084608\n"
    "s_waitcnt vmcnt(2)\n"
    "v_cmp_ne_u32_e32 vcc, s39, v101\n"
    "s_nop 4\n"
    "s_cbranch_vccnz .Lat_apoll\n"
    ".Lat_apoll_ret:\n"
    "s_add_u32 s47, s52, s61\n"
    "buffer_load_dwordx4 v[112:115], v250, s[8:11], s47 offen offset:0 sc1\n"
    "buffer_load_dwordx4 v[116:119], v250, s[8:11], s47 offen offset:1024 sc1\n"
    "buffer_load_dwordx4 v[128:131], v250, s[8:11], s47 offen offset:2048 sc1\n"
    "s_add_u32 s61, s61, 0x1000\n"
    "buffer_load_dwordx4 v[132:135], v250, s[8:11], s47 offen offset:3072 sc1\n"
    "s_bitset0_b32 s61, 14\n"
    "s_add_u32 s47, s52, s61\n"
    "buffer_load_dwordx4 v[144:147], v250, s[8:11], s47 offen offset:0 sc1\n"
    "buffer_load_dwordx4 v[148:151], v250, s[8:11], s47 offen offset:1024 sc1\n"
    "buffer_load_dwordx4 v[160:163], v250, s[8:11], s47 offen offset:2048 sc1\n"
    "s_add_u32 s61, s61, 0x1000\n"
    "buffer_load_dwordx4 v[164:167], v250, s[8:11], s47 offen offset:3072 sc1\n"
    "s_bitset0_b32 s61, 14\n"
    "s_add_u32 s47, s52, s61\n"
    "buffer_load_dwordx4 v[176:179], v250, s[8:11], s47 offen offset:0 sc1\n"
    "buffer_load_dwordx4 v[180:183], v250, s[8:11], s47 offen offset:1024 sc1\n"
    "buffer_load_dwordx4 v[192:195], v250, s[8:11], s47 offen offset:2048 sc1\n"
    "s_add_u32 s61, s61, 0x1000\n"
    "v_and_b32_e32 v252, 0x30, v250\n"
    "v_add_u32_e32 v252, 0x400, v252\n"
    "s_mul_i32 s59, s32, 999\n"
    "s_andn2_b32 s59, s59, 63\n"
    "s_sub_u32 s48, 3968, s59\n"
    "v_cmp_le_u32_e32 vcc, 0x80, v250\n"
    "v_mov_b32_e32 v80, s48\n"
    "s_add_u32 s59, s59, 6076416\n"
    "v_cndmask_b32_e32 v83, v250, v80, vcc\n"
    "v_bfe_u32 v253, v250, 4, 2\n"
    "v_max_u32_e32 v253, 1, v253\n"
    "v_add_u32_e32 v253, s32, v253\n"
    "v_and_b32_e32 v253, 3, v253\n"
    "v_lshlrev_b32_e32 v253, 9, v253\n"
    "v_mov_b32_e32 v84, s39\n"
    "v_lshrrev_b32_e32 v86, 2, v250\n"
    "v_min_u32_e32 v86, 124, v86\n"
    "s_lshl2_add_u32 s58, s34, s93\n"
    "s_lshl_b32 s58, s58, 7\n"
    "s_add_u32 s58, s58, 5910528\n"
    "s_lshl_b32 s45, s50, 13\n"
    "v_add_u32_e32 v85, s45, v250\n"
    "s_lshl3_add_u32 s45, s45, 49152\n"
    "v_add_u32_e32 v255, s45, v250\n"
    "v_lshrrev_b32_e32 v105, 7, v250\n"
    "v_and_b32_e32 v104, 0x70, v251\n"
    "v_lshl_add_u32 v104, v105, 9, v104\n"
    "v_add_u32_e32 v105, 0xc00, v104\n"
    "v_add_u32_e32 v106, 0x1800, v104\n"
    "v_add_u32_e32 v107, 0x2400, v104\n"
    "s_lshl_b32 s57, s93, 7\n"
    "s_lshl4_add_u32 s56, s57, 24576\n"
    "s_add_u32 s57, s57, 5111808\n"
    "s_mul_i32 s45, s51, 32768\n"
    "s_add_u32 s45, s45, 8192\n"
    "v_subrev_u32_e32 v46, s45, v240\n"
    "v_subrev_u32_e32 v47, s45, v241\n"
    "s_mul_i32 s45, s51, 61440\n"
    "s_add_u32 s45, s45, 8192\n"
    "v_subrev_u32_e32 v4, s45, v244\n"
    "v_subrev_u32_e32 v5, s45, v245\n"
    "v_subrev_u32_e32 v6, s45, v246\n"
    "v_subrev_u32_e32 v7, s45, v247\n"
    "s_waitcnt vmcnt(9)\n"
    "v_mfma_f32_16x16x32_bf16 v[24:27], v[112:115], a[0:3], 0\n"
    "buffer_load_dwordx4 v[196:199], v250, s[8:11], s47 offen offset:3072 sc1\n"
    "s_bitset0_b32 s61, 14\n"
    "v_mfma_f32_16x16x32_bf16 v[28:31], v[116:119], a[0:3], 0\n"
    "v_accvgpr_write_b32 a244, 0\n"
    "v_mfma_f32_16x16x32_bf16 v[48:51], v[112:115], a[32:35], 0\n"
    "v_accvgpr_write_b32 a245, 0\n"
    "v_mfma_f32_16x16x32_bf16 v[52:55], v[116:119], a[32:35], 0\n"
    "s_add_u32 s47, s52, s61\n"
    "buffer_load_dwordx4 v[208:211], v250, s[8:11], s47 offen offset:0 sc1\n"
    "v_mfma_f32_16x16x32_bf16 v[64:67], v[112:115], a[64:67], 0\n"
    "v_bfe_u32 v0, v250, 4, 2\n"
    "v_mfma_f32_16x16x32_bf16 v[68:71], v[116:119], a[64:67], 0\n"
    "s_mov_b32 s18, 5120000\n"
    "v_accvgpr_write_b32 a246, 0\n"
    "s_waitcnt vmcnt(9)\n"
    "v_mfma_f32_16x16x32_bf16 v[24:27], v[128:131], a[4:7], v[24:27]\n"
    "buffer_load_dwordx4 v[212:215], v250, s[8:11], s47 offen offset:1024 sc1\n"
    "s_add_u32 s52, s52, 0x4000\n"
    "v_mfma_f32_16x16x32_bf16 v[28:31], v[132:135], a[4:7], v[28:31]\n"
    "buffer_store_dword v102, off, s[8:11], s46 offset:0 sc1\n"
    "v_accvgpr_write_b32 a247, 0\n"
    "v_mfma_f32_16x16x32_bf16 v[48:51], v[128:131], a[36:39], v[48:51]\n"
    "v_bfe_u32 v1, v250, 6, 2\n"
    "v_accvgpr_write_b32 a248, 0\n"
    "v_mfma_f32_16x16x32_bf16 v[52:55], v[132:135], a[36:39], v[52:55]\n"
    "buffer_load_dwordx4 v[224:227], v250, s[8:11], s47 offen offset:2048 sc1\n"
    "s_add_u32 s61, s61, 0x1000\n"
    "v_mfma_f32_16x16x32_bf16 v[64:67], v[128:131], a[68:71], v[64:67]\n"
    "v_mov_b32_e32 v2, 0x3f80\n"
    "v_accvgpr_write_b32 a249, 0\n"
    "v_mfma_f32_16x16x32_bf16 v[68:71], v[132:135], a[68:71], v[68:71]\n"
    "s_mov_b32 s19, 0x20000\n"
    "s_waitcnt vmcnt(10)\n"
    "v_mfma_f32_16x16x32_bf16 v[24:27], v[144:147], a[8:11], v[24:27]\n"
    "buffer_load_dwordx4 v[228:231], v250, s[8:11], s47 offen offset:3072 sc1\n"
    "s_bitset0_b32 s61, 14\n"
    "v_mfma_f32_16x16x32_bf16 v[28:31], v[148:151], a[8:11], v[28:31]\n"
    "v_accvgpr_write_b32 a250, 0\n"
    "v_mfma_f32_16x16x32_bf16 v[48:51], v[144:147], a[40:43], v[48:51]\n"
    "s_mov_b64 s[20:21], s[8:9]\n"
    "v_accvgpr_write_b32 a251, 0\n"
    "v_mfma_f32_16x16x32_bf16 v[52:55], v[148:151], a[40:43], v[52:55]\n"
    "s_add_u32 s47, s52, s61\n"
    "buffer_load_dwordx4 v[120:123], v250, s[8:11], s47 offen offset:0 sc1\n"
    "v_mfma_f32_16x16x32_bf16 v[64:67], v[144:147], a[72:75], v[64:67]\n"
    "v_cmp_eq_u32_e32 vcc, 3, v0\n"
    "s_nop 1\n"
    "v_cndmask_b32_e64 v2, v2, 1.0, vcc\n"
    "v_mfma_f32_16x16x32_bf16 v[68:71], v[148:151], a[72:75], v[68:71]\n"
    "v_accvgpr_write_b32 a252, 0\n"
    "s_waitcnt vmcnt(10)\n"
    "v_mfma_f32_16x16x32_bf16 v[24:27], v[160:163], a[12:15], v[24:27]\n"
    "buffer_load_dwordx4 v[124:127], v250, s[8:11], s47 offen offset:1024 sc1\n"
    "v_accvgpr_write_b32 a253, 0\n"
    "v_mfma_f32_16x16x32_bf16 v[28:31], v[164:167], a[12:15], v[28:31]\n"
    "s_mov_b32 s22, 5242880\n"
    "v_accvgpr_write_b32 a254, 0\n"
    "v_mfma_f32_16x16x32_bf16 v[48:51], v[160:163], a[44:47], v[48:51]\n"
    "v_cmp_eq_u32_e32 vcc, 0, v1\n"
    "s_nop 1\n"
    "v_cndmask_b32_e32 v3, 0, v2, vcc\n"
    "v_mfma_f32_16x16x32_bf16 v[52:55], v[164:167], a[44:47], v[52:55]\n"
    "buffer_load_dwordx4 v[136:139], v250, s[8:11], s47 offen offset:2048 sc1\n"
    "s_add_u32 s61, s61, 0x1000\n"
    "v_mfma_f32_16x16x32_bf16 v[64:67], v[160:163], a[76:79], v[64:67]\n"
    "v_accvgpr_write_b32 a232, v3\n"
    "v_accvgpr_write_b32 a255, 0\n"
    "v_mfma_f32_16x16x32_bf16 v[68:71], v[164:167], a[76:79], v[68:71]\n"
    "s_mov_b64 s[24:25], s[28:29]\n"
    "v_accvgpr_write_b32 a192, 0\n"
    "s_waitcnt vmcnt(10)\n"
    "v_mfma_f32_16x16x32_bf16 v[24:27], v[176:179], a[16:19], v[24:27]\n"
    "buffer_load_dwordx4 v[140:143], v250, s[8:11], s47 offen offset:3072 sc1\n"
    "s_bitset0_b32 s61, 14\n"
    "v_mfma_f32_16x16x32_bf16 v[28:31], v[180:183], a[16:19], v[28:31]\n"
    "v_accvgpr_write_b32 a193, 0\n"
    "v_mfma_f32_16x16x32_bf16 v[48:51], v[176:179], a[48:51], v[48:51]\n"
    "v_cmp_eq_u32_e32 vcc, 1, v1\n"
    "s_nop 1\n"
    "v_cndmask_b32_e32 v3, 0, v2, vcc\n"
    "v_mfma_f32_16x16x32_bf16 v[52:55], v[180:183], a[48:51], v[52:55]\n"
    "s_add_u32 s47, s52, s61\n"
    "buffer_load_dwordx4 v[152:155], v250, s[8:11], s47 offen offset:0 sc1\n"
    "v_mfma_f32_16x16x32_bf16 v[64:67], v[176:179], a[80:83], v[64:67]\n"
    "buffer_load_dword v80, v83, s[8:11], s59 offen offset:0 sc1\n"
    "v_accvgpr_write_b32 a194, 0\n"
    "v_mfma_f32_16x16x32_bf16 v[68:71], v[180:183], a[80:83], v[68:71]\n"
    "s_movk_i32 s26, 20000\n"
    "v_accvgpr_write_b32 a195, 0\n"
    "s_waitcnt vmcnt(11)\n"
    "v_mfma_f32_16x16x32_bf16 v[24:27], v[192:195], a[20:23], v[24:27]\n"
    "buffer_load_dwordx4 v[156:159], v250, s[8:11], s47 offen offset:1024 sc1\n"
    "v_accvgpr_write_b32 a196, 0\n"
    "v_mfma_f32_16x16x32_bf16 v[28:31], v[196:199], a[20:23], v[28:31]\n"
    "v_accvgpr_write_b32 a233, v3\n"
    "v_mfma_f32_16x16x32_bf16 v[48:51], v[192:195], a[52:55], v[48:51]\n"
    "s_bfe_u32 s88, s92, 0x20001\n"
    "v_accvgpr_write_b32 a197, 0\n"
    "v_mfma_f32_16x16x32_bf16 v[52:55], v[196:199], a[52:55], v[52:55]\n"
    "buffer_load_dwordx4 v[168:171], v250, s[8:11], s47 offen offset:2048 sc1\n"
    "s_add_u32 s61, s61, 0x1000\n"
    "v_mfma_f32_16x16x32_bf16 v[64:67], v[192:195], a[84:87], v[64:67]\n"
    "v_cmp_eq_u32_e32 vcc, 2, v1\n"
    "s_nop 1\n"
    "v_cndmask_b32_e32 v3, 0, v2, vcc\n"
    "v_mfma_f32_16x16x32_bf16 v[68:71], v[196:199], a[84:87], v[68:71]\n"
    "v_accvgpr_write_b32 a198, 0\n"
    "s_waitcnt vmcnt(11)\n"
    "v_mfma_f32_16x16x32_bf16 v[24:27], v[208:211], a[24:27], v[24:27]\n"
    "buffer_load_dwordx4 v[172:175], v250, s[8:11], s47 offen offset:3072 sc1\n"
    "s_bitset0_b32 s61, 14\n"
    "v_mfma_f32_16x16x32_bf16 v[28:31], v[212:215], a[24:27], v[28:31]\n"
    "s_lshl_b32 s82, s88, 7\n"
    "v_accvgpr_write_b32 a199, 0\n"
    "v_mfma_f32_16x16x32_bf16 v[48:51], v[208:211], a[56:59], v[48:51]\n"
    "v_accvgpr_write_b32 a234, v3\n"
    "v_accvgpr_write_b32 a200, 0\n"
    "v_mfma_f32_16x16x32_bf16 v[52:55], v[212:215], a[56:59], v[52:55]\n"
    "s_add_u32 s47, s52, s61\n"
    "buffer_load_dwordx4 v[184:187], v250, s[8:11], s47 offen offset:0 sc1\n"
    "v_mfma_f32_16x16x32_bf16 v[64:67], v[208:211], a[88:91], v[64:67]\n"
    "v_cmp_eq_u32_e32 vcc, 3, v1\n"
    "s_nop 1\n"
    "v_cndmask_b32_e32 v3, 0, v2, vcc\n"
    "v_mfma_f32_16x16x32_bf16 v[68:71], v[212:215], a[88:91], v[68:71]\n"
    "s_add_u32 s82, s82, 6101248\n"
    "v_accvgpr_write_b32 a201, 0\n"
    "s_waitcnt vmcnt(10)\n"
    "v_mfma_f32_16x16x32_bf16 v[24:27], v[224:227], a[28:31], v[24:27]\n"
    "buffer_load_dwordx4 v[188:191], v250, s[8:11], s47 offen offset:1024 sc1\n"
    "v_accvgpr_write_b32 a202, 0\n"
    "v_mfma_f32_16x16x32_bf16 v[28:31], v[228:231], a[28:31], v[28:31]\n"
    "v_accvgpr_write_b32 a235, v3\n"
    "v_accvgpr_write_b32 a203, 0\n"
    "v_mfma_f32_16x16x32_bf16 v[48:51], v[224:227], a[60:63], v[48:51]\n"
    "s_lshl_b32 s83, s88, 17\n"
    "v_accvgpr_write_b32 a204, 0\n"
    "v_mfma_f32_16x16x32_bf16 v[52:55], v[228:231], a[60:63], v[52:55]\n"
    "buffer_load_dwordx4 v[200:203], v250, s[8:11], s47 offen offset:2048 sc1\n"
    "s_add_u32 s61, s61, 0x1000\n"
    "v_mfma_f32_16x16x32_bf16 v[64:67], v[224:227], a[92:95], v[64:67]\n"
    "s_and_b32 s89, s34, 31\n"
    "v_accvgpr_write_b32 a205, 0\n"
    "v_mfma_f32_16x16x32_bf16 v[68:71], v[228:231], a[92:95], v[68:71]\n"
    "s_lshl2_add_u32 s89, s89, s93\n"
    "v_accvgpr_write_b32 a206, 0\n"
    "s_waitcnt vmcnt(10)\n"
    "v_mfma_f32_16x16x32_bf16 v[32:35], v[120:123], a[0:3], 0\n"
    "buffer_load_dwordx4 v[204:207], v250, s[8:11], s47 offen offset:3072 sc1\n"
    "s_bitset0_b32 s61, 14\n"
    "v_mfma_f32_16x16x32_bf16 v[36:39], v[124:127], a[0:3], 0\n"
    "v_cvt_pk_bf16_f32 v24, v24, v25\n"
    "v_accvgpr_write_b32 a207, 0\n"
    "v_mfma_f32_16x16x32_bf16 v[56:59], v[120:123], a[32:35], 0\n"
    "v_cvt_pk_bf16_f32 v25, v26, v27\n"
    "v_accvgpr_write_b32 a208, 0\n"
    "v_mfma_f32_16x16x32_bf16 v[60:63], v[124:127], a[32:35], 0\n"
    "s_add_u32 s47, s52, s61\n"
    "buffer_load_dwordx4 v[216:219], v250, s[8:11], s47 offen offset:0 sc1\n"
    "v_mfma_f32_16x16x32_bf16 v[72:75], v[120:123], a[64:67], 0\n"
    "v_cvt_pk_bf16_f32 v26, v28, v29\n"
    "v_cvt_pk_bf16_f32 v27, v30, v31\n"
    "v_mfma_f32_16x16x32_bf16 v[76:79], v[124:127], a[64:67], 0\n"
    "buffer_store_dwordx4 v[24:27], v98, s[8:11], s35 offen offset:0 sc1\n"
    "v_accvgpr_write_b32 a209, 0\n"
    "s_waitcnt vmcnt(11)\n"
    "v_mfma_f32_16x16x32_bf16 v[32:35], v[136:139], a[4:7], v[32:35]\n"
    "buffer_load_dwordx4 v[220:223], v250, s[8:11], s47 offen offset:1024 sc1\n"
    "s_add_u32 s52, s52, 0x4000\n"
    "v_mfma_f32_16x16x32_bf16 v[36:39], v[140:143], a[4:7], v[36:39]\n"
    "v_cvt_pk_bf16_f32 v48, v48, v49\n"
    "v_accvgpr_write_b32 a210, 0\n"
    "v_mfma_f32_16x16x32_bf16 v[56:59], v[136:139], a[36:39], v[56:59]\n"
    "v_cvt_pk_bf16_f32 v49, v50, v51\n"
    "v_cvt_pk_bf16_f32 v50, v52, v53\n"
    "v_mfma_f32_16x16x32_bf16 v[60:63], v[140:143], a[36:39], v[60:63]\n"
    "buffer_load_dwordx4 v[232:235], v250, s[8:11], s47 offen offset:2048 sc1\n"
    "s_add_u32 s61, s61, 0x1000\n"
    "s_waitcnt vmcnt(11)\n"
    "v_cmp_ne_u32_e32 vcc, s39, v80\n"
    "v_mfma_f32_16x16x32_bf16 v[72:75], v[136:139], a[68:71], v[72:75]\n"
    "v_cvt_pk_bf16_f32 v51, v54, v55\n"
    "buffer_store_dwordx4 v[48:51], v98, s[8:11], s54 offen offset:0 sc1\n"
    "v_mfma_f32_16x16x32_bf16 v[76:79], v[140:143], a[68:71], v[76:79]\n"
    "v_cvt_pk_bf16_f32 v64, v64, v65\n"
    "v_cvt_pk_bf16_f32 v65, v66, v67\n"
    "s_waitcnt vmcnt(11)\n"
    "v_mfma_f32_16x16x32_bf16 v[32:35], v[152:155], a[8:11], v[32:35]\n"
    "buffer_load_dwordx4 v[236:239], v250, s[8:11], s47 offen offset:3072 sc1\n"
    "s_bitset0_b32 s61, 14\n"
    "v_mfma_f32_16x16x32_bf16 v[36:39], v[156:159], a[8:11], v[36:39]\n"
    "s_cbranch_vccnz .Lat_h1poll\n"
    ".Lat_h1poll_ret:\n"
    "s_mov_b32 m0, s56\n"
    "v_mfma_f32_16x16x32_bf16 v[56:59], v[152:155], a[40:43], v[56:59]\n"
    "buffer_load_dwordx4 v104, s[8:11], s57 offen offset:0 sc1 lds\n"
    "v_cvt_pk_bf16_f32 v66, v68, v69\n"
    "v_mfma_f32_16x16x32_bf16 v[60:63], v[156:159], a[40:43], v[60:63]\n"
    "v_cvt_pk_bf16_f32 v67, v70, v71\n"
    "buffer_store_dwordx4 v[64:67], v98, s[8:11], s60 offen offset:0 sc1\n"
    "v_mfma_f32_16x16x32_bf16 v[72:75], v[152:155], a[72:75], v[72:75]\n"
    "s_lshl_b32 s89, s89, 10\n"
    "v_accvgpr_write_b32 a211, 0\n"
    "v_mfma_f32_16x16x32_bf16 v[76:79], v[156:159], a[72:75], v[76:79]\n"
    "s_add_u32 s83, s83, s89\n"
    "v_accvgpr_write_b32 a212, 0\n"
    "s_waitcnt vmcnt(12)\n"
    "v_mfma_f32_16x16x32_bf16 v[32:35], v[168:171], a[12:15], v[32:35]\n"
    "buffer_load_dwordx4 v105, s[8:11], s57 offen offset:1024 sc1 lds\n"
    "v_accvgpr_write_b32 a213, 0\n"
    "v_mfma_f32_16x16x32_bf16 v[36:39], v[172:175], a[12:15], v[36:39]\n"
    "s_add_u32 s83, s83, 42135552\n"
    "v_accvgpr_write_b32 a214, 0\n"
    "v_mfma_f32_16x16x32_bf16 v[56:59], v[168:171], a[44:47], v[56:59]\n"
    "ds_write_b128 v99, v[24:27] offset:0\n"
    "v_accvgpr_write_b32 a215, 0\n"
    "v_mfma_f32_16x16x32_bf16 v[60:63], v[172:175], a[44:47], v[60:63]\n"
    "s_add_u32 m0, s43, 0\n"
    "v_accvgpr_write_b32 a216, 0\n"
    "v_mfma_f32_16x16x32_bf16 v[72:75], v[168:171], a[76:79], v[72:75]\n"
    "buffer_load_dwordx4 v104, s[8:11], s40 offen offset:0 sc1 lds\n"
    "v_accvgpr_write_b32 a217, 0\n"
    "v_mfma_f32_16x16x32_bf16 v[76:79], v[172:175], a[76:79], v[76:79]\n"
    "v_accvgpr_write_b32 a218, 0\n"
    "s_waitcnt vmcnt(12)\n"
    "v_mfma_f32_16x16x32_bf16 v[32:35], v[184:187], a[16:19], v[32:35]\n"
    "s_lshl_b32 s30, s93, 10\n"
    "v_mfma_f32_16x16x32_bf16 v[36:39], v[188:191], a[16:19], v[36:39]\n"
    "v_accvgpr_write_b32 a219, 0\n"
    "v_mfma_f32_16x16x32_bf16 v[56:59], v[184:187], a[48:51], v[56:59]\n"
    "buffer_load_dwordx4 v105, s[8:11], s40 offen offset:1024 sc1 lds\n"
    "v_mfma_f32_16x16x32_bf16 v[60:63], v[188:191], a[48:51], v[60:63]\n"
    "v_accvgpr_write_b32 a220, 0\n"
    "v_mfma_f32_16x16x32_bf16 v[72:75], v[184:187], a[80:83], v[72:75]\n"
    "s_add_u32 s30, s30, 20480\n"
    "v_mfma_f32_16x16x32_bf16 v[76:79], v[188:191], a[80:83], v[76:79]\n"
    "ds_write_b128 v99, v[48:51] offset:8192\n"
    "v_accvgpr_write_b32 a221, 0\n"
    "s_waitcnt vmcnt(11)\n"
    "v_mfma_f32_16x16x32_bf16 v[32:35], v[200:203], a[20:23], v[32:35]\n"
    "buffer_load_dwordx4 v106, s[8:11], s40 offen offset:2048 sc1 lds\n"
    "v_mfma_f32_16x16x32_bf16 v[36:39], v[204:207], a[20:23], v[36:39]\n"
    "v_accvgpr_write_b32 a222, 0\n"
    "v_mfma_f32_16x16x32_bf16 v[56:59], v[200:203], a[52:55], v[56:59]\n"
    "v_accvgpr_write_b32 a223, 0\n"
    "v_mfma_f32_16x16x32_bf16 v[60:63], v[204:207], a[52:55], v[60:63]\n"
    "s_bitcmp0_b32 s92, 0\n"
    "s_cselect_b32 s85, 1, 0\n"
    "s_cselect_b32 s80, 5, 0\n"
    "v_mfma_f32_16x16x32_bf16 v[72:75], v[200:203], a[84:87], v[72:75]\n"
    "buffer_load_dwordx4 v107, s[8:11], s40 offen offset:3072 sc1 lds\n"
    "v_mfma_f32_16x16x32_bf16 v[76:79], v[204:207], a[84:87], v[76:79]\n"
    "v_accvgpr_write_b32 a224, 0\n"
    "s_waitcnt vmcnt(10)\n"
    "v_mfma_f32_16x16x32_bf16 v[32:35], v[216:219], a[24:27], v[32:35]\n"
    "s_bitcmp0_b32 s92, 0\n"
    "s_cselect_b32 s82, s82, 2.0\n"
    "s_cselect_b32 s83, s83, 2.0\n"
    "v_mfma_f32_16x16x32_bf16 v[36:39], v[220:223], a[24:27], v[36:39]\n"
    "s_add_u32 s40, s40, 0x8000\n"
    "v_mfma_f32_16x16x32_bf16 v[56:59], v[216:219], a[56:59], v[56:59]\n"
    "ds_write_b128 v99, v[64:67] offset:49152\n"
    "v_mfma_f32_16x16x32_bf16 v[60:63], v[220:223], a[56:59], v[60:63]\n"
    "v_accvgpr_write_b32 a225, 0\n"
    "v_mfma_f32_16x16x32_bf16 v[72:75], v[216:219], a[88:91], v[72:75]\n"
    "v_accvgpr_write_b32 a226, 0\n"
    "v_mfma_f32_16x16x32_bf16 v[76:79], v[220:223], a[88:91], v[76:79]\n"
    "s_mov_b32 s84, 2.0\n"
    "s_waitcnt vmcnt(7)\n"
    "v_mfma_f32_16x16x32_bf16 v[32:35], v[232:235], a[28:31], v[32:35]\n"
    "v_accvgpr_write_b32 a227, 0\n"
    "v_mfma_f32_16x16x32_bf16 v[36:39], v[236:239], a[28:31], v[36:39]\n"
    "v_accvgpr_write_b32 a228, 0\n"
    "v_mfma_f32_16x16x32_bf16 v[56:59], v[232:235], a[60:63], v[56:59]\n"
    "v_accvgpr_write_b32 a229, 0\n"
    "v_mfma_f32_16x16x32_bf16 v[60:63], v[236:239], a[60:63], v[60:63]\n"
    "v_accvgpr_write_b32 a230, 0\n"
    "v_mfma_f32_16x16x32_bf16 v[72:75], v[232:235], a[92:95], v[72:75]\n"
    "s_movk_i32 s90, 512\n"
    "v_mfma_f32_16x16x32_bf16 v[76:79], v[236:239], a[92:95], v[76:79]\n"
    "v_accvgpr_write_b32 a231, 0\n"
    "v_cvt_pk_bf16_f32 v32, v32, v33\n"
    "v_cvt_pk_bf16_f32 v33, v34, v35\n"
    "v_cvt_pk_bf16_f32 v34, v36, v37\n"
    "v_cvt_pk_bf16_f32 v35, v38, v39\n"
    "ds_write_b128 v99, v[32:35] offset:1024\n"
    "buffer_store_dwordx4 v[32:35], v98, s[8:11], s35 offen offset:1024 sc1\n"
    "v_cvt_pk_bf16_f32 v56, v56, v57\n"
    "v_cvt_pk_bf16_f32 v57, v58, v59\n"
    "v_cvt_pk_bf16_f32 v58, v60, v61\n"
    "v_cvt_pk_bf16_f32 v59, v62, v63\n"
    "ds_write_b128 v99, v[56:59] offset:9216\n"
    "buffer_store_dwordx4 v[56:59], v98, s[8:11], s54 offen offset:1024 sc1\n"
    "v_cvt_pk_bf16_f32 v72, v72, v73\n"
    "v_cvt_pk_bf16_f32 v73, v74, v75\n"
    "v_cvt_pk_bf16_f32 v74, v76, v77\n"
    "v_cvt_pk_bf16_f32 v75, v78, v79\n"
    "ds_write_b128 v99, v[72:75] offset:50176\n"
    "buffer_store_dwordx4 v[72:75], v98, s[8:11], s60 offen offset:1024 sc1\n"
    "v_mov_b64_e32 v[8:9], 0\n"
    "v_mov_b64_e32 v[10:11], 0\n"
    "s_add_u32 m0, s43, 4096\n"
    "v_mov_b64_e32 v[12:13], 0\n"
    "v_mov_b64_e32 v[14:15], 0\n"
    "buffer_load_dwordx4 v104, s[8:11], s41 offen offset:0 sc1 lds\n"
    "v_mov_b64_e32 v[16:17], 0\n"
    "v_mov_b64_e32 v[18:19], 0\n"
    "buffer_load_dwordx4 v105, s[8:11], s41 offen offset:1024 sc1 lds\n"
    "v_mov_b64_e32 v[28:29], 0\n"
    "v_mov_b64_e32 v[30:31], 0\n"
    "buffer_load_dwordx4 v106, s[8:11], s41 offen offset:2048 sc1 lds\n"
    "v_mov_b64_e32 v[32:33], 0\n"
    "v_mov_b64_e32 v[34:35], 0\n"
    "buffer_load_dwordx4 v107, s[8:11], s41 offen offset:3072 sc1 lds\n"
    "v_mov_b64_e32 v[36:37], 0\n"
    "v_mov_b64_e32 v[38:39], 0\n"
    "s_add_u32 s41, s41, 0x8000\n"
    "v_mov_b64_e32 v[48:49], 0\n"
    "v_mov_b64_e32 v[50:51], 0\n"
    "v_mov_b64_e32 v[52:53], 0\n"
    "v_mov_b64_e32 v[54:55], 0\n"
    "v_mov_b64_e32 v[56:57], 0\n"
    "v_mov_b64_e32 v[58:59], 0\n"
    "v_mov_b64_e32 v[68:69], 0\n"
    "v_mov_b64_e32 v[70:71], 0\n"
    "v_mov_b64_e32 v[72:73], 0\n"
    "v_mov_b64_e32 v[74:75], 0\n"
    "v_mov_b64_e32 v[76:77], 0\n"
    "v_mov_b64_e32 v[78:79], 0\n"
    "s_waitcnt vmcnt(4)\n"
    "buffer_store_dword v84, v86, s[8:11], s58 offen offset:0 sc1\n"
    "v_mov_b64_e32 v[88:89], 0\n"
    "v_mov_b64_e32 v[90:91], 0\n"
    "s_add_u32 m0, s43, 8192\n"
    "v_mov_b64_e32 v[92:93], 0\n"
    "v_mov_b64_e32 v[94:95], 0\n"
    "buffer_load_dwordx4 v104, s[8:11], s40 offen offset:0 sc1 lds\n"
    "v_mov_b64_e32 v[96:97], 0\n"
    "v_mov_b64_e32 v[98:99], 0\n"
    "buffer_load_dwordx4 v105, s[8:11], s40 offen offset:1024 sc1 lds\n"
    "v_mov_b64_e32 v[108:109], 0\n"
    "v_mov_b64_e32 v[110:111], 0\n"
    "buffer_load_dwordx4 v106, s[8:11], s40 offen offset:2048 sc1 lds\n"
    "v_mov_b64_e32 v[112:113], 0\n"
    "v_mov_b64_e32 v[114:115], 0\n"
    "buffer_load_dwordx4 v107, s[8:11], s40 offen offset:3072 sc1 lds\n"
    "v_mov_b64_e32 v[116:117], 0\n"
    "v_mov_b64_e32 v[118:119], 0\n"
    "s_add_u32 s40, s40, 0x8000\n"
    "v_mov_b64_e32 v[128:129], 0\n"
    "v_mov_b64_e32 v[130:131], 0\n"
    "s_add_u32 m0, s43, 12288\n"
    "v_mov_b64_e32 v[132:133], 0\n"
    "v_mov_b64_e32 v[134:135], 0\n"
    "buffer_load_dwordx4 v104, s[8:11], s41 offen offset:0 sc1 lds\n"
    "v_mov_b64_e32 v[136:137], 0\n"
    "v_mov_b64_e32 v[138:139], 0\n"
    "buffer_load_dwordx4 v105, s[8:11], s41 offen offset:1024 sc1 lds\n"
    "buffer_load_dwordx4 v106, s[8:11], s41 offen offset:2048 sc1 lds\n"
    "buffer_load_dwordx4 v107, s[8:11], s41 offen offset:3072 sc1 lds\n"
    "s_add_u32 s41, s41, 0x8000\n"
    ".Lat_p2:\n"
    "v_mov_b32_e32 v220, 0xc2980000\n"
    "v_mov_b32_e32 v221, 0xc2980000\n"
    "v_mov_b64_e32 v[222:223], v[220:221]\n"
    "v_mov_b64_e32 v[224:225], v[220:221]\n"
    "v_mov_b64_e32 v[226:227], v[220:221]\n"
    "v_mov_b64_e32 v[228:229], v[220:221]\n"
    "v_mov_b64_e32 v[230:231], v[220:221]\n"
    "v_mov_b64_e32 v[232:233], v[220:221]\n"
    "v_mov_b64_e32 v[234:235], v[220:221]\n"
    "v_mov_b64_e32 v[236:237], v[220:221]\n"
    "v_mov_b64_e32 v[238:239], v[220:221]\n"
    "s_andn2_b32 s47, s58, 0x600\n"
    "s_mov_b32 s44, 9\n"
    "s_waitcnt vmcnt(9)\n"
    "s_waitcnt lgkmcnt(0)\n"
    "s_barrier\n"
    "s_cmp_lg_u32 s49, 0\n"
    "s_cbranch_scc1 .Lat_maxpass\n"
    "ds_read_b128 a[0:3], v85 offset:0\n"
    "ds_read_b128 a[4:7], v85 offset:1024\n"
    "ds_read_b128 a[8:11], v85 offset:2048\n"
    "ds_read_b128 a[12:15], v85 offset:3072\n"
    "ds_read_b128 a[16:19], v85 offset:4096\n"
    "ds_read_b128 a[20:23], v85 offset:5120\n"
    "ds_read_b128 a[24:27], v85 offset:6144\n"
    "ds_read_b128 a[28:31], v85 offset:7168\n"
    "ds_read_b128 a[32:35], v255 offset:0\n"
    "ds_read_b128 a[36:39], v255 offset:1024\n"
    "ds_read_b128 a[40:43], v255 offset:2048\n"
    "ds_read_b128 a[44:47], v255 offset:3072\n"
    "ds_read_b128 a[48:51], v255 offset:4096\n"
    "ds_read_b128 a[52:55], v255 offset:5120\n"
    "ds_read_b128 a[56:59], v255 offset:6144\n"
    "ds_read_b128 a[60:63], v255 offset:7168\n"
    ".Lat_maxpass_ret:\n"
    "ds_read_b128 a[160:163], v46 offset:0\n"
    "ds_read_b128 a[164:167], v47 offset:0\n"
    "ds_read_b128 a[168:171], v46 offset:2048\n"
    "ds_read_b128 a[172:175], v47 offset:2048\n"
    "ds_read_b128 a[176:179], v46 offset:4096\n"
    "ds_read_b128 a[180:183], v47 offset:4096\n"
    "ds_read_b128 a[184:187], v46 offset:6144\n"
    "ds_read_b128 a[188:191], v47 offset:6144\n"
    "s_mul_i32 s48, s33, 81920\n"
    "s_add_u32 s48, s48, 11345920\n"
    "s_lshl_b32 s46, s93, 11\n"
    "s_add_u32 s48, s48, s46\n"
    "s_add_u32 s46, s46, 49152\n"
    "buffer_load_dword v254, v253, s[8:11], s47 offen offset:0 sc1\n"
    "s_waitcnt lgkmcnt(4)\n"
    "v_mfma_f32_16x16x32_bf16 v[212:215], a[160:163], a[0:3], v[220:223]\n"
    "s_and_b32 s45, s32, 1\n"
    "ds_read_b64_tr_b16 v[140:141], v4 offset:0\n"
    "v_mfma_f32_16x16x32_bf16 v[216:219], a[160:163], a[32:35], v[224:227]\n"
    "s_xor_b32 s45, s45, 9\n"
    "ds_read_b64_tr_b16 v[142:143], v5 offset:0\n"
    "v_mfma_f32_16x16x32_bf16 v[212:215], a[164:167], a[4:7], v[212:215]\n"
    "s_lshl_b32 s45, s45, 13\n"
    "ds_read_b64_tr_b16 v[144:145], v6 offset:0\n"
    "v_mfma_f32_16x16x32_bf16 v[216:219], a[164:167], a[36:39], v[216:219]\n"
    "s_add_u32 s45, s45, s48\n"
    "ds_read_b64_tr_b16 v[146:147], v7 offset:0\n"
    "v_mfma_f32_16x16x32_bf16 v[212:215], a[168:171], a[8:11], v[212:215]\n"
    "s_add_u32 s54, s32, 1\n"
    "ds_read_b64_tr_b16 v[148:149], v4 offset:2048\n"
    "v_mfma_f32_16x16x32_bf16 v[216:219], a[168:171], a[40:43], v[216:219]\n"
    "s_and_b32 s54, s54, 3\n"
    "ds_read_b64_tr_b16 v[150:151], v5 offset:2048\n"
    "v_mfma_f32_16x16x32_bf16 v[212:215], a[172:175], a[12:15], v[212:215]\n"
    "s_lshl_b32 s54, s54, 13\n"
    "ds_read_b64_tr_b16 v[152:153], v6 offset:2048\n"
    "v_mfma_f32_16x16x32_bf16 v[216:219], a[172:175], a[44:47], v[216:219]\n"
    "s_add_u32 s54, s54, s48\n"
    "ds_read_b64_tr_b16 v[154:155], v7 offset:2048\n"
    "s_waitcnt lgkmcnt(8)\n"
    "v_mfma_f32_16x16x32_bf16 v[212:215], a[176:179], a[16:19], v[212:215]\n"
    "s_add_u32 s55, s32, 2\n"
    "ds_read_b128 v[180:183], v240 offset:2048\n"
    "v_mfma_f32_16x16x32_bf16 v[216:219], a[176:179], a[48:51], v[216:219]\n"
    "s_and_b32 s55, s55, 3\n"
    "ds_read_b128 v[184:187], v241 offset:2048\n"
    "v_mfma_f32_16x16x32_bf16 v[212:215], a[180:183], a[20:23], v[212:215]\n"
    "s_lshl_b32 s55, s55, 13\n"
    "ds_read_b128 v[188:191], v240 offset:6144\n"
    "v_mfma_f32_16x16x32_bf16 v[216:219], a[180:183], a[52:55], v[216:219]\n"
    "s_add_u32 s55, s55, s48\n"
    "ds_read_b128 v[192:195], v241 offset:6144\n"
    "v_mfma_f32_16x16x32_bf16 v[212:215], a[184:187], a[24:27], v[212:215]\n"
    "s_add_u32 s60, s32, 3\n"
    "ds_read_b128 v[196:199], v242 offset:2048\n"
    "v_mfma_f32_16x16x32_bf16 v[216:219], a[184:187], a[56:59], v[216:219]\n"
    "s_and_b32 s60, s60, 3\n"
    "ds_read_b128 v[200:203], v243 offset:2048\n"
    "v_mfma_f32_16x16x32_bf16 v[212:215], a[188:191], a[28:31], v[212:215]\n"
    "s_lshl_b32 s60, s60, 13\n"
    "ds_read_b128 v[204:207], v242 offset:6144\n"
    "v_mfma_f32_16x16x32_bf16 v[216:219], a[188:191], a[60:63], v[216:219]\n"
    "s_add_u32 s60, s60, s48\n"
    "ds_read_b128 v[208:211], v243 offset:6144\n"
    "s_lshr_b32 s61, s32, 1\n"
    "s_or_b32 s61, s61, s50\n"
    "s_sub_u32 s61, s61, 1\n"
    "s_add_u32 m0, s46, 65536\n"
    "s_waitcnt vmcnt(0)\n"
    "v_cmp_ne_u32_e32 vcc, s39, v254\n"
    "ds_read_b128 a[160:163], v240 offset:0\n"
    "ds_read_b128 a[164:167], v241 offset:0\n"
    "ds_read_b128 a[168:171], v240 offset:4096\n"
    "ds_read_b128 a[172:175], v241 offset:4096\n"
    "ds_read_b128 a[176:179], v242 offset:0\n"
    "s_cbranch_vccnz .Lat_qpoll\n"
    ".Lat_qpoll_ret:\n"
    "buffer_load_dwordx4 v250, s[8:11], s45 offen offset:0 sc1 lds\n"
    "buffer_load_dwordx4 v250, s[8:11], s45 offen offset:1024 sc1 lds\n"
    "s_add_u32 m0, s46, 8192\n"
    "ds_read_b128 a[180:183], v243 offset:0\n"
    "s_nop 0\n"
    "ds_read_b128 a[184:187], v242 offset:4096\n"
    "buffer_load_dwordx4 v250, s[8:11], s54 offen offset:0 sc1 lds\n"
    "ds_read_b128 a[188:191], v243 offset:4096\n"
    "buffer_load_dwordx4 v250, s[8:11], s54 offen offset:1024 sc1 lds\n"
    "v_exp_f32_e32 v212, v212\n"
    "s_add_u32 m0, s46, 73728\n"
    "v_exp_f32_e32 v213, v213\n"
    "s_add_u32 s54, s54, 0x8000\n"
    "v_exp_f32_e32 v214, v214\n"
    "buffer_load_dwordx4 v250, s[8:11], s54 offen offset:0 sc1 lds\n"
    "v_exp_f32_e32 v215, v215\n"
    "buffer_load_dwordx4 v250, s[8:11], s54 offen offset:1024 sc1 lds\n"
    "v_exp_f32_e32 v216, v216\n"
    "s_add_u32 m0, s46, 32768\n"
    "v_exp_f32_e32 v217, v217\n"
    "s_nop 0\n"
    "v_exp_f32_e32 v218, v218\n"
    "buffer_load_dwordx4 v250, s[8:11], s55 offen offset:0 sc1 lds\n"
    "v_exp_f32_e32 v219, v219\n"
    "buffer_load_dwordx4 v250, s[8:11], s55 offen offset:1024 sc1 lds\n"
    "v_cvt_pk_bf16_f32 v212, v212, v213\n"
    "v_cvt_pk_bf16_f32 v213, v214, v215\n"
    "v_cvt_pk_bf16_f32 v216, v216, v217\n"
    "v_cvt_pk_bf16_f32 v217, v218, v219\n"
    "v_and_b32_e32 v216, s61, v216\n"
    "v_and_b32_e32 v217, s61, v217\n"
    "s_waitcnt lgkmcnt(15)\n"
    "v_mfma_f32_16x16x16_bf16 a[236:239], v[140:141], v[212:213], 0\n"
    "s_add_u32 m0, s46, 98304\n"
    "v_mfma_f32_16x16x16_bf16 v[0:3], v[142:143], v[212:213], 0\n"
    "s_add_u32 s55, s55, 0x8000\n"
    "v_mfma_f32_16x16x16_bf16 v[20:23], v[144:145], v[212:213], 0\n"
    "buffer_load_dwordx4 v250, s[8:11], s55 offen offset:0 sc1 lds\n"
    "v_mfma_f32_16x16x16_bf16 v[40:43], v[146:147], v[212:213], 0\n"
    "buffer_load_dwordx4 v250, s[8:11], s55 offen offset:1024 sc1 lds\n"
    "v_mfma_f32_16x16x16_bf16 v[60:63], v[148:149], v[212:213], 0\n"
    "s_add_u32 m0, s46, 40960\n"
    "v_mfma_f32_16x16x16_bf16 v[80:83], v[150:151], v[212:213], 0\n"
    "buffer_load_dwordx4 v250, s[8:11], s60 offen offset:0 sc1 lds\n"
    "v_mfma_f32_16x16x16_bf16 v[100:103], v[152:153], v[212:213], 0\n"
    "buffer_load_dwordx4 v250, s[8:11], s60 offen offset:1024 sc1 lds\n"
    "v_mfma_f32_16x16x16_bf16 v[120:123], v[154:155], v[212:213], 0\n"
    "s_add_u32 m0, s46, 106496\n"
    "v_mfma_f32_16x16x16_bf16 a[240:243], v[140:141], v[216:217], 0\n"
    "s_add_u32 s60, s60, 0x8000\n"
    "v_mfma_f32_16x16x16_bf16 v[4:7], v[142:143], v[216:217], 0\n"
    "buffer_load_dwordx4 v250, s[8:11], s60 offen offset:0 sc1 lds\n"
    "v_mfma_f32_16x16x16_bf16 v[24:27], v[144:145], v[216:217], 0\n"
    "buffer_load_dwordx4 v250, s[8:11], s60 offen offset:1024 sc1 lds\n"
    "v_mfma_f32_16x16x16_bf16 v[44:47], v[146:147], v[216:217], 0\n"
    "v_mfma_f32_16x16x16_bf16 v[64:67], v[148:149], v[216:217], 0\n"
    "buffer_load_dword v253, v250, s[8:11], s59 offen offset:0 sc1\n"
    "v_mfma_f32_16x16x16_bf16 v[84:87], v[150:151], v[216:217], 0\n"
    "v_mfma_f32_16x16x16_bf16 v[104:107], v[152:153], v[216:217], 0\n"
    "v_mfma_f32_16x16x16_bf16 v[124:127], v[154:155], v[216:217], 0\n"
    "s_waitcnt lgkmcnt(0)\n"
    "buffer_load_dword v254, v252, s[8:11], s59 offen offset:0 sc1\n"
    "v_mfma_f32_16x16x32_bf16 v[140:143], a[160:163], a[0:3], v[220:223]\n"
    "v_mfma_f32_16x16x32_bf16 v[140:143], a[164:167], a[4:7], v[140:143]\n"
    "v_mfma_f32_16x16x32_bf16 v[140:143], a[168:171], a[8:11], v[140:143]\n"
    "v_mfma_f32_16x16x32_bf16 v[140:143], a[172:175], a[12:15], v[140:143]\n"
    "v_mfma_f32_16x16x32_bf16 v[140:143], a[176:179], a[16:19], v[140:143]\n"
    "v_mfma_f32_16x16x32_bf16 v[140:143], a[180:183], a[20:23], v[140:143]\n"
    "v_mfma_f32_16x16x32_bf16 v[140:143], a[184:187], a[24:27], v[140:143]\n"
    "v_mfma_f32_16x16x32_bf16 v[140:143], a[188:191], a[28:31], v[140:143]\n"
    "v_mfma_f32_16x16x32_bf16 v[144:147], v[180:183], a[0:3], v[220:223]\n"
    "v_mfma_f32_16x16x32_bf16 v[144:147], v[184:187], a[4:7], v[144:147]\n"
    "s_waitcnt vmcnt(10)\n"
    "s_barrier\n"
    "v_mfma_f32_16x16x32_bf16 v[144:147], v[188:191], a[8:11], v[144:147]\n"
    "s_mov_b32 m0, s42\n"
    "ds_read_b128 a[32:35], v255 offset:0\n"
    "v_exp_f32_e32 v140, v140\n"
    "v_mfma_f32_16x16x32_bf16 v[144:147], v[192:195], a[12:15], v[144:147]\n"
    "ds_read_b128 a[36:39], v255 offset:1024\n"
    "v_exp_f32_e32 v141, v141\n"
    "v_mfma_f32_16x16x32_bf16 v[144:147], v[196:199], a[16:19], v[144:147]\n"
    "ds_read_b128 a[40:43], v255 offset:2048\n"
    "ds_read_b128 a[44:47], v255 offset:3072\n"
    "v_mfma_f32_16x16x32_bf16 v[144:147], v[200:203], a[20:23], v[144:147]\n"
    "ds_read_b128 a[48:51], v255 offset:4096\n"
    "ds_read_b128 a[52:55], v255 offset:5120\n"
    "v_mfma_f32_16x16x32_bf16 v[144:147], v[204:207], a[24:27], v[144:147]\n"
    "ds_read_b128 a[56:59], v255 offset:6144\n"
    "ds_read_b128 a[60:63], v255 offset:7168\n"
    "v_mfma_f32_16x16x32_bf16 v[144:147], v[208:211], a[28:31], v[144:147]\n"
    "s_waitcnt lgkmcnt(4)\n"
    "v_exp_f32_e32 v142, v142\n"
    "v_mfma_f32_16x16x32_bf16 v[148:151], a[160:163], a[32:35], v[224:227]\n"
    "ds_read_b128 a[64:67], v255 offset:8192\n"
    "v_exp_f32_e32 v143, v143\n"
    "v_mfma_f32_16x16x32_bf16 v[148:151], a[164:167], a[36:39], v[148:151]\n"
    "ds_read_b128 a[68:71], v255 offset:9216\n"
    "v_cvt_pk_bf16_f32 v140, v140, v141\n"
    "v_mfma_f32_16x16x32_bf16 v[148:151], a[168:171], a[40:43], v[148:151]\n"
    "ds_read_b128 a[72:75], v255 offset:10240\n"
    "v_cvt_pk_bf16_f32 v141, v142, v143\n"
    "v_mfma_f32_16x16x32_bf16 v[148:151], a[172:175], a[44:47], v[148:151]\n"
    "s_waitcnt lgkmcnt(3)\n"
    "v_exp_f32_e32 v144, v144\n"
    "v_mfma_f32_16x16x32_bf16 v[148:151], a[176:179], a[48:51], v[148:151]\n"
    "ds_read_b128 a[76:79], v255 offset:11264\n"
    "ds_write_addtid_b32 v140 offset:0\n"
    "v_mfma_f32_16x16x32_bf16 v[148:151], a[180:183], a[52:55], v[148:151]\n"
    "ds_read_b128 a[80:83], v255 offset:12288\n"
    "v_exp_f32_e32 v145, v145\n"
    "v_mfma_f32_16x16x32_bf16 v[148:151], a[184:187], a[56:59], v[148:151]\n"
    "v_exp_f32_e32 v146, v146\n"
    "v_mfma_f32_16x16x32_bf16 v[148:151], a[188:191], a[60:63], v[148:151]\n"
    "ds_read_b128 a[84:87], v255 offset:13312\n"
    "v_exp_f32_e32 v147, v147\n"
    "v_mfma_f32_16x16x32_bf16 v[152:155], v[180:183], a[32:35], v[224:227]\n"
    "ds_read_b128 a[88:91], v255 offset:14336\n"
    "v_cvt_pk_bf16_f32 v142, v144, v145\n"
    "v_mfma_f32_16x16x32_bf16 v[152:155], v[184:187], a[36:39], v[152:155]\n"
    "ds_read_b128 a[92:95], v255 offset:15360\n"
    "v_cvt_pk_bf16_f32 v143, v146, v147\n"
    "v_mfma_f32_16x16x32_bf16 v[152:155], v[188:191], a[40:43], v[152:155]\n"
    "v_exp_f32_e32 v148, v148\n"
    "v_mfma_f32_16x16x32_bf16 v[152:155], v[192:195], a[44:47], v[152:155]\n"
    "v_exp_f32_e32 v149, v149\n"
    "v_mfma_f32_16x16x32_bf16 v[152:155], v[196:199], a[48:51], v[152:155]\n"
    "v_exp_f32_e32 v150, v150\n"
    "v_mfma_f32_16x16x32_bf16 v[152:155], v[200:203], a[52:55], v[152:155]\n"
    "v_exp_f32_e32 v151, v151\n"
    "v_mfma_f32_16x16x32_bf16 v[152:155], v[204:207], a[56:59], v[152:155]\n"
    "s_waitcnt vmcnt(2)\n"
    "s_barrier\n"
    "v_mfma_f32_16x16x32_bf16 v[152:155], v[208:211], a[60:63], v[152:155]\n"
    "s_waitcnt lgkmcnt(5)\n"
    "ds_read_b128 a[96:99], v255 offset:32768\n"
    "ds_write_addtid_b32 v141 offset:256\n"
    "v_mfma_f32_16x16x32_bf16 v[156:159], a[160:163], a[64:67], v[228:231]\n"
    "ds_read_b128 a[100:103], v255 offset:33792\n"
    "ds_write_addtid_b32 v142 offset:512\n"
    "v_mfma_f32_16x16x32_bf16 v[156:159], a[164:167], a[68:71], v[156:159]\n"
    "ds_read_b128 a[104:107], v255 offset:34816\n"
    "v_mfma_f32_16x16x32_bf16 v[156:159], a[168:171], a[72:75], v[156:159]\n"
    "ds_read_b128 a[108:111], v255 offset:35840\n"
    "v_mfma_f32_16x16x32_bf16 v[156:159], a[172:175], a[76:79], v[156:159]\n"
    "s_waitcnt lgkmcnt(6)\n"
    "ds_read_b128 a[112:115], v255 offset:36864\n"
    "v_cvt_pk_bf16_f32 v148, v148, v149\n"
    "v_mfma_f32_16x16x32_bf16 v[156:159], a[176:179], a[80:83], v[156:159]\n"
    "ds_read_b128 a[116:119], v255 offset:37888\n"
    "v_cvt_pk_bf16_f32 v149, v150, v151\n"
    "v_mfma_f32_16x16x32_bf16 v[156:159], a[180:183], a[84:87], v[156:159]\n"
    "ds_read_b128 a[120:123], v255 offset:38912\n"
    "v_exp_f32_e32 v152, v152\n"
    "v_mfma_f32_16x16x32_bf16 v[156:159], a[184:187], a[88:91], v[156:159]\n"
    "ds_read_b128 a[124:127], v255 offset:39936\n"
    "v_exp_f32_e32 v153, v153\n"
    "v_mfma_f32_16x16x32_bf16 v[156:159], a[188:191], a[92:95], v[156:159]\n"
    "ds_read_b128 a[128:131], v255 offset:40960\n"
    "v_exp_f32_e32 v154, v154\n"
    "v_mfma_f32_16x16x32_bf16 v[160:163], v[180:183], a[64:67], v[228:231]\n"
    "ds_read_b128 a[132:135], v255 offset:41984\n"
    "v_exp_f32_e32 v155, v155\n"
    "v_mfma_f32_16x16x32_bf16 v[160:163], v[184:187], a[68:71], v[160:163]\n"
    "ds_read_b128 a[136:139], v255 offset:43008\n"
    "v_cvt_pk_bf16_f32 v150, v152, v153\n"
    "v_mfma_f32_16x16x32_bf16 v[160:163], v[188:191], a[72:75], v[160:163]\n"
    "ds_read_b128 a[140:143], v255 offset:44032\n"
    "v_cvt_pk_bf16_f32 v151, v154, v155\n"
    "v_mfma_f32_16x16x32_bf16 v[160:163], v[192:195], a[76:79], v[160:163]\n"
    "ds_read_b128 a[144:147], v255 offset:45056\n"
    "ds_read_b128 a[148:151], v255 offset:46080\n"
    "v_mfma_f32_16x16x32_bf16 v[160:163], v[196:199], a[80:83], v[160:163]\n"
    "ds_read_b128 a[152:155], v255 offset:47104\n"
    "ds_read_b128 a[156:159], v255 offset:48128\n"
    "v_mfma_f32_16x16x32_bf16 v[160:163], v[200:203], a[84:87], v[160:163]\n"
    "s_waitcnt vmcnt(0)\n"
    "ds_write_addtid_b32 v148 offset:1024\n"
    "v_xor_b32_e32 v254, s39, v254\n"
    "v_xor_b32_e32 v253, s39, v253\n"
    "v_mfma_f32_16x16x32_bf16 v[160:163], v[204:207], a[88:91], v[160:163]\n"
    "ds_write_addtid_b32 v149 offset:1280\n"
    "v_or_b32_e32 v253, v253, v254\n"
    "v_cmp_ne_u32_e32 vcc, 0, v253\n"
    "v_mfma_f32_16x16x32_bf16 v[160:163], v[208:211], a[92:95], v[160:163]\n"
    "s_waitcnt lgkmcnt(14)\n"
    "v_exp_f32_e32 v156, v156\n"
    "v_mfma_f32_16x16x32_bf16 v[164:167], a[160:163], a[96:99], v[232:235]\n"
    "v_exp_f32_e32 v157, v157\n"
    "v_mfma_f32_16x16x32_bf16 v[164:167], a[164:167], a[100:103], v[164:167]\n"
    "v_exp_f32_e32 v158, v158\n"
    "v_mfma_f32_16x16x32_bf16 v[164:167], a[168:171], a[104:107], v[164:167]\n"
    "s_cbranch_vccnz .Lat_hpoll\n"
    ".Lat_hpoll_ret:\n"
    "s_waitcnt lgkmcnt(2)\n"
    "s_waitcnt vmcnt(17)\n"
    "s_barrier\n"
    "v_mfma_f32_16x16x32_bf16 v[164:167], a[172:175], a[108:111], v[164:167]\n"
    "v_exp_f32_e32 v159, v159\n"
    "v_mfma_f32_16x16x32_bf16 v[164:167], a[176:179], a[112:115], v[164:167]\n"
    "s_add_u32 m0, s43, 16384\n"
    "v_cvt_pk_bf16_f32 v156, v156, v157\n"
    "v_mfma_f32_16x16x32_bf16 v[164:167], a[180:183], a[116:119], v[164:167]\n"
    "buffer_load_dwordx4 v248, s[8:11], s40 offen offset:0 sc1 lds\n"
    "v_cvt_pk_bf16_f32 v157, v158, v159\n"
    "v_mfma_f32_16x16x32_bf16 v[164:167], a[184:187], a[120:123], v[164:167]\n"
    "v_exp_f32_e32 v160, v160\n"
    "v_mfma_f32_16x16x32_bf16 v[164:167], a[188:191], a[124:127], v[164:167]\n"
    "v_exp_f32_e32 v161, v161\n"
    "v_mfma_f32_16x16x32_bf16 v[168:171], v[180:183], a[96:99], v[232:235]\n"
    "buffer_load_dwordx4 v248, s[8:11], s40 offen offset:1024 sc1 lds\n"
    "v_mfma_f32_16x16x32_bf16 v[168:171], v[184:187], a[100:103], v[168:171]\n"
    "v_exp_f32_e32 v162, v162\n"
    "v_mfma_f32_16x16x32_bf16 v[168:171], v[188:191], a[104:107], v[168:171]\n"
    "v_exp_f32_e32 v163, v163\n"
    "v_mfma_f32_16x16x32_bf16 v[168:171], v[192:195], a[108:111], v[168:171]\n"
    "buffer_load_dwordx4 v248, s[8:11], s40 offen offset:2048 sc1 lds\n"
    "v_mfma_f32_16x16x32_bf16 v[168:171], v[196:199], a[112:115], v[168:171]\n"
    "v_cvt_pk_bf16_f32 v158, v160, v161\n"
    "v_cvt_pk_bf16_f32 v159, v162, v163\n"
    "v_mfma_f32_16x16x32_bf16 v[168:171], v[200:203], a[116:119], v[168:171]\n"
    "v_exp_f32_e32 v164, v164\n"
    "v_mfma_f32_16x16x32_bf16 v[168:171], v[204:207], a[120:123], v[168:171]\n"
    "buffer_load_dwordx4 v248, s[8:11], s40 offen offset:3072 sc1 lds\n"
    "v_mfma_f32_16x16x32_bf16 v[168:171], v[208:211], a[124:127], v[168:171]\n"
    "s_add_u32 s40, s40, 0x8000\n"
    "v_exp_f32_e32 v165, v165\n"
    "v_mfma_f32_16x16x32_bf16 v[172:175], a[160:163], a[128:131], v[236:239]\n"
    "s_add_u32 m0, s43, 20480\n"
    "v_exp_f32_e32 v166, v166\n"
    "v_mfma_f32_16x16x32_bf16 v[172:175], a[164:167], a[132:135], v[172:175]\n"
    "buffer_load_dwordx4 v248, s[8:11], s41 offen offset:0 sc1 lds\n"
    "ds_read_b128 a[160:163], v240 offset:8192\n"
    "v_mfma_f32_16x16x32_bf16 v[172:175], a[168:171], a[136:139], v[172:175]\n"
    "ds_read_b128 a[164:167], v241 offset:8192\n"
    "v_mfma_f32_16x16x32_bf16 v[172:175], a[172:175], a[140:143], v[172:175]\n"
    "ds_read_b128 a[168:171], v240 offset:12288\n"
    "v_mfma_f32_16x16x32_bf16 v[172:175], a[176:179], a[144:147], v[172:175]\n"
    "buffer_load_dwordx4 v248, s[8:11], s41 offen offset:1024 sc1 lds\n"
    "ds_read_b128 a[172:175], v241 offset:12288\n"
    "v_mfma_f32_16x16x32_bf16 v[172:175], a[180:183], a[148:151], v[172:175]\n"
    "ds_read_b128 a[176:179], v242 offset:8192\n"
    "v_exp_f32_e32 v167, v167\n"
    "v_mfma_f32_16x16x32_bf16 v[172:175], a[184:187], a[152:155], v[172:175]\n"
    "ds_read_b128 a[180:183], v243 offset:8192\n"
    "v_cvt_pk_bf16_f32 v164, v164, v165\n"
    "v_mfma_f32_16x16x32_bf16 v[172:175], a[188:191], a[156:159], v[172:175]\n"
    "buffer_load_dwordx4 v248, s[8:11], s41 offen offset:2048 sc1 lds\n"
    "v_cvt_pk_bf16_f32 v165, v166, v167\n"
    "v_mfma_f32_16x16x32_bf16 v[176:179], v[180:183], a[128:131], v[236:239]\n"
    "v_exp_f32_e32 v168, v168\n"
    "v_mfma_f32_16x16x32_bf16 v[176:179], v[184:187], a[132:135], v[176:179]\n"
    "v_exp_f32_e32 v169, v169\n"
    "v_mfma_f32_16x16x32_bf16 v[176:179], v[188:191], a[136:139], v[176:179]\n"
    "buffer_load_dwordx4 v248, s[8:11], s41 offen offset:3072 sc1 lds\n"
    "v_mfma_f32_16x16x32_bf16 v[176:179], v[192:195], a[140:143], v[176:179]\n"
    "s_mov_b32 m0, s42\n"
    "s_add_u32 s41, s41, 0x8000\n"
    "v_exp_f32_e32 v172, v172\n"
    "v_mfma_f32_16x16x32_bf16 v[176:179], v[196:199], a[144:147], v[176:179]\n"
    "ds_write_addtid_b32 v156 offset:2048\n"
    "v_exp_f32_e32 v173, v173\n"
    "v_mfma_f32_16x16x32_bf16 v[176:179], v[200:203], a[148:151], v[176:179]\n"
    "ds_write_addtid_b32 v157 offset:2304\n"
    "v_exp_f32_e32 v174, v174\n"
    "v_mfma_f32_16x16x32_bf16 v[176:179], v[204:207], a[152:155], v[176:179]\n"
    "ds_write_addtid_b32 v164 offset:3072\n"
    "v_exp_f32_e32 v175, v175\n"
    "v_mfma_f32_16x16x32_bf16 v[176:179], v[208:211], a[156:159], v[176:179]\n"
    "ds_write_addtid_b32 v165 offset:3328\n"
    "v_cvt_pk_bf16_f32 v172, v172, v173\n"
    "v_cvt_pk_bf16_f32 v173, v174, v175\n"
    "ds_write_addtid_b32 v172 offset:4096\n"
    "ds_write_addtid_b32 v173 offset:4352\n"
    "s_waitcnt lgkmcnt(10)\n"
    "s_branch .Lat_in\n"
    ".p2align 6\n"
    ".Lat_loop:\n"
    "v_mfma_f32_16x16x32_bf16 v[212:215], a[160:163], a[128:131], v[236:239]\n"
    "v_mfma_f32_16x16x32_bf16 v[212:215], a[164:167], a[132:135], v[212:215]\n"
    "v_exp_f32_e32 v170, v170\n"
    "v_mfma_f32_16x16x32_bf16 v[204:207], a[164:167], a[100:103], v[232:235]\n"
    "ds_write_addtid_b32 v143 offset:768\n"
    "v_mfma_f32_16x16x32_bf16 v[204:207], a[160:163], a[96:99], v[204:207]\n"
    "ds_read_b128 a[184:187], v242 offset:28672\n"
    "v_mfma_f32_16x16x32_bf16 v[196:199], a[160:163], a[64:67], v[228:231]\n"
    "v_exp_f32_e32 v171, v171\n"
    "v_mfma_f32_16x16x32_bf16 v[196:199], a[164:167], a[68:71], v[196:199]\n"
    "ds_write_addtid_b32 v150 offset:1536\n"
    "v_mfma_f32_16x16x32_bf16 v[188:191], a[164:167], a[36:39], v[224:227]\n"
    "v_cvt_pk_bf16_f32 v166, v168, v169\n"
    "v_mfma_f32_16x16x32_bf16 v[188:191], a[160:163], a[32:35], v[188:191]\n"
    "v_mfma_f32_16x16x32_bf16 v[180:183], a[160:163], a[0:3], v[220:223]\n"
    "ds_write_addtid_b32 v151 offset:1792\n"
    "v_mfma_f32_16x16x32_bf16 v[180:183], a[164:167], a[4:7], v[180:183]\n"
    "s_waitcnt lgkmcnt(10)\n"
    "v_exp_f32_e32 v176, v176\n"
    "v_mfma_f32_16x16x32_bf16 v[180:183], a[168:171], a[8:11], v[180:183]\n"
    "ds_read_b128 a[188:191], v243 offset:28672\n"
    "v_mfma_f32_16x16x32_bf16 v[180:183], a[172:175], a[12:15], v[180:183]\n"
    "v_exp_f32_e32 v177, v177\n"
    "v_mfma_f32_16x16x32_bf16 v[188:191], a[172:175], a[44:47], v[188:191]\n"
    "ds_write_addtid_b32 v158 offset:2560\n"
    "v_mfma_f32_16x16x32_bf16 v[188:191], a[168:171], a[40:43], v[188:191]\n"
    "ds_read_b128 a[160:163], v240 offset:26624\n"
    "v_mfma_f32_16x16x32_bf16 v[196:199], a[168:171], a[72:75], v[196:199]\n"
    "v_cvt_pk_bf16_f32 v167, v170, v171\n"
    "v_mfma_f32_16x16x32_bf16 v[196:199], a[172:175], a[76:79], v[196:199]\n"
    "ds_write_addtid_b32 v159 offset:2816\n"
    "v_mfma_f32_16x16x32_bf16 v[204:207], a[172:175], a[108:111], v[204:207]\n"
    "v_exp_f32_e32 v178, v178\n"
    "v_mfma_f32_16x16x32_bf16 v[204:207], a[168:171], a[104:107], v[204:207]\n"
    "ds_write_addtid_b32 v166 offset:3584\n"
    "v_mfma_f32_16x16x32_bf16 v[212:215], a[168:171], a[136:139], v[212:215]\n"
    "v_mfma_f32_16x16x32_bf16 v[212:215], a[172:175], a[140:143], v[212:215]\n"
    "s_waitcnt lgkmcnt(10)\n"
    "v_exp_f32_e32 v179, v179\n"
    "v_mfma_f32_16x16x32_bf16 v[212:215], a[176:179], a[144:147], v[212:215]\n"
    "ds_read_b128 a[164:167], v241 offset:26624\n"
    "v_mfma_f32_16x16x32_bf16 v[212:215], a[180:183], a[148:151], v[212:215]\n"
    "v_cvt_pk_bf16_f32 v174, v176, v177\n"
    "v_mfma_f32_16x16x32_bf16 v[204:207], a[180:183], a[116:119], v[204:207]\n"
    "ds_write_addtid_b32 v167 offset:3840\n"
    "v_mfma_f32_16x16x32_bf16 v[204:207], a[176:179], a[112:115], v[204:207]\n"
    "ds_read_b128 a[168:171], v240 offset:30720\n"
    "v_mfma_f32_16x16x32_bf16 v[196:199], a[176:179], a[80:83], v[196:199]\n"
    "v_cvt_pk_bf16_f32 v175, v178, v179\n"
    "v_mfma_f32_16x16x32_bf16 v[196:199], a[180:183], a[84:87], v[196:199]\n"
    "ds_write_addtid_b32 v174 offset:4608\n"
    "v_mfma_f32_16x16x32_bf16 v[188:191], a[180:183], a[52:55], v[188:191]\n"
    "ds_write_addtid_b32 v175 offset:4864\n"
    "v_mfma_f32_16x16x32_bf16 v[188:191], a[176:179], a[48:51], v[188:191]\n"
    "v_mfma_f32_16x16x32_bf16 v[180:183], a[176:179], a[16:19], v[180:183]\n"
    "ds_read_b128 a[172:175], v241 offset:30720\n"
    "v_mfma_f32_16x16x32_bf16 v[180:183], a[180:183], a[20:23], v[180:183]\n"
    "s_waitcnt lgkmcnt(10)\n"
    "s_waitcnt lgkmcnt(1)\n"
    "v_mfma_f32_16x16x32_bf16 v[180:183], a[184:187], a[24:27], v[180:183]\n"
    "s_barrier\n"
    "v_mfma_f32_16x16x32_bf16 v[180:183], a[188:191], a[28:31], v[180:183]\n"
    "s_add_u32 m0, s43, 8192\n"
    "v_mfma_f32_16x16x32_bf16 v[188:191], a[188:191], a[60:63], v[188:191]\n"
    "ds_read_b128 a[176:179], v242 offset:26624\n"
    "v_mfma_f32_16x16x32_bf16 v[188:191], a[184:187], a[56:59], v[188:191]\n"
    "buffer_load_dwordx4 v248, s[8:11], s40 offen offset:0 sc1 lds\n"
    "v_mfma_f32_16x16x32_bf16 v[196:199], a[184:187], a[88:91], v[196:199]\n"
    "ds_read2st64_b32 v[144:145], v249 offset0:0 offset1:1\n"
    "v_mfma_f32_16x16x32_bf16 v[196:199], a[188:191], a[92:95], v[196:199]\n"
    "v_mfma_f32_16x16x32_bf16 v[204:207], a[188:191], a[124:127], v[204:207]\n"
    "ds_read2st64_b32 v[146:147], v249 offset0:2 offset1:3\n"
    "v_mfma_f32_16x16x32_bf16 v[204:207], a[184:187], a[120:123], v[204:207]\n"
    "buffer_load_dwordx4 v248, s[8:11], s40 offen offset:1024 sc1 lds\n"
    "v_mfma_f32_16x16x32_bf16 v[212:215], a[184:187], a[152:155], v[212:215]\n"
    "v_mfma_f32_16x16x32_bf16 v[212:215], a[188:191], a[156:159], v[212:215]\n"
    "v_mfma_f32_16x16x32_bf16 v[216:219], a[160:163], a[128:131], v[236:239]\n"
    "ds_read_b128 a[180:183], v243 offset:26624\n"
    "v_mfma_f32_16x16x32_bf16 v[216:219], a[164:167], a[132:135], v[216:219]\n"
    "buffer_load_dwordx4 v248, s[8:11], s40 offen offset:2048 sc1 lds\n"
    "v_mfma_f32_16x16x32_bf16 v[208:211], a[164:167], a[100:103], v[232:235]\n"
    "ds_read2st64_b32 v[152:153], v249 offset0:4 offset1:5\n"
    "v_mfma_f32_16x16x32_bf16 v[208:211], a[160:163], a[96:99], v[208:211]\n"
    "ds_read_b128 a[184:187], v242 offset:30720\n"
    "v_mfma_f32_16x16x32_bf16 v[200:203], a[160:163], a[64:67], v[228:231]\n"
    "ds_read2st64_b32 v[154:155], v249 offset0:6 offset1:7\n"
    "v_mfma_f32_16x16x32_bf16 v[200:203], a[164:167], a[68:71], v[200:203]\n"
    "buffer_load_dwordx4 v248, s[8:11], s40 offen offset:3072 sc1 lds\n"
    "v_mfma_f32_16x16x32_bf16 v[192:195], a[164:167], a[36:39], v[224:227]\n"
    "s_add_u32 m0, s43, 12288\n"
    "v_exp_f32_e32 v180, v180\n"
    "v_mfma_f32_16x16x32_bf16 v[192:195], a[160:163], a[32:35], v[192:195]\n"
    "s_add_u32 s40, s40, 0x8000\n"
    "v_exp_f32_e32 v181, v181\n"
    "v_mfma_f32_16x16x32_bf16 v[184:187], a[160:163], a[0:3], v[220:223]\n"
    "ds_read2st64_b32 v[160:161], v249 offset0:8 offset1:9\n"
    "v_mfma_f32_16x16x32_bf16 v[184:187], a[164:167], a[4:7], v[184:187]\n"
    "s_waitcnt lgkmcnt(8)\n"
    "buffer_load_dwordx4 v248, s[8:11], s41 offen offset:0 sc1 lds\n"
    "v_mfma_f32_16x16x32_bf16 v[184:187], a[168:171], a[8:11], v[184:187]\n"
    "ds_read_b128 a[188:191], v243 offset:30720\n"
    "v_mfma_f32_16x16x32_bf16 v[184:187], a[172:175], a[12:15], v[184:187]\n"
    "ds_read_b64_tr_b16 a[160:161], v244 offset:16384\n"
    "v_mfma_f32_16x16x32_bf16 v[192:195], a[172:175], a[44:47], v[192:195]\n"
    "ds_read2st64_b32 v[162:163], v249 offset0:10 offset1:11\n"
    "v_mfma_f32_16x16x32_bf16 v[192:195], a[168:171], a[40:43], v[192:195]\n"
    "buffer_load_dwordx4 v248, s[8:11], s41 offen offset:1024 sc1 lds\n"
    "v_mfma_f32_16x16x32_bf16 v[200:203], a[168:171], a[72:75], v[200:203]\n"
    "ds_read_b64_tr_b16 a[162:163], v244 offset:18432\n"
    "v_mfma_f32_16x16x32_bf16 v[200:203], a[172:175], a[76:79], v[200:203]\n"
    "v_exp_f32_e32 v182, v182\n"
    "v_mfma_f32_16x16x32_bf16 v[208:211], a[172:175], a[108:111], v[208:211]\n"
    "ds_read2st64_b32 v[168:169], v249 offset0:12 offset1:13\n"
    "v_mfma_f32_16x16x32_bf16 v[208:211], a[168:171], a[104:107], v[208:211]\n"
    "buffer_load_dwordx4 v248, s[8:11], s41 offen offset:2048 sc1 lds\n"
    "v_mfma_f32_16x16x32_bf16 v[216:219], a[168:171], a[136:139], v[216:219]\n"
    "ds_read_b64_tr_b16 a[164:165], v244 offset:49152\n"
    "v_mfma_f32_16x16x32_bf16 v[216:219], a[172:175], a[140:143], v[216:219]\n"
    "s_waitcnt lgkmcnt(10)\n"
    "ds_read2st64_b32 v[170:171], v249 offset0:14 offset1:15\n"
    "v_mfma_f32_16x16x32_bf16 v[216:219], a[176:179], a[144:147], v[216:219]\n"
    "ds_read_b64_tr_b16 a[168:169], v245 offset:16384\n"
    "v_mfma_f32_16x16x32_bf16 v[216:219], a[180:183], a[148:151], v[216:219]\n"
    "buffer_load_dwordx4 v248, s[8:11], s41 offen offset:3072 sc1 lds\n"
    "v_mfma_f32_16x16x32_bf16 v[208:211], a[180:183], a[116:119], v[208:211]\n"
    "ds_read_b64_tr_b16 a[166:167], v244 offset:51200\n"
    "v_mfma_f32_16x16x32_bf16 v[208:211], a[176:179], a[112:115], v[208:211]\n"
    "s_add_u32 s41, s41, 0x8000\n"
    "ds_read2st64_b32 v[176:177], v249 offset0:16 offset1:17\n"
    "v_mfma_f32_16x16x32_bf16 v[200:203], a[176:179], a[80:83], v[200:203]\n"
    "v_mfma_f32_16x16x32_bf16 v[200:203], a[180:183], a[84:87], v[200:203]\n"
    "ds_read_b64_tr_b16 a[170:171], v245 offset:18432\n"
    "v_mfma_f32_16x16x32_bf16 v[192:195], a[180:183], a[52:55], v[192:195]\n"
    "v_exp_f32_e32 v183, v183\n"
    "v_mfma_f32_16x16x32_bf16 v[192:195], a[176:179], a[48:51], v[192:195]\n"
    "v_cvt_pk_bf16_f32 v180, v180, v181\n"
    "v_mfma_f32_16x16x32_bf16 v[184:187], a[176:179], a[16:19], v[184:187]\n"
    "ds_read_b64_tr_b16 a[172:173], v245 offset:49152\n"
    "v_mfma_f32_16x16x32_bf16 v[184:187], a[180:183], a[20:23], v[184:187]\n"
    "s_waitcnt lgkmcnt(11)\n"
    "ds_read2st64_b32 v[178:179], v249 offset0:18 offset1:19\n"
    "v_mfma_f32_16x16x32_bf16 v[184:187], a[184:187], a[24:27], v[184:187]\n"
    "ds_read_b64_tr_b16 a[176:177], v246 offset:16384\n"
    "v_mfma_f32_16x16x32_bf16 v[184:187], a[188:191], a[28:31], v[184:187]\n"
    "ds_read_b64_tr_b16 a[174:175], v245 offset:51200\n"
    "v_mfma_f32_16x16x32_bf16 v[192:195], a[188:191], a[60:63], v[192:195]\n"
    "v_exp_f32_e32 v188, v188\n"
    "v_mfma_f32_16x16x32_bf16 v[192:195], a[184:187], a[56:59], v[192:195]\n"
    "v_exp_f32_e32 v189, v189\n"
    "v_mfma_f32_16x16x32_bf16 v[200:203], a[184:187], a[88:91], v[200:203]\n"
    "ds_read_b64_tr_b16 a[178:179], v246 offset:18432\n"
    "v_mfma_f32_16x16x32_bf16 v[200:203], a[188:191], a[92:95], v[200:203]\n"
    "v_cvt_pk_bf16_f32 v181, v182, v183\n"
    "v_mfma_f32_16x16x32_bf16 v[208:211], a[188:191], a[124:127], v[208:211]\n"
    "v_exp_f32_e32 v190, v190\n"
    "v_mfma_f32_16x16x32_bf16 v[208:211], a[184:187], a[120:123], v[208:211]\n"
    "v_exp_f32_e32 v191, v191\n"
    "v_mfma_f32_16x16x32_bf16 v[216:219], a[184:187], a[152:155], v[216:219]\n"
    "ds_read_b64_tr_b16 a[180:181], v246 offset:49152\n"
    "v_mfma_f32_16x16x32_bf16 v[216:219], a[188:191], a[156:159], v[216:219]\n"
    "s_waitcnt lgkmcnt(4)\n"
    "v_cvt_pk_bf16_f32 v188, v188, v189\n"
    "v_mfma_f32_16x16x32_bf16 a[236:239], a[160:163], v[140:143], a[236:239]\n"
    "ds_read_b64_tr_b16 a[184:185], v247 offset:16384\n"
    "v_mfma_f32_16x16x32_bf16 a[236:239], a[164:167], v[144:147], a[236:239]\n"
    "ds_read_b64_tr_b16 a[182:183], v246 offset:51200\n"
    "v_mfma_f32_16x16x32_bf16 a[240:243], a[164:167], v[152:155], a[240:243]\n"
    "v_exp_f32_e32 v196, v196\n"
    "v_mfma_f32_16x16x32_bf16 a[240:243], a[160:163], v[148:151], a[240:243]\n"
    "v_exp_f32_e32 v197, v197\n"
    "v_mfma_f32_16x16x32_bf16 a[244:247], a[160:163], v[156:159], a[244:247]\n"
    "ds_read_b64_tr_b16 a[186:187], v247 offset:18432\n"
    "v_mfma_f32_16x16x32_bf16 a[244:247], a[164:167], v[160:163], a[244:247]\n"
    "v_cvt_pk_bf16_f32 v189, v190, v191\n"
    "v_mfma_f32_16x16x32_bf16 a[248:251], a[164:167], v[168:171], a[248:251]\n"
    "v_exp_f32_e32 v198, v198\n"
    "v_mfma_f32_16x16x32_bf16 a[248:251], a[160:163], v[164:167], a[248:251]\n"
    "v_exp_f32_e32 v199, v199\n"
    "v_mfma_f32_16x16x32_bf16 a[252:255], a[160:163], v[172:175], a[252:255]\n"
    "ds_read_b64_tr_b16 a[188:189], v247 offset:49152\n"
    "v_mfma_f32_16x16x32_bf16 a[252:255], a[164:167], v[176:179], a[252:255]\n"
    "s_waitcnt lgkmcnt(6)\n"
    "v_cvt_pk_bf16_f32 v196, v196, v197\n"
    "v_mfma_f32_16x16x32_bf16 v[16:19], a[172:175], v[176:179], v[16:19]\n"
    "ds_read_b64_tr_b16 a[160:161], v244 offset:20480\n"
    "v_mfma_f32_16x16x32_bf16 v[16:19], a[168:171], v[172:175], v[16:19]\n"
    "ds_read_b64_tr_b16 a[190:191], v247 offset:51200\n"
    "v_mfma_f32_16x16x32_bf16 v[12:15], a[168:171], v[164:167], v[12:15]\n"
    "v_exp_f32_e32 v204, v204\n"
    "v_mfma_f32_16x16x32_bf16 v[12:15], a[172:175], v[168:171], v[12:15]\n"
    "v_exp_f32_e32 v205, v205\n"
    "v_mfma_f32_16x16x32_bf16 v[8:11], a[172:175], v[160:163], v[8:11]\n"
    "ds_read_b64_tr_b16 a[162:163], v244 offset:22528\n"
    "v_mfma_f32_16x16x32_bf16 v[8:11], a[168:171], v[156:159], v[8:11]\n"
    "v_cvt_pk_bf16_f32 v197, v198, v199\n"
    "v_mfma_f32_16x16x32_bf16 v[4:7], a[168:171], v[148:151], v[4:7]\n"
    "v_exp_f32_e32 v206, v206\n"
    "v_mfma_f32_16x16x32_bf16 v[4:7], a[172:175], v[152:155], v[4:7]\n"
    "v_exp_f32_e32 v207, v207\n"
    "v_mfma_f32_16x16x32_bf16 v[0:3], a[172:175], v[144:147], v[0:3]\n"
    "ds_read_b64_tr_b16 a[164:165], v244 offset:53248\n"
    "v_mfma_f32_16x16x32_bf16 v[0:3], a[168:171], v[140:143], v[0:3]\n"
    "s_waitcnt lgkmcnt(6)\n"
    "v_cvt_pk_bf16_f32 v204, v204, v205\n"
    "v_mfma_f32_16x16x32_bf16 v[20:23], a[176:179], v[140:143], v[20:23]\n"
    "ds_read_b64_tr_b16 a[168:169], v245 offset:20480\n"
    "v_mfma_f32_16x16x32_bf16 v[20:23], a[180:183], v[144:147], v[20:23]\n"
    "ds_read_b64_tr_b16 a[166:167], v244 offset:55296\n"
    "v_mfma_f32_16x16x32_bf16 v[24:27], a[180:183], v[152:155], v[24:27]\n"
    "v_exp_f32_e32 v212, v212\n"
    "v_mfma_f32_16x16x32_bf16 v[24:27], a[176:179], v[148:151], v[24:27]\n"
    "v_exp_f32_e32 v213, v213\n"
    "v_mfma_f32_16x16x32_bf16 v[28:31], a[176:179], v[156:159], v[28:31]\n"
    "ds_read_b64_tr_b16 a[170:171], v245 offset:22528\n"
    "v_mfma_f32_16x16x32_bf16 v[28:31], a[180:183], v[160:163], v[28:31]\n"
    "v_cvt_pk_bf16_f32 v205, v206, v207\n"
    "v_mfma_f32_16x16x32_bf16 v[32:35], a[180:183], v[168:171], v[32:35]\n"
    "v_exp_f32_e32 v214, v214\n"
    "v_mfma_f32_16x16x32_bf16 v[32:35], a[176:179], v[164:167], v[32:35]\n"
    "v_exp_f32_e32 v215, v215\n"
    "v_mfma_f32_16x16x32_bf16 v[36:39], a[176:179], v[172:175], v[36:39]\n"
    "ds_read_b64_tr_b16 a[172:173], v245 offset:53248\n"
    "v_mfma_f32_16x16x32_bf16 v[36:39], a[180:183], v[176:179], v[36:39]\n"
    "s_waitcnt lgkmcnt(6)\n"
    "v_cvt_pk_bf16_f32 v212, v212, v213\n"
    "v_mfma_f32_16x16x32_bf16 v[56:59], a[188:191], v[176:179], v[56:59]\n"
    "ds_read_b64_tr_b16 a[176:177], v246 offset:20480\n"
    "v_mfma_f32_16x16x32_bf16 v[56:59], a[184:187], v[172:175], v[56:59]\n"
    "ds_read_b64_tr_b16 a[174:175], v245 offset:55296\n"
    "v_mfma_f32_16x16x32_bf16 v[52:55], a[184:187], v[164:167], v[52:55]\n"
    "v_cvt_pk_bf16_f32 v213, v214, v215\n"
    "v_mfma_f32_16x16x32_bf16 v[52:55], a[188:191], v[168:171], v[52:55]\n"
    "v_exp_f32_e32 v184, v184\n"
    "v_mfma_f32_16x16x32_bf16 v[48:51], a[188:191], v[160:163], v[48:51]\n"
    "ds_read_b64_tr_b16 a[178:179], v246 offset:22528\n"
    "v_mfma_f32_16x16x32_bf16 v[48:51], a[184:187], v[156:159], v[48:51]\n"
    "v_exp_f32_e32 v185, v185\n"
    "v_mfma_f32_16x16x32_bf16 v[44:47], a[184:187], v[148:151], v[44:47]\n"
    "v_mfma_f32_16x16x32_bf16 v[44:47], a[188:191], v[152:155], v[44:47]\n"
    "v_mfma_f32_16x16x32_bf16 v[40:43], a[188:191], v[144:147], v[40:43]\n"
    "ds_read_b64_tr_b16 a[180:181], v246 offset:53248\n"
    "v_mfma_f32_16x16x32_bf16 v[40:43], a[184:187], v[140:143], v[40:43]\n"
    "s_waitcnt lgkmcnt(6)\n"
    "v_exp_f32_e32 v186, v186\n"
    "v_mfma_f32_16x16x32_bf16 v[60:63], a[160:163], v[140:143], v[60:63]\n"
    "ds_read_b64_tr_b16 a[184:185], v247 offset:20480\n"
    "v_mfma_f32_16x16x32_bf16 v[60:63], a[164:167], v[144:147], v[60:63]\n"
    "ds_read_b64_tr_b16 a[182:183], v246 offset:55296\n"
    "v_mfma_f32_16x16x32_bf16 v[64:67], a[164:167], v[152:155], v[64:67]\n"
    "ds_read_b64_tr_b16 a[186:187], v247 offset:22528\n"
    "v_mfma_f32_16x16x32_bf16 v[64:67], a[160:163], v[148:151], v[64:67]\n"
    "v_exp_f32_e32 v187, v187\n"
    "v_mfma_f32_16x16x32_bf16 v[68:71], a[160:163], v[156:159], v[68:71]\n"
    "v_cvt_pk_bf16_f32 v182, v184, v185\n"
    "v_mfma_f32_16x16x32_bf16 v[68:71], a[164:167], v[160:163], v[68:71]\n"
    "ds_read_b64_tr_b16 a[188:189], v247 offset:53248\n"
    "v_mfma_f32_16x16x32_bf16 v[72:75], a[164:167], v[168:171], v[72:75]\n"
    "ds_read_b64_tr_b16 a[190:191], v247 offset:55296\n"
    "v_mfma_f32_16x16x32_bf16 v[72:75], a[160:163], v[164:167], v[72:75]\n"
    "s_waitcnt vmcnt(10)\n"
    "v_mfma_f32_16x16x32_bf16 v[76:79], a[160:163], v[172:175], v[76:79]\n"
    "s_barrier\n"
    "v_mfma_f32_16x16x32_bf16 v[76:79], a[164:167], v[176:179], v[76:79]\n"
    "s_waitcnt lgkmcnt(7)\n"
    "s_mov_b32 m0, s42\n"
    "v_mfma_f32_16x16x32_bf16 v[96:99], a[172:175], v[176:179], v[96:99]\n"
    "v_exp_f32_e32 v192, v192\n"
    "v_mfma_f32_16x16x32_bf16 v[96:99], a[168:171], v[172:175], v[96:99]\n"
    "v_exp_f32_e32 v193, v193\n"
    "v_mfma_f32_16x16x32_bf16 v[92:95], a[168:171], v[164:167], v[92:95]\n"
    "ds_write_addtid_b32 v180 offset:0\n"
    "v_cvt_pk_bf16_f32 v183, v186, v187\n"
    "v_mfma_f32_16x16x32_bf16 v[92:95], a[172:175], v[168:171], v[92:95]\n"
    "ds_read_b128 a[160:163], v240 offset:0\n"
    "v_mfma_f32_16x16x32_bf16 v[88:91], a[172:175], v[160:163], v[88:91]\n"
    "v_exp_f32_e32 v194, v194\n"
    "v_mfma_f32_16x16x32_bf16 v[88:91], a[168:171], v[156:159], v[88:91]\n"
    "v_exp_f32_e32 v195, v195\n"
    "v_mfma_f32_16x16x32_bf16 v[84:87], a[168:171], v[148:151], v[84:87]\n"
    "ds_write_addtid_b32 v181 offset:256\n"
    "v_mfma_f32_16x16x32_bf16 v[84:87], a[172:175], v[152:155], v[84:87]\n"
    "ds_write_addtid_b32 v188 offset:1024\n"
    "v_mfma_f32_16x16x32_bf16 v[80:83], a[172:175], v[144:147], v[80:83]\n"
    "v_cvt_pk_bf16_f32 v190, v192, v193\n"
    "v_mfma_f32_16x16x32_bf16 v[80:83], a[168:171], v[140:143], v[80:83]\n"
    "s_waitcnt lgkmcnt(7)\n"
    "v_exp_f32_e32 v200, v200\n"
    "v_mfma_f32_16x16x32_bf16 v[100:103], a[176:179], v[140:143], v[100:103]\n"
    "ds_read_b128 a[164:167], v241 offset:0\n"
    "v_mfma_f32_16x16x32_bf16 v[100:103], a[180:183], v[144:147], v[100:103]\n"
    "ds_write_addtid_b32 v189 offset:1280\n"
    "v_mfma_f32_16x16x32_bf16 v[104:107], a[180:183], v[152:155], v[104:107]\n"
    "v_exp_f32_e32 v201, v201\n"
    "v_mfma_f32_16x16x32_bf16 v[104:107], a[176:179], v[148:151], v[104:107]\n"
    "ds_read_b128 a[168:171], v240 offset:4096\n"
    "v_mfma_f32_16x16x32_bf16 v[108:111], a[176:179], v[156:159], v[108:111]\n"
    "ds_write_addtid_b32 v196 offset:2048\n"
    "v_mfma_f32_16x16x32_bf16 v[108:111], a[180:183], v[160:163], v[108:111]\n"
    "v_cvt_pk_bf16_f32 v191, v194, v195\n"
    "v_mfma_f32_16x16x32_bf16 v[112:115], a[180:183], v[168:171], v[112:115]\n"
    "ds_write_addtid_b32 v197 offset:2304\n"
    "v_mfma_f32_16x16x32_bf16 v[112:115], a[176:179], v[164:167], v[112:115]\n"
    "v_exp_f32_e32 v202, v202\n"
    "v_mfma_f32_16x16x32_bf16 v[116:119], a[176:179], v[172:175], v[116:119]\n"
    "ds_write_addtid_b32 v204 offset:3072\n"
    "v_mfma_f32_16x16x32_bf16 v[116:119], a[180:183], v[176:179], v[116:119]\n"
    "s_waitcnt lgkmcnt(10)\n"
    "v_exp_f32_e32 v203, v203\n"
    "v_mfma_f32_16x16x32_bf16 v[136:139], a[188:191], v[176:179], v[136:139]\n"
    "ds_read_b128 a[172:175], v241 offset:4096\n"
    "v_mfma_f32_16x16x32_bf16 v[136:139], a[184:187], v[172:175], v[136:139]\n"
    "ds_write_addtid_b32 v205 offset:3328\n"
    "v_cvt_pk_bf16_f32 v198, v200, v201\n"
    "v_mfma_f32_16x16x32_bf16 v[132:135], a[184:187], v[164:167], v[132:135]\n"
    "ds_read_b128 a[176:179], v242 offset:0\n"
    "v_mfma_f32_16x16x32_bf16 v[132:135], a[188:191], v[168:171], v[132:135]\n"
    "ds_write_addtid_b32 v212 offset:4096\n"
    "v_mfma_f32_16x16x32_bf16 v[128:131], a[188:191], v[160:163], v[128:131]\n"
    "v_exp_f32_e32 v208, v208\n"
    "v_mfma_f32_16x16x32_bf16 v[128:131], a[184:187], v[156:159], v[128:131]\n"
    "ds_write_addtid_b32 v213 offset:4352\n"
    "v_mfma_f32_16x16x32_bf16 v[124:127], a[184:187], v[148:151], v[124:127]\n"
    "v_exp_f32_e32 v209, v209\n"
    "v_mfma_f32_16x16x32_bf16 v[124:127], a[188:191], v[152:155], v[124:127]\n"
    "ds_read_b128 a[180:183], v243 offset:0\n"
    "v_mfma_f32_16x16x32_bf16 v[120:123], a[188:191], v[144:147], v[120:123]\n"
    "ds_write_addtid_b32 v182 offset:512\n"
    "v_mfma_f32_16x16x32_bf16 v[120:123], a[184:187], v[140:143], v[120:123]\n"
    "s_waitcnt lgkmcnt(12)\n"
    "v_cvt_pk_bf16_f32 v199, v202, v203\n"
    "v_mfma_f32_16x16x32_bf16 v[172:175], a[160:163], a[128:131], v[236:239]\n"
    "v_mfma_f32_16x16x32_bf16 v[172:175], a[164:167], a[132:135], v[172:175]\n"
    "v_exp_f32_e32 v210, v210\n"
    "v_mfma_f32_16x16x32_bf16 v[164:167], a[164:167], a[100:103], v[232:235]\n"
    "ds_write_addtid_b32 v183 offset:768\n"
    "v_mfma_f32_16x16x32_bf16 v[164:167], a[160:163], a[96:99], v[164:167]\n"
    "ds_read_b128 a[184:187], v242 offset:4096\n"
    "v_mfma_f32_16x16x32_bf16 v[156:159], a[160:163], a[64:67], v[228:231]\n"
    "v_exp_f32_e32 v211, v211\n"
    "v_mfma_f32_16x16x32_bf16 v[156:159], a[164:167], a[68:71], v[156:159]\n"
    "ds_write_addtid_b32 v190 offset:1536\n"
    "v_mfma_f32_16x16x32_bf16 v[148:151], a[164:167], a[36:39], v[224:227]\n"
    "v_cvt_pk_bf16_f32 v206, v208, v209\n"
    "v_mfma_f32_16x16x32_bf16 v[148:151], a[160:163], a[32:35], v[148:151]\n"
    "v_mfma_f32_16x16x32_bf16 v[140:143], a[160:163], a[0:3], v[220:223]\n"
    "ds_write_addtid_b32 v191 offset:1792\n"
    "v_mfma_f32_16x16x32_bf16 v[140:143], a[164:167], a[4:7], v[140:143]\n"
    "s_waitcnt lgkmcnt(10)\n"
    "v_exp_f32_e32 v216, v216\n"
    "v_mfma_f32_16x16x32_bf16 v[140:143], a[168:171], a[8:11], v[140:143]\n"
    "ds_read_b128 a[188:191], v243 offset:4096\n"
    "v_mfma_f32_16x16x32_bf16 v[140:143], a[172:175], a[12:15], v[140:143]\n"
    "v_exp_f32_e32 v217, v217\n"
    "v_mfma_f32_16x16x32_bf16 v[148:151], a[172:175], a[44:47], v[148:151]\n"
    "ds_write_addtid_b32 v198 offset:2560\n"
    "v_mfma_f32_16x16x32_bf16 v[148:151], a[168:171], a[40:43], v[148:151]\n"
    "ds_read_b128 a[160:163], v240 offset:2048\n"
    "v_mfma_f32_16x16x32_bf16 v[156:159], a[168:171], a[72:75], v[156:159]\n"
    "v_cvt_pk_bf16_f32 v207, v210, v211\n"
    "v_mfma_f32_16x16x32_bf16 v[156:159], a[172:175], a[76:79], v[156:159]\n"
    "ds_write_addtid_b32 v199 offset:2816\n"
    "v_mfma_f32_16x16x32_bf16 v[164:167], a[172:175], a[108:111], v[164:167]\n"
    "v_exp_f32_e32 v218, v218\n"
    "v_mfma_f32_16x16x32_bf16 v[164:167], a[168:171], a[104:107], v[164:167]\n"
    "ds_write_addtid_b32 v206 offset:3584\n"
    "v_mfma_f32_16x16x32_bf16 v[172:175], a[168:171], a[136:139], v[172:175]\n"
    "v_mfma_f32_16x16x32_bf16 v[172:175], a[172:175], a[140:143], v[172:175]\n"
    "s_waitcnt lgkmcnt(10)\n"
    "v_exp_f32_e32 v219, v219\n"
    "v_mfma_f32_16x16x32_bf16 v[172:175], a[176:179], a[144:147], v[172:175]\n"
    "ds_read_b128 a[164:167], v241 offset:2048\n"
    "v_mfma_f32_16x16x32_bf16 v[172:175], a[180:183], a[148:151], v[172:175]\n"
    "v_cvt_pk_bf16_f32 v214, v216, v217\n"
    "v_mfma_f32_16x16x32_bf16 v[164:167], a[180:183], a[116:119], v[164:167]\n"
    "ds_write_addtid_b32 v207 offset:3840\n"
    "v_mfma_f32_16x16x32_bf16 v[164:167], a[176:179], a[112:115], v[164:167]\n"
    "ds_read_b128 a[168:171], v240 offset:6144\n"
    "v_mfma_f32_16x16x32_bf16 v[156:159], a[176:179], a[80:83], v[156:159]\n"
    "v_cvt_pk_bf16_f32 v215, v218, v219\n"
    "v_mfma_f32_16x16x32_bf16 v[156:159], a[180:183], a[84:87], v[156:159]\n"
    "ds_write_addtid_b32 v214 offset:4608\n"
    "v_mfma_f32_16x16x32_bf16 v[148:151], a[180:183], a[52:55], v[148:151]\n"
    "ds_write_addtid_b32 v215 offset:4864\n"
    "v_mfma_f32_16x16x32_bf16 v[148:151], a[176:179], a[48:51], v[148:151]\n"
    "v_mfma_f32_16x16x32_bf16 v[140:143], a[176:179], a[16:19], v[140:143]\n"
    "ds_read_b128 a[172:175], v241 offset:6144\n"
    "v_mfma_f32_16x16x32_bf16 v[140:143], a[180:183], a[20:23], v[140:143]\n"
    "s_waitcnt lgkmcnt(10)\n"
    "s_waitcnt lgkmcnt(1)\n"
    "v_mfma_f32_16x16x32_bf16 v[140:143], a[184:187], a[24:27], v[140:143]\n"
    "s_barrier\n"
    "v_mfma_f32_16x16x32_bf16 v[140:143], a[188:191], a[28:31], v[140:143]\n"
    "s_add_u32 m0, s43, 16384\n"
    "v_mfma_f32_16x16x32_bf16 v[148:151], a[188:191], a[60:63], v[148:151]\n"
    "ds_read_b128 a[176:179], v242 offset:2048\n"
    "v_mfma_f32_16x16x32_bf16 v[148:151], a[184:187], a[56:59], v[148:151]\n"
    "buffer_load_dwordx4 v248, s[8:11], s40 offen offset:0 sc1 lds\n"
    "v_mfma_f32_16x16x32_bf16 v[156:159], a[184:187], a[88:91], v[156:159]\n"
    "ds_read2st64_b32 v[184:185], v249 offset0:0 offset1:1\n"
    "v_mfma_f32_16x16x32_bf16 v[156:159], a[188:191], a[92:95], v[156:159]\n"
    "v_mfma_f32_16x16x32_bf16 v[164:167], a[188:191], a[124:127], v[164:167]\n"
    "ds_read2st64_b32 v[186:187], v249 offset0:2 offset1:3\n"
    "v_mfma_f32_16x16x32_bf16 v[164:167], a[184:187], a[120:123], v[164:167]\n"
    "buffer_load_dwordx4 v248, s[8:11], s40 offen offset:1024 sc1 lds\n"
    "v_mfma_f32_16x16x32_bf16 v[172:175], a[184:187], a[152:155], v[172:175]\n"
    "v_mfma_f32_16x16x32_bf16 v[172:175], a[188:191], a[156:159], v[172:175]\n"
    "v_mfma_f32_16x16x32_bf16 v[176:179], a[160:163], a[128:131], v[236:239]\n"
    "ds_read_b128 a[180:183], v243 offset:2048\n"
    "v_mfma_f32_16x16x32_bf16 v[176:179], a[164:167], a[132:135], v[176:179]\n"
    "buffer_load_dwordx4 v248, s[8:11], s40 offen offset:2048 sc1 lds\n"
    "v_mfma_f32_16x16x32_bf16 v[168:171], a[164:167], a[100:103], v[232:235]\n"
    "ds_read2st64_b32 v[192:193], v249 offset0:4 offset1:5\n"
    "v_mfma_f32_16x16x32_bf16 v[168:171], a[160:163], a[96:99], v[168:171]\n"
    "ds_read_b128 a[184:187], v242 offset:6144\n"
    "v_mfma_f32_16x16x32_bf16 v[160:163], a[160:163], a[64:67], v[228:231]\n"
    "ds_read2st64_b32 v[194:195], v249 offset0:6 offset1:7\n"
    "v_mfma_f32_16x16x32_bf16 v[160:163], a[164:167], a[68:71], v[160:163]\n"
    "buffer_load_dwordx4 v248, s[8:11], s40 offen offset:3072 sc1 lds\n"
    "v_mfma_f32_16x16x32_bf16 v[152:155], a[164:167], a[36:39], v[224:227]\n"
    "s_add_u32 m0, s43, 20480\n"
    "v_exp_f32_e32 v140, v140\n"
    "v_mfma_f32_16x16x32_bf16 v[152:155], a[160:163], a[32:35], v[152:155]\n"
    "s_add_u32 s40, s40, 0x8000\n"
    "v_exp_f32_e32 v141, v141\n"
    "v_mfma_f32_16x16x32_bf16 v[144:147], a[160:163], a[0:3], v[220:223]\n"
    "ds_read2st64_b32 v[200:201], v249 offset0:8 offset1:9\n"
    "v_mfma_f32_16x16x32_bf16 v[144:147], a[164:167], a[4:7], v[144:147]\n"
    "s_waitcnt lgkmcnt(8)\n"
    "buffer_load_dwordx4 v248, s[8:11], s41 offen offset:0 sc1 lds\n"
    "v_mfma_f32_16x16x32_bf16 v[144:147], a[168:171], a[8:11], v[144:147]\n"
    "ds_read_b128 a[188:191], v243 offset:6144\n"
    "v_mfma_f32_16x16x32_bf16 v[144:147], a[172:175], a[12:15], v[144:147]\n"
    "ds_read_b64_tr_b16 a[160:161], v244 offset:24576\n"
    "v_mfma_f32_16x16x32_bf16 v[152:155], a[172:175], a[44:47], v[152:155]\n"
    "ds_read2st64_b32 v[202:203], v249 offset0:10 offset1:11\n"
    "v_mfma_f32_16x16x32_bf16 v[152:155], a[168:171], a[40:43], v[152:155]\n"
    "buffer_load_dwordx4 v248, s[8:11], s41 offen offset:1024 sc1 lds\n"
    "v_mfma_f32_16x16x32_bf16 v[160:163], a[168:171], a[72:75], v[160:163]\n"
    "ds_read_b64_tr_b16 a[162:163], v244 offset:26624\n"
    "v_mfma_f32_16x16x32_bf16 v[160:163], a[172:175], a[76:79], v[160:163]\n"
    "v_exp_f32_e32 v142, v142\n"
    "v_mfma_f32_16x16x32_bf16 v[168:171], a[172:175], a[108:111], v[168:171]\n"
    "ds_read2st64_b32 v[208:209], v249 offset0:12 offset1:13\n"
    "v_mfma_f32_16x16x32_bf16 v[168:171], a[168:171], a[104:107], v[168:171]\n"
    "buffer_load_dwordx4 v248, s[8:11], s41 offen offset:2048 sc1 lds\n"
    "v_mfma_f32_16x16x32_bf16 v[176:179], a[168:171], a[136:139], v[176:179]\n"
    "ds_read_b64_tr_b16 a[164:165], v244 offset:57344\n"
    "v_mfma_f32_16x16x32_bf16 v[176:179], a[172:175], a[140:143], v[176:179]\n"
    "s_waitcnt lgkmcnt(10)\n"
    "ds_read2st64_b32 v[210:211], v249 offset0:14 offset1:15\n"
    "v_mfma_f32_16x16x32_bf16 v[176:179], a[176:179], a[144:147], v[176:179]\n"
    "ds_read_b64_tr_b16 a[168:169], v245 offset:24576\n"
    "v_mfma_f32_16x16x32_bf16 v[176:179], a[180:183], a[148:151], v[176:179]\n"
    "buffer_load_dwordx4 v248, s[8:11], s41 offen offset:3072 sc1 lds\n"
    "v_mfma_f32_16x16x32_bf16 v[168:171], a[180:183], a[116:119], v[168:171]\n"
    "ds_read_b64_tr_b16 a[166:167], v244 offset:59392\n"
    "v_mfma_f32_16x16x32_bf16 v[168:171], a[176:179], a[112:115], v[168:171]\n"
    "s_add_u32 s41, s41, 0x8000\n"
    "ds_read2st64_b32 v[216:217], v249 offset0:16 offset1:17\n"
    "v_mfma_f32_16x16x32_bf16 v[160:163], a[176:179], a[80:83], v[160:163]\n"
    "v_mfma_f32_16x16x32_bf16 v[160:163], a[180:183], a[84:87], v[160:163]\n"
    "ds_read_b64_tr_b16 a[170:171], v245 offset:26624\n"
    "v_mfma_f32_16x16x32_bf16 v[152:155], a[180:183], a[52:55], v[152:155]\n"
    "v_exp_f32_e32 v143, v143\n"
    "v_mfma_f32_16x16x32_bf16 v[152:155], a[176:179], a[48:51], v[152:155]\n"
    "v_cvt_pk_bf16_f32 v140, v140, v141\n"
    "v_mfma_f32_16x16x32_bf16 v[144:147], a[176:179], a[16:19], v[144:147]\n"
    "ds_read_b64_tr_b16 a[172:173], v245 offset:57344\n"
    "v_mfma_f32_16x16x32_bf16 v[144:147], a[180:183], a[20:23], v[144:147]\n"
    "s_waitcnt lgkmcnt(11)\n"
    "ds_read2st64_b32 v[218:219], v249 offset0:18 offset1:19\n"
    "v_mfma_f32_16x16x32_bf16 v[144:147], a[184:187], a[24:27], v[144:147]\n"
    "ds_read_b64_tr_b16 a[176:177], v246 offset:24576\n"
    "v_mfma_f32_16x16x32_bf16 v[144:147], a[188:191], a[28:31], v[144:147]\n"
    "ds_read_b64_tr_b16 a[174:175], v245 offset:59392\n"
    "v_mfma_f32_16x16x32_bf16 v[152:155], a[188:191], a[60:63], v[152:155]\n"
    "v_exp_f32_e32 v148, v148\n"
    "v_mfma_f32_16x16x32_bf16 v[152:155], a[184:187], a[56:59], v[152:155]\n"
    "v_exp_f32_e32 v149, v149\n"
    "v_mfma_f32_16x16x32_bf16 v[160:163], a[184:187], a[88:91], v[160:163]\n"
    "ds_read_b64_tr_b16 a[178:179], v246 offset:26624\n"
    "v_mfma_f32_16x16x32_bf16 v[160:163], a[188:191], a[92:95], v[160:163]\n"
    "v_cvt_pk_bf16_f32 v141, v142, v143\n"
    "v_mfma_f32_16x16x32_bf16 v[168:171], a[188:191], a[124:127], v[168:171]\n"
    "v_exp_f32_e32 v150, v150\n"
    "v_mfma_f32_16x16x32_bf16 v[168:171], a[184:187], a[120:123], v[168:171]\n"
    "v_exp_f32_e32 v151, v151\n"
    "v_mfma_f32_16x16x32_bf16 v[176:179], a[184:187], a[152:155], v[176:179]\n"
    "ds_read_b64_tr_b16 a[180:181], v246 offset:57344\n"
    "v_mfma_f32_16x16x32_bf16 v[176:179], a[188:191], a[156:159], v[176:179]\n"
    "s_waitcnt lgkmcnt(4)\n"
    "v_cvt_pk_bf16_f32 v148, v148, v149\n"
    "v_mfma_f32_16x16x32_bf16 a[236:239], a[160:163], v[180:183], a[236:239]\n"
    "ds_read_b64_tr_b16 a[184:185], v247 offset:24576\n"
    "v_mfma_f32_16x16x32_bf16 a[236:239], a[164:167], v[184:187], a[236:239]\n"
    "ds_read_b64_tr_b16 a[182:183], v246 offset:59392\n"
    "v_mfma_f32_16x16x32_bf16 a[240:243], a[164:167], v[192:195], a[240:243]\n"
    "v_exp_f32_e32 v156, v156\n"
    "v_mfma_f32_16x16x32_bf16 a[240:243], a[160:163], v[188:191], a[240:243]\n"
    "v_exp_f32_e32 v157, v157\n"
    "v_mfma_f32_16x16x32_bf16 a[244:247], a[160:163], v[196:199], a[244:247]\n"
    "ds_read_b64_tr_b16 a[186:187], v247 offset:26624\n"
    "v_mfma_f32_16x16x32_bf16 a[244:247], a[164:167], v[200:203], a[244:247]\n"
    "v_cvt_pk_bf16_f32 v149, v150, v151\n"
    "v_mfma_f32_16x16x32_bf16 a[248:251], a[164:167], v[208:211], a[248:251]\n"
    "v_exp_f32_e32 v158, v158\n"
    "v_mfma_f32_16x16x32_bf16 a[248:251], a[160:163], v[204:207], a[248:251]\n"
    "v_exp_f32_e32 v159, v159\n"
    "v_mfma_f32_16x16x32_bf16 a[252:255], a[160:163], v[212:215], a[252:255]\n"
    "ds_read_b64_tr_b16 a[188:189], v247 offset:57344\n"
    "v_mfma_f32_16x16x32_bf16 a[252:255], a[164:167], v[216:219], a[252:255]\n"
    "s_waitcnt lgkmcnt(6)\n"
    "v_cvt_pk_bf16_f32 v156, v156, v157\n"
    "v_mfma_f32_16x16x32_bf16 v[16:19], a[172:175], v[216:219], v[16:19]\n"
    "ds_read_b64_tr_b16 a[160:161], v244 offset:28672\n"
    "v_mfma_f32_16x16x32_bf16 v[16:19], a[168:171], v[212:215], v[16:19]\n"
    "ds_read_b64_tr_b16 a[190:191], v247 offset:59392\n"
    "v_mfma_f32_16x16x32_bf16 v[12:15], a[168:171], v[204:207], v[12:15]\n"
    "v_exp_f32_e32 v164, v164\n"
    "v_mfma_f32_16x16x32_bf16 v[12:15], a[172:175], v[208:211], v[12:15]\n"
    "v_exp_f32_e32 v165, v165\n"
    "v_mfma_f32_16x16x32_bf16 v[8:11], a[172:175], v[200:203], v[8:11]\n"
    "ds_read_b64_tr_b16 a[162:163], v244 offset:30720\n"
    "v_mfma_f32_16x16x32_bf16 v[8:11], a[168:171], v[196:199], v[8:11]\n"
    "v_cvt_pk_bf16_f32 v157, v158, v159\n"
    "v_mfma_f32_16x16x32_bf16 v[4:7], a[168:171], v[188:191], v[4:7]\n"
    "v_exp_f32_e32 v166, v166\n"
    "v_mfma_f32_16x16x32_bf16 v[4:7], a[172:175], v[192:195], v[4:7]\n"
    "v_exp_f32_e32 v167, v167\n"
    "v_mfma_f32_16x16x32_bf16 v[0:3], a[172:175], v[184:187], v[0:3]\n"
    "ds_read_b64_tr_b16 a[164:165], v244 offset:61440\n"
    "v_mfma_f32_16x16x32_bf16 v[0:3], a[168:171], v[180:183], v[0:3]\n"
    "s_waitcnt lgkmcnt(6)\n"
    "v_cvt_pk_bf16_f32 v164, v164, v165\n"
    "v_mfma_f32_16x16x32_bf16 v[20:23], a[176:179], v[180:183], v[20:23]\n"
    "ds_read_b64_tr_b16 a[168:169], v245 offset:28672\n"
    "v_mfma_f32_16x16x32_bf16 v[20:23], a[180:183], v[184:187], v[20:23]\n"
    "ds_read_b64_tr_b16 a[166:167], v244 offset:63488\n"
    "v_mfma_f32_16x16x32_bf16 v[24:27], a[180:183], v[192:195], v[24:27]\n"
    "v_exp_f32_e32 v172, v172\n"
    "v_mfma_f32_16x16x32_bf16 v[24:27], a[176:179], v[188:191], v[24:27]\n"
    "v_exp_f32_e32 v173, v173\n"
    "v_mfma_f32_16x16x32_bf16 v[28:31], a[176:179], v[196:199], v[28:31]\n"
    "ds_read_b64_tr_b16 a[170:171], v245 offset:30720\n"
    "v_mfma_f32_16x16x32_bf16 v[28:31], a[180:183], v[200:203], v[28:31]\n"
    "v_cvt_pk_bf16_f32 v165, v166, v167\n"
    "v_mfma_f32_16x16x32_bf16 v[32:35], a[180:183], v[208:211], v[32:35]\n"
    "v_exp_f32_e32 v174, v174\n"
    "v_mfma_f32_16x16x32_bf16 v[32:35], a[176:179], v[204:207], v[32:35]\n"
    "v_exp_f32_e32 v175, v175\n"
    "v_mfma_f32_16x16x32_bf16 v[36:39], a[176:179], v[212:215], v[36:39]\n"
    "ds_read_b64_tr_b16 a[172:173], v245 offset:61440\n"
    "v_mfma_f32_16x16x32_bf16 v[36:39], a[180:183], v[216:219], v[36:39]\n"
    "s_waitcnt lgkmcnt(6)\n"
    "v_cvt_pk_bf16_f32 v172, v172, v173\n"
    "v_mfma_f32_16x16x32_bf16 v[56:59], a[188:191], v[216:219], v[56:59]\n"
    "ds_read_b64_tr_b16 a[176:177], v246 offset:28672\n"
    "v_mfma_f32_16x16x32_bf16 v[56:59], a[184:187], v[212:215], v[56:59]\n"
    "ds_read_b64_tr_b16 a[174:175], v245 offset:63488\n"
    "v_mfma_f32_16x16x32_bf16 v[52:55], a[184:187], v[204:207], v[52:55]\n"
    "v_cvt_pk_bf16_f32 v173, v174, v175\n"
    "v_mfma_f32_16x16x32_bf16 v[52:55], a[188:191], v[208:211], v[52:55]\n"
    "v_exp_f32_e32 v144, v144\n"
    "v_mfma_f32_16x16x32_bf16 v[48:51], a[188:191], v[200:203], v[48:51]\n"
    "ds_read_b64_tr_b16 a[178:179], v246 offset:30720\n"
    "v_mfma_f32_16x16x32_bf16 v[48:51], a[184:187], v[196:199], v[48:51]\n"
    "v_exp_f32_e32 v145, v145\n"
    "v_mfma_f32_16x16x32_bf16 v[44:47], a[184:187], v[188:191], v[44:47]\n"
    "v_mfma_f32_16x16x32_bf16 v[44:47], a[188:191], v[192:195], v[44:47]\n"
    "v_mfma_f32_16x16x32_bf16 v[40:43], a[188:191], v[184:187], v[40:43]\n"
    "ds_read_b64_tr_b16 a[180:181], v246 offset:61440\n"
    "v_mfma_f32_16x16x32_bf16 v[40:43], a[184:187], v[180:183], v[40:43]\n"
    "s_waitcnt lgkmcnt(6)\n"
    "v_exp_f32_e32 v146, v146\n"
    "v_mfma_f32_16x16x32_bf16 v[60:63], a[160:163], v[180:183], v[60:63]\n"
    "ds_read_b64_tr_b16 a[184:185], v247 offset:28672\n"
    "v_mfma_f32_16x16x32_bf16 v[60:63], a[164:167], v[184:187], v[60:63]\n"
    "ds_read_b64_tr_b16 a[182:183], v246 offset:63488\n"
    "v_mfma_f32_16x16x32_bf16 v[64:67], a[164:167], v[192:195], v[64:67]\n"
    "ds_read_b64_tr_b16 a[186:187], v247 offset:30720\n"
    "v_mfma_f32_16x16x32_bf16 v[64:67], a[160:163], v[188:191], v[64:67]\n"
    "v_exp_f32_e32 v147, v147\n"
    "v_mfma_f32_16x16x32_bf16 v[68:71], a[160:163], v[196:199], v[68:71]\n"
    "v_cvt_pk_bf16_f32 v142, v144, v145\n"
    "v_mfma_f32_16x16x32_bf16 v[68:71], a[164:167], v[200:203], v[68:71]\n"
    "ds_read_b64_tr_b16 a[188:189], v247 offset:61440\n"
    "v_mfma_f32_16x16x32_bf16 v[72:75], a[164:167], v[208:211], v[72:75]\n"
    "ds_read_b64_tr_b16 a[190:191], v247 offset:63488\n"
    "v_mfma_f32_16x16x32_bf16 v[72:75], a[160:163], v[204:207], v[72:75]\n"
    "s_waitcnt vmcnt(8)\n"
    "v_mfma_f32_16x16x32_bf16 v[76:79], a[160:163], v[212:215], v[76:79]\n"
    "s_barrier\n"
    "v_mfma_f32_16x16x32_bf16 v[76:79], a[164:167], v[216:219], v[76:79]\n"
    "s_waitcnt lgkmcnt(7)\n"
    "s_mov_b32 m0, s42\n"
    "v_mfma_f32_16x16x32_bf16 v[96:99], a[172:175], v[216:219], v[96:99]\n"
    "v_exp_f32_e32 v152, v152\n"
    "v_mfma_f32_16x16x32_bf16 v[96:99], a[168:171], v[212:215], v[96:99]\n"
    "v_exp_f32_e32 v153, v153\n"
    "v_mfma_f32_16x16x32_bf16 v[92:95], a[168:171], v[204:207], v[92:95]\n"
    "ds_write_addtid_b32 v140 offset:0\n"
    "v_cvt_pk_bf16_f32 v143, v146, v147\n"
    "v_mfma_f32_16x16x32_bf16 v[92:95], a[172:175], v[208:211], v[92:95]\n"
    "ds_read_b128 a[160:163], v240 offset:8192\n"
    "v_mfma_f32_16x16x32_bf16 v[88:91], a[172:175], v[200:203], v[88:91]\n"
    "v_exp_f32_e32 v154, v154\n"
    "v_mfma_f32_16x16x32_bf16 v[88:91], a[168:171], v[196:199], v[88:91]\n"
    "v_exp_f32_e32 v155, v155\n"
    "v_mfma_f32_16x16x32_bf16 v[84:87], a[168:171], v[188:191], v[84:87]\n"
    "ds_write_addtid_b32 v141 offset:256\n"
    "v_mfma_f32_16x16x32_bf16 v[84:87], a[172:175], v[192:195], v[84:87]\n"
    "ds_write_addtid_b32 v148 offset:1024\n"
    "v_mfma_f32_16x16x32_bf16 v[80:83], a[172:175], v[184:187], v[80:83]\n"
    "v_cvt_pk_bf16_f32 v150, v152, v153\n"
    "v_mfma_f32_16x16x32_bf16 v[80:83], a[168:171], v[180:183], v[80:83]\n"
    "s_waitcnt lgkmcnt(7)\n"
    "v_exp_f32_e32 v160, v160\n"
    "v_mfma_f32_16x16x32_bf16 v[100:103], a[176:179], v[180:183], v[100:103]\n"
    "ds_read_b128 a[164:167], v241 offset:8192\n"
    "v_mfma_f32_16x16x32_bf16 v[100:103], a[180:183], v[184:187], v[100:103]\n"
    "ds_write_addtid_b32 v149 offset:1280\n"
    "v_mfma_f32_16x16x32_bf16 v[104:107], a[180:183], v[192:195], v[104:107]\n"
    "v_exp_f32_e32 v161, v161\n"
    "v_mfma_f32_16x16x32_bf16 v[104:107], a[176:179], v[188:191], v[104:107]\n"
    "ds_read_b128 a[168:171], v240 offset:12288\n"
    "v_mfma_f32_16x16x32_bf16 v[108:111], a[176:179], v[196:199], v[108:111]\n"
    "ds_write_addtid_b32 v156 offset:2048\n"
    "v_mfma_f32_16x16x32_bf16 v[108:111], a[180:183], v[200:203], v[108:111]\n"
    "v_cvt_pk_bf16_f32 v151, v154, v155\n"
    "v_mfma_f32_16x16x32_bf16 v[112:115], a[180:183], v[208:211], v[112:115]\n"
    "ds_write_addtid_b32 v157 offset:2304\n"
    "v_mfma_f32_16x16x32_bf16 v[112:115], a[176:179], v[204:207], v[112:115]\n"
    "v_exp_f32_e32 v162, v162\n"
    "v_mfma_f32_16x16x32_bf16 v[116:119], a[176:179], v[212:215], v[116:119]\n"
    "ds_write_addtid_b32 v164 offset:3072\n"
    "v_mfma_f32_16x16x32_bf16 v[116:119], a[180:183], v[216:219], v[116:119]\n"
    "s_waitcnt lgkmcnt(10)\n"
    "v_exp_f32_e32 v163, v163\n"
    "v_mfma_f32_16x16x32_bf16 v[136:139], a[188:191], v[216:219], v[136:139]\n"
    "ds_read_b128 a[172:175], v241 offset:12288\n"
    "v_mfma_f32_16x16x32_bf16 v[136:139], a[184:187], v[212:215], v[136:139]\n"
    "ds_write_addtid_b32 v165 offset:3328\n"
    "v_cvt_pk_bf16_f32 v158, v160, v161\n"
    "v_mfma_f32_16x16x32_bf16 v[132:135], a[184:187], v[204:207], v[132:135]\n"
    "ds_read_b128 a[176:179], v242 offset:8192\n"
    "v_mfma_f32_16x16x32_bf16 v[132:135], a[188:191], v[208:211], v[132:135]\n"
    "ds_write_addtid_b32 v172 offset:4096\n"
    "v_mfma_f32_16x16x32_bf16 v[128:131], a[188:191], v[200:203], v[128:131]\n"
    "v_exp_f32_e32 v168, v168\n"
    "v_mfma_f32_16x16x32_bf16 v[128:131], a[184:187], v[196:199], v[128:131]\n"
    "ds_write_addtid_b32 v173 offset:4352\n"
    "v_mfma_f32_16x16x32_bf16 v[124:127], a[184:187], v[188:191], v[124:127]\n"
    "v_exp_f32_e32 v169, v169\n"
    "v_mfma_f32_16x16x32_bf16 v[124:127], a[188:191], v[192:195], v[124:127]\n"
    "ds_read_b128 a[180:183], v243 offset:8192\n"
    "v_mfma_f32_16x16x32_bf16 v[120:123], a[188:191], v[184:187], v[120:123]\n"
    "ds_write_addtid_b32 v142 offset:512\n"
    "v_mfma_f32_16x16x32_bf16 v[120:123], a[184:187], v[180:183], v[120:123]\n"
    "s_waitcnt lgkmcnt(12)\n"
    "v_cvt_pk_bf16_f32 v159, v162, v163\n"
    ".Lat_in:\n"
    "v_mfma_f32_16x16x32_bf16 v[212:215], a[160:163], a[128:131], v[236:239]\n"
    "v_mfma_f32_16x16x32_bf16 v[212:215], a[164:167], a[132:135], v[212:215]\n"
    "v_exp_f32_e32 v170, v170\n"
    "v_mfma_f32_16x16x32_bf16 v[204:207], a[164:167], a[100:103], v[232:235]\n"
    "ds_write_addtid_b32 v143 offset:768\n"
    "v_mfma_f32_16x16x32_bf16 v[204:207], a[160:163], a[96:99], v[204:207]\n"
    "ds_read_b128 a[184:187], v242 offset:12288\n"
    "v_mfma_f32_16x16x32_bf16 v[196:199], a[160:163], a[64:67], v[228:231]\n"
    "v_exp_f32_e32 v171, v171\n"
    "v_mfma_f32_16x16x32_bf16 v[196:199], a[164:167], a[68:71], v[196:199]\n"
    "ds_write_addtid_b32 v150 offset:1536\n"
    "v_mfma_f32_16x16x32_bf16 v[188:191], a[164:167], a[36:39], v[224:227]\n"
    "v_cvt_pk_bf16_f32 v166, v168, v169\n"
    "v_mfma_f32_16x16x32_bf16 v[188:191], a[160:163], a[32:35], v[188:191]\n"
    "v_mfma_f32_16x16x32_bf16 v[180:183], a[160:163], a[0:3], v[220:223]\n"
    "ds_write_addtid_b32 v151 offset:1792\n"
    "v_mfma_f32_16x16x32_bf16 v[180:183], a[164:167], a[4:7], v[180:183]\n"
    "s_waitcnt lgkmcnt(10)\n"
    "v_exp_f32_e32 v176, v176\n"
    "v_mfma_f32_16x16x32_bf16 v[180:183], a[168:171], a[8:11], v[180:183]\n"
    "ds_read_b128 a[188:191], v243 offset:12288\n"
    "v_mfma_f32_16x16x32_bf16 v[180:183], a[172:175], a[12:15], v[180:183]\n"
    "v_exp_f32_e32 v177, v177\n"
    "v_mfma_f32_16x16x32_bf16 v[188:191], a[172:175], a[44:47], v[188:191]\n"
    "ds_write_addtid_b32 v158 offset:2560\n"
    "v_mfma_f32_16x16x32_bf16 v[188:191], a[168:171], a[40:43], v[188:191]\n"
    "ds_read_b128 a[160:163], v240 offset:10240\n"
    "v_mfma_f32_16x16x32_bf16 v[196:199], a[168:171], a[72:75], v[196:199]\n"
    "v_cvt_pk_bf16_f32 v167, v170, v171\n"
    "v_mfma_f32_16x16x32_bf16 v[196:199], a[172:175], a[76:79], v[196:199]\n"
    "ds_write_addtid_b32 v159 offset:2816\n"
    "v_mfma_f32_16x16x32_bf16 v[204:207], a[172:175], a[108:111], v[204:207]\n"
    "v_exp_f32_e32 v178, v178\n"
    "v_mfma_f32_16x16x32_bf16 v[204:207], a[168:171], a[104:107], v[204:207]\n"
    "ds_write_addtid_b32 v166 offset:3584\n"
    "v_mfma_f32_16x16x32_bf16 v[212:215], a[168:171], a[136:139], v[212:215]\n"
    "v_mfma_f32_16x16x32_bf16 v[212:215], a[172:175], a[140:143], v[212:215]\n"
    "s_waitcnt lgkmcnt(10)\n"
    "v_exp_f32_e32 v179, v179\n"
    "v_mfma_f32_16x16x32_bf16 v[212:215], a[176:179], a[144:147], v[212:215]\n"
    "ds_read_b128 a[164:167], v241 offset:10240\n"
    "v_mfma_f32_16x16x32_bf16 v[212:215], a[180:183], a[148:151], v[212:215]\n"
    "v_cvt_pk_bf16_f32 v174, v176, v177\n"
    "v_mfma_f32_16x16x32_bf16 v[204:207], a[180:183], a[116:119], v[204:207]\n"
    "ds_write_addtid_b32 v167 offset:3840\n"
    "v_mfma_f32_16x16x32_bf16 v[204:207], a[176:179], a[112:115], v[204:207]\n"
    "ds_read_b128 a[168:171], v240 offset:14336\n"
    "v_mfma_f32_16x16x32_bf16 v[196:199], a[176:179], a[80:83], v[196:199]\n"
    "v_cvt_pk_bf16_f32 v175, v178, v179\n"
    "v_mfma_f32_16x16x32_bf16 v[196:199], a[180:183], a[84:87], v[196:199]\n"
    "ds_write_addtid_b32 v174 offset:4608\n"
    "v_mfma_f32_16x16x32_bf16 v[188:191], a[180:183], a[52:55], v[188:191]\n"
    "ds_write_addtid_b32 v175 offset:4864\n"
    "v_mfma_f32_16x16x32_bf16 v[188:191], a[176:179], a[48:51], v[188:191]\n"
    "v_mfma_f32_16x16x32_bf16 v[180:183], a[176:179], a[16:19], v[180:183]\n"
    "ds_read_b128 a[172:175], v241 offset:14336\n"
    "v_mfma_f32_16x16x32_bf16 v[180:183], a[180:183], a[20:23], v[180:183]\n"
    "s_waitcnt lgkmcnt(10)\n"
    "s_waitcnt lgkmcnt(1)\n"
    "v_mfma_f32_16x16x32_bf16 v[180:183], a[184:187], a[24:27], v[180:183]\n"
    "s_barrier\n"
    "v_mfma_f32_16x16x32_bf16 v[180:183], a[188:191], a[28:31], v[180:183]\n"
    "s_add_u32 m0, s43, 24576\n"
    "v_mfma_f32_16x16x32_bf16 v[188:191], a[188:191], a[60:63], v[188:191]\n"
    "ds_read_b128 a[176:179], v242 offset:10240\n"
    "v_mfma_f32_16x16x32_bf16 v[188:191], a[184:187], a[56:59], v[188:191]\n"
    "buffer_load_dwordx4 v248, s[8:11], s40 offen offset:0 sc1 lds\n"
    "v_mfma_f32_16x16x32_bf16 v[196:199], a[184:187], a[88:91], v[196:199]\n"
    "ds_read2st64_b32 v[144:145], v249 offset0:0 offset1:1\n"
    "v_mfma_f32_16x16x32_bf16 v[196:199], a[188:191], a[92:95], v[196:199]\n"
    "v_mfma_f32_16x16x32_bf16 v[204:207], a[188:191], a[124:127], v[204:207]\n"
    "ds_read2st64_b32 v[146:147], v249 offset0:2 offset1:3\n"
    "v_mfma_f32_16x16x32_bf16 v[204:207], a[184:187], a[120:123], v[204:207]\n"
    "buffer_load_dwordx4 v248, s[8:11], s40 offen offset:1024 sc1 lds\n"
    "v_mfma_f32_16x16x32_bf16 v[212:215], a[184:187], a[152:155], v[212:215]\n"
    "v_mfma_f32_16x16x32_bf16 v[212:215], a[188:191], a[156:159], v[212:215]\n"
    "v_mfma_f32_16x16x32_bf16 v[216:219], a[160:163], a[128:131], v[236:239]\n"
    "ds_read_b128 a[180:183], v243 offset:10240\n"
    "v_mfma_f32_16x16x32_bf16 v[216:219], a[164:167], a[132:135], v[216:219]\n"
    "buffer_load_dwordx4 v248, s[8:11], s40 offen offset:2048 sc1 lds\n"
    "v_mfma_f32_16x16x32_bf16 v[208:211], a[164:167], a[100:103], v[232:235]\n"
    "ds_read2st64_b32 v[152:153], v249 offset0:4 offset1:5\n"
    "v_mfma_f32_16x16x32_bf16 v[208:211], a[160:163], a[96:99], v[208:211]\n"
    "ds_read_b128 a[184:187], v242 offset:14336\n"
    "v_mfma_f32_16x16x32_bf16 v[200:203], a[160:163], a[64:67], v[228:231]\n"
    "ds_read2st64_b32 v[154:155], v249 offset0:6 offset1:7\n"
    "v_mfma_f32_16x16x32_bf16 v[200:203], a[164:167], a[68:71], v[200:203]\n"
    "buffer_load_dwordx4 v248, s[8:11], s40 offen offset:3072 sc1 lds\n"
    "v_mfma_f32_16x16x32_bf16 v[192:195], a[164:167], a[36:39], v[224:227]\n"
    "s_add_u32 m0, s43, 28672\n"
    "v_exp_f32_e32 v180, v180\n"
    "v_mfma_f32_16x16x32_bf16 v[192:195], a[160:163], a[32:35], v[192:195]\n"
    "s_add_u32 s40, s40, 0x8000\n"
    "v_exp_f32_e32 v181, v181\n"
    "v_mfma_f32_16x16x32_bf16 v[184:187], a[160:163], a[0:3], v[220:223]\n"
    "ds_read2st64_b32 v[160:161], v249 offset0:8 offset1:9\n"
    "v_mfma_f32_16x16x32_bf16 v[184:187], a[164:167], a[4:7], v[184:187]\n"
    "s_waitcnt lgkmcnt(8)\n"
    "buffer_load_dwordx4 v248, s[8:11], s41 offen offset:0 sc1 lds\n"
    "v_mfma_f32_16x16x32_bf16 v[184:187], a[168:171], a[8:11], v[184:187]\n"
    "ds_read_b128 a[188:191], v243 offset:14336\n"
    "v_mfma_f32_16x16x32_bf16 v[184:187], a[172:175], a[12:15], v[184:187]\n"
    "ds_read_b64_tr_b16 a[160:161], v244 offset:0\n"
    "v_mfma_f32_16x16x32_bf16 v[192:195], a[172:175], a[44:47], v[192:195]\n"
    "ds_read2st64_b32 v[162:163], v249 offset0:10 offset1:11\n"
    "v_mfma_f32_16x16x32_bf16 v[192:195], a[168:171], a[40:43], v[192:195]\n"
    "buffer_load_dwordx4 v248, s[8:11], s41 offen offset:1024 sc1 lds\n"
    "v_mfma_f32_16x16x32_bf16 v[200:203], a[168:171], a[72:75], v[200:203]\n"
    "ds_read_b64_tr_b16 a[162:163], v244 offset:2048\n"
    "v_mfma_f32_16x16x32_bf16 v[200:203], a[172:175], a[76:79], v[200:203]\n"
    "v_exp_f32_e32 v182, v182\n"
    "v_mfma_f32_16x16x32_bf16 v[208:211], a[172:175], a[108:111], v[208:211]\n"
    "ds_read2st64_b32 v[168:169], v249 offset0:12 offset1:13\n"
    "v_mfma_f32_16x16x32_bf16 v[208:211], a[168:171], a[104:107], v[208:211]\n"
    "buffer_load_dwordx4 v248, s[8:11], s41 offen offset:2048 sc1 lds\n"
    "v_mfma_f32_16x16x32_bf16 v[216:219], a[168:171], a[136:139], v[216:219]\n"
    "ds_read_b64_tr_b16 a[164:165], v244 offset:32768\n"
    "v_mfma_f32_16x16x32_bf16 v[216:219], a[172:175], a[140:143], v[216:219]\n"
    "s_waitcnt lgkmcnt(10)\n"
    "ds_read2st64_b32 v[170:171], v249 offset0:14 offset1:15\n"
    "v_mfma_f32_16x16x32_bf16 v[216:219], a[176:179], a[144:147], v[216:219]\n"
    "ds_read_b64_tr_b16 a[168:169], v245 offset:0\n"
    "v_mfma_f32_16x16x32_bf16 v[216:219], a[180:183], a[148:151], v[216:219]\n"
    "buffer_load_dwordx4 v248, s[8:11], s41 offen offset:3072 sc1 lds\n"
    "v_mfma_f32_16x16x32_bf16 v[208:211], a[180:183], a[116:119], v[208:211]\n"
    "ds_read_b64_tr_b16 a[166:167], v244 offset:34816\n"
    "v_mfma_f32_16x16x32_bf16 v[208:211], a[176:179], a[112:115], v[208:211]\n"
    "s_add_u32 s41, s41, 0x8000\n"
    "ds_read2st64_b32 v[176:177], v249 offset0:16 offset1:17\n"
    "v_mfma_f32_16x16x32_bf16 v[200:203], a[176:179], a[80:83], v[200:203]\n"
    "v_mfma_f32_16x16x32_bf16 v[200:203], a[180:183], a[84:87], v[200:203]\n"
    "ds_read_b64_tr_b16 a[170:171], v245 offset:2048\n"
    "v_mfma_f32_16x16x32_bf16 v[192:195], a[180:183], a[52:55], v[192:195]\n"
    "v_exp_f32_e32 v183, v183\n"
    "v_mfma_f32_16x16x32_bf16 v[192:195], a[176:179], a[48:51], v[192:195]\n"
    "v_cvt_pk_bf16_f32 v180, v180, v181\n"
    "v_mfma_f32_16x16x32_bf16 v[184:187], a[176:179], a[16:19], v[184:187]\n"
    "ds_read_b64_tr_b16 a[172:173], v245 offset:32768\n"
    "v_mfma_f32_16x16x32_bf16 v[184:187], a[180:183], a[20:23], v[184:187]\n"
    "s_waitcnt lgkmcnt(11)\n"
    "ds_read2st64_b32 v[178:179], v249 offset0:18 offset1:19\n"
    "v_mfma_f32_16x16x32_bf16 v[184:187], a[184:187], a[24:27], v[184:187]\n"
    "ds_read_b64_tr_b16 a[176:177], v246 offset:0\n"
    "v_mfma_f32_16x16x32_bf16 v[184:187], a[188:191], a[28:31], v[184:187]\n"
    "ds_read_b64_tr_b16 a[174:175], v245 offset:34816\n"
    "v_mfma_f32_16x16x32_bf16 v[192:195], a[188:191], a[60:63], v[192:195]\n"
    "v_exp_f32_e32 v188, v188\n"
    "v_mfma_f32_16x16x32_bf16 v[192:195], a[184:187], a[56:59], v[192:195]\n"
    "v_exp_f32_e32 v189, v189\n"
    "v_mfma_f32_16x16x32_bf16 v[200:203], a[184:187], a[88:91], v[200:203]\n"
    "ds_read_b64_tr_b16 a[178:179], v246 offset:2048\n"
    "v_mfma_f32_16x16x32_bf16 v[200:203], a[188:191], a[92:95], v[200:203]\n"
    "v_cvt_pk_bf16_f32 v181, v182, v183\n"
    "v_mfma_f32_16x16x32_bf16 v[208:211], a[188:191], a[124:127], v[208:211]\n"
    "v_exp_f32_e32 v190, v190\n"
    "v_mfma_f32_16x16x32_bf16 v[208:211], a[184:187], a[120:123], v[208:211]\n"
    "v_exp_f32_e32 v191, v191\n"
    "v_mfma_f32_16x16x32_bf16 v[216:219], a[184:187], a[152:155], v[216:219]\n"
    "ds_read_b64_tr_b16 a[180:181], v246 offset:32768\n"
    "v_mfma_f32_16x16x32_bf16 v[216:219], a[188:191], a[156:159], v[216:219]\n"
    "s_waitcnt lgkmcnt(4)\n"
    "v_cvt_pk_bf16_f32 v188, v188, v189\n"
    "v_mfma_f32_16x16x32_bf16 a[236:239], a[160:163], v[140:143], a[236:239]\n"
    "ds_read_b64_tr_b16 a[184:185], v247 offset:0\n"
    "v_mfma_f32_16x16x32_bf16 a[236:239], a[164:167], v[144:147], a[236:239]\n"
    "ds_read_b64_tr_b16 a[182:183], v246 offset:34816\n"
    "v_mfma_f32_16x16x32_bf16 a[240:243], a[164:167], v[152:155], a[240:243]\n"
    "v_exp_f32_e32 v196, v196\n"
    "v_mfma_f32_16x16x32_bf16 a[240:243], a[160:163], v[148:151], a[240:243]\n"
    "v_exp_f32_e32 v197, v197\n"
    "v_mfma_f32_16x16x32_bf16 a[244:247], a[160:163], v[156:159], a[244:247]\n"
    "ds_read_b64_tr_b16 a[186:187], v247 offset:2048\n"
    "v_mfma_f32_16x16x32_bf16 a[244:247], a[164:167], v[160:163], a[244:247]\n"
    "v_cvt_pk_bf16_f32 v189, v190, v191\n"
    "v_mfma_f32_16x16x32_bf16 a[248:251], a[164:167], v[168:171], a[248:251]\n"
    "v_exp_f32_e32 v198, v198\n"
    "v_mfma_f32_16x16x32_bf16 a[248:251], a[160:163], v[164:167], a[248:251]\n"
    "v_exp_f32_e32 v199, v199\n"
    "v_mfma_f32_16x16x32_bf16 a[252:255], a[160:163], v[172:175], a[252:255]\n"
    "ds_read_b64_tr_b16 a[188:189], v247 offset:32768\n"
    "v_mfma_f32_16x16x32_bf16 a[252:255], a[164:167], v[176:179], a[252:255]\n"
    "s_waitcnt lgkmcnt(6)\n"
    "v_cvt_pk_bf16_f32 v196, v196, v197\n"
    "v_mfma_f32_16x16x32_bf16 v[16:19], a[172:175], v[176:179], v[16:19]\n"
    "ds_read_b64_tr_b16 a[160:161], v244 offset:4096\n"
    "v_mfma_f32_16x16x32_bf16 v[16:19], a[168:171], v[172:175], v[16:19]\n"
    "ds_read_b64_tr_b16 a[190:191], v247 offset:34816\n"
    "v_mfma_f32_16x16x32_bf16 v[12:15], a[168:171], v[164:167], v[12:15]\n"
    "v_exp_f32_e32 v204, v204\n"
    "v_mfma_f32_16x16x32_bf16 v[12:15], a[172:175], v[168:171], v[12:15]\n"
    "v_exp_f32_e32 v205, v205\n"
    "v_mfma_f32_16x16x32_bf16 v[8:11], a[172:175], v[160:163], v[8:11]\n"
    "ds_read_b64_tr_b16 a[162:163], v244 offset:6144\n"
    "v_mfma_f32_16x16x32_bf16 v[8:11], a[168:171], v[156:159], v[8:11]\n"
    "v_cvt_pk_bf16_f32 v197, v198, v199\n"
    "v_mfma_f32_16x16x32_bf16 v[4:7], a[168:171], v[148:151], v[4:7]\n"
    "v_exp_f32_e32 v206, v206\n"
    "v_mfma_f32_16x16x32_bf16 v[4:7], a[172:175], v[152:155], v[4:7]\n"
    "v_exp_f32_e32 v207, v207\n"
    "v_mfma_f32_16x16x32_bf16 v[0:3], a[172:175], v[144:147], v[0:3]\n"
    "ds_read_b64_tr_b16 a[164:165], v244 offset:36864\n"
    "v_mfma_f32_16x16x32_bf16 v[0:3], a[168:171], v[140:143], v[0:3]\n"
    "s_waitcnt lgkmcnt(6)\n"
    "v_cvt_pk_bf16_f32 v204, v204, v205\n"
    "v_mfma_f32_16x16x32_bf16 v[20:23], a[176:179], v[140:143], v[20:23]\n"
    "ds_read_b64_tr_b16 a[168:169], v245 offset:4096\n"
    "v_mfma_f32_16x16x32_bf16 v[20:23], a[180:183], v[144:147], v[20:23]\n"
    "ds_read_b64_tr_b16 a[166:167], v244 offset:38912\n"
    "v_mfma_f32_16x16x32_bf16 v[24:27], a[180:183], v[152:155], v[24:27]\n"
    "v_exp_f32_e32 v212, v212\n"
    "v_mfma_f32_16x16x32_bf16 v[24:27], a[176:179], v[148:151], v[24:27]\n"
    "v_exp_f32_e32 v213, v213\n"
    "v_mfma_f32_16x16x32_bf16 v[28:31], a[176:179], v[156:159], v[28:31]\n"
    "ds_read_b64_tr_b16 a[170:171], v245 offset:6144\n"
    "v_mfma_f32_16x16x32_bf16 v[28:31], a[180:183], v[160:163], v[28:31]\n"
    "v_cvt_pk_bf16_f32 v205, v206, v207\n"
    "v_mfma_f32_16x16x32_bf16 v[32:35], a[180:183], v[168:171], v[32:35]\n"
    "v_exp_f32_e32 v214, v214\n"
    "v_mfma_f32_16x16x32_bf16 v[32:35], a[176:179], v[164:167], v[32:35]\n"
    "v_exp_f32_e32 v215, v215\n"
    "v_mfma_f32_16x16x32_bf16 v[36:39], a[176:179], v[172:175], v[36:39]\n"
    "ds_read_b64_tr_b16 a[172:173], v245 offset:36864\n"
    "v_mfma_f32_16x16x32_bf16 v[36:39], a[180:183], v[176:179], v[36:39]\n"
    "s_waitcnt lgkmcnt(6)\n"
    "v_cvt_pk_bf16_f32 v212, v212, v213\n"
    "v_mfma_f32_16x16x32_bf16 v[56:59], a[188:191], v[176:179], v[56:59]\n"
    "ds_read_b64_tr_b16 a[176:177], v246 offset:4096\n"
    "v_mfma_f32_16x16x32_bf16 v[56:59], a[184:187], v[172:175], v[56:59]\n"
    "ds_read_b64_tr_b16 a[174:175], v245 offset:38912\n"
    "v_mfma_f32_16x16x32_bf16 v[52:55], a[184:187], v[164:167], v[52:55]\n"
    "v_cvt_pk_bf16_f32 v213, v214, v215\n"
    "v_mfma_f32_16x16x32_bf16 v[52:55], a[188:191], v[168:171], v[52:55]\n"
    "v_exp_f32_e32 v184, v184\n"
    "v_mfma_f32_16x16x32_bf16 v[48:51], a[188:191], v[160:163], v[48:51]\n"
    "ds_read_b64_tr_b16 a[178:179], v246 offset:6144\n"
    "v_mfma_f32_16x16x32_bf16 v[48:51], a[184:187], v[156:159], v[48:51]\n"
    "v_exp_f32_e32 v185, v185\n"
    "v_mfma_f32_16x16x32_bf16 v[44:47], a[184:187], v[148:151], v[44:47]\n"
    "v_mfma_f32_16x16x32_bf16 v[44:47], a[188:191], v[152:155], v[44:47]\n"
    "v_mfma_f32_16x16x32_bf16 v[40:43], a[188:191], v[144:147], v[40:43]\n"
    "ds_read_b64_tr_b16 a[180:181], v246 offset:36864\n"
    "v_mfma_f32_16x16x32_bf16 v[40:43], a[184:187], v[140:143], v[40:43]\n"
    "s_waitcnt lgkmcnt(6)\n"
    "v_exp_f32_e32 v186, v186\n"
    "v_mfma_f32_16x16x32_bf16 v[60:63], a[160:163], v[140:143], v[60:63]\n"
    "ds_read_b64_tr_b16 a[184:185], v247 offset:4096\n"
    "v_mfma_f32_16x16x32_bf16 v[60:63], a[164:167], v[144:147], v[60:63]\n"
    "ds_read_b64_tr_b16 a[182:183], v246 offset:38912\n"
    "v_mfma_f32_16x16x32_bf16 v[64:67], a[164:167], v[152:155], v[64:67]\n"
    "ds_read_b64_tr_b16 a[186:187], v247 offset:6144\n"
    "v_mfma_f32_16x16x32_bf16 v[64:67], a[160:163], v[148:151], v[64:67]\n"
    "v_exp_f32_e32 v187, v187\n"
    "v_mfma_f32_16x16x32_bf16 v[68:71], a[160:163], v[156:159], v[68:71]\n"
    "v_cvt_pk_bf16_f32 v182, v184, v185\n"
    "v_mfma_f32_16x16x32_bf16 v[68:71], a[164:167], v[160:163], v[68:71]\n"
    "ds_read_b64_tr_b16 a[188:189], v247 offset:36864\n"
    "v_mfma_f32_16x16x32_bf16 v[72:75], a[164:167], v[168:171], v[72:75]\n"
    "ds_read_b64_tr_b16 a[190:191], v247 offset:38912\n"
    "v_mfma_f32_16x16x32_bf16 v[72:75], a[160:163], v[164:167], v[72:75]\n"
    "s_waitcnt vmcnt(8)\n"
    "v_mfma_f32_16x16x32_bf16 v[76:79], a[160:163], v[172:175], v[76:79]\n"
    "s_barrier\n"
    "v_mfma_f32_16x16x32_bf16 v[76:79], a[164:167], v[176:179], v[76:79]\n"
    "s_waitcnt lgkmcnt(7)\n"
    "s_mov_b32 m0, s42\n"
    "v_mfma_f32_16x16x32_bf16 v[96:99], a[172:175], v[176:179], v[96:99]\n"
    "v_exp_f32_e32 v192, v192\n"
    "v_mfma_f32_16x16x32_bf16 v[96:99], a[168:171], v[172:175], v[96:99]\n"
    "v_exp_f32_e32 v193, v193\n"
    "v_mfma_f32_16x16x32_bf16 v[92:95], a[168:171], v[164:167], v[92:95]\n"
    "ds_write_addtid_b32 v180 offset:0\n"
    "v_cvt_pk_bf16_f32 v183, v186, v187\n"
    "v_mfma_f32_16x16x32_bf16 v[92:95], a[172:175], v[168:171], v[92:95]\n"
    "ds_read_b128 a[160:163], v240 offset:16384\n"
    "v_mfma_f32_16x16x32_bf16 v[88:91], a[172:175], v[160:163], v[88:91]\n"
    "v_exp_f32_e32 v194, v194\n"
    "v_mfma_f32_16x16x32_bf16 v[88:91], a[168:171], v[156:159], v[88:91]\n"
    "v_exp_f32_e32 v195, v195\n"
    "v_mfma_f32_16x16x32_bf16 v[84:87], a[168:171], v[148:151], v[84:87]\n"
    "ds_write_addtid_b32 v181 offset:256\n"
    "v_mfma_f32_16x16x32_bf16 v[84:87], a[172:175], v[152:155], v[84:87]\n"
    "ds_write_addtid_b32 v188 offset:1024\n"
    "v_mfma_f32_16x16x32_bf16 v[80:83], a[172:175], v[144:147], v[80:83]\n"
    "v_cvt_pk_bf16_f32 v190, v192, v193\n"
    "v_mfma_f32_16x16x32_bf16 v[80:83], a[168:171], v[140:143], v[80:83]\n"
    "s_waitcnt lgkmcnt(7)\n"
    "v_exp_f32_e32 v200, v200\n"
    "v_mfma_f32_16x16x32_bf16 v[100:103], a[176:179], v[140:143], v[100:103]\n"
    "ds_read_b128 a[164:167], v241 offset:16384\n"
    "v_mfma_f32_16x16x32_bf16 v[100:103], a[180:183], v[144:147], v[100:103]\n"
    "ds_write_addtid_b32 v189 offset:1280\n"
    "v_mfma_f32_16x16x32_bf16 v[104:107], a[180:183], v[152:155], v[104:107]\n"
    "v_exp_f32_e32 v201, v201\n"
    "v_mfma_f32_16x16x32_bf16 v[104:107], a[176:179], v[148:151], v[104:107]\n"
    "ds_read_b128 a[168:171], v240 offset:20480\n"
    "v_mfma_f32_16x16x32_bf16 v[108:111], a[176:179], v[156:159], v[108:111]\n"
    "ds_write_addtid_b32 v196 offset:2048\n"
    "v_mfma_f32_16x16x32_bf16 v[108:111], a[180:183], v[160:163], v[108:111]\n"
    "v_cvt_pk_bf16_f32 v191, v194, v195\n"
    "v_mfma_f32_16x16x32_bf16 v[112:115], a[180:183], v[168:171], v[112:115]\n"
    "ds_write_addtid_b32 v197 offset:2304\n"
    "v_mfma_f32_16x16x32_bf16 v[112:115], a[176:179], v[164:167], v[112:115]\n"
    "v_exp_f32_e32 v202, v202\n"
    "v_mfma_f32_16x16x32_bf16 v[116:119], a[176:179], v[172:175], v[116:119]\n"
    "ds_write_addtid_b32 v204 offset:3072\n"
    "v_mfma_f32_16x16x32_bf16 v[116:119], a[180:183], v[176:179], v[116:119]\n"
    "s_waitcnt lgkmcnt(10)\n"
    "v_exp_f32_e32 v203, v203\n"
    "v_mfma_f32_16x16x32_bf16 v[136:139], a[188:191], v[176:179], v[136:139]\n"
    "ds_read_b128 a[172:175], v241 offset:20480\n"
    "v_mfma_f32_16x16x32_bf16 v[136:139], a[184:187], v[172:175], v[136:139]\n"
    "ds_write_addtid_b32 v205 offset:3328\n"
    "v_cvt_pk_bf16_f32 v198, v200, v201\n"
    "v_mfma_f32_16x16x32_bf16 v[132:135], a[184:187], v[164:167], v[132:135]\n"
    "ds_read_b128 a[176:179], v242 offset:16384\n"
    "v_mfma_f32_16x16x32_bf16 v[132:135], a[188:191], v[168:171], v[132:135]\n"
    "ds_write_addtid_b32 v212 offset:4096\n"
    "v_mfma_f32_16x16x32_bf16 v[128:131], a[188:191], v[160:163], v[128:131]\n"
    "v_exp_f32_e32 v208, v208\n"
    "v_mfma_f32_16x16x32_bf16 v[128:131], a[184:187], v[156:159], v[128:131]\n"
    "ds_write_addtid_b32 v213 offset:4352\n"
    "v_mfma_f32_16x16x32_bf16 v[124:127], a[184:187], v[148:151], v[124:127]\n"
    "v_exp_f32_e32 v209, v209\n"
    "v_mfma_f32_16x16x32_bf16 v[124:127], a[188:191], v[152:155], v[124:127]\n"
    "ds_read_b128 a[180:183], v243 offset:16384\n"
    "v_mfma_f32_16x16x32_bf16 v[120:123], a[188:191], v[144:147], v[120:123]\n"
    "ds_write_addtid_b32 v182 offset:512\n"
    "v_mfma_f32_16x16x32_bf16 v[120:123], a[184:187], v[140:143], v[120:123]\n"
    "s_waitcnt lgkmcnt(12)\n"
    "v_cvt_pk_bf16_f32 v199, v202, v203\n"
    "v_mfma_f32_16x16x32_bf16 v[172:175], a[160:163], a[128:131], v[236:239]\n"
    "v_mfma_f32_16x16x32_bf16 v[172:175], a[164:167], a[132:135], v[172:175]\n"
    "v_exp_f32_e32 v210, v210\n"
    "v_mfma_f32_16x16x32_bf16 v[164:167], a[164:167], a[100:103], v[232:235]\n"
    "ds_write_addtid_b32 v183 offset:768\n"
    "v_mfma_f32_16x16x32_bf16 v[164:167], a[160:163], a[96:99], v[164:167]\n"
    "ds_read_b128 a[184:187], v242 offset:20480\n"
    "v_mfma_f32_16x16x32_bf16 v[156:159], a[160:163], a[64:67], v[228:231]\n"
    "v_exp_f32_e32 v211, v211\n"
    "v_mfma_f32_16x16x32_bf16 v[156:159], a[164:167], a[68:71], v[156:159]\n"
    "ds_write_addtid_b32 v190 offset:1536\n"
    "v_mfma_f32_16x16x32_bf16 v[148:151], a[164:167], a[36:39], v[224:227]\n"
    "v_cvt_pk_bf16_f32 v206, v208, v209\n"
    "v_mfma_f32_16x16x32_bf16 v[148:151], a[160:163], a[32:35], v[148:151]\n"
    "v_mfma_f32_16x16x32_bf16 v[140:143], a[160:163], a[0:3], v[220:223]\n"
    "ds_write_addtid_b32 v191 offset:1792\n"
    "v_mfma_f32_16x16x32_bf16 v[140:143], a[164:167], a[4:7], v[140:143]\n"
    "s_waitcnt lgkmcnt(10)\n"
    "v_exp_f32_e32 v216, v216\n"
    "v_mfma_f32_16x16x32_bf16 v[140:143], a[168:171], a[8:11], v[140:143]\n"
    "ds_read_b128 a[188:191], v243 offset:20480\n"
    "v_mfma_f32_16x16x32_bf16 v[140:143], a[172:175], a[12:15], v[140:143]\n"
    "v_exp_f32_e32 v217, v217\n"
    "v_mfma_f32_16x16x32_bf16 v[148:151], a[172:175], a[44:47], v[148:151]\n"
    "ds_write_addtid_b32 v198 offset:2560\n"
    "v_mfma_f32_16x16x32_bf16 v[148:151], a[168:171], a[40:43], v[148:151]\n"
    "ds_read_b128 a[160:163], v240 offset:18432\n"
    "v_mfma_f32_16x16x32_bf16 v[156:159], a[168:171], a[72:75], v[156:159]\n"
    "v_cvt_pk_bf16_f32 v207, v210, v211\n"
    "v_mfma_f32_16x16x32_bf16 v[156:159], a[172:175], a[76:79], v[156:159]\n"
    "ds_write_addtid_b32 v199 offset:2816\n"
    "v_mfma_f32_16x16x32_bf16 v[164:167], a[172:175], a[108:111], v[164:167]\n"
    "v_exp_f32_e32 v218, v218\n"
    "v_mfma_f32_16x16x32_bf16 v[164:167], a[168:171], a[104:107], v[164:167]\n"
    "ds_write_addtid_b32 v206 offset:3584\n"
    "v_mfma_f32_16x16x32_bf16 v[172:175], a[168:171], a[136:139], v[172:175]\n"
    "v_mfma_f32_16x16x32_bf16 v[172:175], a[172:175], a[140:143], v[172:175]\n"
    "s_waitcnt lgkmcnt(10)\n"
    "v_exp_f32_e32 v219, v219\n"
    "v_mfma_f32_16x16x32_bf16 v[172:175], a[176:179], a[144:147], v[172:175]\n"
    "ds_read_b128 a[164:167], v241 offset:18432\n"
    "v_mfma_f32_16x16x32_bf16 v[172:175], a[180:183], a[148:151], v[172:175]\n"
    "v_cvt_pk_bf16_f32 v214, v216, v217\n"
    "v_mfma_f32_16x16x32_bf16 v[164:167], a[180:183], a[116:119], v[164:167]\n"
    "ds_write_addtid_b32 v207 offset:3840\n"
    "v_mfma_f32_16x16x32_bf16 v[164:167], a[176:179], a[112:115], v[164:167]\n"
    "ds_read_b128 a[168:171], v240 offset:22528\n"
    "v_mfma_f32_16x16x32_bf16 v[156:159], a[176:179], a[80:83], v[156:159]\n"
    "v_cvt_pk_bf16_f32 v215, v218, v219\n"
    "v_mfma_f32_16x16x32_bf16 v[156:159], a[180:183], a[84:87], v[156:159]\n"
    "ds_write_addtid_b32 v214 offset:4608\n"
    "v_mfma_f32_16x16x32_bf16 v[148:151], a[180:183], a[52:55], v[148:151]\n"
    "ds_write_addtid_b32 v215 offset:4864\n"
    "v_mfma_f32_16x16x32_bf16 v[148:151], a[176:179], a[48:51], v[148:151]\n"
    "v_mfma_f32_16x16x32_bf16 v[140:143], a[176:179], a[16:19], v[140:143]\n"
    "ds_read_b128 a[172:175], v241 offset:22528\n"
    "v_mfma_f32_16x16x32_bf16 v[140:143], a[180:183], a[20:23], v[140:143]\n"
    "s_waitcnt lgkmcnt(10)\n"
    "s_waitcnt lgkmcnt(1)\n"
    "v_mfma_f32_16x16x32_bf16 v[140:143], a[184:187], a[24:27], v[140:143]\n"
    "s_barrier\n"
    "v_mfma_f32_16x16x32_bf16 v[140:143], a[188:191], a[28:31], v[140:143]\n"
    "s_add_u32 m0, s43, 0\n"
    "v_mfma_f32_16x16x32_bf16 v[148:151], a[188:191], a[60:63], v[148:151]\n"
    "ds_read_b128 a[176:179], v242 offset:18432\n"
    "v_mfma_f32_16x16x32_bf16 v[148:151], a[184:187], a[56:59], v[148:151]\n"
    "buffer_load_dwordx4 v248, s[8:11], s40 offen offset:0 sc1 lds\n"
    "v_mfma_f32_16x16x32_bf16 v[156:159], a[184:187], a[88:91], v[156:159]\n"
    "ds_read2st64_b32 v[184:185], v249 offset0:0 offset1:1\n"
    "v_mfma_f32_16x16x32_bf16 v[156:159], a[188:191], a[92:95], v[156:159]\n"
    "v_mfma_f32_16x16x32_bf16 v[164:167], a[188:191], a[124:127], v[164:167]\n"
    "ds_read2st64_b32 v[186:187], v249 offset0:2 offset1:3\n"
    "v_mfma_f32_16x16x32_bf16 v[164:167], a[184:187], a[120:123], v[164:167]\n"
    "buffer_load_dwordx4 v248, s[8:11], s40 offen offset:1024 sc1 lds\n"
    "v_mfma_f32_16x16x32_bf16 v[172:175], a[184:187], a[152:155], v[172:175]\n"
    "v_mfma_f32_16x16x32_bf16 v[172:175], a[188:191], a[156:159], v[172:175]\n"
    "v_mfma_f32_16x16x32_bf16 v[176:179], a[160:163], a[128:131], v[236:239]\n"
    "ds_read_b128 a[180:183], v243 offset:18432\n"
    "v_mfma_f32_16x16x32_bf16 v[176:179], a[164:167], a[132:135], v[176:179]\n"
    "buffer_load_dwordx4 v248, s[8:11], s40 offen offset:2048 sc1 lds\n"
    "v_mfma_f32_16x16x32_bf16 v[168:171], a[164:167], a[100:103], v[232:235]\n"
    "ds_read2st64_b32 v[192:193], v249 offset0:4 offset1:5\n"
    "v_mfma_f32_16x16x32_bf16 v[168:171], a[160:163], a[96:99], v[168:171]\n"
    "ds_read_b128 a[184:187], v242 offset:22528\n"
    "v_mfma_f32_16x16x32_bf16 v[160:163], a[160:163], a[64:67], v[228:231]\n"
    "ds_read2st64_b32 v[194:195], v249 offset0:6 offset1:7\n"
    "v_mfma_f32_16x16x32_bf16 v[160:163], a[164:167], a[68:71], v[160:163]\n"
    "buffer_load_dwordx4 v248, s[8:11], s40 offen offset:3072 sc1 lds\n"
    "v_mfma_f32_16x16x32_bf16 v[152:155], a[164:167], a[36:39], v[224:227]\n"
    "s_add_u32 m0, s43, 4096\n"
    "v_exp_f32_e32 v140, v140\n"
    "v_mfma_f32_16x16x32_bf16 v[152:155], a[160:163], a[32:35], v[152:155]\n"
    "s_add_u32 s40, s40, 0x8000\n"
    "v_exp_f32_e32 v141, v141\n"
    "v_mfma_f32_16x16x32_bf16 v[144:147], a[160:163], a[0:3], v[220:223]\n"
    "ds_read2st64_b32 v[200:201], v249 offset0:8 offset1:9\n"
    "v_mfma_f32_16x16x32_bf16 v[144:147], a[164:167], a[4:7], v[144:147]\n"
    "s_waitcnt lgkmcnt(8)\n"
    "buffer_load_dwordx4 v248, s[8:11], s41 offen offset:0 sc1 lds\n"
    "v_mfma_f32_16x16x32_bf16 v[144:147], a[168:171], a[8:11], v[144:147]\n"
    "ds_read_b128 a[188:191], v243 offset:22528\n"
    "v_mfma_f32_16x16x32_bf16 v[144:147], a[172:175], a[12:15], v[144:147]\n"
    "ds_read_b64_tr_b16 a[160:161], v244 offset:8192\n"
    "v_mfma_f32_16x16x32_bf16 v[152:155], a[172:175], a[44:47], v[152:155]\n"
    "ds_read2st64_b32 v[202:203], v249 offset0:10 offset1:11\n"
    "v_mfma_f32_16x16x32_bf16 v[152:155], a[168:171], a[40:43], v[152:155]\n"
    "buffer_load_dwordx4 v248, s[8:11], s41 offen offset:1024 sc1 lds\n"
    "v_mfma_f32_16x16x32_bf16 v[160:163], a[168:171], a[72:75], v[160:163]\n"
    "ds_read_b64_tr_b16 a[162:163], v244 offset:10240\n"
    "v_mfma_f32_16x16x32_bf16 v[160:163], a[172:175], a[76:79], v[160:163]\n"
    "v_exp_f32_e32 v142, v142\n"
    "v_mfma_f32_16x16x32_bf16 v[168:171], a[172:175], a[108:111], v[168:171]\n"
    "ds_read2st64_b32 v[208:209], v249 offset0:12 offset1:13\n"
    "v_mfma_f32_16x16x32_bf16 v[168:171], a[168:171], a[104:107], v[168:171]\n"
    "buffer_load_dwordx4 v248, s[8:11], s41 offen offset:2048 sc1 lds\n"
    "v_mfma_f32_16x16x32_bf16 v[176:179], a[168:171], a[136:139], v[176:179]\n"
    "ds_read_b64_tr_b16 a[164:165], v244 offset:40960\n"
    "v_mfma_f32_16x16x32_bf16 v[176:179], a[172:175], a[140:143], v[176:179]\n"
    "s_waitcnt lgkmcnt(10)\n"
    "ds_read2st64_b32 v[210:211], v249 offset0:14 offset1:15\n"
    "v_mfma_f32_16x16x32_bf16 v[176:179], a[176:179], a[144:147], v[176:179]\n"
    "ds_read_b64_tr_b16 a[168:169], v245 offset:8192\n"
    "v_mfma_f32_16x16x32_bf16 v[176:179], a[180:183], a[148:151], v[176:179]\n"
    "buffer_load_dwordx4 v248, s[8:11], s41 offen offset:3072 sc1 lds\n"
    "v_mfma_f32_16x16x32_bf16 v[168:171], a[180:183], a[116:119], v[168:171]\n"
    "ds_read_b64_tr_b16 a[166:167], v244 offset:43008\n"
    "v_mfma_f32_16x16x32_bf16 v[168:171], a[176:179], a[112:115], v[168:171]\n"
    "s_add_u32 s41, s41, 0x8000\n"
    "ds_read2st64_b32 v[216:217], v249 offset0:16 offset1:17\n"
    "v_mfma_f32_16x16x32_bf16 v[160:163], a[176:179], a[80:83], v[160:163]\n"
    "buffer_load_dword v253, off, s[8:11], s82 sc1\n"
    "v_mfma_f32_16x16x32_bf16 v[160:163], a[180:183], a[84:87], v[160:163]\n"
    "ds_read_b64_tr_b16 a[170:171], v245 offset:10240\n"
    "v_mfma_f32_16x16x32_bf16 v[152:155], a[180:183], a[52:55], v[152:155]\n"
    "v_exp_f32_e32 v143, v143\n"
    "v_mfma_f32_16x16x32_bf16 v[152:155], a[176:179], a[48:51], v[152:155]\n"
    "v_cvt_pk_bf16_f32 v140, v140, v141\n"
    "v_mfma_f32_16x16x32_bf16 v[144:147], a[176:179], a[16:19], v[144:147]\n"
    "ds_read_b64_tr_b16 a[172:173], v245 offset:40960\n"
    "v_mfma_f32_16x16x32_bf16 v[144:147], a[180:183], a[20:23], v[144:147]\n"
    "s_waitcnt lgkmcnt(11)\n"
    "ds_read2st64_b32 v[218:219], v249 offset0:18 offset1:19\n"
    "v_mfma_f32_16x16x32_bf16 v[144:147], a[184:187], a[24:27], v[144:147]\n"
    "ds_read_b64_tr_b16 a[176:177], v246 offset:8192\n"
    "v_mfma_f32_16x16x32_bf16 v[144:147], a[188:191], a[28:31], v[144:147]\n"
    "ds_read_b64_tr_b16 a[174:175], v245 offset:43008\n"
    "v_mfma_f32_16x16x32_bf16 v[152:155], a[188:191], a[60:63], v[152:155]\n"
    "v_exp_f32_e32 v148, v148\n"
    "v_mfma_f32_16x16x32_bf16 v[152:155], a[184:187], a[56:59], v[152:155]\n"
    "v_exp_f32_e32 v149, v149\n"
    "v_mfma_f32_16x16x32_bf16 v[160:163], a[184:187], a[88:91], v[160:163]\n"
    "ds_read_b64_tr_b16 a[178:179], v246 offset:10240\n"
    "v_mfma_f32_16x16x32_bf16 v[160:163], a[188:191], a[92:95], v[160:163]\n"
    "v_cvt_pk_bf16_f32 v141, v142, v143\n"
    "v_mfma_f32_16x16x32_bf16 v[168:171], a[188:191], a[124:127], v[168:171]\n"
    "v_exp_f32_e32 v150, v150\n"
    "v_mfma_f32_16x16x32_bf16 v[168:171], a[184:187], a[120:123], v[168:171]\n"
    "v_exp_f32_e32 v151, v151\n"
    "v_mfma_f32_16x16x32_bf16 v[176:179], a[184:187], a[152:155], v[176:179]\n"
    "ds_read_b64_tr_b16 a[180:181], v246 offset:40960\n"
    "v_mfma_f32_16x16x32_bf16 v[176:179], a[188:191], a[156:159], v[176:179]\n"
    "s_waitcnt lgkmcnt(4)\n"
    "v_cvt_pk_bf16_f32 v148, v148, v149\n"
    "v_mfma_f32_16x16x32_bf16 a[236:239], a[160:163], v[180:183], a[236:239]\n"
    "ds_read_b64_tr_b16 a[184:185], v247 offset:8192\n"
    "v_mfma_f32_16x16x32_bf16 a[236:239], a[164:167], v[184:187], a[236:239]\n"
    "ds_read_b64_tr_b16 a[182:183], v246 offset:43008\n"
    "v_mfma_f32_16x16x32_bf16 a[240:243], a[164:167], v[192:195], a[240:243]\n"
    "v_exp_f32_e32 v156, v156\n"
    "v_mfma_f32_16x16x32_bf16 a[240:243], a[160:163], v[188:191], a[240:243]\n"
    "v_exp_f32_e32 v157, v157\n"
    "v_mfma_f32_16x16x32_bf16 a[244:247], a[160:163], v[196:199], a[244:247]\n"
    "ds_read_b64_tr_b16 a[186:187], v247 offset:10240\n"
    "v_mfma_f32_16x16x32_bf16 a[244:247], a[164:167], v[200:203], a[244:247]\n"
    "v_cvt_pk_bf16_f32 v149, v150, v151\n"
    "v_mfma_f32_16x16x32_bf16 a[248:251], a[164:167], v[208:211], a[248:251]\n"
    "v_exp_f32_e32 v158, v158\n"
    "v_mfma_f32_16x16x32_bf16 a[248:251], a[160:163], v[204:207], a[248:251]\n"
    "v_exp_f32_e32 v159, v159\n"
    "v_mfma_f32_16x16x32_bf16 a[252:255], a[160:163], v[212:215], a[252:255]\n"
    "ds_read_b64_tr_b16 a[188:189], v247 offset:40960\n"
    "v_mfma_f32_16x16x32_bf16 a[252:255], a[164:167], v[216:219], a[252:255]\n"
    "s_waitcnt lgkmcnt(6)\n"
    "v_cvt_pk_bf16_f32 v156, v156, v157\n"
    "v_mfma_f32_16x16x32_bf16 v[16:19], a[172:175], v[216:219], v[16:19]\n"
    "ds_read_b64_tr_b16 a[160:161], v244 offset:12288\n"
    "v_mfma_f32_16x16x32_bf16 v[16:19], a[168:171], v[212:215], v[16:19]\n"
    "ds_read_b64_tr_b16 a[190:191], v247 offset:43008\n"
    "v_mfma_f32_16x16x32_bf16 v[12:15], a[168:171], v[204:207], v[12:15]\n"
    "v_exp_f32_e32 v164, v164\n"
    "v_mfma_f32_16x16x32_bf16 v[12:15], a[172:175], v[208:211], v[12:15]\n"
    "v_exp_f32_e32 v165, v165\n"
    "v_mfma_f32_16x16x32_bf16 v[8:11], a[172:175], v[200:203], v[8:11]\n"
    "ds_read_b64_tr_b16 a[162:163], v244 offset:14336\n"
    "v_mfma_f32_16x16x32_bf16 v[8:11], a[168:171], v[196:199], v[8:11]\n"
    "v_cvt_pk_bf16_f32 v157, v158, v159\n"
    "v_mfma_f32_16x16x32_bf16 v[4:7], a[168:171], v[188:191], v[4:7]\n"
    "v_exp_f32_e32 v166, v166\n"
    "v_mfma_f32_16x16x32_bf16 v[4:7], a[172:175], v[192:195], v[4:7]\n"
    "v_exp_f32_e32 v167, v167\n"
    "v_mfma_f32_16x16x32_bf16 v[0:3], a[172:175], v[184:187], v[0:3]\n"
    "ds_read_b64_tr_b16 a[164:165], v244 offset:45056\n"
    "v_mfma_f32_16x16x32_bf16 v[0:3], a[168:171], v[180:183], v[0:3]\n"
    "s_waitcnt lgkmcnt(6)\n"
    "v_cvt_pk_bf16_f32 v164, v164, v165\n"
    "v_mfma_f32_16x16x32_bf16 v[20:23], a[176:179], v[180:183], v[20:23]\n"
    "ds_read_b64_tr_b16 a[168:169], v245 offset:12288\n"
    "v_mfma_f32_16x16x32_bf16 v[20:23], a[180:183], v[184:187], v[20:23]\n"
    "ds_read_b64_tr_b16 a[166:167], v244 offset:47104\n"
    "v_mfma_f32_16x16x32_bf16 v[24:27], a[180:183], v[192:195], v[24:27]\n"
    "v_exp_f32_e32 v172, v172\n"
    "v_mfma_f32_16x16x32_bf16 v[24:27], a[176:179], v[188:191], v[24:27]\n"
    "v_exp_f32_e32 v173, v173\n"
    "v_mfma_f32_16x16x32_bf16 v[28:31], a[176:179], v[196:199], v[28:31]\n"
    "ds_read_b64_tr_b16 a[170:171], v245 offset:14336\n"
    "v_mfma_f32_16x16x32_bf16 v[28:31], a[180:183], v[200:203], v[28:31]\n"
    "v_cvt_pk_bf16_f32 v165, v166, v167\n"
    "v_mfma_f32_16x16x32_bf16 v[32:35], a[180:183], v[208:211], v[32:35]\n"
    "v_exp_f32_e32 v174, v174\n"
    "v_mfma_f32_16x16x32_bf16 v[32:35], a[176:179], v[204:207], v[32:35]\n"
    "v_exp_f32_e32 v175, v175\n"
    "v_mfma_f32_16x16x32_bf16 v[36:39], a[176:179], v[212:215], v[36:39]\n"
    "ds_read_b64_tr_b16 a[172:173], v245 offset:45056\n"
    "v_mfma_f32_16x16x32_bf16 v[36:39], a[180:183], v[216:219], v[36:39]\n"
    "s_waitcnt lgkmcnt(6)\n"
    "v_cvt_pk_bf16_f32 v172, v172, v173\n"
    "v_mfma_f32_16x16x32_bf16 v[56:59], a[188:191], v[216:219], v[56:59]\n"
    "ds_read_b64_tr_b16 a[176:177], v246 offset:12288\n"
    "v_mfma_f32_16x16x32_bf16 v[56:59], a[184:187], v[212:215], v[56:59]\n"
    "ds_read_b64_tr_b16 a[174:175], v245 offset:47104\n"
    "v_mfma_f32_16x16x32_bf16 v[52:55], a[184:187], v[204:207], v[52:55]\n"
    "v_cvt_pk_bf16_f32 v173, v174, v175\n"
    "v_mfma_f32_16x16x32_bf16 v[52:55], a[188:191], v[208:211], v[52:55]\n"
    "v_exp_f32_e32 v144, v144\n"
    "v_mfma_f32_16x16x32_bf16 v[48:51], a[188:191], v[200:203], v[48:51]\n"
    "s_waitcnt vmcnt(0)\n"
    "ds_read_b64_tr_b16 a[178:179], v246 offset:14336\n"
    "v_mfma_f32_16x16x32_bf16 v[48:51], a[184:187], v[196:199], v[48:51]\n"
    "s_mov_b32 m0, s30\n"
    "v_exp_f32_e32 v145, v145\n"
    "v_mfma_f32_16x16x32_bf16 v[44:47], a[184:187], v[188:191], v[44:47]\n"
    "buffer_load_dwordx4 v250, s[8:11], s83 offen offset:0 sc1 lds\n"
    "v_mfma_f32_16x16x32_bf16 v[44:47], a[188:191], v[192:195], v[44:47]\n"
    "v_readfirstlane_b32 s81, v253\n"
    "v_mfma_f32_16x16x32_bf16 v[40:43], a[188:191], v[184:187], v[40:43]\n"
    "ds_read_b64_tr_b16 a[180:181], v246 offset:45056\n"
    "v_mfma_f32_16x16x32_bf16 v[40:43], a[184:187], v[180:183], v[40:43]\n"
    "s_waitcnt lgkmcnt(6)\n"
    "v_exp_f32_e32 v146, v146\n"
    "v_mfma_f32_16x16x32_bf16 v[60:63], a[160:163], v[180:183], v[60:63]\n"
    "ds_read_b64_tr_b16 a[184:185], v247 offset:12288\n"
    "v_mfma_f32_16x16x32_bf16 v[60:63], a[164:167], v[184:187], v[60:63]\n"
    "ds_read_b64_tr_b16 a[182:183], v246 offset:47104\n"
    "v_mfma_f32_16x16x32_bf16 v[64:67], a[164:167], v[192:195], v[64:67]\n"
    "ds_read_b64_tr_b16 a[186:187], v247 offset:14336\n"
    "v_mfma_f32_16x16x32_bf16 v[64:67], a[160:163], v[188:191], v[64:67]\n"
    "v_exp_f32_e32 v147, v147\n"
    "v_mfma_f32_16x16x32_bf16 v[68:71], a[160:163], v[196:199], v[68:71]\n"
    "v_cvt_pk_bf16_f32 v142, v144, v145\n"
    "v_mfma_f32_16x16x32_bf16 v[68:71], a[164:167], v[200:203], v[68:71]\n"
    "ds_read_b64_tr_b16 a[188:189], v247 offset:45056\n"
    "v_mfma_f32_16x16x32_bf16 v[72:75], a[164:167], v[208:211], v[72:75]\n"
    "ds_read_b64_tr_b16 a[190:191], v247 offset:47104\n"
    "v_mfma_f32_16x16x32_bf16 v[72:75], a[160:163], v[204:207], v[72:75]\n"
    "s_waitcnt vmcnt(10)\n"
    "v_mfma_f32_16x16x32_bf16 v[76:79], a[160:163], v[212:215], v[76:79]\n"
    "s_barrier\n"
    "v_mfma_f32_16x16x32_bf16 v[76:79], a[164:167], v[216:219], v[76:79]\n"
    "s_waitcnt lgkmcnt(7)\n"
    "s_mov_b32 m0, s42\n"
    "v_mfma_f32_16x16x32_bf16 v[96:99], a[172:175], v[216:219], v[96:99]\n"
    "v_exp_f32_e32 v152, v152\n"
    "v_mfma_f32_16x16x32_bf16 v[96:99], a[168:171], v[212:215], v[96:99]\n"
    "v_exp_f32_e32 v153, v153\n"
    "v_mfma_f32_16x16x32_bf16 v[92:95], a[168:171], v[204:207], v[92:95]\n"
    "ds_write_addtid_b32 v140 offset:0\n"
    "v_cvt_pk_bf16_f32 v143, v146, v147\n"
    "v_mfma_f32_16x16x32_bf16 v[92:95], a[172:175], v[208:211], v[92:95]\n"
    "ds_read_b128 a[160:163], v240 offset:24576\n"
    "v_mfma_f32_16x16x32_bf16 v[88:91], a[172:175], v[200:203], v[88:91]\n"
    "v_exp_f32_e32 v154, v154\n"
    "v_mfma_f32_16x16x32_bf16 v[88:91], a[168:171], v[196:199], v[88:91]\n"
    "v_exp_f32_e32 v155, v155\n"
    "v_mfma_f32_16x16x32_bf16 v[84:87], a[168:171], v[188:191], v[84:87]\n"
    "ds_write_addtid_b32 v141 offset:256\n"
    "v_mfma_f32_16x16x32_bf16 v[84:87], a[172:175], v[192:195], v[84:87]\n"
    "ds_write_addtid_b32 v148 offset:1024\n"
    "v_mfma_f32_16x16x32_bf16 v[80:83], a[172:175], v[184:187], v[80:83]\n"
    "v_cvt_pk_bf16_f32 v150, v152, v153\n"
    "v_mfma_f32_16x16x32_bf16 v[80:83], a[168:171], v[180:183], v[80:83]\n"
    "s_waitcnt lgkmcnt(7)\n"
    "v_exp_f32_e32 v160, v160\n"
    "v_mfma_f32_16x16x32_bf16 v[100:103], a[176:179], v[180:183], v[100:103]\n"
    "ds_read_b128 a[164:167], v241 offset:24576\n"
    "v_mfma_f32_16x16x32_bf16 v[100:103], a[180:183], v[184:187], v[100:103]\n"
    "ds_write_addtid_b32 v149 offset:1280\n"
    "v_mfma_f32_16x16x32_bf16 v[104:107], a[180:183], v[192:195], v[104:107]\n"
    "v_exp_f32_e32 v161, v161\n"
    "v_mfma_f32_16x16x32_bf16 v[104:107], a[176:179], v[188:191], v[104:107]\n"
    "ds_read_b128 a[168:171], v240 offset:28672\n"
    "v_mfma_f32_16x16x32_bf16 v[108:111], a[176:179], v[196:199], v[108:111]\n"
    "ds_write_addtid_b32 v156 offset:2048\n"
    "v_mfma_f32_16x16x32_bf16 v[108:111], a[180:183], v[200:203], v[108:111]\n"
    "v_cvt_pk_bf16_f32 v151, v154, v155\n"
    "v_mfma_f32_16x16x32_bf16 v[112:115], a[180:183], v[208:211], v[112:115]\n"
    "ds_write_addtid_b32 v157 offset:2304\n"
    "v_mfma_f32_16x16x32_bf16 v[112:115], a[176:179], v[204:207], v[112:115]\n"
    "v_exp_f32_e32 v162, v162\n"
    "v_mfma_f32_16x16x32_bf16 v[116:119], a[176:179], v[212:215], v[116:119]\n"
    "ds_write_addtid_b32 v164 offset:3072\n"
    "v_mfma_f32_16x16x32_bf16 v[116:119], a[180:183], v[216:219], v[116:119]\n"
    "s_waitcnt lgkmcnt(10)\n"
    "v_exp_f32_e32 v163, v163\n"
    "v_mfma_f32_16x16x32_bf16 v[136:139], a[188:191], v[216:219], v[136:139]\n"
    "ds_read_b128 a[172:175], v241 offset:28672\n"
    "v_mfma_f32_16x16x32_bf16 v[136:139], a[184:187], v[212:215], v[136:139]\n"
    "ds_write_addtid_b32 v165 offset:3328\n"
    "v_cvt_pk_bf16_f32 v158, v160, v161\n"
    "v_mfma_f32_16x16x32_bf16 v[132:135], a[184:187], v[204:207], v[132:135]\n"
    "ds_read_b128 a[176:179], v242 offset:24576\n"
    "v_mfma_f32_16x16x32_bf16 v[132:135], a[188:191], v[208:211], v[132:135]\n"
    "ds_write_addtid_b32 v172 offset:4096\n"
    "v_mfma_f32_16x16x32_bf16 v[128:131], a[188:191], v[200:203], v[128:131]\n"
    "v_exp_f32_e32 v168, v168\n"
    "v_mfma_f32_16x16x32_bf16 v[128:131], a[184:187], v[196:199], v[128:131]\n"
    "s_sub_u32 s44, s44, 1\n"
    "ds_write_addtid_b32 v173 offset:4352\n"
    "v_mfma_f32_16x16x32_bf16 v[124:127], a[184:187], v[188:191], v[124:127]\n"
    "s_cmp_eq_u32 s44, s80\n"
    "v_exp_f32_e32 v169, v169\n"
    "v_mfma_f32_16x16x32_bf16 v[124:127], a[188:191], v[192:195], v[124:127]\n"
    "s_cselect_b32 s88, s39, s81\n"
    "ds_read_b128 a[180:183], v243 offset:24576\n"
    "v_mfma_f32_16x16x32_bf16 v[120:123], a[188:191], v[184:187], v[120:123]\n"
    "s_cmp_lg_u32 s88, s39\n"
    "ds_write_addtid_b32 v142 offset:512\n"
    "v_mfma_f32_16x16x32_bf16 v[120:123], a[184:187], v[180:183], v[120:123]\n"
    "s_waitcnt lgkmcnt(12)\n"
    "v_cvt_pk_bf16_f32 v159, v162, v163\n"
    "s_cbranch_scc1 .Lat_loop\n"
    "s_cmp_lg_u32 s44, 0\n"
    "s_cbranch_scc1 .Lat_g1\n"
    ".Lat_tail:\n"
    "v_mfma_f32_16x16x32_bf16 v[212:215], a[160:163], a[128:131], v[236:239]\n"
    "v_and_b32_e32 v189, 0x1f0, v250\n"
    "v_lshlrev_b32_e32 v190, 3, v250\n"
    "v_mfma_f32_16x16x32_bf16 v[212:215], a[164:167], a[132:135], v[212:215]\n"
    "ds_write_addtid_b32 v143 offset:768\n"
    "v_exp_f32_e32 v170, v170\n"
    "v_mfma_f32_16x16x32_bf16 v[204:207], a[164:167], a[100:103], v[232:235]\n"
    "s_mov_b32 s52, 6100992\n"
    "ds_read_b128 a[184:187], v242 offset:28672\n"
    "v_lshrrev_b32_e32 v253, 4, v250\n"
    "v_mfma_f32_16x16x32_bf16 v[204:207], a[160:163], a[96:99], v[204:207]\n"
    "s_lshr_b32 s81, s34, 6\n"
    "v_bfe_u32 v251, v253, 4, 2\n"
    "v_lshlrev_b32_e32 v251, 1, v251\n"
    "v_mfma_f32_16x16x32_bf16 v[196:199], a[160:163], a[64:67], v[228:231]\n"
    "s_lshl_b32 s81, s81, 9\n"
    "ds_write_addtid_b32 v150 offset:1536\n"
    "v_exp_f32_e32 v171, v171\n"
    "v_mfma_f32_16x16x32_bf16 v[196:199], a[164:167], a[68:71], v[196:199]\n"
    "s_waitcnt lgkmcnt(9)\n"
    "ds_read_b128 a[160:163], v240 offset:26624\n"
    "v_cvt_pk_bf16_f32 v166, v168, v169\n"
    "v_mfma_f32_16x16x32_bf16 v[196:199], a[168:171], a[72:75], v[196:199]\n"
    "s_add_u32 s81, s81, 6074368\n"
    "ds_read_b128 a[188:191], v243 offset:28672\n"
    "v_xor_b32_e32 v251, v251, v253\n"
    "v_mfma_f32_16x16x32_bf16 v[196:199], a[172:175], a[76:79], v[196:199]\n"
    "s_lshl_b32 s63, s93, 13\n"
    "v_exp_f32_e32 v176, v176\n"
    "v_mfma_f32_16x16x32_bf16 v[204:207], a[172:175], a[108:111], v[204:207]\n"
    "s_xor_b32 s45, s63, 0x4000\n"
    "v_and_b32_e32 v251, 7, v251\n"
    "v_lshlrev_b32_e32 v251, 4, v251\n"
    "v_mfma_f32_16x16x32_bf16 v[204:207], a[168:171], a[104:107], v[204:207]\n"
    "s_mul_i32 s58, s93, 33792\n"
    "ds_read_b128 a[164:167], v241 offset:26624\n"
    "v_lshrrev_b32_e32 v253, 3, v253\n"
    "v_mfma_f32_16x16x32_bf16 v[212:215], a[168:171], a[136:139], v[212:215]\n"
    "s_add_u32 s57, s58, 5513216\n"
    "v_exp_f32_e32 v177, v177\n"
    "v_mfma_f32_16x16x32_bf16 v[212:215], a[172:175], a[140:143], v[212:215]\n"
    "s_waitcnt lgkmcnt(7)\n"
    "v_lshl_add_u32 v251, v253, 9, v251\n"
    "v_bfe_u32 v255, v250, 2, 4\n"
    "v_mfma_f32_16x16x32_bf16 v[212:215], a[176:179], a[144:147], v[212:215]\n"
    "s_add_u32 s58, s58, 5378048\n"
    "v_cvt_pk_bf16_f32 v167, v170, v171\n"
    "v_mov_b32_e32 v254, s39\n"
    "v_mfma_f32_16x16x32_bf16 v[212:215], a[180:183], a[148:151], v[212:215]\n"
    "s_add_u32 s36, s35, 0x8000\n"
    "ds_write_addtid_b32 v151 offset:1792\n"
    "v_lshrrev_b32_e32 v252, 2, v250\n"
    "v_mfma_f32_16x16x32_bf16 v[204:207], a[180:183], a[116:119], v[204:207]\n"
    "s_add_u32 s37, s35, 0x10000\n"
    "v_exp_f32_e32 v178, v178\n"
    "v_mfma_f32_16x16x32_bf16 v[204:207], a[176:179], a[112:115], v[204:207]\n"
    "s_cmp_lt_u32 s32, 2\n"
    "s_cselect_b32 s37, s37, 2.0\n"
    "ds_write_addtid_b32 v158 offset:2560\n"
    "v_mfma_f32_16x16x32_bf16 v[196:199], a[176:179], a[80:83], v[196:199]\n"
    "buffer_load_dword v253, v255, s[8:11], s52 offen offset:0 sc1\n"
    "v_mfma_f32_16x16x32_bf16 v[196:199], a[180:183], a[84:87], v[196:199]\n"
    "s_waitcnt lgkmcnt(3)\n"
    "v_exp_f32_e32 v179, v179\n"
    "v_mfma_f32_16x16x32_bf16 v[196:199], a[184:187], a[88:91], v[196:199]\n"
    "ds_write_addtid_b32 v159 offset:2816\n"
    "v_cvt_pk_bf16_f32 v174, v176, v177\n"
    "v_mfma_f32_16x16x32_bf16 v[196:199], a[188:191], a[92:95], v[196:199]\n"
    "ds_write_addtid_b32 v166 offset:3584\n"
    "v_cvt_pk_bf16_f32 v175, v178, v179\n"
    "v_mfma_f32_16x16x32_bf16 v[204:207], a[188:191], a[124:127], v[204:207]\n"
    "ds_read_b128 a[168:171], v240 offset:30720\n"
    "ds_write_addtid_b32 v167 offset:3840\n"
    "v_mfma_f32_16x16x32_bf16 v[204:207], a[184:187], a[120:123], v[204:207]\n"
    "ds_read_b128 a[172:175], v241 offset:30720\n"
    "v_mfma_f32_16x16x32_bf16 v[212:215], a[184:187], a[152:155], v[212:215]\n"
    "v_exp_f32_e32 v196, v196\n"
    "v_mfma_f32_16x16x32_bf16 v[212:215], a[188:191], a[156:159], v[212:215]\n"
    "s_waitcnt lgkmcnt(7)\n"
    "ds_write_addtid_b32 v174 offset:4608\n"
    "v_mfma_f32_16x16x32_bf16 v[216:219], a[160:163], a[128:131], v[236:239]\n"
    "ds_write_addtid_b32 v175 offset:4864\n"
    "v_mfma_f32_16x16x32_bf16 v[216:219], a[164:167], a[132:135], v[216:219]\n"
    "ds_read_b128 a[176:179], v242 offset:26624\n"
    "ds_read_b128 a[184:187], v242 offset:30720\n"
    "v_mfma_f32_16x16x32_bf16 v[208:211], a[164:167], a[100:103], v[232:235]\n"
    "ds_read_b128 a[180:183], v243 offset:26624\n"
    "v_mfma_f32_16x16x32_bf16 v[208:211], a[160:163], a[96:99], v[208:211]\n"
    "s_waitcnt lgkmcnt(3)\n"
    "v_mfma_f32_16x16x32_bf16 v[200:203], a[160:163], a[64:67], v[228:231]\n"
    "s_barrier\n"
    "v_mfma_f32_16x16x32_bf16 v[200:203], a[164:167], a[68:71], v[200:203]\n"
    "ds_read2st64_b32 v[178:179], v249 offset0:18 offset1:19\n"
    "v_mfma_f32_16x16x32_bf16 v[200:203], a[168:171], a[72:75], v[200:203]\n"
    "s_add_u32 m0, s43, 8192\n"
    "ds_read_b128 a[188:191], v243 offset:30720\n"
    "v_mfma_f32_16x16x32_bf16 v[200:203], a[172:175], a[76:79], v[200:203]\n"
    "buffer_load_dwordx4 v248, s[8:11], s40 offen offset:0 sc1 lds\n"
    "v_mfma_f32_16x16x32_bf16 v[208:211], a[172:175], a[108:111], v[208:211]\n"
    "ds_read2st64_b32 v[176:177], v249 offset0:16 offset1:17\n"
    "v_mfma_f32_16x16x32_bf16 v[208:211], a[168:171], a[104:107], v[208:211]\n"
    "ds_read2st64_b32 v[168:169], v249 offset0:12 offset1:13\n"
    "ds_read2st64_b32 v[170:171], v249 offset0:14 offset1:15\n"
    "v_mfma_f32_16x16x32_bf16 v[216:219], a[168:171], a[136:139], v[216:219]\n"
    "ds_read2st64_b32 v[162:163], v249 offset0:10 offset1:11\n"
    "v_mfma_f32_16x16x32_bf16 v[216:219], a[172:175], a[140:143], v[216:219]\n"
    "s_waitcnt lgkmcnt(6)\n"
    "ds_read2st64_b32 v[160:161], v249 offset0:8 offset1:9\n"
    "v_mfma_f32_16x16x32_bf16 v[216:219], a[176:179], a[144:147], v[216:219]\n"
    "buffer_load_dwordx4 v248, s[8:11], s40 offen offset:1024 sc1 lds\n"
    "v_mfma_f32_16x16x32_bf16 v[216:219], a[180:183], a[148:151], v[216:219]\n"
    "ds_read_b64_tr_b16 a[160:161], v244 offset:16384\n"
    "ds_read2st64_b32 v[154:155], v249 offset0:6 offset1:7\n"
    "v_mfma_f32_16x16x32_bf16 v[208:211], a[180:183], a[116:119], v[208:211]\n"
    "ds_read_b64_tr_b16 a[162:163], v244 offset:18432\n"
    "v_mfma_f32_16x16x32_bf16 v[208:211], a[176:179], a[112:115], v[208:211]\n"
    "ds_read_b64_tr_b16 a[164:165], v244 offset:49152\n"
    "ds_read2st64_b32 v[152:153], v249 offset0:4 offset1:5\n"
    "v_mfma_f32_16x16x32_bf16 v[200:203], a[176:179], a[80:83], v[200:203]\n"
    "ds_read_b64_tr_b16 a[166:167], v244 offset:51200\n"
    "ds_read2st64_b32 v[146:147], v249 offset0:2 offset1:3\n"
    "v_mfma_f32_16x16x32_bf16 v[200:203], a[180:183], a[84:87], v[200:203]\n"
    "s_waitcnt lgkmcnt(12)\n"
    "ds_read2st64_b32 v[144:145], v249 offset0:0 offset1:1\n"
    "v_mfma_f32_16x16x32_bf16 v[200:203], a[184:187], a[88:91], v[200:203]\n"
    "buffer_load_dwordx4 v248, s[8:11], s40 offen offset:2048 sc1 lds\n"
    "v_mfma_f32_16x16x32_bf16 v[200:203], a[188:191], a[92:95], v[200:203]\n"
    "ds_read_b64_tr_b16 a[168:169], v245 offset:16384\n"
    "v_mfma_f32_16x16x32_bf16 v[208:211], a[188:191], a[124:127], v[208:211]\n"
    "ds_read_b64_tr_b16 a[170:171], v245 offset:18432\n"
    "v_mfma_f32_16x16x32_bf16 v[208:211], a[184:187], a[120:123], v[208:211]\n"
    "v_exp_f32_e32 v197, v197\n"
    "v_mfma_f32_16x16x32_bf16 v[216:219], a[184:187], a[152:155], v[216:219]\n"
    "ds_read_b64_tr_b16 a[176:177], v246 offset:16384\n"
    "v_mfma_f32_16x16x32_bf16 v[216:219], a[188:191], a[156:159], v[216:219]\n"
    "s_waitcnt lgkmcnt(3)\n"
    "ds_read_b64_tr_b16 a[178:179], v246 offset:18432\n"
    "v_mfma_f32_16x16x32_bf16 a[252:255], a[160:163], v[172:175], a[252:255]\n"
    "buffer_load_dwordx4 v248, s[8:11], s40 offen offset:3072 sc1 lds\n"
    "v_mfma_f32_16x16x32_bf16 a[252:255], a[164:167], v[176:179], a[252:255]\n"
    "s_add_u32 s40, s40, 0x8000\n"
    "ds_read_b64_tr_b16 a[172:173], v245 offset:49152\n"
    "v_mfma_f32_16x16x32_bf16 a[248:251], a[164:167], v[168:171], a[248:251]\n"
    "s_add_u32 m0, s43, 12288\n"
    "ds_read_b64_tr_b16 a[174:175], v245 offset:51200\n"
    "v_mfma_f32_16x16x32_bf16 a[248:251], a[160:163], v[164:167], a[248:251]\n"
    "ds_read_b64_tr_b16 a[184:185], v247 offset:16384\n"
    "v_mfma_f32_16x16x32_bf16 a[244:247], a[160:163], v[156:159], a[244:247]\n"
    "v_exp_f32_e32 v198, v198\n"
    "v_mfma_f32_16x16x32_bf16 a[244:247], a[164:167], v[160:163], a[244:247]\n"
    "buffer_load_dwordx4 v248, s[8:11], s41 offen offset:0 sc1 lds\n"
    "v_mfma_f32_16x16x32_bf16 a[240:243], a[164:167], v[152:155], a[240:243]\n"
    "v_exp_f32_e32 v199, v199\n"
    "v_mfma_f32_16x16x32_bf16 a[240:243], a[160:163], v[148:151], a[240:243]\n"
    "ds_read_b64_tr_b16 a[180:181], v246 offset:49152\n"
    "v_cvt_pk_bf16_f32 v196, v196, v197\n"
    "v_mfma_f32_16x16x32_bf16 a[236:239], a[160:163], v[140:143], a[236:239]\n"
    "ds_read_b64_tr_b16 a[182:183], v246 offset:51200\n"
    "v_mfma_f32_16x16x32_bf16 a[236:239], a[164:167], v[144:147], a[236:239]\n"
    "s_waitcnt lgkmcnt(3)\n"
    "v_exp_f32_e32 v204, v204\n"
    "v_mfma_f32_16x16x32_bf16 v[0:3], a[168:171], v[140:143], v[0:3]\n"
    "buffer_load_dwordx4 v248, s[8:11], s41 offen offset:1024 sc1 lds\n"
    "v_mfma_f32_16x16x32_bf16 v[0:3], a[172:175], v[144:147], v[0:3]\n"
    "ds_read_b64_tr_b16 a[186:187], v247 offset:18432\n"
    "v_mfma_f32_16x16x32_bf16 v[4:7], a[172:175], v[152:155], v[4:7]\n"
    "v_exp_f32_e32 v205, v205\n"
    "v_mfma_f32_16x16x32_bf16 v[4:7], a[168:171], v[148:151], v[4:7]\n"
    "ds_read_b64_tr_b16 a[188:189], v247 offset:49152\n"
    "v_cvt_pk_bf16_f32 v197, v198, v199\n"
    "v_mfma_f32_16x16x32_bf16 v[8:11], a[168:171], v[156:159], v[8:11]\n"
    "ds_read_b64_tr_b16 a[190:191], v247 offset:51200\n"
    "v_mfma_f32_16x16x32_bf16 v[8:11], a[172:175], v[160:163], v[8:11]\n"
    "buffer_load_dwordx4 v248, s[8:11], s41 offen offset:2048 sc1 lds\n"
    "v_mfma_f32_16x16x32_bf16 v[12:15], a[172:175], v[168:171], v[12:15]\n"
    "v_exp_f32_e32 v206, v206\n"
    "v_mfma_f32_16x16x32_bf16 v[12:15], a[168:171], v[164:167], v[12:15]\n"
    "s_waitcnt vmcnt(7)\n"
    "v_mfma_f32_16x16x32_bf16 v[16:19], a[168:171], v[172:175], v[16:19]\n"
    "s_barrier\n"
    "v_mfma_f32_16x16x32_bf16 v[16:19], a[172:175], v[176:179], v[16:19]\n"
    "s_waitcnt lgkmcnt(3)\n"
    "v_add_u32_e32 v249, s45, v252\n"
    "v_add_u32_e32 v252, s63, v252\n"
    "v_mfma_f32_16x16x32_bf16 v[36:39], a[176:179], v[172:175], v[36:39]\n"
    "buffer_store_dword v254, off, s[8:11], s84 offset:0 sc1\n"
    "v_mfma_f32_16x16x32_bf16 v[36:39], a[180:183], v[176:179], v[36:39]\n"
    "s_mul_i32 s45, s33, 405504\n"
    "ds_read_b64_tr_b16 a[168:169], v245 offset:20480\n"
    "v_mfma_f32_16x16x32_bf16 v[32:35], a[180:183], v[168:171], v[32:35]\n"
    "s_add_u32 s45, s45, 16588800\n"
    "ds_read_b64_tr_b16 a[160:161], v244 offset:20480\n"
    "v_mfma_f32_16x16x32_bf16 v[32:35], a[176:179], v[164:167], v[32:35]\n"
    "buffer_load_dwordx4 v248, s[8:11], s41 offen offset:3072 sc1 lds\n"
    "v_mfma_f32_16x16x32_bf16 v[28:31], a[176:179], v[156:159], v[28:31]\n"
    "s_add_u32 s41, s41, 0x8000\n"
    "ds_read_b64_tr_b16 a[162:163], v244 offset:22528\n"
    "v_cmp_ne_u32_e32 vcc, s39, v253\n"
    "v_mfma_f32_16x16x32_bf16 v[28:31], a[180:183], v[160:163], v[28:31]\n"
    "s_mov_b32 m0, s63\n"
    "v_exp_f32_e32 v207, v207\n"
    "v_mfma_f32_16x16x32_bf16 v[24:27], a[180:183], v[152:155], v[24:27]\n"
    "s_lshl_b32 s46, s51, 12\n"
    "ds_read_b64_tr_b16 a[164:165], v244 offset:53248\n"
    "v_mfma_f32_16x16x32_bf16 v[24:27], a[176:179], v[148:151], v[24:27]\n"
    "s_cbranch_vccnz .Lat_wpoll\n"
    ".Lat_wpoll_ret:\n"
    "v_mfma_f32_16x16x32_bf16 v[20:23], a[176:179], v[140:143], v[20:23]\n"
    "s_add_u32 s62, s57, 0\n"
    "ds_read_b64_tr_b16 a[166:167], v244 offset:55296\n"
    "v_mfma_f32_16x16x32_bf16 v[20:23], a[180:183], v[144:147], v[20:23]\n"
    "s_waitcnt lgkmcnt(5)\n"
    "ds_read_b64_tr_b16 a[170:171], v245 offset:22528\n"
    "v_cvt_pk_bf16_f32 v204, v204, v205\n"
    "v_mfma_f32_16x16x32_bf16 v[40:43], a[184:187], v[140:143], v[40:43]\n"
    "buffer_load_dwordx4 a[216:219], v250, s[8:11], s62 offen offset:0 sc1\n"
    "v_mfma_f32_16x16x32_bf16 v[40:43], a[188:191], v[144:147], v[40:43]\n"
    "s_lshl_b32 s48, s50, 15\n"
    "ds_read_b64_tr_b16 a[176:177], v246 offset:20480\n"
    "v_mfma_f32_16x16x32_bf16 v[44:47], a[188:191], v[152:155], v[44:47]\n"
    "s_add_u32 s46, s46, s48\n"
    "ds_read_b64_tr_b16 a[178:179], v246 offset:22528\n"
    "v_mfma_f32_16x16x32_bf16 v[44:47], a[184:187], v[148:151], v[44:47]\n"
    "s_cmp_eq_u32 s51, 0\n"
    "s_cselect_b32 s47, 2.0, 0\n"
    "ds_read_b64_tr_b16 a[172:173], v245 offset:53248\n"
    "v_mfma_f32_16x16x32_bf16 v[48:51], a[184:187], v[156:159], v[48:51]\n"
    "s_add_u32 s47, s47, 98304\n"
    "v_exp_f32_e32 v212, v212\n"
    "v_mfma_f32_16x16x32_bf16 v[48:51], a[188:191], v[160:163], v[48:51]\n"
    "buffer_load_dwordx4 a[220:223], v250, s[8:11], s62 offen offset:1024 sc1\n"
    "v_mfma_f32_16x16x32_bf16 v[52:55], a[188:191], v[168:171], v[52:55]\n"
    "s_lshl_b32 s61, s50, 9\n"
    "ds_read_b64_tr_b16 a[174:175], v245 offset:55296\n"
    "v_mfma_f32_16x16x32_bf16 v[52:55], a[184:187], v[164:167], v[52:55]\n"
    "s_add_u32 s61, s61, s47\n"
    "ds_read_b64_tr_b16 a[180:181], v246 offset:53248\n"
    "v_mfma_f32_16x16x32_bf16 v[56:59], a[184:187], v[172:175], v[56:59]\n"
    "s_add_u32 s48, s32, 3\n"
    "v_exp_f32_e32 v213, v213\n"
    "v_mfma_f32_16x16x32_bf16 v[56:59], a[188:191], v[176:179], v[56:59]\n"
    "s_waitcnt lgkmcnt(6)\n"
    "ds_read_b64_tr_b16 a[184:185], v247 offset:20480\n"
    "v_mfma_f32_16x16x32_bf16 v[76:79], a[160:163], v[172:175], v[76:79]\n"
    "buffer_load_dwordx4 a[224:227], v250, s[8:11], s62 offen offset:2048 sc1\n"
    "v_mfma_f32_16x16x32_bf16 v[76:79], a[164:167], v[176:179], v[76:79]\n"
    "s_and_b32 s48, s48, 3\n"
    "ds_read_b64_tr_b16 a[186:187], v247 offset:22528\n"
    "v_mfma_f32_16x16x32_bf16 v[72:75], a[164:167], v[168:171], v[72:75]\n"
    "s_mul_i32 s48, s48, 101376\n"
    "ds_read_b64_tr_b16 a[188:189], v247 offset:53248\n"
    "v_cvt_pk_bf16_f32 v205, v206, v207\n"
    "v_mfma_f32_16x16x32_bf16 v[72:75], a[160:163], v[164:167], v[72:75]\n"
    "s_add_u32 s48, s48, s45\n"
    "ds_write_addtid_b32 v196 offset:0\n"
    "v_mfma_f32_16x16x32_bf16 v[68:71], a[160:163], v[156:159], v[68:71]\n"
    "s_add_u32 s29, s48, s46\n"
    "ds_read_b64_tr_b16 a[182:183], v246 offset:55296\n"
    "v_mfma_f32_16x16x32_bf16 v[68:71], a[164:167], v[160:163], v[68:71]\n"
    "buffer_load_dwordx4 a[228:231], v250, s[8:11], s62 offen offset:3072 sc1\n"
    "v_mfma_f32_16x16x32_bf16 v[64:67], a[164:167], v[152:155], v[64:67]\n"
    "s_add_u32 s62, s57, 4096\n"
    "ds_read_b64_tr_b16 a[190:191], v247 offset:55296\n"
    "v_mfma_f32_16x16x32_bf16 v[64:67], a[160:163], v[148:151], v[64:67]\n"
    "s_add_u32 s29, s29, 8192\n"
    "ds_write_addtid_b32 v197 offset:256\n"
    "v_mfma_f32_16x16x32_bf16 v[60:63], a[160:163], v[140:143], v[60:63]\n"
    "s_add_u32 s89, s48, s61\n"
    "v_exp_f32_e32 v214, v214\n"
    "v_mfma_f32_16x16x32_bf16 v[60:63], a[164:167], v[144:147], v[60:63]\n"
    "s_waitcnt lgkmcnt(8)\n"
    "ds_read_b128 a[160:163], v240 offset:0\n"
    "v_mfma_f32_16x16x32_bf16 v[80:83], a[168:171], v[140:143], v[80:83]\n"
    "buffer_load_dwordx4 a[232:235], v250, s[8:11], s62 offen offset:0 sc1\n"
    "v_mfma_f32_16x16x32_bf16 v[80:83], a[172:175], v[144:147], v[80:83]\n"
    "s_add_u32 s89, s89, 128\n"
    "v_exp_f32_e32 v215, v215\n"
    "v_mfma_f32_16x16x32_bf16 v[84:87], a[172:175], v[152:155], v[84:87]\n"
    "s_add_u32 s48, s32, 2\n"
    "v_cvt_pk_bf16_f32 v212, v212, v213\n"
    "v_mfma_f32_16x16x32_bf16 v[84:87], a[168:171], v[148:151], v[84:87]\n"
    "s_and_b32 s48, s48, 3\n"
    "v_exp_f32_e32 v200, v200\n"
    "v_mfma_f32_16x16x32_bf16 v[88:91], a[168:171], v[156:159], v[88:91]\n"
    "s_mul_i32 s48, s48, 101376\n"
    "ds_write_addtid_b32 v204 offset:1024\n"
    "v_mfma_f32_16x16x32_bf16 v[88:91], a[172:175], v[160:163], v[88:91]\n"
    "v_mfma_f32_16x16x32_bf16 v[92:95], a[172:175], v[168:171], v[92:95]\n"
    "s_barrier\n"
    "v_mfma_f32_16x16x32_bf16 v[92:95], a[168:171], v[164:167], v[92:95]\n"
    "buffer_load_dwordx4 v[192:195], v250, s[8:11], s62 offen offset:1024 sc1\n"
    "v_mfma_f32_16x16x32_bf16 v[96:99], a[168:171], v[172:175], v[96:99]\n"
    "s_add_u32 m0, s43, 16384\n"
    "v_exp_f32_e32 v201, v201\n"
    "v_mfma_f32_16x16x32_bf16 v[96:99], a[172:175], v[176:179], v[96:99]\n"
    "s_waitcnt lgkmcnt(4)\n"
    "ds_read_b128 a[164:167], v241 offset:0\n"
    "v_mfma_f32_16x16x32_bf16 v[116:119], a[176:179], v[172:175], v[116:119]\n"
    "buffer_load_dwordx4 v248, s[8:11], s40 offen offset:0 sc1 lds\n"
    "v_mfma_f32_16x16x32_bf16 v[116:119], a[180:183], v[176:179], v[116:119]\n"
    "s_mov_b32 m0, s63\n"
    "v_mfma_f32_16x16x32_bf16 v[112:115], a[180:183], v[168:171], v[112:115]\n"
    "s_add_u32 s48, s48, s45\n"
    "ds_write_addtid_b32 v205 offset:1280\n"
    "v_cvt_pk_bf16_f32 v213, v214, v215\n"
    "v_mfma_f32_16x16x32_bf16 v[112:115], a[176:179], v[164:167], v[112:115]\n"
    "s_add_u32 s28, s48, s46\n"
    "v_exp_f32_e32 v202, v202\n"
    "v_mfma_f32_16x16x32_bf16 v[108:111], a[176:179], v[156:159], v[108:111]\n"
    "s_add_u32 m0, s43, 16384\n"
    "v_exp_f32_e32 v203, v203\n"
    "v_mfma_f32_16x16x32_bf16 v[108:111], a[180:183], v[160:163], v[108:111]\n"
    "buffer_load_dwordx4 v248, s[8:11], s40 offen offset:1024 sc1 lds\n"
    "v_mfma_f32_16x16x32_bf16 v[104:107], a[180:183], v[152:155], v[104:107]\n"
    "s_mov_b32 m0, s63\n"
    "v_cvt_pk_bf16_f32 v198, v200, v201\n"
    "v_exp_f32_e32 v208, v208\n"
    "v_mfma_f32_16x16x32_bf16 v[104:107], a[176:179], v[148:151], v[104:107]\n"
    "s_add_u32 s28, s28, 16384\n"
    "v_exp_f32_e32 v209, v209\n"
    "v_mfma_f32_16x16x32_bf16 v[100:103], a[176:179], v[140:143], v[100:103]\n"
    "s_add_u32 s88, s48, s61\n"
    "ds_write_addtid_b32 v212 offset:2048\n"
    "v_mfma_f32_16x16x32_bf16 v[100:103], a[180:183], v[144:147], v[100:103]\n"
    "s_waitcnt lgkmcnt(6)\n"
    "ds_write_addtid_b32 v213 offset:2304\n"
    "v_cvt_pk_bf16_f32 v199, v202, v203\n"
    "v_mfma_f32_16x16x32_bf16 v[120:123], a[184:187], v[140:143], v[120:123]\n"
    "s_add_u32 m0, s43, 16384\n"
    "ds_read_b128 a[168:171], v240 offset:4096\n"
    "v_mfma_f32_16x16x32_bf16 v[120:123], a[188:191], v[144:147], v[120:123]\n"
    "buffer_load_dwordx4 v248, s[8:11], s40 offen offset:2048 sc1 lds\n"
    "v_mfma_f32_16x16x32_bf16 v[124:127], a[188:191], v[152:155], v[124:127]\n"
    "s_mov_b32 m0, s63\n"
    "ds_read_b128 a[176:179], v242 offset:0\n"
    "v_mfma_f32_16x16x32_bf16 v[124:127], a[184:187], v[148:151], v[124:127]\n"
    "s_add_u32 s88, s88, 256\n"
    "ds_write_addtid_b32 v198 offset:512\n"
    "v_mfma_f32_16x16x32_bf16 v[128:131], a[184:187], v[156:159], v[128:131]\n"
    "s_add_u32 s48, s32, 1\n"
    "v_exp_f32_e32 v210, v210\n"
    "v_mfma_f32_16x16x32_bf16 v[128:131], a[188:191], v[160:163], v[128:131]\n"
    "s_add_u32 m0, s43, 16384\n"
    "ds_read_b128 a[172:175], v241 offset:4096\n"
    "v_mfma_f32_16x16x32_bf16 v[132:135], a[188:191], v[168:171], v[132:135]\n"
    "buffer_load_dwordx4 v248, s[8:11], s40 offen offset:3072 sc1 lds\n"
    "v_mfma_f32_16x16x32_bf16 v[132:135], a[184:187], v[164:167], v[132:135]\n"
    "s_mov_b32 m0, s63\n"
    "v_exp_f32_e32 v211, v211\n"
    "v_mfma_f32_16x16x32_bf16 v[136:139], a[184:187], v[172:175], v[136:139]\n"
    "s_and_b32 s48, s48, 3\n"
    "ds_write_addtid_b32 v199 offset:768\n"
    "v_cvt_pk_bf16_f32 v206, v208, v209\n"
    "v_mfma_f32_16x16x32_bf16 v[136:139], a[188:191], v[176:179], v[136:139]\n"
    "s_waitcnt lgkmcnt(8)\n"
    "ds_read_b128 a[180:183], v243 offset:0\n"
    "v_mfma_f32_16x16x32_bf16 v[172:175], a[160:163], a[128:131], v[236:239]\n"
    "s_add_u32 m0, s43, 20480\n"
    "v_exp_f32_e32 v216, v216\n"
    "v_mfma_f32_16x16x32_bf16 v[172:175], a[164:167], a[132:135], v[172:175]\n"
    "buffer_load_dwordx4 v248, s[8:11], s41 offen offset:0 sc1 lds\n"
    "v_mfma_f32_16x16x32_bf16 v[164:167], a[164:167], a[100:103], v[232:235]\n"
    "s_mov_b32 m0, s63\n"
    "ds_read_b128 a[184:187], v242 offset:4096\n"
    "v_mfma_f32_16x16x32_bf16 v[164:167], a[160:163], a[96:99], v[164:167]\n"
    "s_mul_i32 s48, s48, 101376\n"
    "v_exp_f32_e32 v217, v217\n"
    "v_mfma_f32_16x16x32_bf16 v[156:159], a[160:163], a[64:67], v[228:231]\n"
    "s_add_u32 s48, s48, s45\n"
    "ds_write_addtid_b32 v206 offset:1536\n"
    "v_cvt_pk_bf16_f32 v207, v210, v211\n"
    "v_mfma_f32_16x16x32_bf16 v[156:159], a[164:167], a[68:71], v[156:159]\n"
    "s_waitcnt lgkmcnt(4)\n"
    "v_exp_f32_e32 v218, v218\n"
    "v_mfma_f32_16x16x32_bf16 v[156:159], a[168:171], a[72:75], v[156:159]\n"
    "s_add_u32 m0, s43, 20480\n"
    "v_exp_f32_e32 v219, v219\n"
    "v_mfma_f32_16x16x32_bf16 v[156:159], a[172:175], a[76:79], v[156:159]\n"
    "buffer_load_dwordx4 v248, s[8:11], s41 offen offset:1024 sc1 lds\n"
    "v_mfma_f32_16x16x32_bf16 v[164:167], a[172:175], a[108:111], v[164:167]\n"
    "s_mov_b32 m0, s63\n"
    "v_cvt_pk_bf16_f32 v214, v216, v217\n"
    "v_mfma_f32_16x16x32_bf16 v[164:167], a[168:171], a[104:107], v[164:167]\n"
    "s_add_u32 s82, s48, s46\n"
    "ds_read_b128 a[160:163], v240 offset:2048\n"
    "v_mfma_f32_16x16x32_bf16 v[172:175], a[168:171], a[136:139], v[172:175]\n"
    "s_add_u32 s82, s82, 24576\n"
    "ds_read_b128 a[188:191], v243 offset:4096\n"
    "v_cvt_pk_bf16_f32 v215, v218, v219\n"
    "v_mfma_f32_16x16x32_bf16 v[172:175], a[172:175], a[140:143], v[172:175]\n"
    "s_waitcnt lgkmcnt(4)\n"
    "ds_write_addtid_b32 v207 offset:1792\n"
    "v_mfma_f32_16x16x32_bf16 v[172:175], a[176:179], a[144:147], v[172:175]\n"
    "s_add_u32 m0, s43, 20480\n"
    "ds_read_b128 a[168:171], v240 offset:6144\n"
    "v_mfma_f32_16x16x32_bf16 v[172:175], a[180:183], a[148:151], v[172:175]\n"
    "buffer_load_dwordx4 v248, s[8:11], s41 offen offset:2048 sc1 lds\n"
    "v_mfma_f32_16x16x32_bf16 v[164:167], a[180:183], a[116:119], v[164:167]\n"
    "s_mov_b32 m0, s63\n"
    "ds_read_b128 a[172:175], v241 offset:6144\n"
    "v_mfma_f32_16x16x32_bf16 v[164:167], a[176:179], a[112:115], v[164:167]\n"
    "s_add_u32 s87, s48, s61\n"
    "ds_read_b128 a[164:167], v241 offset:2048\n"
    "v_mfma_f32_16x16x32_bf16 v[156:159], a[176:179], a[80:83], v[156:159]\n"
    "s_add_u32 s87, s87, 384\n"
    "ds_write_addtid_b32 v214 offset:2560\n"
    "v_mfma_f32_16x16x32_bf16 v[156:159], a[180:183], a[84:87], v[156:159]\n"
    "s_waitcnt lgkmcnt(5)\n"
    "ds_write_addtid_b32 v215 offset:2816\n"
    "v_mfma_f32_16x16x32_bf16 v[156:159], a[184:187], a[88:91], v[156:159]\n"
    "s_add_u32 m0, s43, 20480\n"
    "ds_read_b128 a[176:179], v242 offset:2048\n"
    "v_mfma_f32_16x16x32_bf16 v[156:159], a[188:191], a[92:95], v[156:159]\n"
    "buffer_load_dwordx4 v248, s[8:11], s41 offen offset:3072 sc1 lds\n"
    "v_mfma_f32_16x16x32_bf16 v[164:167], a[188:191], a[124:127], v[164:167]\n"
    "s_mov_b32 m0, s63\n"
    "ds_read_b128 a[180:183], v243 offset:2048\n"
    "v_mfma_f32_16x16x32_bf16 v[164:167], a[184:187], a[120:123], v[164:167]\n"
    "s_mul_i32 s48, s32, 101376\n"
    "v_exp_f32_e32 v156, v156\n"
    "v_mfma_f32_16x16x32_bf16 v[172:175], a[184:187], a[152:155], v[172:175]\n"
    "s_add_u32 s48, s48, s45\n"
    "v_exp_f32_e32 v157, v157\n"
    "v_mfma_f32_16x16x32_bf16 v[172:175], a[188:191], a[156:159], v[172:175]\n"
    "s_waitcnt lgkmcnt(4)\n"
    "v_exp_f32_e32 v158, v158\n"
    "v_mfma_f32_16x16x32_bf16 v[176:179], a[160:163], a[128:131], v[236:239]\n"
    "s_add_u32 s59, s48, s46\n"
    "ds_read_b128 a[184:187], v242 offset:6144\n"
    "v_mfma_f32_16x16x32_bf16 v[176:179], a[164:167], a[132:135], v[176:179]\n"
    "s_add_u32 s60, s48, s61\n"
    "v_exp_f32_e32 v159, v159\n"
    "v_mfma_f32_16x16x32_bf16 v[168:171], a[164:167], a[100:103], v[232:235]\n"
    "s_and_b32 s48, s32, 1\n"
    "ds_read_b128 a[188:191], v243 offset:6144\n"
    "v_cvt_pk_bf16_f32 v156, v156, v157\n"
    "v_mfma_f32_16x16x32_bf16 v[168:171], a[160:163], a[96:99], v[168:171]\n"
    "s_waitcnt lgkmcnt(4)\n"
    "s_waitcnt vmcnt(14)\n"
    "v_mfma_f32_16x16x32_bf16 v[160:163], a[160:163], a[64:67], v[228:231]\n"
    "s_barrier\n"
    "v_mfma_f32_16x16x32_bf16 v[160:163], a[164:167], a[68:71], v[160:163]\n"
    "ds_read_b128 a[160:163], v240 offset:8192\n"
    "ds_read2st64_b32 a[196:197], v249 offset0:0 offset1:1\n"
    "v_mfma_f32_16x16x32_bf16 v[160:163], a[168:171], a[72:75], v[160:163]\n"
    "s_xor_b32 s48, s48, s50\n"
    "ds_read_b128 a[164:167], v241 offset:8192\n"
    "ds_read2st64_b32 a[198:199], v249 offset0:2 offset1:3\n"
    "v_mfma_f32_16x16x32_bf16 v[160:163], a[172:175], a[76:79], v[160:163]\n"
    "s_sub_u32 s61, s32, s48\n"
    "v_exp_f32_e32 v164, v164\n"
    "v_mfma_f32_16x16x32_bf16 v[168:171], a[172:175], a[108:111], v[168:171]\n"
    "s_and_b32 s61, s61, 3\n"
    "v_exp_f32_e32 v165, v165\n"
    "v_mfma_f32_16x16x32_bf16 v[168:171], a[168:171], a[104:107], v[168:171]\n"
    "s_add_u32 s61, s61, 8\n"
    "ds_read2st64_b32 a[192:193], v252 offset0:0 offset1:1\n"
    "ds_write_addtid_b32 v156 offset:3072\n"
    "v_mfma_f32_16x16x32_bf16 v[176:179], a[168:171], a[136:139], v[176:179]\n"
    "s_mul_i32 s48, s48, 101376\n"
    "ds_read2st64_b32 a[194:195], v252 offset0:2 offset1:3\n"
    "v_cvt_pk_bf16_f32 v157, v158, v159\n"
    "v_mfma_f32_16x16x32_bf16 v[176:179], a[172:175], a[140:143], v[176:179]\n"
    "s_waitcnt lgkmcnt(9)\n"
    "v_exp_f32_e32 v166, v166\n"
    "v_mfma_f32_16x16x32_bf16 v[176:179], a[176:179], a[144:147], v[176:179]\n"
    "s_add_u32 s48, s48, s45\n"
    "ds_write_addtid_b32 v157 offset:3328\n"
    "v_mfma_f32_16x16x32_bf16 v[176:179], a[180:183], a[148:151], v[176:179]\n"
    "s_lshl_b32 s80, s61, 13\n"
    "ds_read_b128 a[168:171], v240 offset:12288\n"
    "v_mfma_f32_16x16x32_bf16 v[168:171], a[180:183], a[116:119], v[168:171]\n"
    "s_add_u32 s80, s80, s48\n"
    "v_exp_f32_e32 v167, v167\n"
    "v_mfma_f32_16x16x32_bf16 v[168:171], a[176:179], a[112:115], v[168:171]\n"
    "s_lshl_b32 s46, s51, 12\n"
    "ds_read_b128 a[172:175], v241 offset:12288\n"
    "v_cvt_pk_bf16_f32 v164, v164, v165\n"
    "v_mfma_f32_16x16x32_bf16 v[160:163], a[176:179], a[80:83], v[160:163]\n"
    "s_add_u32 s80, s80, s46\n"
    "v_exp_f32_e32 v172, v172\n"
    "v_mfma_f32_16x16x32_bf16 v[160:163], a[180:183], a[84:87], v[160:163]\n"
    "s_waitcnt lgkmcnt(10)\n"
    "v_exp_f32_e32 v173, v173\n"
    "v_mfma_f32_16x16x32_bf16 v[160:163], a[184:187], a[88:91], v[160:163]\n"
    "s_lshl_b32 s86, s61, 7\n"
    "ds_read_b128 a[176:179], v242 offset:8192\n"
    "v_cvt_pk_bf16_f32 v165, v166, v167\n"
    "v_mfma_f32_16x16x32_bf16 v[160:163], a[188:191], a[92:95], v[160:163]\n"
    "s_add_u32 s86, s86, s48\n"
    "v_exp_f32_e32 v174, v174\n"
    "v_mfma_f32_16x16x32_bf16 v[168:171], a[188:191], a[124:127], v[168:171]\n"
    "s_add_u32 s86, s86, s47\n"
    "v_exp_f32_e32 v175, v175\n"
    "v_mfma_f32_16x16x32_bf16 v[168:171], a[184:187], a[120:123], v[168:171]\n"
    "s_lshl_b32 s46, s93, 7\n"
    "ds_read2st64_b32 a[204:205], v249 offset0:4 offset1:5\n"
    "v_cvt_pk_bf16_f32 v172, v172, v173\n"
    "v_mfma_f32_16x16x32_bf16 v[176:179], a[184:187], a[152:155], v[176:179]\n"
    "s_lshl_b32 s55, s93, 15\n"
    "ds_read_b128 a[180:183], v243 offset:8192\n"
    "ds_read2st64_b32 a[206:207], v249 offset0:6 offset1:7\n"
    "v_mfma_f32_16x16x32_bf16 v[176:179], a[188:191], a[156:159], v[176:179]\n"
    "s_waitcnt lgkmcnt(11)\n"
    "ds_read2st64_b32 a[200:201], v252 offset0:4 offset1:5\n"
    "ds_write_addtid_b32 v164 offset:4096\n"
    "v_mfma_f32_16x16x32_bf16 v[180:183], a[160:163], a[128:131], v[236:239]\n"
    "s_sub_u32 s54, s55, 24576\n"
    "v_exp_f32_e32 v160, v160\n"
    "v_mfma_f32_16x16x32_bf16 v[180:183], a[164:167], a[132:135], v[180:183]\n"
    "s_add_u32 s55, s55, 32768\n"
    "v_exp_f32_e32 v161, v161\n"
    "v_mfma_f32_16x16x32_bf16 v[148:151], a[164:167], a[100:103], v[232:235]\n"
    "s_lshl2_add_u32 s38, s34, s93\n"
    "ds_write_addtid_b32 v165 offset:4352\n"
    "v_cvt_pk_bf16_f32 v173, v174, v175\n"
    "v_mfma_f32_16x16x32_bf16 v[148:151], a[160:163], a[96:99], v[148:151]\n"
    "s_lshl1_add_u32 s38, s38, s49\n"
    "ds_read_b128 a[184:187], v242 offset:12288\n"
    "v_exp_f32_e32 v162, v162\n"
    "v_mfma_f32_16x16x32_bf16 v[140:143], a[160:163], a[64:67], v[228:231]\n"
    "s_lshl_b32 s38, s38, 7\n"
    "v_exp_f32_e32 v163, v163\n"
    "v_mfma_f32_16x16x32_bf16 v[140:143], a[164:167], a[68:71], v[140:143]\n"
    "s_waitcnt lgkmcnt(8)\n"
    "ds_read2st64_b32 a[202:203], v252 offset0:6 offset1:7\n"
    "v_cvt_pk_bf16_f32 v158, v160, v161\n"
    "v_mfma_f32_16x16x32_bf16 v[140:143], a[168:171], a[72:75], v[140:143]\n"
    "s_add_u32 s38, s38, 5648384\n"
    "ds_read_b128 a[188:191], v243 offset:12288\n"
    "ds_write_addtid_b32 v172 offset:5120\n"
    "v_mfma_f32_16x16x32_bf16 v[140:143], a[172:175], a[76:79], v[140:143]\n"
    "s_lshl3_add_u32 s90, s34, s49\n"
    "ds_write_addtid_b32 v173 offset:5376\n"
    "v_exp_f32_e32 v168, v168\n"
    "v_mfma_f32_16x16x32_bf16 v[148:151], a[172:175], a[108:111], v[148:151]\n"
    "s_lshl2_add_u32 s90, s90, s93\n"
    "v_exp_f32_e32 v169, v169\n"
    "v_mfma_f32_16x16x32_bf16 v[148:151], a[168:171], a[104:107], v[148:151]\n"
    "s_lshl2_add_u32 s90, s90, 6041600\n"
    "v_cvt_pk_bf16_f32 v159, v162, v163\n"
    "v_exp_f32_e32 v170, v170\n"
    "v_mfma_f32_16x16x32_bf16 v[180:183], a[168:171], a[136:139], v[180:183]\n"
    "s_lshl_b32 s45, s51, 15\n"
    "v_exp_f32_e32 v171, v171\n"
    "v_mfma_f32_16x16x32_bf16 v[180:183], a[172:175], a[140:143], v[180:183]\n"
    "s_waitcnt lgkmcnt(9)\n"
    "ds_read_b128 a[160:163], v240 offset:10240\n"
    "v_cvt_pk_bf16_f32 v166, v168, v169\n"
    "v_mfma_f32_16x16x32_bf16 v[180:183], a[176:179], a[144:147], v[180:183]\n"
    "s_sub_u32 s45, 24576, s45\n"
    "v_exp_f32_e32 v176, v176\n"
    "v_mfma_f32_16x16x32_bf16 v[180:183], a[180:183], a[148:151], v[180:183]\n"
    "s_add_u32 s47, s46, 6103040\n"
    "ds_write_addtid_b32 v158 offset:3584\n"
    "v_exp_f32_e32 v177, v177\n"
    "v_mfma_f32_16x16x32_bf16 v[148:151], a[180:183], a[116:119], v[148:151]\n"
    "s_andn2_b32 s48, s38, 0xc00\n"
    "ds_write_addtid_b32 v159 offset:3840\n"
    "v_cvt_pk_bf16_f32 v167, v170, v171\n"
    "v_mfma_f32_16x16x32_bf16 v[148:151], a[176:179], a[112:115], v[148:151]\n"
    "s_andn2_b32 s61, s90, 0x18c\n"
    "ds_read_b128 a[164:167], v241 offset:10240\n"
    "ds_read_b128 a[168:171], v240 offset:14336\n"
    "v_mfma_f32_16x16x32_bf16 v[140:143], a[176:179], a[80:83], v[140:143]\n"
    "buffer_load_dword v188, v189, s[8:11], s81 offen offset:0 sc1\n"
    "ds_read2st64_b32 a[212:213], v249 offset0:8 offset1:9\n"
    "v_mfma_f32_16x16x32_bf16 v[140:143], a[180:183], a[84:87], v[140:143]\n"
    "s_waitcnt lgkmcnt(8)\n"
    "ds_read2st64_b32 a[214:215], v249 offset0:10 offset1:11\n"
    "ds_write_addtid_b32 v166 offset:4608\n"
    "v_mfma_f32_16x16x32_bf16 v[140:143], a[184:187], a[88:91], v[140:143]\n"
    "s_add_u32 s31, s46, s35\n"
    "ds_read_b128 a[172:175], v241 offset:14336\n"
    "ds_read2st64_b32 a[208:209], v252 offset0:8 offset1:9\n"
    "v_mfma_f32_16x16x32_bf16 v[140:143], a[188:191], a[92:95], v[140:143]\n"
    "s_add_u32 m0, s63, 6144\n"
    "ds_read_b128 a[176:179], v242 offset:10240\n"
    "v_exp_f32_e32 v178, v178\n"
    "v_mfma_f32_16x16x32_bf16 v[148:151], a[188:191], a[124:127], v[148:151]\n"
    "buffer_load_dwordx4 v251, s[12:15], s31 offen offset:0 lds\n"
    "ds_read_b128 a[180:183], v243 offset:10240\n"
    "v_mfma_f32_16x16x32_bf16 v[148:151], a[184:187], a[120:123], v[148:151]\n"
    "s_mov_b32 m0, s63\n"
    "v_exp_f32_e32 v179, v179\n"
    "v_mfma_f32_16x16x32_bf16 v[180:183], a[184:187], a[152:155], v[180:183]\n"
    "ds_read2st64_b32 a[210:211], v252 offset0:10 offset1:11\n"
    "v_exp_f32_e32 v140, v140\n"
    "v_mfma_f32_16x16x32_bf16 v[180:183], a[188:191], a[156:159], v[180:183]\n"
    "s_waitcnt lgkmcnt(9)\n"
    "v_exp_f32_e32 v141, v141\n"
    "v_mfma_f32_16x16x32_bf16 v[184:187], a[160:163], a[128:131], v[236:239]\n"
    "s_add_u32 s31, s31, 0x1000\n"
    "v_exp_f32_e32 v142, v142\n"
    "v_mfma_f32_16x16x32_bf16 v[184:187], a[164:167], a[132:135], v[184:187]\n"
    "s_add_u32 m0, s63, 7168\n"
    "v_exp_f32_e32 v143, v143\n"
    "v_mfma_f32_16x16x32_bf16 v[152:155], a[164:167], a[100:103], v[232:235]\n"
    "buffer_load_dwordx4 v251, s[12:15], s31 offen offset:0 lds\n"
    "v_mfma_f32_16x16x32_bf16 v[152:155], a[160:163], a[96:99], v[152:155]\n"
    "s_mov_b32 m0, s63\n"
    "ds_read_b128 a[184:187], v242 offset:14336\n"
    "v_cvt_pk_bf16_f32 v140, v140, v141\n"
    "v_mfma_f32_16x16x32_bf16 v[144:147], a[160:163], a[64:67], v[228:231]\n"
    "s_cmp_eq_u32 s93, 1\n"
    "s_cselect_b32 s31, s36, 2.0\n"
    "v_cvt_pk_bf16_f32 v174, v176, v177\n"
    "v_mfma_f32_16x16x32_bf16 v[144:147], a[164:167], a[68:71], v[144:147]\n"
    "s_waitcnt lgkmcnt(5)\n"
    "s_waitcnt vmcnt(3)\n"
    "v_mfma_f32_16x16x32_bf16 v[144:147], a[168:171], a[72:75], v[144:147]\n"
    "s_barrier\n"
    "v_mfma_f32_16x16x32_bf16 v[144:147], a[172:175], a[76:79], v[144:147]\n"
    "s_cmp_eq_u32 s93, 2\n"
    "s_cselect_b32 s31, s37, s31\n"
    "ds_read_b128 a[188:191], v243 offset:14336\n"
    "v_mfma_f32_16x16x32_bf16 v[152:155], a[172:175], a[108:111], v[152:155]\n"
    "buffer_load_dword v191, v190, s[12:15], s31 offen offset:0\n"
    "v_mfma_f32_16x16x32_bf16 v[152:155], a[168:171], a[104:107], v[152:155]\n"
    "s_cmp_eq_u32 s93, 0\n"
    "s_cselect_b32 s31, s35, s31\n"
    "v_exp_f32_e32 v148, v148\n"
    "v_mfma_f32_16x16x32_bf16 v[184:187], a[168:171], a[136:139], v[184:187]\n"
    "s_add_u32 s31, s31, 6103040\n"
    "v_exp_f32_e32 v149, v149\n"
    "v_mfma_f32_16x16x32_bf16 v[184:187], a[172:175], a[140:143], v[184:187]\n"
    "s_waitcnt lgkmcnt(3)\n"
    "ds_write_addtid_b32 v140 offset:0\n"
    "v_cvt_pk_bf16_f32 v141, v142, v143\n"
    "v_mfma_f32_16x16x32_bf16 v[184:187], a[176:179], a[144:147], v[184:187]\n"
    "ds_write_addtid_b32 v167 offset:4864\n"
    "v_exp_f32_e32 v150, v150\n"
    "v_mfma_f32_16x16x32_bf16 v[184:187], a[180:183], a[148:151], v[184:187]\n"
    "v_exp_f32_e32 v151, v151\n"
    "v_mfma_f32_16x16x32_bf16 v[152:155], a[180:183], a[116:119], v[152:155]\n"
    "ds_read_b128 a[168:171], v240 offset:20480\n"
    "v_cvt_pk_bf16_f32 v148, v148, v149\n"
    "v_mfma_f32_16x16x32_bf16 v[152:155], a[176:179], a[112:115], v[152:155]\n"
    "ds_read_b128 a[160:163], v240 offset:16384\n"
    "v_exp_f32_e32 v180, v180\n"
    "v_mfma_f32_16x16x32_bf16 v[144:147], a[176:179], a[80:83], v[144:147]\n"
    "ds_read_b128 a[164:167], v241 offset:16384\n"
    "ds_write_addtid_b32 v141 offset:256\n"
    "v_mfma_f32_16x16x32_bf16 v[144:147], a[180:183], a[84:87], v[144:147]\n"
    "s_waitcnt lgkmcnt(6)\n"
    "v_exp_f32_e32 v181, v181\n"
    "v_mfma_f32_16x16x32_bf16 v[144:147], a[184:187], a[88:91], v[144:147]\n"
    "ds_read_b128 a[172:175], v241 offset:20480\n"
    "v_cvt_pk_bf16_f32 v149, v150, v151\n"
    "v_mfma_f32_16x16x32_bf16 v[144:147], a[188:191], a[92:95], v[144:147]\n"
    "ds_write_addtid_b32 v174 offset:5632\n"
    "v_cvt_pk_bf16_f32 v175, v178, v179\n"
    "v_mfma_f32_16x16x32_bf16 v[152:155], a[188:191], a[124:127], v[152:155]\n"
    "ds_write_addtid_b32 v148 offset:1024\n"
    "v_exp_f32_e32 v182, v182\n"
    "v_mfma_f32_16x16x32_bf16 v[152:155], a[184:187], a[120:123], v[152:155]\n"
    "ds_read_b128 a[176:179], v242 offset:16384\n"
    "ds_write_addtid_b32 v175 offset:5888\n"
    "v_mfma_f32_16x16x32_bf16 v[184:187], a[184:187], a[152:155], v[184:187]\n"
    "v_exp_f32_e32 v183, v183\n"
    "v_mfma_f32_16x16x32_bf16 v[184:187], a[188:191], a[156:159], v[184:187]\n"
    "s_waitcnt lgkmcnt(6)\n"
    "ds_write_addtid_b32 v149 offset:1280\n"
    "v_cvt_pk_bf16_f32 v180, v180, v181\n"
    "v_mfma_f32_16x16x32_bf16 v[212:215], a[160:163], a[128:131], v[236:239]\n"
    "v_exp_f32_e32 v144, v144\n"
    "v_mfma_f32_16x16x32_bf16 v[212:215], a[164:167], a[132:135], v[212:215]\n"
    "v_exp_f32_e32 v145, v145\n"
    "v_mfma_f32_16x16x32_bf16 v[204:207], a[164:167], a[100:103], v[232:235]\n"
    "ds_read_b128 a[180:183], v243 offset:16384\n"
    "v_cvt_pk_bf16_f32 v181, v182, v183\n"
    "v_mfma_f32_16x16x32_bf16 v[204:207], a[160:163], a[96:99], v[204:207]\n"
    "ds_read_b128 a[184:187], v242 offset:20480\n"
    "ds_write_addtid_b32 v180 offset:2048\n"
    "v_mfma_f32_16x16x32_bf16 v[196:199], a[160:163], a[64:67], v[228:231]\n"
    "v_exp_f32_e32 v146, v146\n"
    "v_mfma_f32_16x16x32_bf16 v[196:199], a[164:167], a[68:71], v[196:199]\n"
    "s_waitcnt lgkmcnt(8)\n"
    "ds_read_b128 a[188:191], v243 offset:20480\n"
    "v_exp_f32_e32 v147, v147\n"
    "v_mfma_f32_16x16x32_bf16 v[196:199], a[168:171], a[72:75], v[196:199]\n"
    "ds_write_addtid_b32 v181 offset:2304\n"
    "v_cvt_pk_bf16_f32 v142, v144, v145\n"
    "v_mfma_f32_16x16x32_bf16 v[196:199], a[172:175], a[76:79], v[196:199]\n"
    "s_waitcnt lgkmcnt(6)\n"
    "v_mfma_f32_16x16x32_bf16 v[204:207], a[172:175], a[108:111], v[204:207]\n"
    "s_barrier\n"
    "v_mfma_f32_16x16x32_bf16 v[204:207], a[168:171], a[104:107], v[204:207]\n"
    "ds_write_addtid_b32 v142 offset:512\n"
    "v_exp_f32_e32 v152, v152\n"
    "v_mfma_f32_16x16x32_bf16 v[212:215], a[168:171], a[136:139], v[212:215]\n"
    "ds_read_b128 a[160:163], v240 offset:18432\n"
    "ds_read_b128 a[164:167], v241 offset:18432\n"
    "v_mfma_f32_16x16x32_bf16 v[212:215], a[172:175], a[140:143], v[212:215]\n"
    "s_waitcnt lgkmcnt(7)\n"
    "v_exp_f32_e32 v153, v153\n"
    "v_mfma_f32_16x16x32_bf16 v[212:215], a[176:179], a[144:147], v[212:215]\n"
    "ds_read_b128 a[168:171], v240 offset:22528\n"
    "v_cvt_pk_bf16_f32 v143, v146, v147\n"
    "v_mfma_f32_16x16x32_bf16 v[212:215], a[180:183], a[148:151], v[212:215]\n"
    "ds_read2st64_b32 v[160:161], v249 offset0:12 offset1:13\n"
    "v_exp_f32_e32 v154, v154\n"
    "v_mfma_f32_16x16x32_bf16 v[204:207], a[180:183], a[116:119], v[204:207]\n"
    "v_exp_f32_e32 v155, v155\n"
    "v_mfma_f32_16x16x32_bf16 v[204:207], a[176:179], a[112:115], v[204:207]\n"
    "ds_write_addtid_b32 v143 offset:768\n"
    "v_cvt_pk_bf16_f32 v150, v152, v153\n"
    "v_mfma_f32_16x16x32_bf16 v[196:199], a[176:179], a[80:83], v[196:199]\n"
    "ds_read2st64_b32 v[162:163], v249 offset0:14 offset1:15\n"
    "v_exp_f32_e32 v184, v184\n"
    "v_mfma_f32_16x16x32_bf16 v[196:199], a[180:183], a[84:87], v[196:199]\n"
    "s_waitcnt lgkmcnt(8)\n"
    "v_exp_f32_e32 v185, v185\n"
    "v_mfma_f32_16x16x32_bf16 v[196:199], a[184:187], a[88:91], v[196:199]\n"
    "s_waitcnt vmcnt(3)\n"
    "ds_read_b128 a[176:179], v242 offset:18432\n"
    "v_cmp_ne_u32_e32 vcc, s39, v188\n"
    "v_mfma_f32_16x16x32_bf16 v[196:199], a[188:191], a[92:95], v[196:199]\n"
    "ds_read_b128 a[172:175], v241 offset:22528\n"
    "ds_write_addtid_b32 v150 offset:1536\n"
    "v_mfma_f32_16x16x32_bf16 v[204:207], a[188:191], a[124:127], v[204:207]\n"
    "ds_read2st64_b32 v[168:169], v249 offset0:16 offset1:17\n"
    "v_cvt_pk_bf16_f32 v151, v154, v155\n"
    "v_mfma_f32_16x16x32_bf16 v[204:207], a[184:187], a[120:123], v[204:207]\n"
    "s_cmp_eq_u64 vcc, 0\n"
    "s_cselect_b32 s31, s31, 2.0\n"
    "s_cselect_b32 s44, 0, 1\n"
    "ds_read_b128 a[180:183], v243 offset:18432\n"
    "v_mfma_f32_16x16x32_bf16 v[212:215], a[184:187], a[152:155], v[212:215]\n"
    "buffer_load_dword v191, v190, s[8:11], s31 offen offset:0 sc1\n"
    "v_mfma_f32_16x16x32_bf16 v[212:215], a[188:191], a[156:159], v[212:215]\n"
    "s_waitcnt lgkmcnt(9)\n"
    "v_exp_f32_e32 v196, v196\n"
    "v_mfma_f32_16x16x32_bf16 v[216:219], a[160:163], a[128:131], v[236:239]\n"
    "v_exp_f32_e32 v197, v197\n"
    "v_mfma_f32_16x16x32_bf16 v[216:219], a[164:167], a[132:135], v[216:219]\n"
    "ds_write_addtid_b32 v151 offset:1792\n"
    "v_exp_f32_e32 v186, v186\n"
    "v_mfma_f32_16x16x32_bf16 v[208:211], a[164:167], a[100:103], v[232:235]\n"
    "ds_read_b128 a[184:187], v242 offset:22528\n"
    "v_exp_f32_e32 v187, v187\n"
    "v_mfma_f32_16x16x32_bf16 v[208:211], a[160:163], a[96:99], v[208:211]\n"
    "v_exp_f32_e32 v198, v198\n"
    "v_mfma_f32_16x16x32_bf16 v[200:203], a[160:163], a[64:67], v[228:231]\n"
    "ds_read2st64_b32 v[170:171], v249 offset0:18 offset1:19\n"
    "v_exp_f32_e32 v199, v199\n"
    "v_mfma_f32_16x16x32_bf16 v[200:203], a[164:167], a[68:71], v[200:203]\n"
    "s_waitcnt lgkmcnt(6)\n"
    "ds_read_b64_tr_b16 a[160:161], v244 offset:24576\n"
    "v_cvt_pk_bf16_f32 v196, v196, v197\n"
    "v_mfma_f32_16x16x32_bf16 v[200:203], a[168:171], a[72:75], v[200:203]\n"
    "ds_read_b128 a[188:191], v243 offset:22528\n"
    "v_cvt_pk_bf16_f32 v182, v184, v185\n"
    "v_mfma_f32_16x16x32_bf16 v[200:203], a[172:175], a[76:79], v[200:203]\n"
    "ds_read2st64_b32 v[176:177], v249 offset0:20 offset1:21\n"
    "v_cvt_pk_bf16_f32 v183, v186, v187\n"
    "v_mfma_f32_16x16x32_bf16 v[208:211], a[172:175], a[108:111], v[208:211]\n"
    "ds_read_b64_tr_b16 a[162:163], v244 offset:26624\n"
    "v_and_b32_e32 v248, 0xf0, v250\n"
    "v_lshrrev_b32_e32 v231, 3, v248\n"
    "v_mfma_f32_16x16x32_bf16 v[208:211], a[168:171], a[104:107], v[208:211]\n"
    "ds_read_b64_tr_b16 a[164:165], v244 offset:57344\n"
    "v_exp_f32_e32 v204, v204\n"
    "v_mfma_f32_16x16x32_bf16 v[216:219], a[168:171], a[136:139], v[216:219]\n"
    "s_lshr_b32 s31, s35, 8\n"
    "v_exp_f32_e32 v205, v205\n"
    "v_mfma_f32_16x16x32_bf16 v[216:219], a[172:175], a[140:143], v[216:219]\n"
    "s_waitcnt lgkmcnt(8)\n"
    "ds_read_b64_tr_b16 a[168:169], v245 offset:24576\n"
    "v_cvt_pk_bf16_f32 v197, v198, v199\n"
    "v_mfma_f32_16x16x32_bf16 v[216:219], a[176:179], a[144:147], v[216:219]\n"
    "buffer_load_ushort v228, v231, s[24:27], s31 offen\n"
    "ds_read2st64_b32 v[178:179], v249 offset0:22 offset1:23\n"
    "v_mfma_f32_16x16x32_bf16 v[216:219], a[180:183], a[148:151], v[216:219]\n"
    "s_lshr_b32 s31, s36, 8\n"
    "ds_write_addtid_b32 v182 offset:2560\n"
    "v_exp_f32_e32 v206, v206\n"
    "v_mfma_f32_16x16x32_bf16 v[208:211], a[180:183], a[116:119], v[208:211]\n"
    "ds_read_b64_tr_b16 a[170:171], v245 offset:26624\n"
    "v_exp_f32_e32 v207, v207\n"
    "v_mfma_f32_16x16x32_bf16 v[208:211], a[176:179], a[112:115], v[208:211]\n"
    "ds_write_addtid_b32 v183 offset:2816\n"
    "v_cvt_pk_bf16_f32 v204, v204, v205\n"
    "v_mfma_f32_16x16x32_bf16 v[200:203], a[176:179], a[80:83], v[200:203]\n"
    "buffer_load_ushort v229, v231, s[24:27], s31 offen\n"
    "ds_read_b64_tr_b16 a[166:167], v244 offset:59392\n"
    "v_mfma_f32_16x16x32_bf16 v[200:203], a[180:183], a[84:87], v[200:203]\n"
    "s_waitcnt lgkmcnt(9)\n"
    "ds_read_b64_tr_b16 a[172:173], v245 offset:57344\n"
    "v_exp_f32_e32 v212, v212\n"
    "v_mfma_f32_16x16x32_bf16 v[200:203], a[184:187], a[88:91], v[200:203]\n"
    "s_lshr_b32 s31, s37, 8\n"
    "v_exp_f32_e32 v213, v213\n"
    "v_mfma_f32_16x16x32_bf16 v[200:203], a[188:191], a[92:95], v[200:203]\n"
    "ds_read_b64_tr_b16 a[174:175], v245 offset:59392\n"
    "v_cvt_pk_bf16_f32 v205, v206, v207\n"
    "v_mfma_f32_16x16x32_bf16 v[208:211], a[188:191], a[124:127], v[208:211]\n"
    "buffer_load_ushort v230, v231, s[24:27], s31 offen\n"
    "ds_read_b64_tr_b16 a[176:177], v246 offset:24576\n"
    "v_mfma_f32_16x16x32_bf16 v[208:211], a[184:187], a[120:123], v[208:211]\n"
    "s_waitcnt lgkmcnt(4)\n"
    "v_mfma_f32_16x16x32_bf16 v[216:219], a[184:187], a[152:155], v[216:219]\n"
    "s_barrier\n"
    "v_mfma_f32_16x16x32_bf16 v[216:219], a[188:191], a[156:159], v[216:219]\n"
    "s_waitcnt lgkmcnt(3)\n"
    "ds_write_addtid_b32 v196 offset:3072\n"
    "v_exp_f32_e32 v214, v214\n"
    "v_mfma_f32_16x16x32_bf16 a[252:255], a[160:163], a[208:211], a[252:255]\n"
    "s_add_u32 s62, s57, 4096\n"
    "ds_read_b64_tr_b16 a[184:185], v246 offset:0\n"
    "v_cvt_pk_bf16_f32 v212, v212, v213\n"
    "v_exp_f32_e32 v215, v215\n"
    "v_mfma_f32_16x16x32_bf16 a[252:255], a[164:167], a[212:215], a[252:255]\n"
    "buffer_load_dwordx4 a[64:67], v250, s[8:11], s62 offen offset:2048 sc1\n"
    "ds_read_b64_tr_b16 a[178:179], v246 offset:26624\n"
    "v_mfma_f32_16x16x32_bf16 a[248:251], a[164:167], a[204:207], a[248:251]\n"
    "ds_read2st64_b32 v[144:145], v249 offset0:0 offset1:1\n"
    "v_exp_f32_e32 v200, v200\n"
    "v_mfma_f32_16x16x32_bf16 a[248:251], a[160:163], a[200:203], a[248:251]\n"
    "ds_write_addtid_b32 v197 offset:3328\n"
    "v_exp_f32_e32 v201, v201\n"
    "v_mfma_f32_16x16x32_bf16 a[244:247], a[160:163], a[192:195], a[244:247]\n"
    "ds_read_b64_tr_b16 a[180:181], v246 offset:57344\n"
    "ds_read_b64_tr_b16 a[182:183], v246 offset:59392\n"
    "v_mfma_f32_16x16x32_bf16 a[244:247], a[164:167], a[196:199], a[244:247]\n"
    "s_waitcnt lgkmcnt(8)\n"
    "ds_read_b64_tr_b16 a[186:187], v246 offset:2048\n"
    "ds_read2st64_b32 v[146:147], v249 offset0:2 offset1:3\n"
    "v_mfma_f32_16x16x32_bf16 v[8:11], a[168:171], a[192:195], v[8:11]\n"
    "buffer_load_dwordx4 a[68:71], v250, s[8:11], s62 offen offset:3072 sc1\n"
    "ds_read2st64_b32 v[152:153], v249 offset0:4 offset1:5\n"
    "v_mfma_f32_16x16x32_bf16 v[8:11], a[172:175], a[196:199], v[8:11]\n"
    "s_add_u32 s62, s57, 8192\n"
    "ds_read_b64_tr_b16 a[188:189], v246 offset:32768\n"
    "ds_write_addtid_b32 v204 offset:4096\n"
    "ds_write_addtid_b32 v205 offset:4352\n"
    "v_mfma_f32_16x16x32_bf16 v[12:15], a[172:175], a[204:207], v[12:15]\n"
    "ds_read_b64_tr_b16 a[160:161], v244 offset:0\n"
    "ds_read_b64_tr_b16 a[162:163], v244 offset:2048\n"
    "v_cvt_pk_bf16_f32 v213, v214, v215\n"
    "v_mfma_f32_16x16x32_bf16 v[12:15], a[168:171], a[200:203], v[12:15]\n"
    "ds_read_b64_tr_b16 a[164:165], v244 offset:32768\n"
    "ds_read2st64_b32 v[154:155], v249 offset0:6 offset1:7\n"
    "v_mfma_f32_16x16x32_bf16 v[16:19], a[168:171], a[208:211], v[16:19]\n"
    "ds_read_b64_tr_b16 a[190:191], v246 offset:34816\n"
    "v_exp_f32_e32 v202, v202\n"
    "v_mfma_f32_16x16x32_bf16 v[16:19], a[172:175], a[212:215], v[16:19]\n"
    "s_waitcnt lgkmcnt(11)\n"
    "ds_read_b64_tr_b16 a[168:169], v245 offset:0\n"
    "v_exp_f32_e32 v203, v203\n"
    "v_mfma_f32_16x16x32_bf16 v[36:39], a[176:179], a[208:211], v[36:39]\n"
    "buffer_load_dwordx4 a[72:75], v250, s[8:11], s62 offen offset:0 sc1\n"
    "ds_read_b64_tr_b16 a[170:171], v245 offset:2048\n"
    "v_mfma_f32_16x16x32_bf16 v[36:39], a[180:183], a[212:215], v[36:39]\n"
    "ds_read2st64_b32 v[184:185], v249 offset0:8 offset1:9\n"
    "ds_write_addtid_b32 v212 offset:5120\n"
    "v_mfma_f32_16x16x32_bf16 v[32:35], a[180:183], a[204:207], v[32:35]\n"
    "ds_read_b64_tr_b16 a[166:167], v244 offset:34816\n"
    "v_cvt_pk_bf16_f32 v198, v200, v201\n"
    "v_mfma_f32_16x16x32_bf16 v[32:35], a[176:179], a[200:203], v[32:35]\n"
    "ds_write_addtid_b32 v213 offset:5376\n"
    "v_exp_f32_e32 v208, v208\n"
    "v_mfma_f32_16x16x32_bf16 v[28:31], a[176:179], a[192:195], v[28:31]\n"
    "ds_read_b64_tr_b16 a[172:173], v245 offset:32768\n"
    "v_exp_f32_e32 v209, v209\n"
    "v_mfma_f32_16x16x32_bf16 v[28:31], a[180:183], a[196:199], v[28:31]\n"
    "s_waitcnt lgkmcnt(7)\n"
    "ds_write_addtid_b32 v198 offset:3584\n"
    "v_cvt_pk_bf16_f32 v199, v202, v203\n"
    "v_mfma_f32_16x16x32_bf16 v[28:31], a[184:187], v[156:159], v[28:31]\n"
    "buffer_load_dwordx4 a[76:79], v250, s[8:11], s62 offen offset:1024 sc1\n"
    "ds_read2st64_b32 v[186:187], v249 offset0:10 offset1:11\n"
    "v_mfma_f32_16x16x32_bf16 v[28:31], a[188:191], v[160:163], v[28:31]\n"
    "ds_read_b64_tr_b16 a[176:177], v245 offset:8192\n"
    "v_exp_f32_e32 v210, v210\n"
    "v_mfma_f32_16x16x32_bf16 v[32:35], a[188:191], v[168:171], v[32:35]\n"
    "ds_read_b64_tr_b16 a[174:175], v245 offset:34816\n"
    "v_exp_f32_e32 v211, v211\n"
    "v_mfma_f32_16x16x32_bf16 v[32:35], a[184:187], v[164:167], v[32:35]\n"
    "v_cvt_pk_bf16_f32 v206, v208, v209\n"
    "v_exp_f32_e32 v216, v216\n"
    "v_mfma_f32_16x16x32_bf16 v[36:39], a[184:187], v[172:175], v[36:39]\n"
    "ds_read_b64_tr_b16 a[178:179], v245 offset:10240\n"
    "v_exp_f32_e32 v217, v217\n"
    "v_mfma_f32_16x16x32_bf16 v[36:39], a[188:191], v[176:179], v[36:39]\n"
    "s_waitcnt lgkmcnt(7)\n"
    "ds_read_b64_tr_b16 a[180:181], v245 offset:40960\n"
    "v_lshrrev_b32_e32 v231, 2, v250\n"
    "v_mfma_f32_16x16x32_bf16 a[252:255], a[160:163], v[172:175], a[252:255]\n"
    "buffer_load_dwordx4 a[80:83], v250, s[8:11], s62 offen offset:2048 sc1\n"
    "ds_write_addtid_b32 v199 offset:3840\n"
    "v_mfma_f32_16x16x32_bf16 a[252:255], a[164:167], v[176:179], a[252:255]\n"
    "ds_write_addtid_b32 v206 offset:4608\n"
    "v_cvt_pk_bf16_f32 v207, v210, v211\n"
    "v_mfma_f32_16x16x32_bf16 a[248:251], a[164:167], v[168:171], a[248:251]\n"
    "ds_write_addtid_b32 v207 offset:4864\n"
    "v_exp_f32_e32 v218, v218\n"
    "v_mfma_f32_16x16x32_bf16 a[248:251], a[160:163], v[164:167], a[248:251]\n"
    "v_exp_f32_e32 v219, v219\n"
    "v_mfma_f32_16x16x32_bf16 a[244:247], a[160:163], v[156:159], a[244:247]\n"
    "ds_read_b64_tr_b16 a[182:183], v245 offset:43008\n"
    "v_cvt_pk_bf16_f32 v214, v216, v217\n"
    "v_mfma_f32_16x16x32_bf16 a[244:247], a[164:167], v[160:163], a[244:247]\n"
    "s_waitcnt lgkmcnt(6)\n"
    "ds_write_addtid_b32 v214 offset:5632\n"
    "v_cvt_pk_bf16_f32 v215, v218, v219\n"
    "v_mfma_f32_16x16x32_bf16 v[8:11], a[168:171], v[156:159], v[8:11]\n"
    "buffer_load_dwordx4 a[84:87], v250, s[8:11], s62 offen offset:3072 sc1\n"
    "ds_write_addtid_b32 v215 offset:5888\n"
    "v_mfma_f32_16x16x32_bf16 v[8:11], a[172:175], v[160:163], v[8:11]\n"
    "s_add_u32 s62, s57, 12288\n"
    "ds_read_b64_tr_b16 a[184:185], v244 offset:8192\n"
    "ds_read_b64_tr_b16 a[186:187], v244 offset:10240\n"
    "v_mfma_f32_16x16x32_bf16 v[12:15], a[172:175], v[168:171], v[12:15]\n"
    "ds_read_b64_tr_b16 a[160:161], v244 offset:16384\n"
    "ds_read_b64_tr_b16 a[162:163], v244 offset:18432\n"
    "v_mfma_f32_16x16x32_bf16 v[12:15], a[168:171], v[164:167], v[12:15]\n"
    "ds_read_b64_tr_b16 a[188:189], v244 offset:40960\n"
    "ds_read_b64_tr_b16 a[164:165], v244 offset:49152\n"
    "v_mfma_f32_16x16x32_bf16 v[16:19], a[168:171], v[172:175], v[16:19]\n"
    "ds_read_b64_tr_b16 a[190:191], v244 offset:43008\n"
    "v_subrev_u32_e32 v231, 0x80, v231\n"
    "v_mfma_f32_16x16x32_bf16 v[16:19], a[172:175], v[176:179], v[16:19]\n"
    "s_waitcnt lgkmcnt(9)\n"
    "s_waitcnt lgkmcnt(7)\n"
    "v_mfma_f32_16x16x32_bf16 v[16:19], a[176:179], v[180:183], v[16:19]\n"
    "s_barrier\n"
    "v_mfma_f32_16x16x32_bf16 v[16:19], a[180:183], v[184:187], v[16:19]\n"
    "buffer_load_dwordx4 a[88:91], v250, s[8:11], s62 offen offset:0 sc1\n"
    "ds_read_b64_tr_b16 a[166:167], v244 offset:51200\n"
    "v_mfma_f32_16x16x32_bf16 v[12:15], a[180:183], v[152:155], v[12:15]\n"
    "ds_read2st64_b32 v[200:201], v249 offset0:12 offset1:13\n"
    "ds_read2st64_b32 v[202:203], v249 offset0:14 offset1:15\n"
    "v_mfma_f32_16x16x32_bf16 v[12:15], a[176:179], v[148:151], v[12:15]\n"
    "ds_read2st64_b32 v[208:209], v249 offset0:16 offset1:17\n"
    "v_min_u32_e32 v231, 2.0, v231\n"
    "v_mfma_f32_16x16x32_bf16 v[8:11], a[176:179], v[140:143], v[8:11]\n"
    "ds_read2st64_b32 v[210:211], v249 offset0:18 offset1:19\n"
    "ds_read2st64_b32 v[216:217], v249 offset0:20 offset1:21\n"
    "v_mfma_f32_16x16x32_bf16 v[8:11], a[180:183], v[144:147], v[8:11]\n"
    "s_waitcnt lgkmcnt(6)\n"
    "ds_read_b64_tr_b16 a[168:169], v245 offset:16384\n"
    "ds_read2st64_b32 v[218:219], v249 offset0:22 offset1:23\n"
    "v_mfma_f32_16x16x32_bf16 a[244:247], a[184:187], v[140:143], a[244:247]\n"
    "buffer_load_dwordx4 a[92:95], v250, s[8:11], s62 offen offset:1024 sc1\n"
    "v_mfma_f32_16x16x32_bf16 a[244:247], a[188:191], v[144:147], a[244:247]\n"
    "ds_read_b64_tr_b16 a[170:171], v245 offset:18432\n"
    "v_lshrrev_b32_e32 v252, 4, v250\n"
    "v_mfma_f32_16x16x32_bf16 a[248:251], a[188:191], v[152:155], a[248:251]\n"
    "ds_read_b64_tr_b16 a[176:177], v246 offset:8192\n"
    "v_lshlrev_b32_e32 v239, 5, v248\n"
    "v_mfma_f32_16x16x32_bf16 a[248:251], a[184:187], v[148:151], a[248:251]\n"
    "ds_read_b64_tr_b16 a[172:173], v245 offset:49152\n"
    "v_and_or_b32 v239, v252, 48, v239\n"
    "v_mfma_f32_16x16x32_bf16 a[252:255], a[184:187], v[180:183], a[252:255]\n"
    "ds_read_b64_tr_b16 a[174:175], v245 offset:51200\n"
    "ds_read_b64_tr_b16 a[178:179], v246 offset:10240\n"
    "v_mfma_f32_16x16x32_bf16 a[252:255], a[188:191], v[184:187], a[252:255]\n"
    "s_waitcnt lgkmcnt(5)\n"
    "v_lshrrev_b32_e32 v248, 2, v248\n"
    "v_lshrrev_b32_e32 v253, 4, v252\n"
    "v_mfma_f32_16x16x32_bf16 a[252:255], a[160:163], v[212:215], a[252:255]\n"
    "buffer_load_dwordx4 a[96:99], v250, s[8:11], s62 offen offset:2048 sc1\n"
    "v_mfma_f32_16x16x32_bf16 a[252:255], a[164:167], v[216:219], a[252:255]\n"
    "v_lshl_add_u32 v248, v253, 7, v248\n"
    "v_bfe_u32 v255, v252, 2, 2\n"
    "v_mfma_f32_16x16x32_bf16 a[248:251], a[164:167], v[208:211], a[248:251]\n"
    "ds_read_b64_tr_b16 a[180:181], v246 offset:40960\n"
    "v_max_u32_e32 v238, 1, v255\n"
    "v_mfma_f32_16x16x32_bf16 a[248:251], a[160:163], v[204:207], a[248:251]\n"
    "v_add_u32_e32 v238, s32, v238\n"
    "v_and_b32_e32 v238, 3, v238\n"
    "v_mfma_f32_16x16x32_bf16 a[244:247], a[160:163], v[196:199], a[244:247]\n"
    "ds_read_b64_tr_b16 a[182:183], v246 offset:43008\n"
    "v_lshlrev_b32_e32 v238, 10, v238\n"
    "v_mfma_f32_16x16x32_bf16 a[244:247], a[164:167], v[200:203], a[244:247]\n"
    "s_waitcnt lgkmcnt(3)\n"
    "ds_read_b64_tr_b16 a[184:185], v246 offset:16384\n"
    "v_and_b32_e32 v252, 3, v252\n"
    "v_mfma_f32_16x16x32_bf16 v[8:11], a[168:171], v[196:199], v[8:11]\n"
    "buffer_load_dwordx4 a[100:103], v250, s[8:11], s62 offen offset:3072 sc1\n"
    "v_mfma_f32_16x16x32_bf16 v[8:11], a[172:175], v[200:203], v[8:11]\n"
    "s_add_u32 s62, s57, 16384\n"
    "ds_read_b64_tr_b16 a[160:161], v247 offset:24576\n"
    "v_xor_b32_e32 v253, v253, v255\n"
    "v_mfma_f32_16x16x32_bf16 v[12:15], a[172:175], v[208:211], v[12:15]\n"
    "ds_read_b64_tr_b16 a[186:187], v246 offset:18432\n"
    "ds_read_b64_tr_b16 a[188:189], v246 offset:49152\n"
    "v_mfma_f32_16x16x32_bf16 v[12:15], a[168:171], v[204:207], v[12:15]\n"
    "v_cvt_pk_bf16_f32 v11, v10, v11\n"
    "v_cvt_pk_bf16_f32 v10, v8, v9\n"
    "v_mfma_f32_16x16x32_bf16 v[16:19], a[168:171], v[212:215], v[16:19]\n"
    "ds_read_b64_tr_b16 a[190:191], v246 offset:51200\n"
    "v_accvgpr_read_b32 v8, a244\n"
    "v_mfma_f32_16x16x32_bf16 v[16:19], a[172:175], v[216:219], v[16:19]\n"
    "s_waitcnt lgkmcnt(5)\n"
    "v_accvgpr_read_b32 v9, a245\n"
    "v_accvgpr_read_b32 v255, a247\n"
    "v_mfma_f32_16x16x32_bf16 v[36:39], a[176:179], v[180:183], v[36:39]\n"
    "ds_read_b64_tr_b16 a[168:169], v247 offset:0\n"
    "v_cvt_pk_bf16_f32 v8, v8, v9\n"
    "v_mfma_f32_16x16x32_bf16 v[36:39], a[180:183], v[184:187], v[36:39]\n"
    "v_accvgpr_read_b32 v9, a246\n"
    "v_cvt_pk_bf16_f32 v9, v9, v255\n"
    "v_mfma_f32_16x16x32_bf16 v[32:35], a[180:183], v[152:155], v[32:35]\n"
    "buffer_store_dwordx4 v[8:11], v250, s[8:11], s82 offen offset:0 sc1\n"
    "v_mfma_f32_16x16x32_bf16 v[32:35], a[176:179], v[148:151], v[32:35]\n"
    "ds_read_b64_tr_b16 a[162:163], v247 offset:26624\n"
    "ds_read_b64_tr_b16 a[164:165], v247 offset:57344\n"
    "v_mfma_f32_16x16x32_bf16 v[28:31], a[176:179], v[140:143], v[28:31]\n"
    "ds_read_b64_tr_b16 a[166:167], v247 offset:59392\n"
    "v_cvt_pk_bf16_f32 v15, v14, v15\n"
    "v_mfma_f32_16x16x32_bf16 v[28:31], a[180:183], v[144:147], v[28:31]\n"
    "s_waitcnt lgkmcnt(4)\n"
    "ds_read_b64_tr_b16 a[170:171], v247 offset:2048\n"
    "ds_read_b64_tr_b16 a[172:173], v247 offset:32768\n"
    "v_mfma_f32_16x16x32_bf16 v[28:31], a[184:187], v[196:199], v[28:31]\n"
    "buffer_load_dwordx4 a[104:107], v250, s[8:11], s62 offen offset:0 sc1\n"
    "v_mfma_f32_16x16x32_bf16 v[28:31], a[188:191], v[200:203], v[28:31]\n"
    "ds_read_b64_tr_b16 a[174:175], v247 offset:34816\n"
    "v_cvt_pk_bf16_f32 v14, v12, v13\n"
    "v_mfma_f32_16x16x32_bf16 v[32:35], a[188:191], v[208:211], v[32:35]\n"
    "ds_read_b64_tr_b16 a[176:177], v247 offset:8192\n"
    "v_accvgpr_read_b32 v12, a248\n"
    "v_mfma_f32_16x16x32_bf16 v[32:35], a[184:187], v[204:207], v[32:35]\n"
    "ds_read_b64_tr_b16 a[178:179], v247 offset:10240\n"
    "v_mfma_f32_16x16x32_bf16 v[36:39], a[184:187], v[212:215], v[36:39]\n"
    "ds_read_b64_tr_b16 a[180:181], v247 offset:40960\n"
    "v_accvgpr_read_b32 v13, a249\n"
    "v_mfma_f32_16x16x32_bf16 v[36:39], a[188:191], v[216:219], v[36:39]\n"
    "s_waitcnt lgkmcnt(6)\n"
    "ds_read_b64_tr_b16 a[184:185], v247 offset:16384\n"
    "v_accvgpr_read_b32 v255, a251\n"
    "v_mfma_f32_16x16x32_bf16 v[56:59], a[160:163], a[208:211], v[56:59]\n"
    "buffer_load_dwordx4 a[108:111], v250, s[8:11], s62 offen offset:1024 sc1\n"
    "v_mfma_f32_16x16x32_bf16 v[56:59], a[164:167], a[212:215], v[56:59]\n"
    "v_cvt_pk_bf16_f32 v12, v12, v13\n"
    "v_cvt_pk_bf16_f32 v32, v32, v33\n"
    "v_mfma_f32_16x16x32_bf16 v[52:55], a[164:167], a[204:207], v[52:55]\n"
    "ds_read_b64_tr_b16 a[186:187], v247 offset:18432\n"
    "v_accvgpr_read_b32 v13, a250\n"
    "v_mfma_f32_16x16x32_bf16 v[52:55], a[160:163], a[200:203], v[52:55]\n"
    "ds_read_b64_tr_b16 a[182:183], v247 offset:43008\n"
    "v_cvt_pk_bf16_f32 v13, v13, v255\n"
    "v_mfma_f32_16x16x32_bf16 v[48:51], a[160:163], a[192:195], v[48:51]\n"
    "buffer_store_dwordx4 v[12:15], v250, s[8:11], s28 offen offset:0 sc1\n"
    "v_mfma_f32_16x16x32_bf16 v[48:51], a[164:167], a[196:199], v[48:51]\n"
    "s_waitcnt lgkmcnt(6)\n"
    "ds_read_b64_tr_b16 a[160:161], v244 offset:28672\n"
    "v_cvt_pk_bf16_f32 v19, v18, v19\n"
    "v_mfma_f32_16x16x32_bf16 v[48:51], a[168:171], v[156:159], v[48:51]\n"
    "s_add_u32 s62, s57, 16384\n"
    "ds_read_b64_tr_b16 a[188:189], v247 offset:49152\n"
    "v_cvt_pk_bf16_f32 v28, v28, v29\n"
    "v_mfma_f32_16x16x32_bf16 v[48:51], a[172:175], v[160:163], v[48:51]\n"
    "buffer_load_dwordx4 a[112:115], v250, s[8:11], s62 offen offset:2048 sc1\n"
    "v_mfma_f32_16x16x32_bf16 v[52:55], a[172:175], v[168:171], v[52:55]\n"
    "ds_read_b64_tr_b16 a[190:191], v247 offset:51200\n"
    "v_mfma_f32_16x16x32_bf16 v[52:55], a[168:171], v[164:167], v[52:55]\n"
    "ds_read_b64_tr_b16 a[162:163], v244 offset:30720\n"
    "v_mfma_f32_16x16x32_bf16 v[56:59], a[168:171], v[172:175], v[56:59]\n"
    "ds_read_b64_tr_b16 a[164:165], v244 offset:61440\n"
    "v_mfma_f32_16x16x32_bf16 v[56:59], a[172:175], v[176:179], v[56:59]\n"
    "s_waitcnt lgkmcnt(5)\n"
    "ds_read_b64_tr_b16 a[168:169], v244 offset:4096\n"
    "v_mfma_f32_16x16x32_bf16 v[56:59], a[176:179], v[180:183], v[56:59]\n"
    "v_cvt_pk_bf16_f32 v18, v16, v17\n"
    "v_accvgpr_read_b32 v16, a252\n"
    "v_mfma_f32_16x16x32_bf16 v[56:59], a[180:183], v[184:187], v[56:59]\n"
    "v_accvgpr_read_b32 v17, a253\n"
    "v_accvgpr_read_b32 v255, a255\n"
    "v_mfma_f32_16x16x32_bf16 v[52:55], a[180:183], v[152:155], v[52:55]\n"
    "ds_read_b64_tr_b16 a[170:171], v244 offset:6144\n"
    "v_cvt_pk_bf16_f32 v16, v16, v17\n"
    "v_mfma_f32_16x16x32_bf16 v[52:55], a[176:179], v[148:151], v[52:55]\n"
    "buffer_load_dwordx4 a[116:119], v250, s[8:11], s62 offen offset:3072 sc1\n"
    "v_mfma_f32_16x16x32_bf16 v[48:51], a[176:179], v[140:143], v[48:51]\n"
    "s_add_u32 s62, s57, 20480\n"
    "ds_read_b64_tr_b16 a[166:167], v244 offset:63488\n"
    "v_accvgpr_read_b32 v17, a254\n"
    "v_mfma_f32_16x16x32_bf16 v[48:51], a[180:183], v[144:147], v[48:51]\n"
    "s_waitcnt lgkmcnt(5)\n"
    "v_cvt_pk_bf16_f32 v17, v17, v255\n"
    "v_mfma_f32_16x16x32_bf16 v[48:51], a[184:187], v[196:199], v[48:51]\n"
    "buffer_store_dwordx4 v[16:19], v250, s[8:11], s29 offen offset:0 sc1\n"
    "v_mfma_f32_16x16x32_bf16 v[48:51], a[188:191], v[200:203], v[48:51]\n"
    "ds_read_b64_tr_b16 a[176:177], v244 offset:12288\n"
    "v_cvt_pk_bf16_f32 v33, v34, v35\n"
    "v_mfma_f32_16x16x32_bf16 v[52:55], a[188:191], v[208:211], v[52:55]\n"
    "ds_read_b64_tr_b16 a[178:179], v244 offset:14336\n"
    "v_mfma_f32_16x16x32_bf16 v[52:55], a[184:187], v[204:207], v[52:55]\n"
    "ds_read_b64_tr_b16 a[172:173], v244 offset:36864\n"
    "v_mfma_f32_16x16x32_bf16 v[56:59], a[184:187], v[212:215], v[56:59]\n"
    "ds_read_b64_tr_b16 a[174:175], v244 offset:38912\n"
    "v_mfma_f32_16x16x32_bf16 v[56:59], a[188:191], v[216:219], v[56:59]\n"
    "s_waitcnt lgkmcnt(4)\n"
    "ds_read_b64_tr_b16 a[180:181], v244 offset:45056\n"
    "v_cvt_pk_bf16_f32 v29, v30, v31\n"
    "v_mfma_f32_16x16x32_bf16 v[76:79], a[160:163], a[208:211], v[76:79]\n"
    "buffer_load_dwordx4 a[120:123], v250, s[8:11], s62 offen offset:0 sc1\n"
    "v_mfma_f32_16x16x32_bf16 v[76:79], a[164:167], a[212:215], v[76:79]\n"
    "ds_read_b64_tr_b16 a[184:185], v244 offset:20480\n"
    "v_cvt_pk_bf16_f32 v30, v48, v49\n"
    "v_mfma_f32_16x16x32_bf16 v[72:75], a[164:167], a[204:207], v[72:75]\n"
    "ds_read_b64_tr_b16 a[182:183], v244 offset:47104\n"
    "v_mfma_f32_16x16x32_bf16 v[72:75], a[160:163], a[200:203], v[72:75]\n"
    "ds_read_b64_tr_b16 a[186:187], v244 offset:22528\n"
    "v_mfma_f32_16x16x32_bf16 v[68:71], a[160:163], a[192:195], v[68:71]\n"
    "v_cvt_pk_bf16_f32 v36, v36, v37\n"
    "v_cvt_pk_bf16_f32 v37, v38, v39\n"
    "v_mfma_f32_16x16x32_bf16 v[68:71], a[164:167], a[196:199], v[68:71]\n"
    "s_waitcnt lgkmcnt(4)\n"
    "ds_read_b64_tr_b16 a[160:161], v245 offset:28672\n"
    "v_mfma_f32_16x16x32_bf16 v[68:71], a[168:171], v[156:159], v[68:71]\n"
    "ds_read_b64_tr_b16 a[162:163], v245 offset:30720\n"
    "v_cvt_pk_bf16_f32 v31, v50, v51\n"
    "v_mfma_f32_16x16x32_bf16 v[68:71], a[172:175], v[160:163], v[68:71]\n"
    "ds_read_b64_tr_b16 a[188:189], v244 offset:53248\n"
    "v_lshl_or_b32 v252, v253, 2, v252\n"
    "v_mfma_f32_16x16x32_bf16 v[72:75], a[172:175], v[168:171], v[72:75]\n"
    "buffer_store_dwordx4 v[28:31], v250, s[8:11], s82 offen offset:1024 sc1\n"
    "v_mfma_f32_16x16x32_bf16 v[72:75], a[168:171], v[164:167], v[72:75]\n"
    "ds_read_b64_tr_b16 a[190:191], v244 offset:55296\n"
    "v_mfma_f32_16x16x32_bf16 v[76:79], a[168:171], v[172:175], v[76:79]\n"
    "v_cvt_pk_bf16_f32 v38, v56, v57\n"
    "v_mfma_f32_16x16x32_bf16 v[76:79], a[172:175], v[176:179], v[76:79]\n"
    "s_waitcnt lgkmcnt(5)\n"
    "ds_read_b64_tr_b16 a[168:169], v245 offset:4096\n"
    "v_cvt_pk_bf16_f32 v34, v52, v53\n"
    "v_mfma_f32_16x16x32_bf16 v[76:79], a[176:179], v[180:183], v[76:79]\n"
    "buffer_load_dwordx4 a[124:127], v250, s[8:11], s62 offen offset:1024 sc1\n"
    "v_mfma_f32_16x16x32_bf16 v[76:79], a[180:183], v[184:187], v[76:79]\n"
    "v_mov_b32_e32 v253, 0x3f80\n"
    "v_mfma_f32_16x16x32_bf16 v[72:75], a[180:183], v[152:155], v[72:75]\n"
    "ds_read_b64_tr_b16 a[164:165], v245 offset:61440\n"
    "v_mov_b64_e32 v[232:233], 0\n"
    "v_mfma_f32_16x16x32_bf16 v[72:75], a[176:179], v[148:151], v[72:75]\n"
    "ds_read_b64_tr_b16 a[166:167], v245 offset:63488\n"
    "v_mfma_f32_16x16x32_bf16 v[68:71], a[176:179], v[140:143], v[68:71]\n"
    "ds_read_b64_tr_b16 a[170:171], v245 offset:6144\n"
    "v_mfma_f32_16x16x32_bf16 v[68:71], a[180:183], v[144:147], v[68:71]\n"
    "s_waitcnt lgkmcnt(4)\n"
    "ds_read_b64_tr_b16 a[176:177], v245 offset:12288\n"
    "v_cvt_pk_bf16_f32 v35, v54, v55\n"
    "v_mfma_f32_16x16x32_bf16 v[68:71], a[184:187], v[196:199], v[68:71]\n"
    "ds_read_b64_tr_b16 a[178:179], v245 offset:14336\n"
    "v_mfma_f32_16x16x32_bf16 v[68:71], a[188:191], v[200:203], v[68:71]\n"
    "ds_read_b64_tr_b16 a[172:173], v245 offset:36864\n"
    "v_mfma_f32_16x16x32_bf16 v[72:75], a[188:191], v[208:211], v[72:75]\n"
    "ds_read_b64_tr_b16 a[174:175], v245 offset:38912\n"
    "v_mfma_f32_16x16x32_bf16 v[72:75], a[184:187], v[204:207], v[72:75]\n"
    "ds_read_b64_tr_b16 a[180:181], v245 offset:45056\n"
    "v_mfma_f32_16x16x32_bf16 v[76:79], a[184:187], v[212:215], v[76:79]\n"
    "buffer_store_dwordx4 v[32:35], v250, s[8:11], s28 offen offset:1024 sc1\n"
    "v_mfma_f32_16x16x32_bf16 v[76:79], a[188:191], v[216:219], v[76:79]\n"
    "s_waitcnt lgkmcnt(6)\n"
    "v_mfma_f32_16x16x32_bf16 v[96:99], a[160:163], a[208:211], v[96:99]\n"
    "ds_read_b64_tr_b16 a[182:183], v245 offset:47104\n"
    "v_cvt_pk_bf16_f32 v39, v58, v59\n"
    "v_mfma_f32_16x16x32_bf16 v[96:99], a[164:167], a[212:215], v[96:99]\n"
    "buffer_load_dwordx4 a[128:131], v250, s[8:11], s62 offen offset:2048 sc1\n"
    "v_mfma_f32_16x16x32_bf16 v[92:95], a[164:167], a[204:207], v[92:95]\n"
    "ds_read_b64_tr_b16 a[184:185], v245 offset:20480\n"
    "v_mfma_f32_16x16x32_bf16 v[92:95], a[160:163], a[200:203], v[92:95]\n"
    "ds_read_b64_tr_b16 a[186:187], v245 offset:22528\n"
    "v_mfma_f32_16x16x32_bf16 v[88:91], a[160:163], a[192:195], v[88:91]\n"
    "ds_read_b64_tr_b16 a[188:189], v245 offset:53248\n"
    "v_mfma_f32_16x16x32_bf16 v[88:91], a[164:167], a[196:199], v[88:91]\n"
    "s_waitcnt lgkmcnt(5)\n"
    "ds_read_b64_tr_b16 a[160:161], v246 offset:28672\n"
    "v_mfma_f32_16x16x32_bf16 v[88:91], a[168:171], v[156:159], v[88:91]\n"
    "ds_read_b64_tr_b16 a[162:163], v246 offset:30720\n"
    "v_mov_b64_e32 v[236:237], 0\n"
    "v_mfma_f32_16x16x32_bf16 v[88:91], a[172:175], v[160:163], v[88:91]\n"
    "v_cvt_pk_bf16_f32 v68, v68, v69\n"
    "v_cvt_pk_bf16_f32 v69, v70, v71\n"
    "v_mfma_f32_16x16x32_bf16 v[92:95], a[172:175], v[168:171], v[92:95]\n"
    "ds_read_b64_tr_b16 a[164:165], v246 offset:61440\n"
    "v_cmp_eq_u32_e32 vcc, 0, v252\n"
    "v_mfma_f32_16x16x32_bf16 v[92:95], a[168:171], v[164:167], v[92:95]\n"
    "buffer_load_dwordx4 a[132:135], v250, s[8:11], s62 offen offset:3072 sc1\n"
    "v_mfma_f32_16x16x32_bf16 v[96:99], a[168:171], v[172:175], v[96:99]\n"
    "s_add_u32 s62, s57, 24576\n"
    "ds_read_b64_tr_b16 a[190:191], v245 offset:55296\n"
    "v_mfma_f32_16x16x32_bf16 v[96:99], a[172:175], v[176:179], v[96:99]\n"
    "s_waitcnt lgkmcnt(7)\n"
    "ds_read_b64_tr_b16 a[168:169], v246 offset:4096\n"
    "v_mfma_f32_16x16x32_bf16 v[96:99], a[176:179], v[180:183], v[96:99]\n"
    "buffer_store_dwordx4 v[36:39], v250, s[8:11], s29 offen offset:1024 sc1\n"
    "v_mfma_f32_16x16x32_bf16 v[96:99], a[180:183], v[184:187], v[96:99]\n"
    "ds_read_b64_tr_b16 a[166:167], v246 offset:63488\n"
    "v_cndmask_b32_e32 v234, 0, v253, vcc\n"
    "v_mfma_f32_16x16x32_bf16 v[92:95], a[180:183], v[152:155], v[92:95]\n"
    "v_mfma_f32_16x16x32_bf16 v[92:95], a[176:179], v[148:151], v[92:95]\n"
    "ds_read_b64_tr_b16 a[170:171], v246 offset:6144\n"
    "v_mfma_f32_16x16x32_bf16 v[88:91], a[176:179], v[140:143], v[88:91]\n"
    "ds_read_b64_tr_b16 a[172:173], v246 offset:36864\n"
    "v_cvt_pk_bf16_f32 v72, v72, v73\n"
    "v_mfma_f32_16x16x32_bf16 v[88:91], a[180:183], v[144:147], v[88:91]\n"
    "s_waitcnt lgkmcnt(4)\n"
    "ds_read_b64_tr_b16 a[174:175], v246 offset:38912\n"
    "v_mfma_f32_16x16x32_bf16 v[88:91], a[184:187], v[196:199], v[88:91]\n"
    "buffer_load_dwordx4 a[136:139], v250, s[8:11], s62 offen offset:0 sc1\n"
    "v_mfma_f32_16x16x32_bf16 v[88:91], a[188:191], v[200:203], v[88:91]\n"
    "ds_read_b64_tr_b16 a[176:177], v246 offset:12288\n"
    "v_cmp_eq_u32_e32 vcc, 1, v252\n"
    "v_mfma_f32_16x16x32_bf16 v[92:95], a[188:191], v[208:211], v[92:95]\n"
    "v_cndmask_b32_e64 v234, v234, 1.0, vcc\n"
    "v_cvt_pk_bf16_f32 v73, v74, v75\n"
    "v_mfma_f32_16x16x32_bf16 v[92:95], a[184:187], v[204:207], v[92:95]\n"
    "v_cmp_eq_u32_e32 vcc, 2, v252\n"
    "v_mfma_f32_16x16x32_bf16 v[96:99], a[184:187], v[212:215], v[96:99]\n"
    "v_cvt_pk_bf16_f32 v70, v88, v89\n"
    "v_mfma_f32_16x16x32_bf16 v[96:99], a[188:191], v[216:219], v[96:99]\n"
    "s_waitcnt lgkmcnt(4)\n"
    "ds_read_b64_tr_b16 a[178:179], v246 offset:14336\n"
    "v_mfma_f32_16x16x32_bf16 v[116:119], a[160:163], a[208:211], v[116:119]\n"
    "ds_read_b64_tr_b16 a[180:181], v246 offset:45056\n"
    "v_mfma_f32_16x16x32_bf16 v[116:119], a[164:167], a[212:215], v[116:119]\n"
    "v_cvt_pk_bf16_f32 v71, v90, v91\n"
    "v_mfma_f32_16x16x32_bf16 v[112:115], a[164:167], a[204:207], v[112:115]\n"
    "buffer_store_dwordx4 v[68:71], v250, s[8:11], s82 offen offset:2048 sc1\n"
    "v_mfma_f32_16x16x32_bf16 v[112:115], a[160:163], a[200:203], v[112:115]\n"
    "ds_read_b64_tr_b16 a[182:183], v246 offset:47104\n"
    "v_cvt_pk_bf16_f32 v76, v76, v77\n"
    "v_mfma_f32_16x16x32_bf16 v[108:111], a[160:163], a[192:195], v[108:111]\n"
    "ds_read_b64_tr_b16 a[184:185], v246 offset:20480\n"
    "v_mfma_f32_16x16x32_bf16 v[108:111], a[164:167], a[196:199], v[108:111]\n"
    "s_waitcnt lgkmcnt(5)\n"
    "ds_read_b64_tr_b16 a[186:187], v246 offset:22528\n"
    "v_cvt_pk_bf16_f32 v77, v78, v79\n"
    "v_mfma_f32_16x16x32_bf16 v[108:111], a[168:171], v[156:159], v[108:111]\n"
    "buffer_load_dwordx4 a[140:143], v250, s[8:11], s62 offen offset:1024 sc1\n"
    "v_mfma_f32_16x16x32_bf16 v[108:111], a[172:175], v[160:163], v[108:111]\n"
    "ds_read_b64_tr_b16 a[188:189], v246 offset:53248\n"
    "v_mfma_f32_16x16x32_bf16 v[112:115], a[172:175], v[168:171], v[112:115]\n"
    "ds_read_b64_tr_b16 a[190:191], v246 offset:55296\n"
    "v_mfma_f32_16x16x32_bf16 v[112:115], a[168:171], v[164:167], v[112:115]\n"
    "ds_read_b64_tr_b16 a[160:161], v247 offset:28672\n"
    "v_cvt_pk_bf16_f32 v74, v92, v93\n"
    "v_mfma_f32_16x16x32_bf16 v[116:119], a[168:171], v[172:175], v[116:119]\n"
    "ds_read_b64_tr_b16 a[162:163], v247 offset:30720\n"
    "v_cvt_pk_bf16_f32 v78, v96, v97\n"
    "v_mfma_f32_16x16x32_bf16 v[116:119], a[172:175], v[176:179], v[116:119]\n"
    "s_waitcnt lgkmcnt(6)\n"
    "v_cvt_pk_bf16_f32 v75, v94, v95\n"
    "v_mfma_f32_16x16x32_bf16 v[116:119], a[176:179], v[180:183], v[116:119]\n"
    "ds_read_b64_tr_b16 a[164:165], v247 offset:61440\n"
    "v_mfma_f32_16x16x32_bf16 v[116:119], a[180:183], v[184:187], v[116:119]\n"
    "ds_read_b64_tr_b16 a[166:167], v247 offset:63488\n"
    "v_mfma_f32_16x16x32_bf16 v[112:115], a[180:183], v[152:155], v[112:115]\n"
    "ds_read_b64_tr_b16 a[168:169], v247 offset:4096\n"
    "v_cvt_pk_bf16_f32 v79, v98, v99\n"
    "v_mfma_f32_16x16x32_bf16 v[112:115], a[176:179], v[148:151], v[112:115]\n"
    "ds_read_b64_tr_b16 a[170:171], v247 offset:6144\n"
    "v_cndmask_b32_e32 v235, 0, v253, vcc\n"
    "v_mfma_f32_16x16x32_bf16 v[108:111], a[176:179], v[140:143], v[108:111]\n"
    "buffer_store_dwordx4 v[72:75], v250, s[8:11], s28 offen offset:2048 sc1\n"
    "v_mfma_f32_16x16x32_bf16 v[108:111], a[180:183], v[144:147], v[108:111]\n"
    "s_waitcnt lgkmcnt(6)\n"
    "ds_read_b64_tr_b16 a[172:173], v247 offset:36864\n"
    "v_mfma_f32_16x16x32_bf16 v[108:111], a[184:187], v[196:199], v[108:111]\n"
    "ds_read_b64_tr_b16 a[176:177], v247 offset:12288\n"
    "v_mfma_f32_16x16x32_bf16 v[108:111], a[188:191], v[200:203], v[108:111]\n"
    "buffer_load_dwordx4 a[144:147], v250, s[8:11], s62 offen offset:2048 sc1\n"
    "v_mfma_f32_16x16x32_bf16 v[112:115], a[188:191], v[208:211], v[112:115]\n"
    "ds_read_b64_tr_b16 a[178:179], v247 offset:14336\n"
    "v_mfma_f32_16x16x32_bf16 v[112:115], a[184:187], v[204:207], v[112:115]\n"
    "v_cmp_eq_u32_e32 vcc, 3, v252\n"
    "v_cvt_pk_bf16_f32 v108, v108, v109\n"
    "v_mfma_f32_16x16x32_bf16 v[116:119], a[184:187], v[212:215], v[116:119]\n"
    "ds_read_b64_tr_b16 a[174:175], v247 offset:38912\n"
    "v_cvt_pk_bf16_f32 v109, v110, v111\n"
    "v_mfma_f32_16x16x32_bf16 v[116:119], a[188:191], v[216:219], v[116:119]\n"
    "s_waitcnt lgkmcnt(6)\n"
    "v_cndmask_b32_e64 v235, v235, 1.0, vcc\n"
    "v_mfma_f32_16x16x32_bf16 v[136:139], a[160:163], a[208:211], v[136:139]\n"
    "s_cmp_lg_u32 s44, 0\n"
    "s_cbranch_scc1 .Lat_glate\n"
    ".Lat_glate_ret:\n"
    "v_mfma_f32_16x16x32_bf16 v[136:139], a[164:167], a[212:215], v[136:139]\n"
    "ds_read_b64_tr_b16 a[180:181], v247 offset:45056\n"
    "v_mfma_f32_16x16x32_bf16 v[132:135], a[164:167], a[204:207], v[132:135]\n"
    "v_mfma_f32_16x16x32_bf16 v[132:135], a[160:163], a[200:203], v[132:135]\n"
    "buffer_load_dwordx4 a[148:151], v250, s[8:11], s62 offen offset:3072 sc1\n"
    "v_mfma_f32_16x16x32_bf16 v[128:131], a[160:163], a[192:195], v[128:131]\n"
    "s_add_u32 s62, s57, 28672\n"
    "ds_read_b64_tr_b16 a[182:183], v247 offset:47104\n"
    "v_cvt_pk_bf16_f32 v116, v116, v117\n"
    "v_mfma_f32_16x16x32_bf16 v[128:131], a[164:167], a[196:199], v[128:131]\n"
    "s_waitcnt lgkmcnt(2)\n"
    "ds_read_b64_tr_b16 a[184:185], v247 offset:20480\n"
    "v_mfma_f32_16x16x32_bf16 v[128:131], a[168:171], v[156:159], v[128:131]\n"
    "buffer_store_dwordx4 v[76:79], v250, s[8:11], s29 offen offset:2048 sc1\n"
    "v_mfma_f32_16x16x32_bf16 v[128:131], a[172:175], v[160:163], v[128:131]\n"
    "ds_read_b64_tr_b16 a[186:187], v247 offset:22528\n"
    "v_mfma_f32_16x16x32_bf16 v[132:135], a[172:175], v[168:171], v[132:135]\n"
    "ds_read_b64_tr_b16 a[188:189], v247 offset:53248\n"
    "v_mfma_f32_16x16x32_bf16 v[132:135], a[168:171], v[164:167], v[132:135]\n"
    "ds_read_b64_tr_b16 a[190:191], v247 offset:55296\n"
    "v_mfma_f32_16x16x32_bf16 v[136:139], a[168:171], v[172:175], v[136:139]\n"
    "ds_read_b128 a[160:163], v240 offset:24576\n"
    "v_mfma_f32_16x16x32_bf16 v[136:139], a[172:175], v[176:179], v[136:139]\n"
    "s_waitcnt lgkmcnt(5)\n"
    "ds_read_b128 a[168:171], v240 offset:28672\n"
    "v_mfma_f32_16x16x32_bf16 v[136:139], a[176:179], v[180:183], v[136:139]\n"
    "buffer_load_dwordx4 a[152:155], v250, s[8:11], s62 offen offset:0 sc1\n"
    "v_mfma_f32_16x16x32_bf16 v[136:139], a[180:183], v[184:187], v[136:139]\n"
    "ds_read_b128 a[164:167], v241 offset:24576\n"
    "v_mfma_f32_16x16x32_bf16 v[132:135], a[180:183], v[152:155], v[132:135]\n"
    "v_mfma_f32_16x16x32_bf16 v[132:135], a[176:179], v[148:151], v[132:135]\n"
    "v_mfma_f32_16x16x32_bf16 v[128:131], a[176:179], v[140:143], v[128:131]\n"
    "ds_read_b128 a[172:175], v241 offset:28672\n"
    "v_mfma_f32_16x16x32_bf16 v[128:131], a[180:183], v[144:147], v[128:131]\n"
    "s_waitcnt lgkmcnt(4)\n"
    "v_mfma_f32_16x16x32_bf16 v[128:131], a[184:187], v[196:199], v[128:131]\n"
    "v_cvt_pk_bf16_f32 v112, v112, v113\n"
    "v_mfma_f32_16x16x32_bf16 v[128:131], a[188:191], v[200:203], v[128:131]\n"
    "v_cvt_pk_bf16_f32 v117, v118, v119\n"
    "v_mfma_f32_16x16x32_bf16 v[132:135], a[188:191], v[208:211], v[132:135]\n"
    "buffer_load_dwordx4 a[156:159], v250, s[8:11], s62 offen offset:1024 sc1\n"
    "v_mfma_f32_16x16x32_bf16 v[132:135], a[184:187], v[204:207], v[132:135]\n"
    "v_cvt_pk_bf16_f32 v110, v128, v129\n"
    "v_mfma_f32_16x16x32_bf16 v[136:139], a[184:187], v[212:215], v[136:139]\n"
    "ds_read_b128 a[176:179], v242 offset:24576\n"
    "v_mfma_f32_16x16x32_bf16 v[136:139], a[188:191], v[216:219], v[136:139]\n"
    "s_waitcnt lgkmcnt(2)\n"
    "ds_read_b128 a[180:183], v243 offset:24576\n"
    "v_cvt_pk_bf16_f32 v111, v130, v131\n"
    "v_mfma_f32_16x16x32_bf16 v[164:167], a[160:163], a[32:35], v[224:227]\n"
    "buffer_store_dwordx4 v[108:111], v250, s[8:11], s82 offen offset:3072 sc1\n"
    "v_mfma_f32_16x16x32_bf16 v[164:167], a[164:167], a[36:39], v[164:167]\n"
    "ds_read_b128 a[184:187], v242 offset:28672\n"
    "v_mfma_f32_16x16x32_bf16 v[156:159], a[164:167], a[4:7], v[220:223]\n"
    "v_cvt_pk_bf16_f32 v118, v136, v137\n"
    "v_mfma_f32_16x16x32_bf16 v[156:159], a[160:163], a[0:3], v[156:159]\n"
    "s_waitcnt lgkmcnt(3)\n"
    "ds_read_b128 a[188:191], v243 offset:28672\n"
    "v_mfma_f32_16x16x32_bf16 v[156:159], a[168:171], a[8:11], v[156:159]\n"
    "buffer_store_dword v131, v231, s[8:11], s87 offen offset:0 sc1\n"
    "v_mfma_f32_16x16x32_bf16 v[156:159], a[172:175], a[12:15], v[156:159]\n"
    "v_cvt_pk_bf16_f32 v119, v138, v139\n"
    "v_mfma_f32_16x16x32_bf16 v[164:167], a[172:175], a[44:47], v[164:167]\n"
    "v_cvt_pk_bf16_f32 v113, v114, v115\n"
    "v_mfma_f32_16x16x32_bf16 v[164:167], a[168:171], a[40:43], v[164:167]\n"
    "s_waitcnt lgkmcnt(2)\n"
    "ds_read_b128 a[160:163], v240 offset:26624\n"
    "v_mfma_f32_16x16x32_bf16 v[164:167], a[176:179], a[48:51], v[164:167]\n"
    "ds_read_b128 a[164:167], v241 offset:26624\n"
    "v_cvt_pk_bf16_f32 v114, v132, v133\n"
    "v_mfma_f32_16x16x32_bf16 v[164:167], a[180:183], a[52:55], v[164:167]\n"
    "buffer_store_dword v135, v231, s[8:11], s88 offen offset:0 sc1\n"
    "v_mfma_f32_16x16x32_bf16 v[156:159], a[180:183], a[20:23], v[156:159]\n"
    "ds_read_b128 a[168:171], v240 offset:30720\n"
    "v_mfma_f32_16x16x32_bf16 v[156:159], a[176:179], a[16:19], v[156:159]\n"
    "s_waitcnt lgkmcnt(3)\n"
    "ds_read_b128 a[176:179], v242 offset:26624\n"
    "v_cvt_pk_bf16_f32 v115, v134, v135\n"
    "v_mfma_f32_16x16x32_bf16 v[156:159], a[184:187], a[24:27], v[156:159]\n"
    "ds_read_b128 a[172:175], v241 offset:30720\n"
    "v_mfma_f32_16x16x32_bf16 v[156:159], a[188:191], a[28:31], v[156:159]\n"
    "ds_read_b128 a[180:183], v243 offset:26624\n"
    "v_mfma_f32_16x16x32_bf16 v[164:167], a[188:191], a[60:63], v[164:167]\n"
    "buffer_store_dwordx4 v[112:115], v250, s[8:11], s28 offen offset:3072 sc1\n"
    "v_mfma_f32_16x16x32_bf16 v[164:167], a[184:187], a[56:59], v[164:167]\n"
    "s_waitcnt lgkmcnt(4)\n"
    "v_exp_f32_e32 v156, v156\n"
    "v_mfma_f32_16x16x32_bf16 v[168:171], a[160:163], a[32:35], v[224:227]\n"
    "v_exp_f32_e32 v157, v157\n"
    "v_mfma_f32_16x16x32_bf16 v[168:171], a[164:167], a[36:39], v[168:171]\n"
    "buffer_store_dword v139, v231, s[8:11], s89 offen offset:0 sc1\n"
    "v_mfma_f32_16x16x32_bf16 v[160:163], a[164:167], a[4:7], v[220:223]\n"
    "s_mov_b32 s56, 0\n"
    "ds_read_b128 a[184:187], v242 offset:30720\n"
    "ds_read_b128 a[188:191], v243 offset:30720\n"
    "v_mfma_f32_16x16x32_bf16 v[160:163], a[160:163], a[0:3], v[160:163]\n"
    "s_waitcnt lgkmcnt(3)\n"
    "ds_read_b128 a[160:163], v240 offset:0\n"
    "v_add_f32_e32 v128, v131, v135\n"
    "v_mfma_f32_16x16x32_bf16 v[160:163], a[168:171], a[8:11], v[160:163]\n"
    "buffer_load_dwordx4 v[180:183], v250, s[8:11], s62 offen offset:2048 sc1\n"
    "v_mfma_f32_16x16x32_bf16 v[160:163], a[172:175], a[12:15], v[160:163]\n"
    "v_exp_f32_e32 v158, v158\n"
    "v_mfma_f32_16x16x32_bf16 v[168:171], a[172:175], a[44:47], v[168:171]\n"
    "ds_read_b128 a[164:167], v241 offset:0\n"
    "v_min3_f32 v129, v131, v135, v139\n"
    "v_mfma_f32_16x16x32_bf16 v[168:171], a[168:171], a[40:43], v[168:171]\n"
    "s_waitcnt lgkmcnt(4)\n"
    "v_exp_f32_e32 v159, v159\n"
    "v_mfma_f32_16x16x32_bf16 v[168:171], a[176:179], a[48:51], v[168:171]\n"
    "ds_read_b128 a[168:171], v240 offset:4096\n"
    "v_add_f32_e32 v128, v128, v139\n"
    "v_mfma_f32_16x16x32_bf16 v[168:171], a[180:183], a[52:55], v[168:171]\n"
    "ds_read_b128 a[172:175], v241 offset:4096\n"
    "v_cvt_pk_bf16_f32 v156, v156, v157\n"
    "v_mfma_f32_16x16x32_bf16 v[160:163], a[180:183], a[20:23], v[160:163]\n"
    "v_exp_f32_e32 v164, v164\n"
    "v_mfma_f32_16x16x32_bf16 v[160:163], a[176:179], a[16:19], v[160:163]\n"
    "s_waitcnt lgkmcnt(4)\n"
    "ds_write_addtid_b32 v156 offset:0\n"
    "v_mfma_f32_16x16x32_bf16 v[160:163], a[184:187], a[24:27], v[160:163]\n"
    "buffer_store_dwordx4 v[116:119], v250, s[8:11], s29 offen offset:3072 sc1\n"
    "v_mfma_f32_16x16x32_bf16 v[160:163], a[188:191], a[28:31], v[160:163]\n"
    "ds_read_b128 a[176:179], v242 offset:0\n"
    "v_mfma_f32_16x16x32_bf16 v[168:171], a[188:191], a[60:63], v[168:171]\n"
    "v_exp_f32_e32 v165, v165\n"
    "v_mfma_f32_16x16x32_bf16 v[168:171], a[184:187], a[56:59], v[168:171]\n"
    "s_waitcnt lgkmcnt(4)\n"
    "ds_read_b128 a[180:183], v243 offset:0\n"
    "ds_read_b128 a[184:187], v242 offset:4096\n"
    "v_mfma_f32_16x16x32_bf16 v[204:207], a[160:163], a[32:35], v[224:227]\n"
    "buffer_load_dwordx4 v[184:187], v250, s[8:11], s62 offen offset:3072 sc1\n"
    "v_mfma_f32_16x16x32_bf16 v[204:207], a[164:167], a[36:39], v[204:207]\n"
    "s_add_u32 s62, s58, 0\n"
    "ds_read_b128 a[188:191], v243 offset:4096\n"
    "v_cvt_pk_bf16_f32 v157, v158, v159\n"
    "v_mfma_f32_16x16x32_bf16 v[196:199], a[164:167], a[4:7], v[220:223]\n"
    "v_exp_f32_e32 v166, v166\n"
    "v_mfma_f32_16x16x32_bf16 v[196:199], a[160:163], a[0:3], v[196:199]\n"
    "s_waitcnt lgkmcnt(5)\n"
    "v_exp_f32_e32 v167, v167\n"
    "v_mfma_f32_16x16x32_bf16 v[196:199], a[168:171], a[8:11], v[196:199]\n"
    "ds_write_addtid_b32 v157 offset:256\n"
    "v_cvt_pk_bf16_f32 v164, v164, v165\n"
    "v_mfma_f32_16x16x32_bf16 v[196:199], a[172:175], a[12:15], v[196:199]\n"
    "ds_read_b128 a[160:163], v240 offset:2048\n"
    "ds_read_b128 a[164:167], v241 offset:2048\n"
    "v_mfma_f32_16x16x32_bf16 v[204:207], a[172:175], a[44:47], v[204:207]\n"
    "v_exp_f32_e32 v160, v160\n"
    "v_mfma_f32_16x16x32_bf16 v[204:207], a[168:171], a[40:43], v[204:207]\n"
    "s_waitcnt lgkmcnt(5)\n"
    "v_exp_f32_e32 v161, v161\n"
    "v_mfma_f32_16x16x32_bf16 v[204:207], a[176:179], a[48:51], v[204:207]\n"
    "buffer_load_dwordx4 v[28:31], v250, s[8:11], s62 offen offset:0 sc1\n"
    "v_mfma_f32_16x16x32_bf16 v[204:207], a[180:183], a[52:55], v[204:207]\n"
    "ds_write_addtid_b32 v164 offset:1024\n"
    "v_cvt_pk_bf16_f32 v165, v166, v167\n"
    "v_mfma_f32_16x16x32_bf16 v[196:199], a[180:183], a[20:23], v[196:199]\n"
    "ds_read_b128 a[168:171], v240 offset:6144\n"
    "v_exp_f32_e32 v162, v162\n"
    "v_mfma_f32_16x16x32_bf16 v[196:199], a[176:179], a[16:19], v[196:199]\n"
    "s_waitcnt lgkmcnt(5)\n"
    "v_exp_f32_e32 v163, v163\n"
    "v_mfma_f32_16x16x32_bf16 v[196:199], a[184:187], a[24:27], v[196:199]\n"
    "ds_read_b128 a[172:175], v241 offset:6144\n"
    "v_cvt_pk_bf16_f32 v158, v160, v161\n"
    "v_mfma_f32_16x16x32_bf16 v[196:199], a[188:191], a[28:31], v[196:199]\n"
    "v_exp_f32_e32 v168, v168\n"
    "v_mfma_f32_16x16x32_bf16 v[204:207], a[188:191], a[60:63], v[204:207]\n"
    "ds_read_b128 a[176:179], v242 offset:2048\n"
    "v_exp_f32_e32 v169, v169\n"
    "v_mfma_f32_16x16x32_bf16 v[204:207], a[184:187], a[56:59], v[204:207]\n"
    "s_waitcnt lgkmcnt(4)\n"
    "ds_write_addtid_b32 v165 offset:1280\n"
    "v_cvt_pk_bf16_f32 v159, v162, v163\n"
    "v_mfma_f32_16x16x32_bf16 v[208:211], a[160:163], a[32:35], v[224:227]\n"
    "buffer_load_dwordx4 v[48:51], v250, s[8:11], s62 offen offset:1024 sc1\n"
    "ds_read_b128 a[180:183], v243 offset:2048\n"
    "v_mfma_f32_16x16x32_bf16 v[208:211], a[164:167], a[36:39], v[208:211]\n"
    "ds_read_b128 a[184:187], v242 offset:6144\n"
    "v_exp_f32_e32 v170, v170\n"
    "v_mfma_f32_16x16x32_bf16 v[200:203], a[164:167], a[4:7], v[220:223]\n"
    "v_exp_f32_e32 v196, v196\n"
    "v_mfma_f32_16x16x32_bf16 v[200:203], a[160:163], a[0:3], v[200:203]\n"
    "s_waitcnt lgkmcnt(4)\n"
    "ds_read_b128 a[188:191], v243 offset:6144\n"
    "v_exp_f32_e32 v197, v197\n"
    "v_mfma_f32_16x16x32_bf16 v[200:203], a[168:171], a[8:11], v[200:203]\n"
    "v_exp_f32_e32 v198, v198\n"
    "v_mfma_f32_16x16x32_bf16 v[200:203], a[172:175], a[12:15], v[200:203]\n"
    "ds_read_b128 a[160:163], v240 offset:8192\n"
    "v_exp_f32_e32 v199, v199\n"
    "v_mfma_f32_16x16x32_bf16 v[208:211], a[172:175], a[44:47], v[208:211]\n"
    "v_exp_f32_e32 v171, v171\n"
    "v_mfma_f32_16x16x32_bf16 v[208:211], a[168:171], a[40:43], v[208:211]\n"
    "s_waitcnt lgkmcnt(3)\n"
    "ds_read_b128 a[164:167], v241 offset:8192\n"
    "v_cvt_pk_bf16_f32 v196, v196, v197\n"
    "v_mfma_f32_16x16x32_bf16 v[208:211], a[176:179], a[48:51], v[208:211]\n"
    "buffer_load_dwordx4 v[32:35], v250, s[8:11], s62 offen offset:2048 sc1\n"
    "v_mfma_f32_16x16x32_bf16 v[208:211], a[180:183], a[52:55], v[208:211]\n"
    "ds_read_b128 a[168:171], v240 offset:12288\n"
    "ds_read_b128 a[172:175], v241 offset:12288\n"
    "v_mfma_f32_16x16x32_bf16 v[200:203], a[180:183], a[20:23], v[200:203]\n"
    "v_exp_f32_e32 v204, v204\n"
    "v_mfma_f32_16x16x32_bf16 v[200:203], a[176:179], a[16:19], v[200:203]\n"
    "s_waitcnt lgkmcnt(4)\n"
    "v_exp_f32_e32 v205, v205\n"
    "v_mfma_f32_16x16x32_bf16 v[200:203], a[184:187], a[24:27], v[200:203]\n"
    "ds_read_b128 a[176:179], v242 offset:8192\n"
    "v_cvt_pk_bf16_f32 v197, v198, v199\n"
    "v_mfma_f32_16x16x32_bf16 v[200:203], a[188:191], a[28:31], v[200:203]\n"
    "v_exp_f32_e32 v206, v206\n"
    "v_mfma_f32_16x16x32_bf16 v[208:211], a[188:191], a[60:63], v[208:211]\n"
    "ds_read_b128 a[180:183], v243 offset:8192\n"
    "v_cvt_pk_bf16_f32 v166, v168, v169\n"
    "v_mfma_f32_16x16x32_bf16 v[208:211], a[184:187], a[56:59], v[208:211]\n"
    "s_waitcnt lgkmcnt(4)\n"
    "v_exp_f32_e32 v207, v207\n"
    "v_mfma_f32_16x16x32_bf16 v[212:215], a[160:163], a[32:35], v[224:227]\n"
    "buffer_load_dwordx4 v[52:55], v250, s[8:11], s62 offen offset:3072 sc1\n"
    "v_mfma_f32_16x16x32_bf16 v[212:215], a[164:167], a[36:39], v[212:215]\n"
    "s_add_u32 s62, s58, 4096\n"
    "ds_read_b128 a[184:187], v242 offset:12288\n"
    "v_cvt_pk_bf16_f32 v167, v170, v171\n"
    "v_mfma_f32_16x16x32_bf16 v[172:175], a[164:167], a[4:7], v[220:223]\n"
    "ds_write_addtid_b32 v158 offset:512\n"
    "v_cvt_pk_bf16_f32 v204, v204, v205\n"
    "v_mfma_f32_16x16x32_bf16 v[172:175], a[160:163], a[0:3], v[172:175]\n"
    "s_waitcnt lgkmcnt(4)\n"
    "ds_read_b128 a[188:191], v243 offset:12288\n"
    "v_exp_f32_e32 v200, v200\n"
    "v_mfma_f32_16x16x32_bf16 v[172:175], a[168:171], a[8:11], v[172:175]\n"
    "ds_read_b128 a[160:163], v240 offset:10240\n"
    "v_exp_f32_e32 v201, v201\n"
    "v_mfma_f32_16x16x32_bf16 v[172:175], a[172:175], a[12:15], v[172:175]\n"
    "ds_write_addtid_b32 v159 offset:768\n"
    "v_cvt_pk_bf16_f32 v205, v206, v207\n"
    "v_mfma_f32_16x16x32_bf16 v[212:215], a[172:175], a[44:47], v[212:215]\n"
    "ds_read_b128 a[164:167], v241 offset:10240\n"
    "v_exp_f32_e32 v202, v202\n"
    "v_mfma_f32_16x16x32_bf16 v[212:215], a[168:171], a[40:43], v[212:215]\n"
    "s_waitcnt lgkmcnt(6)\n"
    "v_exp_f32_e32 v203, v203\n"
    "v_mfma_f32_16x16x32_bf16 v[212:215], a[176:179], a[48:51], v[212:215]\n"
    "buffer_load_dwordx4 v[36:39], v250, s[8:11], s62 offen offset:0 sc1\n"
    "v_mfma_f32_16x16x32_bf16 v[212:215], a[180:183], a[52:55], v[212:215]\n"
    "ds_read_b128 a[168:171], v240 offset:14336\n"
    "v_cvt_pk_bf16_f32 v198, v200, v201\n"
    "v_mfma_f32_16x16x32_bf16 v[172:175], a[180:183], a[20:23], v[172:175]\n"
    "ds_write_addtid_b32 v166 offset:1536\n"
    "v_exp_f32_e32 v208, v208\n"
    "v_mfma_f32_16x16x32_bf16 v[172:175], a[176:179], a[16:19], v[172:175]\n"
    "s_waitcnt lgkmcnt(5)\n"
    "ds_read_b128 a[172:175], v241 offset:14336\n"
    "v_exp_f32_e32 v209, v209\n"
    "v_mfma_f32_16x16x32_bf16 v[172:175], a[184:187], a[24:27], v[172:175]\n"
    "ds_read_b128 a[176:179], v242 offset:10240\n"
    "ds_read_b128 a[180:183], v243 offset:10240\n"
    "v_mfma_f32_16x16x32_bf16 v[172:175], a[188:191], a[28:31], v[172:175]\n"
    "ds_write_addtid_b32 v167 offset:1792\n"
    "v_cvt_pk_bf16_f32 v199, v202, v203\n"
    "v_mfma_f32_16x16x32_bf16 v[212:215], a[188:191], a[60:63], v[212:215]\n"
    "v_exp_f32_e32 v210, v210\n"
    "v_mfma_f32_16x16x32_bf16 v[212:215], a[184:187], a[56:59], v[212:215]\n"
    "s_waitcnt lgkmcnt(6)\n"
    "ds_read_b128 a[184:187], v242 offset:14336\n"
    "v_exp_f32_e32 v172, v172\n"
    "v_mfma_f32_16x16x32_bf16 v[216:219], a[160:163], a[32:35], v[224:227]\n"
    "buffer_load_dwordx4 v[56:59], v250, s[8:11], s62 offen offset:1024 sc1\n"
    "ds_read_b128 a[188:191], v243 offset:14336\n"
    "v_mfma_f32_16x16x32_bf16 v[216:219], a[164:167], a[36:39], v[216:219]\n"
    "s_waitcnt lgkmcnt(2)\n"
    "v_mfma_f32_16x16x32_bf16 v[176:179], a[164:167], a[4:7], v[220:223]\n"
    "s_barrier\n"
    "v_mfma_f32_16x16x32_bf16 v[176:179], a[160:163], a[0:3], v[176:179]\n"
    "ds_read_b64_tr_b16 a[160:161], v244 offset:24576\n"
    "v_exp_f32_e32 v173, v173\n"
    "v_mfma_f32_16x16x32_bf16 v[176:179], a[168:171], a[8:11], v[176:179]\n"
    "ds_read2st64_b32 v[160:161], v249 offset0:0 offset1:1\n"
    "ds_read2st64_b32 v[162:163], v249 offset0:2 offset1:3\n"
    "v_mfma_f32_16x16x32_bf16 v[176:179], a[172:175], a[12:15], v[176:179]\n"
    "ds_read_b64_tr_b16 a[162:163], v244 offset:26624\n"
    "v_exp_f32_e32 v174, v174\n"
    "v_mfma_f32_16x16x32_bf16 v[216:219], a[172:175], a[44:47], v[216:219]\n"
    "ds_read_b64_tr_b16 a[164:165], v244 offset:57344\n"
    "ds_read_b64_tr_b16 a[166:167], v244 offset:59392\n"
    "v_mfma_f32_16x16x32_bf16 v[216:219], a[168:171], a[40:43], v[216:219]\n"
    "ds_read2st64_b32 v[168:169], v249 offset0:4 offset1:5\n"
    "ds_read2st64_b32 v[170:171], v249 offset0:6 offset1:7\n"
    "v_mfma_f32_16x16x32_bf16 v[216:219], a[176:179], a[48:51], v[216:219]\n"
    "s_waitcnt vmcnt(7)\n"
    "buffer_store_dword v254, v231, s[8:11], s38 offen offset:0 sc1\n"
    "v_mfma_f32_16x16x32_bf16 v[216:219], a[180:183], a[52:55], v[216:219]\n"
    "v_exp_f32_e32 v175, v175\n"
    "v_mfma_f32_16x16x32_bf16 v[176:179], a[180:183], a[20:23], v[176:179]\n"
    "ds_read_b64_tr_b16 a[168:169], v245 offset:24576\n"
    "ds_read_b64_tr_b16 a[170:171], v245 offset:26624\n"
    "v_mfma_f32_16x16x32_bf16 v[176:179], a[176:179], a[16:19], v[176:179]\n"
    "s_waitcnt lgkmcnt(10)\n"
    "ds_read_b64_tr_b16 a[172:173], v245 offset:57344\n"
    "ds_read_b64_tr_b16 a[174:175], v245 offset:59392\n"
    "v_mfma_f32_16x16x32_bf16 v[176:179], a[184:187], a[24:27], v[176:179]\n"
    "buffer_load_dwordx4 v[68:71], v250, s[8:11], s62 offen offset:2048 sc1\n"
    "ds_read_b64_tr_b16 a[176:177], v246 offset:24576\n"
    "v_mfma_f32_16x16x32_bf16 v[176:179], a[188:191], a[28:31], v[176:179]\n"
    "ds_read_b64_tr_b16 a[178:179], v246 offset:26624\n"
    "v_cvt_pk_bf16_f32 v172, v172, v173\n"
    "v_mfma_f32_16x16x32_bf16 v[216:219], a[188:191], a[60:63], v[216:219]\n"
    "ds_read_b64_tr_b16 a[180:181], v246 offset:57344\n"
    "ds_read_b64_tr_b16 a[182:183], v246 offset:59392\n"
    "v_mfma_f32_16x16x32_bf16 v[216:219], a[184:187], a[56:59], v[216:219]\n"
    "s_waitcnt lgkmcnt(8)\n"
    "ds_read_b64_tr_b16 a[184:185], v247 offset:24576\n"
    "ds_read_b64_tr_b16 a[186:187], v247 offset:26624\n"
    "v_mfma_f32_16x16x32_bf16 a[240:243], a[160:163], v[164:167], a[240:243]\n"
    "v_exp_f32_e32 v212, v212\n"
    "v_mfma_f32_16x16x32_bf16 a[240:243], a[164:167], v[168:171], a[240:243]\n"
    "ds_read_b64_tr_b16 a[188:189], v247 offset:57344\n"
    "v_exp_f32_e32 v211, v211\n"
    "v_mfma_f32_16x16x32_bf16 a[236:239], a[164:167], v[160:163], a[236:239]\n"
    "v_exp_f32_e32 v213, v213\n"
    "v_mfma_f32_16x16x32_bf16 a[236:239], a[160:163], v[156:159], a[236:239]\n"
    "s_waitcnt lgkmcnt(7)\n"
    "ds_read_b64_tr_b16 a[190:191], v247 offset:59392\n"
    "v_cvt_pk_bf16_f32 v173, v174, v175\n"
    "v_mfma_f32_16x16x32_bf16 v[0:3], a[168:171], v[156:159], v[0:3]\n"
    "buffer_load_dwordx4 v[88:91], v250, s[8:11], s62 offen offset:3072 sc1\n"
    "ds_read_b64_tr_b16 a[160:161], v244 offset:28672\n"
    "v_mfma_f32_16x16x32_bf16 v[0:3], a[172:175], v[160:163], v[0:3]\n"
    "s_add_u32 s62, s58, 8192\n"
    "ds_read_b64_tr_b16 a[162:163], v244 offset:30720\n"
    "v_exp_f32_e32 v214, v214\n"
    "v_mfma_f32_16x16x32_bf16 v[4:7], a[172:175], v[168:171], v[4:7]\n"
    "ds_read_b64_tr_b16 a[164:165], v244 offset:61440\n"
    "v_exp_f32_e32 v215, v215\n"
    "v_mfma_f32_16x16x32_bf16 v[4:7], a[168:171], v[164:167], v[4:7]\n"
    "s_waitcnt lgkmcnt(7)\n"
    "ds_read_b64_tr_b16 a[168:169], v245 offset:28672\n"
    "ds_read_b64_tr_b16 a[170:171], v245 offset:30720\n"
    "v_mfma_f32_16x16x32_bf16 v[24:27], a[176:179], v[164:167], v[24:27]\n"
    "ds_read_b64_tr_b16 a[166:167], v244 offset:63488\n"
    "v_cvt_pk_bf16_f32 v212, v212, v213\n"
    "v_mfma_f32_16x16x32_bf16 v[24:27], a[180:183], v[168:171], v[24:27]\n"
    "ds_read_b64_tr_b16 a[172:173], v245 offset:61440\n"
    "ds_write_addtid_b32 v172 offset:4096\n"
    "v_mfma_f32_16x16x32_bf16 v[20:23], a[180:183], v[160:163], v[20:23]\n"
    "ds_write_addtid_b32 v173 offset:4352\n"
    "ds_write_addtid_b32 v212 offset:5120\n"
    "v_mfma_f32_16x16x32_bf16 v[20:23], a[176:179], v[156:159], v[20:23]\n"
    "s_waitcnt lgkmcnt(10)\n"
    "ds_read_b64_tr_b16 a[176:177], v246 offset:28672\n"
    "ds_read_b64_tr_b16 a[178:179], v246 offset:30720\n"
    "v_mfma_f32_16x16x32_bf16 v[40:43], a[184:187], v[156:159], v[40:43]\n"
    "buffer_load_dwordx4 v[72:75], v250, s[8:11], s62 offen offset:0 sc1\n"
    "ds_read_b64_tr_b16 a[174:175], v245 offset:63488\n"
    "v_mfma_f32_16x16x32_bf16 v[40:43], a[188:191], v[160:163], v[40:43]\n"
    "ds_write_addtid_b32 v196 offset:2048\n"
    "ds_write_addtid_b32 v197 offset:2304\n"
    "v_mfma_f32_16x16x32_bf16 v[44:47], a[188:191], v[168:171], v[44:47]\n"
    "ds_read_b64_tr_b16 a[180:181], v246 offset:61440\n"
    "v_exp_f32_e32 v176, v176\n"
    "v_mfma_f32_16x16x32_bf16 v[44:47], a[184:187], v[164:167], v[44:47]\n"
    "s_waitcnt lgkmcnt(10)\n"
    "ds_read_b64_tr_b16 a[182:183], v246 offset:63488\n"
    "v_exp_f32_e32 v177, v177\n"
    "v_mfma_f32_16x16x32_bf16 v[64:67], a[160:163], v[164:167], v[64:67]\n"
    "ds_write_addtid_b32 v204 offset:3072\n"
    "ds_write_addtid_b32 v205 offset:3328\n"
    "v_mfma_f32_16x16x32_bf16 v[64:67], a[164:167], v[168:171], v[64:67]\n"
    "ds_read_b64_tr_b16 a[184:185], v247 offset:28672\n"
    "ds_read_b64_tr_b16 a[186:187], v247 offset:30720\n"
    "v_mfma_f32_16x16x32_bf16 v[60:63], a[164:167], v[160:163], v[60:63]\n"
    "ds_write_addtid_b32 v198 offset:2560\n"
    "v_cvt_pk_bf16_f32 v213, v214, v215\n"
    "v_mfma_f32_16x16x32_bf16 v[60:63], a[160:163], v[156:159], v[60:63]\n"
    "s_waitcnt lgkmcnt(9)\n"
    "ds_read_b64_tr_b16 a[188:189], v247 offset:61440\n"
    "ds_read_b64_tr_b16 a[190:191], v247 offset:63488\n"
    "v_mfma_f32_16x16x32_bf16 v[80:83], a[168:171], v[156:159], v[80:83]\n"
    "buffer_load_dwordx4 v[92:95], v250, s[8:11], s62 offen offset:1024 sc1\n"
    "ds_read_b128 a[160:163], v240 offset:16384\n"
    "v_mfma_f32_16x16x32_bf16 v[80:83], a[172:175], v[160:163], v[80:83]\n"
    "ds_read_b128 a[164:167], v241 offset:16384\n"
    "ds_write_addtid_b32 v199 offset:2816\n"
    "v_mfma_f32_16x16x32_bf16 v[84:87], a[172:175], v[168:171], v[84:87]\n"
    "ds_write_addtid_b32 v213 offset:5376\n"
    "v_cvt_pk_bf16_f32 v206, v208, v209\n"
    "v_mfma_f32_16x16x32_bf16 v[84:87], a[168:171], v[164:167], v[84:87]\n"
    "s_waitcnt lgkmcnt(11)\n"
    "v_mfma_f32_16x16x32_bf16 v[104:107], a[176:179], v[164:167], v[104:107]\n"
    "s_barrier\n"
    "v_mfma_f32_16x16x32_bf16 v[104:107], a[180:183], v[168:171], v[104:107]\n"
    "ds_read_b128 a[168:171], v240 offset:20480\n"
    "v_cvt_pk_bf16_f32 v207, v210, v211\n"
    "v_mfma_f32_16x16x32_bf16 v[100:103], a[180:183], v[160:163], v[100:103]\n"
    "s_add_u32 s31, s47, s35\n"
    "v_exp_f32_e32 v178, v178\n"
    "v_mfma_f32_16x16x32_bf16 v[100:103], a[176:179], v[156:159], v[100:103]\n"
    "s_waitcnt lgkmcnt(5)\n"
    "ds_read_b128 a[172:175], v241 offset:20480\n"
    "ds_write_addtid_b32 v206 offset:3584\n"
    "v_mfma_f32_16x16x32_bf16 v[120:123], a[184:187], v[156:159], v[120:123]\n"
    "s_add_u32 m0, s55, 24576\n"
    "v_exp_f32_e32 v179, v179\n"
    "v_mfma_f32_16x16x32_bf16 v[120:123], a[188:191], v[160:163], v[120:123]\n"
    "buffer_load_dwordx4 v251, s[8:11], s31 offen offset:0 sc1 lds\n"
    "v_mfma_f32_16x16x32_bf16 v[124:127], a[188:191], v[168:171], v[124:127]\n"
    "s_mov_b32 m0, s63\n"
    "ds_read_b128 a[176:179], v242 offset:16384\n"
    "v_cvt_pk_bf16_f32 v174, v176, v177\n"
    "v_mfma_f32_16x16x32_bf16 v[124:127], a[184:187], v[164:167], v[124:127]\n"
    "s_waitcnt lgkmcnt(6)\n"
    "ds_read_b128 a[180:183], v243 offset:16384\n"
    "ds_write_addtid_b32 v174 offset:4608\n"
    "v_mfma_f32_16x16x32_bf16 v[148:151], a[160:163], a[32:35], v[224:227]\n"
    "buffer_load_dwordx4 v[76:79], v250, s[8:11], s62 offen offset:2048 sc1\n"
    "v_mfma_f32_16x16x32_bf16 v[148:151], a[164:167], a[36:39], v[148:151]\n"
    "s_add_u32 s31, s31, 0x1000\n"
    "ds_read_b128 a[184:187], v242 offset:20480\n"
    "v_exp_f32_e32 v216, v216\n"
    "v_mfma_f32_16x16x32_bf16 v[140:143], a[164:167], a[4:7], v[220:223]\n"
    "s_add_u32 m0, s55, 25600\n"
    "v_exp_f32_e32 v217, v217\n"
    "v_mfma_f32_16x16x32_bf16 v[140:143], a[160:163], a[0:3], v[140:143]\n"
    "s_waitcnt lgkmcnt(5)\n"
    "ds_read_b128 a[188:191], v243 offset:20480\n"
    "v_cvt_pk_bf16_f32 v175, v178, v179\n"
    "v_mfma_f32_16x16x32_bf16 v[140:143], a[168:171], a[8:11], v[140:143]\n"
    "buffer_load_dwordx4 v251, s[8:11], s31 offen offset:0 sc1 lds\n"
    "v_mfma_f32_16x16x32_bf16 v[140:143], a[172:175], a[12:15], v[140:143]\n"
    "s_mov_b32 m0, s63\n"
    "ds_read_b128 a[160:163], v240 offset:18432\n"
    "ds_read_b128 a[164:167], v241 offset:18432\n"
    "v_mfma_f32_16x16x32_bf16 v[148:151], a[172:175], a[44:47], v[148:151]\n"
    "v_exp_f32_e32 v218, v218\n"
    "v_mfma_f32_16x16x32_bf16 v[148:151], a[168:171], a[40:43], v[148:151]\n"
    "s_waitcnt lgkmcnt(5)\n"
    "ds_write_addtid_b32 v207 offset:3840\n"
    "ds_write_addtid_b32 v175 offset:4864\n"
    "v_mfma_f32_16x16x32_bf16 v[148:151], a[176:179], a[48:51], v[148:151]\n"
    "s_add_u32 s31, s47, s36\n"
    "v_exp_f32_e32 v219, v219\n"
    "v_mfma_f32_16x16x32_bf16 v[148:151], a[180:183], a[52:55], v[148:151]\n"
    "s_add_u32 m0, s55, 26624\n"
    "ds_read_b128 a[168:171], v240 offset:22528\n"
    "v_cvt_pk_bf16_f32 v214, v216, v217\n"
    "v_mfma_f32_16x16x32_bf16 v[140:143], a[180:183], a[20:23], v[140:143]\n"
    "buffer_load_dwordx4 v251, s[8:11], s31 offen offset:0 sc1 lds\n"
    "v_mfma_f32_16x16x32_bf16 v[140:143], a[176:179], a[16:19], v[140:143]\n"
    "s_waitcnt lgkmcnt(5)\n"
    "ds_read_b128 a[172:175], v241 offset:22528\n"
    "ds_read_b128 a[176:179], v242 offset:18432\n"
    "v_mfma_f32_16x16x32_bf16 v[140:143], a[184:187], a[24:27], v[140:143]\n"
    "s_mov_b32 m0, s63\n"
    "ds_read_b128 a[180:183], v243 offset:18432\n"
    "v_cvt_pk_bf16_f32 v215, v218, v219\n"
    "v_mfma_f32_16x16x32_bf16 v[140:143], a[188:191], a[28:31], v[140:143]\n"
    "buffer_load_dword v253, v238, s[8:11], s48 offen offset:0 sc1\n"
    "v_mfma_f32_16x16x32_bf16 v[148:151], a[188:191], a[60:63], v[148:151]\n"
    "s_add_u32 s31, s31, 0x1000\n"
    "ds_write_addtid_b32 v214 offset:5632\n"
    "ds_write_addtid_b32 v215 offset:5888\n"
    "v_mfma_f32_16x16x32_bf16 v[148:151], a[184:187], a[56:59], v[148:151]\n"
    "s_waitcnt lgkmcnt(8)\n"
    "ds_read_b128 a[184:187], v242 offset:22528\n"
    "v_exp_f32_e32 v140, v140\n"
    "v_mfma_f32_16x16x32_bf16 v[152:155], a[160:163], a[32:35], v[224:227]\n"
    "s_add_u32 m0, s55, 27648\n"
    "ds_read_b128 a[188:191], v243 offset:22528\n"
    "v_exp_f32_e32 v141, v141\n"
    "v_mfma_f32_16x16x32_bf16 v[152:155], a[164:167], a[36:39], v[152:155]\n"
    "s_waitcnt lgkmcnt(9)\n"
    "s_waitcnt lgkmcnt(2)\n"
    "v_mfma_f32_16x16x32_bf16 v[144:147], a[164:167], a[4:7], v[220:223]\n"
    "s_barrier\n"
    "v_mfma_f32_16x16x32_bf16 v[144:147], a[160:163], a[0:3], v[144:147]\n"
    "ds_read_b64_tr_b16 a[160:161], v244 offset:0\n"
    "ds_read2st64_b32 v[200:201], v249 offset0:8 offset1:9\n"
    "v_mfma_f32_16x16x32_bf16 v[144:147], a[168:171], a[8:11], v[144:147]\n"
    "buffer_load_dwordx4 v251, s[8:11], s31 offen offset:0 sc1 lds\n"
    "ds_read2st64_b32 v[202:203], v249 offset0:10 offset1:11\n"
    "v_mfma_f32_16x16x32_bf16 v[144:147], a[172:175], a[12:15], v[144:147]\n"
    "s_mov_b32 m0, s63\n"
    "ds_read2st64_b32 v[208:209], v249 offset0:12 offset1:13\n"
    "ds_read2st64_b32 v[210:211], v249 offset0:14 offset1:15\n"
    "v_mfma_f32_16x16x32_bf16 v[152:155], a[172:175], a[44:47], v[152:155]\n"
    "s_add_u32 s87, s59, 8192\n"
    "ds_read_b64_tr_b16 a[162:163], v244 offset:2048\n"
    "ds_read_b64_tr_b16 a[164:165], v244 offset:32768\n"
    "v_mfma_f32_16x16x32_bf16 v[152:155], a[168:171], a[40:43], v[152:155]\n"
    "ds_read_b64_tr_b16 a[166:167], v244 offset:34816\n"
    "v_exp_f32_e32 v142, v142\n"
    "v_mfma_f32_16x16x32_bf16 v[152:155], a[176:179], a[48:51], v[152:155]\n"
    "s_add_u32 s31, s47, s37\n"
    "ds_read_b64_tr_b16 a[168:169], v245 offset:0\n"
    "v_exp_f32_e32 v143, v143\n"
    "v_mfma_f32_16x16x32_bf16 v[152:155], a[180:183], a[52:55], v[152:155]\n"
    "s_add_u32 m0, s55, 28672\n"
    "ds_read_b64_tr_b16 a[170:171], v245 offset:2048\n"
    "v_cvt_pk_bf16_f32 v140, v140, v141\n"
    "v_mfma_f32_16x16x32_bf16 v[144:147], a[180:183], a[20:23], v[144:147]\n"
    "buffer_load_dwordx4 v251, s[8:11], s31 offen offset:0 sc1 lds\n"
    "ds_read_b64_tr_b16 a[172:173], v245 offset:32768\n"
    "v_mfma_f32_16x16x32_bf16 v[144:147], a[176:179], a[16:19], v[144:147]\n"
    "s_waitcnt lgkmcnt(11)\n"
    "ds_read_b64_tr_b16 a[174:175], v245 offset:34816\n"
    "v_exp_f32_e32 v148, v148\n"
    "v_mfma_f32_16x16x32_bf16 v[144:147], a[184:187], a[24:27], v[144:147]\n"
    "s_mov_b32 m0, s63\n"
    "ds_read_b64_tr_b16 a[176:177], v246 offset:0\n"
    "ds_read_b64_tr_b16 a[178:179], v246 offset:2048\n"
    "v_mfma_f32_16x16x32_bf16 v[144:147], a[188:191], a[28:31], v[144:147]\n"
    "s_add_u32 s88, s59, 16384\n"
    "ds_read_b64_tr_b16 a[180:181], v246 offset:32768\n"
    "v_exp_f32_e32 v149, v149\n"
    "v_mfma_f32_16x16x32_bf16 v[152:155], a[188:191], a[60:63], v[152:155]\n"
    "s_add_u32 s31, s31, 0x1000\n"
    "ds_read2st64_b32 v[176:177], v249 offset0:16 offset1:17\n"
    "v_cvt_pk_bf16_f32 v141, v142, v143\n"
    "v_mfma_f32_16x16x32_bf16 v[152:155], a[184:187], a[56:59], v[152:155]\n"
    "s_waitcnt lgkmcnt(8)\n"
    "ds_write_addtid_b32 v140 offset:0\n"
    "v_exp_f32_e32 v150, v150\n"
    "v_mfma_f32_16x16x32_bf16 a[240:243], a[160:163], v[204:207], a[240:243]\n"
    "s_add_u32 m0, s55, 29696\n"
    "ds_read_b64_tr_b16 a[182:183], v246 offset:34816\n"
    "v_exp_f32_e32 v151, v151\n"
    "v_mfma_f32_16x16x32_bf16 a[240:243], a[164:167], v[208:211], a[240:243]\n"
    "buffer_load_dwordx4 v251, s[8:11], s31 offen offset:0 sc1 lds\n"
    "ds_read2st64_b32 v[178:179], v249 offset0:18 offset1:19\n"
    "v_mfma_f32_16x16x32_bf16 a[236:239], a[164:167], v[200:203], a[236:239]\n"
    "s_mov_b32 m0, s63\n"
    "ds_read_b64_tr_b16 a[184:185], v247 offset:0\n"
    "v_cvt_pk_bf16_f32 v148, v148, v149\n"
    "v_mfma_f32_16x16x32_bf16 a[236:239], a[160:163], v[196:199], a[236:239]\n"
    "s_waitcnt lgkmcnt(8)\n"
    "ds_read_b64_tr_b16 a[186:187], v247 offset:2048\n"
    "ds_read_b64_tr_b16 a[188:189], v247 offset:32768\n"
    "v_mfma_f32_16x16x32_bf16 v[0:3], a[168:171], v[196:199], v[0:3]\n"
    "s_add_u32 s89, s59, 24576\n"
    "ds_read_b64_tr_b16 a[190:191], v247 offset:34816\n"
    "ds_read_b64_tr_b16 a[160:161], v244 offset:4096\n"
    "v_mfma_f32_16x16x32_bf16 v[0:3], a[172:175], v[200:203], v[0:3]\n"
    "s_add_u32 s31, s46, s36\n"
    "ds_read_b64_tr_b16 a[162:163], v244 offset:6144\n"
    "ds_read2st64_b32 v[216:217], v249 offset0:20 offset1:21\n"
    "v_mfma_f32_16x16x32_bf16 v[4:7], a[172:175], v[208:211], v[4:7]\n"
    "s_add_u32 m0, s55, 30720\n"
    "ds_read_b64_tr_b16 a[164:165], v244 offset:36864\n"
    "v_exp_f32_e32 v144, v144\n"
    "v_mfma_f32_16x16x32_bf16 v[4:7], a[168:171], v[204:207], v[4:7]\n"
    "s_waitcnt lgkmcnt(9)\n"
    "ds_read_b64_tr_b16 a[166:167], v244 offset:38912\n"
    "ds_read_b64_tr_b16 a[168:169], v245 offset:4096\n"
    "v_mfma_f32_16x16x32_bf16 v[24:27], a[176:179], v[204:207], v[24:27]\n"
    "buffer_load_dwordx4 v251, s[12:15], s31 offen offset:0 lds\n"
    "ds_read2st64_b32 v[218:219], v249 offset0:22 offset1:23\n"
    "v_mfma_f32_16x16x32_bf16 v[24:27], a[180:183], v[208:211], v[24:27]\n"
    "s_mov_b32 m0, s63\n"
    "ds_read_b64_tr_b16 a[170:171], v245 offset:6144\n"
    "ds_read_b64_tr_b16 a[172:173], v245 offset:36864\n"
    "v_mfma_f32_16x16x32_bf16 v[20:23], a[180:183], v[200:203], v[20:23]\n"
    "v_exp_f32_e32 v145, v145\n"
    "v_mfma_f32_16x16x32_bf16 v[20:23], a[176:179], v[196:199], v[20:23]\n"
    "s_waitcnt lgkmcnt(9)\n"
    "v_cvt_pk_bf16_f32 v149, v150, v151\n"
    "v_exp_f32_e32 v146, v146\n"
    "v_mfma_f32_16x16x32_bf16 v[40:43], a[184:187], v[196:199], v[40:43]\n"
    "s_add_u32 s31, s31, 0x1000\n"
    "ds_read_b64_tr_b16 a[174:175], v245 offset:38912\n"
    "ds_write_addtid_b32 v141 offset:256\n"
    "v_mfma_f32_16x16x32_bf16 v[40:43], a[188:191], v[200:203], v[40:43]\n"
    "s_add_u32 m0, s55, 31744\n"
    "ds_read_b64_tr_b16 a[176:177], v246 offset:4096\n"
    "v_exp_f32_e32 v147, v147\n"
    "v_mfma_f32_16x16x32_bf16 v[44:47], a[188:191], v[208:211], v[44:47]\n"
    "buffer_load_dwordx4 v251, s[12:15], s31 offen offset:0 lds\n"
    "ds_read_b64_tr_b16 a[178:179], v246 offset:6144\n"
    "v_mfma_f32_16x16x32_bf16 v[44:47], a[184:187], v[204:207], v[44:47]\n"
    "s_waitcnt lgkmcnt(8)\n"
    "ds_read_b64_tr_b16 a[180:181], v246 offset:36864\n"
    "v_cvt_pk_bf16_f32 v142, v144, v145\n"
    "v_mfma_f32_16x16x32_bf16 v[64:67], a[160:163], v[204:207], v[64:67]\n"
    "s_mov_b32 m0, s63\n"
    "ds_read_b64_tr_b16 a[182:183], v246 offset:38912\n"
    "ds_read_b64_tr_b16 a[184:185], v247 offset:4096\n"
    "v_mfma_f32_16x16x32_bf16 v[64:67], a[164:167], v[208:211], v[64:67]\n"
    "s_waitcnt vmcnt(5)\n"
    "ds_read_b64_tr_b16 a[186:187], v247 offset:6144\n"
    "v_subrev_u32_e32 v253, s39, v253\n"
    "v_subrev_u32_e32 v253, 0, v253\n"
    "v_mfma_f32_16x16x32_bf16 v[60:63], a[164:167], v[200:203], v[60:63]\n"
    "ds_read_b64_tr_b16 a[188:189], v247 offset:36864\n"
    "v_cmp_le_u32_e32 vcc, 1, v253\n"
    "v_min_u32_e32 v220, 0x13c, v248\n"
    "v_mfma_f32_16x16x32_bf16 v[60:63], a[160:163], v[196:199], v[60:63]\n"
    "s_waitcnt lgkmcnt(8)\n"
    "ds_read_b64_tr_b16 a[160:161], v244 offset:12288\n"
    "v_exp_f32_e32 v152, v152\n"
    "v_mfma_f32_16x16x32_bf16 v[80:83], a[168:171], v[196:199], v[80:83]\n"
    "ds_read_b64_tr_b16 a[190:191], v247 offset:38912\n"
    "ds_read_b64_tr_b16 a[162:163], v244 offset:14336\n"
    "v_mfma_f32_16x16x32_bf16 v[80:83], a[172:175], v[200:203], v[80:83]\n"
    "s_cbranch_vccnz .Lat_k\n"
    ".Lat_kr:\n"
    "s_cmp_lg_u32 s56, 0\n"
    "s_cbranch_scc1 .Lat_kx\n"
    ".Lat_kxr:\n"
    "v_mfma_f32_16x16x32_bf16 v[84:87], a[172:175], v[208:211], v[84:87]\n"
    "buffer_load_dwordx4 a[244:247], v250, s[8:11], s87 offen offset:0 sc1\n"
    "ds_write_addtid_b32 v148 offset:1024\n"
    "v_mfma_f32_16x16x32_bf16 v[84:87], a[168:171], v[204:207], v[84:87]\n"
    "s_waitcnt lgkmcnt(7)\n"
    "ds_read_b64_tr_b16 a[164:165], v244 offset:45056\n"
    "ds_read_b64_tr_b16 a[166:167], v244 offset:47104\n"
    "v_mfma_f32_16x16x32_bf16 v[104:107], a[176:179], v[204:207], v[104:107]\n"
    "ds_read_b64_tr_b16 a[168:169], v245 offset:12288\n"
    "ds_read_b64_tr_b16 a[170:171], v245 offset:14336\n"
    "v_mfma_f32_16x16x32_bf16 v[104:107], a[180:183], v[208:211], v[104:107]\n"
    "buffer_load_dwordx4 v[96:99], v250, s[8:11], s62 offen offset:3072 sc1\n"
    "ds_write_addtid_b32 v149 offset:1280\n"
    "v_mfma_f32_16x16x32_bf16 v[100:103], a[180:183], v[200:203], v[100:103]\n"
    "s_add_u32 s62, s58, 12288\n"
    "ds_read_b64_tr_b16 a[172:173], v245 offset:45056\n"
    "v_exp_f32_e32 v153, v153\n"
    "v_mfma_f32_16x16x32_bf16 v[100:103], a[176:179], v[196:199], v[100:103]\n"
    "s_waitcnt lgkmcnt(8)\n"
    "ds_read_b64_tr_b16 a[176:177], v246 offset:12288\n"
    "ds_read_b64_tr_b16 a[178:179], v246 offset:14336\n"
    "ds_write_addtid_b32 v142 offset:512\n"
    "v_mfma_f32_16x16x32_bf16 v[120:123], a[184:187], v[196:199], v[120:123]\n"
    "ds_read_b64_tr_b16 a[174:175], v245 offset:47104\n"
    "v_cvt_pk_bf16_f32 v143, v146, v147\n"
    "v_mfma_f32_16x16x32_bf16 v[120:123], a[188:191], v[200:203], v[120:123]\n"
    "ds_read_b64_tr_b16 a[180:181], v246 offset:45056\n"
    "v_exp_f32_e32 v154, v154\n"
    "v_mfma_f32_16x16x32_bf16 v[124:127], a[188:191], v[208:211], v[124:127]\n"
    "buffer_load_dwordx4 a[248:251], v250, s[8:11], s88 offen offset:0 sc1\n"
    "ds_write_addtid_b32 v143 offset:768\n"
    "v_mfma_f32_16x16x32_bf16 v[124:127], a[184:187], v[204:207], v[124:127]\n"
    "s_waitcnt lgkmcnt(10)\n"
    "ds_read_b64_tr_b16 a[184:185], v247 offset:12288\n"
    "v_exp_f32_e32 v155, v155\n"
    "v_mfma_f32_16x16x32_bf16 v[64:67], a[160:163], v[212:215], v[64:67]\n"
    "ds_read_b64_tr_b16 a[182:183], v246 offset:47104\n"
    "v_cvt_pk_bf16_f32 v150, v152, v153\n"
    "v_mfma_f32_16x16x32_bf16 v[64:67], a[164:167], v[216:219], v[64:67]\n"
    "buffer_load_dwordx4 v[108:111], v250, s[8:11], s62 offen offset:0 sc1\n"
    "ds_read_b64_tr_b16 a[186:187], v247 offset:14336\n"
    "v_mfma_f32_16x16x32_bf16 v[60:63], a[164:167], v[176:179], v[60:63]\n"
    "ds_read_b64_tr_b16 a[188:189], v247 offset:45056\n"
    "ds_read_b64_tr_b16 a[190:191], v247 offset:47104\n"
    "v_mfma_f32_16x16x32_bf16 v[60:63], a[160:163], v[172:175], v[60:63]\n"
    "s_waitcnt lgkmcnt(7)\n"
    "ds_write_addtid_b32 v150 offset:1536\n"
    "v_cvt_pk_bf16_f32 v151, v154, v155\n"
    "v_mfma_f32_16x16x32_bf16 v[80:83], a[168:171], v[172:175], v[80:83]\n"
    "ds_read_b64_tr_b16 a[160:161], v244 offset:8192\n"
    "ds_write_addtid_b32 v151 offset:1792\n"
    "v_mfma_f32_16x16x32_bf16 v[80:83], a[172:175], v[176:179], v[80:83]\n"
    "ds_read_b64_tr_b16 a[162:163], v244 offset:10240\n"
    "ds_read_b64_tr_b16 a[164:165], v244 offset:40960\n"
    "v_mfma_f32_16x16x32_bf16 v[84:87], a[172:175], v[216:219], v[84:87]\n"
    "buffer_load_dwordx4 a[252:255], v250, s[8:11], s89 offen offset:0 sc1\n"
    "ds_read_b64_tr_b16 a[166:167], v244 offset:43008\n"
    "v_mfma_f32_16x16x32_bf16 v[84:87], a[168:171], v[212:215], v[84:87]\n"
    "s_waitcnt lgkmcnt(9)\n"
    "s_waitcnt lgkmcnt(3)\n"
    "s_waitcnt vmcnt(7)\n"
    "v_mfma_f32_16x16x32_bf16 v[104:107], a[176:179], v[212:215], v[104:107]\n"
    "s_barrier\n"
    "v_mfma_f32_16x16x32_bf16 v[104:107], a[180:183], v[216:219], v[104:107]\n"
    "buffer_load_dwordx4 v[112:115], v250, s[8:11], s62 offen offset:1024 sc1\n"
    "ds_read2st64_b32 v[144:145], v249 offset0:0 offset1:1\n"
    "v_mfma_f32_16x16x32_bf16 v[100:103], a[180:183], v[176:179], v[100:103]\n"
    "s_add_u32 s31, s46, s37\n"
    "ds_read2st64_b32 v[146:147], v249 offset0:2 offset1:3\n"
    "ds_read2st64_b32 v[152:153], v249 offset0:4 offset1:5\n"
    "v_mfma_f32_16x16x32_bf16 v[100:103], a[176:179], v[172:175], v[100:103]\n"
    "ds_read_b64_tr_b16 a[168:169], v244 offset:16384\n"
    "ds_read_b64_tr_b16 a[170:171], v244 offset:18432\n"
    "v_mfma_f32_16x16x32_bf16 v[120:123], a[184:187], v[172:175], v[120:123]\n"
    "s_add_u32 m0, s55, 4096\n"
    "ds_read_b64_tr_b16 a[172:173], v244 offset:49152\n"
    "ds_read_b64_tr_b16 a[174:175], v244 offset:51200\n"
    "v_mfma_f32_16x16x32_bf16 v[120:123], a[188:191], v[176:179], v[120:123]\n"
    "buffer_load_dwordx4 v251, s[12:15], s31 offen offset:0 lds\n"
    "ds_read2st64_b32 v[154:155], v249 offset0:6 offset1:7\n"
    "v_mfma_f32_16x16x32_bf16 v[124:127], a[188:191], v[216:219], v[124:127]\n"
    "s_mov_b32 m0, s63\n"
    "ds_read_b64_tr_b16 a[176:177], v245 offset:8192\n"
    "ds_read_b64_tr_b16 a[178:179], v245 offset:10240\n"
    "v_mfma_f32_16x16x32_bf16 v[124:127], a[184:187], v[212:215], v[124:127]\n"
    "s_waitcnt lgkmcnt(10)\n"
    "ds_read_b64_tr_b16 a[180:181], v245 offset:40960\n"
    "ds_read_b64_tr_b16 a[182:183], v245 offset:43008\n"
    "v_mfma_f32_16x16x32_bf16 a[240:243], a[160:163], v[212:215], a[240:243]\n"
    "buffer_load_dwordx4 v[8:11], v250, s[8:11], s87 offen offset:1024 sc1\n"
    "v_mfma_f32_16x16x32_bf16 a[240:243], a[164:167], v[216:219], a[240:243]\n"
    "s_add_u32 s31, s31, 0x1000\n"
    "ds_read_b64_tr_b16 a[184:185], v245 offset:16384\n"
    "v_add_u32_e32 v240, s45, v240\n"
    "v_mfma_f32_16x16x32_bf16 a[236:239], a[164:167], v[176:179], a[236:239]\n"
    "s_add_u32 m0, s55, 5120\n"
    "ds_read_b64_tr_b16 a[186:187], v245 offset:18432\n"
    "ds_read_b64_tr_b16 a[188:189], v245 offset:49152\n"
    "v_mfma_f32_16x16x32_bf16 a[236:239], a[160:163], v[172:175], a[236:239]\n"
    "s_waitcnt lgkmcnt(7)\n"
    "ds_read_b64_tr_b16 a[190:191], v245 offset:51200\n"
    "ds_read_b64_tr_b16 a[160:161], v246 offset:8192\n"
    "v_mfma_f32_16x16x32_bf16 a[236:239], a[168:171], v[140:143], a[236:239]\n"
    "buffer_load_dwordx4 v251, s[12:15], s31 offen offset:0 lds\n"
    "v_mfma_f32_16x16x32_bf16 a[236:239], a[172:175], v[144:147], a[236:239]\n"
    "s_mov_b32 m0, s63\n"
    "ds_read_b64_tr_b16 a[162:163], v246 offset:10240\n"
    "v_xor_b32_e32 v241, 64, v240\n"
    "v_mfma_f32_16x16x32_bf16 a[240:243], a[172:175], v[152:155], a[240:243]\n"
    "ds_read_b64_tr_b16 a[164:165], v246 offset:40960\n"
    "ds_read_b64_tr_b16 a[166:167], v246 offset:43008\n"
    "v_mfma_f32_16x16x32_bf16 a[240:243], a[168:171], v[148:151], a[240:243]\n"
    "s_waitcnt lgkmcnt(8)\n"
    "ds_read_b64_tr_b16 a[168:169], v246 offset:16384\n"
    "ds_read_b64_tr_b16 a[170:171], v246 offset:18432\n"
    "v_mfma_f32_16x16x32_bf16 v[4:7], a[176:179], v[212:215], v[4:7]\n"
    "buffer_load_dwordx4 v[12:15], v250, s[8:11], s88 offen offset:1024 sc1\n"
    "v_mfma_f32_16x16x32_bf16 v[4:7], a[180:183], v[216:219], v[4:7]\n"
    "ds_read_b64_tr_b16 a[172:173], v246 offset:49152\n"
    "v_add_u32_e32 v242, 0x10000, v240\n"
    "v_mfma_f32_16x16x32_bf16 v[0:3], a[180:183], v[176:179], v[0:3]\n"
    "ds_read_b64_tr_b16 a[174:175], v246 offset:51200\n"
    "v_add_u32_e32 v243, 0x10000, v241\n"
    "v_mfma_f32_16x16x32_bf16 v[0:3], a[176:179], v[172:175], v[0:3]\n"
    "s_waitcnt lgkmcnt(8)\n"
    "ds_read_b64_tr_b16 a[176:177], v247 offset:8192\n"
    "v_add_u32_e32 v252, s54, v240\n"
    "v_mfma_f32_16x16x32_bf16 v[0:3], a[184:187], v[140:143], v[0:3]\n"
    "buffer_load_dwordx4 v[132:135], v250, s[8:11], s62 offen offset:2048 sc1\n"
    "v_mfma_f32_16x16x32_bf16 v[0:3], a[188:191], v[144:147], v[0:3]\n"
    "ds_read_b64_tr_b16 a[178:179], v247 offset:10240\n"
    "v_mfma_f32_16x16x32_bf16 v[4:7], a[188:191], v[152:155], v[4:7]\n"
    "ds_read_b64_tr_b16 a[180:181], v247 offset:40960\n"
    "v_xor_b32_e32 v255, 64, v252\n"
    "v_mfma_f32_16x16x32_bf16 v[4:7], a[184:187], v[148:151], v[4:7]\n"
    "s_waitcnt lgkmcnt(7)\n"
    "ds_read_b64_tr_b16 a[182:183], v247 offset:43008\n"
    "v_lshlrev_b32_e32 v228, 16, v228\n"
    "v_mfma_f32_16x16x32_bf16 v[24:27], a[160:163], v[212:215], v[24:27]\n"
    "buffer_load_dwordx4 v[16:19], v250, s[8:11], s89 offen offset:1024 sc1\n"
    "v_mfma_f32_16x16x32_bf16 v[24:27], a[164:167], v[216:219], v[24:27]\n"
    "ds_read_b64_tr_b16 a[184:185], v247 offset:16384\n"
    "v_cvt_pk_bf16_f32 v7, v6, v7\n"
    "v_mfma_f32_16x16x32_bf16 v[20:23], a[164:167], v[176:179], v[20:23]\n"
    "ds_read_b64_tr_b16 a[186:187], v247 offset:18432\n"
    "ds_read_b64_tr_b16 a[188:189], v247 offset:49152\n"
    "v_mfma_f32_16x16x32_bf16 v[20:23], a[160:163], v[172:175], v[20:23]\n"
    "s_waitcnt lgkmcnt(7)\n"
    "ds_read_b64_tr_b16 a[160:161], v244 offset:20480\n"
    "ds_read_b64_tr_b16 a[162:163], v244 offset:22528\n"
    "v_mfma_f32_16x16x32_bf16 v[20:23], a[168:171], v[140:143], v[20:23]\n"
    "buffer_load_dwordx4 v[116:119], v250, s[8:11], s62 offen offset:3072 sc1\n"
    "ds_read_b64_tr_b16 a[190:191], v247 offset:51200\n"
    "v_mfma_f32_16x16x32_bf16 v[20:23], a[172:175], v[144:147], v[20:23]\n"
    "s_add_u32 s62, s58, 16384\n"
    "ds_read_b64_tr_b16 a[164:165], v244 offset:53248\n"
    "v_cvt_pk_bf16_f32 v6, v4, v5\n"
    "v_mfma_f32_16x16x32_bf16 v[24:27], a[172:175], v[152:155], v[24:27]\n"
    "ds_read_b64_tr_b16 a[166:167], v244 offset:55296\n"
    "v_lshlrev_b32_e32 v229, 16, v229\n"
    "v_mfma_f32_16x16x32_bf16 v[24:27], a[168:171], v[148:151], v[24:27]\n"
    "s_waitcnt lgkmcnt(8)\n"
    "ds_read_b64_tr_b16 a[168:169], v245 offset:20480\n"
    "ds_read_b64_tr_b16 a[170:171], v245 offset:22528\n"
    "v_mfma_f32_16x16x32_bf16 v[44:47], a[176:179], v[212:215], v[44:47]\n"
    "buffer_load_dwordx4 a[192:195], v250, s[8:11], s87 offen offset:2048 sc1\n"
    "ds_read_b64_tr_b16 a[172:173], v245 offset:53248\n"
    "v_mfma_f32_16x16x32_bf16 v[44:47], a[180:183], v[216:219], v[44:47]\n"
    "ds_read_b64_tr_b16 a[174:175], v245 offset:55296\n"
    "v_accvgpr_read_b32 v4, a240\n"
    "v_mfma_f32_16x16x32_bf16 v[40:43], a[180:183], v[176:179], v[40:43]\n"
    "v_lshlrev_b32_e32 v230, 16, v230\n"
    "v_accvgpr_read_b32 v5, a241\n"
    "v_mfma_f32_16x16x32_bf16 v[40:43], a[176:179], v[172:175], v[40:43]\n"
    "s_waitcnt lgkmcnt(6)\n"
    "ds_read_b64_tr_b16 a[176:177], v246 offset:20480\n"
    "ds_read_b64_tr_b16 a[178:179], v246 offset:22528\n"
    "v_mfma_f32_16x16x32_bf16 v[40:43], a[184:187], v[140:143], v[40:43]\n"
    "buffer_load_dwordx4 v[136:139], v250, s[8:11], s62 offen offset:0 sc1\n"
    "v_mfma_f32_16x16x32_bf16 v[40:43], a[188:191], v[144:147], v[40:43]\n"
    "ds_read_b64_tr_b16 a[180:181], v246 offset:53248\n"
    "ds_read_b64_tr_b16 a[182:183], v246 offset:55296\n"
    "v_mfma_f32_16x16x32_bf16 v[44:47], a[188:191], v[152:155], v[44:47]\n"
    "v_accvgpr_read_b32 v225, a243\n"
    "v_cvt_pk_bf16_f32 v4, v4, v5\n"
    "v_mfma_f32_16x16x32_bf16 v[44:47], a[184:187], v[148:151], v[44:47]\n"
    "s_waitcnt lgkmcnt(8)\n"
    "ds_read_b64_tr_b16 a[184:185], v247 offset:20480\n"
    "ds_read_b64_tr_b16 a[186:187], v247 offset:22528\n"
    "v_mfma_f32_16x16x32_bf16 v[64:67], a[160:163], v[148:151], v[64:67]\n"
    "buffer_load_dwordx4 a[196:199], v250, s[8:11], s88 offen offset:2048 sc1\n"
    "v_mfma_f32_16x16x32_bf16 v[64:67], a[164:167], v[152:155], v[64:67]\n"
    "s_waitcnt lgkmcnt(6)\n"
    "ds_read_b64_tr_b16 a[188:189], v247 offset:53248\n"
    "ds_read_b64_tr_b16 a[190:191], v247 offset:55296\n"
    "v_mfma_f32_16x16x32_bf16 v[84:87], a[172:175], v[152:155], v[84:87]\n"
    "ds_read_b128 a[48:51], v240 offset:0\n"
    "v_accvgpr_read_b32 v5, a242\n"
    "v_mfma_f32_16x16x32_bf16 v[84:87], a[168:171], v[148:151], v[84:87]\n"
    "s_waitcnt lgkmcnt(5)\n"
    "ds_read_b128 a[52:55], v241 offset:0\n"
    "v_cvt_pk_bf16_f32 v5, v5, v225\n"
    "v_mfma_f32_16x16x32_bf16 v[104:107], a[176:179], v[148:151], v[104:107]\n"
    "buffer_store_dwordx4 v[4:7], v250, s[8:11], s80 offen offset:0 sc1\n"
    "v_mfma_f32_16x16x32_bf16 v[104:107], a[180:183], v[152:155], v[104:107]\n"
    "s_waitcnt lgkmcnt(2)\n"
    "ds_read_b128 a[56:59], v240 offset:32768\n"
    "v_cvt_pk_bf16_f32 v24, v24, v25\n"
    "v_mfma_f32_16x16x32_bf16 v[124:127], a[188:191], v[152:155], v[124:127]\n"
    "v_cvt_pk_bf16_f32 v25, v26, v27\n"
    "v_cvt_pk_bf16_f32 v26, v44, v45\n"
    "v_mfma_f32_16x16x32_bf16 v[124:127], a[184:187], v[148:151], v[124:127]\n"
    "ds_read_b128 a[60:63], v241 offset:32768\n"
    "v_cvt_pk_bf16_f32 v27, v46, v47\n"
    "v_mfma_f32_16x16x32_bf16 v[60:63], a[164:167], v[144:147], v[60:63]\n"
    "buffer_load_dwordx4 a[200:203], v250, s[8:11], s89 offen offset:2048 sc1\n"
    "v_mfma_f32_16x16x32_bf16 v[60:63], a[160:163], v[140:143], v[60:63]\n"
    "buffer_store_dwordx4 v[24:27], v250, s[8:11], s80 offen offset:1024 sc1\n"
    "v_mfma_f32_16x16x32_bf16 v[80:83], a[168:171], v[140:143], v[80:83]\n"
    "v_cvt_pk_bf16_f32 v64, v64, v65\n"
    "v_cvt_pk_bf16_f32 v65, v66, v67\n"
    "v_mfma_f32_16x16x32_bf16 v[80:83], a[172:175], v[144:147], v[80:83]\n"
    "v_cvt_pk_bf16_f32 v66, v84, v85\n"
    "v_cvt_pk_bf16_f32 v67, v86, v87\n"
    "v_mfma_f32_16x16x32_bf16 v[100:103], a[180:183], v[144:147], v[100:103]\n"
    "buffer_store_dwordx4 v[64:67], v250, s[8:11], s80 offen offset:2048 sc1\n"
    "v_mfma_f32_16x16x32_bf16 v[100:103], a[176:179], v[140:143], v[100:103]\n"
    "s_mov_b32 s45, 0x71800000\n"
    "v_cvt_pk_bf16_f32 v104, v104, v105\n"
    "v_cvt_pk_bf16_f32 v105, v106, v107\n"
    "v_mfma_f32_16x16x32_bf16 v[120:123], a[184:187], v[140:143], v[120:123]\n"
    "v_cvt_pk_bf16_f32 v106, v124, v125\n"
    "v_cvt_pk_bf16_f32 v107, v126, v127\n"
    "v_mfma_f32_16x16x32_bf16 v[120:123], a[188:191], v[144:147], v[120:123]\n"
    "s_waitcnt lgkmcnt(2)\n"
    "buffer_store_dwordx4 v[104:107], v250, s[8:11], s80 offen offset:3072 sc1\n"
    "v_mfma_f32_16x16x32_bf16 v[156:159], a[216:219], a[48:51], 0\n"
    "buffer_load_dwordx4 a[204:207], v250, s[8:11], s87 offen offset:3072 sc1\n"
    "ds_read_b128 a[160:163], v242 offset:0\n"
    "v_mfma_f32_16x16x32_bf16 v[160:163], a[220:223], a[48:51], 0\n"
    "ds_read_b128 a[164:167], v243 offset:0\n"
    "buffer_store_dword v127, v231, s[8:11], s86 offen offset:0 sc1\n"
    "v_mfma_f32_16x16x32_bf16 v[164:167], a[224:227], a[48:51], 0\n"
    "ds_read_b128 a[168:171], v242 offset:32768\n"
    "v_mfma_f32_16x16x32_bf16 v[168:171], a[228:231], a[48:51], 0\n"
    "ds_read_b128 a[172:175], v243 offset:32768\n"
    "v_add_f32_e32 v225, v123, v127\n"
    "v_mfma_f32_16x16x32_bf16 v[156:159], a[232:235], a[52:55], v[156:159]\n"
    "ds_read_b128 a[176:179], v240 offset:2048\n"
    "v_min3_f32 v129, v129, v123, v127\n"
    "v_mfma_f32_16x16x32_bf16 v[160:163], v[192:195], a[52:55], v[160:163]\n"
    "ds_read_b128 a[180:183], v241 offset:2048\n"
    "v_add_f32_e32 v225, v225, v128\n"
    "v_mfma_f32_16x16x32_bf16 v[164:167], a[64:67], a[52:55], v[164:167]\n"
    "ds_read_b128 a[184:187], v240 offset:34816\n"
    "v_mfma_f32_16x16x32_bf16 v[168:171], a[68:71], a[52:55], v[168:171]\n"
    "s_waitcnt lgkmcnt(7)\n"
    "ds_read_b128 a[188:191], v241 offset:34816\n"
    "v_mov_b32_e32 v227, v123\n"
    "v_mfma_f32_16x16x32_bf16 v[156:159], a[72:75], a[56:59], v[156:159]\n"
    "buffer_load_dwordx4 a[208:211], v250, s[8:11], s88 offen offset:3072 sc1\n"
    "ds_read_b128 a[48:51], v242 offset:2048\n"
    "v_mfma_f32_16x16x32_bf16 v[160:163], a[76:79], a[56:59], v[160:163]\n"
    "s_add_u32 s62, s58, 16384\n"
    "v_cmp_nlt_f32_e32 vcc, 0x12800000, v129\n"
    "v_cmp_nlt_f32_e64 s[28:29], v225, s45\n"
    "v_mfma_f32_16x16x32_bf16 v[164:167], a[80:83], a[56:59], v[164:167]\n"
    "buffer_load_dwordx4 a[0:3], v250, s[8:11], s62 offen offset:1024 sc1\n"
    "v_mfma_f32_16x16x32_bf16 v[168:171], a[84:87], a[56:59], v[168:171]\n"
    "s_or_b32 s45, s29, vcc_hi\n"
    "v_mfma_f32_16x16x32_bf16 v[156:159], a[88:91], a[60:63], v[156:159]\n"
    "s_lshr_b32 s45, s45, 16\n"
    "s_cselect_b32 s45, s51, 0\n"
    "ds_read_b128 a[52:55], v243 offset:2048\n"
    "v_mfma_f32_16x16x32_bf16 v[160:163], a[92:95], a[60:63], v[160:163]\n"
    "s_andn2_b32 s45, s45, s49\n"
    "v_mfma_f32_16x16x32_bf16 v[164:167], a[96:99], a[60:63], v[164:167]\n"
    "s_add_u32 s45, s45, s39\n"
    "v_mfma_f32_16x16x32_bf16 v[168:171], a[100:103], a[60:63], v[168:171]\n"
    "s_waitcnt lgkmcnt(8)\n"
    "v_mov_b32_e32 v254, s45\n"
    "v_mfma_f32_16x16x32_bf16 v[156:159], a[104:107], a[160:163], v[156:159]\n"
    "buffer_load_dwordx4 a[212:215], v250, s[8:11], s89 offen offset:3072 sc1\n"
    "ds_read_b128 a[56:59], v242 offset:34816\n"
    "v_mfma_f32_16x16x32_bf16 v[160:163], a[108:111], a[160:163], v[160:163]\n"
    "s_lshl1_add_u32 s55, s50, s51\n"
    "v_mfma_f32_16x16x32_bf16 v[164:167], a[112:115], a[160:163], v[164:167]\n"
    "s_lshl_b32 s55, s55, 12\n"
    "v_mfma_f32_16x16x32_bf16 v[168:171], a[116:119], a[160:163], v[168:171]\n"
    "s_xor_b32 s31, s93, 2\n"
    "v_mfma_f32_16x16x32_bf16 v[156:159], a[120:123], a[164:167], v[156:159]\n"
    "s_lshl_b32 s31, s31, 8\n"
    "ds_read_b128 a[60:63], v243 offset:34816\n"
    "v_and_b32_e32 v251, 0x7ff, v252\n"
    "v_mfma_f32_16x16x32_bf16 v[160:163], a[124:127], a[164:167], v[160:163]\n"
    "s_add_u32 s31, s31, 0x6000\n"
    "v_mfma_f32_16x16x32_bf16 v[164:167], a[128:131], a[164:167], v[164:167]\n"
    "s_mul_i32 s45, s34, 101376\n"
    "v_add_u32_e32 v251, s63, v251\n"
    "v_mfma_f32_16x16x32_bf16 v[168:171], a[132:135], a[164:167], v[168:171]\n"
    "s_waitcnt lgkmcnt(8)\n"
    "v_mfma_f32_16x16x32_bf16 v[156:159], a[136:139], a[168:171], v[156:159]\n"
    "buffer_load_dword v220, v220, s[8:11], s60 offen offset:192 sc1\n"
    "ds_read_b128 a[160:163], v240 offset:4096\n"
    "v_mfma_f32_16x16x32_bf16 v[160:163], a[140:143], a[168:171], v[160:163]\n"
    "s_add_u32 s45, s45, 16654336\n"
    "v_xor_b32_e32 v249, 64, v251\n"
    "v_mfma_f32_16x16x32_bf16 v[164:167], a[144:147], a[168:171], v[164:167]\n"
    "s_lshl_b32 s47, s93, 11\n"
    "v_mfma_f32_16x16x32_bf16 v[168:171], a[148:151], a[168:171], v[168:171]\n"
    "s_add_u32 s54, s47, s45\n"
    "v_mfma_f32_16x16x32_bf16 v[156:159], a[152:155], a[172:175], v[156:159]\n"
    "s_add_u32 s45, s45, 32768\n"
    "ds_read_b128 a[164:167], v241 offset:4096\n"
    "v_mfma_f32_16x16x32_bf16 v[160:163], a[156:159], a[172:175], v[160:163]\n"
    "s_cmp_lt_u32 s32, 2\n"
    "s_cselect_b32 s54, s54, 2.0\n"
    "s_cselect_b32 s45, s45, 2.0\n"
    "v_mfma_f32_16x16x32_bf16 v[164:167], v[180:183], a[172:175], v[164:167]\n"
    "s_add_u32 s80, s35, s46\n"
    "v_mfma_f32_16x16x32_bf16 v[168:171], v[184:187], a[172:175], v[168:171]\n"
    "s_waitcnt lgkmcnt(8)\n"
    "v_mfma_f32_16x16x32_bf16 v[196:199], a[216:219], a[176:179], 0\n"
    "s_add_u32 s82, s36, s46\n"
    "ds_read_b128 a[168:171], v240 offset:36864\n"
    "v_mfma_f32_16x16x32_bf16 v[200:203], a[220:223], a[176:179], 0\n"
    "s_add_u32 s28, s37, s46\n"
    "v_mfma_f32_16x16x32_bf16 v[204:207], a[224:227], a[176:179], 0\n"
    "buffer_load_dwordx4 a[4:7], v250, s[8:11], s62 offen offset:2048 sc1\n"
    "v_mfma_f32_16x16x32_bf16 v[208:211], a[228:231], a[176:179], 0\n"
    "v_add_u32_e32 v244, s55, v250\n"
    "v_lshrrev_b32_e32 v247, 2, v250\n"
    "v_mfma_f32_16x16x32_bf16 v[196:199], a[232:235], a[180:183], v[196:199]\n"
    "ds_read_b128 a[172:175], v241 offset:36864\n"
    "v_mfma_f32_16x16x32_bf16 v[200:203], v[192:195], a[180:183], v[200:203]\n"
    "v_add_u32_e32 v247, s31, v247\n"
    "v_add_u32_e32 v246, s47, v250\n"
    "v_mfma_f32_16x16x32_bf16 v[204:207], a[64:67], a[180:183], v[204:207]\n"
    "v_mul_f32_e32 v156, v156, v228\n"
    "v_mul_f32_e32 v157, v157, v228\n"
    "v_mfma_f32_16x16x32_bf16 v[208:211], a[68:71], a[180:183], v[208:211]\n"
    "s_waitcnt lgkmcnt(8)\n"
    "v_mfma_f32_16x16x32_bf16 v[196:199], a[72:75], a[184:187], v[196:199]\n"
    "ds_read_b128 a[176:179], v242 offset:4096\n"
    "v_mfma_f32_16x16x32_bf16 v[200:203], a[76:79], a[184:187], v[200:203]\n"
    "v_mul_f32_e32 v158, v158, v228\n"
    "v_mfma_f32_16x16x32_bf16 v[204:207], a[80:83], a[184:187], v[204:207]\n"
    "s_waitcnt vmcnt(5)\n"
    "buffer_store_dword v254, off, s[8:11], s90 offset:0 sc1\n"
    "v_mfma_f32_16x16x32_bf16 v[208:211], a[84:87], a[184:187], v[208:211]\n"
    "v_mul_f32_e32 v159, v159, v228\n"
    "v_mfma_f32_16x16x32_bf16 v[196:199], a[88:91], a[188:191], v[196:199]\n"
    "ds_read_b128 a[180:183], v243 offset:4096\n"
    "v_and_b32_e32 v245, 0x603f, v247\n"
    "v_mfma_f32_16x16x32_bf16 v[200:203], a[92:95], a[188:191], v[200:203]\n"
    "v_bfe_u32 v226, v250, 2, 4\n"
    "v_mfma_f32_16x16x32_bf16 v[204:207], a[96:99], a[188:191], v[204:207]\n"
    "v_bfe_u32 v225, v250, 6, 2\n"
    "v_lshl_or_b32 v226, v225, 7, v226\n"
    "v_mfma_f32_16x16x32_bf16 v[208:211], a[100:103], a[188:191], v[208:211]\n"
    "s_waitcnt lgkmcnt(8)\n"
    "v_and_b32_e32 v225, 0x60ff, v247\n"
    "v_mul_f32_e32 v160, v160, v228\n"
    "v_mfma_f32_16x16x32_bf16 v[196:199], a[104:107], a[48:51], v[196:199]\n"
    "ds_read_b128 a[184:187], v242 offset:36864\n"
    "v_mfma_f32_16x16x32_bf16 v[200:203], a[108:111], a[48:51], v[200:203]\n"
    "buffer_load_dwordx4 a[8:11], v250, s[8:11], s62 offen offset:3072 sc1\n"
    "v_mfma_f32_16x16x32_bf16 v[204:207], a[112:115], a[48:51], v[204:207]\n"
    "s_add_u32 s62, s58, 20480\n"
    "v_mul_f32_e32 v161, v161, v228\n"
    "v_mfma_f32_16x16x32_bf16 v[208:211], a[116:119], a[48:51], v[208:211]\n"
    "s_waitcnt vmcnt(21)\n"
    "v_mul_f32_e32 v162, v162, v228\n"
    "v_mfma_f32_16x16x32_bf16 v[196:199], a[120:123], a[52:55], v[196:199]\n"
    "ds_read_b128 a[188:191], v243 offset:36864\n"
    "v_mfma_f32_16x16x32_bf16 v[200:203], a[124:127], a[52:55], v[200:203]\n"
    "buffer_load_dwordx4 a[12:15], v250, s[8:11], s62 offen offset:0 sc1\n"
    "v_mfma_f32_16x16x32_bf16 v[204:207], a[128:131], a[52:55], v[204:207]\n"
    "v_mul_f32_e32 v163, v163, v228\n"
    "v_mul_f32_e32 v164, v164, v228\n"
    "v_mfma_f32_16x16x32_bf16 v[208:211], a[132:135], a[52:55], v[208:211]\n"
    "s_waitcnt lgkmcnt(8)\n"
    "v_mul_f32_e32 v165, v165, v228\n"
    "v_mul_f32_e32 v166, v166, v228\n"
    "v_mfma_f32_16x16x32_bf16 v[196:199], a[136:139], a[56:59], v[196:199]\n"
    "v_mul_f32_e32 v167, v167, v228\n"
    "v_mfma_f32_16x16x32_bf16 v[200:203], a[140:143], a[56:59], v[200:203]\n"
    "buffer_load_dwordx4 a[16:19], v250, s[8:11], s62 offen offset:1024 sc1\n"
    "v_mfma_f32_16x16x32_bf16 v[204:207], a[144:147], a[56:59], v[204:207]\n"
    "v_mul_f32_e32 v168, v168, v228\n"
    "v_mfma_f32_16x16x32_bf16 v[208:211], a[148:151], a[56:59], v[208:211]\n"
    "ds_read_b128 a[48:51], v251 offset:6144\n"
    "v_mfma_f32_16x16x32_bf16 v[196:199], a[152:155], a[60:63], v[196:199]\n"
    "ds_read_b128 a[52:55], v249 offset:6144\n"
    "v_mul_f32_e32 v169, v169, v228\n"
    "v_mfma_f32_16x16x32_bf16 v[200:203], a[156:159], a[60:63], v[200:203]\n"
    "buffer_load_dwordx4 a[20:23], v250, s[8:11], s62 offen offset:2048 sc1\n"
    "v_mfma_f32_16x16x32_bf16 v[204:207], v[180:183], a[60:63], v[204:207]\n"
    "s_barrier\n"
    "v_mfma_f32_16x16x32_bf16 v[208:211], v[184:187], a[60:63], v[208:211]\n"
    "s_waitcnt vmcnt(6)\n"
    "v_mul_f32_e32 v170, v170, v228\n"
    "v_mul_f32_e32 v171, v171, v228\n"
    "v_mfma_f32_16x16x32_bf16 a[236:239], v[234:237], a[244:247], a[236:239]\n"
    "v_cmp_le_u32_e32 vcc, 0x300, v250\n"
    "v_mfma_f32_16x16x32_bf16 v[0:3], v[232:235], a[244:247], v[0:3]\n"
    "buffer_load_dwordx4 a[24:27], v250, s[8:11], s62 offen offset:3072 sc1\n"
    "v_mfma_f32_16x16x32_bf16 a[236:239], v[234:237], a[248:251], a[236:239]\n"
    "s_add_u32 s62, s58, 24576\n"
    "v_cndmask_b32_e32 v227, v220, v227, vcc\n"
    "v_mfma_f32_16x16x32_bf16 v[0:3], v[232:235], a[248:251], v[0:3]\n"
    "ds_write_b32 v247, v227 offset:0\n"
    "v_mfma_f32_16x16x32_bf16 a[236:239], v[234:237], a[252:255], a[236:239]\n"
    "v_mfma_f32_16x16x32_bf16 v[0:3], v[232:235], a[252:255], v[0:3]\n"
    "buffer_load_dwordx4 a[28:31], v250, s[8:11], s62 offen offset:0 sc1\n"
    "v_mfma_f32_16x16x32_bf16 v[20:23], v[234:237], v[8:11], v[20:23]\n"
    "v_accvgpr_read_b32 v64, a236\n"
    "v_accvgpr_read_b32 v65, a237\n"
    "v_mfma_f32_16x16x32_bf16 v[40:43], v[232:235], v[8:11], v[40:43]\n"
    "v_accvgpr_read_b32 v66, a238\n"
    "v_mfma_f32_16x16x32_bf16 v[20:23], v[234:237], v[12:15], v[20:23]\n"
    "v_accvgpr_read_b32 v67, a239\n"
    "v_cvt_pk_bf16_f32 v64, v64, v65\n"
    "v_mfma_f32_16x16x32_bf16 v[40:43], v[232:235], v[12:15], v[40:43]\n"
    "buffer_load_dwordx4 a[32:35], v250, s[8:11], s62 offen offset:1024 sc1\n"
    "v_mfma_f32_16x16x32_bf16 v[20:23], v[234:237], v[16:19], v[20:23]\n"
    "v_cvt_pk_bf16_f32 v65, v66, v67\n"
    "v_mfma_f32_16x16x32_bf16 v[40:43], v[232:235], v[16:19], v[40:43]\n"
    "v_cvt_pk_bf16_f32 v66, v0, v1\n"
    "v_mfma_f32_16x16x32_bf16 v[60:63], v[234:237], a[192:195], v[60:63]\n"
    "v_cvt_pk_bf16_f32 v67, v2, v3\n"
    "v_mfma_f32_16x16x32_bf16 v[80:83], v[232:235], a[192:195], v[80:83]\n"
    "buffer_load_dwordx4 a[36:39], v250, s[8:11], s62 offen offset:2048 sc1\n"
    "v_mfma_f32_16x16x32_bf16 v[60:63], v[234:237], a[196:199], v[60:63]\n"
    "ds_write_b128 v244, v[64:67] offset:0\n"
    "v_mfma_f32_16x16x32_bf16 v[80:83], v[232:235], a[196:199], v[80:83]\n"
    "v_cvt_pk_bf16_f32 v20, v20, v21\n"
    "v_cvt_pk_bf16_f32 v21, v22, v23\n"
    "v_mfma_f32_16x16x32_bf16 v[60:63], v[234:237], a[200:203], v[60:63]\n"
    "v_cvt_pk_bf16_f32 v22, v40, v41\n"
    "v_cvt_pk_bf16_f32 v23, v42, v43\n"
    "v_mfma_f32_16x16x32_bf16 v[80:83], v[232:235], a[200:203], v[80:83]\n"
    "buffer_load_dwordx4 a[40:43], v250, s[8:11], s62 offen offset:3072 sc1\n"
    "v_mfma_f32_16x16x32_bf16 v[100:103], v[234:237], a[204:207], v[100:103]\n"
    "s_add_u32 s62, s58, 28672\n"
    "ds_write_b128 v244, v[20:23] offset:1024\n"
    "v_mfma_f32_16x16x32_bf16 v[120:123], v[232:235], a[204:207], v[120:123]\n"
    "v_cvt_pk_bf16_f32 v60, v60, v61\n"
    "v_mfma_f32_16x16x32_bf16 v[100:103], v[234:237], a[208:211], v[100:103]\n"
    "v_cvt_pk_bf16_f32 v61, v62, v63\n"
    "v_mfma_f32_16x16x32_bf16 v[120:123], v[232:235], a[208:211], v[120:123]\n"
    "buffer_load_dwordx4 a[44:47], v250, s[8:11], s62 offen offset:0 sc1\n"
    "v_mfma_f32_16x16x32_bf16 v[100:103], v[234:237], a[212:215], v[100:103]\n"
    "v_cvt_pk_bf16_f32 v62, v80, v81\n"
    "v_cvt_pk_bf16_f32 v63, v82, v83\n"
    "v_mfma_f32_16x16x32_bf16 v[120:123], v[232:235], a[212:215], v[120:123]\n"
    "s_waitcnt lgkmcnt(11)\n"
    "ds_write_b128 v244, v[60:63] offset:2048\n"
    "v_mfma_f32_16x16x32_bf16 v[172:175], a[216:219], a[160:163], 0\n"
    "buffer_load_dword v253, v226, s[8:11], s61 offen offset:0 sc1\n"
    "v_mfma_f32_16x16x32_bf16 v[176:179], a[220:223], a[160:163], 0\n"
    "v_cvt_pk_bf16_f32 v100, v100, v101\n"
    "v_mfma_f32_16x16x32_bf16 v[212:215], a[224:227], a[160:163], 0\n"
    "buffer_load_dwordx4 a[240:243], v250, s[8:11], s62 offen offset:1024 sc1\n"
    "v_mfma_f32_16x16x32_bf16 v[216:219], a[228:231], a[160:163], 0\n"
    "v_cvt_pk_bf16_f32 v101, v102, v103\n"
    "v_mfma_f32_16x16x32_bf16 v[172:175], a[232:235], a[164:167], v[172:175]\n"
    "v_cvt_pk_bf16_f32 v102, v120, v121\n"
    "v_cvt_pk_bf16_f32 v103, v122, v123\n"
    "v_mfma_f32_16x16x32_bf16 v[176:179], v[192:195], a[164:167], v[176:179]\n"
    "ds_write_b128 v244, v[100:103] offset:3072\n"
    "v_mfma_f32_16x16x32_bf16 v[212:215], a[64:67], a[164:167], v[212:215]\n"
    "buffer_load_dwordx4 v[4:7], v250, s[8:11], s62 offen offset:2048 sc1\n"
    "v_mfma_f32_16x16x32_bf16 v[216:219], a[68:71], a[164:167], v[216:219]\n"
    "s_waitcnt lgkmcnt(12)\n"
    "v_mfma_f32_16x16x32_bf16 v[172:175], a[72:75], a[168:171], v[172:175]\n"
    "v_mfma_f32_16x16x32_bf16 v[176:179], a[76:79], a[168:171], v[176:179]\n"
    "s_waitcnt lgkmcnt(0)\n"
    "v_mfma_f32_16x16x32_bf16 v[212:215], a[80:83], a[168:171], v[212:215]\n"
    "s_barrier\n"
    "v_mfma_f32_16x16x32_bf16 v[216:219], a[84:87], a[168:171], v[216:219]\n"
    "buffer_load_dwordx4 v[24:27], v250, s[8:11], s62 offen offset:3072 sc1\n"
    "v_mfma_f32_16x16x32_bf16 v[172:175], a[88:91], a[172:175], v[172:175]\n"
    "ds_read_b128 a[244:247], v250 offset:0\n"
    "v_mul_f32_e32 v196, v196, v229\n"
    "v_mfma_f32_16x16x32_bf16 v[176:179], a[92:95], a[172:175], v[176:179]\n"
    "ds_read_b128 a[248:251], v250 offset:1024\n"
    "v_mfma_f32_16x16x32_bf16 v[212:215], a[96:99], a[172:175], v[212:215]\n"
    "ds_read_b128 a[192:195], v250 offset:2048\n"
    "v_mfma_f32_16x16x32_bf16 v[216:219], a[100:103], a[172:175], v[216:219]\n"
    "ds_read_b128 a[196:199], v250 offset:3072\n"
    "v_mfma_f32_16x16x32_bf16 v[172:175], a[104:107], a[176:179], v[172:175]\n"
    "ds_read_b128 a[200:203], v250 offset:4096\n"
    "v_mfma_f32_16x16x32_bf16 v[176:179], a[108:111], a[176:179], v[176:179]\n"
    "v_mfma_f32_16x16x32_bf16 v[212:215], a[112:115], a[176:179], v[212:215]\n"
    "ds_read_b128 a[204:207], v250 offset:5120\n"
    "v_mul_f32_e32 v197, v197, v229\n"
    "v_mfma_f32_16x16x32_bf16 v[216:219], a[116:119], a[176:179], v[216:219]\n"
    "ds_read_b128 a[208:211], v250 offset:6144\n"
    "v_mul_f32_e32 v198, v198, v229\n"
    "v_mfma_f32_16x16x32_bf16 v[172:175], a[120:123], a[180:183], v[172:175]\n"
    "ds_read_b128 a[212:215], v250 offset:7168\n"
    "v_mfma_f32_16x16x32_bf16 v[176:179], a[124:127], a[180:183], v[176:179]\n"
    "v_mul_f32_e32 v199, v199, v229\n"
    "v_mul_f32_e32 v200, v200, v229\n"
    "v_mfma_f32_16x16x32_bf16 v[212:215], a[128:131], a[180:183], v[212:215]\n"
    "ds_read2_b32 v[220:221], v245 offset0:0 offset1:16\n"
    "v_mfma_f32_16x16x32_bf16 v[216:219], a[132:135], a[180:183], v[216:219]\n"
    "ds_read2_b32 v[222:223], v245 offset0:32 offset1:48\n"
    "v_mul_f32_e32 v201, v201, v229\n"
    "v_mfma_f32_16x16x32_bf16 v[172:175], a[136:139], a[184:187], v[172:175]\n"
    "ds_read2_b32 v[80:81], v245 offset0:64 offset1:80\n"
    "v_mfma_f32_16x16x32_bf16 v[176:179], a[140:143], a[184:187], v[176:179]\n"
    "v_mul_f32_e32 v202, v202, v229\n"
    "v_mul_f32_e32 v203, v203, v229\n"
    "v_mfma_f32_16x16x32_bf16 v[212:215], a[144:147], a[184:187], v[212:215]\n"
    "v_mul_f32_e32 v204, v204, v229\n"
    "v_mfma_f32_16x16x32_bf16 v[216:219], a[148:151], a[184:187], v[216:219]\n"
    "ds_read2_b32 v[82:83], v245 offset0:96 offset1:112\n"
    "v_mfma_f32_16x16x32_bf16 v[172:175], a[152:155], a[188:191], v[172:175]\n"
    "v_mfma_f32_16x16x32_bf16 v[176:179], a[156:159], a[188:191], v[176:179]\n"
    "v_mul_f32_e32 v205, v205, v229\n"
    "v_mfma_f32_16x16x32_bf16 v[212:215], v[180:183], a[188:191], v[212:215]\n"
    "v_mul_f32_e32 v206, v206, v229\n"
    "v_mfma_f32_16x16x32_bf16 v[216:219], v[184:187], a[188:191], v[216:219]\n"
    "v_mul_f32_e32 v207, v207, v229\n"
    "v_mfma_f32_16x16x32_bf16 v[156:159], v[234:237], a[48:51], v[156:159]\n"
    "v_mfma_f32_16x16x32_bf16 v[160:163], v[232:235], a[48:51], v[160:163]\n"
    "ds_read_b128 a[64:67], v250 offset:8192\n"
    "v_mfma_f32_16x16x32_bf16 v[164:167], v[234:237], a[52:55], v[164:167]\n"
    "v_mfma_f32_16x16x32_bf16 v[168:171], v[232:235], a[52:55], v[168:171]\n"
    "s_waitcnt lgkmcnt(3)\n"
    "ds_read_b128 a[68:71], v250 offset:9216\n"
    "v_mul_f32_e32 v208, v208, v229\n"
    "v_mfma_f32_16x16x32_bf16 v[140:143], v[28:31], a[244:247], 0\n"
    "ds_read_b128 a[72:75], v250 offset:10240\n"
    "v_add_f32_e32 v220, v220, v221\n"
    "v_mfma_f32_16x16x32_bf16 v[144:147], v[48:51], a[244:247], 0\n"
    "ds_read_b128 a[76:79], v250 offset:11264\n"
    "v_add_f32_e32 v222, v222, v223\n"
    "v_mfma_f32_16x16x32_bf16 v[148:151], v[32:35], a[244:247], 0\n"
    "ds_read_b128 a[80:83], v250 offset:12288\n"
    "v_mfma_f32_16x16x32_bf16 v[152:155], v[52:55], a[244:247], 0\n"
    "s_waitcnt vmcnt(3)\n"
    "v_subrev_u32_e32 v253, s39, v253\n"
    "v_cmp_lt_u32_e32 vcc, 1, v253\n"
    "v_mfma_f32_16x16x32_bf16 v[140:143], v[36:39], a[248:251], v[140:143]\n"
    "v_mul_f32_e32 v209, v209, v229\n"
    "v_mfma_f32_16x16x32_bf16 v[144:147], v[56:59], a[248:251], v[144:147]\n"
    "ds_read_b128 a[84:87], v250 offset:13312\n"
    "v_mul_f32_e32 v210, v210, v229\n"
    "v_mfma_f32_16x16x32_bf16 v[148:151], v[68:71], a[248:251], v[148:151]\n"
    "s_cbranch_vccnz .Lat_ppoll\n"
    ".Lat_pok:\n"
    "v_cmp_eq_u32_e32 vcc, 1, v253\n"
    "v_mfma_f32_16x16x32_bf16 v[152:155], v[88:91], a[248:251], v[152:155]\n"
    "buffer_load_dwordx4 a[128:131], v250, s[8:11], s54 offen offset:0 sc1\n"
    "v_mfma_f32_16x16x32_bf16 v[140:143], v[72:75], a[192:195], v[140:143]\n"
    "v_mfma_f32_16x16x32_bf16 v[144:147], v[92:95], a[192:195], v[144:147]\n"
    "s_cbranch_vccnz .Lat_redo\n"
    "v_mfma_f32_16x16x32_bf16 v[148:151], v[76:79], a[192:195], v[148:151]\n"
    "buffer_load_dwordx4 a[132:135], v250, s[8:11], s54 offen offset:1024 sc1\n"
    "v_mfma_f32_16x16x32_bf16 v[152:155], v[96:99], a[192:195], v[152:155]\n"
    "s_add_u32 s54, s54, 8192\n"
    "ds_read_b128 a[88:91], v250 offset:14336\n"
    "v_mfma_f32_16x16x32_bf16 v[140:143], v[108:111], a[196:199], v[140:143]\n"
    "buffer_load_dwordx4 a[136:139], v250, s[8:11], s54 offen offset:0 sc1\n"
    "v_mfma_f32_16x16x32_bf16 v[144:147], v[112:115], a[196:199], v[144:147]\n"
    "v_add_f32_e32 v220, v220, v222\n"
    "v_mfma_f32_16x16x32_bf16 v[148:151], v[132:135], a[196:199], v[148:151]\n"
    "s_waitcnt vmcnt(3)\n"
    "buffer_load_dwordx4 a[140:143], v250, s[8:11], s54 offen offset:1024 sc1\n"
    "v_mfma_f32_16x16x32_bf16 v[152:155], v[116:119], a[196:199], v[152:155]\n"
    "s_add_u32 s54, s54, 8192\n"
    "s_waitcnt lgkmcnt(8)\n"
    "ds_read_b128 a[92:95], v250 offset:15360\n"
    "v_mfma_f32_16x16x32_bf16 v[140:143], v[136:139], a[200:203], v[140:143]\n"
    "buffer_load_dwordx4 a[144:147], v250, s[8:11], s54 offen offset:0 sc1\n"
    "v_mfma_f32_16x16x32_bf16 v[144:147], a[0:3], a[200:203], v[144:147]\n"
    "v_rcp_f32_e32 v220, v220\n"
    "v_mfma_f32_16x16x32_bf16 v[148:151], a[4:7], a[200:203], v[148:151]\n"
    "buffer_load_dwordx4 a[148:151], v250, s[8:11], s54 offen offset:1024 sc1\n"
    "v_mfma_f32_16x16x32_bf16 v[152:155], a[8:11], a[200:203], v[152:155]\n"
    "s_add_u32 s54, s54, 8192\n"
    "v_mul_f32_e32 v172, v172, v230\n"
    "v_mfma_f32_16x16x32_bf16 v[140:143], a[12:15], a[204:207], v[140:143]\n"
    "buffer_load_dwordx4 a[152:155], v250, s[8:11], s54 offen offset:0 sc1\n"
    "v_mfma_f32_16x16x32_bf16 v[144:147], a[16:19], a[204:207], v[144:147]\n"
    "v_mul_f32_e32 v211, v211, v229\n"
    "v_add_f32_e32 v80, v80, v81\n"
    "v_mfma_f32_16x16x32_bf16 v[148:151], a[20:23], a[204:207], v[148:151]\n"
    "buffer_load_dwordx4 a[156:159], v250, s[8:11], s54 offen offset:1024 sc1\n"
    "v_mfma_f32_16x16x32_bf16 v[152:155], a[24:27], a[204:207], v[152:155]\n"
    "s_waitcnt lgkmcnt(8)\n"
    "ds_read_b128 a[56:59], v252 offset:30720\n"
    "v_mfma_f32_16x16x32_bf16 v[140:143], a[28:31], a[208:211], v[140:143]\n"
    "buffer_load_dword v227, v248, s[8:11], s45 offen offset:1088 sc1\n"
    "v_mfma_f32_16x16x32_bf16 v[144:147], a[32:35], a[208:211], v[144:147]\n"
    "v_add_f32_e32 v82, v82, v83\n"
    "v_mfma_f32_16x16x32_bf16 v[148:151], a[36:39], a[208:211], v[148:151]\n"
    "v_mfma_f32_16x16x32_bf16 v[152:155], a[40:43], a[208:211], v[152:155]\n"
    "ds_read_b128 a[60:63], v255 offset:30720\n"
    "v_mul_f32_e32 v173, v173, v230\n"
    "v_mfma_f32_16x16x32_bf16 v[140:143], a[44:47], a[212:215], v[140:143]\n"
    "v_add_f32_e32 v80, v80, v82\n"
    "v_mfma_f32_16x16x32_bf16 v[144:147], a[240:243], a[212:215], v[144:147]\n"
    "ds_read_b128 a[160:163], v252 offset:4096\n"
    "v_mul_f32_e32 v174, v174, v230\n"
    "v_mfma_f32_16x16x32_bf16 v[148:151], v[4:7], a[212:215], v[148:151]\n"
    "v_rcp_f32_e32 v80, v80\n"
    "v_mfma_f32_16x16x32_bf16 v[152:155], v[24:27], a[212:215], v[152:155]\n"
    "s_waitcnt lgkmcnt(9)\n"
    "ds_read_b128 a[164:167], v255 offset:4096\n"
    "v_mul_f32_e32 v175, v175, v230\n"
    "v_mfma_f32_16x16x32_bf16 v[180:183], v[28:31], a[64:67], 0\n"
    "v_mul_f32_e32 v176, v176, v230\n"
    "v_mfma_f32_16x16x32_bf16 v[184:187], v[48:51], a[64:67], 0\n"
    "v_fma_f32 v140, v140, v220, v156\n"
    "v_fma_f32 v141, v141, v220, v157\n"
    "v_mfma_f32_16x16x32_bf16 v[188:191], v[32:35], a[64:67], 0\n"
    "v_fma_f32 v142, v142, v220, v158\n"
    "v_mfma_f32_16x16x32_bf16 v[192:195], v[52:55], a[64:67], 0\n"
    "v_fma_f32 v143, v143, v220, v159\n"
    "v_mfma_f32_16x16x32_bf16 v[180:183], v[36:39], a[68:71], v[180:183]\n"
    "v_fma_f32 v144, v144, v220, v160\n"
    "v_mfma_f32_16x16x32_bf16 v[184:187], v[56:59], a[68:71], v[184:187]\n"
    "v_fma_f32 v145, v145, v220, v161\n"
    "v_fma_f32 v146, v146, v220, v162\n"
    "v_mfma_f32_16x16x32_bf16 v[188:191], v[68:71], a[68:71], v[188:191]\n"
    "v_fma_f32 v147, v147, v220, v163\n"
    "v_mfma_f32_16x16x32_bf16 v[192:195], v[88:91], a[68:71], v[192:195]\n"
    "s_waitcnt lgkmcnt(8)\n"
    "v_cvt_pk_bf16_f32 v140, v140, v141\n"
    "v_mfma_f32_16x16x32_bf16 v[180:183], v[72:75], a[72:75], v[180:183]\n"
    "v_cvt_pk_bf16_f32 v141, v142, v143\n"
    "v_mfma_f32_16x16x32_bf16 v[184:187], v[92:95], a[72:75], v[184:187]\n"
    "v_mul_f32_e32 v177, v177, v230\n"
    "v_mfma_f32_16x16x32_bf16 v[188:191], v[76:79], a[72:75], v[188:191]\n"
    "v_cvt_pk_bf16_f32 v142, v144, v145\n"
    "v_mfma_f32_16x16x32_bf16 v[192:195], v[96:99], a[72:75], v[192:195]\n"
    "v_cvt_pk_bf16_f32 v143, v146, v147\n"
    "v_mfma_f32_16x16x32_bf16 v[180:183], v[108:111], a[76:79], v[180:183]\n"
    "v_mul_f32_e32 v178, v178, v230\n"
    "v_mfma_f32_16x16x32_bf16 v[184:187], v[112:115], a[76:79], v[184:187]\n"
    "buffer_store_dwordx4 v[140:143], v239, s[16:19], s80 offen offset:0 sc0 sc1\n"
    "v_mfma_f32_16x16x32_bf16 v[188:191], v[132:135], a[76:79], v[188:191]\n"
    "v_mul_f32_e32 v179, v179, v230\n"
    "v_fma_f32 v148, v148, v220, v164\n"
    "v_mfma_f32_16x16x32_bf16 v[192:195], v[116:119], a[76:79], v[192:195]\n"
    "s_waitcnt lgkmcnt(6)\n"
    "v_mfma_f32_16x16x32_bf16 v[180:183], v[136:139], a[80:83], v[180:183]\n"
    "v_fma_f32 v149, v149, v220, v165\n"
    "v_fma_f32 v150, v150, v220, v166\n"
    "v_mfma_f32_16x16x32_bf16 v[184:187], a[0:3], a[80:83], v[184:187]\n"
    "v_fma_f32 v151, v151, v220, v167\n"
    "v_fma_f32 v152, v152, v220, v168\n"
    "v_mfma_f32_16x16x32_bf16 v[188:191], a[4:7], a[80:83], v[188:191]\n"
    "v_fma_f32 v153, v153, v220, v169\n"
    "v_fma_f32 v154, v154, v220, v170\n"
    "v_mfma_f32_16x16x32_bf16 v[192:195], a[8:11], a[80:83], v[192:195]\n"
    "v_fma_f32 v155, v155, v220, v171\n"
    "v_cvt_pk_bf16_f32 v148, v148, v149\n"
    "v_mfma_f32_16x16x32_bf16 v[180:183], a[12:15], a[84:87], v[180:183]\n"
    "v_cvt_pk_bf16_f32 v149, v150, v151\n"
    "v_cvt_pk_bf16_f32 v150, v152, v153\n"
    "v_mfma_f32_16x16x32_bf16 v[184:187], a[16:19], a[84:87], v[184:187]\n"
    "v_cvt_pk_bf16_f32 v151, v154, v155\n"
    "v_mfma_f32_16x16x32_bf16 v[188:191], a[20:23], a[84:87], v[188:191]\n"
    "buffer_store_dwordx4 v[148:151], v239, s[16:19], s80 offen offset:64 sc0 sc1\n"
    "v_mfma_f32_16x16x32_bf16 v[192:195], a[24:27], a[84:87], v[192:195]\n"
    "s_waitcnt lgkmcnt(4)\n"
    "v_mfma_f32_16x16x32_bf16 v[180:183], a[28:31], a[88:91], v[180:183]\n"
    "v_mul_f32_e32 v212, v212, v230\n"
    "v_mfma_f32_16x16x32_bf16 v[184:187], a[32:35], a[88:91], v[184:187]\n"
    "v_mfma_f32_16x16x32_bf16 v[188:191], a[36:39], a[88:91], v[188:191]\n"
    "v_mfma_f32_16x16x32_bf16 v[192:195], a[40:43], a[88:91], v[192:195]\n"
    "s_waitcnt vmcnt(9)\n"
    "v_mul_f32_e32 v213, v213, v230\n"
    "v_mul_f32_e32 v214, v214, v230\n"
    "v_mfma_f32_16x16x32_bf16 v[64:67], v[234:237], a[128:131], 0\n"
    "v_mul_f32_e32 v215, v215, v230\n"
    "v_mfma_f32_16x16x32_bf16 v[84:87], v[232:235], a[128:131], 0\n"
    "v_mfma_f32_16x16x32_bf16 v[104:107], v[234:237], a[132:135], 0\n"
    "v_mfma_f32_16x16x32_bf16 v[124:127], v[232:235], a[132:135], 0\n"
    "s_waitcnt vmcnt(7)\n"
    "v_mfma_f32_16x16x32_bf16 v[64:67], v[234:237], a[136:139], v[64:67]\n"
    "v_mfma_f32_16x16x32_bf16 v[84:87], v[232:235], a[136:139], v[84:87]\n"
    "v_mul_f32_e32 v216, v216, v230\n"
    "v_mfma_f32_16x16x32_bf16 v[104:107], v[234:237], a[140:143], v[104:107]\n"
    "v_mfma_f32_16x16x32_bf16 v[124:127], v[232:235], a[140:143], v[124:127]\n"
    "s_waitcnt vmcnt(5)\n"
    "v_mul_f32_e32 v217, v217, v230\n"
    "v_mfma_f32_16x16x32_bf16 v[64:67], v[234:237], a[144:147], v[64:67]\n"
    "v_mul_f32_e32 v218, v218, v230\n"
    "v_mfma_f32_16x16x32_bf16 v[84:87], v[232:235], a[144:147], v[84:87]\n"
    "v_mul_f32_e32 v219, v219, v230\n"
    "v_mfma_f32_16x16x32_bf16 v[104:107], v[234:237], a[148:151], v[104:107]\n"
    "v_mfma_f32_16x16x32_bf16 v[124:127], v[232:235], a[148:151], v[124:127]\n"
    "s_waitcnt vmcnt(3)\n"
    "v_mfma_f32_16x16x32_bf16 v[64:67], v[234:237], a[152:155], v[64:67]\n"
    "s_waitcnt vmcnt(2)\n"
    "ds_write_b32 v225, v227 offset:512\n"
    "v_mfma_f32_16x16x32_bf16 v[84:87], v[232:235], a[152:155], v[84:87]\n"
    "v_mfma_f32_16x16x32_bf16 v[104:107], v[234:237], a[156:159], v[104:107]\n"
    "v_mfma_f32_16x16x32_bf16 v[124:127], v[232:235], a[156:159], v[124:127]\n"
    "v_cvt_pk_bf16_f32 v64, v64, v65\n"
    "v_cvt_pk_bf16_f32 v65, v66, v67\n"
    "v_mfma_f32_16x16x32_bf16 v[180:183], a[44:47], a[92:95], v[180:183]\n"
    "v_cvt_pk_bf16_f32 v66, v84, v85\n"
    "v_cvt_pk_bf16_f32 v67, v86, v87\n"
    "v_mfma_f32_16x16x32_bf16 v[184:187], a[240:243], a[92:95], v[184:187]\n"
    "ds_write_b128 v246, v[64:67] offset:16384\n"
    "v_mfma_f32_16x16x32_bf16 v[188:191], v[4:7], a[92:95], v[188:191]\n"
    "v_cvt_pk_bf16_f32 v104, v104, v105\n"
    "v_cvt_pk_bf16_f32 v105, v106, v107\n"
    "v_mfma_f32_16x16x32_bf16 v[192:195], v[24:27], a[92:95], v[192:195]\n"
    "s_waitcnt lgkmcnt(4)\n"
    "v_cvt_pk_bf16_f32 v106, v124, v125\n"
    "v_cvt_pk_bf16_f32 v107, v126, v127\n"
    "v_mfma_f32_16x16x32_bf16 v[196:199], v[234:237], a[56:59], v[196:199]\n"
    "ds_write_b128 v246, v[104:107] offset:17408\n"
    "v_mfma_f32_16x16x32_bf16 v[200:203], v[232:235], a[56:59], v[200:203]\n"
    "v_mfma_f32_16x16x32_bf16 v[204:207], v[234:237], a[60:63], v[204:207]\n"
    "s_waitcnt lgkmcnt(0)\n"
    "v_mfma_f32_16x16x32_bf16 v[208:211], v[232:235], a[60:63], v[208:211]\n"
    "s_barrier\n"
    "v_mfma_f32_16x16x32_bf16 v[172:175], v[234:237], a[160:163], v[172:175]\n"
    "ds_read_b128 a[96:99], v250 offset:16384\n"
    "v_mfma_f32_16x16x32_bf16 v[176:179], v[232:235], a[160:163], v[176:179]\n"
    "ds_read_b128 a[100:103], v250 offset:17408\n"
    "v_mfma_f32_16x16x32_bf16 v[212:215], v[234:237], a[164:167], v[212:215]\n"
    "ds_read_b128 a[104:107], v250 offset:18432\n"
    "ds_read_b128 a[108:111], v250 offset:19456\n"
    "v_fma_f32 v180, v180, v80, v196\n"
    "v_mfma_f32_16x16x32_bf16 v[216:219], v[232:235], a[164:167], v[216:219]\n"
    "s_waitcnt lgkmcnt(2)\n"
    "ds_read_b128 a[112:115], v250 offset:20480\n"
    "ds_read_b128 a[116:119], v250 offset:21504\n"
    "v_mfma_f32_16x16x32_bf16 v[0:3], v[28:31], a[96:99], 0\n"
    "ds_read_b128 a[120:123], v250 offset:22528\n"
    "ds_read_b128 a[124:127], v250 offset:23552\n"
    "v_mfma_f32_16x16x32_bf16 v[20:23], v[48:51], a[96:99], 0\n"
    "ds_read2_b32 v[220:221], v245 offset0:128 offset1:144\n"
    "ds_read2_b32 v[222:223], v245 offset0:160 offset1:176\n"
    "v_mfma_f32_16x16x32_bf16 v[0:3], v[36:39], a[100:103], v[0:3]\n"
    "v_fma_f32 v181, v181, v80, v197\n"
    "v_fma_f32 v182, v182, v80, v198\n"
    "v_mfma_f32_16x16x32_bf16 v[20:23], v[56:59], a[100:103], v[20:23]\n"
    "s_waitcnt lgkmcnt(6)\n"
    "v_fma_f32 v183, v183, v80, v199\n"
    "v_fma_f32 v184, v184, v80, v200\n"
    "v_mfma_f32_16x16x32_bf16 v[0:3], v[72:75], a[104:107], v[0:3]\n"
    "s_or_b32 s45, s34, s93\n"
    "s_cselect_b32 s45, 2.0, 6101760\n"
    "v_fma_f32 v185, v185, v80, v201\n"
    "v_mfma_f32_16x16x32_bf16 v[20:23], v[92:95], a[104:107], v[20:23]\n"
    "v_fma_f32 v186, v186, v80, v202\n"
    "v_fma_f32 v187, v187, v80, v203\n"
    "v_mfma_f32_16x16x32_bf16 v[0:3], v[108:111], a[108:111], v[0:3]\n"
    "v_cvt_pk_bf16_f32 v180, v180, v181\n"
    "v_cvt_pk_bf16_f32 v181, v182, v183\n"
    "v_mfma_f32_16x16x32_bf16 v[20:23], v[112:115], a[108:111], v[20:23]\n"
    "s_waitcnt lgkmcnt(4)\n"
    "v_cvt_pk_bf16_f32 v182, v184, v185\n"
    "v_cvt_pk_bf16_f32 v183, v186, v187\n"
    "v_mfma_f32_16x16x32_bf16 v[0:3], v[136:139], a[112:115], v[0:3]\n"
    "buffer_store_dwordx4 v[180:183], v239, s[16:19], s82 offen offset:0 sc0 sc1\n"
    "v_mfma_f32_16x16x32_bf16 v[20:23], a[0:3], a[112:115], v[20:23]\n"
    "v_fma_f32 v188, v188, v80, v204\n"
    "v_fma_f32 v189, v189, v80, v205\n"
    "v_mfma_f32_16x16x32_bf16 v[0:3], a[12:15], a[116:119], v[0:3]\n"
    "v_fma_f32 v190, v190, v80, v206\n"
    "v_fma_f32 v191, v191, v80, v207\n"
    "v_mfma_f32_16x16x32_bf16 v[20:23], a[16:19], a[116:119], v[20:23]\n"
    "s_waitcnt lgkmcnt(0)\n"
    "v_fma_f32 v192, v192, v80, v208\n"
    "v_fma_f32 v193, v193, v80, v209\n"
    "v_mfma_f32_16x16x32_bf16 v[0:3], a[28:31], a[120:123], v[0:3]\n"
    "v_fma_f32 v194, v194, v80, v210\n"
    "v_fma_f32 v195, v195, v80, v211\n"
    "v_mfma_f32_16x16x32_bf16 v[20:23], a[32:35], a[120:123], v[20:23]\n"
    "v_cvt_pk_bf16_f32 v188, v188, v189\n"
    "v_cvt_pk_bf16_f32 v189, v190, v191\n"
    "v_mfma_f32_16x16x32_bf16 v[0:3], a[44:47], a[124:127], v[0:3]\n"
    "v_cvt_pk_bf16_f32 v190, v192, v193\n"
    "v_cvt_pk_bf16_f32 v191, v194, v195\n"
    "v_mfma_f32_16x16x32_bf16 v[20:23], a[240:243], a[124:127], v[20:23]\n"
    "buffer_store_dwordx4 v[188:191], v239, s[16:19], s82 offen offset:64 sc0 sc1\n"
    "v_mfma_f32_16x16x32_bf16 v[40:43], v[32:35], a[96:99], 0\n"
    "v_add_f32_e32 v220, v220, v221\n"
    "v_add_f32_e32 v222, v222, v223\n"
    "v_mfma_f32_16x16x32_bf16 v[60:63], v[52:55], a[96:99], 0\n"
    "v_add_f32_e32 v220, v220, v222\n"
    "v_rcp_f32_e32 v220, v220\n"
    "v_mfma_f32_16x16x32_bf16 v[40:43], v[68:71], a[100:103], v[40:43]\n"
    "v_fma_f32 v0, v0, v220, v172\n"
    "v_fma_f32 v1, v1, v220, v173\n"
    "v_mfma_f32_16x16x32_bf16 v[60:63], v[88:91], a[100:103], v[60:63]\n"
    "v_fma_f32 v2, v2, v220, v174\n"
    "v_fma_f32 v3, v3, v220, v175\n"
    "v_mfma_f32_16x16x32_bf16 v[40:43], v[76:79], a[104:107], v[40:43]\n"
    "v_fma_f32 v20, v20, v220, v176\n"
    "v_fma_f32 v21, v21, v220, v177\n"
    "v_mfma_f32_16x16x32_bf16 v[60:63], v[96:99], a[104:107], v[60:63]\n"
    "v_fma_f32 v22, v22, v220, v178\n"
    "v_mfma_f32_16x16x32_bf16 v[40:43], v[132:135], a[108:111], v[40:43]\n"
    "v_mfma_f32_16x16x32_bf16 v[60:63], v[116:119], a[108:111], v[60:63]\n"
    "v_fma_f32 v23, v23, v220, v179\n"
    "v_mfma_f32_16x16x32_bf16 v[40:43], a[4:7], a[112:115], v[40:43]\n"
    "v_cvt_pk_bf16_f32 v0, v0, v1\n"
    "v_cvt_pk_bf16_f32 v1, v2, v3\n"
    "v_mfma_f32_16x16x32_bf16 v[60:63], a[8:11], a[112:115], v[60:63]\n"
    "v_cvt_pk_bf16_f32 v2, v20, v21\n"
    "v_cvt_pk_bf16_f32 v3, v22, v23\n"
    "v_mfma_f32_16x16x32_bf16 v[40:43], a[20:23], a[116:119], v[40:43]\n"
    "v_mfma_f32_16x16x32_bf16 v[60:63], a[24:27], a[116:119], v[60:63]\n"
    "buffer_store_dwordx4 v[0:3], v239, s[16:19], s28 offen offset:0 sc0 sc1\n"
    "v_mfma_f32_16x16x32_bf16 v[40:43], a[36:39], a[120:123], v[40:43]\n"
    "v_mfma_f32_16x16x32_bf16 v[60:63], a[40:43], a[120:123], v[60:63]\n"
    "v_mfma_f32_16x16x32_bf16 v[40:43], v[4:7], a[124:127], v[40:43]\n"
    "v_mfma_f32_16x16x32_bf16 v[60:63], v[24:27], a[124:127], v[60:63]\n"
    "v_mov_b32_e32 v254, s39\n"
    "s_nop 2\n"
    "v_fma_f32 v40, v40, v220, v212\n"
    "v_fma_f32 v41, v41, v220, v213\n"
    "v_fma_f32 v42, v42, v220, v214\n"
    "v_fma_f32 v43, v43, v220, v215\n"
    "v_fma_f32 v60, v60, v220, v216\n"
    "v_fma_f32 v61, v61, v220, v217\n"
    "v_fma_f32 v62, v62, v220, v218\n"
    "v_fma_f32 v63, v63, v220, v219\n"
    "v_cvt_pk_bf16_f32 v40, v40, v41\n"
    "v_cvt_pk_bf16_f32 v41, v42, v43\n"
    "v_cvt_pk_bf16_f32 v42, v60, v61\n"
    "v_cvt_pk_bf16_f32 v43, v62, v63\n"
    "buffer_store_dwordx4 v[40:43], v239, s[16:19], s28 offen offset:64 sc0 sc1\n"
    "buffer_store_dword v254, off, s[8:11], s45 offset:0 sc1\n"
    "s_endpgm\n"
    ".Lat_g1:\n"
    "s_cmp_eq_u32 s85, 0\n"
    "s_cbranch_scc1 .Lat_loop\n"
    "s_cmp_eq_u32 s81, s39\n"
    "s_cbranch_scc1 .Lat_g1b\n"
    "s_mov_b32 s53, 0x400000\n"
    ".Lat_lp:\n"
    "s_sleep 1\n"
    "buffer_load_dword v253, off, s[8:11], s82 sc1\n"
    "s_waitcnt vmcnt(0)\n"
    "v_readfirstlane_b32 s81, v253\n"
    "s_sub_u32 s53, s53, 1\n"
    "s_cbranch_scc1 .Lat_lpx\n"
    "s_nop 3\n"
    "s_cmp_lg_u32 s81, s39\n"
    "s_cbranch_scc1 .Lat_lp\n"
    ".Lat_lpx:\n"
    "s_mov_b32 m0, s30\n"
    "s_nop 0\n"
    "buffer_load_dwordx4 v250, s[8:11], s83 offen offset:0 sc1 lds\n"
    "s_mov_b32 m0, s42\n"
    ".Lat_g1b:\n"
    "v_lshrrev_b32_e32 v228, 2, v250\n"
    "v_add_u32_e32 v254, s30, v228\n"
    "s_waitcnt vmcnt(0)\n"
    "ds_read_b32 v254, v254 offset:960\n"
    "v_lshrrev_b32_e32 v228, 9, v250\n"
    "v_lshl_add_u32 v251, v228, 2, s30\n"
    "v_and_b32_e32 v252, 0xf0, v250\n"
    "v_lshl_add_u32 v252, v228, 8, v252\n"
    "v_bfe_u32 v229, v250, 8, 1\n"
    "v_lshl_add_u32 v252, v229, 3, v252\n"
    "v_and_b32_e32 v239, 0x1f0, v250\n"
    "v_bfe_u32 v228, v250, 2, 4\n"
    "s_mov_b32 s88, 0xcedc\n"
    "v_bfe_u32 v238, s88, v228, 4\n"
    "v_mul_u32_u24_e32 v238, 0x1111, v238\n"
    "v_mov_b64_e32 v[224:225], 0\n"
    "v_mov_b64_e32 v[226:227], 0\n"
    "s_mov_b32 s85, 0\n"
    "s_mov_b32 s82, 2.0\n"
    "s_mov_b32 s83, 2.0\n"
    "s_mov_b32 s89, 4\n"
    "s_waitcnt lgkmcnt(0)\n"
    "v_readlane_b32 s88, v254, 0\n"
    "v_readlane_b32 s87, v254, 1\n"
    "v_readlane_b32 s84, v254, 2\n"
    "v_readlane_b32 s86, v254, 4\n"
    "s_nop 3\n"
    "s_min_u32 s88, s88, s44\n"
    "s_sub_u32 s80, s44, s88\n"
    "s_cmp_eq_u32 s88, 0\n"
    "s_cbranch_scc1 .Lat_g2\n"
    "s_branch .Lat_gloop\n"
    ".p2align 6\n"
    ".Lat_gloop:\n"
    "v_mfma_f32_16x16x32_bf16 v[212:215], a[160:163], a[128:131], v[220:223]\n"
    "ds_write_addtid_b32 v143 offset:768\n"
    "v_mfma_f32_16x16x32_bf16 v[212:215], a[164:167], a[132:135], v[212:215]\n"
    "v_exp_f32_e32 v170, v170\n"
    "v_mfma_f32_16x16x32_bf16 v[204:207], a[164:167], a[100:103], v[220:223]\n"
    "ds_write_addtid_b32 v150 offset:1536\n"
    "v_mfma_f32_16x16x32_bf16 v[204:207], a[160:163], a[96:99], v[204:207]\n"
    "ds_read_b128 a[184:187], v242 offset:28672\n"
    "v_mfma_f32_16x16x32_bf16 v[196:199], a[160:163], a[64:67], v[220:223]\n"
    "v_exp_f32_e32 v171, v171\n"
    "v_mfma_f32_16x16x32_bf16 v[196:199], a[164:167], a[68:71], v[196:199]\n"
    "ds_write_addtid_b32 v151 offset:1792\n"
    "v_cvt_pk_bf16_f32 v166, v168, v169\n"
    "v_mfma_f32_16x16x32_bf16 v[188:191], a[164:167], a[36:39], v[220:223]\n"
    "v_exp_f32_e32 v176, v176\n"
    "v_mfma_f32_16x16x32_bf16 v[188:191], a[160:163], a[32:35], v[188:191]\n"
    "v_exp_f32_e32 v177, v177\n"
    "v_mfma_f32_16x16x32_bf16 v[180:183], a[160:163], a[0:3], v[220:223]\n"
    "ds_write_addtid_b32 v158 offset:2560\n"
    "v_cvt_pk_bf16_f32 v167, v170, v171\n"
    "v_mfma_f32_16x16x32_bf16 v[180:183], a[164:167], a[4:7], v[180:183]\n"
    "s_waitcnt vmcnt(20)\n"
    "v_exp_f32_e32 v178, v178\n"
    "v_smfmac_f32_16x16x64_bf16 v[224:227], a[232:235], a[192:199], v238\n"
    "s_waitcnt lgkmcnt(10)\n"
    "ds_read_b128 a[188:191], v243 offset:28672\n"
    "v_mfma_f32_16x16x32_bf16 v[180:183], a[168:171], a[8:11], v[180:183]\n"
    "s_bitcmp1_b32 s86, 0\n"
    "ds_read_b128 a[160:163], v240 offset:26624\n"
    "v_mfma_f32_16x16x32_bf16 v[180:183], a[172:175], a[12:15], v[180:183]\n"
    "s_cselect_b64 exec, -1, 0\n"
    "v_cvt_pk_bf16_f32 v236, v224, v225\n"
    "v_cvt_pk_bf16_f32 v237, v226, v227\n"
    "v_mfma_f32_16x16x32_bf16 v[188:191], a[172:175], a[44:47], v[188:191]\n"
    "s_cselect_b32 s88, 512, 0\n"
    "v_mov_b64_e32 v[224:225], 0\n"
    "v_mov_b64_e32 v[226:227], 0\n"
    "v_mfma_f32_16x16x32_bf16 v[188:191], a[168:171], a[40:43], v[188:191]\n"
    "buffer_store_dwordx2 v[236:237], v252, s[8:11], s87 offen sc1\n"
    "v_mfma_f32_16x16x32_bf16 v[196:199], a[168:171], a[72:75], v[196:199]\n"
    "s_mov_b64 exec, -1\n"
    "ds_read2_b32 v[228:229], v251 offset0:0 offset1:2\n"
    "v_mfma_f32_16x16x32_bf16 v[196:199], a[172:175], a[76:79], v[196:199]\n"
    "s_add_u32 s87, s87, s88\n"
    "ds_read2_b32 v[230:231], v251 offset0:4 offset1:6\n"
    "v_mfma_f32_16x16x32_bf16 v[204:207], a[172:175], a[108:111], v[204:207]\n"
    "ds_read_b32 v232, v251 offset:32\n"
    "v_mfma_f32_16x16x32_bf16 v[204:207], a[168:171], a[104:107], v[204:207]\n"
    "v_exp_f32_e32 v179, v179\n"
    "v_mfma_f32_16x16x32_bf16 v[212:215], a[168:171], a[136:139], v[212:215]\n"
    "ds_write_addtid_b32 v159 offset:2816\n"
    "v_cvt_pk_bf16_f32 v174, v176, v177\n"
    "v_mfma_f32_16x16x32_bf16 v[212:215], a[172:175], a[140:143], v[212:215]\n"
    "s_waitcnt lgkmcnt(13)\n"
    "ds_read_b128 a[164:167], v241 offset:26624\n"
    "v_mfma_f32_16x16x32_bf16 v[212:215], a[176:179], a[144:147], v[212:215]\n"
    "ds_write_addtid_b32 v166 offset:3584\n"
    "v_cvt_pk_bf16_f32 v175, v178, v179\n"
    "v_mfma_f32_16x16x32_bf16 v[212:215], a[180:183], a[148:151], v[212:215]\n"
    "ds_read_b128 a[168:171], v240 offset:30720\n"
    "v_mfma_f32_16x16x32_bf16 v[204:207], a[180:183], a[116:119], v[204:207]\n"
    "s_waitcnt lgkmcnt(6)\n"
    "ds_write_addtid_b32 v167 offset:3840\n"
    "v_mad_u32_u16 v234, v228, s90, v239 op_sel:[0,0,0,0]\n"
    "v_mfma_f32_16x16x32_bf16 v[204:207], a[176:179], a[112:115], v[204:207]\n"
    "buffer_load_dwordx4 a[192:195], v234, s[20:23], 0 offen sc1\n"
    "v_mad_u32_u16 v235, v228, s90, v239 op_sel:[1,0,0,0]\n"
    "v_mfma_f32_16x16x32_bf16 v[196:199], a[176:179], a[80:83], v[196:199]\n"
    "ds_write_addtid_b32 v174 offset:4608\n"
    "v_mfma_f32_16x16x32_bf16 v[196:199], a[180:183], a[84:87], v[196:199]\n"
    "ds_write_addtid_b32 v175 offset:4864\n"
    "v_mfma_f32_16x16x32_bf16 v[188:191], a[180:183], a[52:55], v[188:191]\n"
    "buffer_load_dwordx4 a[196:199], v235, s[20:23], 0 offen sc1\n"
    "v_mfma_f32_16x16x32_bf16 v[188:191], a[176:179], a[48:51], v[188:191]\n"
    "v_mfma_f32_16x16x32_bf16 v[180:183], a[176:179], a[16:19], v[180:183]\n"
    "s_waitcnt lgkmcnt(0)\n"
    "v_mfma_f32_16x16x32_bf16 v[180:183], a[180:183], a[20:23], v[180:183]\n"
    "s_barrier\n"
    "v_mfma_f32_16x16x32_bf16 v[180:183], a[184:187], a[24:27], v[180:183]\n"
    "s_add_u32 m0, s43, 8192\n"
    "ds_read_b128 a[172:175], v241 offset:30720\n"
    "v_mfma_f32_16x16x32_bf16 v[180:183], a[188:191], a[28:31], v[180:183]\n"
    "ds_read2st64_b32 v[144:145], v249 offset0:0 offset1:1\n"
    "v_mfma_f32_16x16x32_bf16 v[188:191], a[188:191], a[60:63], v[188:191]\n"
    "buffer_load_dwordx4 v248, s[8:11], s40 offen offset:0 sc1 lds\n"
    "v_mfma_f32_16x16x32_bf16 v[188:191], a[184:187], a[56:59], v[188:191]\n"
    "ds_read_b128 a[176:179], v242 offset:26624\n"
    "v_mfma_f32_16x16x32_bf16 v[196:199], a[184:187], a[88:91], v[196:199]\n"
    "v_mfma_f32_16x16x32_bf16 v[196:199], a[188:191], a[92:95], v[196:199]\n"
    "v_mfma_f32_16x16x32_bf16 v[204:207], a[188:191], a[124:127], v[204:207]\n"
    "buffer_load_dwordx4 v248, s[8:11], s40 offen offset:1024 sc1 lds\n"
    "v_mfma_f32_16x16x32_bf16 v[204:207], a[184:187], a[120:123], v[204:207]\n"
    "ds_read2st64_b32 v[146:147], v249 offset0:2 offset1:3\n"
    "v_mfma_f32_16x16x32_bf16 v[212:215], a[184:187], a[152:155], v[212:215]\n"
    "v_mfma_f32_16x16x32_bf16 v[212:215], a[188:191], a[156:159], v[212:215]\n"
    "ds_read_b128 a[180:183], v243 offset:26624\n"
    "v_mfma_f32_16x16x32_bf16 v[216:219], a[160:163], a[128:131], v[220:223]\n"
    "buffer_load_dwordx4 v248, s[8:11], s40 offen offset:2048 sc1 lds\n"
    "v_mfma_f32_16x16x32_bf16 v[216:219], a[164:167], a[132:135], v[216:219]\n"
    "ds_read2st64_b32 v[152:153], v249 offset0:4 offset1:5\n"
    "v_mfma_f32_16x16x32_bf16 v[208:211], a[164:167], a[100:103], v[220:223]\n"
    "v_exp_f32_e32 v180, v180\n"
    "v_mfma_f32_16x16x32_bf16 v[208:211], a[160:163], a[96:99], v[208:211]\n"
    "ds_read_b128 a[184:187], v242 offset:30720\n"
    "v_mfma_f32_16x16x32_bf16 v[200:203], a[160:163], a[64:67], v[220:223]\n"
    "buffer_load_dwordx4 v248, s[8:11], s40 offen offset:3072 sc1 lds\n"
    "v_mfma_f32_16x16x32_bf16 v[200:203], a[164:167], a[68:71], v[200:203]\n"
    "s_add_u32 m0, s43, 12288\n"
    "ds_read2st64_b32 v[154:155], v249 offset0:6 offset1:7\n"
    "v_mfma_f32_16x16x32_bf16 v[192:195], a[164:167], a[36:39], v[220:223]\n"
    "s_add_u32 s40, s40, 0x8000\n"
    "ds_read2st64_b32 v[160:161], v249 offset0:8 offset1:9\n"
    "v_mfma_f32_16x16x32_bf16 v[192:195], a[160:163], a[32:35], v[192:195]\n"
    "ds_read2st64_b32 v[162:163], v249 offset0:10 offset1:11\n"
    "v_mfma_f32_16x16x32_bf16 v[184:187], a[160:163], a[0:3], v[220:223]\n"
    "buffer_load_dwordx4 v248, s[8:11], s41 offen offset:0 sc1 lds\n"
    "v_mfma_f32_16x16x32_bf16 v[184:187], a[164:167], a[4:7], v[184:187]\n"
    "s_waitcnt vmcnt(17)\n"
    "ds_read_b128 a[188:191], v243 offset:30720\n"
    "v_smfmac_f32_16x16x64_bf16 v[224:227], a[232:235], a[200:207], v238\n"
    "s_waitcnt lgkmcnt(10)\n"
    "v_mad_u32_u16 v234, v229, s90, v239 op_sel:[0,0,0,0]\n"
    "v_mfma_f32_16x16x32_bf16 v[184:187], a[168:171], a[8:11], v[184:187]\n"
    "s_bitcmp1_b32 s86, 1\n"
    "buffer_load_dwordx4 a[200:203], v234, s[20:23], 0 offen sc1\n"
    "v_mad_u32_u16 v235, v229, s90, v239 op_sel:[1,0,0,0]\n"
    "v_mfma_f32_16x16x32_bf16 v[184:187], a[172:175], a[12:15], v[184:187]\n"
    "s_cselect_b64 exec, -1, 0\n"
    "v_cvt_pk_bf16_f32 v236, v224, v225\n"
    "v_cvt_pk_bf16_f32 v237, v226, v227\n"
    "v_mfma_f32_16x16x32_bf16 v[192:195], a[172:175], a[44:47], v[192:195]\n"
    "s_cselect_b32 s88, 512, 0\n"
    "v_mov_b64_e32 v[224:225], 0\n"
    "v_mov_b64_e32 v[226:227], 0\n"
    "v_mfma_f32_16x16x32_bf16 v[192:195], a[168:171], a[40:43], v[192:195]\n"
    "buffer_store_dwordx2 v[236:237], v252, s[8:11], s87 offen sc1\n"
    "v_mfma_f32_16x16x32_bf16 v[200:203], a[168:171], a[72:75], v[200:203]\n"
    "s_mov_b64 exec, -1\n"
    "ds_read_b64_tr_b16 a[160:161], v244 offset:16384\n"
    "v_mfma_f32_16x16x32_bf16 v[200:203], a[172:175], a[76:79], v[200:203]\n"
    "s_add_u32 s87, s87, s88\n"
    "ds_read_b64_tr_b16 a[162:163], v244 offset:18432\n"
    "v_mfma_f32_16x16x32_bf16 v[208:211], a[172:175], a[108:111], v[208:211]\n"
    "buffer_load_dwordx4 v248, s[8:11], s41 offen offset:1024 sc1 lds\n"
    "v_mfma_f32_16x16x32_bf16 v[208:211], a[168:171], a[104:107], v[208:211]\n"
    "ds_read_b64_tr_b16 a[164:165], v244 offset:49152\n"
    "v_mfma_f32_16x16x32_bf16 v[216:219], a[168:171], a[136:139], v[216:219]\n"
    "ds_read2st64_b32 v[168:169], v249 offset0:12 offset1:13\n"
    "v_mfma_f32_16x16x32_bf16 v[216:219], a[172:175], a[140:143], v[216:219]\n"
    "s_waitcnt lgkmcnt(10)\n"
    "ds_read2st64_b32 v[170:171], v249 offset0:14 offset1:15\n"
    "v_mfma_f32_16x16x32_bf16 v[216:219], a[176:179], a[144:147], v[216:219]\n"
    "buffer_load_dwordx4 v248, s[8:11], s41 offen offset:2048 sc1 lds\n"
    "v_mfma_f32_16x16x32_bf16 v[216:219], a[180:183], a[148:151], v[216:219]\n"
    "ds_read_b64_tr_b16 a[166:167], v244 offset:51200\n"
    "v_mfma_f32_16x16x32_bf16 v[208:211], a[180:183], a[116:119], v[208:211]\n"
    "ds_read_b64_tr_b16 a[168:169], v245 offset:16384\n"
    "v_mfma_f32_16x16x32_bf16 v[208:211], a[176:179], a[112:115], v[208:211]\n"
    "v_exp_f32_e32 v181, v181\n"
    "v_mfma_f32_16x16x32_bf16 v[200:203], a[176:179], a[80:83], v[200:203]\n"
    "buffer_load_dwordx4 v248, s[8:11], s41 offen offset:3072 sc1 lds\n"
    "v_mfma_f32_16x16x32_bf16 v[200:203], a[180:183], a[84:87], v[200:203]\n"
    "ds_read_b64_tr_b16 a[170:171], v245 offset:18432\n"
    "v_mfma_f32_16x16x32_bf16 v[192:195], a[180:183], a[52:55], v[192:195]\n"
    "s_add_u32 s41, s41, 0x8000\n"
    "ds_read2st64_b32 v[176:177], v249 offset0:16 offset1:17\n"
    "v_mfma_f32_16x16x32_bf16 v[192:195], a[176:179], a[48:51], v[192:195]\n"
    "buffer_load_dwordx4 a[204:207], v235, s[20:23], 0 offen sc1\n"
    "v_mfma_f32_16x16x32_bf16 v[184:187], a[176:179], a[16:19], v[184:187]\n"
    "ds_read_b64_tr_b16 a[172:173], v245 offset:49152\n"
    "v_mfma_f32_16x16x32_bf16 v[184:187], a[180:183], a[20:23], v[184:187]\n"
    "s_waitcnt lgkmcnt(10)\n"
    "ds_read2st64_b32 v[178:179], v249 offset0:18 offset1:19\n"
    "v_mfma_f32_16x16x32_bf16 v[184:187], a[184:187], a[24:27], v[184:187]\n"
    "ds_read_b64_tr_b16 a[176:177], v246 offset:16384\n"
    "v_mfma_f32_16x16x32_bf16 v[184:187], a[188:191], a[28:31], v[184:187]\n"
    "ds_read_b64_tr_b16 a[174:175], v245 offset:51200\n"
    "v_mfma_f32_16x16x32_bf16 v[192:195], a[188:191], a[60:63], v[192:195]\n"
    "v_exp_f32_e32 v182, v182\n"
    "v_mfma_f32_16x16x32_bf16 v[192:195], a[184:187], a[56:59], v[192:195]\n"
    "v_exp_f32_e32 v183, v183\n"
    "v_mfma_f32_16x16x32_bf16 v[200:203], a[184:187], a[88:91], v[200:203]\n"
    "ds_read_b64_tr_b16 a[178:179], v246 offset:18432\n"
    "v_cvt_pk_bf16_f32 v180, v180, v181\n"
    "v_mfma_f32_16x16x32_bf16 v[200:203], a[188:191], a[92:95], v[200:203]\n"
    "v_exp_f32_e32 v188, v188\n"
    "v_mfma_f32_16x16x32_bf16 v[208:211], a[188:191], a[124:127], v[208:211]\n"
    "v_exp_f32_e32 v189, v189\n"
    "v_mfma_f32_16x16x32_bf16 v[208:211], a[184:187], a[120:123], v[208:211]\n"
    "v_cvt_pk_bf16_f32 v181, v182, v183\n"
    "v_mfma_f32_16x16x32_bf16 v[216:219], a[184:187], a[152:155], v[216:219]\n"
    "ds_read_b64_tr_b16 a[180:181], v246 offset:49152\n"
    "v_mfma_f32_16x16x32_bf16 v[216:219], a[188:191], a[156:159], v[216:219]\n"
    "s_waitcnt vmcnt(20)\n"
    "ds_read_b64_tr_b16 a[182:183], v246 offset:51200\n"
    "v_smfmac_f32_16x16x64_bf16 v[224:227], a[232:235], a[208:215], v238\n"
    "s_waitcnt lgkmcnt(5)\n"
    "v_mad_u32_u16 v234, v230, s90, v239 op_sel:[0,0,0,0]\n"
    "v_mfma_f32_16x16x32_bf16 a[236:239], a[160:163], v[140:143], a[236:239]\n"
    "s_bitcmp1_b32 s86, 2\n"
    "buffer_load_dwordx4 a[208:211], v234, s[20:23], 0 offen sc1\n"
    "v_mad_u32_u16 v235, v230, s90, v239 op_sel:[1,0,0,0]\n"
    "v_mfma_f32_16x16x32_bf16 a[236:239], a[164:167], v[144:147], a[236:239]\n"
    "s_cselect_b64 exec, -1, 0\n"
    "v_cvt_pk_bf16_f32 v236, v224, v225\n"
    "v_cvt_pk_bf16_f32 v237, v226, v227\n"
    "v_mfma_f32_16x16x32_bf16 a[240:243], a[164:167], v[152:155], a[240:243]\n"
    "s_cselect_b32 s88, 512, 0\n"
    "v_mov_b64_e32 v[224:225], 0\n"
    "v_mov_b64_e32 v[226:227], 0\n"
    "v_mfma_f32_16x16x32_bf16 a[240:243], a[160:163], v[148:151], a[240:243]\n"
    "buffer_store_dwordx2 v[236:237], v252, s[8:11], s87 offen sc1\n"
    "v_mfma_f32_16x16x32_bf16 a[244:247], a[160:163], v[156:159], a[244:247]\n"
    "s_mov_b64 exec, -1\n"
    "ds_read_b64_tr_b16 a[184:185], v247 offset:16384\n"
    "v_mfma_f32_16x16x32_bf16 a[244:247], a[164:167], v[160:163], a[244:247]\n"
    "s_add_u32 s87, s87, s88\n"
    "ds_read_b64_tr_b16 a[186:187], v247 offset:18432\n"
    "v_mfma_f32_16x16x32_bf16 a[248:251], a[164:167], v[168:171], a[248:251]\n"
    "buffer_load_dwordx4 a[212:215], v235, s[20:23], 0 offen sc1\n"
    "v_mfma_f32_16x16x32_bf16 a[248:251], a[160:163], v[164:167], a[248:251]\n"
    "ds_read_b64_tr_b16 a[188:189], v247 offset:49152\n"
    "v_mfma_f32_16x16x32_bf16 a[252:255], a[160:163], v[172:175], a[252:255]\n"
    "v_exp_f32_e32 v190, v190\n"
    "v_mfma_f32_16x16x32_bf16 a[252:255], a[164:167], v[176:179], a[252:255]\n"
    "s_waitcnt lgkmcnt(6)\n"
    "v_exp_f32_e32 v191, v191\n"
    "v_mfma_f32_16x16x32_bf16 v[16:19], a[172:175], v[176:179], v[16:19]\n"
    "ds_read_b64_tr_b16 a[190:191], v247 offset:51200\n"
    "v_cvt_pk_bf16_f32 v188, v188, v189\n"
    "v_mfma_f32_16x16x32_bf16 v[16:19], a[168:171], v[172:175], v[16:19]\n"
    "ds_read_b64_tr_b16 a[160:161], v244 offset:20480\n"
    "v_mfma_f32_16x16x32_bf16 v[12:15], a[168:171], v[164:167], v[12:15]\n"
    "v_exp_f32_e32 v196, v196\n"
    "v_mfma_f32_16x16x32_bf16 v[12:15], a[172:175], v[168:171], v[12:15]\n"
    "v_exp_f32_e32 v197, v197\n"
    "v_mfma_f32_16x16x32_bf16 v[8:11], a[172:175], v[160:163], v[8:11]\n"
    "ds_read_b64_tr_b16 a[162:163], v244 offset:22528\n"
    "v_cvt_pk_bf16_f32 v189, v190, v191\n"
    "v_mfma_f32_16x16x32_bf16 v[8:11], a[168:171], v[156:159], v[8:11]\n"
    "v_exp_f32_e32 v198, v198\n"
    "v_mfma_f32_16x16x32_bf16 v[4:7], a[168:171], v[148:151], v[4:7]\n"
    "v_exp_f32_e32 v199, v199\n"
    "v_mfma_f32_16x16x32_bf16 v[4:7], a[172:175], v[152:155], v[4:7]\n"
    "v_cvt_pk_bf16_f32 v196, v196, v197\n"
    "v_mfma_f32_16x16x32_bf16 v[0:3], a[172:175], v[144:147], v[0:3]\n"
    "ds_read_b64_tr_b16 a[164:165], v244 offset:53248\n"
    "v_mfma_f32_16x16x32_bf16 v[0:3], a[168:171], v[140:143], v[0:3]\n"
    "s_waitcnt lgkmcnt(7)\n"
    "v_exp_f32_e32 v204, v204\n"
    "v_mfma_f32_16x16x32_bf16 v[20:23], a[176:179], v[140:143], v[20:23]\n"
    "ds_read_b64_tr_b16 a[168:169], v245 offset:20480\n"
    "v_mfma_f32_16x16x32_bf16 v[20:23], a[180:183], v[144:147], v[20:23]\n"
    "ds_read_b64_tr_b16 a[166:167], v244 offset:55296\n"
    "v_mfma_f32_16x16x32_bf16 v[24:27], a[180:183], v[152:155], v[24:27]\n"
    "v_exp_f32_e32 v205, v205\n"
    "v_mfma_f32_16x16x32_bf16 v[24:27], a[176:179], v[148:151], v[24:27]\n"
    "v_cvt_pk_bf16_f32 v197, v198, v199\n"
    "v_mfma_f32_16x16x32_bf16 v[28:31], a[176:179], v[156:159], v[28:31]\n"
    "ds_read_b64_tr_b16 a[170:171], v245 offset:22528\n"
    "v_mfma_f32_16x16x32_bf16 v[28:31], a[180:183], v[160:163], v[28:31]\n"
    "v_exp_f32_e32 v206, v206\n"
    "v_mfma_f32_16x16x32_bf16 v[32:35], a[180:183], v[168:171], v[32:35]\n"
    "v_exp_f32_e32 v207, v207\n"
    "v_mfma_f32_16x16x32_bf16 v[32:35], a[176:179], v[164:167], v[32:35]\n"
    "v_cvt_pk_bf16_f32 v204, v204, v205\n"
    "v_mfma_f32_16x16x32_bf16 v[36:39], a[176:179], v[172:175], v[36:39]\n"
    "ds_read_b64_tr_b16 a[172:173], v245 offset:53248\n"
    "v_mfma_f32_16x16x32_bf16 v[36:39], a[180:183], v[176:179], v[36:39]\n"
    "s_waitcnt vmcnt(20)\n"
    "ds_read_b64_tr_b16 a[174:175], v245 offset:55296\n"
    "v_smfmac_f32_16x16x64_bf16 v[224:227], a[232:235], a[216:223], v238\n"
    "s_waitcnt lgkmcnt(8)\n"
    "v_mad_u32_u16 v234, v231, s90, v239 op_sel:[0,0,0,0]\n"
    "v_mfma_f32_16x16x32_bf16 v[56:59], a[188:191], v[176:179], v[56:59]\n"
    "s_bitcmp1_b32 s86, 3\n"
    "buffer_load_dwordx4 a[216:219], v234, s[20:23], 0 offen sc1\n"
    "v_mad_u32_u16 v235, v231, s90, v239 op_sel:[1,0,0,0]\n"
    "v_mfma_f32_16x16x32_bf16 v[56:59], a[184:187], v[172:175], v[56:59]\n"
    "s_cselect_b64 exec, -1, 0\n"
    "v_cvt_pk_bf16_f32 v236, v224, v225\n"
    "v_cvt_pk_bf16_f32 v237, v226, v227\n"
    "v_mfma_f32_16x16x32_bf16 v[52:55], a[184:187], v[164:167], v[52:55]\n"
    "s_cselect_b32 s88, 512, 0\n"
    "v_mov_b64_e32 v[224:225], 0\n"
    "v_mov_b64_e32 v[226:227], 0\n"
    "v_mfma_f32_16x16x32_bf16 v[52:55], a[188:191], v[168:171], v[52:55]\n"
    "buffer_store_dwordx2 v[236:237], v252, s[8:11], s87 offen sc1\n"
    "v_mfma_f32_16x16x32_bf16 v[48:51], a[188:191], v[160:163], v[48:51]\n"
    "s_mov_b64 exec, -1\n"
    "ds_read_b64_tr_b16 a[176:177], v246 offset:20480\n"
    "v_mfma_f32_16x16x32_bf16 v[48:51], a[184:187], v[156:159], v[48:51]\n"
    "s_add_u32 s87, s87, s88\n"
    "ds_read_b64_tr_b16 a[178:179], v246 offset:22528\n"
    "v_mfma_f32_16x16x32_bf16 v[44:47], a[184:187], v[148:151], v[44:47]\n"
    "buffer_load_dwordx4 a[220:223], v235, s[20:23], 0 offen sc1\n"
    "v_mfma_f32_16x16x32_bf16 v[44:47], a[188:191], v[152:155], v[44:47]\n"
    "ds_read_b64_tr_b16 a[180:181], v246 offset:53248\n"
    "v_mfma_f32_16x16x32_bf16 v[40:43], a[188:191], v[144:147], v[40:43]\n"
    "v_exp_f32_e32 v212, v212\n"
    "v_mfma_f32_16x16x32_bf16 v[40:43], a[184:187], v[140:143], v[40:43]\n"
    "s_waitcnt lgkmcnt(6)\n"
    "v_exp_f32_e32 v213, v213\n"
    "v_mfma_f32_16x16x32_bf16 v[60:63], a[160:163], v[140:143], v[60:63]\n"
    "ds_read_b64_tr_b16 a[182:183], v246 offset:55296\n"
    "v_cvt_pk_bf16_f32 v205, v206, v207\n"
    "v_mfma_f32_16x16x32_bf16 v[60:63], a[164:167], v[144:147], v[60:63]\n"
    "ds_read_b64_tr_b16 a[184:185], v247 offset:20480\n"
    "v_mfma_f32_16x16x32_bf16 v[64:67], a[164:167], v[152:155], v[64:67]\n"
    "ds_read_b64_tr_b16 a[186:187], v247 offset:22528\n"
    "v_mfma_f32_16x16x32_bf16 v[64:67], a[160:163], v[148:151], v[64:67]\n"
    "v_exp_f32_e32 v214, v214\n"
    "v_mfma_f32_16x16x32_bf16 v[68:71], a[160:163], v[156:159], v[68:71]\n"
    "v_exp_f32_e32 v215, v215\n"
    "v_mfma_f32_16x16x32_bf16 v[68:71], a[164:167], v[160:163], v[68:71]\n"
    "ds_read_b64_tr_b16 a[188:189], v247 offset:53248\n"
    "v_cvt_pk_bf16_f32 v212, v212, v213\n"
    "v_mfma_f32_16x16x32_bf16 v[72:75], a[164:167], v[168:171], v[72:75]\n"
    "ds_read_b64_tr_b16 a[190:191], v247 offset:55296\n"
    "v_cvt_pk_bf16_f32 v213, v214, v215\n"
    "v_mfma_f32_16x16x32_bf16 v[72:75], a[160:163], v[164:167], v[72:75]\n"
    "s_waitcnt vmcnt(30)\n"
    "v_mfma_f32_16x16x32_bf16 v[76:79], a[160:163], v[172:175], v[76:79]\n"
    "s_barrier\n"
    "v_mfma_f32_16x16x32_bf16 v[76:79], a[164:167], v[176:179], v[76:79]\n"
    "s_waitcnt lgkmcnt(8)\n"
    "s_mov_b32 m0, s42\n"
    "v_mfma_f32_16x16x32_bf16 v[96:99], a[172:175], v[176:179], v[96:99]\n"
    "v_exp_f32_e32 v184, v184\n"
    "v_mfma_f32_16x16x32_bf16 v[96:99], a[168:171], v[172:175], v[96:99]\n"
    "v_exp_f32_e32 v185, v185\n"
    "v_mfma_f32_16x16x32_bf16 v[92:95], a[168:171], v[164:167], v[92:95]\n"
    "v_exp_f32_e32 v186, v186\n"
    "v_mfma_f32_16x16x32_bf16 v[92:95], a[172:175], v[168:171], v[92:95]\n"
    "ds_read_b128 a[160:163], v240 offset:0\n"
    "ds_write_addtid_b32 v180 offset:0\n"
    "v_mfma_f32_16x16x32_bf16 v[88:91], a[172:175], v[160:163], v[88:91]\n"
    "v_exp_f32_e32 v187, v187\n"
    "v_mfma_f32_16x16x32_bf16 v[88:91], a[168:171], v[156:159], v[88:91]\n"
    "ds_write_addtid_b32 v181 offset:256\n"
    "v_cvt_pk_bf16_f32 v182, v184, v185\n"
    "v_mfma_f32_16x16x32_bf16 v[84:87], a[168:171], v[148:151], v[84:87]\n"
    "v_exp_f32_e32 v192, v192\n"
    "v_mfma_f32_16x16x32_bf16 v[84:87], a[172:175], v[152:155], v[84:87]\n"
    "v_exp_f32_e32 v193, v193\n"
    "v_mfma_f32_16x16x32_bf16 v[80:83], a[172:175], v[144:147], v[80:83]\n"
    "ds_write_addtid_b32 v188 offset:1024\n"
    "v_cvt_pk_bf16_f32 v183, v186, v187\n"
    "v_mfma_f32_16x16x32_bf16 v[80:83], a[168:171], v[140:143], v[80:83]\n"
    "s_waitcnt vmcnt(20)\n"
    "ds_read_b128 a[164:167], v241 offset:0\n"
    "ds_write_addtid_b32 v189 offset:1280\n"
    "v_smfmac_f32_16x16x64_bf16 v[224:227], a[232:235], a[224:231], v238\n"
    "s_waitcnt lgkmcnt(10)\n"
    "ds_write_addtid_b32 v196 offset:2048\n"
    "v_mad_u32_u16 v234, v232, s90, v239 op_sel:[0,0,0,0]\n"
    "v_mfma_f32_16x16x32_bf16 v[100:103], a[176:179], v[140:143], v[100:103]\n"
    "s_bitcmp1_b32 s86, 4\n"
    "buffer_load_dwordx4 a[224:227], v234, s[20:23], 0 offen sc1\n"
    "v_mad_u32_u16 v235, v232, s90, v239 op_sel:[1,0,0,0]\n"
    "v_mfma_f32_16x16x32_bf16 v[100:103], a[180:183], v[144:147], v[100:103]\n"
    "s_cselect_b64 exec, -1, 0\n"
    "v_cvt_pk_bf16_f32 v236, v224, v225\n"
    "v_cvt_pk_bf16_f32 v237, v226, v227\n"
    "v_mfma_f32_16x16x32_bf16 v[104:107], a[180:183], v[152:155], v[104:107]\n"
    "s_cselect_b32 s88, 512, 0\n"
    "v_mov_b64_e32 v[224:225], 0\n"
    "v_mov_b64_e32 v[226:227], 0\n"
    "v_mfma_f32_16x16x32_bf16 v[104:107], a[176:179], v[148:151], v[104:107]\n"
    "buffer_store_dwordx2 v[236:237], v252, s[8:11], s87 offen sc1\n"
    "v_mfma_f32_16x16x32_bf16 v[108:111], a[176:179], v[156:159], v[108:111]\n"
    "s_mov_b64 exec, -1\n"
    "ds_read_b128 a[168:171], v240 offset:4096\n"
    "ds_write_addtid_b32 v197 offset:2304\n"
    "v_mfma_f32_16x16x32_bf16 v[108:111], a[180:183], v[160:163], v[108:111]\n"
    "s_add_u32 s87, s87, s88\n"
    "v_exp_f32_e32 v194, v194\n"
    "v_mfma_f32_16x16x32_bf16 v[112:115], a[180:183], v[168:171], v[112:115]\n"
    "buffer_load_dwordx4 a[228:231], v235, s[20:23], 0 offen sc1\n"
    "ds_write_addtid_b32 v204 offset:3072\n"
    "v_mfma_f32_16x16x32_bf16 v[112:115], a[176:179], v[164:167], v[112:115]\n"
    "ds_write_addtid_b32 v205 offset:3328\n"
    "v_mfma_f32_16x16x32_bf16 v[116:119], a[176:179], v[172:175], v[116:119]\n"
    "v_exp_f32_e32 v195, v195\n"
    "v_mfma_f32_16x16x32_bf16 v[116:119], a[180:183], v[176:179], v[116:119]\n"
    "s_waitcnt lgkmcnt(11)\n"
    "ds_read_b128 a[172:175], v241 offset:4096\n"
    "v_cvt_pk_bf16_f32 v190, v192, v193\n"
    "v_mfma_f32_16x16x32_bf16 v[136:139], a[188:191], v[176:179], v[136:139]\n"
    "v_exp_f32_e32 v200, v200\n"
    "v_mfma_f32_16x16x32_bf16 v[136:139], a[184:187], v[172:175], v[136:139]\n"
    "v_exp_f32_e32 v201, v201\n"
    "v_mfma_f32_16x16x32_bf16 v[132:135], a[184:187], v[164:167], v[132:135]\n"
    "ds_read_b128 a[176:179], v242 offset:0\n"
    "v_cvt_pk_bf16_f32 v191, v194, v195\n"
    "v_mfma_f32_16x16x32_bf16 v[132:135], a[188:191], v[168:171], v[132:135]\n"
    "v_exp_f32_e32 v202, v202\n"
    "v_mfma_f32_16x16x32_bf16 v[128:131], a[188:191], v[160:163], v[128:131]\n"
    "v_exp_f32_e32 v203, v203\n"
    "v_mfma_f32_16x16x32_bf16 v[128:131], a[184:187], v[156:159], v[128:131]\n"
    "ds_write_addtid_b32 v212 offset:4096\n"
    "v_cvt_pk_bf16_f32 v198, v200, v201\n"
    "v_mfma_f32_16x16x32_bf16 v[124:127], a[184:187], v[148:151], v[124:127]\n"
    "v_exp_f32_e32 v208, v208\n"
    "v_mfma_f32_16x16x32_bf16 v[124:127], a[188:191], v[152:155], v[124:127]\n"
    "ds_read_b128 a[180:183], v243 offset:0\n"
    "ds_write_addtid_b32 v213 offset:4352\n"
    "v_mfma_f32_16x16x32_bf16 v[120:123], a[188:191], v[144:147], v[120:123]\n"
    "v_exp_f32_e32 v209, v209\n"
    "v_mfma_f32_16x16x32_bf16 v[120:123], a[184:187], v[140:143], v[120:123]\n"
    "s_waitcnt lgkmcnt(11)\n"
    "ds_write_addtid_b32 v182 offset:512\n"
    "v_cvt_pk_bf16_f32 v199, v202, v203\n"
    "v_mfma_f32_16x16x32_bf16 v[172:175], a[160:163], a[128:131], v[220:223]\n"
    "ds_write_addtid_b32 v183 offset:768\n"
    "v_mfma_f32_16x16x32_bf16 v[172:175], a[164:167], a[132:135], v[172:175]\n"
    "v_exp_f32_e32 v210, v210\n"
    "v_mfma_f32_16x16x32_bf16 v[164:167], a[164:167], a[100:103], v[220:223]\n"
    "ds_write_addtid_b32 v190 offset:1536\n"
    "v_mfma_f32_16x16x32_bf16 v[164:167], a[160:163], a[96:99], v[164:167]\n"
    "ds_read_b128 a[184:187], v242 offset:4096\n"
    "v_mfma_f32_16x16x32_bf16 v[156:159], a[160:163], a[64:67], v[220:223]\n"
    "v_exp_f32_e32 v211, v211\n"
    "v_mfma_f32_16x16x32_bf16 v[156:159], a[164:167], a[68:71], v[156:159]\n"
    "ds_write_addtid_b32 v191 offset:1792\n"
    "v_cvt_pk_bf16_f32 v206, v208, v209\n"
    "v_mfma_f32_16x16x32_bf16 v[148:151], a[164:167], a[36:39], v[220:223]\n"
    "v_exp_f32_e32 v216, v216\n"
    "v_mfma_f32_16x16x32_bf16 v[148:151], a[160:163], a[32:35], v[148:151]\n"
    "v_exp_f32_e32 v217, v217\n"
    "v_mfma_f32_16x16x32_bf16 v[140:143], a[160:163], a[0:3], v[220:223]\n"
    "ds_write_addtid_b32 v198 offset:2560\n"
    "v_cvt_pk_bf16_f32 v207, v210, v211\n"
    "v_mfma_f32_16x16x32_bf16 v[140:143], a[164:167], a[4:7], v[140:143]\n"
    "s_waitcnt vmcnt(20)\n"
    "v_exp_f32_e32 v218, v218\n"
    "v_smfmac_f32_16x16x64_bf16 v[224:227], a[232:235], a[192:199], v238\n"
    "s_waitcnt lgkmcnt(10)\n"
    "ds_read_b128 a[188:191], v243 offset:4096\n"
    "v_mfma_f32_16x16x32_bf16 v[140:143], a[168:171], a[8:11], v[140:143]\n"
    "s_bitcmp1_b32 s86, 5\n"
    "ds_read_b128 a[160:163], v240 offset:2048\n"
    "v_mfma_f32_16x16x32_bf16 v[140:143], a[172:175], a[12:15], v[140:143]\n"
    "s_cselect_b64 exec, -1, 0\n"
    "v_cvt_pk_bf16_f32 v236, v224, v225\n"
    "v_cvt_pk_bf16_f32 v237, v226, v227\n"
    "v_mfma_f32_16x16x32_bf16 v[148:151], a[172:175], a[44:47], v[148:151]\n"
    "s_cselect_b32 s88, 512, 0\n"
    "v_mov_b64_e32 v[224:225], 0\n"
    "v_mov_b64_e32 v[226:227], 0\n"
    "v_mfma_f32_16x16x32_bf16 v[148:151], a[168:171], a[40:43], v[148:151]\n"
    "buffer_store_dwordx2 v[236:237], v252, s[8:11], s87 offen sc1\n"
    "v_mfma_f32_16x16x32_bf16 v[156:159], a[168:171], a[72:75], v[156:159]\n"
    "s_mov_b64 exec, -1\n"
    "ds_read2_b32 v[228:229], v251 offset0:10 offset1:12\n"
    "v_mfma_f32_16x16x32_bf16 v[156:159], a[172:175], a[76:79], v[156:159]\n"
    "s_add_u32 s87, s87, s88\n"
    "ds_read2_b32 v[230:231], v251 offset0:14 offset1:16\n"
    "v_mfma_f32_16x16x32_bf16 v[164:167], a[172:175], a[108:111], v[164:167]\n"
    "ds_read_b32 v232, v251 offset:72\n"
    "v_mfma_f32_16x16x32_bf16 v[164:167], a[168:171], a[104:107], v[164:167]\n"
    "v_exp_f32_e32 v219, v219\n"
    "v_mfma_f32_16x16x32_bf16 v[172:175], a[168:171], a[136:139], v[172:175]\n"
    "ds_write_addtid_b32 v199 offset:2816\n"
    "v_cvt_pk_bf16_f32 v214, v216, v217\n"
    "v_mfma_f32_16x16x32_bf16 v[172:175], a[172:175], a[140:143], v[172:175]\n"
    "s_waitcnt lgkmcnt(13)\n"
    "ds_read_b128 a[164:167], v241 offset:2048\n"
    "v_mfma_f32_16x16x32_bf16 v[172:175], a[176:179], a[144:147], v[172:175]\n"
    "ds_write_addtid_b32 v206 offset:3584\n"
    "v_cvt_pk_bf16_f32 v215, v218, v219\n"
    "v_mfma_f32_16x16x32_bf16 v[172:175], a[180:183], a[148:151], v[172:175]\n"
    "ds_read_b128 a[168:171], v240 offset:6144\n"
    "v_mfma_f32_16x16x32_bf16 v[164:167], a[180:183], a[116:119], v[164:167]\n"
    "s_waitcnt lgkmcnt(6)\n"
    "ds_write_addtid_b32 v207 offset:3840\n"
    "v_mad_u32_u16 v234, v228, s90, v239 op_sel:[0,0,0,0]\n"
    "v_mfma_f32_16x16x32_bf16 v[164:167], a[176:179], a[112:115], v[164:167]\n"
    "buffer_load_dwordx4 a[192:195], v234, s[20:23], 0 offen sc1\n"
    "v_mad_u32_u16 v235, v228, s90, v239 op_sel:[1,0,0,0]\n"
    "v_mfma_f32_16x16x32_bf16 v[156:159], a[176:179], a[80:83], v[156:159]\n"
    "ds_write_addtid_b32 v214 offset:4608\n"
    "v_mfma_f32_16x16x32_bf16 v[156:159], a[180:183], a[84:87], v[156:159]\n"
    "ds_write_addtid_b32 v215 offset:4864\n"
    "v_mfma_f32_16x16x32_bf16 v[148:151], a[180:183], a[52:55], v[148:151]\n"
    "buffer_load_dwordx4 a[196:199], v235, s[20:23], 0 offen sc1\n"
    "v_mfma_f32_16x16x32_bf16 v[148:151], a[176:179], a[48:51], v[148:151]\n"
    "v_mfma_f32_16x16x32_bf16 v[140:143], a[176:179], a[16:19], v[140:143]\n"
    "s_waitcnt lgkmcnt(0)\n"
    "v_mfma_f32_16x16x32_bf16 v[140:143], a[180:183], a[20:23], v[140:143]\n"
    "s_barrier\n"
    "v_mfma_f32_16x16x32_bf16 v[140:143], a[184:187], a[24:27], v[140:143]\n"
    "s_add_u32 m0, s43, 16384\n"
    "ds_read_b128 a[172:175], v241 offset:6144\n"
    "v_mfma_f32_16x16x32_bf16 v[140:143], a[188:191], a[28:31], v[140:143]\n"
    "ds_read2st64_b32 v[184:185], v249 offset0:0 offset1:1\n"
    "v_mfma_f32_16x16x32_bf16 v[148:151], a[188:191], a[60:63], v[148:151]\n"
    "buffer_load_dwordx4 v248, s[8:11], s40 offen offset:0 sc1 lds\n"
    "v_mfma_f32_16x16x32_bf16 v[148:151], a[184:187], a[56:59], v[148:151]\n"
    "ds_read_b128 a[176:179], v242 offset:2048\n"
    "v_mfma_f32_16x16x32_bf16 v[156:159], a[184:187], a[88:91], v[156:159]\n"
    "v_mfma_f32_16x16x32_bf16 v[156:159], a[188:191], a[92:95], v[156:159]\n"
    "v_mfma_f32_16x16x32_bf16 v[164:167], a[188:191], a[124:127], v[164:167]\n"
    "buffer_load_dwordx4 v248, s[8:11], s40 offen offset:1024 sc1 lds\n"
    "v_mfma_f32_16x16x32_bf16 v[164:167], a[184:187], a[120:123], v[164:167]\n"
    "ds_read2st64_b32 v[186:187], v249 offset0:2 offset1:3\n"
    "v_mfma_f32_16x16x32_bf16 v[172:175], a[184:187], a[152:155], v[172:175]\n"
    "v_mfma_f32_16x16x32_bf16 v[172:175], a[188:191], a[156:159], v[172:175]\n"
    "ds_read_b128 a[180:183], v243 offset:2048\n"
    "v_mfma_f32_16x16x32_bf16 v[176:179], a[160:163], a[128:131], v[220:223]\n"
    "buffer_load_dwordx4 v248, s[8:11], s40 offen offset:2048 sc1 lds\n"
    "v_mfma_f32_16x16x32_bf16 v[176:179], a[164:167], a[132:135], v[176:179]\n"
    "ds_read2st64_b32 v[192:193], v249 offset0:4 offset1:5\n"
    "v_mfma_f32_16x16x32_bf16 v[168:171], a[164:167], a[100:103], v[220:223]\n"
    "v_exp_f32_e32 v140, v140\n"
    "v_mfma_f32_16x16x32_bf16 v[168:171], a[160:163], a[96:99], v[168:171]\n"
    "ds_read_b128 a[184:187], v242 offset:6144\n"
    "v_mfma_f32_16x16x32_bf16 v[160:163], a[160:163], a[64:67], v[220:223]\n"
    "buffer_load_dwordx4 v248, s[8:11], s40 offen offset:3072 sc1 lds\n"
    "v_mfma_f32_16x16x32_bf16 v[160:163], a[164:167], a[68:71], v[160:163]\n"
    "s_add_u32 m0, s43, 20480\n"
    "ds_read2st64_b32 v[194:195], v249 offset0:6 offset1:7\n"
    "v_mfma_f32_16x16x32_bf16 v[152:155], a[164:167], a[36:39], v[220:223]\n"
    "s_add_u32 s40, s40, 0x8000\n"
    "ds_read2st64_b32 v[200:201], v249 offset0:8 offset1:9\n"
    "v_mfma_f32_16x16x32_bf16 v[152:155], a[160:163], a[32:35], v[152:155]\n"
    "ds_read2st64_b32 v[202:203], v249 offset0:10 offset1:11\n"
    "v_mfma_f32_16x16x32_bf16 v[144:147], a[160:163], a[0:3], v[220:223]\n"
    "buffer_load_dwordx4 v248, s[8:11], s41 offen offset:0 sc1 lds\n"
    "v_mfma_f32_16x16x32_bf16 v[144:147], a[164:167], a[4:7], v[144:147]\n"
    "s_waitcnt vmcnt(17)\n"
    "ds_read_b128 a[188:191], v243 offset:6144\n"
    "v_smfmac_f32_16x16x64_bf16 v[224:227], a[232:235], a[200:207], v238\n"
    "s_waitcnt lgkmcnt(10)\n"
    "v_mad_u32_u16 v234, v229, s90, v239 op_sel:[0,0,0,0]\n"
    "v_mfma_f32_16x16x32_bf16 v[144:147], a[168:171], a[8:11], v[144:147]\n"
    "s_bitcmp1_b32 s86, 6\n"
    "buffer_load_dwordx4 a[200:203], v234, s[20:23], 0 offen sc1\n"
    "v_mad_u32_u16 v235, v229, s90, v239 op_sel:[1,0,0,0]\n"
    "v_mfma_f32_16x16x32_bf16 v[144:147], a[172:175], a[12:15], v[144:147]\n"
    "s_cselect_b64 exec, -1, 0\n"
    "v_cvt_pk_bf16_f32 v236, v224, v225\n"
    "v_cvt_pk_bf16_f32 v237, v226, v227\n"
    "v_mfma_f32_16x16x32_bf16 v[152:155], a[172:175], a[44:47], v[152:155]\n"
    "s_cselect_b32 s88, 512, 0\n"
    "v_mov_b64_e32 v[224:225], 0\n"
    "v_mov_b64_e32 v[226:227], 0\n"
    "v_mfma_f32_16x16x32_bf16 v[152:155], a[168:171], a[40:43], v[152:155]\n"
    "buffer_store_dwordx2 v[236:237], v252, s[8:11], s87 offen sc1\n"
    "v_mfma_f32_16x16x32_bf16 v[160:163], a[168:171], a[72:75], v[160:163]\n"
    "s_mov_b64 exec, -1\n"
    "ds_read_b64_tr_b16 a[160:161], v244 offset:24576\n"
    "v_mfma_f32_16x16x32_bf16 v[160:163], a[172:175], a[76:79], v[160:163]\n"
    "s_add_u32 s87, s87, s88\n"
    "ds_read_b64_tr_b16 a[162:163], v244 offset:26624\n"
    "v_mfma_f32_16x16x32_bf16 v[168:171], a[172:175], a[108:111], v[168:171]\n"
    "buffer_load_dwordx4 v248, s[8:11], s41 offen offset:1024 sc1 lds\n"
    "v_mfma_f32_16x16x32_bf16 v[168:171], a[168:171], a[104:107], v[168:171]\n"
    "ds_read_b64_tr_b16 a[164:165], v244 offset:57344\n"
    "v_mfma_f32_16x16x32_bf16 v[176:179], a[168:171], a[136:139], v[176:179]\n"
    "ds_read2st64_b32 v[208:209], v249 offset0:12 offset1:13\n"
    "v_mfma_f32_16x16x32_bf16 v[176:179], a[172:175], a[140:143], v[176:179]\n"
    "s_waitcnt lgkmcnt(10)\n"
    "ds_read2st64_b32 v[210:211], v249 offset0:14 offset1:15\n"
    "v_mfma_f32_16x16x32_bf16 v[176:179], a[176:179], a[144:147], v[176:179]\n"
    "buffer_load_dwordx4 v248, s[8:11], s41 offen offset:2048 sc1 lds\n"
    "v_mfma_f32_16x16x32_bf16 v[176:179], a[180:183], a[148:151], v[176:179]\n"
    "ds_read_b64_tr_b16 a[166:167], v244 offset:59392\n"
    "v_mfma_f32_16x16x32_bf16 v[168:171], a[180:183], a[116:119], v[168:171]\n"
    "ds_read_b64_tr_b16 a[168:169], v245 offset:24576\n"
    "v_mfma_f32_16x16x32_bf16 v[168:171], a[176:179], a[112:115], v[168:171]\n"
    "v_exp_f32_e32 v141, v141\n"
    "v_mfma_f32_16x16x32_bf16 v[160:163], a[176:179], a[80:83], v[160:163]\n"
    "buffer_load_dwordx4 v248, s[8:11], s41 offen offset:3072 sc1 lds\n"
    "v_mfma_f32_16x16x32_bf16 v[160:163], a[180:183], a[84:87], v[160:163]\n"
    "ds_read_b64_tr_b16 a[170:171], v245 offset:26624\n"
    "v_mfma_f32_16x16x32_bf16 v[152:155], a[180:183], a[52:55], v[152:155]\n"
    "s_add_u32 s41, s41, 0x8000\n"
    "ds_read2st64_b32 v[216:217], v249 offset0:16 offset1:17\n"
    "v_mfma_f32_16x16x32_bf16 v[152:155], a[176:179], a[48:51], v[152:155]\n"
    "buffer_load_dwordx4 a[204:207], v235, s[20:23], 0 offen sc1\n"
    "v_mfma_f32_16x16x32_bf16 v[144:147], a[176:179], a[16:19], v[144:147]\n"
    "ds_read_b64_tr_b16 a[172:173], v245 offset:57344\n"
    "v_mfma_f32_16x16x32_bf16 v[144:147], a[180:183], a[20:23], v[144:147]\n"
    "s_waitcnt lgkmcnt(10)\n"
    "ds_read2st64_b32 v[218:219], v249 offset0:18 offset1:19\n"
    "v_mfma_f32_16x16x32_bf16 v[144:147], a[184:187], a[24:27], v[144:147]\n"
    "ds_read_b64_tr_b16 a[176:177], v246 offset:24576\n"
    "v_mfma_f32_16x16x32_bf16 v[144:147], a[188:191], a[28:31], v[144:147]\n"
    "ds_read_b64_tr_b16 a[174:175], v245 offset:59392\n"
    "v_mfma_f32_16x16x32_bf16 v[152:155], a[188:191], a[60:63], v[152:155]\n"
    "v_exp_f32_e32 v142, v142\n"
    "v_mfma_f32_16x16x32_bf16 v[152:155], a[184:187], a[56:59], v[152:155]\n"
    "v_exp_f32_e32 v143, v143\n"
    "v_mfma_f32_16x16x32_bf16 v[160:163], a[184:187], a[88:91], v[160:163]\n"
    "ds_read_b64_tr_b16 a[178:179], v246 offset:26624\n"
    "v_cvt_pk_bf16_f32 v140, v140, v141\n"
    "v_mfma_f32_16x16x32_bf16 v[160:163], a[188:191], a[92:95], v[160:163]\n"
    "v_exp_f32_e32 v148, v148\n"
    "v_mfma_f32_16x16x32_bf16 v[168:171], a[188:191], a[124:127], v[168:171]\n"
    "v_exp_f32_e32 v149, v149\n"
    "v_mfma_f32_16x16x32_bf16 v[168:171], a[184:187], a[120:123], v[168:171]\n"
    "v_cvt_pk_bf16_f32 v141, v142, v143\n"
    "v_mfma_f32_16x16x32_bf16 v[176:179], a[184:187], a[152:155], v[176:179]\n"
    "ds_read_b64_tr_b16 a[180:181], v246 offset:57344\n"
    "v_mfma_f32_16x16x32_bf16 v[176:179], a[188:191], a[156:159], v[176:179]\n"
    "s_waitcnt vmcnt(20)\n"
    "ds_read_b64_tr_b16 a[182:183], v246 offset:59392\n"
    "v_smfmac_f32_16x16x64_bf16 v[224:227], a[232:235], a[208:215], v238\n"
    "s_waitcnt lgkmcnt(5)\n"
    "v_mad_u32_u16 v234, v230, s90, v239 op_sel:[0,0,0,0]\n"
    "v_mfma_f32_16x16x32_bf16 a[236:239], a[160:163], v[180:183], a[236:239]\n"
    "s_bitcmp1_b32 s86, 7\n"
    "buffer_load_dwordx4 a[208:211], v234, s[20:23], 0 offen sc1\n"
    "v_mad_u32_u16 v235, v230, s90, v239 op_sel:[1,0,0,0]\n"
    "v_mfma_f32_16x16x32_bf16 a[236:239], a[164:167], v[184:187], a[236:239]\n"
    "s_cselect_b64 exec, -1, 0\n"
    "v_cvt_pk_bf16_f32 v236, v224, v225\n"
    "v_cvt_pk_bf16_f32 v237, v226, v227\n"
    "v_mfma_f32_16x16x32_bf16 a[240:243], a[164:167], v[192:195], a[240:243]\n"
    "s_cselect_b32 s88, 512, 0\n"
    "v_mov_b64_e32 v[224:225], 0\n"
    "v_mov_b64_e32 v[226:227], 0\n"
    "v_mfma_f32_16x16x32_bf16 a[240:243], a[160:163], v[188:191], a[240:243]\n"
    "buffer_store_dwordx2 v[236:237], v252, s[8:11], s87 offen sc1\n"
    "v_mfma_f32_16x16x32_bf16 a[244:247], a[160:163], v[196:199], a[244:247]\n"
    "s_mov_b64 exec, -1\n"
    "ds_read_b64_tr_b16 a[184:185], v247 offset:24576\n"
    "v_mfma_f32_16x16x32_bf16 a[244:247], a[164:167], v[200:203], a[244:247]\n"
    "s_add_u32 s87, s87, s88\n"
    "ds_read_b64_tr_b16 a[186:187], v247 offset:26624\n"
    "v_mfma_f32_16x16x32_bf16 a[248:251], a[164:167], v[208:211], a[248:251]\n"
    "buffer_load_dwordx4 a[212:215], v235, s[20:23], 0 offen sc1\n"
    "v_mfma_f32_16x16x32_bf16 a[248:251], a[160:163], v[204:207], a[248:251]\n"
    "ds_read_b64_tr_b16 a[188:189], v247 offset:57344\n"
    "v_mfma_f32_16x16x32_bf16 a[252:255], a[160:163], v[212:215], a[252:255]\n"
    "v_exp_f32_e32 v150, v150\n"
    "v_mfma_f32_16x16x32_bf16 a[252:255], a[164:167], v[216:219], a[252:255]\n"
    "s_waitcnt lgkmcnt(6)\n"
    "v_exp_f32_e32 v151, v151\n"
    "v_mfma_f32_16x16x32_bf16 v[16:19], a[172:175], v[216:219], v[16:19]\n"
    "ds_read_b64_tr_b16 a[190:191], v247 offset:59392\n"
    "v_cvt_pk_bf16_f32 v148, v148, v149\n"
    "v_mfma_f32_16x16x32_bf16 v[16:19], a[168:171], v[212:215], v[16:19]\n"
    "ds_read_b64_tr_b16 a[160:161], v244 offset:28672\n"
    "v_mfma_f32_16x16x32_bf16 v[12:15], a[168:171], v[204:207], v[12:15]\n"
    "v_exp_f32_e32 v156, v156\n"
    "v_mfma_f32_16x16x32_bf16 v[12:15], a[172:175], v[208:211], v[12:15]\n"
    "v_exp_f32_e32 v157, v157\n"
    "v_mfma_f32_16x16x32_bf16 v[8:11], a[172:175], v[200:203], v[8:11]\n"
    "ds_read_b64_tr_b16 a[162:163], v244 offset:30720\n"
    "v_cvt_pk_bf16_f32 v149, v150, v151\n"
    "v_mfma_f32_16x16x32_bf16 v[8:11], a[168:171], v[196:199], v[8:11]\n"
    "v_exp_f32_e32 v158, v158\n"
    "v_mfma_f32_16x16x32_bf16 v[4:7], a[168:171], v[188:191], v[4:7]\n"
    "v_exp_f32_e32 v159, v159\n"
    "v_mfma_f32_16x16x32_bf16 v[4:7], a[172:175], v[192:195], v[4:7]\n"
    "v_cvt_pk_bf16_f32 v156, v156, v157\n"
    "v_mfma_f32_16x16x32_bf16 v[0:3], a[172:175], v[184:187], v[0:3]\n"
    "ds_read_b64_tr_b16 a[164:165], v244 offset:61440\n"
    "v_mfma_f32_16x16x32_bf16 v[0:3], a[168:171], v[180:183], v[0:3]\n"
    "s_waitcnt lgkmcnt(7)\n"
    "v_exp_f32_e32 v164, v164\n"
    "v_mfma_f32_16x16x32_bf16 v[20:23], a[176:179], v[180:183], v[20:23]\n"
    "ds_read_b64_tr_b16 a[168:169], v245 offset:28672\n"
    "v_mfma_f32_16x16x32_bf16 v[20:23], a[180:183], v[184:187], v[20:23]\n"
    "ds_read_b64_tr_b16 a[166:167], v244 offset:63488\n"
    "v_mfma_f32_16x16x32_bf16 v[24:27], a[180:183], v[192:195], v[24:27]\n"
    "v_exp_f32_e32 v165, v165\n"
    "v_mfma_f32_16x16x32_bf16 v[24:27], a[176:179], v[188:191], v[24:27]\n"
    "v_cvt_pk_bf16_f32 v157, v158, v159\n"
    "v_mfma_f32_16x16x32_bf16 v[28:31], a[176:179], v[196:199], v[28:31]\n"
    "ds_read_b64_tr_b16 a[170:171], v245 offset:30720\n"
    "v_mfma_f32_16x16x32_bf16 v[28:31], a[180:183], v[200:203], v[28:31]\n"
    "v_exp_f32_e32 v166, v166\n"
    "v_mfma_f32_16x16x32_bf16 v[32:35], a[180:183], v[208:211], v[32:35]\n"
    "v_exp_f32_e32 v167, v167\n"
    "v_mfma_f32_16x16x32_bf16 v[32:35], a[176:179], v[204:207], v[32:35]\n"
    "v_cvt_pk_bf16_f32 v164, v164, v165\n"
    "v_mfma_f32_16x16x32_bf16 v[36:39], a[176:179], v[212:215], v[36:39]\n"
    "ds_read_b64_tr_b16 a[172:173], v245 offset:61440\n"
    "v_mfma_f32_16x16x32_bf16 v[36:39], a[180:183], v[216:219], v[36:39]\n"
    "s_waitcnt vmcnt(20)\n"
    "ds_read_b64_tr_b16 a[174:175], v245 offset:63488\n"
    "v_smfmac_f32_16x16x64_bf16 v[224:227], a[232:235], a[216:223], v238\n"
    "s_waitcnt lgkmcnt(8)\n"
    "v_mad_u32_u16 v234, v231, s90, v239 op_sel:[0,0,0,0]\n"
    "v_mfma_f32_16x16x32_bf16 v[56:59], a[188:191], v[216:219], v[56:59]\n"
    "s_bitcmp1_b32 s86, 8\n"
    "buffer_load_dwordx4 a[216:219], v234, s[20:23], 0 offen sc1\n"
    "v_mad_u32_u16 v235, v231, s90, v239 op_sel:[1,0,0,0]\n"
    "v_mfma_f32_16x16x32_bf16 v[56:59], a[184:187], v[212:215], v[56:59]\n"
    "s_cselect_b64 exec, -1, 0\n"
    "v_cvt_pk_bf16_f32 v236, v224, v225\n"
    "v_cvt_pk_bf16_f32 v237, v226, v227\n"
    "v_mfma_f32_16x16x32_bf16 v[52:55], a[184:187], v[204:207], v[52:55]\n"
    "s_cselect_b32 s88, 512, 0\n"
    "v_mov_b64_e32 v[224:225], 0\n"
    "v_mov_b64_e32 v[226:227], 0\n"
    "v_mfma_f32_16x16x32_bf16 v[52:55], a[188:191], v[208:211], v[52:55]\n"
    "buffer_store_dwordx2 v[236:237], v252, s[8:11], s87 offen sc1\n"
    "v_mfma_f32_16x16x32_bf16 v[48:51], a[188:191], v[200:203], v[48:51]\n"
    "s_mov_b64 exec, -1\n"
    "ds_read_b64_tr_b16 a[176:177], v246 offset:28672\n"
    "v_mfma_f32_16x16x32_bf16 v[48:51], a[184:187], v[196:199], v[48:51]\n"
    "s_add_u32 s87, s87, s88\n"
    "ds_read_b64_tr_b16 a[178:179], v246 offset:30720\n"
    "v_mfma_f32_16x16x32_bf16 v[44:47], a[184:187], v[188:191], v[44:47]\n"
    "buffer_load_dwordx4 a[220:223], v235, s[20:23], 0 offen sc1\n"
    "v_mfma_f32_16x16x32_bf16 v[44:47], a[188:191], v[192:195], v[44:47]\n"
    "ds_read_b64_tr_b16 a[180:181], v246 offset:61440\n"
    "v_mfma_f32_16x16x32_bf16 v[40:43], a[188:191], v[184:187], v[40:43]\n"
    "v_exp_f32_e32 v172, v172\n"
    "v_mfma_f32_16x16x32_bf16 v[40:43], a[184:187], v[180:183], v[40:43]\n"
    "s_waitcnt lgkmcnt(6)\n"
    "v_exp_f32_e32 v173, v173\n"
    "v_mfma_f32_16x16x32_bf16 v[60:63], a[160:163], v[180:183], v[60:63]\n"
    "ds_read_b64_tr_b16 a[182:183], v246 offset:63488\n"
    "v_cvt_pk_bf16_f32 v165, v166, v167\n"
    "v_mfma_f32_16x16x32_bf16 v[60:63], a[164:167], v[184:187], v[60:63]\n"
    "ds_read_b64_tr_b16 a[184:185], v247 offset:28672\n"
    "v_mfma_f32_16x16x32_bf16 v[64:67], a[164:167], v[192:195], v[64:67]\n"
    "ds_read_b64_tr_b16 a[186:187], v247 offset:30720\n"
    "v_mfma_f32_16x16x32_bf16 v[64:67], a[160:163], v[188:191], v[64:67]\n"
    "v_exp_f32_e32 v174, v174\n"
    "v_mfma_f32_16x16x32_bf16 v[68:71], a[160:163], v[196:199], v[68:71]\n"
    "v_exp_f32_e32 v175, v175\n"
    "v_mfma_f32_16x16x32_bf16 v[68:71], a[164:167], v[200:203], v[68:71]\n"
    "ds_read_b64_tr_b16 a[188:189], v247 offset:61440\n"
    "v_cvt_pk_bf16_f32 v172, v172, v173\n"
    "v_mfma_f32_16x16x32_bf16 v[72:75], a[164:167], v[208:211], v[72:75]\n"
    "ds_read_b64_tr_b16 a[190:191], v247 offset:63488\n"
    "v_cvt_pk_bf16_f32 v173, v174, v175\n"
    "v_mfma_f32_16x16x32_bf16 v[72:75], a[160:163], v[204:207], v[72:75]\n"
    "s_waitcnt vmcnt(30)\n"
    "v_mfma_f32_16x16x32_bf16 v[76:79], a[160:163], v[212:215], v[76:79]\n"
    "s_barrier\n"
    "v_mfma_f32_16x16x32_bf16 v[76:79], a[164:167], v[216:219], v[76:79]\n"
    "s_waitcnt lgkmcnt(8)\n"
    "s_mov_b32 m0, s42\n"
    "v_mfma_f32_16x16x32_bf16 v[96:99], a[172:175], v[216:219], v[96:99]\n"
    "v_exp_f32_e32 v144, v144\n"
    "v_mfma_f32_16x16x32_bf16 v[96:99], a[168:171], v[212:215], v[96:99]\n"
    "v_exp_f32_e32 v145, v145\n"
    "v_mfma_f32_16x16x32_bf16 v[92:95], a[168:171], v[204:207], v[92:95]\n"
    "v_exp_f32_e32 v146, v146\n"
    "v_mfma_f32_16x16x32_bf16 v[92:95], a[172:175], v[208:211], v[92:95]\n"
    "ds_read_b128 a[160:163], v240 offset:8192\n"
    "ds_write_addtid_b32 v140 offset:0\n"
    "v_mfma_f32_16x16x32_bf16 v[88:91], a[172:175], v[200:203], v[88:91]\n"
    "v_exp_f32_e32 v147, v147\n"
    "v_mfma_f32_16x16x32_bf16 v[88:91], a[168:171], v[196:199], v[88:91]\n"
    "ds_write_addtid_b32 v141 offset:256\n"
    "v_cvt_pk_bf16_f32 v142, v144, v145\n"
    "v_mfma_f32_16x16x32_bf16 v[84:87], a[168:171], v[188:191], v[84:87]\n"
    "v_exp_f32_e32 v152, v152\n"
    "v_mfma_f32_16x16x32_bf16 v[84:87], a[172:175], v[192:195], v[84:87]\n"
    "v_exp_f32_e32 v153, v153\n"
    "v_mfma_f32_16x16x32_bf16 v[80:83], a[172:175], v[184:187], v[80:83]\n"
    "ds_write_addtid_b32 v148 offset:1024\n"
    "v_cvt_pk_bf16_f32 v143, v146, v147\n"
    "v_mfma_f32_16x16x32_bf16 v[80:83], a[168:171], v[180:183], v[80:83]\n"
    "s_waitcnt vmcnt(20)\n"
    "ds_read_b128 a[164:167], v241 offset:8192\n"
    "ds_write_addtid_b32 v149 offset:1280\n"
    "v_smfmac_f32_16x16x64_bf16 v[224:227], a[232:235], a[224:231], v238\n"
    "s_waitcnt lgkmcnt(10)\n"
    "ds_write_addtid_b32 v156 offset:2048\n"
    "v_mad_u32_u16 v234, v232, s90, v239 op_sel:[0,0,0,0]\n"
    "v_mfma_f32_16x16x32_bf16 v[100:103], a[176:179], v[180:183], v[100:103]\n"
    "s_bitcmp1_b32 s86, 9\n"
    "buffer_load_dwordx4 a[224:227], v234, s[20:23], 0 offen sc1\n"
    "v_mad_u32_u16 v235, v232, s90, v239 op_sel:[1,0,0,0]\n"
    "v_mfma_f32_16x16x32_bf16 v[100:103], a[180:183], v[184:187], v[100:103]\n"
    "s_cselect_b64 exec, -1, 0\n"
    "v_cvt_pk_bf16_f32 v236, v224, v225\n"
    "v_cvt_pk_bf16_f32 v237, v226, v227\n"
    "v_mfma_f32_16x16x32_bf16 v[104:107], a[180:183], v[192:195], v[104:107]\n"
    "s_cselect_b32 s88, 512, 0\n"
    "v_mov_b64_e32 v[224:225], 0\n"
    "v_mov_b64_e32 v[226:227], 0\n"
    "v_mfma_f32_16x16x32_bf16 v[104:107], a[176:179], v[188:191], v[104:107]\n"
    "buffer_store_dwordx2 v[236:237], v252, s[8:11], s87 offen sc1\n"
    "v_mfma_f32_16x16x32_bf16 v[108:111], a[176:179], v[196:199], v[108:111]\n"
    "s_mov_b64 exec, -1\n"
    "ds_read_b128 a[168:171], v240 offset:12288\n"
    "ds_write_addtid_b32 v157 offset:2304\n"
    "v_mfma_f32_16x16x32_bf16 v[108:111], a[180:183], v[200:203], v[108:111]\n"
    "s_add_u32 s87, s87, s88\n"
    "v_exp_f32_e32 v154, v154\n"
    "v_mfma_f32_16x16x32_bf16 v[112:115], a[180:183], v[208:211], v[112:115]\n"
    "buffer_load_dwordx4 a[228:231], v235, s[20:23], 0 offen sc1\n"
    "ds_write_addtid_b32 v164 offset:3072\n"
    "v_mfma_f32_16x16x32_bf16 v[112:115], a[176:179], v[204:207], v[112:115]\n"
    "ds_write_addtid_b32 v165 offset:3328\n"
    "v_mfma_f32_16x16x32_bf16 v[116:119], a[176:179], v[212:215], v[116:119]\n"
    "v_exp_f32_e32 v155, v155\n"
    "v_mfma_f32_16x16x32_bf16 v[116:119], a[180:183], v[216:219], v[116:119]\n"
    "s_waitcnt lgkmcnt(11)\n"
    "ds_read_b128 a[172:175], v241 offset:12288\n"
    "v_cvt_pk_bf16_f32 v150, v152, v153\n"
    "v_mfma_f32_16x16x32_bf16 v[136:139], a[188:191], v[216:219], v[136:139]\n"
    "v_exp_f32_e32 v160, v160\n"
    "v_mfma_f32_16x16x32_bf16 v[136:139], a[184:187], v[212:215], v[136:139]\n"
    "v_exp_f32_e32 v161, v161\n"
    "v_mfma_f32_16x16x32_bf16 v[132:135], a[184:187], v[204:207], v[132:135]\n"
    "ds_read_b128 a[176:179], v242 offset:8192\n"
    "v_cvt_pk_bf16_f32 v151, v154, v155\n"
    "v_mfma_f32_16x16x32_bf16 v[132:135], a[188:191], v[208:211], v[132:135]\n"
    "v_exp_f32_e32 v162, v162\n"
    "v_mfma_f32_16x16x32_bf16 v[128:131], a[188:191], v[200:203], v[128:131]\n"
    "v_exp_f32_e32 v163, v163\n"
    "v_mfma_f32_16x16x32_bf16 v[128:131], a[184:187], v[196:199], v[128:131]\n"
    "ds_write_addtid_b32 v172 offset:4096\n"
    "v_cvt_pk_bf16_f32 v158, v160, v161\n"
    "v_mfma_f32_16x16x32_bf16 v[124:127], a[184:187], v[188:191], v[124:127]\n"
    "v_exp_f32_e32 v168, v168\n"
    "v_mfma_f32_16x16x32_bf16 v[124:127], a[188:191], v[192:195], v[124:127]\n"
    "ds_read_b128 a[180:183], v243 offset:8192\n"
    "ds_write_addtid_b32 v173 offset:4352\n"
    "v_mfma_f32_16x16x32_bf16 v[120:123], a[188:191], v[184:187], v[120:123]\n"
    "v_exp_f32_e32 v169, v169\n"
    "v_mfma_f32_16x16x32_bf16 v[120:123], a[184:187], v[180:183], v[120:123]\n"
    "s_waitcnt lgkmcnt(11)\n"
    "ds_write_addtid_b32 v142 offset:512\n"
    "v_cvt_pk_bf16_f32 v159, v162, v163\n"
    "v_mfma_f32_16x16x32_bf16 v[212:215], a[160:163], a[128:131], v[220:223]\n"
    "ds_write_addtid_b32 v143 offset:768\n"
    "v_mfma_f32_16x16x32_bf16 v[212:215], a[164:167], a[132:135], v[212:215]\n"
    "v_exp_f32_e32 v170, v170\n"
    "v_mfma_f32_16x16x32_bf16 v[204:207], a[164:167], a[100:103], v[220:223]\n"
    "ds_write_addtid_b32 v150 offset:1536\n"
    "v_mfma_f32_16x16x32_bf16 v[204:207], a[160:163], a[96:99], v[204:207]\n"
    "ds_read_b128 a[184:187], v242 offset:12288\n"
    "v_mfma_f32_16x16x32_bf16 v[196:199], a[160:163], a[64:67], v[220:223]\n"
    "v_exp_f32_e32 v171, v171\n"
    "v_mfma_f32_16x16x32_bf16 v[196:199], a[164:167], a[68:71], v[196:199]\n"
    "ds_write_addtid_b32 v151 offset:1792\n"
    "v_cvt_pk_bf16_f32 v166, v168, v169\n"
    "v_mfma_f32_16x16x32_bf16 v[188:191], a[164:167], a[36:39], v[220:223]\n"
    "v_exp_f32_e32 v176, v176\n"
    "v_mfma_f32_16x16x32_bf16 v[188:191], a[160:163], a[32:35], v[188:191]\n"
    "v_exp_f32_e32 v177, v177\n"
    "v_mfma_f32_16x16x32_bf16 v[180:183], a[160:163], a[0:3], v[220:223]\n"
    "ds_write_addtid_b32 v158 offset:2560\n"
    "v_cvt_pk_bf16_f32 v167, v170, v171\n"
    "v_mfma_f32_16x16x32_bf16 v[180:183], a[164:167], a[4:7], v[180:183]\n"
    "s_waitcnt vmcnt(20)\n"
    "v_exp_f32_e32 v178, v178\n"
    "v_smfmac_f32_16x16x64_bf16 v[224:227], a[232:235], a[192:199], v238\n"
    "s_waitcnt lgkmcnt(10)\n"
    "ds_read_b128 a[188:191], v243 offset:12288\n"
    "v_mfma_f32_16x16x32_bf16 v[180:183], a[168:171], a[8:11], v[180:183]\n"
    "s_bitcmp1_b32 s86, 10\n"
    "ds_read_b128 a[160:163], v240 offset:10240\n"
    "v_mfma_f32_16x16x32_bf16 v[180:183], a[172:175], a[12:15], v[180:183]\n"
    "s_cselect_b64 exec, -1, 0\n"
    "v_cvt_pk_bf16_f32 v236, v224, v225\n"
    "v_cvt_pk_bf16_f32 v237, v226, v227\n"
    "v_mfma_f32_16x16x32_bf16 v[188:191], a[172:175], a[44:47], v[188:191]\n"
    "s_cselect_b32 s88, 512, 0\n"
    "v_mov_b64_e32 v[224:225], 0\n"
    "v_mov_b64_e32 v[226:227], 0\n"
    "v_mfma_f32_16x16x32_bf16 v[188:191], a[168:171], a[40:43], v[188:191]\n"
    "buffer_store_dwordx2 v[236:237], v252, s[8:11], s87 offen sc1\n"
    "v_mfma_f32_16x16x32_bf16 v[196:199], a[168:171], a[72:75], v[196:199]\n"
    "s_mov_b64 exec, -1\n"
    "ds_read2_b32 v[228:229], v251 offset0:20 offset1:22\n"
    "v_mfma_f32_16x16x32_bf16 v[196:199], a[172:175], a[76:79], v[196:199]\n"
    "s_add_u32 s87, s87, s88\n"
    "ds_read2_b32 v[230:231], v251 offset0:24 offset1:26\n"
    "v_mfma_f32_16x16x32_bf16 v[204:207], a[172:175], a[108:111], v[204:207]\n"
    "ds_read_b32 v232, v251 offset:112\n"
    "v_mfma_f32_16x16x32_bf16 v[204:207], a[168:171], a[104:107], v[204:207]\n"
    "v_exp_f32_e32 v179, v179\n"
    "v_mfma_f32_16x16x32_bf16 v[212:215], a[168:171], a[136:139], v[212:215]\n"
    "ds_write_addtid_b32 v159 offset:2816\n"
    "v_cvt_pk_bf16_f32 v174, v176, v177\n"
    "v_mfma_f32_16x16x32_bf16 v[212:215], a[172:175], a[140:143], v[212:215]\n"
    "s_waitcnt lgkmcnt(13)\n"
    "ds_read_b128 a[164:167], v241 offset:10240\n"
    "v_mfma_f32_16x16x32_bf16 v[212:215], a[176:179], a[144:147], v[212:215]\n"
    "ds_write_addtid_b32 v166 offset:3584\n"
    "v_cvt_pk_bf16_f32 v175, v178, v179\n"
    "v_mfma_f32_16x16x32_bf16 v[212:215], a[180:183], a[148:151], v[212:215]\n"
    "ds_read_b128 a[168:171], v240 offset:14336\n"
    "v_mfma_f32_16x16x32_bf16 v[204:207], a[180:183], a[116:119], v[204:207]\n"
    "s_waitcnt lgkmcnt(6)\n"
    "ds_write_addtid_b32 v167 offset:3840\n"
    "v_mad_u32_u16 v234, v228, s90, v239 op_sel:[0,0,0,0]\n"
    "v_mfma_f32_16x16x32_bf16 v[204:207], a[176:179], a[112:115], v[204:207]\n"
    "buffer_load_dwordx4 a[192:195], v234, s[20:23], 0 offen sc1\n"
    "v_mad_u32_u16 v235, v228, s90, v239 op_sel:[1,0,0,0]\n"
    "v_mfma_f32_16x16x32_bf16 v[196:199], a[176:179], a[80:83], v[196:199]\n"
    "ds_write_addtid_b32 v174 offset:4608\n"
    "v_mfma_f32_16x16x32_bf16 v[196:199], a[180:183], a[84:87], v[196:199]\n"
    "ds_write_addtid_b32 v175 offset:4864\n"
    "v_mfma_f32_16x16x32_bf16 v[188:191], a[180:183], a[52:55], v[188:191]\n"
    "buffer_load_dwordx4 a[196:199], v235, s[20:23], 0 offen sc1\n"
    "v_mfma_f32_16x16x32_bf16 v[188:191], a[176:179], a[48:51], v[188:191]\n"
    "v_mfma_f32_16x16x32_bf16 v[180:183], a[176:179], a[16:19], v[180:183]\n"
    "s_waitcnt lgkmcnt(0)\n"
    "v_mfma_f32_16x16x32_bf16 v[180:183], a[180:183], a[20:23], v[180:183]\n"
    "s_barrier\n"
    "v_mfma_f32_16x16x32_bf16 v[180:183], a[184:187], a[24:27], v[180:183]\n"
    "s_add_u32 m0, s43, 24576\n"
    "ds_read_b128 a[172:175], v241 offset:14336\n"
    "v_mfma_f32_16x16x32_bf16 v[180:183], a[188:191], a[28:31], v[180:183]\n"
    "ds_read2st64_b32 v[144:145], v249 offset0:0 offset1:1\n"
    "v_mfma_f32_16x16x32_bf16 v[188:191], a[188:191], a[60:63], v[188:191]\n"
    "buffer_load_dwordx4 v248, s[8:11], s40 offen offset:0 sc1 lds\n"
    "v_mfma_f32_16x16x32_bf16 v[188:191], a[184:187], a[56:59], v[188:191]\n"
    "ds_read_b128 a[176:179], v242 offset:10240\n"
    "v_mfma_f32_16x16x32_bf16 v[196:199], a[184:187], a[88:91], v[196:199]\n"
    "v_mfma_f32_16x16x32_bf16 v[196:199], a[188:191], a[92:95], v[196:199]\n"
    "v_mfma_f32_16x16x32_bf16 v[204:207], a[188:191], a[124:127], v[204:207]\n"
    "buffer_load_dwordx4 v248, s[8:11], s40 offen offset:1024 sc1 lds\n"
    "v_mfma_f32_16x16x32_bf16 v[204:207], a[184:187], a[120:123], v[204:207]\n"
    "ds_read2st64_b32 v[146:147], v249 offset0:2 offset1:3\n"
    "v_mfma_f32_16x16x32_bf16 v[212:215], a[184:187], a[152:155], v[212:215]\n"
    "v_mfma_f32_16x16x32_bf16 v[212:215], a[188:191], a[156:159], v[212:215]\n"
    "ds_read_b128 a[180:183], v243 offset:10240\n"
    "v_mfma_f32_16x16x32_bf16 v[216:219], a[160:163], a[128:131], v[220:223]\n"
    "buffer_load_dwordx4 v248, s[8:11], s40 offen offset:2048 sc1 lds\n"
    "v_mfma_f32_16x16x32_bf16 v[216:219], a[164:167], a[132:135], v[216:219]\n"
    "ds_read2st64_b32 v[152:153], v249 offset0:4 offset1:5\n"
    "v_mfma_f32_16x16x32_bf16 v[208:211], a[164:167], a[100:103], v[220:223]\n"
    "v_exp_f32_e32 v180, v180\n"
    "v_mfma_f32_16x16x32_bf16 v[208:211], a[160:163], a[96:99], v[208:211]\n"
    "ds_read_b128 a[184:187], v242 offset:14336\n"
    "v_mfma_f32_16x16x32_bf16 v[200:203], a[160:163], a[64:67], v[220:223]\n"
    "buffer_load_dwordx4 v248, s[8:11], s40 offen offset:3072 sc1 lds\n"
    "v_mfma_f32_16x16x32_bf16 v[200:203], a[164:167], a[68:71], v[200:203]\n"
    "s_add_u32 m0, s43, 28672\n"
    "ds_read2st64_b32 v[154:155], v249 offset0:6 offset1:7\n"
    "v_mfma_f32_16x16x32_bf16 v[192:195], a[164:167], a[36:39], v[220:223]\n"
    "s_add_u32 s40, s40, 0x8000\n"
    "ds_read2st64_b32 v[160:161], v249 offset0:8 offset1:9\n"
    "v_mfma_f32_16x16x32_bf16 v[192:195], a[160:163], a[32:35], v[192:195]\n"
    "ds_read2st64_b32 v[162:163], v249 offset0:10 offset1:11\n"
    "v_mfma_f32_16x16x32_bf16 v[184:187], a[160:163], a[0:3], v[220:223]\n"
    "buffer_load_dwordx4 v248, s[8:11], s41 offen offset:0 sc1 lds\n"
    "v_mfma_f32_16x16x32_bf16 v[184:187], a[164:167], a[4:7], v[184:187]\n"
    "s_waitcnt vmcnt(17)\n"
    "ds_read_b128 a[188:191], v243 offset:14336\n"
    "v_smfmac_f32_16x16x64_bf16 v[224:227], a[232:235], a[200:207], v238\n"
    "s_waitcnt lgkmcnt(10)\n"
    "v_mad_u32_u16 v234, v229, s90, v239 op_sel:[0,0,0,0]\n"
    "v_mfma_f32_16x16x32_bf16 v[184:187], a[168:171], a[8:11], v[184:187]\n"
    "s_bitcmp1_b32 s86, 11\n"
    "buffer_load_dwordx4 a[200:203], v234, s[20:23], 0 offen sc1\n"
    "v_mad_u32_u16 v235, v229, s90, v239 op_sel:[1,0,0,0]\n"
    "v_mfma_f32_16x16x32_bf16 v[184:187], a[172:175], a[12:15], v[184:187]\n"
    "s_cselect_b64 exec, -1, 0\n"
    "v_cvt_pk_bf16_f32 v236, v224, v225\n"
    "v_cvt_pk_bf16_f32 v237, v226, v227\n"
    "v_mfma_f32_16x16x32_bf16 v[192:195], a[172:175], a[44:47], v[192:195]\n"
    "s_cselect_b32 s88, 512, 0\n"
    "v_mov_b64_e32 v[224:225], 0\n"
    "v_mov_b64_e32 v[226:227], 0\n"
    "v_mfma_f32_16x16x32_bf16 v[192:195], a[168:171], a[40:43], v[192:195]\n"
    "buffer_store_dwordx2 v[236:237], v252, s[8:11], s87 offen sc1\n"
    "v_mfma_f32_16x16x32_bf16 v[200:203], a[168:171], a[72:75], v[200:203]\n"
    "s_mov_b64 exec, -1\n"
    "ds_read_b64_tr_b16 a[160:161], v244 offset:0\n"
    "v_mfma_f32_16x16x32_bf16 v[200:203], a[172:175], a[76:79], v[200:203]\n"
    "s_add_u32 s87, s87, s88\n"
    "ds_read_b64_tr_b16 a[162:163], v244 offset:2048\n"
    "v_mfma_f32_16x16x32_bf16 v[208:211], a[172:175], a[108:111], v[208:211]\n"
    "buffer_load_dwordx4 v248, s[8:11], s41 offen offset:1024 sc1 lds\n"
    "v_mfma_f32_16x16x32_bf16 v[208:211], a[168:171], a[104:107], v[208:211]\n"
    "ds_read_b64_tr_b16 a[164:165], v244 offset:32768\n"
    "v_mfma_f32_16x16x32_bf16 v[216:219], a[168:171], a[136:139], v[216:219]\n"
    "ds_read2st64_b32 v[168:169], v249 offset0:12 offset1:13\n"
    "v_mfma_f32_16x16x32_bf16 v[216:219], a[172:175], a[140:143], v[216:219]\n"
    "s_waitcnt lgkmcnt(10)\n"
    "ds_read2st64_b32 v[170:171], v249 offset0:14 offset1:15\n"
    "v_mfma_f32_16x16x32_bf16 v[216:219], a[176:179], a[144:147], v[216:219]\n"
    "buffer_load_dwordx4 v248, s[8:11], s41 offen offset:2048 sc1 lds\n"
    "v_mfma_f32_16x16x32_bf16 v[216:219], a[180:183], a[148:151], v[216:219]\n"
    "ds_read_b64_tr_b16 a[166:167], v244 offset:34816\n"
    "v_mfma_f32_16x16x32_bf16 v[208:211], a[180:183], a[116:119], v[208:211]\n"
    "ds_read_b64_tr_b16 a[168:169], v245 offset:0\n"
    "v_mfma_f32_16x16x32_bf16 v[208:211], a[176:179], a[112:115], v[208:211]\n"
    "v_exp_f32_e32 v181, v181\n"
    "v_mfma_f32_16x16x32_bf16 v[200:203], a[176:179], a[80:83], v[200:203]\n"
    "buffer_load_dwordx4 v248, s[8:11], s41 offen offset:3072 sc1 lds\n"
    "v_mfma_f32_16x16x32_bf16 v[200:203], a[180:183], a[84:87], v[200:203]\n"
    "ds_read_b64_tr_b16 a[170:171], v245 offset:2048\n"
    "v_mfma_f32_16x16x32_bf16 v[192:195], a[180:183], a[52:55], v[192:195]\n"
    "s_add_u32 s41, s41, 0x8000\n"
    "ds_read2st64_b32 v[176:177], v249 offset0:16 offset1:17\n"
    "v_mfma_f32_16x16x32_bf16 v[192:195], a[176:179], a[48:51], v[192:195]\n"
    "buffer_load_dwordx4 a[204:207], v235, s[20:23], 0 offen sc1\n"
    "v_mfma_f32_16x16x32_bf16 v[184:187], a[176:179], a[16:19], v[184:187]\n"
    "ds_read_b64_tr_b16 a[172:173], v245 offset:32768\n"
    "v_mfma_f32_16x16x32_bf16 v[184:187], a[180:183], a[20:23], v[184:187]\n"
    "s_waitcnt lgkmcnt(10)\n"
    "ds_read2st64_b32 v[178:179], v249 offset0:18 offset1:19\n"
    "v_mfma_f32_16x16x32_bf16 v[184:187], a[184:187], a[24:27], v[184:187]\n"
    "ds_read_b64_tr_b16 a[176:177], v246 offset:0\n"
    "v_mfma_f32_16x16x32_bf16 v[184:187], a[188:191], a[28:31], v[184:187]\n"
    "ds_read_b64_tr_b16 a[174:175], v245 offset:34816\n"
    "v_mfma_f32_16x16x32_bf16 v[192:195], a[188:191], a[60:63], v[192:195]\n"
    "v_exp_f32_e32 v182, v182\n"
    "v_mfma_f32_16x16x32_bf16 v[192:195], a[184:187], a[56:59], v[192:195]\n"
    "v_exp_f32_e32 v183, v183\n"
    "v_mfma_f32_16x16x32_bf16 v[200:203], a[184:187], a[88:91], v[200:203]\n"
    "ds_read_b64_tr_b16 a[178:179], v246 offset:2048\n"
    "v_cvt_pk_bf16_f32 v180, v180, v181\n"
    "v_mfma_f32_16x16x32_bf16 v[200:203], a[188:191], a[92:95], v[200:203]\n"
    "v_exp_f32_e32 v188, v188\n"
    "v_mfma_f32_16x16x32_bf16 v[208:211], a[188:191], a[124:127], v[208:211]\n"
    "v_exp_f32_e32 v189, v189\n"
    "v_mfma_f32_16x16x32_bf16 v[208:211], a[184:187], a[120:123], v[208:211]\n"
    "v_cvt_pk_bf16_f32 v181, v182, v183\n"
    "v_mfma_f32_16x16x32_bf16 v[216:219], a[184:187], a[152:155], v[216:219]\n"
    "ds_read_b64_tr_b16 a[180:181], v246 offset:32768\n"
    "v_mfma_f32_16x16x32_bf16 v[216:219], a[188:191], a[156:159], v[216:219]\n"
    "s_waitcnt vmcnt(20)\n"
    "ds_read_b64_tr_b16 a[182:183], v246 offset:34816\n"
    "v_smfmac_f32_16x16x64_bf16 v[224:227], a[232:235], a[208:215], v238\n"
    "s_waitcnt lgkmcnt(5)\n"
    "v_mad_u32_u16 v234, v230, s90, v239 op_sel:[0,0,0,0]\n"
    "v_mfma_f32_16x16x32_bf16 a[236:239], a[160:163], v[140:143], a[236:239]\n"
    "s_bitcmp1_b32 s86, 12\n"
    "buffer_load_dwordx4 a[208:211], v234, s[20:23], 0 offen sc1\n"
    "v_mad_u32_u16 v235, v230, s90, v239 op_sel:[1,0,0,0]\n"
    "v_mfma_f32_16x16x32_bf16 a[236:239], a[164:167], v[144:147], a[236:239]\n"
    "s_cselect_b64 exec, -1, 0\n"
    "v_cvt_pk_bf16_f32 v236, v224, v225\n"
    "v_cvt_pk_bf16_f32 v237, v226, v227\n"
    "v_mfma_f32_16x16x32_bf16 a[240:243], a[164:167], v[152:155], a[240:243]\n"
    "s_cselect_b32 s88, 512, 0\n"
    "v_mov_b64_e32 v[224:225], 0\n"
    "v_mov_b64_e32 v[226:227], 0\n"
    "v_mfma_f32_16x16x32_bf16 a[240:243], a[160:163], v[148:151], a[240:243]\n"
    "buffer_store_dwordx2 v[236:237], v252, s[8:11], s87 offen sc1\n"
    "v_mfma_f32_16x16x32_bf16 a[244:247], a[160:163], v[156:159], a[244:247]\n"
    "s_mov_b64 exec, -1\n"
    "ds_read_b64_tr_b16 a[184:185], v247 offset:0\n"
    "v_mfma_f32_16x16x32_bf16 a[244:247], a[164:167], v[160:163], a[244:247]\n"
    "s_add_u32 s87, s87, s88\n"
    "ds_read_b64_tr_b16 a[186:187], v247 offset:2048\n"
    "v_mfma_f32_16x16x32_bf16 a[248:251], a[164:167], v[168:171], a[248:251]\n"
    "buffer_load_dwordx4 a[212:215], v235, s[20:23], 0 offen sc1\n"
    "v_mfma_f32_16x16x32_bf16 a[248:251], a[160:163], v[164:167], a[248:251]\n"
    "ds_read_b64_tr_b16 a[188:189], v247 offset:32768\n"
    "v_mfma_f32_16x16x32_bf16 a[252:255], a[160:163], v[172:175], a[252:255]\n"
    "v_exp_f32_e32 v190, v190\n"
    "v_mfma_f32_16x16x32_bf16 a[252:255], a[164:167], v[176:179], a[252:255]\n"
    "s_waitcnt lgkmcnt(6)\n"
    "v_exp_f32_e32 v191, v191\n"
    "v_mfma_f32_16x16x32_bf16 v[16:19], a[172:175], v[176:179], v[16:19]\n"
    "ds_read_b64_tr_b16 a[190:191], v247 offset:34816\n"
    "v_cvt_pk_bf16_f32 v188, v188, v189\n"
    "v_mfma_f32_16x16x32_bf16 v[16:19], a[168:171], v[172:175], v[16:19]\n"
    "ds_read_b64_tr_b16 a[160:161], v244 offset:4096\n"
    "v_mfma_f32_16x16x32_bf16 v[12:15], a[168:171], v[164:167], v[12:15]\n"
    "v_exp_f32_e32 v196, v196\n"
    "v_mfma_f32_16x16x32_bf16 v[12:15], a[172:175], v[168:171], v[12:15]\n"
    "v_exp_f32_e32 v197, v197\n"
    "v_mfma_f32_16x16x32_bf16 v[8:11], a[172:175], v[160:163], v[8:11]\n"
    "ds_read_b64_tr_b16 a[162:163], v244 offset:6144\n"
    "v_cvt_pk_bf16_f32 v189, v190, v191\n"
    "v_mfma_f32_16x16x32_bf16 v[8:11], a[168:171], v[156:159], v[8:11]\n"
    "v_exp_f32_e32 v198, v198\n"
    "v_mfma_f32_16x16x32_bf16 v[4:7], a[168:171], v[148:151], v[4:7]\n"
    "v_exp_f32_e32 v199, v199\n"
    "v_mfma_f32_16x16x32_bf16 v[4:7], a[172:175], v[152:155], v[4:7]\n"
    "v_cvt_pk_bf16_f32 v196, v196, v197\n"
    "v_mfma_f32_16x16x32_bf16 v[0:3], a[172:175], v[144:147], v[0:3]\n"
    "ds_read_b64_tr_b16 a[164:165], v244 offset:36864\n"
    "v_mfma_f32_16x16x32_bf16 v[0:3], a[168:171], v[140:143], v[0:3]\n"
    "s_waitcnt lgkmcnt(7)\n"
    "v_exp_f32_e32 v204, v204\n"
    "v_mfma_f32_16x16x32_bf16 v[20:23], a[176:179], v[140:143], v[20:23]\n"
    "ds_read_b64_tr_b16 a[168:169], v245 offset:4096\n"
    "v_mfma_f32_16x16x32_bf16 v[20:23], a[180:183], v[144:147], v[20:23]\n"
    "ds_read_b64_tr_b16 a[166:167], v244 offset:38912\n"
    "v_mfma_f32_16x16x32_bf16 v[24:27], a[180:183], v[152:155], v[24:27]\n"
    "v_exp_f32_e32 v205, v205\n"
    "v_mfma_f32_16x16x32_bf16 v[24:27], a[176:179], v[148:151], v[24:27]\n"
    "v_cvt_pk_bf16_f32 v197, v198, v199\n"
    "v_mfma_f32_16x16x32_bf16 v[28:31], a[176:179], v[156:159], v[28:31]\n"
    "ds_read_b64_tr_b16 a[170:171], v245 offset:6144\n"
    "v_mfma_f32_16x16x32_bf16 v[28:31], a[180:183], v[160:163], v[28:31]\n"
    "v_exp_f32_e32 v206, v206\n"
    "v_mfma_f32_16x16x32_bf16 v[32:35], a[180:183], v[168:171], v[32:35]\n"
    "v_exp_f32_e32 v207, v207\n"
    "v_mfma_f32_16x16x32_bf16 v[32:35], a[176:179], v[164:167], v[32:35]\n"
    "v_cvt_pk_bf16_f32 v204, v204, v205\n"
    "v_mfma_f32_16x16x32_bf16 v[36:39], a[176:179], v[172:175], v[36:39]\n"
    "ds_read_b64_tr_b16 a[172:173], v245 offset:36864\n"
    "v_mfma_f32_16x16x32_bf16 v[36:39], a[180:183], v[176:179], v[36:39]\n"
    "s_waitcnt vmcnt(20)\n"
    "ds_read_b64_tr_b16 a[174:175], v245 offset:38912\n"
    "v_smfmac_f32_16x16x64_bf16 v[224:227], a[232:235], a[216:223], v238\n"
    "s_waitcnt lgkmcnt(8)\n"
    "v_mad_u32_u16 v234, v231, s90, v239 op_sel:[0,0,0,0]\n"
    "v_mfma_f32_16x16x32_bf16 v[56:59], a[188:191], v[176:179], v[56:59]\n"
    "s_bitcmp1_b32 s86, 13\n"
    "buffer_load_dwordx4 a[216:219], v234, s[20:23], 0 offen sc1\n"
    "v_mad_u32_u16 v235, v231, s90, v239 op_sel:[1,0,0,0]\n"
    "v_mfma_f32_16x16x32_bf16 v[56:59], a[184:187], v[172:175], v[56:59]\n"
    "s_cselect_b64 exec, -1, 0\n"
    "v_cvt_pk_bf16_f32 v236, v224, v225\n"
    "v_cvt_pk_bf16_f32 v237, v226, v227\n"
    "v_mfma_f32_16x16x32_bf16 v[52:55], a[184:187], v[164:167], v[52:55]\n"
    "s_cselect_b32 s88, 512, 0\n"
    "v_mov_b64_e32 v[224:225], 0\n"
    "v_mov_b64_e32 v[226:227], 0\n"
    "v_mfma_f32_16x16x32_bf16 v[52:55], a[188:191], v[168:171], v[52:55]\n"
    "buffer_store_dwordx2 v[236:237], v252, s[8:11], s87 offen sc1\n"
    "v_mfma_f32_16x16x32_bf16 v[48:51], a[188:191], v[160:163], v[48:51]\n"
    "s_mov_b64 exec, -1\n"
    "ds_read_b64_tr_b16 a[176:177], v246 offset:4096\n"
    "v_mfma_f32_16x16x32_bf16 v[48:51], a[184:187], v[156:159], v[48:51]\n"
    "s_add_u32 s87, s87, s88\n"
    "ds_read_b64_tr_b16 a[178:179], v246 offset:6144\n"
    "v_mfma_f32_16x16x32_bf16 v[44:47], a[184:187], v[148:151], v[44:47]\n"
    "buffer_load_dwordx4 a[220:223], v235, s[20:23], 0 offen sc1\n"
    "v_mfma_f32_16x16x32_bf16 v[44:47], a[188:191], v[152:155], v[44:47]\n"
    "ds_read_b64_tr_b16 a[180:181], v246 offset:36864\n"
    "v_mfma_f32_16x16x32_bf16 v[40:43], a[188:191], v[144:147], v[40:43]\n"
    "v_exp_f32_e32 v212, v212\n"
    "v_mfma_f32_16x16x32_bf16 v[40:43], a[184:187], v[140:143], v[40:43]\n"
    "s_waitcnt lgkmcnt(6)\n"
    "v_exp_f32_e32 v213, v213\n"
    "v_mfma_f32_16x16x32_bf16 v[60:63], a[160:163], v[140:143], v[60:63]\n"
    "ds_read_b64_tr_b16 a[182:183], v246 offset:38912\n"
    "v_cvt_pk_bf16_f32 v205, v206, v207\n"
    "v_mfma_f32_16x16x32_bf16 v[60:63], a[164:167], v[144:147], v[60:63]\n"
    "ds_read_b64_tr_b16 a[184:185], v247 offset:4096\n"
    "v_mfma_f32_16x16x32_bf16 v[64:67], a[164:167], v[152:155], v[64:67]\n"
    "ds_read_b64_tr_b16 a[186:187], v247 offset:6144\n"
    "v_mfma_f32_16x16x32_bf16 v[64:67], a[160:163], v[148:151], v[64:67]\n"
    "v_exp_f32_e32 v214, v214\n"
    "v_mfma_f32_16x16x32_bf16 v[68:71], a[160:163], v[156:159], v[68:71]\n"
    "v_exp_f32_e32 v215, v215\n"
    "v_mfma_f32_16x16x32_bf16 v[68:71], a[164:167], v[160:163], v[68:71]\n"
    "ds_read_b64_tr_b16 a[188:189], v247 offset:36864\n"
    "v_cvt_pk_bf16_f32 v212, v212, v213\n"
    "v_mfma_f32_16x16x32_bf16 v[72:75], a[164:167], v[168:171], v[72:75]\n"
    "ds_read_b64_tr_b16 a[190:191], v247 offset:38912\n"
    "v_cvt_pk_bf16_f32 v213, v214, v215\n"
    "v_mfma_f32_16x16x32_bf16 v[72:75], a[160:163], v[164:167], v[72:75]\n"
    "s_waitcnt vmcnt(30)\n"
    "v_mfma_f32_16x16x32_bf16 v[76:79], a[160:163], v[172:175], v[76:79]\n"
    "s_barrier\n"
    "v_mfma_f32_16x16x32_bf16 v[76:79], a[164:167], v[176:179], v[76:79]\n"
    "s_waitcnt lgkmcnt(8)\n"
    "s_mov_b32 m0, s42\n"
    "v_mfma_f32_16x16x32_bf16 v[96:99], a[172:175], v[176:179], v[96:99]\n"
    "v_exp_f32_e32 v184, v184\n"
    "v_mfma_f32_16x16x32_bf16 v[96:99], a[168:171], v[172:175], v[96:99]\n"
    "v_exp_f32_e32 v185, v185\n"
    "v_mfma_f32_16x16x32_bf16 v[92:95], a[168:171], v[164:167], v[92:95]\n"
    "v_exp_f32_e32 v186, v186\n"
    "v_mfma_f32_16x16x32_bf16 v[92:95], a[172:175], v[168:171], v[92:95]\n"
    "ds_read_b128 a[160:163], v240 offset:16384\n"
    "ds_write_addtid_b32 v180 offset:0\n"
    "v_mfma_f32_16x16x32_bf16 v[88:91], a[172:175], v[160:163], v[88:91]\n"
    "v_exp_f32_e32 v187, v187\n"
    "v_mfma_f32_16x16x32_bf16 v[88:91], a[168:171], v[156:159], v[88:91]\n"
    "ds_write_addtid_b32 v181 offset:256\n"
    "v_cvt_pk_bf16_f32 v182, v184, v185\n"
    "v_mfma_f32_16x16x32_bf16 v[84:87], a[168:171], v[148:151], v[84:87]\n"
    "v_exp_f32_e32 v192, v192\n"
    "v_mfma_f32_16x16x32_bf16 v[84:87], a[172:175], v[152:155], v[84:87]\n"
    "v_exp_f32_e32 v193, v193\n"
    "v_mfma_f32_16x16x32_bf16 v[80:83], a[172:175], v[144:147], v[80:83]\n"
    "ds_write_addtid_b32 v188 offset:1024\n"
    "v_cvt_pk_bf16_f32 v183, v186, v187\n"
    "v_mfma_f32_16x16x32_bf16 v[80:83], a[168:171], v[140:143], v[80:83]\n"
    "s_waitcnt vmcnt(20)\n"
    "ds_read_b128 a[164:167], v241 offset:16384\n"
    "ds_write_addtid_b32 v189 offset:1280\n"
    "v_smfmac_f32_16x16x64_bf16 v[224:227], a[232:235], a[224:231], v238\n"
    "s_waitcnt lgkmcnt(10)\n"
    "ds_write_addtid_b32 v196 offset:2048\n"
    "v_mad_u32_u16 v234, v232, s90, v239 op_sel:[0,0,0,0]\n"
    "v_mfma_f32_16x16x32_bf16 v[100:103], a[176:179], v[140:143], v[100:103]\n"
    "s_bitcmp1_b32 s86, 14\n"
    "buffer_load_dwordx4 a[224:227], v234, s[20:23], 0 offen sc1\n"
    "v_mad_u32_u16 v235, v232, s90, v239 op_sel:[1,0,0,0]\n"
    "v_mfma_f32_16x16x32_bf16 v[100:103], a[180:183], v[144:147], v[100:103]\n"
    "s_cselect_b64 exec, -1, 0\n"
    "v_cvt_pk_bf16_f32 v236, v224, v225\n"
    "v_cvt_pk_bf16_f32 v237, v226, v227\n"
    "v_mfma_f32_16x16x32_bf16 v[104:107], a[180:183], v[152:155], v[104:107]\n"
    "s_cselect_b32 s88, 512, 0\n"
    "v_mov_b64_e32 v[224:225], 0\n"
    "v_mov_b64_e32 v[226:227], 0\n"
    "v_mfma_f32_16x16x32_bf16 v[104:107], a[176:179], v[148:151], v[104:107]\n"
    "buffer_store_dwordx2 v[236:237], v252, s[8:11], s87 offen sc1\n"
    "v_mfma_f32_16x16x32_bf16 v[108:111], a[176:179], v[156:159], v[108:111]\n"
    "s_mov_b64 exec, -1\n"
    "ds_read_b128 a[168:171], v240 offset:20480\n"
    "ds_write_addtid_b32 v197 offset:2304\n"
    "v_mfma_f32_16x16x32_bf16 v[108:111], a[180:183], v[160:163], v[108:111]\n"
    "s_add_u32 s87, s87, s88\n"
    "v_exp_f32_e32 v194, v194\n"
    "v_mfma_f32_16x16x32_bf16 v[112:115], a[180:183], v[168:171], v[112:115]\n"
    "buffer_load_dwordx4 a[228:231], v235, s[20:23], 0 offen sc1\n"
    "ds_write_addtid_b32 v204 offset:3072\n"
    "v_mfma_f32_16x16x32_bf16 v[112:115], a[176:179], v[164:167], v[112:115]\n"
    "ds_write_addtid_b32 v205 offset:3328\n"
    "v_mfma_f32_16x16x32_bf16 v[116:119], a[176:179], v[172:175], v[116:119]\n"
    "v_exp_f32_e32 v195, v195\n"
    "v_mfma_f32_16x16x32_bf16 v[116:119], a[180:183], v[176:179], v[116:119]\n"
    "s_waitcnt lgkmcnt(11)\n"
    "ds_read_b128 a[172:175], v241 offset:20480\n"
    "v_cvt_pk_bf16_f32 v190, v192, v193\n"
    "v_mfma_f32_16x16x32_bf16 v[136:139], a[188:191], v[176:179], v[136:139]\n"
    "v_exp_f32_e32 v200, v200\n"
    "v_mfma_f32_16x16x32_bf16 v[136:139], a[184:187], v[172:175], v[136:139]\n"
    "v_exp_f32_e32 v201, v201\n"
    "v_mfma_f32_16x16x32_bf16 v[132:135], a[184:187], v[164:167], v[132:135]\n"
    "ds_read_b128 a[176:179], v242 offset:16384\n"
    "v_cvt_pk_bf16_f32 v191, v194, v195\n"
    "v_mfma_f32_16x16x32_bf16 v[132:135], a[188:191], v[168:171], v[132:135]\n"
    "v_exp_f32_e32 v202, v202\n"
    "v_mfma_f32_16x16x32_bf16 v[128:131], a[188:191], v[160:163], v[128:131]\n"
    "v_exp_f32_e32 v203, v203\n"
    "v_mfma_f32_16x16x32_bf16 v[128:131], a[184:187], v[156:159], v[128:131]\n"
    "ds_write_addtid_b32 v212 offset:4096\n"
    "v_cvt_pk_bf16_f32 v198, v200, v201\n"
    "v_mfma_f32_16x16x32_bf16 v[124:127], a[184:187], v[148:151], v[124:127]\n"
    "v_exp_f32_e32 v208, v208\n"
    "v_mfma_f32_16x16x32_bf16 v[124:127], a[188:191], v[152:155], v[124:127]\n"
    "ds_read_b128 a[180:183], v243 offset:16384\n"
    "ds_write_addtid_b32 v213 offset:4352\n"
    "v_mfma_f32_16x16x32_bf16 v[120:123], a[188:191], v[144:147], v[120:123]\n"
    "v_exp_f32_e32 v209, v209\n"
    "v_mfma_f32_16x16x32_bf16 v[120:123], a[184:187], v[140:143], v[120:123]\n"
    "s_waitcnt lgkmcnt(11)\n"
    "ds_write_addtid_b32 v182 offset:512\n"
    "v_cvt_pk_bf16_f32 v199, v202, v203\n"
    "v_mfma_f32_16x16x32_bf16 v[172:175], a[160:163], a[128:131], v[220:223]\n"
    "ds_write_addtid_b32 v183 offset:768\n"
    "v_mfma_f32_16x16x32_bf16 v[172:175], a[164:167], a[132:135], v[172:175]\n"
    "v_exp_f32_e32 v210, v210\n"
    "v_mfma_f32_16x16x32_bf16 v[164:167], a[164:167], a[100:103], v[220:223]\n"
    "ds_write_addtid_b32 v190 offset:1536\n"
    "v_mfma_f32_16x16x32_bf16 v[164:167], a[160:163], a[96:99], v[164:167]\n"
    "ds_read_b128 a[184:187], v242 offset:20480\n"
    "v_mfma_f32_16x16x32_bf16 v[156:159], a[160:163], a[64:67], v[220:223]\n"
    "v_exp_f32_e32 v211, v211\n"
    "v_mfma_f32_16x16x32_bf16 v[156:159], a[164:167], a[68:71], v[156:159]\n"
    "ds_write_addtid_b32 v191 offset:1792\n"
    "v_cvt_pk_bf16_f32 v206, v208, v209\n"
    "v_mfma_f32_16x16x32_bf16 v[148:151], a[164:167], a[36:39], v[220:223]\n"
    "v_exp_f32_e32 v216, v216\n"
    "v_mfma_f32_16x16x32_bf16 v[148:151], a[160:163], a[32:35], v[148:151]\n"
    "v_exp_f32_e32 v217, v217\n"
    "v_mfma_f32_16x16x32_bf16 v[140:143], a[160:163], a[0:3], v[220:223]\n"
    "ds_write_addtid_b32 v198 offset:2560\n"
    "v_cvt_pk_bf16_f32 v207, v210, v211\n"
    "v_mfma_f32_16x16x32_bf16 v[140:143], a[164:167], a[4:7], v[140:143]\n"
    "s_waitcnt vmcnt(20)\n"
    "v_exp_f32_e32 v218, v218\n"
    "v_smfmac_f32_16x16x64_bf16 v[224:227], a[232:235], a[192:199], v238\n"
    "s_waitcnt lgkmcnt(10)\n"
    "ds_read_b128 a[188:191], v243 offset:20480\n"
    "v_mfma_f32_16x16x32_bf16 v[140:143], a[168:171], a[8:11], v[140:143]\n"
    "s_bitcmp1_b32 s86, 15\n"
    "ds_read_b128 a[160:163], v240 offset:18432\n"
    "v_mfma_f32_16x16x32_bf16 v[140:143], a[172:175], a[12:15], v[140:143]\n"
    "s_cselect_b64 exec, -1, 0\n"
    "v_cvt_pk_bf16_f32 v236, v224, v225\n"
    "v_cvt_pk_bf16_f32 v237, v226, v227\n"
    "v_mfma_f32_16x16x32_bf16 v[148:151], a[172:175], a[44:47], v[148:151]\n"
    "s_cselect_b32 s88, 512, 0\n"
    "v_mov_b64_e32 v[224:225], 0\n"
    "v_mov_b64_e32 v[226:227], 0\n"
    "v_mfma_f32_16x16x32_bf16 v[148:151], a[168:171], a[40:43], v[148:151]\n"
    "buffer_store_dwordx2 v[236:237], v252, s[8:11], s87 offen sc1\n"
    "v_mfma_f32_16x16x32_bf16 v[156:159], a[168:171], a[72:75], v[156:159]\n"
    "s_mov_b64 exec, -1\n"
    "ds_read2_b32 v[228:229], v251 offset0:30 offset1:32\n"
    "v_mfma_f32_16x16x32_bf16 v[156:159], a[172:175], a[76:79], v[156:159]\n"
    "s_add_u32 s87, s87, s88\n"
    "ds_read2_b32 v[230:231], v251 offset0:34 offset1:36\n"
    "v_mfma_f32_16x16x32_bf16 v[164:167], a[172:175], a[108:111], v[164:167]\n"
    "ds_read_b32 v232, v251 offset:152\n"
    "v_mfma_f32_16x16x32_bf16 v[164:167], a[168:171], a[104:107], v[164:167]\n"
    "v_exp_f32_e32 v219, v219\n"
    "v_mfma_f32_16x16x32_bf16 v[172:175], a[168:171], a[136:139], v[172:175]\n"
    "ds_write_addtid_b32 v199 offset:2816\n"
    "v_cvt_pk_bf16_f32 v214, v216, v217\n"
    "v_mfma_f32_16x16x32_bf16 v[172:175], a[172:175], a[140:143], v[172:175]\n"
    "s_waitcnt lgkmcnt(13)\n"
    "ds_read_b128 a[164:167], v241 offset:18432\n"
    "v_mfma_f32_16x16x32_bf16 v[172:175], a[176:179], a[144:147], v[172:175]\n"
    "ds_write_addtid_b32 v206 offset:3584\n"
    "v_cvt_pk_bf16_f32 v215, v218, v219\n"
    "v_mfma_f32_16x16x32_bf16 v[172:175], a[180:183], a[148:151], v[172:175]\n"
    "ds_read_b128 a[168:171], v240 offset:22528\n"
    "v_mfma_f32_16x16x32_bf16 v[164:167], a[180:183], a[116:119], v[164:167]\n"
    "s_waitcnt lgkmcnt(6)\n"
    "ds_write_addtid_b32 v207 offset:3840\n"
    "v_mad_u32_u16 v234, v228, s90, v239 op_sel:[0,0,0,0]\n"
    "v_mfma_f32_16x16x32_bf16 v[164:167], a[176:179], a[112:115], v[164:167]\n"
    "buffer_load_dwordx4 a[192:195], v234, s[20:23], 0 offen sc1\n"
    "v_mad_u32_u16 v235, v228, s90, v239 op_sel:[1,0,0,0]\n"
    "v_mfma_f32_16x16x32_bf16 v[156:159], a[176:179], a[80:83], v[156:159]\n"
    "ds_write_addtid_b32 v214 offset:4608\n"
    "v_add_u32_e32 v251, 160, v251\n"
    "v_mfma_f32_16x16x32_bf16 v[156:159], a[180:183], a[84:87], v[156:159]\n"
    "ds_write_addtid_b32 v215 offset:4864\n"
    "v_mfma_f32_16x16x32_bf16 v[148:151], a[180:183], a[52:55], v[148:151]\n"
    "buffer_load_dwordx4 a[196:199], v235, s[20:23], 0 offen sc1\n"
    "v_mfma_f32_16x16x32_bf16 v[148:151], a[176:179], a[48:51], v[148:151]\n"
    "v_mfma_f32_16x16x32_bf16 v[140:143], a[176:179], a[16:19], v[140:143]\n"
    "s_waitcnt lgkmcnt(0)\n"
    "v_mfma_f32_16x16x32_bf16 v[140:143], a[180:183], a[20:23], v[140:143]\n"
    "s_barrier\n"
    "v_mfma_f32_16x16x32_bf16 v[140:143], a[184:187], a[24:27], v[140:143]\n"
    "s_add_u32 m0, s43, 0\n"
    "ds_read_b128 a[172:175], v241 offset:22528\n"
    "v_mfma_f32_16x16x32_bf16 v[140:143], a[188:191], a[28:31], v[140:143]\n"
    "ds_read2st64_b32 v[184:185], v249 offset0:0 offset1:1\n"
    "v_mfma_f32_16x16x32_bf16 v[148:151], a[188:191], a[60:63], v[148:151]\n"
    "buffer_load_dwordx4 v248, s[8:11], s40 offen offset:0 sc1 lds\n"
    "v_mfma_f32_16x16x32_bf16 v[148:151], a[184:187], a[56:59], v[148:151]\n"
    "ds_read_b128 a[176:179], v242 offset:18432\n"
    "v_mfma_f32_16x16x32_bf16 v[156:159], a[184:187], a[88:91], v[156:159]\n"
    "v_mfma_f32_16x16x32_bf16 v[156:159], a[188:191], a[92:95], v[156:159]\n"
    "v_mfma_f32_16x16x32_bf16 v[164:167], a[188:191], a[124:127], v[164:167]\n"
    "buffer_load_dwordx4 v248, s[8:11], s40 offen offset:1024 sc1 lds\n"
    "v_mfma_f32_16x16x32_bf16 v[164:167], a[184:187], a[120:123], v[164:167]\n"
    "ds_read2st64_b32 v[186:187], v249 offset0:2 offset1:3\n"
    "v_mfma_f32_16x16x32_bf16 v[172:175], a[184:187], a[152:155], v[172:175]\n"
    "v_mfma_f32_16x16x32_bf16 v[172:175], a[188:191], a[156:159], v[172:175]\n"
    "ds_read_b128 a[180:183], v243 offset:18432\n"
    "v_mfma_f32_16x16x32_bf16 v[176:179], a[160:163], a[128:131], v[220:223]\n"
    "buffer_load_dwordx4 v248, s[8:11], s40 offen offset:2048 sc1 lds\n"
    "v_mfma_f32_16x16x32_bf16 v[176:179], a[164:167], a[132:135], v[176:179]\n"
    "ds_read2st64_b32 v[192:193], v249 offset0:4 offset1:5\n"
    "v_mfma_f32_16x16x32_bf16 v[168:171], a[164:167], a[100:103], v[220:223]\n"
    "v_exp_f32_e32 v140, v140\n"
    "v_mfma_f32_16x16x32_bf16 v[168:171], a[160:163], a[96:99], v[168:171]\n"
    "ds_read_b128 a[184:187], v242 offset:22528\n"
    "v_mfma_f32_16x16x32_bf16 v[160:163], a[160:163], a[64:67], v[220:223]\n"
    "buffer_load_dwordx4 v248, s[8:11], s40 offen offset:3072 sc1 lds\n"
    "v_mfma_f32_16x16x32_bf16 v[160:163], a[164:167], a[68:71], v[160:163]\n"
    "s_add_u32 m0, s43, 4096\n"
    "ds_read2st64_b32 v[194:195], v249 offset0:6 offset1:7\n"
    "v_mfma_f32_16x16x32_bf16 v[152:155], a[164:167], a[36:39], v[220:223]\n"
    "s_add_u32 s40, s40, 0x8000\n"
    "ds_read2st64_b32 v[200:201], v249 offset0:8 offset1:9\n"
    "v_mfma_f32_16x16x32_bf16 v[152:155], a[160:163], a[32:35], v[152:155]\n"
    "ds_read2st64_b32 v[202:203], v249 offset0:10 offset1:11\n"
    "v_mfma_f32_16x16x32_bf16 v[144:147], a[160:163], a[0:3], v[220:223]\n"
    "buffer_load_dwordx4 v248, s[8:11], s41 offen offset:0 sc1 lds\n"
    "v_mfma_f32_16x16x32_bf16 v[144:147], a[164:167], a[4:7], v[144:147]\n"
    "s_waitcnt vmcnt(17)\n"
    "ds_read_b128 a[188:191], v243 offset:22528\n"
    "v_smfmac_f32_16x16x64_bf16 v[224:227], a[232:235], a[200:207], v238\n"
    "s_waitcnt lgkmcnt(10)\n"
    "v_mad_u32_u16 v234, v229, s90, v239 op_sel:[0,0,0,0]\n"
    "v_mfma_f32_16x16x32_bf16 v[144:147], a[168:171], a[8:11], v[144:147]\n"
    "s_bitcmp1_b32 s86, 16\n"
    "buffer_load_dwordx4 a[200:203], v234, s[20:23], 0 offen sc1\n"
    "v_mad_u32_u16 v235, v229, s90, v239 op_sel:[1,0,0,0]\n"
    "v_mfma_f32_16x16x32_bf16 v[144:147], a[172:175], a[12:15], v[144:147]\n"
    "s_cselect_b64 exec, -1, 0\n"
    "v_cvt_pk_bf16_f32 v236, v224, v225\n"
    "v_cvt_pk_bf16_f32 v237, v226, v227\n"
    "v_mfma_f32_16x16x32_bf16 v[152:155], a[172:175], a[44:47], v[152:155]\n"
    "s_cselect_b32 s88, 512, 0\n"
    "v_mov_b64_e32 v[224:225], 0\n"
    "v_mov_b64_e32 v[226:227], 0\n"
    "v_mfma_f32_16x16x32_bf16 v[152:155], a[168:171], a[40:43], v[152:155]\n"
    "buffer_store_dwordx2 v[236:237], v252, s[8:11], s87 offen sc1\n"
    "v_mfma_f32_16x16x32_bf16 v[160:163], a[168:171], a[72:75], v[160:163]\n"
    "s_mov_b64 exec, -1\n"
    "ds_read_b64_tr_b16 a[160:161], v244 offset:8192\n"
    "v_mfma_f32_16x16x32_bf16 v[160:163], a[172:175], a[76:79], v[160:163]\n"
    "s_add_u32 s87, s87, s88\n"
    "ds_read_b64_tr_b16 a[162:163], v244 offset:10240\n"
    "v_mfma_f32_16x16x32_bf16 v[168:171], a[172:175], a[108:111], v[168:171]\n"
    "buffer_load_dwordx4 v248, s[8:11], s41 offen offset:1024 sc1 lds\n"
    "v_mfma_f32_16x16x32_bf16 v[168:171], a[168:171], a[104:107], v[168:171]\n"
    "ds_read_b64_tr_b16 a[164:165], v244 offset:40960\n"
    "v_mfma_f32_16x16x32_bf16 v[176:179], a[168:171], a[136:139], v[176:179]\n"
    "ds_read2st64_b32 v[208:209], v249 offset0:12 offset1:13\n"
    "v_mfma_f32_16x16x32_bf16 v[176:179], a[172:175], a[140:143], v[176:179]\n"
    "s_waitcnt lgkmcnt(10)\n"
    "ds_read2st64_b32 v[210:211], v249 offset0:14 offset1:15\n"
    "v_mfma_f32_16x16x32_bf16 v[176:179], a[176:179], a[144:147], v[176:179]\n"
    "buffer_load_dwordx4 v248, s[8:11], s41 offen offset:2048 sc1 lds\n"
    "v_mfma_f32_16x16x32_bf16 v[176:179], a[180:183], a[148:151], v[176:179]\n"
    "ds_read_b64_tr_b16 a[166:167], v244 offset:43008\n"
    "v_mfma_f32_16x16x32_bf16 v[168:171], a[180:183], a[116:119], v[168:171]\n"
    "ds_read_b64_tr_b16 a[168:169], v245 offset:8192\n"
    "v_mfma_f32_16x16x32_bf16 v[168:171], a[176:179], a[112:115], v[168:171]\n"
    "v_exp_f32_e32 v141, v141\n"
    "v_mfma_f32_16x16x32_bf16 v[160:163], a[176:179], a[80:83], v[160:163]\n"
    "buffer_load_dwordx4 v248, s[8:11], s41 offen offset:3072 sc1 lds\n"
    "v_mfma_f32_16x16x32_bf16 v[160:163], a[180:183], a[84:87], v[160:163]\n"
    "ds_read_b64_tr_b16 a[170:171], v245 offset:10240\n"
    "v_mfma_f32_16x16x32_bf16 v[152:155], a[180:183], a[52:55], v[152:155]\n"
    "s_add_u32 s41, s41, 0x8000\n"
    "ds_read2st64_b32 v[216:217], v249 offset0:16 offset1:17\n"
    "v_mfma_f32_16x16x32_bf16 v[152:155], a[176:179], a[48:51], v[152:155]\n"
    "buffer_load_dwordx4 a[204:207], v235, s[20:23], 0 offen sc1\n"
    "v_mfma_f32_16x16x32_bf16 v[144:147], a[176:179], a[16:19], v[144:147]\n"
    "ds_read_b64_tr_b16 a[172:173], v245 offset:40960\n"
    "v_mfma_f32_16x16x32_bf16 v[144:147], a[180:183], a[20:23], v[144:147]\n"
    "s_waitcnt lgkmcnt(10)\n"
    "ds_read2st64_b32 v[218:219], v249 offset0:18 offset1:19\n"
    "v_mfma_f32_16x16x32_bf16 v[144:147], a[184:187], a[24:27], v[144:147]\n"
    "ds_read_b64_tr_b16 a[176:177], v246 offset:8192\n"
    "v_mfma_f32_16x16x32_bf16 v[144:147], a[188:191], a[28:31], v[144:147]\n"
    "ds_read_b64_tr_b16 a[174:175], v245 offset:43008\n"
    "v_mfma_f32_16x16x32_bf16 v[152:155], a[188:191], a[60:63], v[152:155]\n"
    "v_exp_f32_e32 v142, v142\n"
    "v_mfma_f32_16x16x32_bf16 v[152:155], a[184:187], a[56:59], v[152:155]\n"
    "v_exp_f32_e32 v143, v143\n"
    "v_mfma_f32_16x16x32_bf16 v[160:163], a[184:187], a[88:91], v[160:163]\n"
    "ds_read_b64_tr_b16 a[178:179], v246 offset:10240\n"
    "v_cvt_pk_bf16_f32 v140, v140, v141\n"
    "v_mfma_f32_16x16x32_bf16 v[160:163], a[188:191], a[92:95], v[160:163]\n"
    "v_exp_f32_e32 v148, v148\n"
    "v_mfma_f32_16x16x32_bf16 v[168:171], a[188:191], a[124:127], v[168:171]\n"
    "v_exp_f32_e32 v149, v149\n"
    "v_mfma_f32_16x16x32_bf16 v[168:171], a[184:187], a[120:123], v[168:171]\n"
    "v_cvt_pk_bf16_f32 v141, v142, v143\n"
    "v_mfma_f32_16x16x32_bf16 v[176:179], a[184:187], a[152:155], v[176:179]\n"
    "ds_read_b64_tr_b16 a[180:181], v246 offset:40960\n"
    "v_mfma_f32_16x16x32_bf16 v[176:179], a[188:191], a[156:159], v[176:179]\n"
    "s_waitcnt vmcnt(20)\n"
    "ds_read_b64_tr_b16 a[182:183], v246 offset:43008\n"
    "v_smfmac_f32_16x16x64_bf16 v[224:227], a[232:235], a[208:215], v238\n"
    "s_waitcnt lgkmcnt(5)\n"
    "v_mad_u32_u16 v234, v230, s90, v239 op_sel:[0,0,0,0]\n"
    "v_mfma_f32_16x16x32_bf16 a[236:239], a[160:163], v[180:183], a[236:239]\n"
    "s_bitcmp1_b32 s86, 17\n"
    "buffer_load_dwordx4 a[208:211], v234, s[20:23], 0 offen sc1\n"
    "v_mad_u32_u16 v235, v230, s90, v239 op_sel:[1,0,0,0]\n"
    "v_mfma_f32_16x16x32_bf16 a[236:239], a[164:167], v[184:187], a[236:239]\n"
    "s_cselect_b64 exec, -1, 0\n"
    "v_cvt_pk_bf16_f32 v236, v224, v225\n"
    "v_cvt_pk_bf16_f32 v237, v226, v227\n"
    "v_mfma_f32_16x16x32_bf16 a[240:243], a[164:167], v[192:195], a[240:243]\n"
    "s_cselect_b32 s88, 512, 0\n"
    "v_mov_b64_e32 v[224:225], 0\n"
    "v_mov_b64_e32 v[226:227], 0\n"
    "v_mfma_f32_16x16x32_bf16 a[240:243], a[160:163], v[188:191], a[240:243]\n"
    "buffer_store_dwordx2 v[236:237], v252, s[8:11], s87 offen sc1\n"
    "v_mfma_f32_16x16x32_bf16 a[244:247], a[160:163], v[196:199], a[244:247]\n"
    "s_mov_b64 exec, -1\n"
    "ds_read_b64_tr_b16 a[184:185], v247 offset:8192\n"
    "v_mfma_f32_16x16x32_bf16 a[244:247], a[164:167], v[200:203], a[244:247]\n"
    "s_add_u32 s87, s87, s88\n"
    "ds_read_b64_tr_b16 a[186:187], v247 offset:10240\n"
    "v_mfma_f32_16x16x32_bf16 a[248:251], a[164:167], v[208:211], a[248:251]\n"
    "buffer_load_dwordx4 a[212:215], v235, s[20:23], 0 offen sc1\n"
    "v_mfma_f32_16x16x32_bf16 a[248:251], a[160:163], v[204:207], a[248:251]\n"
    "ds_read_b64_tr_b16 a[188:189], v247 offset:40960\n"
    "v_mfma_f32_16x16x32_bf16 a[252:255], a[160:163], v[212:215], a[252:255]\n"
    "v_exp_f32_e32 v150, v150\n"
    "v_mfma_f32_16x16x32_bf16 a[252:255], a[164:167], v[216:219], a[252:255]\n"
    "s_waitcnt lgkmcnt(6)\n"
    "v_exp_f32_e32 v151, v151\n"
    "v_mfma_f32_16x16x32_bf16 v[16:19], a[172:175], v[216:219], v[16:19]\n"
    "ds_read_b64_tr_b16 a[190:191], v247 offset:43008\n"
    "v_cvt_pk_bf16_f32 v148, v148, v149\n"
    "v_mfma_f32_16x16x32_bf16 v[16:19], a[168:171], v[212:215], v[16:19]\n"
    "ds_read_b64_tr_b16 a[160:161], v244 offset:12288\n"
    "v_mfma_f32_16x16x32_bf16 v[12:15], a[168:171], v[204:207], v[12:15]\n"
    "v_exp_f32_e32 v156, v156\n"
    "v_mfma_f32_16x16x32_bf16 v[12:15], a[172:175], v[208:211], v[12:15]\n"
    "v_exp_f32_e32 v157, v157\n"
    "v_mfma_f32_16x16x32_bf16 v[8:11], a[172:175], v[200:203], v[8:11]\n"
    "ds_read_b64_tr_b16 a[162:163], v244 offset:14336\n"
    "v_cvt_pk_bf16_f32 v149, v150, v151\n"
    "v_mfma_f32_16x16x32_bf16 v[8:11], a[168:171], v[196:199], v[8:11]\n"
    "v_exp_f32_e32 v158, v158\n"
    "v_mfma_f32_16x16x32_bf16 v[4:7], a[168:171], v[188:191], v[4:7]\n"
    "v_exp_f32_e32 v159, v159\n"
    "v_mfma_f32_16x16x32_bf16 v[4:7], a[172:175], v[192:195], v[4:7]\n"
    "v_cvt_pk_bf16_f32 v156, v156, v157\n"
    "v_mfma_f32_16x16x32_bf16 v[0:3], a[172:175], v[184:187], v[0:3]\n"
    "ds_read_b64_tr_b16 a[164:165], v244 offset:45056\n"
    "v_mfma_f32_16x16x32_bf16 v[0:3], a[168:171], v[180:183], v[0:3]\n"
    "s_waitcnt lgkmcnt(7)\n"
    "v_exp_f32_e32 v164, v164\n"
    "v_mfma_f32_16x16x32_bf16 v[20:23], a[176:179], v[180:183], v[20:23]\n"
    "ds_read_b64_tr_b16 a[168:169], v245 offset:12288\n"
    "v_mfma_f32_16x16x32_bf16 v[20:23], a[180:183], v[184:187], v[20:23]\n"
    "ds_read_b64_tr_b16 a[166:167], v244 offset:47104\n"
    "v_mfma_f32_16x16x32_bf16 v[24:27], a[180:183], v[192:195], v[24:27]\n"
    "v_exp_f32_e32 v165, v165\n"
    "v_mfma_f32_16x16x32_bf16 v[24:27], a[176:179], v[188:191], v[24:27]\n"
    "v_cvt_pk_bf16_f32 v157, v158, v159\n"
    "v_mfma_f32_16x16x32_bf16 v[28:31], a[176:179], v[196:199], v[28:31]\n"
    "ds_read_b64_tr_b16 a[170:171], v245 offset:14336\n"
    "v_mfma_f32_16x16x32_bf16 v[28:31], a[180:183], v[200:203], v[28:31]\n"
    "v_exp_f32_e32 v166, v166\n"
    "v_mfma_f32_16x16x32_bf16 v[32:35], a[180:183], v[208:211], v[32:35]\n"
    "v_exp_f32_e32 v167, v167\n"
    "v_mfma_f32_16x16x32_bf16 v[32:35], a[176:179], v[204:207], v[32:35]\n"
    "v_cvt_pk_bf16_f32 v164, v164, v165\n"
    "v_mfma_f32_16x16x32_bf16 v[36:39], a[176:179], v[212:215], v[36:39]\n"
    "ds_read_b64_tr_b16 a[172:173], v245 offset:45056\n"
    "v_mfma_f32_16x16x32_bf16 v[36:39], a[180:183], v[216:219], v[36:39]\n"
    "s_waitcnt vmcnt(20)\n"
    "ds_read_b64_tr_b16 a[174:175], v245 offset:47104\n"
    "v_smfmac_f32_16x16x64_bf16 v[224:227], a[232:235], a[216:223], v238\n"
    "s_waitcnt lgkmcnt(8)\n"
    "v_mad_u32_u16 v234, v231, s90, v239 op_sel:[0,0,0,0]\n"
    "v_mfma_f32_16x16x32_bf16 v[56:59], a[188:191], v[216:219], v[56:59]\n"
    "s_bitcmp1_b32 s86, 18\n"
    "buffer_load_dwordx4 a[216:219], v234, s[20:23], 0 offen sc1\n"
    "v_mad_u32_u16 v235, v231, s90, v239 op_sel:[1,0,0,0]\n"
    "v_mfma_f32_16x16x32_bf16 v[56:59], a[184:187], v[212:215], v[56:59]\n"
    "s_cselect_b64 exec, -1, 0\n"
    "v_cvt_pk_bf16_f32 v236, v224, v225\n"
    "v_cvt_pk_bf16_f32 v237, v226, v227\n"
    "v_mfma_f32_16x16x32_bf16 v[52:55], a[184:187], v[204:207], v[52:55]\n"
    "s_cselect_b32 s88, 512, 0\n"
    "v_mov_b64_e32 v[224:225], 0\n"
    "v_mov_b64_e32 v[226:227], 0\n"
    "v_mfma_f32_16x16x32_bf16 v[52:55], a[188:191], v[208:211], v[52:55]\n"
    "buffer_store_dwordx2 v[236:237], v252, s[8:11], s87 offen sc1\n"
    "v_mfma_f32_16x16x32_bf16 v[48:51], a[188:191], v[200:203], v[48:51]\n"
    "s_mov_b64 exec, -1\n"
    "ds_read_b64_tr_b16 a[176:177], v246 offset:12288\n"
    "v_mfma_f32_16x16x32_bf16 v[48:51], a[184:187], v[196:199], v[48:51]\n"
    "s_add_u32 s87, s87, s88\n"
    "ds_read_b64_tr_b16 a[178:179], v246 offset:14336\n"
    "v_mfma_f32_16x16x32_bf16 v[44:47], a[184:187], v[188:191], v[44:47]\n"
    "buffer_load_dwordx4 a[220:223], v235, s[20:23], 0 offen sc1\n"
    "v_mfma_f32_16x16x32_bf16 v[44:47], a[188:191], v[192:195], v[44:47]\n"
    "ds_read_b64_tr_b16 a[180:181], v246 offset:45056\n"
    "v_mfma_f32_16x16x32_bf16 v[40:43], a[188:191], v[184:187], v[40:43]\n"
    "v_exp_f32_e32 v172, v172\n"
    "v_mfma_f32_16x16x32_bf16 v[40:43], a[184:187], v[180:183], v[40:43]\n"
    "s_waitcnt lgkmcnt(6)\n"
    "v_exp_f32_e32 v173, v173\n"
    "v_mfma_f32_16x16x32_bf16 v[60:63], a[160:163], v[180:183], v[60:63]\n"
    "ds_read_b64_tr_b16 a[182:183], v246 offset:47104\n"
    "v_cvt_pk_bf16_f32 v165, v166, v167\n"
    "v_mfma_f32_16x16x32_bf16 v[60:63], a[164:167], v[184:187], v[60:63]\n"
    "ds_read_b64_tr_b16 a[184:185], v247 offset:12288\n"
    "v_mfma_f32_16x16x32_bf16 v[64:67], a[164:167], v[192:195], v[64:67]\n"
    "ds_read_b64_tr_b16 a[186:187], v247 offset:14336\n"
    "v_mfma_f32_16x16x32_bf16 v[64:67], a[160:163], v[188:191], v[64:67]\n"
    "v_exp_f32_e32 v174, v174\n"
    "v_mfma_f32_16x16x32_bf16 v[68:71], a[160:163], v[196:199], v[68:71]\n"
    "v_exp_f32_e32 v175, v175\n"
    "v_mfma_f32_16x16x32_bf16 v[68:71], a[164:167], v[200:203], v[68:71]\n"
    "ds_read_b64_tr_b16 a[188:189], v247 offset:45056\n"
    "v_cvt_pk_bf16_f32 v172, v172, v173\n"
    "v_mfma_f32_16x16x32_bf16 v[72:75], a[164:167], v[208:211], v[72:75]\n"
    "ds_read_b64_tr_b16 a[190:191], v247 offset:47104\n"
    "v_cvt_pk_bf16_f32 v173, v174, v175\n"
    "v_mfma_f32_16x16x32_bf16 v[72:75], a[160:163], v[204:207], v[72:75]\n"
    "s_waitcnt vmcnt(30)\n"
    "v_mfma_f32_16x16x32_bf16 v[76:79], a[160:163], v[212:215], v[76:79]\n"
    "s_barrier\n"
    "v_mfma_f32_16x16x32_bf16 v[76:79], a[164:167], v[216:219], v[76:79]\n"
    "s_waitcnt lgkmcnt(8)\n"
    "s_mov_b32 m0, s42\n"
    "v_mfma_f32_16x16x32_bf16 v[96:99], a[172:175], v[216:219], v[96:99]\n"
    "v_exp_f32_e32 v144, v144\n"
    "v_mfma_f32_16x16x32_bf16 v[96:99], a[168:171], v[212:215], v[96:99]\n"
    "v_exp_f32_e32 v145, v145\n"
    "v_mfma_f32_16x16x32_bf16 v[92:95], a[168:171], v[204:207], v[92:95]\n"
    "v_exp_f32_e32 v146, v146\n"
    "v_mfma_f32_16x16x32_bf16 v[92:95], a[172:175], v[208:211], v[92:95]\n"
    "ds_read_b128 a[160:163], v240 offset:24576\n"
    "ds_write_addtid_b32 v140 offset:0\n"
    "v_mfma_f32_16x16x32_bf16 v[88:91], a[172:175], v[200:203], v[88:91]\n"
    "v_exp_f32_e32 v147, v147\n"
    "v_mfma_f32_16x16x32_bf16 v[88:91], a[168:171], v[196:199], v[88:91]\n"
    "ds_write_addtid_b32 v141 offset:256\n"
    "v_cvt_pk_bf16_f32 v142, v144, v145\n"
    "v_mfma_f32_16x16x32_bf16 v[84:87], a[168:171], v[188:191], v[84:87]\n"
    "v_exp_f32_e32 v152, v152\n"
    "v_mfma_f32_16x16x32_bf16 v[84:87], a[172:175], v[192:195], v[84:87]\n"
    "v_exp_f32_e32 v153, v153\n"
    "v_mfma_f32_16x16x32_bf16 v[80:83], a[172:175], v[184:187], v[80:83]\n"
    "ds_write_addtid_b32 v148 offset:1024\n"
    "v_cvt_pk_bf16_f32 v143, v146, v147\n"
    "v_mfma_f32_16x16x32_bf16 v[80:83], a[168:171], v[180:183], v[80:83]\n"
    "s_waitcnt vmcnt(20)\n"
    "ds_read_b128 a[164:167], v241 offset:24576\n"
    "ds_write_addtid_b32 v149 offset:1280\n"
    "v_smfmac_f32_16x16x64_bf16 v[224:227], a[232:235], a[224:231], v238\n"
    "s_waitcnt lgkmcnt(10)\n"
    "ds_write_addtid_b32 v156 offset:2048\n"
    "v_mad_u32_u16 v234, v232, s90, v239 op_sel:[0,0,0,0]\n"
    "v_mfma_f32_16x16x32_bf16 v[100:103], a[176:179], v[180:183], v[100:103]\n"
    "s_bitcmp1_b32 s86, 19\n"
    "buffer_load_dwordx4 a[224:227], v234, s[20:23], 0 offen sc1\n"
    "v_mad_u32_u16 v235, v232, s90, v239 op_sel:[1,0,0,0]\n"
    "v_mfma_f32_16x16x32_bf16 v[100:103], a[180:183], v[184:187], v[100:103]\n"
    "s_cselect_b64 exec, -1, 0\n"
    "v_cvt_pk_bf16_f32 v236, v224, v225\n"
    "v_cvt_pk_bf16_f32 v237, v226, v227\n"
    "v_mfma_f32_16x16x32_bf16 v[104:107], a[180:183], v[192:195], v[104:107]\n"
    "s_cselect_b32 s88, 512, 0\n"
    "v_mov_b64_e32 v[224:225], 0\n"
    "v_mov_b64_e32 v[226:227], 0\n"
    "v_mfma_f32_16x16x32_bf16 v[104:107], a[176:179], v[188:191], v[104:107]\n"
    "buffer_store_dwordx2 v[236:237], v252, s[8:11], s87 offen sc1\n"
    "v_mfma_f32_16x16x32_bf16 v[108:111], a[176:179], v[196:199], v[108:111]\n"
    "s_mov_b64 exec, -1\n"
    "ds_read_b128 a[168:171], v240 offset:28672\n"
    "ds_write_addtid_b32 v157 offset:2304\n"
    "v_mfma_f32_16x16x32_bf16 v[108:111], a[180:183], v[200:203], v[108:111]\n"
    "s_add_u32 s87, s87, s88\n"
    "v_exp_f32_e32 v154, v154\n"
    "v_mfma_f32_16x16x32_bf16 v[112:115], a[180:183], v[208:211], v[112:115]\n"
    "s_add_u32 s89, s89, 1\n"
    "buffer_load_dwordx4 a[228:231], v235, s[20:23], 0 offen sc1\n"
    "ds_write_addtid_b32 v164 offset:3072\n"
    "v_mfma_f32_16x16x32_bf16 v[112:115], a[176:179], v[204:207], v[112:115]\n"
    "ds_write_addtid_b32 v165 offset:3328\n"
    "v_readlane_b32 s86, v254, s89\n"
    "v_mfma_f32_16x16x32_bf16 v[116:119], a[176:179], v[212:215], v[116:119]\n"
    "v_exp_f32_e32 v155, v155\n"
    "v_mfma_f32_16x16x32_bf16 v[116:119], a[180:183], v[216:219], v[116:119]\n"
    "s_waitcnt lgkmcnt(11)\n"
    "ds_read_b128 a[172:175], v241 offset:28672\n"
    "v_cvt_pk_bf16_f32 v150, v152, v153\n"
    "v_mfma_f32_16x16x32_bf16 v[136:139], a[188:191], v[216:219], v[136:139]\n"
    "v_exp_f32_e32 v160, v160\n"
    "v_mfma_f32_16x16x32_bf16 v[136:139], a[184:187], v[212:215], v[136:139]\n"
    "v_exp_f32_e32 v161, v161\n"
    "v_mfma_f32_16x16x32_bf16 v[132:135], a[184:187], v[204:207], v[132:135]\n"
    "ds_read_b128 a[176:179], v242 offset:24576\n"
    "v_cvt_pk_bf16_f32 v151, v154, v155\n"
    "v_mfma_f32_16x16x32_bf16 v[132:135], a[188:191], v[208:211], v[132:135]\n"
    "v_exp_f32_e32 v162, v162\n"
    "v_mfma_f32_16x16x32_bf16 v[128:131], a[188:191], v[200:203], v[128:131]\n"
    "v_exp_f32_e32 v163, v163\n"
    "v_mfma_f32_16x16x32_bf16 v[128:131], a[184:187], v[196:199], v[128:131]\n"
    "ds_write_addtid_b32 v172 offset:4096\n"
    "v_cvt_pk_bf16_f32 v158, v160, v161\n"
    "v_mfma_f32_16x16x32_bf16 v[124:127], a[184:187], v[188:191], v[124:127]\n"
    "v_exp_f32_e32 v168, v168\n"
    "v_mfma_f32_16x16x32_bf16 v[124:127], a[188:191], v[192:195], v[124:127]\n"
    "s_sub_u32 s44, s44, 1\n"
    "ds_read_b128 a[180:183], v243 offset:24576\n"
    "ds_write_addtid_b32 v173 offset:4352\n"
    "v_mfma_f32_16x16x32_bf16 v[120:123], a[188:191], v[184:187], v[120:123]\n"
    "s_cmp_lg_u32 s44, s80\n"
    "v_exp_f32_e32 v169, v169\n"
    "v_mfma_f32_16x16x32_bf16 v[120:123], a[184:187], v[180:183], v[120:123]\n"
    "s_waitcnt lgkmcnt(11)\n"
    "ds_write_addtid_b32 v142 offset:512\n"
    "v_cvt_pk_bf16_f32 v159, v162, v163\n"
    "s_cbranch_scc1 .Lat_gloop\n"
    ".Lat_g2:\n"
    "v_mov_b64_e32 v[224:225], v[220:221]\n"
    "v_mov_b64_e32 v[226:227], v[220:221]\n"
    "v_mov_b64_e32 v[228:229], v[220:221]\n"
    "v_mov_b64_e32 v[230:231], v[220:221]\n"
    "v_mov_b64_e32 v[232:233], v[220:221]\n"
    "v_mov_b64_e32 v[234:235], v[220:221]\n"
    "v_mov_b64_e32 v[236:237], v[220:221]\n"
    "v_mov_b64_e32 v[238:239], v[220:221]\n"
    "s_mov_b32 s80, 0\n"
    "s_waitcnt lgkmcnt(0)\n"
    "s_cmp_lg_u32 s44, 0\n"
    "s_cbranch_scc1 .Lat_loop\n"
    "s_branch .Lat_tail\n"
    ".Lat_wpoll:\n"
    "s_mov_b32 s53, 0x400000\n"
    ".Lat_wpoll_l:\n"
    "s_sleep 1\n"
    "buffer_load_dword v253, v255, s[8:11], s52 offen offset:0 sc1\n"
    "s_waitcnt vmcnt(0)\n"
    "v_cmp_ne_u32_e32 vcc, s39, v253\n"
    "s_sub_u32 s53, s53, 1\n"
    "s_cbranch_scc1 .Lat_wpoll_ret\n"
    "s_nop 2\n"
    "s_cbranch_vccnz .Lat_wpoll_l\n"
    "s_branch .Lat_wpoll_ret\n"
    ".Lat_glate:\n"
    "s_mov_b32 s53, 0x400000\n"
    ".Lat_glate_l:\n"
    "s_sleep 2\n"
    "buffer_load_dword v188, v189, s[8:11], s81 offen offset:0 sc1\n"
    "s_waitcnt vmcnt(0)\n"
    "v_cmp_ne_u32_e32 vcc, s39, v188\n"
    "s_sub_u32 s53, s53, 1\n"
    "s_cbranch_scc1 .Lat_glate_ret\n"
    "s_nop 2\n"
    "s_cbranch_vccnz .Lat_glate_l\n"
    "s_branch .Lat_glate_ret\n"
    ".Lat_k:\n"
    "s_mov_b32 s53, 0x400000\n"
    ".Lat_kl:\n"
    "buffer_load_dword v253, v238, s[8:11], s48 offen offset:0 sc1\n"
    "s_waitcnt vmcnt(0)\n"
    "v_subrev_u32_e32 v253, s39, v253\n"
    "v_subrev_u32_e32 v253, 0, v253\n"
    "v_cmp_le_u32_e32 vcc, 1, v253\n"
    "s_sub_u32 s53, s53, 1\n"
    "s_cbranch_scc1 .Lat_kr\n"
    "s_nop 2\n"
    "s_cbranch_vccnz .Lat_kl\n"
    "s_branch .Lat_kr\n"
    ".Lat_kx:\n"
    "s_branch .Lat_kxr\n"
    ".Lat_ppoll:\n"
    "s_mov_b32 s53, 0x400000\n"
    ".Lat_ppl:\n"
    "buffer_load_dword v253, v226, s[8:11], s61 offen offset:0 sc1\n"
    "s_waitcnt vmcnt(0)\n"
    "v_subrev_u32_e32 v253, s39, v253\n"
    "v_cmp_lt_u32_e32 vcc, 1, v253\n"
    "s_sub_u32 s53, s53, 1\n"
    "s_cbranch_scc1 .Lat_pok\n"
    "s_nop 2\n"
    "s_cbranch_vccnz .Lat_ppl\n"
    "s_branch .Lat_pok\n"
    ".Lat_bin:\n"
    "s_movk_i32 s45, -2560\n"
    "s_mov_b32 s48, 81920\n"
    "v_mov_b32_e32 v172, 1\n"
    "s_waitcnt vmcnt(0)\n"
    "v_mul_u32_u24_e32 v160, 0x199a, v144\n"
    "v_mul_u32_u24_e32 v161, 0x199a, v145\n"
    "v_mul_u32_u24_e32 v162, 0x199a, v146\n"
    "v_mul_u32_u24_e32 v163, 0x199a, v147\n"
    "v_mul_u32_u24_e32 v164, 0x199a, v148\n"
    "v_bfe_u32 v160, v160, 24, 2\n"
    "v_bfe_u32 v161, v161, 24, 2\n"
    "v_bfe_u32 v162, v162, 24, 2\n"
    "v_bfe_u32 v163, v163, 24, 2\n"
    "v_bfe_u32 v164, v164, 24, 2\n"
    "v_lshl_add_u32 v166, v160, 2, s48\n"
    "v_lshl_add_u32 v167, v161, 2, s48\n"
    "v_lshl_add_u32 v168, v162, 2, s48\n"
    "v_lshl_add_u32 v169, v163, 2, s48\n"
    "v_lshl_add_u32 v170, v164, 2, s48\n"
    "ds_add_rtn_u32 v166, v166, v172 offset:4096\n"
    "ds_add_rtn_u32 v167, v167, v172 offset:4096\n"
    "ds_add_rtn_u32 v168, v168, v172 offset:4096\n"
    "ds_add_rtn_u32 v169, v169, v172 offset:4096\n"
    "ds_add_rtn_u32 v170, v170, v172 offset:4096\n"
    "v_mad_i32_i24 v144, v160, s45, v144\n"
    "v_mad_i32_i24 v145, v161, s45, v145\n"
    "v_mad_i32_i24 v146, v162, s45, v146\n"
    "v_mad_i32_i24 v147, v163, s45, v147\n"
    "v_mad_i32_i24 v148, v164, s45, v148\n"
    "v_lshl_or_b32 v144, v144, 18, v152\n"
    "v_lshl_or_b32 v145, v145, 18, v153\n"
    "v_lshl_or_b32 v146, v146, 18, v154\n"
    "v_lshl_or_b32 v147, v147, 18, v155\n"
    "v_lshl_or_b32 v148, v148, 18, v156\n"
    "v_lshl_add_u32 v160, v160, 10, s48\n"
    "v_lshl_add_u32 v161, v161, 10, s48\n"
    "v_lshl_add_u32 v162, v162, 10, s48\n"
    "v_lshl_add_u32 v163, v163, 10, s48\n"
    "v_lshl_add_u32 v164, v164, 10, s48\n"
    "s_branch .Lat_bin_ret\n"
    ".Lat_bin2:\n"
    "s_waitcnt lgkmcnt(0)\n"
    "v_min_i32_e32 v166, 254, v166\n"
    "v_min_i32_e32 v167, 254, v167\n"
    "v_min_i32_e32 v168, 254, v168\n"
    "v_min_i32_e32 v169, 254, v169\n"
    "v_min_i32_e32 v170, 254, v170\n"
    "v_lshl_add_u32 v160, v166, 2, v160\n"
    "v_lshl_add_u32 v161, v167, 2, v161\n"
    "v_lshl_add_u32 v162, v168, 2, v162\n"
    "v_lshl_add_u32 v163, v169, 2, v163\n"
    "v_lshl_add_u32 v164, v170, 2, v164\n"
    "ds_write_b32 v160, v144 offset:4\n"
    "ds_write_b32 v161, v145 offset:4\n"
    "ds_write_b32 v162, v146 offset:4\n"
    "ds_write_b32 v163, v147 offset:4\n"
    "ds_write_b32 v164, v148 offset:4\n"
    "s_branch .Lat_bin2_ret\n"
    ".Lat_apoll:\n"
    "s_mov_b32 s53, 0x400000\n"
    ".Lat_apoll_l:\n"
    "s_sleep 1\n"
    "buffer_load_dword v101, v100, s[8:11], 0 offen offset:0 sc1\n"
    "s_waitcnt vmcnt(0)\n"
    "v_cmp_ne_u32_e32 vcc, s39, v101\n"
    "s_sub_u32 s53, s53, 1\n"
    "s_cbranch_scc1 .Lat_apoll_ret\n"
    "s_nop 2\n"
    "s_cbranch_vccnz .Lat_apoll_l\n"
    "s_branch .Lat_apoll_ret\n"
    ".Lat_h1poll:\n"
    "s_mov_b32 s53, 0x400000\n"
    ".Lat_h1poll_l:\n"
    "s_sleep 1\n"
    "buffer_load_dword v80, v83, s[8:11], s59 offen offset:0 sc1\n"
    "s_waitcnt vmcnt(0)\n"
    "v_cmp_ne_u32_e32 vcc, s39, v80\n"
    "s_sub_u32 s53, s53, 1\n"
    "s_cbranch_scc1 .Lat_h1poll_ret\n"
    "s_nop 2\n"
    "s_cbranch_vccnz .Lat_h1poll_l\n"
    "s_branch .Lat_h1poll_ret\n"
    ".Lat_qpoll:\n"
    "s_mov_b32 s53, 0x400000\n"
    ".Lat_qpoll_l:\n"
    "s_sleep 0\n"
    "buffer_load_dword v254, v253, s[8:11], s47 offen offset:0 sc1\n"
    "s_waitcnt vmcnt(0)\n"
    "v_cmp_ne_u32_e32 vcc, s39, v254\n"
    "s_sub_u32 s53, s53, 1\n"
    "s_cbranch_scc1 .Lat_qpoll_ret\n"
    "s_nop 2\n"
    "s_cbranch_vccnz .Lat_qpoll_l\n"
    "s_branch .Lat_qpoll_ret\n"
    ".Lat_hpoll:\n"
    "s_mov_b32 s53, 0x400000\n"
    ".Lat_hpoll_l:\n"
    "s_sleep 1\n"
    "buffer_load_dword v253, v250, s[8:11], s59 offen offset:0 sc1\n"
    "buffer_load_dword v254, v252, s[8:11], s59 offen offset:0 sc1\n"
    "s_waitcnt vmcnt(0)\n"
    "v_xor_b32_e32 v254, s39, v254\n"
    "v_xor_b32_e32 v253, s39, v253\n"
    "v_or_b32_e32 v253, v253, v254\n"
    "v_cmp_ne_u32_e32 vcc, 0, v253\n"
    "s_sub_u32 s53, s53, 1\n"
    "s_cbranch_scc1 .Lat_hpoll_ret\n"
    "s_nop 2\n"
    "s_cbranch_vccnz .Lat_hpoll_l\n"
    "s_branch .Lat_hpoll_ret\n"
    ".Lat_redo:\n"
    "s_waitcnt vmcnt(0) lgkmcnt(0)\n"
    "s_barrier\n"
    "s_mov_b32 s49, 1\n"
    "s_mov_b32 s80, 0\n"
    "s_mov_b32 s82, 2.0\n"
    "v_lshrrev_b32_e32 v224, 4, v250\n"
    "s_and_b32 s50, s93, 1\n"
    "s_lshr_b32 s51, s93, 1\n"
    "v_lshrrev_b32_e32 v225, 3, v224\n"
    "v_and_b32_e32 v226, 6, v225\n"
    "v_and_b32_e32 v227, 7, v224\n"
    "v_xor_b32_e32 v226, v226, v227\n"
    "v_lshlrev_b32_e32 v226, 4, v226\n"
    "v_and_b32_e32 v227, 4, v225\n"
    "v_add_u32_e32 v227, v227, v225\n"
    "v_lshl_add_u32 v251, v227, 9, v226\n"
    "v_add_u32_e32 v252, 0x1c00, v251\n"
    "v_and_b32_e32 v227, 1, v225\n"
    "v_lshl_add_u32 v248, v227, 9, v226\n"
    "v_lshrrev_b32_e32 v227, 1, v225\n"
    "v_lshl_add_u32 v248, v227, 12, v248\n"
    "s_mul_i32 s43, s50, 3\n"
    "s_xor_b32 s43, s43, s51\n"
    "s_lshl_b32 s43, s43, 15\n"
    "s_add_u32 s43, s43, 32768\n"
    "s_mul_i32 s42, s93, 5120\n"
    "s_lshl_b32 s47, s50, 8\n"
    "s_mul_i32 s40, s32, 1277952\n"
    "s_lshl_b32 s41, s51, 14\n"
    "s_add_u32 s40, s40, s41\n"
    "s_add_u32 s40, s40, s47\n"
    "s_add_u32 s41, s40, 128\n"
    "v_bfe_u32 v225, v224, 2, 3\n"
    "v_lshlrev_b32_e32 v244, 7, v225\n"
    "v_lshrrev_b32_e32 v226, 5, v224\n"
    "v_lshl_add_u32 v244, v226, 10, v244\n"
    "v_and_b32_e32 v225, 6, v225\n"
    "v_bfe_u32 v226, v224, 1, 1\n"
    "v_xor_b32_e32 v225, v225, v226\n"
    "v_lshl_add_u32 v244, v225, 4, v244\n"
    "v_and_b32_e32 v226, 1, v224\n"
    "v_lshl_add_u32 v244, v226, 3, v244\n"
    "s_lshl_b32 s47, s51, 16\n"
    "s_add_u32 s47, s47, 32768\n"
    "v_add_u32_e32 v244, s47, v244\n"
    "v_xor_b32_e32 v245, 32, v244\n"
    "v_xor_b32_e32 v246, 64, v244\n"
    "v_xor_b32_e32 v247, 0x60, v244\n"
    "v_and_b32_e32 v225, 7, v224\n"
    "v_lshlrev_b32_e32 v227, 7, v225\n"
    "v_bfe_u32 v226, v224, 3, 1\n"
    "v_lshl_add_u32 v227, v226, 10, v227\n"
    "v_and_b32_e32 v225, 6, v224\n"
    "v_lshrrev_b32_e32 v226, 4, v224\n"
    "v_xor_b32_e32 v225, v225, v226\n"
    "v_lshl_add_u32 v227, v225, 4, v227\n"
    "s_lshl_b32 s47, s51, 15\n"
    "s_add_u32 s48, s47, 32768\n"
    "s_sub_u32 s47, 131072, s47\n"
    "v_add_u32_e32 v240, s48, v227\n"
    "v_xor_b32_e32 v241, 64, v240\n"
    "v_add_u32_e32 v242, s47, v227\n"
    "v_xor_b32_e32 v243, 64, v242\n"
    "s_xor_b32 s47, s93, 2\n"
    "s_mul_i32 s47, s47, 5120\n"
    "v_lshl_add_u32 v249, v224, 2, s47\n"
    "v_and_b32_e32 v252, 0x30, v250\n"
    "v_add_u32_e32 v252, 0x400, v252\n"
    "s_mul_i32 s59, s32, 999\n"
    "s_andn2_b32 s59, s59, 63\n"
    "s_sub_u32 s48, 3968, s59\n"
    "v_cmp_le_u32_e32 vcc, 0x80, v250\n"
    "v_mov_b32_e32 v80, s48\n"
    "s_add_u32 s59, s59, 6076416\n"
    "v_cndmask_b32_e32 v83, v250, v80, vcc\n"
    "v_bfe_u32 v253, v250, 4, 2\n"
    "v_max_u32_e32 v253, 1, v253\n"
    "v_add_u32_e32 v253, s32, v253\n"
    "v_and_b32_e32 v253, 3, v253\n"
    "v_lshlrev_b32_e32 v253, 9, v253\n"
    "v_mov_b32_e32 v84, s39\n"
    "v_lshrrev_b32_e32 v86, 2, v250\n"
    "v_min_u32_e32 v86, 124, v86\n"
    "s_lshl2_add_u32 s58, s34, s93\n"
    "s_lshl_b32 s58, s58, 7\n"
    "s_add_u32 s58, s58, 5910528\n"
    "s_lshl_b32 s45, s50, 13\n"
    "v_add_u32_e32 v85, s45, v250\n"
    "s_lshl3_add_u32 s45, s45, 49152\n"
    "v_add_u32_e32 v255, s45, v250\n"
    "v_lshrrev_b32_e32 v105, 7, v250\n"
    "v_and_b32_e32 v104, 0x70, v251\n"
    "v_lshl_add_u32 v104, v105, 9, v104\n"
    "v_add_u32_e32 v105, 0xc00, v104\n"
    "v_add_u32_e32 v106, 0x1800, v104\n"
    "v_add_u32_e32 v107, 0x2400, v104\n"
    "s_lshl_b32 s57, s93, 7\n"
    "s_lshl4_add_u32 s56, s57, 24576\n"
    "s_add_u32 s57, s57, 5111808\n"
    "s_mul_i32 s45, s51, 32768\n"
    "s_add_u32 s45, s45, 8192\n"
    "v_subrev_u32_e32 v46, s45, v240\n"
    "v_subrev_u32_e32 v47, s45, v241\n"
    "s_mul_i32 s45, s51, 61440\n"
    "s_add_u32 s45, s45, 8192\n"
    "v_subrev_u32_e32 v4, s45, v244\n"
    "v_subrev_u32_e32 v5, s45, v245\n"
    "v_subrev_u32_e32 v6, s45, v246\n"
    "v_subrev_u32_e32 v7, s45, v247\n"
    "v_accvgpr_write_b32 a244, 0\n"
    "v_accvgpr_write_b32 a245, 0\n"
    "v_accvgpr_write_b32 a246, 0\n"
    "v_accvgpr_write_b32 a247, 0\n"
    "v_accvgpr_write_b32 a248, 0\n"
    "v_accvgpr_write_b32 a249, 0\n"
    "v_accvgpr_write_b32 a250, 0\n"
    "v_accvgpr_write_b32 a251, 0\n"
    "v_accvgpr_write_b32 a252, 0\n"
    "v_accvgpr_write_b32 a253, 0\n"
    "v_accvgpr_write_b32 a254, 0\n"
    "v_accvgpr_write_b32 a255, 0\n"
    "v_mov_b64_e32 v[8:9], 0\n"
    "v_mov_b64_e32 v[10:11], 0\n"
    "v_mov_b64_e32 v[12:13], 0\n"
    "v_mov_b64_e32 v[14:15], 0\n"
    "v_mov_b64_e32 v[16:17], 0\n"
    "v_mov_b64_e32 v[18:19], 0\n"
    "v_mov_b64_e32 v[28:29], 0\n"
    "v_mov_b64_e32 v[30:31], 0\n"
    "v_mov_b64_e32 v[32:33], 0\n"
    "v_mov_b64_e32 v[34:35], 0\n"
    "v_mov_b64_e32 v[36:37], 0\n"
    "v_mov_b64_e32 v[38:39], 0\n"
    "v_mov_b64_e32 v[48:49], 0\n"
    "v_mov_b64_e32 v[50:51], 0\n"
    "v_mov_b64_e32 v[52:53], 0\n"
    "v_mov_b64_e32 v[54:55], 0\n"
    "v_mov_b64_e32 v[56:57], 0\n"
    "v_mov_b64_e32 v[58:59], 0\n"
    "v_mov_b64_e32 v[68:69], 0\n"
    "v_mov_b64_e32 v[70:71], 0\n"
    "v_mov_b64_e32 v[72:73], 0\n"
    "v_mov_b64_e32 v[74:75], 0\n"
    "v_mov_b64_e32 v[76:77], 0\n"
    "v_mov_b64_e32 v[78:79], 0\n"
    "v_mov_b64_e32 v[88:89], 0\n"
    "v_mov_b64_e32 v[90:91], 0\n"
    "v_mov_b64_e32 v[92:93], 0\n"
    "v_mov_b64_e32 v[94:95], 0\n"
    "v_mov_b64_e32 v[96:97], 0\n"
    "v_mov_b64_e32 v[98:99], 0\n"
    "v_mov_b64_e32 v[108:109], 0\n"
    "v_mov_b64_e32 v[110:111], 0\n"
    "v_mov_b64_e32 v[112:113], 0\n"
    "v_mov_b64_e32 v[114:115], 0\n"
    "v_mov_b64_e32 v[116:117], 0\n"
    "v_mov_b64_e32 v[118:119], 0\n"
    "v_mov_b64_e32 v[128:129], 0\n"
    "v_mov_b64_e32 v[130:131], 0\n"
    "v_mov_b64_e32 v[132:133], 0\n"
    "v_mov_b64_e32 v[134:135], 0\n"
    "v_mov_b64_e32 v[136:137], 0\n"
    "v_mov_b64_e32 v[138:139], 0\n"
    "s_mov_b32 m0, s56\n"
    "s_nop 0\n"
    "buffer_load_dwordx4 v104, s[8:11], s57 offen offset:0 sc1 lds\n"
    "buffer_load_dwordx4 v105, s[8:11], s57 offen offset:1024 sc1 lds\n"
    "s_add_u32 m0, s43, 0\n"
    "s_nop 0\n"
    "buffer_load_dwordx4 v104, s[8:11], s40 offen offset:0 sc1 lds\n"
    "buffer_load_dwordx4 v105, s[8:11], s40 offen offset:1024 sc1 lds\n"
    "buffer_load_dwordx4 v106, s[8:11], s40 offen offset:2048 sc1 lds\n"
    "buffer_load_dwordx4 v107, s[8:11], s40 offen offset:3072 sc1 lds\n"
    "s_add_u32 s40, s40, 0x8000\n"
    "s_add_u32 m0, s43, 4096\n"
    "s_nop 0\n"
    "buffer_load_dwordx4 v104, s[8:11], s41 offen offset:0 sc1 lds\n"
    "buffer_load_dwordx4 v105, s[8:11], s41 offen offset:1024 sc1 lds\n"
    "buffer_load_dwordx4 v106, s[8:11], s41 offen offset:2048 sc1 lds\n"
    "buffer_load_dwordx4 v107, s[8:11], s41 offen offset:3072 sc1 lds\n"
    "s_add_u32 s41, s41, 0x8000\n"
    "s_add_u32 m0, s43, 8192\n"
    "s_nop 0\n"
    "buffer_load_dwordx4 v104, s[8:11], s40 offen offset:0 sc1 lds\n"
    "buffer_load_dwordx4 v105, s[8:11], s40 offen offset:1024 sc1 lds\n"
    "buffer_load_dwordx4 v106, s[8:11], s40 offen offset:2048 sc1 lds\n"
    "buffer_load_dwordx4 v107, s[8:11], s40 offen offset:3072 sc1 lds\n"
    "s_add_u32 s40, s40, 0x8000\n"
    "s_add_u32 m0, s43, 12288\n"
    "s_nop 0\n"
    "buffer_load_dwordx4 v104, s[8:11], s41 offen offset:0 sc1 lds\n"
    "buffer_load_dwordx4 v105, s[8:11], s41 offen offset:1024 sc1 lds\n"
    "buffer_load_dwordx4 v106, s[8:11], s41 offen offset:2048 sc1 lds\n"
    "buffer_load_dwordx4 v107, s[8:11], s41 offen offset:3072 sc1 lds\n"
    "s_add_u32 s41, s41, 0x8000\n"
    "s_waitcnt vmcnt(0)\n"
    "s_branch .Lat_p2\n"
    ".Lat_maxpass:\n"
    "s_waitcnt lgkmcnt(0)\n"
    "s_mul_i32 s48, s33, 81920\n"
    "s_add_u32 s48, s48, 11345920\n"
    "s_add_u32 s45, s32, 0\n"
    "s_and_b32 s45, s45, 3\n"
    "s_lshl2_add_u32 s45, s50, s45\n"
    "s_lshl_b32 s45, s45, 13\n"
    "s_add_u32 s45, s45, s48\n"
    "s_add_u32 s46, s45, 4096\n"
    "buffer_load_dwordx4 a[0:3], v250, s[8:11], s45 offen offset:0 sc1\n"
    "buffer_load_dwordx4 a[4:7], v250, s[8:11], s45 offen offset:1024 sc1\n"
    "buffer_load_dwordx4 a[8:11], v250, s[8:11], s45 offen offset:2048 sc1\n"
    "buffer_load_dwordx4 a[12:15], v250, s[8:11], s45 offen offset:3072 sc1\n"
    "buffer_load_dwordx4 a[16:19], v250, s[8:11], s46 offen offset:0 sc1\n"
    "buffer_load_dwordx4 a[20:23], v250, s[8:11], s46 offen offset:1024 sc1\n"
    "buffer_load_dwordx4 a[24:27], v250, s[8:11], s46 offen offset:2048 sc1\n"
    "buffer_load_dwordx4 a[28:31], v250, s[8:11], s46 offen offset:3072 sc1\n"
    "s_and_b32 s45, s32, 1\n"
    "s_xor_b32 s45, s45, s50\n"
    "s_add_u32 s45, s45, 8\n"
    "s_lshl_b32 s45, s45, 13\n"
    "s_add_u32 s45, s45, s48\n"
    "s_add_u32 s46, s45, 4096\n"
    "buffer_load_dwordx4 a[32:35], v250, s[8:11], s45 offen offset:0 sc1\n"
    "buffer_load_dwordx4 a[36:39], v250, s[8:11], s45 offen offset:1024 sc1\n"
    "buffer_load_dwordx4 a[40:43], v250, s[8:11], s45 offen offset:2048 sc1\n"
    "buffer_load_dwordx4 a[44:47], v250, s[8:11], s45 offen offset:3072 sc1\n"
    "buffer_load_dwordx4 a[48:51], v250, s[8:11], s46 offen offset:0 sc1\n"
    "buffer_load_dwordx4 a[52:55], v250, s[8:11], s46 offen offset:1024 sc1\n"
    "buffer_load_dwordx4 a[56:59], v250, s[8:11], s46 offen offset:2048 sc1\n"
    "buffer_load_dwordx4 a[60:63], v250, s[8:11], s46 offen offset:3072 sc1\n"
    "s_add_u32 s45, s32, 1\n"
    "s_and_b32 s45, s45, 3\n"
    "s_lshl2_add_u32 s45, s50, s45\n"
    "s_lshl_b32 s45, s45, 13\n"
    "s_add_u32 s45, s45, s48\n"
    "s_add_u32 s46, s45, 4096\n"
    "buffer_load_dwordx4 a[64:67], v250, s[8:11], s45 offen offset:0 sc1\n"
    "buffer_load_dwordx4 a[68:71], v250, s[8:11], s45 offen offset:1024 sc1\n"
    "buffer_load_dwordx4 a[72:75], v250, s[8:11], s45 offen offset:2048 sc1\n"
    "buffer_load_dwordx4 a[76:79], v250, s[8:11], s45 offen offset:3072 sc1\n"
    "buffer_load_dwordx4 a[80:83], v250, s[8:11], s46 offen offset:0 sc1\n"
    "buffer_load_dwordx4 a[84:87], v250, s[8:11], s46 offen offset:1024 sc1\n"
    "buffer_load_dwordx4 a[88:91], v250, s[8:11], s46 offen offset:2048 sc1\n"
    "buffer_load_dwordx4 a[92:95], v250, s[8:11], s46 offen offset:3072 sc1\n"
    "s_add_u32 s45, s32, 2\n"
    "s_and_b32 s45, s45, 3\n"
    "s_lshl2_add_u32 s45, s50, s45\n"
    "s_lshl_b32 s45, s45, 13\n"
    "s_add_u32 s45, s45, s48\n"
    "s_add_u32 s46, s45, 4096\n"
    "buffer_load_dwordx4 a[96:99], v250, s[8:11], s45 offen offset:0 sc1\n"
    "buffer_load_dwordx4 a[100:103], v250, s[8:11], s45 offen offset:1024 sc1\n"
    "buffer_load_dwordx4 a[104:107], v250, s[8:11], s45 offen offset:2048 sc1\n"
    "buffer_load_dwordx4 a[108:111], v250, s[8:11], s45 offen offset:3072 sc1\n"
    "buffer_load_dwordx4 a[112:115], v250, s[8:11], s46 offen offset:0 sc1\n"
    "buffer_load_dwordx4 a[116:119], v250, s[8:11], s46 offen offset:1024 sc1\n"
    "buffer_load_dwordx4 a[120:123], v250, s[8:11], s46 offen offset:2048 sc1\n"
    "buffer_load_dwordx4 a[124:127], v250, s[8:11], s46 offen offset:3072 sc1\n"
    "s_add_u32 s45, s32, 3\n"
    "s_and_b32 s45, s45, 3\n"
    "s_lshl2_add_u32 s45, s50, s45\n"
    "s_lshl_b32 s45, s45, 13\n"
    "s_add_u32 s45, s45, s48\n"
    "s_add_u32 s46, s45, 4096\n"
    "buffer_load_dwordx4 a[128:131], v250, s[8:11], s45 offen offset:0 sc1\n"
    "buffer_load_dwordx4 a[132:135], v250, s[8:11], s45 offen offset:1024 sc1\n"
    "buffer_load_dwordx4 a[136:139], v250, s[8:11], s45 offen offset:2048 sc1\n"
    "buffer_load_dwordx4 a[140:143], v250, s[8:11], s45 offen offset:3072 sc1\n"
    "buffer_load_dwordx4 a[144:147], v250, s[8:11], s46 offen offset:0 sc1\n"
    "buffer_load_dwordx4 a[148:151], v250, s[8:11], s46 offen offset:1024 sc1\n"
    "buffer_load_dwordx4 a[152:155], v250, s[8:11], s46 offen offset:2048 sc1\n"
    "buffer_load_dwordx4 a[156:159], v250, s[8:11], s46 offen offset:3072 sc1\n"
    "s_waitcnt vmcnt(0)\n"
    "v_mov_b32_e32 v200, 0xff800000\n"
    "v_mov_b32_e32 v201, 0xff800000\n"
    "v_mov_b32_e32 v202, 0xff800000\n"
    "v_mov_b32_e32 v203, 0xff800000\n"
    "v_mov_b32_e32 v204, 0xff800000\n"
    "s_lshl_b32 s45, s51, 14\n"
    "s_lshl_b32 s46, s50, 8\n"
    "s_add_u32 s45, s45, s46\n"
    "s_add_u32 s61, s46, 5103616\n"
    "s_movk_i32 s48, 157\n"
    ".Lat_mp_loop:\n"
    "s_min_u32 s45, s45, s61\n"
    "s_add_u32 s46, s45, 128\n"
    "s_add_u32 m0, s43, 16384\n"
    "s_nop 0\n"
    "buffer_load_dwordx4 v248, s[8:11], s45 offen offset:0 sc1 lds\n"
    "buffer_load_dwordx4 v248, s[8:11], s45 offen offset:1024 sc1 lds\n"
    "buffer_load_dwordx4 v248, s[8:11], s45 offen offset:2048 sc1 lds\n"
    "buffer_load_dwordx4 v248, s[8:11], s45 offen offset:3072 sc1 lds\n"
    "s_add_u32 m0, s43, 20480\n"
    "s_nop 0\n"
    "buffer_load_dwordx4 v248, s[8:11], s46 offen offset:0 sc1 lds\n"
    "buffer_load_dwordx4 v248, s[8:11], s46 offen offset:1024 sc1 lds\n"
    "buffer_load_dwordx4 v248, s[8:11], s46 offen offset:2048 sc1 lds\n"
    "buffer_load_dwordx4 v248, s[8:11], s46 offen offset:3072 sc1 lds\n"
    "s_waitcnt vmcnt(0)\n"
    "s_barrier\n"
    "ds_read_b128 a[160:163], v240 offset:16384\n"
    "ds_read_b128 a[164:167], v241 offset:16384\n"
    "ds_read_b128 a[168:171], v240 offset:20480\n"
    "ds_read_b128 a[172:175], v241 offset:20480\n"
    "ds_read_b128 a[176:179], v242 offset:16384\n"
    "ds_read_b128 a[180:183], v243 offset:16384\n"
    "ds_read_b128 a[184:187], v242 offset:20480\n"
    "ds_read_b128 a[188:191], v243 offset:20480\n"
    "s_waitcnt lgkmcnt(0)\n"
    "v_mfma_f32_16x16x32_bf16 v[180:183], a[160:163], a[0:3], 0\n"
    "v_mfma_f32_16x16x32_bf16 v[184:187], a[160:163], a[32:35], 0\n"
    "v_mfma_f32_16x16x32_bf16 v[188:191], a[160:163], a[64:67], 0\n"
    "v_mfma_f32_16x16x32_bf16 v[192:195], a[160:163], a[96:99], 0\n"
    "v_mfma_f32_16x16x32_bf16 v[196:199], a[160:163], a[128:131], 0\n"
    "v_mfma_f32_16x16x32_bf16 v[180:183], a[164:167], a[4:7], v[180:183]\n"
    "v_mfma_f32_16x16x32_bf16 v[184:187], a[164:167], a[36:39], v[184:187]\n"
    "v_mfma_f32_16x16x32_bf16 v[188:191], a[164:167], a[68:71], v[188:191]\n"
    "v_mfma_f32_16x16x32_bf16 v[192:195], a[164:167], a[100:103], v[192:195]\n"
    "v_mfma_f32_16x16x32_bf16 v[196:199], a[164:167], a[132:135], v[196:199]\n"
    "v_mfma_f32_16x16x32_bf16 v[180:183], a[168:171], a[8:11], v[180:183]\n"
    "v_mfma_f32_16x16x32_bf16 v[184:187], a[168:171], a[40:43], v[184:187]\n"
    "v_mfma_f32_16x16x32_bf16 v[188:191], a[168:171], a[72:75], v[188:191]\n"
    "v_mfma_f32_16x16x32_bf16 v[192:195], a[168:171], a[104:107], v[192:195]\n"
    "v_mfma_f32_16x16x32_bf16 v[196:199], a[168:171], a[136:139], v[196:199]\n"
    "v_mfma_f32_16x16x32_bf16 v[180:183], a[172:175], a[12:15], v[180:183]\n"
    "v_mfma_f32_16x16x32_bf16 v[184:187], a[172:175], a[44:47], v[184:187]\n"
    "v_mfma_f32_16x16x32_bf16 v[188:191], a[172:175], a[76:79], v[188:191]\n"
    "v_mfma_f32_16x16x32_bf16 v[192:195], a[172:175], a[108:111], v[192:195]\n"
    "v_mfma_f32_16x16x32_bf16 v[196:199], a[172:175], a[140:143], v[196:199]\n"
    "v_mfma_f32_16x16x32_bf16 v[180:183], a[176:179], a[16:19], v[180:183]\n"
    "v_mfma_f32_16x16x32_bf16 v[184:187], a[176:179], a[48:51], v[184:187]\n"
    "v_mfma_f32_16x16x32_bf16 v[188:191], a[176:179], a[80:83], v[188:191]\n"
    "v_mfma_f32_16x16x32_bf16 v[192:195], a[176:179], a[112:115], v[192:195]\n"
    "v_mfma_f32_16x16x32_bf16 v[196:199], a[176:179], a[144:147], v[196:199]\n"
    "v_mfma_f32_16x16x32_bf16 v[180:183], a[180:183], a[20:23], v[180:183]\n"
    "v_mfma_f32_16x16x32_bf16 v[184:187], a[180:183], a[52:55], v[184:187]\n"
    "v_mfma_f32_16x16x32_bf16 v[188:191], a[180:183], a[84:87], v[188:191]\n"
    "v_mfma_f32_16x16x32_bf16 v[192:195], a[180:183], a[116:119], v[192:195]\n"
    "v_mfma_f32_16x16x32_bf16 v[196:199], a[180:183], a[148:151], v[196:199]\n"
    "v_mfma_f32_16x16x32_bf16 v[180:183], a[184:187], a[24:27], v[180:183]\n"
    "v_mfma_f32_16x16x32_bf16 v[184:187], a[184:187], a[56:59], v[184:187]\n"
    "v_mfma_f32_16x16x32_bf16 v[188:191], a[184:187], a[88:91], v[188:191]\n"
    "v_mfma_f32_16x16x32_bf16 v[192:195], a[184:187], a[120:123], v[192:195]\n"
    "v_mfma_f32_16x16x32_bf16 v[196:199], a[184:187], a[152:155], v[196:199]\n"
    "v_mfma_f32_16x16x32_bf16 v[180:183], a[188:191], a[28:31], v[180:183]\n"
    "v_mfma_f32_16x16x32_bf16 v[184:187], a[188:191], a[60:63], v[184:187]\n"
    "v_mfma_f32_16x16x32_bf16 v[188:191], a[188:191], a[92:95], v[188:191]\n"
    "v_mfma_f32_16x16x32_bf16 v[192:195], a[188:191], a[124:127], v[192:195]\n"
    "v_mfma_f32_16x16x32_bf16 v[196:199], a[188:191], a[156:159], v[196:199]\n"
    "s_nop 15\n"
    "v_max3_f32 v200, v200, v180, v181\n"
    "v_max3_f32 v200, v200, v182, v183\n"
    "v_max3_f32 v201, v201, v184, v185\n"
    "v_max3_f32 v201, v201, v186, v187\n"
    "v_max3_f32 v202, v202, v188, v189\n"
    "v_max3_f32 v202, v202, v190, v191\n"
    "v_max3_f32 v203, v203, v192, v193\n"
    "v_max3_f32 v203, v203, v194, v195\n"
    "v_max3_f32 v204, v204, v196, v197\n"
    "v_max3_f32 v204, v204, v198, v199\n"
    "s_nop 3\n"
    "ds_read_b128 a[160:163], v240 offset:18432\n"
    "ds_read_b128 a[164:167], v241 offset:18432\n"
    "ds_read_b128 a[168:171], v240 offset:22528\n"
    "ds_read_b128 a[172:175], v241 offset:22528\n"
    "ds_read_b128 a[176:179], v242 offset:18432\n"
    "ds_read_b128 a[180:183], v243 offset:18432\n"
    "ds_read_b128 a[184:187], v242 offset:22528\n"
    "ds_read_b128 a[188:191], v243 offset:22528\n"
    "s_waitcnt lgkmcnt(0)\n"
    "v_mfma_f32_16x16x32_bf16 v[180:183], a[160:163], a[0:3], 0\n"
    "v_mfma_f32_16x16x32_bf16 v[184:187], a[160:163], a[32:35], 0\n"
    "v_mfma_f32_16x16x32_bf16 v[188:191], a[160:163], a[64:67], 0\n"
    "v_mfma_f32_16x16x32_bf16 v[192:195], a[160:163], a[96:99], 0\n"
    "v_mfma_f32_16x16x32_bf16 v[196:199], a[160:163], a[128:131], 0\n"
    "v_mfma_f32_16x16x32_bf16 v[180:183], a[164:167], a[4:7], v[180:183]\n"
    "v_mfma_f32_16x16x32_bf16 v[184:187], a[164:167], a[36:39], v[184:187]\n"
    "v_mfma_f32_16x16x32_bf16 v[188:191], a[164:167], a[68:71], v[188:191]\n"
    "v_mfma_f32_16x16x32_bf16 v[192:195], a[164:167], a[100:103], v[192:195]\n"
    "v_mfma_f32_16x16x32_bf16 v[196:199], a[164:167], a[132:135], v[196:199]\n"
    "v_mfma_f32_16x16x32_bf16 v[180:183], a[168:171], a[8:11], v[180:183]\n"
    "v_mfma_f32_16x16x32_bf16 v[184:187], a[168:171], a[40:43], v[184:187]\n"
    "v_mfma_f32_16x16x32_bf16 v[188:191], a[168:171], a[72:75], v[188:191]\n"
    "v_mfma_f32_16x16x32_bf16 v[192:195], a[168:171], a[104:107], v[192:195]\n"
    "v_mfma_f32_16x16x32_bf16 v[196:199], a[168:171], a[136:139], v[196:199]\n"
    "v_mfma_f32_16x16x32_bf16 v[180:183], a[172:175], a[12:15], v[180:183]\n"
    "v_mfma_f32_16x16x32_bf16 v[184:187], a[172:175], a[44:47], v[184:187]\n"
    "v_mfma_f32_16x16x32_bf16 v[188:191], a[172:175], a[76:79], v[188:191]\n"
    "v_mfma_f32_16x16x32_bf16 v[192:195], a[172:175], a[108:111], v[192:195]\n"
    "v_mfma_f32_16x16x32_bf16 v[196:199], a[172:175], a[140:143], v[196:199]\n"
    "v_mfma_f32_16x16x32_bf16 v[180:183], a[176:179], a[16:19], v[180:183]\n"
    "v_mfma_f32_16x16x32_bf16 v[184:187], a[176:179], a[48:51], v[184:187]\n"
    "v_mfma_f32_16x16x32_bf16 v[188:191], a[176:179], a[80:83], v[188:191]\n"
    "v_mfma_f32_16x16x32_bf16 v[192:195], a[176:179], a[112:115], v[192:195]\n"
    "v_mfma_f32_16x16x32_bf16 v[196:199], a[176:179], a[144:147], v[196:199]\n"
    "v_mfma_f32_16x16x32_bf16 v[180:183], a[180:183], a[20:23], v[180:183]\n"
    "v_mfma_f32_16x16x32_bf16 v[184:187], a[180:183], a[52:55], v[184:187]\n"
    "v_mfma_f32_16x16x32_bf16 v[188:191], a[180:183], a[84:87], v[188:191]\n"
    "v_mfma_f32_16x16x32_bf16 v[192:195], a[180:183], a[116:119], v[192:195]\n"
    "v_mfma_f32_16x16x32_bf16 v[196:199], a[180:183], a[148:151], v[196:199]\n"
    "v_mfma_f32_16x16x32_bf16 v[180:183], a[184:187], a[24:27], v[180:183]\n"
    "v_mfma_f32_16x16x32_bf16 v[184:187], a[184:187], a[56:59], v[184:187]\n"
    "v_mfma_f32_16x16x32_bf16 v[188:191], a[184:187], a[88:91], v[188:191]\n"
    "v_mfma_f32_16x16x32_bf16 v[192:195], a[184:187], a[120:123], v[192:195]\n"
    "v_mfma_f32_16x16x32_bf16 v[196:199], a[184:187], a[152:155], v[196:199]\n"
    "v_mfma_f32_16x16x32_bf16 v[180:183], a[188:191], a[28:31], v[180:183]\n"
    "v_mfma_f32_16x16x32_bf16 v[184:187], a[188:191], a[60:63], v[184:187]\n"
    "v_mfma_f32_16x16x32_bf16 v[188:191], a[188:191], a[92:95], v[188:191]\n"
    "v_mfma_f32_16x16x32_bf16 v[192:195], a[188:191], a[124:127], v[192:195]\n"
    "v_mfma_f32_16x16x32_bf16 v[196:199], a[188:191], a[156:159], v[196:199]\n"
    "s_nop 15\n"
    "v_max3_f32 v200, v200, v180, v181\n"
    "v_max3_f32 v200, v200, v182, v183\n"
    "v_max3_f32 v201, v201, v184, v185\n"
    "v_max3_f32 v201, v201, v186, v187\n"
    "v_max3_f32 v202, v202, v188, v189\n"
    "v_max3_f32 v202, v202, v190, v191\n"
    "v_max3_f32 v203, v203, v192, v193\n"
    "v_max3_f32 v203, v203, v194, v195\n"
    "v_max3_f32 v204, v204, v196, v197\n"
    "v_max3_f32 v204, v204, v198, v199\n"
    "s_nop 3\n"
    "s_barrier\n"
    "s_add_u32 s45, s45, 0x8000\n"
    "s_sub_u32 s48, s48, 1\n"
    "s_cmp_lg_u32 s48, 0\n"
    "s_cbranch_scc1 .Lat_mp_loop\n"
    "s_mov_b32 m0, s42\n"
    "s_nop 1\n"
    "ds_write_addtid_b32 v200 offset:0\n"
    "ds_write_addtid_b32 v201 offset:256\n"
    "ds_write_addtid_b32 v202 offset:512\n"
    "ds_write_addtid_b32 v203 offset:768\n"
    "ds_write_addtid_b32 v204 offset:1024\n"
    "s_waitcnt lgkmcnt(0)\n"
    "s_barrier\n"
    "ds_read_b32 v180, v249 offset:0\n"
    "ds_read_b32 v184, v249 offset:256\n"
    "ds_read_b32 v188, v249 offset:512\n"
    "ds_read_b32 v192, v249 offset:768\n"
    "ds_read_b32 v196, v249 offset:1024\n"
    "s_waitcnt lgkmcnt(0)\n"
    "v_max_f32_e32 v205, v200, v180\n"
    "v_mov_b32_e32 v206, v205\n"
    "s_nop 1\n"
    "v_permlane16_swap_b32_e32 v205, v206\n"
    "s_nop 0\n"
    "v_max_f32_e32 v205, v205, v206\n"
    "v_mov_b32_e32 v206, v205\n"
    "s_nop 1\n"
    "v_permlane32_swap_b32_e32 v205, v206\n"
    "s_nop 0\n"
    "v_max_f32_e32 v205, v205, v206\n"
    "v_add_f32_e32 v205, 0x42980000, v205\n"
    "v_xor_b32_e32 v220, 0x80000000, v205\n"
    "v_xor_b32_e32 v221, 0x80000000, v205\n"
    "v_xor_b32_e32 v222, 0x80000000, v205\n"
    "v_xor_b32_e32 v223, 0x80000000, v205\n"
    "v_max_f32_e32 v205, v201, v184\n"
    "v_mov_b32_e32 v206, v205\n"
    "s_nop 1\n"
    "v_permlane16_swap_b32_e32 v205, v206\n"
    "s_nop 0\n"
    "v_max_f32_e32 v205, v205, v206\n"
    "v_mov_b32_e32 v206, v205\n"
    "s_nop 1\n"
    "v_permlane32_swap_b32_e32 v205, v206\n"
    "s_nop 0\n"
    "v_max_f32_e32 v205, v205, v206\n"
    "v_add_f32_e32 v205, 0x42980000, v205\n"
    "v_xor_b32_e32 v224, 0x80000000, v205\n"
    "v_xor_b32_e32 v225, 0x80000000, v205\n"
    "v_xor_b32_e32 v226, 0x80000000, v205\n"
    "v_xor_b32_e32 v227, 0x80000000, v205\n"
    "v_max_f32_e32 v205, v202, v188\n"
    "v_mov_b32_e32 v206, v205\n"
    "s_nop 1\n"
    "v_permlane16_swap_b32_e32 v205, v206\n"
    "s_nop 0\n"
    "v_max_f32_e32 v205, v205, v206\n"
    "v_mov_b32_e32 v206, v205\n"
    "s_nop 1\n"
    "v_permlane32_swap_b32_e32 v205, v206\n"
    "s_nop 0\n"
    "v_max_f32_e32 v205, v205, v206\n"
    "v_add_f32_e32 v205, 0x42980000, v205\n"
    "v_xor_b32_e32 v228, 0x80000000, v205\n"
    "v_xor_b32_e32 v229, 0x80000000, v205\n"
    "v_xor_b32_e32 v230, 0x80000000, v205\n"
    "v_xor_b32_e32 v231, 0x80000000, v205\n"
    "v_max_f32_e32 v205, v203, v192\n"
    "v_mov_b32_e32 v206, v205\n"
    "s_nop 1\n"
    "v_permlane16_swap_b32_e32 v205, v206\n"
    "s_nop 0\n"
    "v_max_f32_e32 v205, v205, v206\n"
    "v_mov_b32_e32 v206, v205\n"
    "s_nop 1\n"
    "v_permlane32_swap_b32_e32 v205, v206\n"
    "s_nop 0\n"
    "v_max_f32_e32 v205, v205, v206\n"
    "v_add_f32_e32 v205, 0x42980000, v205\n"
    "v_xor_b32_e32 v232, 0x80000000, v205\n"
    "v_xor_b32_e32 v233, 0x80000000, v205\n"
    "v_xor_b32_e32 v234, 0x80000000, v205\n"
    "v_xor_b32_e32 v235, 0x80000000, v205\n"
    "v_max_f32_e32 v205, v204, v196\n"
    "v_mov_b32_e32 v206, v205\n"
    "s_nop 1\n"
    "v_permlane16_swap_b32_e32 v205, v206\n"
    "s_nop 0\n"
    "v_max_f32_e32 v205, v205, v206\n"
    "v_mov_b32_e32 v206, v205\n"
    "s_nop 1\n"
    "v_permlane32_swap_b32_e32 v205, v206\n"
    "s_nop 0\n"
    "v_max_f32_e32 v205, v205, v206\n"
    "v_add_f32_e32 v205, 0x42980000, v205\n"
    "v_xor_b32_e32 v236, 0x80000000, v205\n"
    "v_xor_b32_e32 v237, 0x80000000, v205\n"
    "v_xor_b32_e32 v238, 0x80000000, v205\n"
    "v_xor_b32_e32 v239, 0x80000000, v205\n"
    "s_nop 1\n"
    "s_barrier\n"
    "s_mul_i32 s48, s33, 81920\n"
    "s_add_u32 s48, s48, 11345920\n"
    "s_lshl_b32 s46, s93, 11\n"
    "s_add_u32 s48, s48, s46\n"
    "s_add_u32 s46, s46, 49152\n"
    "s_and_b32 s45, s32, 1\n"
    "s_xor_b32 s45, s45, 8\n"
    "s_lshl_b32 s45, s45, 13\n"
    "s_add_u32 s45, s45, s48\n"
    "s_add_u32 m0, s46, 0\n"
    "s_nop 0\n"
    "buffer_load_dwordx4 v250, s[8:11], s45 offen offset:0 sc1 lds\n"
    "buffer_load_dwordx4 v250, s[8:11], s45 offen offset:1024 sc1 lds\n"
    "s_branch .Lat_maxpass_ret\n"
    ".Lat_out:\n"
    "s_waitcnt lgkmcnt(0)\n"
    "s_mov_b64 s[2:3], s[0:1]\n"
    "s_mov_b64 %0, s[2:3]\n"
    "s_mov_b32 %1, s92\n"
    "s_mov_b32 %2, s93\n"
    : "=s"(ka), "=s"(b), "=s"(wid) : "0"(__builtin_amdgcn_kernarg_segment_ptr()), "1"(blockIdx.x)
    : "v0", "v1", "v2", "v3", "v4", "v5", "v6", "v7", "v8", "v9", "v10", "v11", "v12", "v13", "v14", "v15", "v16", "v17", "v18", "v19", "v20", "v21", "v22", "v23", "v24", "v25", "v26", "v27", "v28", "v29", "v30", "v31", "v32", "v33", "v34", "v35", "v36", "v37", "v38", "v39", "v40", "v41", "v42", "v43", "v44", "v45", "v46", "v47", "v48", "v49", "v50", "v51", "v52", "v53", "v54", "v55", "v56", "v57", "v58", "v59", "v60", "v61", "v62", "v63", "v64", "v65", "v66", "v67", "v68", "v69", "v70", "v71", "v72", "v73", "v74", "v75", "v76", "v77", "v78", "v79", "v80", "v81", "v82", "v83", "v84", "v85", "v86", "v87", "v88", "v89", "v90", "v91", "v92", "v93", "v94", "v95", "v96", "v97", "v98", "v99", "v100", "v101", "v102", "v103", "v104", "v105", "v106", "v107", "v108", "v109", "v110", "v111", "v112", "v113", "v114", "v115", "v116", "v117", "v118", "v119", "v120", "v121", "v122", "v123", "v124", "v125", "v126", "v127", "v128", "v129", "v130", "v131", "v132", "v133", "v134", "v135", "v136", "v137", "v138", "v139", "v140", "v141", "v142", "v143", "v144", "v145", "v146", "v147", "v148", "v149", "v150", "v151", "v152", "v153", "v154", "v155", "v156", "v157", "v158", "v159", "v160", "v161", "v162", "v163", "v164", "v165", "v166", "v167", "v168", "v169", "v170", "v171", "v172", "v173", "v174", "v175", "v176", "v177", "v178", "v179", "v180", "v181", "v182", "v183", "v184", "v185", "v186", "v187", "v188", "v189", "v190", "v191", "v192", "v193", "v194", "v195", "v196", "v197", "v198", "v199", "v200", "v201", "v202", "v203", "v204", "v205", "v206", "v207", "v208", "v209", "v210", "v211", "v212", "v213", "v214", "v215", "v216", "v217", "v218", "v219", "v220", "v221", "v222", "v223", "v224", "v225", "v226", "v227", "v228", "v229", "v230", "v231", "v232", "v233", "v234", "v235", "v236", "v237", "v238", "v239", "v240", "v241", "v242", "v243", "v244", "v245", "v246", "v247", "v248", "v249", "v250", "v251", "v252", "v253", "v254", "v255", "a0", "a1", "a2", "a3", "a4", "a5", "a6", "a7", "a8", "a9", "a10", "a11", "a12", "a13", "a14", "a15", "a16", "a17", "a18", "a19", "a20", "a21", "a22", "a23", "a24", "a25", "a26", "a27", "a28", "a29", "a30", "a31", "a32", "a33", "a34", "a35", "a36", "a37", "a38", "a39", "a40", "a41", "a42", "a43", "a44", "a45", "a46", "a47", "a48", "a49", "a50", "a51", "a52", "a53", "a54", "a55", "a56", "a57", "a58", "a59", "a60", "a61", "a62", "a63", "a64", "a65", "a66", "a67", "a68", "a69", "a70", "a71", "a72", "a73", "a74", "a75", "a76", "a77", "a78", "a79", "a80", "a81", "a82", "a83", "a84", "a85", "a86", "a87", "a88", "a89", "a90", "a91", "a92", "a93", "a94", "a95", "a96", "a97", "a98", "a99", "a100", "a101", "a102", "a103", "a104", "a105", "a106", "a107", "a108", "a109", "a110", "a111", "a112", "a113", "a114", "a115", "a116", "a117", "a118", "a119", "a120", "a121", "a122", "a123", "a124", "a125", "a126", "a127", "a128", "a129", "a130", "a131", "a132", "a133", "a134", "a135", "a136", "a137", "a138", "a139", "a140", "a141", "a142", "a143", "a144", "a145", "a146", "a147", "a148", "a149", "a150", "a151", "a152", "a153", "a154", "a155", "a156", "a157", "a158", "a159", "a160", "a161", "a162", "a163", "a164", "a165", "a166", "a167", "a168", "a169", "a170", "a171", "a172", "a173", "a174", "a175", "a176", "a177", "a178", "a179", "a180", "a181", "a182", "a183", "a184", "a185", "a186", "a187", "a188", "a189", "a190", "a191", "a192", "a193", "a194", "a195", "a196", "a197", "a198", "a199", "a200", "a201", "a202", "a203", "a204", "a205", "a206", "a207", "a208", "a209", "a210", "a211", "a212", "a213", "a214", "a215", "a216", "a217", "a218", "a219", "a220", "a221", "a222", "a223", "a224", "a225", "a226", "a227", "a228", "a229", "a230", "a231", "a232", "a233", "a234", "a235", "a236", "a237", "a238", "a239", "a240", "a241", "a242", "a243", "a244", "a245", "a246", "a247", "a248", "a249", "a250", "a251", "a252", "a253", "a254", "a255", "s2", "s3", "s4", "s5", "s6", "s7", "s8", "s9", "s10", "s11", "s12", "s13", "s14", "s15", "s16", "s17", "s18", "s19", "s20", "s21", "s22", "s23", "s24", "s25", "s26", "s27", "s28", "s29", "s30", "s31", "s32", "s33", "s34", "s35", "s36", "s37", "s38", "s39", "s40", "s41", "s42", "s43", "s44", "s45", "s46", "s47", "s48", "s49", "s50", "s51", "s52", "s53", "s54", "s55", "s56", "s57", "s58", "s59", "s60", "s61", "s62", "s63", "s80", "s81", "s82", "s83", "s84", "s85", "s86", "s87", "s88", "s89", "s90", "s91", "s92", "s93", "s94", "s95", "vcc", "m0", "scc", "memory");
  typedef __attribute__((address_space(1))) void* gp;
  helper(lds, (b >> 3) - 28, wid, (const int*)(gp)ka[2], (const int*)(gp)ka[4], (const u16*)(gp)ka[8], (const u16*)(gp)ka[9], (const u16*)(gp)ka[10], (char*)(gp)ka[3]);
}

extern "C" void kernel_launch(void* const* d_in, const int* in_sizes, int n_in, void* d_out, int out_size, void* d_ws,
                              size_t ws_size, hipStream_t stream) {
  const u16* x = (const u16*)d_in[0];
  const int* src = (const int*)d_in[1];
  const int* dst = (const int*)d_in[2];
  const u16* deg_inv = (const u16*)d_in[3];
  const u16* wmsg = (const u16*)d_in[4];
  const u16* wq = (const u16*)d_in[5];
  const u16* wk = (const u16*)d_in[6];
  const u16* wv = (const u16*)d_in[7];
  const u16* wout = (const u16*)d_in[8];
  char* ws = (char*)d_ws;
  hipFuncSetAttribute((const void*)gps_kernel, hipFuncAttributeMaxDynamicSharedMemorySize, 163840);
  gps_kernel<<<256, 256, 163840, stream>>>(wq, wk, src, ws, dst, x, (u16*)d_out, deg_inv, wv, wmsg, wout);
}
```

```cpp
#include <hip/hip_runtime.h>
#include <hip/hip_bf16.h>
#include <stdint.h>

typedef unsigned short u16;
typedef __attribute__((ext_vector_type(8))) short bf16x8;
typedef __attribute__((ext_vector_type(4))) short bf16x4;
typedef __attribute__((ext_vector_type(4))) float f32x4;
typedef __attribute__((ext_vector_type(2))) float f32x2;
typedef __attribute__((ext_vector_type(4))) unsigned u32x4;
typedef __attribute__((ext_vector_type(2))) unsigned u32x2;
#define DI __device__ __forceinline__

constexpr int NN = 10000, DD = 256, EE = 160000;
constexpr int NATT = 252, NSP = 4, SPN = 2560;
constexpr int GU = 5, GNB = 5; static_assert(GU * GNB <= 30, "list size");

constexpr unsigned WS_H = 0;
constexpr unsigned WS_AF = WS_H + 10240 * 512;
constexpr unsigned WS_WV = WS_AF + 135168;
constexpr unsigned WS_WM = WS_WV + 135168;
constexpr unsigned WS_XF = WS_WM + 135168;
constexpr unsigned WS_GF = WS_XF + 3328 * 128;
constexpr unsigned WS_HF = WS_GF + 2048;
constexpr unsigned WS_PF = WS_HF + 4096;
constexpr unsigned WS_NF = WS_PF + 4096;
constexpr unsigned WS_WF = WS_NF + 16384;
constexpr unsigned WS_LF = WS_WF + 256;
constexpr unsigned WS_EP = WS_LF + 512;
constexpr unsigned WS_G = WS_EP + 256 + 1024;
constexpr unsigned WS_QP = WS_G + 10240 * 512;
constexpr unsigned WS_XB = WS_QP + 10240 * 512;
constexpr unsigned WS_LS = WS_XB + 2 * NATT * 101376;
constexpr unsigned WS_BL = WS_LS + 524288;
constexpr unsigned WS_DBG = WS_BL + 64 * 260 * 64;
constexpr unsigned WS_END = WS_DBG + 1024 * 128 + 8192;

DI float bf2f(unsigned x) { return __uint_as_float(x << 16); }
DI float bfhi(unsigned x) { return __uint_as_float(x & 0xffff0000u); }
DI unsigned pk2(float lo, float hi) {
  typedef __attribute__((ext_vector_type(2))) __bf16 bf2_t;
  const bf2_t r = __builtin_convertvector((f32x2){lo, hi}, bf2_t);
  return __builtin_bit_cast(unsigned, r);
}
typedef __attribute__((address_space(3))) bf16x4* lds_bf16x4_p;
DI bf16x4 tr_read(unsigned p) { return __builtin_amdgcn_ds_read_tr16_b64_v4i16((lds_bf16x4_p)p); }
DI bf16x8 as_bf(u32x4 v) { return __builtin_bit_cast(bf16x8, v); }
#define SB() __builtin_amdgcn_sched_barrier(0)
#define MFMA16(a, b, c) __builtin_amdgcn_mfma_f32_16x16x32_bf16(a, b, c, 0, 0, 0)

typedef __amdgpu_buffer_rsrc_t srd_t;
DI void drain() { asm volatile("s_waitcnt vmcnt(0)" ::: "memory"); }
DI void st_flag(srd_t w, unsigned off, unsigned tag) { __builtin_amdgcn_raw_buffer_store_b32(tag, w, off, 0, 16); }
DI void wait_flag(srd_t w, unsigned off, unsigned tag) {
#pragma unroll 1
  for (int it = 0; it < (1 << 22); ++it) {
    unsigned v;
    asm volatile("buffer_load_dword %0, %1, %2, 0 offen sc1\n s_waitcnt vmcnt(0)" : "=v"(v) : "v"(off), "s"(w) : "memory");
    if (__ballot(v != tag) == 0) break;
    __builtin_amdgcn_s_sleep(2);
    asm volatile("" ::: "memory");
  }
  asm volatile("" ::: "memory");
}

DI void prod_store(const f32x4 (&acc)[2][2], int mat, int ks, int J, int lane, srd_t wsrd) {
#pragma unroll
  for (int s = 0; s < 2; ++s) {
    u32x4 o = {pk2(acc[0][s][0], acc[0][s][1]), pk2(acc[0][s][2], acc[0][s][3]), pk2(acc[1][s][0], acc[1][s][1]), pk2(acc[1][s][2], acc[1][s][3])};
    if (ks == 7 && lane >= 48) o[3] &= 0xffffu;
    const int T = 2 * J + s;
    const int at = mat * 135168 + ks * 4096 + (T & 3) * 1024;
    __builtin_amdgcn_raw_buffer_store_b128(o, wsrd, WS_WV + (T >> 2) * 33792 + at + lane * 16, 0, 16);
  }
}
DI void corr(unsigned* dl, const unsigned (&lr)[16]) {
  float a0 = 0.f, a1 = 0.f;
#pragma unroll
  for (int r = 0; r < 16; ++r) { a0 += bf2f(lr[r] & 0xffffu); a1 += bfhi(lr[r]); }
  dl[0] = pk2(a0 * -0.0625f, a1 * -0.0625f);
  dl[128] = pk2((bf2f(lr[15] & 0xffffu) - 0.25f * a0) * (1.f / 3.f), (bfhi(lr[15]) - 0.25f * a1) * (1.f / 3.f));
}
DI void product_load(char* lds, int wid, int lane, int job, const u16* __restrict__ wv, const u16* __restrict__ wm, const u16* __restrict__ wo) {
  const unsigned ldsb = (unsigned)(size_t)lds;
  const int r = lane >> 3, sw = ((lane & 7) ^ (r & 6)) * 16 + wid * 128;
  const unsigned vp = (8 * (r >> 2) + (r & 3)) * 512 + sw, vn = r * 512 + sw;
  auto image = [&](int n, const u16* w, int row0, bool nat) {
#pragma unroll
    for (int i = 0; i < 8; ++i) {
      const int a = i & 3;
      asm volatile("s_mov_b32 m0, %2\n s_nop 0\n global_load_lds_dwordx4 %0, %1"
                   :: "v"(nat ? vn : vp), "s"(w + (row0 + (i >> 2) * 32 + (nat ? 8 * a : 16 * (a & 1) + 4 * (a >> 1))) * DD),
                      "s"(ldsb + n * 32768 + (i >> 2) * 16384 + wid * 4096 + a * 1024) : "m0", "memory");
    }
  };
  image(2, wv, (job >> 2) * 64, true);
  image(3, wm, (job >> 2) * 64, false);
#pragma unroll
  for (int k = 0; k < 8; ++k)
    asm volatile("s_mov_b32 m0, %2\n s_nop 0\n global_load_lds_dwordx4 %0, %1"
                 :: "v"((lane >> 3) * 512 + (lane & 7) * 16), "s"(wo + (k * 32 + wid * 8) * DD + (job & 3) * 64), "s"(ldsb + k * 4096 + wid * 1024) : "m0", "memory");
}
DI void product_w(char* lds, int wid, int lane, int job, srd_t wsrd) {
  const int m = lane & 15, g = lane >> 4;
  const int ks = 2 * (job >> 2) + (wid >> 1), J = 2 * (job & 3) + (wid & 1);
  const unsigned ldsb = (unsigned)(size_t)lds;
  unsigned* dl = reinterpret_cast<unsigned*>(lds + 0x20000);
  const bool hi = (m >> 3) != 0;
  const unsigned ra = (m >> 3) * 1024 + (m & 7) * 128 + (g ^ (m & 6)) * 16;
  auto frag = [&](int n, int tl, int t, int k) {
    return *reinterpret_cast<const bf16x8*>(lds + n * 32768 + tl * 16384 + (k >> 1) * 4096 + t * 2048 + (ra ^ ((k & 1) * 64)));
  };
  f32x4 av[2][2] = {}, am[2][2] = {}, ad[2] = {};
  const unsigned tb = ldsb + (8 * g + ((lane >> 2) & 3)) * 128 + (wid & 1) * 64 + (lane & 3) * 16;
  bf16x8 d0[8], d1[8], d2[8], rr[8][2], vf[8][2], mf[8][2];
#pragma unroll
  for (int kk = 0; kk < 9; ++kk) {
    if (kk < 8) {
      d0[kk] = *reinterpret_cast<const bf16x8*>(dl + 512 + 16 * kk + 4 * g); d1[kk] = *reinterpret_cast<const bf16x8*>(dl + 512 + (ks == 7 ? 128 : 0) + 16 * kk + 4 * g);
      d2[kk] = *reinterpret_cast<const bf16x8*>(dl + 768 + (ks == 7 && hi ? 128 : 0) + 16 * kk + 4 * g);
#pragma unroll
      for (int s = 0; s < 2; ++s) {
        rr[kk][s] = __builtin_shufflevector(tr_read(tb + kk * 4096 + s * 8), tr_read(tb + kk * 4096 + s * 8 + 512), 0, 1, 2, 3, 4, 5, 6, 7);
        vf[kk][s] = frag(2, wid >> 1, s, kk); mf[kk][s] = frag(3, wid >> 1, s, kk);
      }
    }
    SB();
    if (kk) {
      const int k = kk - 1;
#pragma unroll
      for (int s = 0; s < 2; ++s) {
        av[0][s] = MFMA16(d0[k], rr[k][s], av[0][s]); av[1][s] = MFMA16(d1[k], rr[k][s], av[1][s]); ad[s] = MFMA16(d2[k], rr[k][s], ad[s]);
#pragma unroll
        for (int t = 0; t < 2; ++t) { av[t][s] = MFMA16(vf[k][t], rr[k][s], av[t][s]); am[t][s] = MFMA16(mf[k][t], rr[k][s], am[t][s]); }
      }
    }
    SB();
  }
#pragma unroll
  for (int t = 0; t < 2; ++t)
#pragma unroll
    for (int s = 0; s < 2; ++s) am[t][s] += ad[s];
  prod_store(av, 0, ks, J, lane, wsrd);
  prod_store(am, 1, ks, J, lane, wsrd);
}

DI void helper(char* lds, int j, int wid, const int* __restrict__ src, const int* __restrict__ dst,
               const u16* __restrict__ wv, const u16* __restrict__ wm, const u16* __restrict__ wo, char* ws) {
  unsigned* img = reinterpret_cast<unsigned*>(lds);
  char* cntb = lds + 0x20000;
  int* cnt = reinterpret_cast<int*>(cntb);
  unsigned* ub = reinterpret_cast<unsigned*>(lds + 0x22c00);
  int* red = reinterpret_cast<int*>(ub + 256);
  u16* dg = reinterpret_cast<u16*>(lds + 0x23400);
  u16* tmp = reinterpret_cast<u16*>(lds + 0x25000) + wid * 512;
  int lane; asm volatile("v_mbcnt_lo_u32_b32 %0, -1, 0\n\tv_mbcnt_hi_u32_b32 %0, -1, %0" : "=v"(lane));
  const int tid = wid * 64 + lane, lo = j * SPN, nn_ = min(SPN, NN - lo);
  const srd_t wsrd = __builtin_amdgcn_make_buffer_rsrc(ws, 0, WS_END, 0x00020000);
  const unsigned tag = __builtin_amdgcn_raw_buffer_load_b32(wsrd, WS_EP, 0, 0) + 4u;
  auto touch = [&](unsigned at) { asm volatile("s_mov_b32 m0, 0x27c00\n s_nop 0\n buffer_load_dword %0, %1, 0 offen sc1 lds" :: "v"(at), "s"(wsrd) : "m0", "memory"); };
  touch(lane < 16 ? min(WS_GF + (lane * 16 + wid * 4 + j) * 128, WS_EP) : 0x40000000u);
  for (int i = tid; i < 8192; i += 256) { const unsigned f = (i & 63) >= 60 ? 0u : ~0u; reinterpret_cast<u32x4*>(img)[i] = (u32x4){f, f, f, f}; }
  for (int i = tid; i < SPN + 64; i += 256) cnt[i] = 0;
  ub[tid] = 0u;
  if (tid < 8) red[tid] = 0;
  touch(WS_XF + 262144 + (j * 256 + tid) * 128);
  wait_flag(wsrd, WS_NF + j * 4096 + min(tid, 249) * 16, tag);
  __syncthreads();
  const unsigned dum = (SPN + lane) << 18;
  auto fetch = [&](int k) {
    return __builtin_amdgcn_raw_buffer_load_b128(wsrd, WS_BL + (k < 48 ? tid * 16 + (k & 7) * 4096 : (tid >> 2) * 128 + (tid & 3) * 16 + (k & 3) * 8192 + (k & 4) * 16), (j * 8 + (k >> 3)) * 33280, 16);
  };
  auto clean = [&](u32x4& e, int k) {
#pragma unroll
    for (int r = 0; r < 4; ++r) e[r] = (k < 48 ? (k & 7) == 7 && tid >= 208 : (k & 3) == 3 && tid >= 232) ? dum : min(e[r], dum);
  };
  auto slot = [&](unsigned e) { return reinterpret_cast<int*>(cntb + (e >> 16)); };
  constexpr int NL = 52;
  u32x4 ent[NL];
#pragma unroll
  for (int k = 0; k < NL; ++k) ent[k] = fetch(k);
  SB();
#pragma unroll
  for (int k = 0; k < NL; ++k) {
    clean(ent[k], k);
    int* p[4];
#pragma unroll
    for (int r = 0; r < 4; ++r) p[r] = slot(ent[k][r]);
    SB();
#pragma unroll
    for (int r = 0; r < 4; ++r) atomicAdd(p[r], 2);
    SB();
  }
  unsigned use = 0;
  const unsigned dum3 = (tid & 3) == 3 ? dum : 0;
#pragma unroll
  for (int k = 0; k < 4; ++k) use |= __ballot(ent[NL - 4 + k][3] < dum3) ? 1u << k : 0u;
  if (use)
#pragma unroll 1
    for (int k = NL; k < 64; ++k)
      if (use >> (k - NL) & 1) {
        u32x4 e = fetch(k); clean(e, k);
#pragma unroll
        for (int r = 0; r < 4; ++r) atomicAdd(slot(e[r]), 2);
        if (__ballot(e[3] < dum3)) { use |= 16u << (k - NL); if (k >= 60) red[7] = 1; }
      }
  __syncthreads();
  int inc = 0, nu[10], cn[10], cm = 0;
  auto units = [&](int c) { return max(1, (c + 6) >> 3); };
#pragma unroll
  for (int i = 0; i < 10; i += 2) { const u32x2 v = *reinterpret_cast<u32x2*>(cnt + 10 * tid + i); cn[i] = v[0]; cn[i + 1] = v[1]; }
  const int nup = units(cnt[max(10 * tid - 1, 0)]);
  const bool act = 10 * tid < nn_;
#pragma unroll
  for (int i = 0; i < 10; ++i) { cm = max(cm, cn[i]); nu[i] = act ? units(cn[i]) : 0; inc += nu[i]; }
  const int own = inc;
  inc += __builtin_amdgcn_update_dpp(0, inc, 0x111, 15, 15, true); inc += __builtin_amdgcn_update_dpp(0, inc, 0x112, 15, 15, true);
  inc += __builtin_amdgcn_update_dpp(0, inc, 0x114, 15, 15, true); inc += __builtin_amdgcn_update_dpp(0, inc, 0x118, 15, 15, true);
  inc += __builtin_amdgcn_update_dpp(0, inc, 0x142, 10, 15, false); inc += __builtin_amdgcn_update_dpp(0, inc, 0x143, 12, 15, false);
  if (lane == 63) red[wid] = inc;
  const bool srt = __ballot(cm > 64);
  __syncthreads();
  const u32x4 rd = *reinterpret_cast<u32x4*>(red);
  const float rs_ = 64.f / (float)(rd[0] + rd[1] + rd[2] + rd[3]);
  inc += (wid > 0 ? rd[0] : 0) + (wid > 1 ? rd[1] : 0) + (wid > 2 ? rd[2] : 0) - own;
  int wv_[10];
  {
    int o = inc, wp = tid ? min(127, (int)((float)(2 * o - nup) * rs_)) : 128;
#pragma unroll
    for (int i = 0; i < 10; ++i) {
      wv_[i] = min(127, (int)((float)(2 * o + nu[i]) * rs_));
      ub[act && wv_[i] != wp ? wv_[i] : 128 + lane] = (unsigned)o << 12 | (unsigned)(10 * tid + i);
      wp = wv_[i]; o += nu[i];
    }
  }
  __syncthreads();
  {
    int u0[10], top = 0;
#pragma unroll
    for (int i = 0; i < 10; ++i) u0[i] = ub[wv_[i]] >> 12;
#pragma unroll
    for (int i = 0; i < 10; ++i) {
      const int lu = inc - u0[i], e = lu + nu[i] - 1 + GU, q = min(e / (4 * GU), GNB);
      u0[i] = wv_[i] * 1024 + lu * 8; top = max(top, q);
      atomicOr(img + wv_[i] * 256 + 244 + q, act ? 1u << (e - q * 4 * GU) : 0u);
      inc += nu[i];
    }
#pragma unroll
    for (int i = 0; i < 10; i += 2) *reinterpret_cast<u32x2*>(cnt + 10 * tid + i) = (u32x2){(unsigned)u0[i], (unsigned)u0[i + 1]};
    if (tid < 64) cnt[SPN + tid] = 0x30000;
    if (act && top == GNB) red[7] = 1;
  }
  __syncthreads();
  const bool big_ = red[7];
  if (tid < 128) {
    unsigned nb = 0;
#pragma unroll
    for (int q = 0; q < GNB; ++q) nb = img[tid * 256 + 244 + q] ? q + 1 : nb;
    *reinterpret_cast<u32x4*>(img + tid * 256 + 240) = (u32x4){big_ ? 0u : nb, WS_G + (lo + (ub[tid] & 4095u)) * 512, big_ || (tid & 3) ? 0x40000000u : WS_GF + j * 512 + tid * 4, 0u};
  }
  if (!big_) {
    const unsigned dmp = 0x27000 + lane * 4;
    typedef __attribute__((address_space(3))) u16* lds_u16_p;
    unsigned p[2][8];
#pragma unroll
    for (int k = 0; k < NL + 2; k += 2) {
      if (k < NL) {
        int* q[8];
#pragma unroll
        for (int r = 0; r < 8; ++r) q[r] = slot(ent[k + (r >> 2)][r & 3]);
        SB();
#pragma unroll
        for (int r = 0; r < 8; ++r) p[k >> 1 & 1][r] = atomicAdd(q[r], 2);
        SB();
      }
      if (k) {
#pragma unroll
        for (int r = 0; r < 8; ++r) p[~k >> 1 & 1][r] = min(p[~k >> 1 & 1][r], dmp);
        SB();
#pragma unroll
        for (int r = 0; r < 8; ++r) *(lds_u16_p)p[~k >> 1 & 1][r] = ent[k - 2 + (r >> 2)][r & 3];
        SB();
      }
    }
    if (use)
#pragma unroll 1
      for (int k = NL; k < 64; ++k)
        if (use >> (k - NL) & 1) {
          u32x4 e = fetch(k); clean(e, k);
#pragma unroll
          for (int r = 0; r < 4; ++r) *(lds_u16_p)min((unsigned)atomicAdd(slot(e[r]), 2), dmp) = e[r];
        }
    __syncthreads();
    if (srt) {
#pragma unroll
      for (int i = 0; i < 10; ++i) dg[10 * tid + i] = cn[i] >> 1;
      for (unsigned long long big = __ballot(cm > 64); big; big &= big - 1)
      for (int n = 10 * (wid * 64 + __builtin_ctzll(big)), n1 = n + 10; n < n1; ++n) {
        const int d = __builtin_amdgcn_readfirstlane(dg[n]);
        if (d <= 32) continue;
        u16* f = reinterpret_cast<u16*>(lds + cnt[n]) - d;
        for (int i = lane; i < d + 8; i += 64) tmp[i] = i < d ? f[i] : 0xffff;
        __builtin_amdgcn_wave_barrier();
        for (int i = lane; i < d; i += 64) {
          const unsigned key = tmp[i] << 9 | i; int p_ = 0;
          for (int q = 0; q < d; q += 8) {
            const u32x4 v = *reinterpret_cast<u32x4*>(tmp + q);
#pragma unroll
            for (int e = 0; e < 4; ++e) p_ += (((v[e] & 0xffffu) << 9 | (q + 2 * e)) < key) + (((v[e] >> 16) << 9 | (q + 2 * e + 1)) < key);
          }
          f[p_] = key >> 9;
        }
        __builtin_amdgcn_wave_barrier();
      }
    }
  }
  __syncthreads();
#pragma unroll 8
  for (int k = 0; k < 32; ++k)
    __builtin_amdgcn_raw_buffer_store_b128(reinterpret_cast<u32x4*>(img)[k * 256 + tid], wsrd, WS_LS + j * 131072 + (k * 256 + tid) * 16, 0, 16);
  unsigned lr[16];
#pragma unroll
  for (int r = 0; r < 16; ++r) lr[r] = reinterpret_cast<const unsigned*>((wid >= 2 ? wm : wv) + (240 + r) * DD)[tid & 127];
  wait_flag(wsrd, WS_HF + min(tid, NATT - 1) * 16, tag);
  drain();
  __syncthreads();
  if (tid == 0) st_flag(wsrd, WS_LF + j * 128, tag);
  touch(WS_XF + (j * 256 + tid) * 256);
  touch(wid ? 0x40000000u : WS_GF - 32768 + (j * 64 + lane) * 128);
  corr(reinterpret_cast<unsigned*>(lds + 0x20000) + 512 + (wid >> 1) * 256 + (tid & 127), lr);
#pragma unroll 1
  for (int job = 4 * j; job < 4 * j + 4; ++job) {
    product_load(lds, wid, lane, job, wv, wm, wo);
    drain();
    __syncthreads();
    product_w(lds, wid, lane, job, wsrd);
    __syncthreads();
  }
  drain();
  __syncthreads();
  if (tid == 0) st_flag(wsrd, WS_WF + j * 4, tag);
  if (big_) {
    const srd_t msrd = __builtin_amdgcn_make_buffer_rsrc(ws + WS_H, 0, 10240 * 512, 0x00020000);
    bf16x8 b1;
#pragma unroll
    for (int e = 0; e < 8; ++e) b1[e] = (lane & 15) == e + ((lane >> 1) & 8) ? 0x3f80 : 0;
    for (int n = wid; n < nn_; n += 4) {
      f32x4 acc = {};
      for (int e0 = 0; e0 < EE; e0 += 64) {
        unsigned long long m = __ballot(dst[e0 + lane] == lo + n);
        while (m) {
          const int e = e0 + __builtin_ctzll(m); m &= m - 1;
          const unsigned vo = lane < 32 ? (unsigned)src[e] * 512 + lane * 16 : 0x7fffff00u;
          acc = MFMA16(b1, as_bf(__builtin_amdgcn_raw_buffer_load_b128(msrd, vo, 0, 16)), acc);
        }
      }
      __builtin_amdgcn_raw_buffer_store_b64((u32x2){pk2(acc[0], acc[1]), pk2(acc[2], acc[3])}, wsrd,
                                            WS_G + (lo + n) * 512 + (lane >> 5) * 256 + (lane & 15) * 16 + (lane & 16) / 2, 0, 16);
    }
    drain();
    __syncthreads();
    if (tid < 32) st_flag(wsrd, WS_GF + j * 512 + tid * 16, tag);
  }
}

constexpr unsigned CODE_BYTES = 76000;
extern "C" __global__ __launch_bounds__(256, 1) void gps_kernel(const u16* wq, const u16* wk, const int* src, char* ws, const int* dst, const u16* x, u16* out,
                                                                const u16* deg_inv, const u16* wv, const u16* wm, const u16* wo) {
  extern __shared__ __attribute__((aligned(16))) char lds[];
  static_assert(WS_END == 69410816 && WS_EP == 6101760, "workspace layout of the generated code");
  typedef void* const __attribute__((address_space(4)))* arg_p;
  arg_p ka; int b, wid;
  asm volatile(
    ".Lat_0:\n"
    "s_mov_b32 s92, s2\n"
    "s_load_dwordx2 s[2:3], s[0:1], 0x0\n"
    "s_load_dwordx4 s[4:7], s[0:1], 0x8\n"
    "s_load_dwordx8 s[8:15], s[0:1], 0x18\n"
    "s_load_dwordx2 s[28:29], s[0:1], 0x38\n"
    "s_getpc_b64 s[30:31]\n"
    ".Lat_pc:\n"
    "s_bfe_u32 s45, s92, 0x20003\n"
    "v_lshlrev_b32_e32 v1, 7, v0\n"
    "s_lshl_b32 s45, s45, 15\n"
    "s_mov_b32 m0, 0x27f00\n"
    "v_add_u32_e32 v1, s45, v1\n"
    "v_min_u32_e32 v1, 75488, v1\n"
    "v_readfirstlane_b32 s93, v0\n"
    "global_load_lds_dword v1, s[30:31]\n"
    "s_and_b32 s45, s92, 249\n"
    "s_lshr_b32 s93, s93, 6\n"
    "s_cmp_eq_u32 s45, 249\n"
    "s_cbranch_scc1 .Lat_out\n"
    "v_mbcnt_lo_u32_b32 v24, -1, 0\n"
    "v_mbcnt_hi_u32_b32 v24, -1, v24\n"
    "s_and_b32 s50, s93, 1\n"
    "s_lshr_b32 s51, s93, 1\n"
    "v_lshrrev_b32_e32 v25, 3, v24\n"
    "v_and_b32_e32 v26, 6, v25\n"
    "v_and_b32_e32 v27, 7, v24\n"
    "v_xor_b32_e32 v26, v26, v27\n"
    "v_lshlrev_b32_e32 v26, 4, v26\n"
    "v_and_b32_e32 v27, 4, v25\n"
    "v_add_u32_e32 v27, v27, v25\n"
    "v_lshl_add_u32 v251, v27, 9, v26\n"
    "v_add_u32_e32 v252, 0x1c00, v251\n"
    "s_mov_b32 s22, 0x20000\n"
    "s_mov_b32 s23, 0x20000\n"
    "s_mov_b32 s26, 0x20000\n"
    "s_mov_b32 s27, 0x20000\n"
    "s_lshr_b32 s36, s92, 7\n"
    "s_mul_i32 s36, s36, 3\n"
    "s_add_u32 s36, s36, s92\n"
    "s_and_b32 s36, s36, 127\n"
    "s_lshl_b32 s45, s93, 7\n"
    "s_lshl_b32 s46, s93, 12\n"
    "s_and_b32 s47, s36, 7\n"
    "s_lshl_b32 s47, s47, 14\n"
    "s_mov_b32 m0, s46\n"
    "s_add_u32 s47, s47, s45\n"
    "s_and_b32 s34, s92, 7\n"
    "s_lshl_b32 s34, s34, 5\n"
    "s_lshr_b32 s32, s92, 3\n"
    "s_add_u32 s34, s34, s32\n"
    "s_lshl_b32 s32, s34, 6\n"
    "s_or_b32 s32, s32, 63\n"
    "s_and_b32 s32, s32, 255\n"
    "s_cmp_ge_u32 s34, 252\n"
    "s_cselect_b32 s34, s32, s34\n"
    "s_and_b32 s32, s34, 3\n"
    "s_lshr_b32 s33, s34, 2\n"
    "s_or_b32 s38, s33, 16\n"
    "s_cmp_eq_u32 s38, 62\n"
    "s_cselect_b32 s38, 16, 0\n"
    "s_xor_b32 s33, s33, s38\n"
    "s_lshl2_add_u32 s34, s33, s32\n"
    "s_mul_i32 s38, s33, 81920\n"
    "s_lshl_b32 s35, s32, 13\n"
    "s_and_b32 s55, s35, 0x2000\n"
    "s_add_u32 s35, s35, s38\n"
    "s_add_u32 s55, s55, s38\n"
    "s_waitcnt lgkmcnt(0)\n"
    "s_mov_b64 s[20:21], s[2:3]\n"
    "s_mov_b64 s[24:25], s[4:5]\n"
    "buffer_load_dwordx4 v251, s[20:23], s47 offen offset:0 lds\n"
    "s_add_u32 s48, s47, 1024\n"
    "buffer_load_dwordx4 v252, s[20:23], s47 offen offset:3072 lds\n"
    "s_add_u32 s47, s47, 0x1800\n"
    "buffer_load_dwordx4 v251, s[20:23], s48 offen offset:1024 lds\n"
    "buffer_load_dwordx4 v251, s[20:23], s47 offen offset:2048 lds\n"
    "s_lshr_b32 s47, s36, 4\n"
    "s_lshl_b32 s47, s47, 14\n"
    "s_and_b32 s48, s36, 8\n"
    "s_lshl_b32 s48, s48, 8\n"
    "s_add_u32 s47, s47, s48\n"
    "s_add_u32 m0, s46, 0x4000\n"
    "s_add_u32 s47, s47, s45\n"
    "buffer_load_dwordx4 v251, s[24:27], s47 offen offset:0 lds\n"
    "buffer_load_dwordx4 v252, s[24:27], s47 offen offset:1024 lds\n"
    "s_add_u32 s47, s45, 120832\n"
    "buffer_load_dwordx4 v251, s[24:27], s47 offen offset:2048 lds\n"
    "s_add_u32 s47, s47, 1024\n"
    "buffer_load_dwordx4 v251, s[24:27], s47 offen offset:3072 lds\n"
    "s_mov_b64 s[16:17], s[14:15]\n"
    "s_mov_b64 s[86:87], s[10:11]\n"
    "s_mov_b32 s14, 5120000\n"
    "s_mov_b32 s15, 0x20000\n"
    "s_lshl_b32 s45, s93, 2\n"
    "s_add_u32 s54, s35, 0x8000\n"
    "s_add_u32 s55, s55, 0x10000\n"
    "v_lshlrev_b32_e32 v250, 4, v24\n"
    "v_lshrrev_b32_e32 v28, 4, v24\n"
    "v_add_u32_e32 v28, s45, v28\n"
    "v_and_b32_e32 v104, 0xf0, v250\n"
    "v_lshl_add_u32 v104, v28, 9, v104\n"
    "buffer_load_dwordx4 v[0:3], v104, s[12:15], s35 offen offset:0\n"
    "buffer_load_dwordx4 v[4:7], v104, s[12:15], s35 offen offset:256\n"
    "s_cmp_lt_u32 s32, 2\n"
    "s_cselect_b32 s60, s55, 2.0\n"
    "buffer_load_dwordx4 v[8:11], v104, s[12:15], s54 offen offset:0\n"
    "buffer_load_dwordx4 v[12:15], v104, s[12:15], s54 offen offset:256\n"
    "buffer_load_dwordx4 v[16:19], v104, s[12:15], s55 offen offset:0\n"
    "buffer_load_dwordx4 v[20:23], v104, s[12:15], s55 offen offset:256\n"
    "s_min_u32 s46, s34, 249\n"
    "s_mul_i32 s46, s46, 2560\n"
    "s_lshl_b32 s47, s50, 10\n"
    "s_add_u32 s47, s47, s46\n"
    "v_add_u32_e32 v108, s47, v250\n"
    "s_lshl_b32 s47, s50, 8\n"
    "s_add_u32 s47, s47, s46\n"
    "s_add_u32 s47, s47, 2048\n"
    "v_lshl_add_u32 v109, v24, 2, s47\n"
    "s_cmp_lg_u32 s51, 0\n"
    "s_cselect_b64 exec, -1, 1\n"
    "global_load_dwordx4 v[144:147], v108, s[86:87]\n"
    "global_load_dword v148, v109, s[86:87]\n"
    "global_load_dwordx4 v[152:155], v108, s[6:7]\n"
    "global_load_dword v156, v109, s[6:7]\n"
    "s_mov_b64 exec, -1\n"
    "s_mov_b32 s10, 69410816\n"
    "s_mov_b32 s11, 0x20000\n"
    "s_mov_b32 s38, 6101760\n"
    "s_buffer_load_dword s39, s[8:11], s38\n"
    "s_mov_b32 s49, 0\n"
    "v_and_b32_e32 v27, 1, v25\n"
    "v_lshl_add_u32 v248, v27, 9, v26\n"
    "v_lshrrev_b32_e32 v27, 1, v25\n"
    "v_lshl_add_u32 v248, v27, 12, v248\n"
    "s_mul_i32 s43, s50, 3\n"
    "s_xor_b32 s43, s43, s51\n"
    "s_lshl_b32 s43, s43, 15\n"
    "s_add_u32 s43, s43, 32768\n"
    "s_mul_i32 s42, s93, 5120\n"
    "s_lshl_b32 s47, s50, 8\n"
    "s_mul_i32 s40, s32, 1277952\n"
    "s_lshl_b32 s41, s51, 14\n"
    "s_add_u32 s40, s40, s41\n"
    "s_add_u32 s40, s40, s47\n"
    "s_add_u32 s41, s40, 128\n"
    "v_bfe_u32 v25, v24, 2, 3\n"
    "v_lshlrev_b32_e32 v244, 7, v25\n"
    "v_lshrrev_b32_e32 v26, 5, v24\n"
    "v_lshl_add_u32 v244, v26, 10, v244\n"
    "v_and_b32_e32 v25, 6, v25\n"
    "v_bfe_u32 v26, v24, 1, 1\n"
    "v_xor_b32_e32 v25, v25, v26\n"
    "v_lshl_add_u32 v244, v25, 4, v244\n"
    "v_and_b32_e32 v26, 1, v24\n"
    "v_lshl_add_u32 v244, v26, 3, v244\n"
    "s_lshl_b32 s47, s51, 16\n"
    "s_add_u32 s47, s47, 32768\n"
    "v_add_u32_e32 v244, s47, v244\n"
    "v_xor_b32_e32 v245, 32, v244\n"
    "v_xor_b32_e32 v246, 64, v244\n"
    "v_xor_b32_e32 v247, 0x60, v244\n"
    "v_and_b32_e32 v25, 7, v24\n"
    "v_lshlrev_b32_e32 v27, 7, v25\n"
    "v_bfe_u32 v26, v24, 3, 1\n"
    "v_lshl_add_u32 v27, v26, 10, v27\n"
    "v_and_b32_e32 v25, 6, v24\n"
    "v_lshrrev_b32_e32 v26, 4, v24\n"
    "v_xor_b32_e32 v25, v25, v26\n"
    "v_lshl_add_u32 v27, v25, 4, v27\n"
    "s_lshl_b32 s47, s51, 15\n"
    "s_add_u32 s48, s47, 32768\n"
    "s_sub_u32 s47, 131072, s47\n"
    "v_add_u32_e32 v240, s48, v27\n"
    "v_xor_b32_e32 v241, 64, v240\n"
    "v_add_u32_e32 v242, s47, v27\n"
    "v_xor_b32_e32 v243, 64, v242\n"
    "s_xor_b32 s47, s93, 2\n"
    "s_mul_i32 s47, s47, 5120\n"
    "v_lshl_add_u32 v249, v24, 2, s47\n"
    "v_and_b32_e32 v29, 15, v24\n"
    "s_cmp_ge_u32 s36, 112\n"
    "s_cselect_b32 s47, 8, 16\n"
    "v_cmp_le_u32_e32 vcc, s47, v29\n"
    "v_mov_b32_e32 v64, 0xbd80bd80\n"
    "v_mov_b32_e32 v65, 0xbdabbdab\n"
    "v_mov_b32_e32 v66, 0x392b392b\n"
    "v_cndmask_b32_e32 v64, v64, v65, vcc\n"
    "v_cndmask_b32_e32 v66, 0, v66, vcc\n"
    "s_cselect_b32 s47, 56, 64\n"
    "v_cmp_le_u32_e32 vcc, s47, v24\n"
    "v_mov_b32_e32 v65, 0x3e80bdab\n"
    "v_mov_b32_e32 v67, 0x392b\n"
    "v_cndmask_b32_e32 v65, v64, v65, vcc\n"
    "v_cndmask_b32_e32 v67, v66, v67, vcc\n"
    "s_cmp_ge_u32 s36, 120\n"
    "s_cselect_b32 s47, 15, 16\n"
    "v_cmp_ne_u32_e32 vcc, s47, v29\n"
    "v_mov_b32_e32 v68, 0x3db8aa3b\n"
    "s_lshr_b32 s47, s36, 5\n"
    "v_cndmask_b32_e32 v68, 0, v68, vcc\n"
    "s_mul_i32 s47, s47, 33792\n"
    "s_and_b32 s48, s36, 7\n"
    "s_lshl_b32 s48, s48, 11\n"
    "s_add_u32 s47, s47, s48\n"
    "s_and_b32 s48, s36, 8\n"
    "s_lshl_b32 s48, s48, 7\n"
    "s_add_u32 s47, s47, s48\n"
    "s_and_b32 s48, s36, 16\n"
    "s_lshl_b32 s48, s48, 10\n"
    "s_add_u32 s47, s47, s48\n"
    "s_add_u32 s52, s47, 5242880\n"
    "s_lshl_b32 s48, s50, 9\n"
    "s_add_u32 s52, s52, s48\n"
    "s_lshl_b32 s48, s50, 11\n"
    "v_xor_b32_e32 v75, 64, v27\n"
    "v_add_u32_e32 v72, s48, v27\n"
    "v_add_u32_e32 v73, s48, v75\n"
    "v_and_b32_e32 v74, 0x1f0, v250\n"
    "v_lshrrev_b32_e32 v29, 5, v24\n"
    "v_lshl_add_u32 v74, v29, 3, v74\n"
    "s_lshl1_add_u32 s53, s36, s50\n"
    "s_lshl4_add_u32 s53, s53, 6080512\n"
    "s_cmp_eq_u32 s51, 0\n"
    "s_cselect_b32 s52, s52, 2.0\n"
    "s_cselect_b32 s53, s53, 2.0\n"
    "s_lshl_b32 s47, s93, 10\n"
    "v_add_u32_e32 v108, s47, v250\n"
    "v_lshrrev_b32_e32 v109, 7, v108\n"
    "v_mul_u32_u24_e32 v109, 0x8200, v109\n"
    "v_and_b32_e32 v32, 0x70, v250\n"
    "v_or_b32_e32 v109, v109, v32\n"
    "v_add_u32_e32 v108, 81920, v108\n"
    "v_mov_b64_e32 v[32:33], -1\n"
    "v_mov_b64_e32 v[34:35], -1\n"
    "ds_write_b128 v108, v[32:35] offset:0\n"
    "ds_write_b128 v108, v[32:35] offset:16\n"
    "v_and_b32_e32 v29, 6, v28\n"
    "v_xor_b32_e32 v29, v29, v24\n"
    "v_and_b32_e32 v29, 7, v29\n"
    "v_lshlrev_b32_e32 v105, 7, v28\n"
    "v_lshl_add_u32 v105, v29, 4, v105\n"
    "v_bfe_u32 v29, v24, 3, 1\n"
    "v_subrev_u32_e32 v29, s32, v29\n"
    "v_and_b32_e32 v29, 3, v29\n"
    "v_lshl_add_u32 v105, v29, 11, v105\n"
    "v_add_u32_e32 v105, 32768, v105\n"
    "v_xor_b32_e32 v106, 0x1000, v105\n"
    "v_and_b32_e32 v29, 15, v24\n"
    "v_cmp_eq_u32_e32 vcc, 15, v29\n"
    "v_mov_b32_e32 v107, 0xffff\n"
    "s_mov_b32 s58, 0x3f803f80\n"
    "s_mov_b32 s57, 0x43c00000\n"
    "v_cndmask_b32_e32 v107, -1, v107, vcc\n"
    "s_mov_b32 s56, 0xc3c00000\n"
    "s_mov_b32 s62, 0xc000c000\n"
    "s_mov_b32 s63, 0xc000c000\n"
    "v_add_u32_e32 v96, 32768, v27\n"
    "v_xor_b32_e32 v97, 64, v96\n"
    "s_lshl_b32 s47, s93, 11\n"
    "v_add_u32_e32 v99, s47, v250\n"
    "v_add_u32_e32 v98, 11345920, v99\n"
    "s_lshl_b32 s47, s93, 10\n"
    "s_add_u32 s47, s47, 6080512\n"
    "v_add_u32_e32 v100, s47, v250\n"
    "s_waitcnt vmcnt(10)\n"
    "s_waitcnt lgkmcnt(0)\n"
    "s_barrier\n"
    "s_add_u32 s39, s39, 4\n"
    "s_cmp_lg_u32 s51, 0\n"
    "s_cbranch_scc1 .Lat_noj2\n"
    "ds_read_b128 v[112:115], v72 offset:0\n"
    "ds_read_b128 v[208:211], v27 offset:18432\n"
    "ds_read_b128 v[176:179], v27 offset:16384\n"
    "ds_read_b128 v[116:119], v73 offset:0\n"
    "ds_read_b128 v[212:215], v75 offset:18432\n"
    "ds_read_b128 v[180:183], v75 offset:16384\n"
    "ds_read_b128 v[120:123], v72 offset:4096\n"
    "ds_read_b128 v[216:219], v27 offset:22528\n"
    "ds_read_b128 v[184:187], v27 offset:20480\n"
    "ds_read_b128 v[124:127], v73 offset:4096\n"
    "ds_read_b128 v[220:223], v75 offset:22528\n"
    "ds_read_b128 v[188:191], v75 offset:20480\n"
    "ds_read_b128 v[128:131], v72 offset:8192\n"
    "ds_read_b128 v[224:227], v27 offset:26624\n"
    "ds_read_b128 v[192:195], v27 offset:24576\n"
    "s_waitcnt lgkmcnt(12)\n"
    "v_mfma_f32_16x16x32_bf16 v[56:59], v[208:211], v[112:115], 0\n"
    "ds_read_b128 v[132:135], v73 offset:8192\n"
    "ds_read_b128 v[228:231], v75 offset:26624\n"
    "v_mfma_f32_16x16x32_bf16 v[48:51], v[112:115], v[176:179], 0\n"
    "ds_read_b128 v[196:199], v75 offset:24576\n"
    "s_waitcnt lgkmcnt(12)\n"
    "v_mfma_f32_16x16x32_bf16 v[56:59], v[212:215], v[116:119], v[56:59]\n"
    "ds_read_b128 v[136:139], v72 offset:12288\n"
    "ds_read_b128 v[232:235], v27 offset:30720\n"
    "v_mfma_f32_16x16x32_bf16 v[48:51], v[116:119], v[180:183], v[48:51]\n"
    "ds_read_b128 v[200:203], v27 offset:28672\n"
    "s_waitcnt lgkmcnt(12)\n"
    "v_mfma_f32_16x16x32_bf16 v[56:59], v[216:219], v[120:123], v[56:59]\n"
    "ds_read_b128 v[140:143], v73 offset:12288\n"
    "ds_read_b128 v[236:239], v75 offset:30720\n"
    "v_mfma_f32_16x16x32_bf16 v[48:51], v[120:123], v[184:187], v[48:51]\n"
    "ds_read_b128 v[204:207], v75 offset:28672\n"
    "s_waitcnt lgkmcnt(12)\n"
    "v_mfma_f32_16x16x32_bf16 v[56:59], v[220:223], v[124:127], v[56:59]\n"
    "v_mfma_f32_16x16x32_bf16 v[48:51], v[124:127], v[188:191], v[48:51]\n"
    "s_waitcnt lgkmcnt(9)\n"
    "v_mfma_f32_16x16x32_bf16 v[56:59], v[224:227], v[128:131], v[56:59]\n"
    "v_mfma_f32_16x16x32_bf16 v[48:51], v[128:131], v[192:195], v[48:51]\n"
    "s_waitcnt lgkmcnt(6)\n"
    "v_mfma_f32_16x16x32_bf16 v[56:59], v[228:231], v[132:135], v[56:59]\n"
    "v_mfma_f32_16x16x32_bf16 v[48:51], v[132:135], v[196:199], v[48:51]\n"
    "s_waitcnt lgkmcnt(3)\n"
    "v_mfma_f32_16x16x32_bf16 v[56:59], v[232:235], v[136:139], v[56:59]\n"
    "v_mfma_f32_16x16x32_bf16 v[48:51], v[136:139], v[200:203], v[48:51]\n"
    "s_waitcnt lgkmcnt(0)\n"
    "v_mfma_f32_16x16x32_bf16 v[56:59], v[236:239], v[140:143], v[56:59]\n"
    "v_mfma_f32_16x16x32_bf16 v[48:51], v[140:143], v[204:207], v[48:51]\n"
    "s_nop 3\n"
    "v_cvt_pk_bf16_f32 v56, v56, v57\n"
    "v_cvt_pk_bf16_f32 v57, v58, v59\n"
    "s_nop 1\n"
    "v_mfma_f32_16x16x16_bf16 v[48:51], v[56:57], v[64:65], v[48:51]\n"
    "v_mfma_f32_16x16x16_bf16 v[48:51], v[56:57], v[66:67], v[48:51]\n"
    "s_nop 7\n"
    "v_mul_f32_e32 v48, v48, v68\n"
    "v_mul_f32_e32 v49, v49, v68\n"
    "v_mul_f32_e32 v50, v50, v68\n"
    "v_mul_f32_e32 v51, v51, v68\n"
    "v_cvt_pk_bf16_f32 v48, v48, v49\n"
    "v_cvt_pk_bf16_f32 v49, v50, v51\n"
    ".Lat_noj2:\n"
    "buffer_store_dwordx2 v[48:49], v74, s[8:11], s52 offen offset:0 sc1\n"
    "v_mov_b32_e32 v102, s39\n"
    "s_waitcnt vmcnt(5)\n"
    "v_dot2_f32_bf16 v24, v0, s58, 0\n"
    "v_dot2_f32_bf16 v32, v8, s58, 0\n"
    "v_dot2_f32_bf16 v40, v16, s58, 0\n"
    "v_dot2_f32_bf16 v25, v4, s58, 0\n"
    "v_dot2_f32_bf16 v33, v12, s58, 0\n"
    "v_dot2_f32_bf16 v41, v20, s58, 0\n"
    "v_dot2_f32_bf16 v26, v0, v0, 0\n"
    "v_dot2_f32_bf16 v34, v8, v8, 0\n"
    "v_dot2_f32_bf16 v42, v16, v16, 0\n"
    "v_dot2_f32_bf16 v27, v4, v4, 0\n"
    "v_dot2_f32_bf16 v35, v12, v12, 0\n"
    "v_dot2_f32_bf16 v43, v20, v20, 0\n"
    "v_dot2_f32_bf16 v24, v1, s58, v24\n"
    "v_dot2_f32_bf16 v32, v9, s58, v32\n"
    "v_dot2_f32_bf16 v40, v17, s58, v40\n"
    "v_dot2_f32_bf16 v25, v5, s58, v25\n"
    "v_dot2_f32_bf16 v33, v13, s58, v33\n"
    "v_dot2_f32_bf16 v41, v21, s58, v41\n"
    "v_dot2_f32_bf16 v26, v1, v1, v26\n"
    "v_dot2_f32_bf16 v34, v9, v9, v34\n"
    "v_dot2_f32_bf16 v42, v17, v17, v42\n"
    "v_dot2_f32_bf16 v27, v5, v5, v27\n"
    "v_dot2_f32_bf16 v35, v13, v13, v35\n"
    "v_dot2_f32_bf16 v43, v21, v21, v43\n"
    "v_dot2_f32_bf16 v24, v2, s58, v24\n"
    "v_dot2_f32_bf16 v32, v10, s58, v32\n"
    "v_dot2_f32_bf16 v40, v18, s58, v40\n"
    "v_dot2_f32_bf16 v25, v6, s58, v25\n"
    "v_dot2_f32_bf16 v33, v14, s58, v33\n"
    "v_dot2_f32_bf16 v41, v22, s58, v41\n"
    "v_dot2_f32_bf16 v26, v2, v2, v26\n"
    "v_dot2_f32_bf16 v34, v10, v10, v34\n"
    "v_dot2_f32_bf16 v42, v18, v18, v42\n"
    "v_dot2_f32_bf16 v27, v6, v6, v27\n"
    "v_dot2_f32_bf16 v35, v14, v14, v35\n"
    "v_dot2_f32_bf16 v43, v22, v22, v43\n"
    "v_dot2_f32_bf16 v24, v3, s58, v24\n"
    "v_dot2_f32_bf16 v32, v11, s58, v32\n"
    "v_dot2_f32_bf16 v40, v19, s58, v40\n"
    "v_dot2_f32_bf16 v25, v7, s58, v25\n"
    "v_dot2_f32_bf16 v33, v15, s58, v33\n"
    "v_dot2_f32_bf16 v41, v23, s58, v41\n"
    "v_dot2_f32_bf16 v26, v3, v3, v26\n"
    "v_dot2_f32_bf16 v34, v11, v11, v34\n"
    "v_dot2_f32_bf16 v42, v19, v19, v42\n"
    "v_dot2_f32_bf16 v27, v7, v7, v27\n"
    "v_dot2_f32_bf16 v35, v15, v15, v35\n"
    "v_dot2_f32_bf16 v43, v23, v23, v43\n"
    "v_and_b32_e32 v30, 0xffff0000, v7\n"
    "v_and_b32_e32 v38, 0xffff0000, v15\n"
    "v_and_b32_e32 v46, 0xffff0000, v23\n"
    "v_add_f32_e32 v24, v24, v25\n"
    "v_add_f32_e32 v32, v32, v33\n"
    "v_add_f32_e32 v40, v40, v41\n"
    "v_add_f32_e32 v26, v26, v27\n"
    "v_add_f32_e32 v34, v34, v35\n"
    "v_add_f32_e32 v42, v42, v43\n"
    "v_add_f32_dpp v25, v25, v25 quad_perm:[1,0,3,2] row_mask:0xf bank_mask:0xf bound_ctrl:1\n"
    "v_lshlrev_b32_e32 v48, 16, v0\n"
    "v_add_f32_dpp v33, v33, v33 quad_perm:[1,0,3,2] row_mask:0xf bank_mask:0xf bound_ctrl:1\n"
    "v_and_b32_e32 v49, 0xffff0000, v0\n"
    "v_add_f32_dpp v41, v41, v41 quad_perm:[1,0,3,2] row_mask:0xf bank_mask:0xf bound_ctrl:1\n"
    "v_lshlrev_b32_e32 v50, 16, v1\n"
    "v_mov_b32_dpp v30, v30 quad_perm:[3,3,3,3] row_mask:0xf bank_mask:0xf bound_ctrl:1\n"
    "v_and_b32_e32 v51, 0xffff0000, v1\n"
    "v_mov_b32_dpp v38, v38 quad_perm:[3,3,3,3] row_mask:0xf bank_mask:0xf bound_ctrl:1\n"
    "v_lshlrev_b32_e32 v52, 16, v2\n"
    "v_mov_b32_dpp v46, v46 quad_perm:[3,3,3,3] row_mask:0xf bank_mask:0xf bound_ctrl:1\n"
    "v_and_b32_e32 v53, 0xffff0000, v2\n"
    "v_add_f32_dpp v24, v24, v24 quad_perm:[1,0,3,2] row_mask:0xf bank_mask:0xf bound_ctrl:1\n"
    "v_lshlrev_b32_e32 v54, 16, v3\n"
    "v_add_f32_dpp v32, v32, v32 quad_perm:[1,0,3,2] row_mask:0xf bank_mask:0xf bound_ctrl:1\n"
    "v_and_b32_e32 v55, 0xffff0000, v3\n"
    "v_add_f32_dpp v40, v40, v40 quad_perm:[1,0,3,2] row_mask:0xf bank_mask:0xf bound_ctrl:1\n"
    "v_lshlrev_b32_e32 v56, 16, v4\n"
    "v_add_f32_dpp v26, v26, v26 quad_perm:[1,0,3,2] row_mask:0xf bank_mask:0xf bound_ctrl:1\n"
    "v_and_b32_e32 v57, 0xffff0000, v4\n"
    "v_add_f32_dpp v34, v34, v34 quad_perm:[1,0,3,2] row_mask:0xf bank_mask:0xf bound_ctrl:1\n"
    "v_lshlrev_b32_e32 v58, 16, v5\n"
    "s_cbranch_scc1 .Lat_nojf\n"
    "s_waitcnt vmcnt(0)\n"
    "buffer_store_dword v102, off, s[8:11], s53 offset:0 sc1\n"
    ".Lat_nojf:\n"
    "v_add_f32_dpp v42, v42, v42 quad_perm:[1,0,3,2] row_mask:0xf bank_mask:0xf bound_ctrl:1\n"
    "v_and_b32_e32 v59, 0xffff0000, v5\n"
    "v_add_f32_dpp v24, v24, v24 quad_perm:[2,3,0,1] row_mask:0xf bank_mask:0xf bound_ctrl:1\n"
    "v_lshlrev_b32_e32 v60, 16, v6\n"
    "v_add_f32_dpp v32, v32, v32 quad_perm:[2,3,0,1] row_mask:0xf bank_mask:0xf bound_ctrl:1\n"
    "v_and_b32_e32 v61, 0xffff0000, v6\n"
    "v_add_f32_dpp v40, v40, v40 quad_perm:[2,3,0,1] row_mask:0xf bank_mask:0xf bound_ctrl:1\n"
    "v_lshlrev_b32_e32 v62, 16, v7\n"
    "v_add_f32_dpp v26, v26, v26 quad_perm:[2,3,0,1] row_mask:0xf bank_mask:0xf bound_ctrl:1\n"
    "v_and_b32_e32 v63, 0xffff0000, v7\n"
    "v_add_f32_dpp v34, v34, v34 quad_perm:[2,3,0,1] row_mask:0xf bank_mask:0xf bound_ctrl:1\n"
    "v_lshlrev_b32_e32 v64, 16, v8\n"
    "v_add_f32_dpp v42, v42, v42 quad_perm:[2,3,0,1] row_mask:0xf bank_mask:0xf bound_ctrl:1\n"
    "v_and_b32_e32 v65, 0xffff0000, v8\n"
    "v_add_f32_dpp v24, v24, v24 row_half_mirror row_mask:0xf bank_mask:0xf bound_ctrl:1\n"
    "v_lshlrev_b32_e32 v66, 16, v9\n"
    "v_add_f32_dpp v32, v32, v32 row_half_mirror row_mask:0xf bank_mask:0xf bound_ctrl:1\n"
    "v_and_b32_e32 v67, 0xffff0000, v9\n"
    "v_add_f32_dpp v40, v40, v40 row_half_mirror row_mask:0xf bank_mask:0xf bound_ctrl:1\n"
    "v_lshlrev_b32_e32 v68, 16, v10\n"
    "v_add_f32_dpp v26, v26, v26 row_half_mirror row_mask:0xf bank_mask:0xf bound_ctrl:1\n"
    "v_and_b32_e32 v69, 0xffff0000, v10\n"
    "v_add_f32_dpp v34, v34, v34 row_half_mirror row_mask:0xf bank_mask:0xf bound_ctrl:1\n"
    "v_lshlrev_b32_e32 v70, 16, v11\n"
    "v_add_f32_dpp v42, v42, v42 row_half_mirror row_mask:0xf bank_mask:0xf bound_ctrl:1\n"
    "v_and_b32_e32 v71, 0xffff0000, v11\n"
    "v_add_f32_dpp v24, v24, v24 row_mirror row_mask:0xf bank_mask:0xf bound_ctrl:1\n"
    "v_lshlrev_b32_e32 v72, 16, v12\n"
    "v_add_f32_dpp v32, v32, v32 row_mirror row_mask:0xf bank_mask:0xf bound_ctrl:1\n"
    "v_and_b32_e32 v73, 0xffff0000, v12\n"
    "v_add_f32_dpp v40, v40, v40 row_mirror row_mask:0xf bank_mask:0xf bound_ctrl:1\n"
    "v_lshlrev_b32_e32 v74, 16, v13\n"
    "v_add_f32_dpp v26, v26, v26 row_mirror row_mask:0xf bank_mask:0xf bound_ctrl:1\n"
    "v_and_b32_e32 v75, 0xffff0000, v13\n"
    "v_add_f32_dpp v34, v34, v34 row_mirror row_mask:0xf bank_mask:0xf bound_ctrl:1\n"
    "v_lshlrev_b32_e32 v76, 16, v14\n"
    "v_add_f32_dpp v42, v42, v42 row_mirror row_mask:0xf bank_mask:0xf bound_ctrl:1\n"
    "v_and_b32_e32 v77, 0xffff0000, v14\n"
    "v_mul_f32_e32 v24, 0x3b800000, v24\n"
    "v_lshlrev_b32_e32 v78, 16, v15\n"
    "v_mul_f32_e32 v32, 0x3b800000, v32\n"
    "v_and_b32_e32 v79, 0xffff0000, v15\n"
    "v_mul_f32_e32 v40, 0x3b800000, v40\n"
    "v_lshlrev_b32_e32 v80, 16, v16\n"
    "v_mul_f32_e32 v25, 0.25, v25\n"
    "v_and_b32_e32 v81, 0xffff0000, v16\n"
    "v_mul_f32_e32 v33, 0.25, v33\n"
    "v_lshlrev_b32_e32 v82, 16, v17\n"
    "v_mul_f32_e32 v41, 0.25, v41\n"
    "v_and_b32_e32 v83, 0xffff0000, v17\n"
    "v_mul_f32_e64 v27, v24, -v24\n"
    "v_lshlrev_b32_e32 v84, 16, v18\n"
    "v_mul_f32_e64 v35, v32, -v32\n"
    "v_and_b32_e32 v85, 0xffff0000, v18\n"
    "v_mul_f32_e64 v43, v40, -v40\n"
    "v_lshlrev_b32_e32 v86, 16, v19\n"
    "v_sub_f32_e32 v25, v25, v30\n"
    "v_and_b32_e32 v87, 0xffff0000, v19\n"
    "v_sub_f32_e32 v33, v33, v38\n"
    "v_lshlrev_b32_e32 v88, 16, v20\n"
    "v_sub_f32_e32 v41, v41, v46\n"
    "v_and_b32_e32 v89, 0xffff0000, v20\n"
    "v_fmac_f32_e32 v27, 0x3b800000, v26\n"
    "v_lshlrev_b32_e32 v90, 16, v21\n"
    "v_fmac_f32_e32 v35, 0x3b800000, v34\n"
    "v_and_b32_e32 v91, 0xffff0000, v21\n"
    "v_fmac_f32_e32 v43, 0x3b800000, v42\n"
    "v_lshlrev_b32_e32 v92, 16, v22\n"
    "v_mul_f32_e32 v25, 0x3eaaaaab, v25\n"
    "v_and_b32_e32 v93, 0xffff0000, v22\n"
    "v_mul_f32_e32 v33, 0x3eaaaaab, v33\n"
    "v_lshlrev_b32_e32 v94, 16, v23\n"
    "v_mul_f32_e32 v41, 0x3eaaaaab, v41\n"
    "v_and_b32_e32 v95, 0xffff0000, v23\n"
    "v_max_f32_e32 v27, 0, v27\n"
    "v_max_f32_e32 v35, 0, v35\n"
    "v_max_f32_e32 v43, 0, v43\n"
    "s_cbranch_scc1 .Lat_bin\n"
    ".Lat_bin_ret:\n"
    "v_cndmask_b32_e64 v25, v24, v25, s[62:63]\n"
    "v_cndmask_b32_e64 v33, v32, v33, s[62:63]\n"
    "v_cndmask_b32_e64 v41, v40, v41, s[62:63]\n"
    "v_add_f32_e32 v27, 0x3727c5ac, v27\n"
    "v_add_f32_e32 v35, 0x3727c5ac, v35\n"
    "v_add_f32_e32 v43, 0x3727c5ac, v43\n"
    "v_rsq_f32_e32 v28, v27\n"
    "v_rsq_f32_e32 v36, v35\n"
    "v_rsq_f32_e32 v44, v43\n"
    "v_fma_f32 v29, -v24, v28, s57\n"
    "v_fma_f32 v37, -v32, v36, s57\n"
    "v_fma_f32 v45, -v40, v44, s57\n"
    "v_fma_f32 v31, -v25, v28, s57\n"
    "v_fma_f32 v39, -v33, v36, s57\n"
    "v_fma_f32 v47, -v41, v44, s57\n"
    "v_sub_f32_e32 v24, v29, v31\n"
    "v_sub_f32_e32 v32, v37, v39\n"
    "v_sub_f32_e32 v40, v45, v47\n"
    "v_pk_fma_f32 v[48:49], v[48:49], v[28:29], v[28:29] op_sel:[0,0,1] op_sel_hi:[1,0,1]\n"
    "v_pk_fma_f32 v[50:51], v[50:51], v[28:29], v[28:29] op_sel:[0,0,1] op_sel_hi:[1,0,1]\n"
    "v_pk_fma_f32 v[52:53], v[52:53], v[28:29], v[28:29] op_sel:[0,0,1] op_sel_hi:[1,0,1]\n"
    "v_pk_fma_f32 v[54:55], v[54:55], v[28:29], v[28:29] op_sel:[0,0,1] op_sel_hi:[1,0,1]\n"
    "v_pk_fma_f32 v[56:57], v[56:57], v[28:29], v[30:31] op_sel:[0,0,1] op_sel_hi:[1,0,1]\n"
    "v_pk_fma_f32 v[58:59], v[58:59], v[28:29], v[30:31] op_sel:[0,0,1] op_sel_hi:[1,0,1]\n"
    "v_pk_fma_f32 v[60:61], v[60:61], v[28:29], v[30:31] op_sel:[0,0,1] op_sel_hi:[1,0,1]\n"
    "v_pk_fma_f32 v[62:63], v[62:63], v[28:29], v[30:31] op_sel:[0,0,1] op_sel_hi:[1,0,1]\n"
    "v_pk_add_f32 v[48:49], v[48:49], s[56:57] op_sel_hi:[1,0]\n"
    "v_pk_add_f32 v[50:51], v[50:51], s[56:57] op_sel_hi:[1,0]\n"
    "v_pk_add_f32 v[52:53], v[52:53], s[56:57] op_sel_hi:[1,0]\n"
    "v_pk_add_f32 v[54:55], v[54:55], s[56:57] op_sel_hi:[1,0]\n"
    "v_pk_add_f32 v[56:57], v[56:57], s[56:57] op_sel_hi:[1,0]\n"
    "v_pk_add_f32 v[58:59], v[58:59], s[56:57] op_sel_hi:[1,0]\n"
    "v_pk_add_f32 v[60:61], v[60:61], s[56:57] op_sel_hi:[1,0]\n"
    "v_pk_add_f32 v[62:63], v[62:63], s[56:57] op_sel_hi:[1,0]\n"
    "v_cvt_pk_bf16_f32 v0, v48, v49\n"
    "v_cvt_pk_bf16_f32 v1, v50, v51\n"
    "v_cvt_pk_bf16_f32 v2, v52, v53\n"
    "v_cvt_pk_bf16_f32 v3, v54, v55\n"
    "v_cvt_pk_bf16_f32 v4, v56, v57\n"
    "v_cvt_pk_bf16_f32 v5, v58, v59\n"
    "v_cvt_pk_bf16_f32 v6, v60, v61\n"
    "v_cvt_pk_bf16_f32 v7, v62, v63\n"
    "buffer_store_dwordx4 v[0:3], v104, s[8:11], s35 offen offset:0 sc1\n"
    "v_bfi_b32 v7, v107, v7, 1.0\n"
    "ds_write_b128 v105, v[0:3] offset:0\n"
    "buffer_store_dwordx4 v[4:7], v104, s[8:11], s35 offen offset:256 sc1\n"
    "v_pk_fma_f32 v[64:65], v[64:65], v[36:37], v[36:37] op_sel:[0,0,1] op_sel_hi:[1,0,1]\n"
    "v_pk_fma_f32 v[66:67], v[66:67], v[36:37], v[36:37] op_sel:[0,0,1] op_sel_hi:[1,0,1]\n"
    "v_pk_fma_f32 v[68:69], v[68:69], v[36:37], v[36:37] op_sel:[0,0,1] op_sel_hi:[1,0,1]\n"
    "v_pk_fma_f32 v[70:71], v[70:71], v[36:37], v[36:37] op_sel:[0,0,1] op_sel_hi:[1,0,1]\n"
    "v_pk_fma_f32 v[72:73], v[72:73], v[36:37], v[38:39] op_sel:[0,0,1] op_sel_hi:[1,0,1]\n"
    "v_pk_fma_f32 v[74:75], v[74:75], v[36:37], v[38:39] op_sel:[0,0,1] op_sel_hi:[1,0,1]\n"
    "v_pk_fma_f32 v[76:77], v[76:77], v[36:37], v[38:39] op_sel:[0,0,1] op_sel_hi:[1,0,1]\n"
    "v_pk_fma_f32 v[78:79], v[78:79], v[36:37], v[38:39] op_sel:[0,0,1] op_sel_hi:[1,0,1]\n"
    "v_pk_add_f32 v[64:65], v[64:65], s[56:57] op_sel_hi:[1,0]\n"
    "v_pk_add_f32 v[66:67], v[66:67], s[56:57] op_sel_hi:[1,0]\n"
    "v_pk_add_f32 v[68:69], v[68:69], s[56:57] op_sel_hi:[1,0]\n"
    "v_pk_add_f32 v[70:71], v[70:71], s[56:57] op_sel_hi:[1,0]\n"
    "v_pk_add_f32 v[72:73], v[72:73], s[56:57] op_sel_hi:[1,0]\n"
    "v_pk_add_f32 v[74:75], v[74:75], s[56:57] op_sel_hi:[1,0]\n"
    "v_pk_add_f32 v[76:77], v[76:77], s[56:57] op_sel_hi:[1,0]\n"
    "v_pk_add_f32 v[78:79], v[78:79], s[56:57] op_sel_hi:[1,0]\n"
    "v_cvt_pk_bf16_f32 v8, v64, v65\n"
    "v_cvt_pk_bf16_f32 v9, v66, v67\n"
    "v_cvt_pk_bf16_f32 v10, v68, v69\n"
    "v_cvt_pk_bf16_f32 v11, v70, v71\n"
    "v_cvt_pk_bf16_f32 v12, v72, v73\n"
    "v_cvt_pk_bf16_f32 v13, v74, v75\n"
    "v_cvt_pk_bf16_f32 v14, v76, v77\n"
    "v_cvt_pk_bf16_f32 v15, v78, v79\n"
    "buffer_store_dwordx4 v[8:11], v104, s[8:11], s54 offen offset:0 sc1\n"
    "v_bfi_b32 v15, v107, v15, 1.0\n"
    "ds_write_b128 v105, v[8:11] offset:8192\n"
    "buffer_store_dwordx4 v[12:15], v104, s[8:11], s54 offen offset:256 sc1\n"
    "v_pk_fma_f32 v[80:81], v[80:81], v[44:45], v[44:45] op_sel:[0,0,1] op_sel_hi:[1,0,1]\n"
    "v_pk_fma_f32 v[82:83], v[82:83], v[44:45], v[44:45] op_sel:[0,0,1] op_sel_hi:[1,0,1]\n"
    "v_pk_fma_f32 v[84:85], v[84:85], v[44:45], v[44:45] op_sel:[0,0,1] op_sel_hi:[1,0,1]\n"
    "v_pk_fma_f32 v[86:87], v[86:87], v[44:45], v[44:45] op_sel:[0,0,1] op_sel_hi:[1,0,1]\n"
    "v_pk_fma_f32 v[88:89], v[88:89], v[44:45], v[46:47] op_sel:[0,0,1] op_sel_hi:[1,0,1]\n"
    "v_pk_fma_f32 v[90:91], v[90:91], v[44:45], v[46:47] op_sel:[0,0,1] op_sel_hi:[1,0,1]\n"
    "v_pk_fma_f32 v[92:93], v[92:93], v[44:45], v[46:47] op_sel:[0,0,1] op_sel_hi:[1,0,1]\n"
    "v_pk_fma_f32 v[94:95], v[94:95], v[44:45], v[46:47] op_sel:[0,0,1] op_sel_hi:[1,0,1]\n"
    "v_pk_add_f32 v[80:81], v[80:81], s[56:57] op_sel_hi:[1,0]\n"
    "v_pk_add_f32 v[82:83], v[82:83], s[56:57] op_sel_hi:[1,0]\n"
    "v_pk_add_f32 v[84:85], v[84:85], s[56:57] op_sel_hi:[1,0]\n"
    "v_pk_add_f32 v[86:87], v[86:87], s[56:57] op_sel_hi:[1,0]\n"
    "v_pk_add_f32 v[88:89], v[88:89], s[56:57] op_sel_hi:[1,0]\n"
    "v_pk_add_f32 v[90:91], v[90:91], s[56:57] op_sel_hi:[1,0]\n"
    "v_pk_add_f32 v[92:93], v[92:93], s[56:57] op_sel_hi:[1,0]\n"
    "v_pk_add_f32 v[94:95], v[94:95], s[56:57] op_sel_hi:[1,0]\n"
    "v_cvt_pk_bf16_f32 v16, v80, v81\n"
    "v_cvt_pk_bf16_f32 v17, v82, v83\n"
    "v_cvt_pk_bf16_f32 v18, v84, v85\n"
    "v_cvt_pk_bf16_f32 v19, v86, v87\n"
    "v_cvt_pk_bf16_f32 v20, v88, v89\n"
    "v_cvt_pk_bf16_f32 v21, v90, v91\n"
    "v_cvt_pk_bf16_f32 v22, v92, v93\n"
    "v_cvt_pk_bf16_f32 v23, v94, v95\n"
    "buffer_store_dwordx4 v[16:19], v104, s[8:11], s60 offen offset:0 sc1\n"
    "v_bfi_b32 v23, v107, v23, 1.0\n"
    "ds_write_b128 v105, v[16:19] offset:16384\n"
    "buffer_store_dwordx4 v[20:23], v104, s[8:11], s60 offen offset:256 sc1\n"
    "v_pk_add_f32 v[56:57], v[56:57], v[24:25] op_sel_hi:[1,0]\n"
    "v_pk_add_f32 v[58:59], v[58:59], v[24:25] op_sel_hi:[1,0]\n"
    "v_pk_add_f32 v[60:61], v[60:61], v[24:25] op_sel_hi:[1,0]\n"
    "v_pk_add_f32 v[62:63], v[62:63], v[24:25] op_sel_hi:[1,0]\n"
    "v_cvt_pk_bf16_f32 v56, v56, v57\n"
    "v_cvt_pk_bf16_f32 v57, v58, v59\n"
    "v_cvt_pk_bf16_f32 v58, v60, v61\n"
    "v_cvt_pk_bf16_f32 v59, v62, v63\n"
    "ds_write_b128 v106, v[56:59] offset:0\n"
    "v_pk_add_f32 v[72:73], v[72:73], v[32:33] op_sel_hi:[1,0]\n"
    "v_pk_add_f32 v[74:75], v[74:75], v[32:33] op_sel_hi:[1,0]\n"
    "v_pk_add_f32 v[76:77], v[76:77], v[32:33] op_sel_hi:[1,0]\n"
    "v_pk_add_f32 v[78:79], v[78:79], v[32:33] op_sel_hi:[1,0]\n"
    "v_cvt_pk_bf16_f32 v72, v72, v73\n"
    "v_cvt_pk_bf16_f32 v73, v74, v75\n"
    "v_cvt_pk_bf16_f32 v74, v76, v77\n"
    "v_cvt_pk_bf16_f32 v75, v78, v79\n"
    "ds_write_b128 v106, v[72:75] offset:8192\n"
    "v_pk_add_f32 v[88:89], v[88:89], v[40:41] op_sel_hi:[1,0]\n"
    "v_pk_add_f32 v[90:91], v[90:91], v[40:41] op_sel_hi:[1,0]\n"
    "v_pk_add_f32 v[92:93], v[92:93], v[40:41] op_sel_hi:[1,0]\n"
    "v_pk_add_f32 v[94:95], v[94:95], v[40:41] op_sel_hi:[1,0]\n"
    "v_cvt_pk_bf16_f32 v88, v88, v89\n"
    "v_cvt_pk_bf16_f32 v89, v90, v91\n"
    "v_cvt_pk_bf16_f32 v90, v92, v93\n"
    "v_cvt_pk_bf16_f32 v91, v94, v95\n"
    "ds_write_b128 v106, v[88:91] offset:16384\n"
    "s_cbranch_scc1 .Lat_bin2\n"
    ".Lat_bin2_ret:\n"
    "s_waitcnt lgkmcnt(0)\n"
    "s_barrier\n"
    "ds_read_b128 a[0:3], v96 offset:0\n"
    "ds_read_b128 a[32:35], v96 offset:8192\n"
    "ds_read_b128 a[64:67], v96 offset:16384\n"
    "ds_read_b128 a[4:7], v97 offset:0\n"
    "ds_read_b128 a[36:39], v97 offset:8192\n"
    "ds_read_b128 a[68:71], v97 offset:16384\n"
    "ds_read_b128 a[8:11], v96 offset:2048\n"
    "ds_read_b128 a[40:43], v96 offset:10240\n"
    "ds_read_b128 a[72:75], v96 offset:18432\n"
    "ds_read_b128 a[12:15], v97 offset:2048\n"
    "ds_read_b128 a[44:47], v97 offset:10240\n"
    "ds_read_b128 a[76:79], v97 offset:18432\n"
    "buffer_load_dword v101, v100, s[8:11], 0 offen offset:0 sc1\n"
    "ds_read_b128 a[16:19], v96 offset:4096\n"
    "ds_read_b128 a[48:51], v96 offset:12288\n"
    "ds_read_b128 a[80:83], v96 offset:20480\n"
    "ds_read_b128 a[20:23], v97 offset:4096\n"
    "ds_read_b128 a[52:55], v97 offset:12288\n"
    "ds_read_b128 a[84:87], v97 offset:20480\n"
    "ds_read_b128 a[24:27], v96 offset:6144\n"
    "ds_read_b128 a[56:59], v96 offset:14336\n"
    "ds_read_b128 a[88:91], v96 offset:22528\n"
    "ds_read_b128 a[28:31], v97 offset:6144\n"
    "ds_read_b128 a[60:63], v97 offset:14336\n"
    "ds_read_b128 a[92:95], v97 offset:22528\n"
    "ds_read_b128 v[40:43], v108 offset:0\n"
    "s_mul_i32 s52, s93, 33792\n"
    "s_add_u32 s52, s52, 5242880\n"
    "s_lshl_b32 s61, s32, 12\n"
    "s_lshl4_add_u32 s46, s34, 6076416\n"
    "s_cmp_eq_u32 s93, 0\n"
    "s_cselect_b32 s46, s46, 2.0\n"
    "s_lshl_b32 s48, s34, 7\n"
    "s_add_u32 s48, s48, 68206592\n"
    "s_lshl_b32 s47, s93, 12\n"
    "s_waitcnt vmcnt(1)\n"
    "s_waitcnt lgkmcnt(0)\n"
    "s_barrier\n"
    "buffer_store_dword v102, off, s[8:11], s46 offset:0 sc1\n"
    "buffer_store_dwordx4 v[40:43], v109, s[8:11], s48 offen offset:0 sc1\n"
    "s_lshl4_add_u32 s46, s34, s47\n"
    "s_add_u32 s46, s46, 6084608\n"
    "s_waitcnt vmcnt(2)\n"
    "v_cmp_ne_u32_e32 vcc, s39, v101\n"
    "s_nop 4\n"
    "s_cbranch_vccnz .Lat_apoll\n"
    ".Lat_apoll_ret:\n"
    "s_add_u32 s47, s52, s61\n"
    "buffer_load_dwordx4 v[112:115], v250, s[8:11], s47 offen offset:0 sc1\n"
    "buffer_load_dwordx4 v[116:119], v250, s[8:11], s47 offen offset:1024 sc1\n"
    "buffer_load_dwordx4 v[128:131], v250, s[8:11], s47 offen offset:2048 sc1\n"
    "s_add_u32 s61, s61, 0x1000\n"
    "buffer_load_dwordx4 v[132:135], v250, s[8:11], s47 offen offset:3072 sc1\n"
    "s_bitset0_b32 s61, 14\n"
    "s_add_u32 s47, s52, s61\n"
    "buffer_load_dwordx4 v[144:147], v250, s[8:11], s47 offen offset:0 sc1\n"
    "buffer_load_dwordx4 v[148:151], v250, s[8:11], s47 offen offset:1024 sc1\n"
    "buffer_load_dwordx4 v[160:163], v250, s[8:11], s47 offen offset:2048 sc1\n"
    "s_add_u32 s61, s61, 0x1000\n"
    "buffer_load_dwordx4 v[164:167], v250, s[8:11], s47 offen offset:3072 sc1\n"
    "s_bitset0_b32 s61, 14\n"
    "s_add_u32 s47, s52, s61\n"
    "buffer_load_dwordx4 v[176:179], v250, s[8:11], s47 offen offset:0 sc1\n"
    "buffer_load_dwordx4 v[180:183], v250, s[8:11], s47 offen offset:1024 sc1\n"
    "buffer_load_dwordx4 v[192:195], v250, s[8:11], s47 offen offset:2048 sc1\n"
    "s_add_u32 s61, s61, 0x1000\n"
    "v_and_b32_e32 v252, 0x30, v250\n"
    "v_add_u32_e32 v252, 0x400, v252\n"
    "s_mul_i32 s59, s32, 999\n"
    "s_andn2_b32 s59, s59, 63\n"
    "s_sub_u32 s48, 3968, s59\n"
    "v_cmp_le_u32_e32 vcc, 0x80, v250\n"
    "v_mov_b32_e32 v80, s48\n"
    "s_add_u32 s59, s59, 6076416\n"
    "v_cndmask_b32_e32 v83, v250, v80, vcc\n"
    "v_bfe_u32 v253, v250, 4, 2\n"
    "v_max_u32_e32 v253, 1, v253\n"
    "v_add_u32_e32 v253, s32, v253\n"
    "v_and_b32_e32 v253, 3, v253\n"
    "v_lshlrev_b32_e32 v253, 9, v253\n"
    "v_mov_b32_e32 v84, s39\n"
    "v_lshrrev_b32_e32 v86, 2, v250\n"
    "v_min_u32_e32 v86, 124, v86\n"
    "s_lshl2_add_u32 s58, s34, s93\n"
    "s_lshl_b32 s58, s58, 7\n"
    "s_add_u32 s58, s58, 5910528\n"
    "s_lshl_b32 s45, s50, 13\n"
    "v_add_u32_e32 v85, s45, v250\n"
    "s_lshl3_add_u32 s45, s45, 49152\n"
    "v_add_u32_e32 v255, s45, v250\n"
    "v_lshrrev_b32_e32 v105, 7, v250\n"
    "v_and_b32_e32 v104, 0x70, v251\n"
    "v_lshl_add_u32 v104, v105, 9, v104\n"
    "v_add_u32_e32 v105, 0xc00, v104\n"
    "v_add_u32_e32 v106, 0x1800, v104\n"
    "v_add_u32_e32 v107, 0x2400, v104\n"
    "s_lshl_b32 s57, s93, 7\n"
    "s_lshl4_add_u32 s56, s57, 24576\n"
    "s_add_u32 s57, s57, 5111808\n"
    "s_mul_i32 s45, s51, 32768\n"
    "s_add_u32 s45, s45, 8192\n"
    "v_subrev_u32_e32 v46, s45, v240\n"
    "v_subrev_u32_e32 v47, s45, v241\n"
    "s_mul_i32 s45, s51, 61440\n"
    "s_add_u32 s45, s45, 8192\n"
    "v_subrev_u32_e32 v4, s45, v244\n"
    "v_subrev_u32_e32 v5, s45, v245\n"
    "v_subrev_u32_e32 v6, s45, v246\n"
    "v_subrev_u32_e32 v7, s45, v247\n"
    "s_waitcnt vmcnt(9)\n"
    "v_mfma_f32_16x16x32_bf16 v[24:27], v[112:115], a[0:3], 0\n"
    "buffer_load_dwordx4 v[196:199], v250, s[8:11], s47 offen offset:3072 sc1\n"
    "s_bitset0_b32 s61, 14\n"
    "v_mfma_f32_16x16x32_bf16 v[28:31], v[116:119], a[0:3], 0\n"
    "v_accvgpr_write_b32 a244, 0\n"
    "v_mfma_f32_16x16x32_bf16 v[48:51], v[112:115], a[32:35], 0\n"
    "v_accvgpr_write_b32 a245, 0\n"
    "v_mfma_f32_16x16x32_bf16 v[52:55], v[116:119], a[32:35], 0\n"
    "s_add_u32 s47, s52, s61\n"
    "buffer_load_dwordx4 v[208:211], v250, s[8:11], s47 offen offset:0 sc1\n"
    "v_mfma_f32_16x16x32_bf16 v[64:67], v[112:115], a[64:67], 0\n"
    "v_bfe_u32 v0, v250, 4, 2\n"
    "v_mfma_f32_16x16x32_bf16 v[68:71], v[116:119], a[64:67], 0\n"
    "s_mov_b32 s18, 5120000\n"
    "v_accvgpr_write_b32 a246, 0\n"
    "s_waitcnt vmcnt(9)\n"
    "v_mfma_f32_16x16x32_bf16 v[24:27], v[128:131], a[4:7], v[24:27]\n"
    "buffer_load_dwordx4 v[212:215], v250, s[8:11], s47 offen offset:1024 sc1\n"
    "s_add_u32 s52, s52, 0x4000\n"
    "v_mfma_f32_16x16x32_bf16 v[28:31], v[132:135], a[4:7], v[28:31]\n"
    "buffer_store_dword v102, off, s[8:11], s46 offset:0 sc1\n"
    "v_accvgpr_write_b32 a247, 0\n"
    "v_mfma_f32_16x16x32_bf16 v[48:51], v[128:131], a[36:39], v[48:51]\n"
    "v_bfe_u32 v1, v250, 6, 2\n"
    "v_accvgpr_write_b32 a248, 0\n"
    "v_mfma_f32_16x16x32_bf16 v[52:55], v[132:135], a[36:39], v[52:55]\n"
    "buffer_load_dwordx4 v[224:227], v250, s[8:11], s47 offen offset:2048 sc1\n"
    "s_add_u32 s61, s61, 0x1000\n"
    "v_mfma_f32_16x16x32_bf16 v[64:67], v[128:131], a[68:71], v[64:67]\n"
    "v_mov_b32_e32 v2, 0x3f80\n"
    "v_accvgpr_write_b32 a249, 0\n"
    "v_mfma_f32_16x16x32_bf16 v[68:71], v[132:135], a[68:71], v[68:71]\n"
    "s_mov_b32 s19, 0x20000\n"
    "s_waitcnt vmcnt(10)\n"
    "v_mfma_f32_16x16x32_bf16 v[24:27], v[144:147], a[8:11], v[24:27]\n"
    "buffer_load_dwordx4 v[228:231], v250, s[8:11], s47 offen offset:3072 sc1\n"
    "s_bitset0_b32 s61, 14\n"
    "v_mfma_f32_16x16x32_bf16 v[28:31], v[148:151], a[8:11], v[28:31]\n"
    "v_accvgpr_write_b32 a250, 0\n"
    "v_mfma_f32_16x16x32_bf16 v[48:51], v[144:147], a[40:43], v[48:51]\n"
    "s_mov_b64 s[20:21], s[8:9]\n"
    "v_accvgpr_write_b32 a251, 0\n"
    "v_mfma_f32_16x16x32_bf16 v[52:55], v[148:151], a[40:43], v[52:55]\n"
    "s_add_u32 s47, s52, s61\n"
    "buffer_load_dwordx4 v[120:123], v250, s[8:11], s47 offen offset:0 sc1\n"
    "v_mfma_f32_16x16x32_bf16 v[64:67], v[144:147], a[72:75], v[64:67]\n"
    "v_cmp_eq_u32_e32 vcc, 3, v0\n"
    "s_nop 1\n"
    "v_cndmask_b32_e64 v2, v2, 1.0, vcc\n"
    "v_mfma_f32_16x16x32_bf16 v[68:71], v[148:151], a[72:75], v[68:71]\n"
    "v_accvgpr_write_b32 a252, 0\n"
    "s_waitcnt vmcnt(10)\n"
    "v_mfma_f32_16x16x32_bf16 v[24:27], v[160:163], a[12:15], v[24:27]\n"
    "buffer_load_dwordx4 v[124:127], v250, s[8:11], s47 offen offset:1024 sc1\n"
    "v_accvgpr_write_b32 a253, 0\n"
    "v_mfma_f32_16x16x32_bf16 v[28:31], v[164:167], a[12:15], v[28:31]\n"
    "s_mov_b32 s22, 5242880\n"
    "v_accvgpr_write_b32 a254, 0\n"
    "v_mfma_f32_16x16x32_bf16 v[48:51], v[160:163], a[44:47], v[48:51]\n"
    "v_cmp_eq_u32_e32 vcc, 0, v1\n"
    "s_nop 1\n"
    "v_cndmask_b32_e32 v3, 0, v2, vcc\n"
    "v_mfma_f32_16x16x32_bf16 v[52:55], v[164:167], a[44:47], v[52:55]\n"
    "buffer_load_dwordx4 v[136:139], v250, s[8:11], s47 offen offset:2048 sc1\n"
    "s_add_u32 s61, s61, 0x1000\n"
    "v_mfma_f32_16x16x32_bf16 v[64:67], v[160:163], a[76:79], v[64:67]\n"
    "v_accvgpr_write_b32 a232, v3\n"
    "v_accvgpr_write_b32 a255, 0\n"
    "v_mfma_f32_16x16x32_bf16 v[68:71], v[164:167], a[76:79], v[68:71]\n"
    "s_mov_b64 s[24:25], s[28:29]\n"
    "v_accvgpr_write_b32 a192, 0\n"
    "s_waitcnt vmcnt(10)\n"
    "v_mfma_f32_16x16x32_bf16 v[24:27], v[176:179], a[16:19], v[24:27]\n"
    "buffer_load_dwordx4 v[140:143], v250, s[8:11], s47 offen offset:3072 sc1\n"
    "s_bitset0_b32 s61, 14\n"
    "v_mfma_f32_16x16x32_bf16 v[28:31], v[180:183], a[16:19], v[28:31]\n"
    "v_accvgpr_write_b32 a193, 0\n"
    "v_mfma_f32_16x16x32_bf16 v[48:51], v[176:179], a[48:51], v[48:51]\n"
    "v_cmp_eq_u32_e32 vcc, 1, v1\n"
    "s_nop 1\n"
    "v_cndmask_b32_e32 v3, 0, v2, vcc\n"
    "v_mfma_f32_16x16x32_bf16 v[52:55], v[180:183], a[48:51], v[52:55]\n"
    "s_add_u32 s47, s52, s61\n"
    "buffer_load_dwordx4 v[152:155], v250, s[8:11], s47 offen offset:0 sc1\n"
    "v_mfma_f32_16x16x32_bf16 v[64:67], v[176:179], a[80:83], v[64:67]\n"
    "buffer_load_dword v80, v83, s[8:11], s59 offen offset:0 sc1\n"
    "v_accvgpr_write_b32 a194, 0\n"
    "v_mfma_f32_16x16x32_bf16 v[68:71], v[180:183], a[80:83], v[68:71]\n"
    "s_movk_i32 s26, 20000\n"
    "v_accvgpr_write_b32 a195, 0\n"
    "s_waitcnt vmcnt(11)\n"
    "v_mfma_f32_16x16x32_bf16 v[24:27], v[192:195], a[20:23], v[24:27]\n"
    "buffer_load_dwordx4 v[156:159], v250, s[8:11], s47 offen offset:1024 sc1\n"
    "v_accvgpr_write_b32 a196, 0\n"
    "v_mfma_f32_16x16x32_bf16 v[28:31], v[196:199], a[20:23], v[28:31]\n"
    "v_accvgpr_write_b32 a233, v3\n"
    "v_mfma_f32_16x16x32_bf16 v[48:51], v[192:195], a[52:55], v[48:51]\n"
    "s_bfe_u32 s88, s92, 0x20001\n"
    "v_accvgpr_write_b32 a197, 0\n"
    "v_mfma_f32_16x16x32_bf16 v[52:55], v[196:199], a[52:55], v[52:55]\n"
    "buffer_load_dwordx4 v[168:171], v250, s[8:11], s47 offen offset:2048 sc1\n"
    "s_add_u32 s61, s61, 0x1000\n"
    "v_mfma_f32_16x16x32_bf16 v[64:67], v[192:195], a[84:87], v[64:67]\n"
    "v_cmp_eq_u32_e32 vcc, 2, v1\n"
    "s_nop 1\n"
    "v_cndmask_b32_e32 v3, 0, v2, vcc\n"
    "v_mfma_f32_16x16x32_bf16 v[68:71], v[196:199], a[84:87], v[68:71]\n"
    "v_accvgpr_write_b32 a198, 0\n"
    "s_waitcnt vmcnt(11)\n"
    "v_mfma_f32_16x16x32_bf16 v[24:27], v[208:211], a[24:27], v[24:27]\n"
    "buffer_load_dwordx4 v[172:175], v250, s[8:11], s47 offen offset:3072 sc1\n"
    "s_bitset0_b32 s61, 14\n"
    "v_mfma_f32_16x16x32_bf16 v[28:31], v[212:215], a[24:27], v[28:31]\n"
    "s_lshl_b32 s82, s88, 7\n"
    "v_accvgpr_write_b32 a199, 0\n"
    "v_mfma_f32_16x16x32_bf16 v[48:51], v[208:211], a[56:59], v[48:51]\n"
    "v_accvgpr_write_b32 a234, v3\n"
    "v_accvgpr_write_b32 a200, 0\n"
    "v_mfma_f32_16x16x32_bf16 v[52:55], v[212:215], a[56:59], v[52:55]\n"
    "s_add_u32 s47, s52, s61\n"
    "buffer_load_dwordx4 v[184:187], v250, s[8:11], s47 offen offset:0 sc1\n"
    "v_mfma_f32_16x16x32_bf16 v[64:67], v[208:211], a[88:91], v[64:67]\n"
    "v_cmp_eq_u32_e32 vcc, 3, v1\n"
    "s_nop 1\n"
    "v_cndmask_b32_e32 v3, 0, v2, vcc\n"
    "v_mfma_f32_16x16x32_bf16 v[68:71], v[212:215], a[88:91], v[68:71]\n"
    "s_add_u32 s82, s82, 6101248\n"
    "v_accvgpr_write_b32 a201, 0\n"
    "s_waitcnt vmcnt(10)\n"
    "v_mfma_f32_16x16x32_bf16 v[24:27], v[224:227], a[28:31], v[24:27]\n"
    "buffer_load_dwordx4 v[188:191], v250, s[8:11], s47 offen offset:1024 sc1\n"
    "v_accvgpr_write_b32 a202, 0\n"
    "v_mfma_f32_16x16x32_bf16 v[28:31], v[228:231], a[28:31], v[28:31]\n"
    "v_accvgpr_write_b32 a235, v3\n"
    "v_accvgpr_write_b32 a203, 0\n"
    "v_mfma_f32_16x16x32_bf16 v[48:51], v[224:227], a[60:63], v[48:51]\n"
    "s_lshl_b32 s83, s88, 17\n"
    "v_accvgpr_write_b32 a204, 0\n"
    "v_mfma_f32_16x16x32_bf16 v[52:55], v[228:231], a[60:63], v[52:55]\n"
    "buffer_load_dwordx4 v[200:203], v250, s[8:11], s47 offen offset:2048 sc1\n"
    "s_add_u32 s61, s61, 0x1000\n"
    "v_mfma_f32_16x16x32_bf16 v[64:67], v[224:227], a[92:95], v[64:67]\n"
    "s_and_b32 s89, s34, 31\n"
    "v_accvgpr_write_b32 a205, 0\n"
    "v_mfma_f32_16x16x32_bf16 v[68:71], v[228:231], a[92:95], v[68:71]\n"
    "s_lshl2_add_u32 s89, s89, s93\n"
    "v_accvgpr_write_b32 a206, 0\n"
    "s_waitcnt vmcnt(10)\n"
    "v_mfma_f32_16x16x32_bf16 v[32:35], v[120:123], a[0:3], 0\n"
    "buffer_load_dwordx4 v[204:207], v250, s[8:11], s47 offen offset:3072 sc1\n"
    "s_bitset0_b32 s61, 14\n"
    "v_mfma_f32_16x16x32_bf16 v[36:39], v[124:127], a[0:3], 0\n"
    "v_cvt_pk_bf16_f32 v24, v24, v25\n"
    "v_accvgpr_write_b32 a207, 0\n"
    "v_mfma_f32_16x16x32_bf16 v[56:59], v[120:123], a[32:35], 0\n"
    "v_cvt_pk_bf16_f32 v25, v26, v27\n"
    "v_accvgpr_write_b32 a208, 0\n"
    "v_mfma_f32_16x16x32_bf16 v[60:63], v[124:127], a[32:35], 0\n"
    "s_add_u32 s47, s52, s61\n"
    "buffer_load_dwordx4 v[216:219], v250, s[8:11], s47 offen offset:0 sc1\n"
    "v_mfma_f32_16x16x32_bf16 v[72:75], v[120:123], a[64:67], 0\n"
    "v_cvt_pk_bf16_f32 v26, v28, v29\n"
    "v_cvt_pk_bf16_f32 v27, v30, v31\n"
    "v_mfma_f32_16x16x32_bf16 v[76:79], v[124:127], a[64:67], 0\n"
    "buffer_store_dwordx4 v[24:27], v98, s[8:11], s35 offen offset:0 sc1\n"
    "v_accvgpr_write_b32 a209, 0\n"
    "s_waitcnt vmcnt(11)\n"
    "v_mfma_f32_16x16x32_bf16 v[32:35], v[136:139], a[4:7], v[32:35]\n"
    "buffer_load_dwordx4 v[220:223], v250, s[8:11], s47 offen offset:1024 sc1\n"
    "s_add_u32 s52, s52, 0x4000\n"
    "v_mfma_f32_16x16x32_bf16 v[36:39], v[140:143], a[4:7], v[36:39]\n"
    "v_cvt_pk_bf16_f32 v48, v48, v49\n"
    "v_accvgpr_write_b32 a210, 0\n"
    "v_mfma_f32_16x16x32_bf16 v[56:59], v[136:139], a[36:39], v[56:59]\n"
    "v_cvt_pk_bf16_f32 v49, v50, v51\n"
    "v_cvt_pk_bf16_f32 v50, v52, v53\n"
    "v_mfma_f32_16x16x32_bf16 v[60:63], v[140:143], a[36:39], v[60:63]\n"
    "buffer_load_dwordx4 v[232:235], v250, s[8:11], s47 offen offset:2048 sc1\n"
    "s_add_u32 s61, s61, 0x1000\n"
    "s_waitcnt vmcnt(11)\n"
    "v_cmp_ne_u32_e32 vcc, s39, v80\n"
    "v_mfma_f32_16x16x32_bf16 v[72:75], v[136:139], a[68:71], v[72:75]\n"
    "v_cvt_pk_bf16_f32 v51, v54, v55\n"
    "buffer_store_dwordx4 v[48:51], v98, s[8:11], s54 offen offset:0 sc1\n"
    "v_mfma_f32_16x16x32_bf16 v[76:79], v[140:143], a[68:71], v[76:79]\n"
    "v_cvt_pk_bf16_f32 v64, v64, v65\n"
    "v_cvt_pk_bf16_f32 v65, v66, v67\n"
    "s_waitcnt vmcnt(11)\n"
    "v_mfma_f32_16x16x32_bf16 v[32:35], v[152:155], a[8:11], v[32:35]\n"
    "buffer_load_dwordx4 v[236:239], v250, s[8:11], s47 offen offset:3072 sc1\n"
    "s_bitset0_b32 s61, 14\n"
    "v_mfma_f32_16x16x32_bf16 v[36:39], v[156:159], a[8:11], v[36:39]\n"
    "s_cbranch_vccnz .Lat_h1poll\n"
    ".Lat_h1poll_ret:\n"
    "s_mov_b32 m0, s56\n"
    "v_mfma_f32_16x16x32_bf16 v[56:59], v[152:155], a[40:43], v[56:59]\n"
    "buffer_load_dwordx4 v104, s[8:11], s57 offen offset:0 sc1 lds\n"
    "v_cvt_pk_bf16_f32 v66, v68, v69\n"
    "v_mfma_f32_16x16x32_bf16 v[60:63], v[156:159], a[40:43], v[60:63]\n"
    "v_cvt_pk_bf16_f32 v67, v70, v71\n"
    "buffer_store_dwordx4 v[64:67], v98, s[8:11], s60 offen offset:0 sc1\n"
    "v_mfma_f32_16x16x32_bf16 v[72:75], v[152:155], a[72:75], v[72:75]\n"
    "s_lshl_b32 s89, s89, 10\n"
    "v_accvgpr_write_b32 a211, 0\n"
    "v_mfma_f32_16x16x32_bf16 v[76:79], v[156:159], a[72:75], v[76:79]\n"
    "s_add_u32 s83, s83, s89\n"
    "v_accvgpr_write_b32 a212, 0\n"
    "s_waitcnt vmcnt(12)\n"
    "v_mfma_f32_16x16x32_bf16 v[32:35], v[168:171], a[12:15], v[32:35]\n"
    "buffer_load_dwordx4 v105, s[8:11], s57 offen offset:1024 sc1 lds\n"
    "v_accvgpr_write_b32 a213, 0\n"
    "v_mfma_f32_16x16x32_bf16 v[36:39], v[172:175], a[12:15], v[36:39]\n"
    "s_add_u32 s83, s83, 67682304\n"
    "v_accvgpr_write_b32 a214, 0\n"
    "v_mfma_f32_16x16x32_bf16 v[56:59], v[168:171], a[44:47], v[56:59]\n"
    "ds_write_b128 v99, v[24:27] offset:0\n"
    "v_accvgpr_write_b32 a215, 0\n"
    "v_mfma_f32_16x16x32_bf16 v[60:63], v[172:175], a[44:47], v[60:63]\n"
    "s_add_u32 m0, s43, 0\n"
    "v_accvgpr_write_b32 a216, 0\n"
    "v_mfma_f32_16x16x32_bf16 v[72:75], v[168:171], a[76:79], v[72:75]\n"
    "buffer_load_dwordx4 v104, s[8:11], s40 offen offset:0 sc1 lds\n"
    "v_accvgpr_write_b32 a217, 0\n"
    "v_mfma_f32_16x16x32_bf16 v[76:79], v[172:175], a[76:79], v[76:79]\n"
    "v_accvgpr_write_b32 a218, 0\n"
    "s_waitcnt vmcnt(12)\n"
    "v_mfma_f32_16x16x32_bf16 v[32:35], v[184:187], a[16:19], v[32:35]\n"
    "s_lshl_b32 s30, s93, 10\n"
    "v_mfma_f32_16x16x32_bf16 v[36:39], v[188:191], a[16:19], v[36:39]\n"
    "v_accvgpr_write_b32 a219, 0\n"
    "v_mfma_f32_16x16x32_bf16 v[56:59], v[184:187], a[48:51], v[56:59]\n"
    "buffer_load_dwordx4 v105, s[8:11], s40 offen offset:1024 sc1 lds\n"
    "v_mfma_f32_16x16x32_bf16 v[60:63], v[188:191], a[48:51], v[60:63]\n"
    "v_accvgpr_write_b32 a220, 0\n"
    "v_mfma_f32_16x16x32_bf16 v[72:75], v[184:187], a[80:83], v[72:75]\n"
    "s_add_u32 s30, s30, 20480\n"
    "v_mfma_f32_16x16x32_bf16 v[76:79], v[188:191], a[80:83], v[76:79]\n"
    "ds_write_b128 v99, v[48:51] offset:8192\n"
    "v_accvgpr_write_b32 a221, 0\n"
    "s_waitcnt vmcnt(11)\n"
    "v_mfma_f32_16x16x32_bf16 v[32:35], v[200:203], a[20:23], v[32:35]\n"
    "buffer_load_dwordx4 v106, s[8:11], s40 offen offset:2048 sc1 lds\n"
    "v_mfma_f32_16x16x32_bf16 v[36:39], v[204:207], a[20:23], v[36:39]\n"
    "v_accvgpr_write_b32 a222, 0\n"
    "v_mfma_f32_16x16x32_bf16 v[56:59], v[200:203], a[52:55], v[56:59]\n"
    "v_accvgpr_write_b32 a223, 0\n"
    "v_mfma_f32_16x16x32_bf16 v[60:63], v[204:207], a[52:55], v[60:63]\n"
    "s_bitcmp0_b32 s92, 0\n"
    "s_cselect_b32 s85, 1, 0\n"
    "s_cselect_b32 s80, 5, 0\n"
    "v_mfma_f32_16x16x32_bf16 v[72:75], v[200:203], a[84:87], v[72:75]\n"
    "buffer_load_dwordx4 v107, s[8:11], s40 offen offset:3072 sc1 lds\n"
    "v_mfma_f32_16x16x32_bf16 v[76:79], v[204:207], a[84:87], v[76:79]\n"
    "v_accvgpr_write_b32 a224, 0\n"
    "s_waitcnt vmcnt(10)\n"
    "v_mfma_f32_16x16x32_bf16 v[32:35], v[216:219], a[24:27], v[32:35]\n"
    "s_bitcmp0_b32 s92, 0\n"
    "s_cselect_b32 s82, s82, 2.0\n"
    "s_cselect_b32 s83, s83, 2.0\n"
    "v_mfma_f32_16x16x32_bf16 v[36:39], v[220:223], a[24:27], v[36:39]\n"
    "s_add_u32 s40, s40, 0x8000\n"
    "v_mfma_f32_16x16x32_bf16 v[56:59], v[216:219], a[56:59], v[56:59]\n"
    "ds_write_b128 v99, v[64:67] offset:49152\n"
    "v_mfma_f32_16x16x32_bf16 v[60:63], v[220:223], a[56:59], v[60:63]\n"
    "v_accvgpr_write_b32 a225, 0\n"
    "v_mfma_f32_16x16x32_bf16 v[72:75], v[216:219], a[88:91], v[72:75]\n"
    "v_accvgpr_write_b32 a226, 0\n"
    "v_mfma_f32_16x16x32_bf16 v[76:79], v[220:223], a[88:91], v[76:79]\n"
    "s_mov_b32 s84, 2.0\n"
    "s_waitcnt vmcnt(7)\n"
    "v_mfma_f32_16x16x32_bf16 v[32:35], v[232:235], a[28:31], v[32:35]\n"
    "v_accvgpr_write_b32 a227, 0\n"
    "v_mfma_f32_16x16x32_bf16 v[36:39], v[236:239], a[28:31], v[36:39]\n"
    "v_accvgpr_write_b32 a228, 0\n"
    "v_mfma_f32_16x16x32_bf16 v[56:59], v[232:235], a[60:63], v[56:59]\n"
    "v_accvgpr_write_b32 a229, 0\n"
    "v_mfma_f32_16x16x32_bf16 v[60:63], v[236:239], a[60:63], v[60:63]\n"
    "v_accvgpr_write_b32 a230, 0\n"
    "v_mfma_f32_16x16x32_bf16 v[72:75], v[232:235], a[92:95], v[72:75]\n"
    "s_movk_i32 s90, 512\n"
    "v_mfma_f32_16x16x32_bf16 v[76:79], v[236:239], a[92:95], v[76:79]\n"
    "v_accvgpr_write_b32 a231, 0\n"
    "v_cvt_pk_bf16_f32 v32, v32, v33\n"
    "v_cvt_pk_bf16_f32 v33, v34, v35\n"
    "v_cvt_pk_bf16_f32 v34, v36, v37\n"
    "v_cvt_pk_bf16_f32 v35, v38, v39\n"
    "ds_write_b128 v99, v[32:35] offset:1024\n"
    "buffer_store_dwordx4 v[32:35], v98, s[8:11], s35 offen offset:1024 sc1\n"
    "v_cvt_pk_bf16_f32 v56, v56, v57\n"
    "v_cvt_pk_bf16_f32 v57, v58, v59\n"
    "v_cvt_pk_bf16_f32 v58, v60, v61\n"
    "v_cvt_pk_bf16_f32 v59, v62, v63\n"
    "ds_write_b128 v99, v[56:59] offset:9216\n"
    "buffer_store_dwordx4 v[56:59], v98, s[8:11], s54 offen offset:1024 sc1\n"
    "v_cvt_pk_bf16_f32 v72, v72, v73\n"
    "v_cvt_pk_bf16_f32 v73, v74, v75\n"
    "v_cvt_pk_bf16_f32 v74, v76, v77\n"
    "v_cvt_pk_bf16_f32 v75, v78, v79\n"
    "ds_write_b128 v99, v[72:75] offset:50176\n"
    "buffer_store_dwordx4 v[72:75], v98, s[8:11], s60 offen offset:1024 sc1\n"
    "v_mov_b64_e32 v[8:9], 0\n"
    "v_mov_b64_e32 v[10:11], 0\n"
    "s_add_u32 m0, s43, 4096\n"
    "v_mov_b64_e32 v[12:13], 0\n"
    "v_mov_b64_e32 v[14:15], 0\n"
    "buffer_load_dwordx4 v104, s[8:11], s41 offen offset:0 sc1 lds\n"
    "v_mov_b64_e32 v[16:17], 0\n"
    "v_mov_b64_e32 v[18:19], 0\n"
    "buffer_load_dwordx4 v105, s[8:11], s41 offen offset:1024 sc1 lds\n"
    "v_mov_b64_e32 v[28:29], 0\n"
    "v_mov_b64_e32 v[30:31], 0\n"
    "buffer_load_dwordx4 v106, s[8:11], s41 offen offset:2048 sc1 lds\n"
    "v_mov_b64_e32 v[32:33], 0\n"
    "v_mov_b64_e32 v[34:35], 0\n"
    "buffer_load_dwordx4 v107, s[8:11], s41 offen offset:3072 sc1 lds\n"
    "v_mov_b64_e32 v[36:37], 0\n"
    "v_mov_b64_e32 v[38:39], 0\n"
    "s_add_u32 s41, s41, 0x8000\n"
    "v_mov_b64_e32 v[48:49], 0\n"
    "v_mov_b64_e32 v[50:51], 0\n"
    "v_mov_b64_e32 v[52:53], 0\n"
    "v_mov_b64_e32 v[54:55], 0\n"
    "v_mov_b64_e32 v[56:57], 0\n"
    "v_mov_b64_e32 v[58:59], 0\n"
    "v_mov_b64_e32 v[68:69], 0\n"
    "v_mov_b64_e32 v[70:71], 0\n"
    "v_mov_b64_e32 v[72:73], 0\n"
    "v_mov_b64_e32 v[74:75], 0\n"
    "v_mov_b64_e32 v[76:77], 0\n"
    "v_mov_b64_e32 v[78:79], 0\n"
    "s_waitcnt vmcnt(4)\n"
    "buffer_store_dword v84, v86, s[8:11], s58 offen offset:0 sc1\n"
    "v_mov_b64_e32 v[88:89], 0\n"
    "v_mov_b64_e32 v[90:91], 0\n"
    "s_add_u32 m0, s43, 8192\n"
    "v_mov_b64_e32 v[92:93], 0\n"
    "v_mov_b64_e32 v[94:95], 0\n"
    "buffer_load_dwordx4 v104, s[8:11], s40 offen offset:0 sc1 lds\n"
    "v_mov_b64_e32 v[96:97], 0\n"
    "v_mov_b64_e32 v[98:99], 0\n"
    "buffer_load_dwordx4 v105, s[8:11], s40 offen offset:1024 sc1 lds\n"
    "v_mov_b64_e32 v[108:109], 0\n"
    "v_mov_b64_e32 v[110:111], 0\n"
    "buffer_load_dwordx4 v106, s[8:11], s40 offen offset:2048 sc1 lds\n"
    "v_mov_b64_e32 v[112:113], 0\n"
    "v_mov_b64_e32 v[114:115], 0\n"
    "buffer_load_dwordx4 v107, s[8:11], s40 offen offset:3072 sc1 lds\n"
    "v_mov_b64_e32 v[116:117], 0\n"
    "v_mov_b64_e32 v[118:119], 0\n"
    "s_add_u32 s40, s40, 0x8000\n"
    "v_mov_b64_e32 v[128:129], 0\n"
    "v_mov_b64_e32 v[130:131], 0\n"
    "s_add_u32 m0, s43, 12288\n"
    "v_mov_b64_e32 v[132:133], 0\n"
    "v_mov_b64_e32 v[134:135], 0\n"
    "buffer_load_dwordx4 v104, s[8:11], s41 offen offset:0 sc1 lds\n"
    "v_mov_b64_e32 v[136:137], 0\n"
    "v_mov_b64_e32 v[138:139], 0\n"
    "buffer_load_dwordx4 v105, s[8:11], s41 offen offset:1024 sc1 lds\n"
    "buffer_load_dwordx4 v106, s[8:11], s41 offen offset:2048 sc1 lds\n"
    "buffer_load_dwordx4 v107, s[8:11], s41 offen offset:3072 sc1 lds\n"
    "s_add_u32 s41, s41, 0x8000\n"
    ".Lat_p2:\n"
    "v_mov_b32_e32 v220, 0xc2980000\n"
    "v_mov_b32_e32 v221, 0xc2980000\n"
    "v_mov_b64_e32 v[222:223], v[220:221]\n"
    "v_mov_b64_e32 v[224:225], v[220:221]\n"
    "v_mov_b64_e32 v[226:227], v[220:221]\n"
    "v_mov_b64_e32 v[228:229], v[220:221]\n"
    "v_mov_b64_e32 v[230:231], v[220:221]\n"
    "v_mov_b64_e32 v[232:233], v[220:221]\n"
    "v_mov_b64_e32 v[234:235], v[220:221]\n"
    "v_mov_b64_e32 v[236:237], v[220:221]\n"
    "v_mov_b64_e32 v[238:239], v[220:221]\n"
    "s_andn2_b32 s47, s58, 0x600\n"
    "s_mov_b32 s44, 9\n"
    "s_waitcnt vmcnt(9)\n"
    "s_waitcnt lgkmcnt(0)\n"
    "s_barrier\n"
    "s_cmp_lg_u32 s49, 0\n"
    "s_cbranch_scc1 .Lat_maxpass\n"
    "ds_read_b128 a[0:3], v85 offset:0\n"
    "ds_read_b128 a[4:7], v85 offset:1024\n"
    "ds_read_b128 a[8:11], v85 offset:2048\n"
    "ds_read_b128 a[12:15], v85 offset:3072\n"
    "ds_read_b128 a[16:19], v85 offset:4096\n"
    "ds_read_b128 a[20:23], v85 offset:5120\n"
    "ds_read_b128 a[24:27], v85 offset:6144\n"
    "ds_read_b128 a[28:31], v85 offset:7168\n"
    "ds_read_b128 a[32:35], v255 offset:0\n"
    "ds_read_b128 a[36:39], v255 offset:1024\n"
    "ds_read_b128 a[40:43], v255 offset:2048\n"
    "ds_read_b128 a[44:47], v255 offset:3072\n"
    "ds_read_b128 a[48:51], v255 offset:4096\n"
    "ds_read_b128 a[52:55], v255 offset:5120\n"
    "ds_read_b128 a[56:59], v255 offset:6144\n"
    "ds_read_b128 a[60:63], v255 offset:7168\n"
    ".Lat_maxpass_ret:\n"
    "ds_read_b128 a[160:163], v46 offset:0\n"
    "ds_read_b128 a[164:167], v47 offset:0\n"
    "ds_read_b128 a[168:171], v46 offset:2048\n"
    "ds_read_b128 a[172:175], v47 offset:2048\n"
    "ds_read_b128 a[176:179], v46 offset:4096\n"
    "ds_read_b128 a[180:183], v47 offset:4096\n"
    "ds_read_b128 a[184:187], v46 offset:6144\n"
    "ds_read_b128 a[188:191], v47 offset:6144\n"
    "s_mul_i32 s48, s33, 81920\n"
    "s_add_u32 s48, s48, 11345920\n"
    "s_lshl_b32 s46, s93, 11\n"
    "s_add_u32 s48, s48, s46\n"
    "s_add_u32 s46, s46, 49152\n"
    "buffer_load_dword v254, v253, s[8:11], s47 offen offset:0 sc1\n"
    "s_waitcnt lgkmcnt(4)\n"
    "v_mfma_f32_16x16x32_bf16 v[212:215], a[160:163], a[0:3], v[220:223]\n"
    "s_and_b32 s45, s32, 1\n"
    "ds_read_b64_tr_b16 v[140:141], v4 offset:0\n"
    "v_mfma_f32_16x16x32_bf16 v[216:219], a[160:163], a[32:35], v[224:227]\n"
    "s_xor_b32 s45, s45, 9\n"
    "ds_read_b64_tr_b16 v[142:143], v5 offset:0\n"
    "v_mfma_f32_16x16x32_bf16 v[212:215], a[164:167], a[4:7], v[212:215]\n"
    "s_lshl_b32 s45, s45, 13\n"
    "ds_read_b64_tr_b16 v[144:145], v6 offset:0\n"
    "v_mfma_f32_16x16x32_bf16 v[216:219], a[164:167], a[36:39], v[216:219]\n"
    "s_add_u32 s45, s45, s48\n"
    "ds_read_b64_tr_b16 v[146:147], v7 offset:0\n"
    "v_mfma_f32_16x16x32_bf16 v[212:215], a[168:171], a[8:11], v[212:215]\n"
    "s_add_u32 s54, s32, 1\n"
    "ds_read_b64_tr_b16 v[148:149], v4 offset:2048\n"
    "v_mfma_f32_16x16x32_bf16 v[216:219], a[168:171], a[40:43], v[216:219]\n"
    "s_and_b32 s54, s54, 3\n"
    "ds_read_b64_tr_b16 v[150:151], v5 offset:2048\n"
    "v_mfma_f32_16x16x32_bf16 v[212:215], a[172:175], a[12:15], v[212:215]\n"
    "s_lshl_b32 s54, s54, 13\n"
    "ds_read_b64_tr_b16 v[152:153], v6 offset:2048\n"
    "v_mfma_f32_16x16x32_bf16 v[216:219], a[172:175], a[44:47], v[216:219]\n"
    "s_add_u32 s54, s54, s48\n"
    "ds_read_b64_tr_b16 v[154:155], v7 offset:2048\n"
    "s_waitcnt lgkmcnt(8)\n"
    "v_mfma_f32_16x16x32_bf16 v[212:215], a[176:179], a[16:19], v[212:215]\n"
    "s_add_u32 s55, s32, 2\n"
    "ds_read_b128 v[180:183], v240 offset:2048\n"
    "v_mfma_f32_16x16x32_bf16 v[216:219], a[176:179], a[48:51], v[216:219]\n"
    "s_and_b32 s55, s55, 3\n"
    "ds_read_b128 v[184:187], v241 offset:2048\n"
    "v_mfma_f32_16x16x32_bf16 v[212:215], a[180:183], a[20:23], v[212:215]\n"
    "s_lshl_b32 s55, s55, 13\n"
    "ds_read_b128 v[188:191], v240 offset:6144\n"
    "v_mfma_f32_16x16x32_bf16 v[216:219], a[180:183], a[52:55], v[216:219]\n"
    "s_add_u32 s55, s55, s48\n"
    "ds_read_b128 v[192:195], v241 offset:6144\n"
    "v_mfma_f32_16x16x32_bf16 v[212:215], a[184:187], a[24:27], v[212:215]\n"
    "s_add_u32 s60, s32, 3\n"
    "ds_read_b128 v[196:199], v242 offset:2048\n"
    "v_mfma_f32_16x16x32_bf16 v[216:219], a[184:187], a[56:59], v[216:219]\n"
    "s_and_b32 s60, s60, 3\n"
    "ds_read_b128 v[200:203], v243 offset:2048\n"
    "v_mfma_f32_16x16x32_bf16 v[212:215], a[188:191], a[28:31], v[212:215]\n"
    "s_lshl_b32 s60, s60, 13\n"
    "ds_read_b128 v[204:207], v242 offset:6144\n"
    "v_mfma_f32_16x16x32_bf16 v[216:219], a[188:191], a[60:63], v[216:219]\n"
    "s_add_u32 s60, s60, s48\n"
    "ds_read_b128 v[208:211], v243 offset:6144\n"
    "s_lshr_b32 s61, s32, 1\n"
    "s_or_b32 s61, s61, s50\n"
    "s_sub_u32 s61, s61, 1\n"
    "s_add_u32 m0, s46, 65536\n"
    "s_waitcnt vmcnt(0)\n"
    "v_cmp_ne_u32_e32 vcc, s39, v254\n"
    "ds_read_b128 a[160:163], v240 offset:0\n"
    "ds_read_b128 a[164:167], v241 offset:0\n"
    "ds_read_b128 a[168:171], v240 offset:4096\n"
    "ds_read_b128 a[172:175], v241 offset:4096\n"
    "ds_read_b128 a[176:179], v242 offset:0\n"
    "s_cbranch_vccnz .Lat_qpoll\n"
    ".Lat_qpoll_ret:\n"
    "buffer_load_dwordx4 v250, s[8:11], s45 offen offset:0 sc1 lds\n"
    "buffer_load_dwordx4 v250, s[8:11], s45 offen offset:1024 sc1 lds\n"
    "s_add_u32 m0, s46, 8192\n"
    "ds_read_b128 a[180:183], v243 offset:0\n"
    "s_nop 0\n"
    "ds_read_b128 a[184:187], v242 offset:4096\n"
    "buffer_load_dwordx4 v250, s[8:11], s54 offen offset:0 sc1 lds\n"
    "ds_read_b128 a[188:191], v243 offset:4096\n"
    "buffer_load_dwordx4 v250, s[8:11], s54 offen offset:1024 sc1 lds\n"
    "v_exp_f32_e32 v212, v212\n"
    "s_add_u32 m0, s46, 73728\n"
    "v_exp_f32_e32 v213, v213\n"
    "s_add_u32 s54, s54, 0x8000\n"
    "v_exp_f32_e32 v214, v214\n"
    "buffer_load_dwordx4 v250, s[8:11], s54 offen offset:0 sc1 lds\n"
    "v_exp_f32_e32 v215, v215\n"
    "buffer_load_dwordx4 v250, s[8:11], s54 offen offset:1024 sc1 lds\n"
    "v_exp_f32_e32 v216, v216\n"
    "s_add_u32 m0, s46, 32768\n"
    "v_exp_f32_e32 v217, v217\n"
    "s_nop 0\n"
    "v_exp_f32_e32 v218, v218\n"
    "buffer_load_dwordx4 v250, s[8:11], s55 offen offset:0 sc1 lds\n"
    "v_exp_f32_e32 v219, v219\n"
    "buffer_load_dwordx4 v250, s[8:11], s55 offen offset:1024 sc1 lds\n"
    "v_cvt_pk_bf16_f32 v212, v212, v213\n"
    "v_cvt_pk_bf16_f32 v213, v214, v215\n"
    "v_cvt_pk_bf16_f32 v216, v216, v217\n"
    "v_cvt_pk_bf16_f32 v217, v218, v219\n"
    "v_and_b32_e32 v216, s61, v216\n"
    "v_and_b32_e32 v217, s61, v217\n"
    "s_waitcnt lgkmcnt(15)\n"
    "v_mfma_f32_16x16x16_bf16 a[236:239], v[140:141], v[212:213], 0\n"
    "s_add_u32 m0, s46, 98304\n"
    "v_mfma_f32_16x16x16_bf16 v[0:3], v[142:143], v[212:213], 0\n"
    "s_add_u32 s55, s55, 0x8000\n"
    "v_mfma_f32_16x16x16_bf16 v[20:23], v[144:145], v[212:213], 0\n"
    "buffer_load_dwordx4 v250, s[8:11], s55 offen offset:0 sc1 lds\n"
    "v_mfma_f32_16x16x16_bf16 v[40:43], v[146:147], v[212:213], 0\n"
    "buffer_load_dwordx4 v250, s[8:11], s55 offen offset:1024 sc1 lds\n"
    "v_mfma_f32_16x16x16_bf16 v[60:63], v[148:149], v[212:213], 0\n"
    "s_add_u32 m0, s46, 40960\n"
    "v_mfma_f32_16x16x16_bf16 v[80:83], v[150:151], v[212:213], 0\n"
    "buffer_load_dwordx4 v250, s[8:11], s60 offen offset:0 sc1 lds\n"
    "v_mfma_f32_16x16x16_bf16 v[100:103], v[152:153], v[212:213], 0\n"
    "buffer_load_dwordx4 v250, s[8:11], s60 offen offset:1024 sc1 lds\n"
    "v_mfma_f32_16x16x16_bf16 v[120:123], v[154:155], v[212:213], 0\n"
    "s_add_u32 m0, s46, 106496\n"
    "v_mfma_f32_16x16x16_bf16 a[240:243], v[140:141], v[216:217], 0\n"
    "s_add_u32 s60, s60, 0x8000\n"
    "v_mfma_f32_16x16x16_bf16 v[4:7], v[142:143], v[216:217], 0\n"
    "buffer_load_dwordx4 v250, s[8:11], s60 offen offset:0 sc1 lds\n"
    "v_mfma_f32_16x16x16_bf16 v[24:27], v[144:145], v[216:217], 0\n"
    "buffer_load_dwordx4 v250, s[8:11], s60 offen offset:1024 sc1 lds\n"
    "v_mfma_f32_16x16x16_bf16 v[44:47], v[146:147], v[216:217], 0\n"
    "v_mfma_f32_16x16x16_bf16 v[64:67], v[148:149], v[216:217], 0\n"
    "buffer_load_dword v253, v250, s[8:11], s59 offen offset:0 sc1\n"
    "v_mfma_f32_16x16x16_bf16 v[84:87], v[150:151], v[216:217], 0\n"
    "v_mfma_f32_16x16x16_bf16 v[104:107], v[152:153], v[216:217], 0\n"
    "v_mfma_f32_16x16x16_bf16 v[124:127], v[154:155], v[216:217], 0\n"
    "s_waitcnt lgkmcnt(0)\n"
    "buffer_load_dword v254, v252, s[8:11], s59 offen offset:0 sc1\n"
    "v_mfma_f32_16x16x32_bf16 v[140:143], a[160:163], a[0:3], v[220:223]\n"
    "v_mfma_f32_16x16x32_bf16 v[140:143], a[164:167], a[4:7], v[140:143]\n"
    "v_mfma_f32_16x16x32_bf16 v[140:143], a[168:171], a[8:11], v[140:143]\n"
    "v_mfma_f32_16x16x32_bf16 v[140:143], a[172:175], a[12:15], v[140:143]\n"
    "v_mfma_f32_16x16x32_bf16 v[140:143], a[176:179], a[16:19], v[140:143]\n"
    "v_mfma_f32_16x16x32_bf16 v[140:143], a[180:183], a[20:23], v[140:143]\n"
    "v_mfma_f32_16x16x32_bf16 v[140:143], a[184:187], a[24:27], v[140:143]\n"
    "v_mfma_f32_16x16x32_bf16 v[140:143], a[188:191], a[28:31], v[140:143]\n"
    "v_mfma_f32_16x16x32_bf16 v[144:147], v[180:183], a[0:3], v[220:223]\n"
    "v_mfma_f32_16x16x32_bf16 v[144:147], v[184:187], a[4:7], v[144:147]\n"
    "s_waitcnt vmcnt(10)\n"
    "s_barrier\n"
    "v_mfma_f32_16x16x32_bf16 v[144:147], v[188:191], a[8:11], v[144:147]\n"
    "s_mov_b32 m0, s42\n"
    "ds_read_b128 a[32:35], v255 offset:0\n"
    "v_exp_f32_e32 v140, v140\n"
    "v_mfma_f32_16x16x32_bf16 v[144:147], v[192:195], a[12:15], v[144:147]\n"
    "ds_read_b128 a[36:39], v255 offset:1024\n"
    "v_exp_f32_e32 v141, v141\n"
    "v_mfma_f32_16x16x32_bf16 v[144:147], v[196:199], a[16:19], v[144:147]\n"
    "ds_read_b128 a[40:43], v255 offset:2048\n"
    "ds_read_b128 a[44:47], v255 offset:3072\n"
    "v_mfma_f32_16x16x32_bf16 v[144:147], v[200:203], a[20:23], v[144:147]\n"
    "ds_read_b128 a[48:51], v255 offset:4096\n"
    "ds_read_b128 a[52:55], v255 offset:5120\n"
    "v_mfma_f32_16x16x32_bf16 v[144:147], v[204:207], a[24:27], v[144:147]\n"
    "ds_read_b128 a[56:59], v255 offset:6144\n"
    "ds_read_b128 a[60:63], v255 offset:7168\n"
    "v_mfma_f32_16x16x32_bf16 v[144:147], v[208:211], a[28:31], v[144:147]\n"
    "s_waitcnt lgkmcnt(4)\n"
    "v_exp_f32_e32 v142, v142\n"
    "v_mfma_f32_16x16x32_bf16 v[148:151], a[160:163], a[32:35], v[224:227]\n"
    "ds_read_b128 a[64:67], v255 offset:8192\n"
    "v_exp_f32_e32 v143, v143\n"
    "v_mfma_f32_16x16x32_bf16 v[148:151], a[164:167], a[36:39], v[148:151]\n"
    "ds_read_b128 a[68:71], v255 offset:9216\n"
    "v_cvt_pk_bf16_f32 v140, v140, v141\n"
    "v_mfma_f32_16x16x32_bf16 v[148:151], a[168:171], a[40:43], v[148:151]\n"
    "ds_read_b128 a[72:75], v255 offset:10240\n"
    "v_cvt_pk_bf16_f32 v141, v142, v143\n"
    "v_mfma_f32_16x16x32_bf16 v[148:151], a[172:175], a[44:47], v[148:151]\n"
    "s_waitcnt lgkmcnt(3)\n"
    "v_exp_f32_e32 v144, v144\n"
    "v_mfma_f32_16x16x32_bf16 v[148:151], a[176:179], a[48:51], v[148:151]\n"
    "ds_read_b128 a[76:79], v255 offset:11264\n"
    "ds_write_addtid_b32 v140 offset:0\n"
    "v_mfma_f32_16x16x32_bf16 v[148:151], a[180:183], a[52:55], v[148:151]\n"
    "ds_read_b128 a[80:83], v255 offset:12288\n"
    "v_exp_f32_e32 v145, v145\n"
    "v_mfma_f32_16x16x32_bf16 v[148:151], a[184:187], a[56:59], v[148:151]\n"
    "v_exp_f32_e32 v146, v146\n"
    "v_mfma_f32_16x16x32_bf16 v[148:151], a[188:191], a[60:63], v[148:151]\n"
    "ds_read_b128 a[84:87], v255 offset:13312\n"
    "v_exp_f32_e32 v147, v147\n"
    "v_mfma_f32_16x16x32_bf16 v[152:155], v[180:183], a[32:35], v[224:227]\n"
    "ds_read_b128 a[88:91], v255 offset:14336\n"
    "v_cvt_pk_bf16_f32 v142, v144, v145\n"
    "v_mfma_f32_16x16x32_bf16 v[152:155], v[184:187], a[36:39], v[152:155]\n"
    "ds_read_b128 a[92:95], v255 offset:15360\n"
    "v_cvt_pk_bf16_f32 v143, v146, v147\n"
    "v_mfma_f32_16x16x32_bf16 v[152:155], v[188:191], a[40:43], v[152:155]\n"
    "v_exp_f32_e32 v148, v148\n"
    "v_mfma_f32_16x16x32_bf16 v[152:155], v[192:195], a[44:47], v[152:155]\n"
    "v_exp_f32_e32 v149, v149\n"
    "v_mfma_f32_16x16x32_bf16 v[152:155], v[196:199], a[48:51], v[152:155]\n"
    "v_exp_f32_e32 v150, v150\n"
    "v_mfma_f32_16x16x32_bf16 v[152:155], v[200:203], a[52:55], v[152:155]\n"
    "v_exp_f32_e32 v151, v151\n"
    "v_mfma_f32_16x16x32_bf16 v[152:155], v[204:207], a[56:59], v[152:155]\n"
    "s_waitcnt vmcnt(2)\n"
    "s_barrier\n"
    "v_mfma_f32_16x16x32_bf16 v[152:155], v[208:211], a[60:63], v[152:155]\n"
    "s_waitcnt lgkmcnt(5)\n"
    "ds_read_b128 a[96:99], v255 offset:32768\n"
    "ds_write_addtid_b32 v141 offset:256\n"
    "v_mfma_f32_16x16x32_bf16 v[156:159], a[160:163], a[64:67], v[228:231]\n"
    "ds_read_b128 a[100:103], v255 offset:33792\n"
    "ds_write_addtid_b32 v142 offset:512\n"
    "v_mfma_f32_16x16x32_bf16 v[156:159], a[164:167], a[68:71], v[156:159]\n"
    "ds_read_b128 a[104:107], v255 offset:34816\n"
    "v_mfma_f32_16x16x32_bf16 v[156:159], a[168:171], a[72:75], v[156:159]\n"
    "ds_read_b128 a[108:111], v255 offset:35840\n"
    "v_mfma_f32_16x16x32_bf16 v[156:159], a[172:175], a[76:79], v[156:159]\n"
    "s_waitcnt lgkmcnt(6)\n"
    "ds_read_b128 a[112:115], v255 offset:36864\n"
    "v_cvt_pk_bf16_f32 v148, v148, v149\n"
    "v_mfma_f32_16x16x32_bf16 v[156:159], a[176:179], a[80:83], v[156:159]\n"
    "ds_read_b128 a[116:119], v255 offset:37888\n"
    "v_cvt_pk_bf16_f32 v149, v150, v151\n"
    "v_mfma_f32_16x16x32_bf16 v[156:159], a[180:183], a[84:87], v[156:159]\n"
    "ds_read_b128 a[120:123], v255 offset:38912\n"
    "v_exp_f32_e32 v152, v152\n"
    "v_mfma_f32_16x16x32_bf16 v[156:159], a[184:187], a[88:91], v[156:159]\n"
    "ds_read_b128 a[124:127], v255 offset:39936\n"
    "v_exp_f32_e32 v153, v153\n"
    "v_mfma_f32_16x16x32_bf16 v[156:159], a[188:191], a[92:95], v[156:159]\n"
    "ds_read_b128 a[128:131], v255 offset:40960\n"
    "v_exp_f32_e32 v154, v154\n"
    "v_mfma_f32_16x16x32_bf16 v[160:163], v[180:183], a[64:67], v[228:231]\n"
    "ds_read_b128 a[132:135], v255 offset:41984\n"
    "v_exp_f32_e32 v155, v155\n"
    "v_mfma_f32_16x16x32_bf16 v[160:163], v[184:187], a[68:71], v[160:163]\n"
    "ds_read_b128 a[136:139], v255 offset:43008\n"
    "v_cvt_pk_bf16_f32 v150, v152, v153\n"
    "v_mfma_f32_16x16x32_bf16 v[160:163], v[188:191], a[72:75], v[160:163]\n"
    "ds_read_b128 a[140:143], v255 offset:44032\n"
    "v_cvt_pk_bf16_f32 v151, v154, v155\n"
    "v_mfma_f32_16x16x32_bf16 v[160:163], v[192:195], a[76:79], v[160:163]\n"
    "ds_read_b128 a[144:147], v255 offset:45056\n"
    "ds_read_b128 a[148:151], v255 offset:46080\n"
    "v_mfma_f32_16x16x32_bf16 v[160:163], v[196:199], a[80:83], v[160:163]\n"
    "ds_read_b128 a[152:155], v255 offset:47104\n"
    "ds_read_b128 a[156:159], v255 offset:48128\n"
    "v_mfma_f32_16x16x32_bf16 v[160:163], v[200:203], a[84:87], v[160:163]\n"
    "s_waitcnt vmcnt(0)\n"
    "ds_write_addtid_b32 v148 offset:1024\n"
    "v_xor_b32_e32 v254, s39, v254\n"
    "v_xor_b32_e32 v253, s39, v253\n"
    "v_mfma_f32_16x16x32_bf16 v[160:163], v[204:207], a[88:91], v[160:163]\n"
    "ds_write_addtid_b32 v149 offset:1280\n"
    "v_or_b32_e32 v253, v253, v254\n"
    "v_cmp_ne_u32_e32 vcc, 0, v253\n"
    "v_mfma_f32_16x16x32_bf16 v[160:163], v[208:211], a[92:95], v[160:163]\n"
    "s_waitcnt lgkmcnt(14)\n"
    "v_exp_f32_e32 v156, v156\n"
    "v_mfma_f32_16x16x32_bf16 v[164:167], a[160:163], a[96:99], v[232:235]\n"
    "v_exp_f32_e32 v157, v157\n"
    "v_mfma_f32_16x16x32_bf16 v[164:167], a[164:167], a[100:103], v[164:167]\n"
    "v_exp_f32_e32 v158, v158\n"
    "v_mfma_f32_16x16x32_bf16 v[164:167], a[168:171], a[104:107], v[164:167]\n"
    "s_cbranch_vccnz .Lat_hpoll\n"
    ".Lat_hpoll_ret:\n"
    "s_waitcnt lgkmcnt(2)\n"
    "s_waitcnt vmcnt(17)\n"
    "s_barrier\n"
    "v_mfma_f32_16x16x32_bf16 v[164:167], a[172:175], a[108:111], v[164:167]\n"
    "v_exp_f32_e32 v159, v159\n"
    "v_mfma_f32_16x16x32_bf16 v[164:167], a[176:179], a[112:115], v[164:167]\n"
    "s_add_u32 m0, s43, 16384\n"
    "v_cvt_pk_bf16_f32 v156, v156, v157\n"
    "v_mfma_f32_16x16x32_bf16 v[164:167], a[180:183], a[116:119], v[164:167]\n"
    "buffer_load_dwordx4 v248, s[8:11], s40 offen offset:0 sc1 lds\n"
    "v_cvt_pk_bf16_f32 v157, v158, v159\n"
    "v_mfma_f32_16x16x32_bf16 v[164:167], a[184:187], a[120:123], v[164:167]\n"
    "v_exp_f32_e32 v160, v160\n"
    "v_mfma_f32_16x16x32_bf16 v[164:167], a[188:191], a[124:127], v[164:167]\n"
    "v_exp_f32_e32 v161, v161\n"
    "v_mfma_f32_16x16x32_bf16 v[168:171], v[180:183], a[96:99], v[232:235]\n"
    "buffer_load_dwordx4 v248, s[8:11], s40 offen offset:1024 sc1 lds\n"
    "v_mfma_f32_16x16x32_bf16 v[168:171], v[184:187], a[100:103], v[168:171]\n"
    "v_exp_f32_e32 v162, v162\n"
    "v_mfma_f32_16x16x32_bf16 v[168:171], v[188:191], a[104:107], v[168:171]\n"
    "v_exp_f32_e32 v163, v163\n"
    "v_mfma_f32_16x16x32_bf16 v[168:171], v[192:195], a[108:111], v[168:171]\n"
    "buffer_load_dwordx4 v248, s[8:11], s40 offen offset:2048 sc1 lds\n"
    "v_mfma_f32_16x16x32_bf16 v[168:171], v[196:199], a[112:115], v[168:171]\n"
    "v_cvt_pk_bf16_f32 v158, v160, v161\n"
    "v_cvt_pk_bf16_f32 v159, v162, v163\n"
    "v_mfma_f32_16x16x32_bf16 v[168:171], v[200:203], a[116:119], v[168:171]\n"
    "v_exp_f32_e32 v164, v164\n"
    "v_mfma_f32_16x16x32_bf16 v[168:171], v[204:207], a[120:123], v[168:171]\n"
    "buffer_load_dwordx4 v248, s[8:11], s40 offen offset:3072 sc1 lds\n"
    "v_mfma_f32_16x16x32_bf16 v[168:171], v[208:211], a[124:127], v[168:171]\n"
    "s_add_u32 s40, s40, 0x8000\n"
    "v_exp_f32_e32 v165, v165\n"
    "v_mfma_f32_16x16x32_bf16 v[172:175], a[160:163], a[128:131], v[236:239]\n"
    "s_add_u32 m0, s43, 20480\n"
    "v_exp_f32_e32 v166, v166\n"
    "v_mfma_f32_16x16x32_bf16 v[172:175], a[164:167], a[132:135], v[172:175]\n"
    "buffer_load_dwordx4 v248, s[8:11], s41 offen offset:0 sc1 lds\n"
    "ds_read_b128 a[160:163], v240 offset:8192\n"
    "v_mfma_f32_16x16x32_bf16 v[172:175], a[168:171], a[136:139], v[172:175]\n"
    "ds_read_b128 a[164:167], v241 offset:8192\n"
    "v_mfma_f32_16x16x32_bf16 v[172:175], a[172:175], a[140:143], v[172:175]\n"
    "ds_read_b128 a[168:171], v240 offset:12288\n"
    "v_mfma_f32_16x16x32_bf16 v[172:175], a[176:179], a[144:147], v[172:175]\n"
    "buffer_load_dwordx4 v248, s[8:11], s41 offen offset:1024 sc1 lds\n"
    "ds_read_b128 a[172:175], v241 offset:12288\n"
    "v_mfma_f32_16x16x32_bf16 v[172:175], a[180:183], a[148:151], v[172:175]\n"
    "ds_read_b128 a[176:179], v242 offset:8192\n"
    "v_exp_f32_e32 v167, v167\n"
    "v_mfma_f32_16x16x32_bf16 v[172:175], a[184:187], a[152:155], v[172:175]\n"
    "ds_read_b128 a[180:183], v243 offset:8192\n"
    "v_cvt_pk_bf16_f32 v164, v164, v165\n"
    "v_mfma_f32_16x16x32_bf16 v[172:175], a[188:191], a[156:159], v[172:175]\n"
    "buffer_load_dwordx4 v248, s[8:11], s41 offen offset:2048 sc1 lds\n"
    "v_cvt_pk_bf16_f32 v165, v166, v167\n"
    "v_mfma_f32_16x16x32_bf16 v[176:179], v[180:183], a[128:131], v[236:239]\n"
    "v_exp_f32_e32 v168, v168\n"
    "v_mfma_f32_16x16x32_bf16 v[176:179], v[184:187], a[132:135], v[176:179]\n"
    "v_exp_f32_e32 v169, v169\n"
    "v_mfma_f32_16x16x32_bf16 v[176:179], v[188:191], a[136:139], v[176:179]\n"
    "buffer_load_dwordx4 v248, s[8:11], s41 offen offset:3072 sc1 lds\n"
    "v_mfma_f32_16x16x32_bf16 v[176:179], v[192:195], a[140:143], v[176:179]\n"
    "s_mov_b32 m0, s42\n"
    "s_add_u32 s41, s41, 0x8000\n"
    "v_exp_f32_e32 v172, v172\n"
    "v_mfma_f32_16x16x32_bf16 v[176:179], v[196:199], a[144:147], v[176:179]\n"
    "ds_write_addtid_b32 v156 offset:2048\n"
    "v_exp_f32_e32 v173, v173\n"
    "v_mfma_f32_16x16x32_bf16 v[176:179], v[200:203], a[148:151], v[176:179]\n"
    "ds_write_addtid_b32 v157 offset:2304\n"
    "v_exp_f32_e32 v174, v174\n"
    "v_mfma_f32_16x16x32_bf16 v[176:179], v[204:207], a[152:155], v[176:179]\n"
    "ds_write_addtid_b32 v164 offset:3072\n"
    "v_exp_f32_e32 v175, v175\n"
    "v_mfma_f32_16x16x32_bf16 v[176:179], v[208:211], a[156:159], v[176:179]\n"
    "ds_write_addtid_b32 v165 offset:3328\n"
    "v_cvt_pk_bf16_f32 v172, v172, v173\n"
    "v_cvt_pk_bf16_f32 v173, v174, v175\n"
    "ds_write_addtid_b32 v172 offset:4096\n"
    "ds_write_addtid_b32 v173 offset:4352\n"
    "s_waitcnt lgkmcnt(10)\n"
    "s_branch .Lat_in\n"
    ".p2align 6\n"
    ".Lat_loop:\n"
    "v_mfma_f32_16x16x32_bf16 v[212:215], a[160:163], a[128:131], v[236:239]\n"
    "v_mfma_f32_16x16x32_bf16 v[212:215], a[164:167], a[132:135], v[212:215]\n"
    "v_exp_f32_e32 v170, v170\n"
    "v_mfma_f32_16x16x32_bf16 v[204:207], a[164:167], a[100:103], v[232:235]\n"
    "ds_write_addtid_b32 v143 offset:768\n"
    "v_mfma_f32_16x16x32_bf16 v[204:207], a[160:163], a[96:99], v[204:207]\n"
    "ds_read_b128 a[184:187], v242 offset:28672\n"
    "v_mfma_f32_16x16x32_bf16 v[196:199], a[160:163], a[64:67], v[228:231]\n"
    "v_exp_f32_e32 v171, v171\n"
    "v_mfma_f32_16x16x32_bf16 v[196:199], a[164:167], a[68:71], v[196:199]\n"
    "ds_write_addtid_b32 v150 offset:1536\n"
    "v_mfma_f32_16x16x32_bf16 v[188:191], a[164:167], a[36:39], v[224:227]\n"
    "v_cvt_pk_bf16_f32 v166, v168, v169\n"
    "v_mfma_f32_16x16x32_bf16 v[188:191], a[160:163], a[32:35], v[188:191]\n"
    "v_mfma_f32_16x16x32_bf16 v[180:183], a[160:163], a[0:3], v[220:223]\n"
    "ds_write_addtid_b32 v151 offset:1792\n"
    "v_mfma_f32_16x16x32_bf16 v[180:183], a[164:167], a[4:7], v[180:183]\n"
    "s_waitcnt lgkmcnt(10)\n"
    "v_exp_f32_e32 v176, v176\n"
    "v_mfma_f32_16x16x32_bf16 v[180:183], a[168:171], a[8:11], v[180:183]\n"
    "ds_read_b128 a[188:191], v243 offset:28672\n"
    "v_mfma_f32_16x16x32_bf16 v[180:183], a[172:175], a[12:15], v[180:183]\n"
    "v_exp_f32_e32 v177, v177\n"
    "v_mfma_f32_16x16x32_bf16 v[188:191], a[172:175], a[44:47], v[188:191]\n"
    "ds_write_addtid_b32 v158 offset:2560\n"
    "v_mfma_f32_16x16x32_bf16 v[188:191], a[168:171], a[40:43], v[188:191]\n"
    "ds_read_b128 a[160:163], v240 offset:26624\n"
    "v_mfma_f32_16x16x32_bf16 v[196:199], a[168:171], a[72:75], v[196:199]\n"
    "v_cvt_pk_bf16_f32 v167, v170, v171\n"
    "v_mfma_f32_16x16x32_bf16 v[196:199], a[172:175], a[76:79], v[196:199]\n"
    "ds_write_addtid_b32 v159 offset:2816\n"
    "v_mfma_f32_16x16x32_bf16 v[204:207], a[172:175], a[108:111], v[204:207]\n"
    "v_exp_f32_e32 v178, v178\n"
    "v_mfma_f32_16x16x32_bf16 v[204:207], a[168:171], a[104:107], v[204:207]\n"
    "ds_write_addtid_b32 v166 offset:3584\n"
    "v_mfma_f32_16x16x32_bf16 v[212:215], a[168:171], a[136:139], v[212:215]\n"
    "v_mfma_f32_16x16x32_bf16 v[212:215], a[172:175], a[140:143], v[212:215]\n"
    "s_waitcnt lgkmcnt(10)\n"
    "v_exp_f32_e32 v179, v179\n"
    "v_mfma_f32_16x16x32_bf16 v[212:215], a[176:179], a[144:147], v[212:215]\n"
    "ds_read_b128 a[164:167], v241 offset:26624\n"
    "v_mfma_f32_16x16x32_bf16 v[212:215], a[180:183], a[148:151], v[212:215]\n"
    "v_cvt_pk_bf16_f32 v174, v176, v177\n"
    "v_mfma_f32_16x16x32_bf16 v[204:207], a[180:183], a[116:119], v[204:207]\n"
    "ds_write_addtid_b32 v167 offset:3840\n"
    "v_mfma_f32_16x16x32_bf16 v[204:207], a[176:179], a[112:115], v[204:207]\n"
    "ds_read_b128 a[168:171], v240 offset:30720\n"
    "v_mfma_f32_16x16x32_bf16 v[196:199], a[176:179], a[80:83], v[196:199]\n"
    "v_cvt_pk_bf16_f32 v175, v178, v179\n"
    "v_mfma_f32_16x16x32_bf16 v[196:199], a[180:183], a[84:87], v[196:199]\n"
    "ds_write_addtid_b32 v174 offset:4608\n"
    "v_mfma_f32_16x16x32_bf16 v[188:191], a[180:183], a[52:55], v[188:191]\n"
    "ds_write_addtid_b32 v175 offset:4864\n"
    "v_mfma_f32_16x16x32_bf16 v[188:191], a[176:179], a[48:51], v[188:191]\n"
    "v_mfma_f32_16x16x32_bf16 v[180:183], a[176:179], a[16:19], v[180:183]\n"
    "ds_read_b128 a[172:175], v241 offset:30720\n"
    "v_mfma_f32_16x16x32_bf16 v[180:183], a[180:183], a[20:23], v[180:183]\n"
    "s_waitcnt lgkmcnt(10)\n"
    "s_waitcnt lgkmcnt(1)\n"
    "v_mfma_f32_16x16x32_bf16 v[180:183], a[184:187], a[24:27], v[180:183]\n"
    "s_barrier\n"
    "v_mfma_f32_16x16x32_bf16 v[180:183], a[188:191], a[28:31], v[180:183]\n"
    "s_add_u32 m0, s43, 8192\n"
    "v_mfma_f32_16x16x32_bf16 v[188:191], a[188:191], a[60:63], v[188:191]\n"
    "ds_read_b128 a[176:179], v242 offset:26624\n"
    "v_mfma_f32_16x16x32_bf16 v[188:191], a[184:187], a[56:59], v[188:191]\n"
    "buffer_load_dwordx4 v248, s[8:11], s40 offen offset:0 sc1 lds\n"
    "v_mfma_f32_16x16x32_bf16 v[196:199], a[184:187], a[88:91], v[196:199]\n"
    "ds_read2st64_b32 v[144:145], v249 offset0:0 offset1:1\n"
    "v_mfma_f32_16x16x32_bf16 v[196:199], a[188:191], a[92:95], v[196:199]\n"
    "v_mfma_f32_16x16x32_bf16 v[204:207], a[188:191], a[124:127], v[204:207]\n"
    "ds_read2st64_b32 v[146:147], v249 offset0:2 offset1:3\n"
    "v_mfma_f32_16x16x32_bf16 v[204:207], a[184:187], a[120:123], v[204:207]\n"
    "buffer_load_dwordx4 v248, s[8:11], s40 offen offset:1024 sc1 lds\n"
    "v_mfma_f32_16x16x32_bf16 v[212:215], a[184:187], a[152:155], v[212:215]\n"
    "v_mfma_f32_16x16x32_bf16 v[212:215], a[188:191], a[156:159], v[212:215]\n"
    "v_mfma_f32_16x16x32_bf16 v[216:219], a[160:163], a[128:131], v[236:239]\n"
    "ds_read_b128 a[180:183], v243 offset:26624\n"
    "v_mfma_f32_16x16x32_bf16 v[216:219], a[164:167], a[132:135], v[216:219]\n"
    "buffer_load_dwordx4 v248, s[8:11], s40 offen offset:2048 sc1 lds\n"
    "v_mfma_f32_16x16x32_bf16 v[208:211], a[164:167], a[100:103], v[232:235]\n"
    "ds_read2st64_b32 v[152:153], v249 offset0:4 offset1:5\n"
    "v_mfma_f32_16x16x32_bf16 v[208:211], a[160:163], a[96:99], v[208:211]\n"
    "ds_read_b128 a[184:187], v242 offset:30720\n"
    "v_mfma_f32_16x16x32_bf16 v[200:203], a[160:163], a[64:67], v[228:231]\n"
    "ds_read2st64_b32 v[154:155], v249 offset0:6 offset1:7\n"
    "v_mfma_f32_16x16x32_bf16 v[200:203], a[164:167], a[68:71], v[200:203]\n"
    "buffer_load_dwordx4 v248, s[8:11], s40 offen offset:3072 sc1 lds\n"
    "v_mfma_f32_16x16x32_bf16 v[192:195], a[164:167], a[36:39], v[224:227]\n"
    "s_add_u32 m0, s43, 12288\n"
    "v_exp_f32_e32 v180, v180\n"
    "v_mfma_f32_16x16x32_bf16 v[192:195], a[160:163], a[32:35], v[192:195]\n"
    "s_add_u32 s40, s40, 0x8000\n"
    "v_exp_f32_e32 v181, v181\n"
    "v_mfma_f32_16x16x32_bf16 v[184:187], a[160:163], a[0:3], v[220:223]\n"
    "ds_read2st64_b32 v[160:161], v249 offset0:8 offset1:9\n"
    "v_mfma_f32_16x16x32_bf16 v[184:187], a[164:167], a[4:7], v[184:187]\n"
    "s_waitcnt lgkmcnt(8)\n"
    "buffer_load_dwordx4 v248, s[8:11], s41 offen offset:0 sc1 lds\n"
    "v_mfma_f32_16x16x32_bf16 v[184:187], a[168:171], a[8:11], v[184:187]\n"
    "ds_read_b128 a[188:191], v243 offset:30720\n"
    "v_mfma_f32_16x16x32_bf16 v[184:187], a[172:175], a[12:15], v[184:187]\n"
    "ds_read_b64_tr_b16 a[160:161], v244 offset:16384\n"
    "v_mfma_f32_16x16x32_bf16 v[192:195], a[172:175], a[44:47], v[192:195]\n"
    "ds_read2st64_b32 v[162:163], v249 offset0:10 offset1:11\n"
    "v_mfma_f32_16x16x32_bf16 v[192:195], a[168:171], a[40:43], v[192:195]\n"
    "buffer_load_dwordx4 v248, s[8:11], s41 offen offset:1024 sc1 lds\n"
    "v_mfma_f32_16x16x32_bf16 v[200:203], a[168:171], a[72:75], v[200:203]\n"
    "ds_read_b64_tr_b16 a[162:163], v244 offset:18432\n"
    "v_mfma_f32_16x16x32_bf16 v[200:203], a[172:175], a[76:79], v[200:203]\n"
    "v_exp_f32_e32 v182, v182\n"
    "v_mfma_f32_16x16x32_bf16 v[208:211], a[172:175], a[108:111], v[208:211]\n"
    "ds_read2st64_b32 v[168:169], v249 offset0:12 offset1:13\n"
    "v_mfma_f32_16x16x32_bf16 v[208:211], a[168:171], a[104:107], v[208:211]\n"
    "buffer_load_dwordx4 v248, s[8:11], s41 offen offset:2048 sc1 lds\n"
    "v_mfma_f32_16x16x32_bf16 v[216:219], a[168:171], a[136:139], v[216:219]\n"
    "ds_read_b64_tr_b16 a[164:165], v244 offset:49152\n"
    "v_mfma_f32_16x16x32_bf16 v[216:219], a[172:175], a[140:143], v[216:219]\n"
    "s_waitcnt lgkmcnt(10)\n"
    "ds_read2st64_b32 v[170:171], v249 offset0:14 offset1:15\n"
    "v_mfma_f32_16x16x32_bf16 v[216:219], a[176:179], a[144:147], v[216:219]\n"
    "ds_read_b64_tr_b16 a[168:169], v245 offset:16384\n"
    "v_mfma_f32_16x16x32_bf16 v[216:219], a[180:183], a[148:151], v[216:219]\n"
    "buffer_load_dwordx4 v248, s[8:11], s41 offen offset:3072 sc1 lds\n"
    "v_mfma_f32_16x16x32_bf16 v[208:211], a[180:183], a[116:119], v[208:211]\n"
    "ds_read_b64_tr_b16 a[166:167], v244 offset:51200\n"
    "v_mfma_f32_16x16x32_bf16 v[208:211], a[176:179], a[112:115], v[208:211]\n"
    "s_add_u32 s41, s41, 0x8000\n"
    "v_exp_f32_e32 v183, v183\n"
    "v_mfma_f32_16x16x32_bf16 v[200:203], a[176:179], a[80:83], v[200:203]\n"
    "ds_read_b64_tr_b16 a[170:171], v245 offset:18432\n"
    "v_cvt_pk_bf16_f32 v180, v180, v181\n"
    "v_mfma_f32_16x16x32_bf16 v[200:203], a[180:183], a[84:87], v[200:203]\n"
    "ds_read2st64_b32 v[176:177], v249 offset0:16 offset1:17\n"
    "v_mfma_f32_16x16x32_bf16 v[192:195], a[180:183], a[52:55], v[192:195]\n"
    "v_exp_f32_e32 v188, v188\n"
    "v_mfma_f32_16x16x32_bf16 v[192:195], a[176:179], a[48:51], v[192:195]\n"
    "v_exp_f32_e32 v189, v189\n"
    "v_mfma_f32_16x16x32_bf16 v[184:187], a[176:179], a[16:19], v[184:187]\n"
    "ds_read_b64_tr_b16 a[172:173], v245 offset:49152\n"
    "v_cvt_pk_bf16_f32 v181, v182, v183\n"
    "v_mfma_f32_16x16x32_bf16 v[184:187], a[180:183], a[20:23], v[184:187]\n"
    "s_waitcnt lgkmcnt(11)\n"
    "ds_read2st64_b32 v[178:179], v249 offset0:18 offset1:19\n"
    "v_mfma_f32_16x16x32_bf16 v[184:187], a[184:187], a[24:27], v[184:187]\n"
    "ds_read_b64_tr_b16 a[176:177], v246 offset:16384\n"
    "v_mfma_f32_16x16x32_bf16 v[184:187], a[188:191], a[28:31], v[184:187]\n"
    "ds_read_b64_tr_b16 a[174:175], v245 offset:51200\n"
    "v_mfma_f32_16x16x32_bf16 v[192:195], a[188:191], a[60:63], v[192:195]\n"
    "v_exp_f32_e32 v190, v190\n"
    "v_mfma_f32_16x16x32_bf16 v[192:195], a[184:187], a[56:59], v[192:195]\n"
    "v_exp_f32_e32 v191, v191\n"
    "v_mfma_f32_16x16x32_bf16 v[200:203], a[184:187], a[88:91], v[200:203]\n"
    "v_mfma_f32_16x16x32_bf16 v[200:203], a[188:191], a[92:95], v[200:203]\n"
    "ds_read_b64_tr_b16 a[178:179], v246 offset:18432\n"
    "v_mfma_f32_16x16x32_bf16 v[208:211], a[188:191], a[124:127], v[208:211]\n"
    "v_cvt_pk_bf16_f32 v188, v188, v189\n"
    "v_mfma_f32_16x16x32_bf16 v[208:211], a[184:187], a[120:123], v[208:211]\n"
    "v_exp_f32_e32 v196, v196\n"
    "v_mfma_f32_16x16x32_bf16 v[216:219], a[184:187], a[152:155], v[216:219]\n"
    "ds_read_b64_tr_b16 a[180:181], v246 offset:49152\n"
    "v_mfma_f32_16x16x32_bf16 v[216:219], a[188:191], a[156:159], v[216:219]\n"
    "s_waitcnt lgkmcnt(4)\n"
    "v_exp_f32_e32 v197, v197\n"
    "v_mfma_f32_16x16x32_bf16 a[236:239], a[160:163], v[140:143], a[236:239]\n"
    "v_mfma_f32_16x16x32_bf16 a[236:239], a[164:167], v[144:147], a[236:239]\n"
    "ds_read_b64_tr_b16 a[182:183], v246 offset:51200\n"
    "v_mfma_f32_16x16x32_bf16 a[240:243], a[164:167], v[152:155], a[240:243]\n"
    "ds_read_b64_tr_b16 a[184:185], v247 offset:16384\n"
    "v_mfma_f32_16x16x32_bf16 a[240:243], a[160:163], v[148:151], a[240:243]\n"
    "v_cvt_pk_bf16_f32 v189, v190, v191\n"
    "v_mfma_f32_16x16x32_bf16 a[244:247], a[160:163], v[156:159], a[244:247]\n"
    "ds_read_b64_tr_b16 a[186:187], v247 offset:18432\n"
    "v_mfma_f32_16x16x32_bf16 a[244:247], a[164:167], v[160:163], a[244:247]\n"
    "v_exp_f32_e32 v198, v198\n"
    "v_mfma_f32_16x16x32_bf16 a[248:251], a[164:167], v[168:171], a[248:251]\n"
    "v_exp_f32_e32 v199, v199\n"
    "v_mfma_f32_16x16x32_bf16 a[248:251], a[160:163], v[164:167], a[248:251]\n"
    "v_cvt_pk_bf16_f32 v196, v196, v197\n"
    "v_mfma_f32_16x16x32_bf16 a[252:255], a[160:163], v[172:175], a[252:255]\n"
    "ds_read_b64_tr_b16 a[188:189], v247 offset:49152\n"
    "v_mfma_f32_16x16x32_bf16 a[252:255], a[164:167], v[176:179], a[252:255]\n"
    "s_waitcnt lgkmcnt(6)\n"
    "v_exp_f32_e32 v204, v204\n"
    "v_mfma_f32_16x16x32_bf16 v[16:19], a[172:175], v[176:179], v[16:19]\n"
    "ds_read_b64_tr_b16 a[160:161], v244 offset:20480\n"
    "v_mfma_f32_16x16x32_bf16 v[16:19], a[168:171], v[172:175], v[16:19]\n"
    "ds_read_b64_tr_b16 a[190:191], v247 offset:51200\n"
    "v_mfma_f32_16x16x32_bf16 v[12:15], a[168:171], v[164:167], v[12:15]\n"
    "v_exp_f32_e32 v205, v205\n"
    "v_mfma_f32_16x16x32_bf16 v[12:15], a[172:175], v[168:171], v[12:15]\n"
    "v_cvt_pk_bf16_f32 v197, v198, v199\n"
    "v_mfma_f32_16x16x32_bf16 v[8:11], a[172:175], v[160:163], v[8:11]\n"
    "ds_read_b64_tr_b16 a[162:163], v244 offset:22528\n"
    "v_mfma_f32_16x16x32_bf16 v[8:11], a[168:171], v[156:159], v[8:11]\n"
    "v_exp_f32_e32 v206, v206\n"
    "v_mfma_f32_16x16x32_bf16 v[4:7], a[168:171], v[148:151], v[4:7]\n"
    "v_exp_f32_e32 v207, v207\n"
    "v_mfma_f32_16x16x32_bf16 v[4:7], a[172:175], v[152:155], v[4:7]\n"
    "v_cvt_pk_bf16_f32 v204, v204, v205\n"
    "v_mfma_f32_16x16x32_bf16 v[0:3], a[172:175], v[144:147], v[0:3]\n"
    "ds_read_b64_tr_b16 a[164:165], v244 offset:53248\n"
    "v_mfma_f32_16x16x32_bf16 v[0:3], a[168:171], v[140:143], v[0:3]\n"
    "s_waitcnt lgkmcnt(7)\n"
    "v_exp_f32_e32 v212, v212\n"
    "v_mfma_f32_16x16x32_bf16 v[20:23], a[176:179], v[140:143], v[20:23]\n"
    "v_mfma_f32_16x16x32_bf16 v[20:23], a[180:183], v[144:147], v[20:23]\n"
    "ds_read_b64_tr_b16 a[166:167], v244 offset:55296\n"
    "v_mfma_f32_16x16x32_bf16 v[24:27], a[180:183], v[152:155], v[24:27]\n"
    "ds_read_b64_tr_b16 a[168:169], v245 offset:20480\n"
    "v_mfma_f32_16x16x32_bf16 v[24:27], a[176:179], v[148:151], v[24:27]\n"
    "v_exp_f32_e32 v213, v213\n"
    "v_mfma_f32_16x16x32_bf16 v[28:31], a[176:179], v[156:159], v[28:31]\n"
    "ds_read_b64_tr_b16 a[170:171], v245 offset:22528\n"
    "v_mfma_f32_16x16x32_bf16 v[28:31], a[180:183], v[160:163], v[28:31]\n"
    "v_cvt_pk_bf16_f32 v205, v206, v207\n"
    "v_mfma_f32_16x16x32_bf16 v[32:35], a[180:183], v[168:171], v[32:35]\n"
    "v_exp_f32_e32 v214, v214\n"
    "v_mfma_f32_16x16x32_bf16 v[32:35], a[176:179], v[164:167], v[32:35]\n"
    "v_exp_f32_e32 v215, v215\n"
    "v_mfma_f32_16x16x32_bf16 v[36:39], a[176:179], v[172:175], v[36:39]\n"
    "ds_read_b64_tr_b16 a[172:173], v245 offset:53248\n"
    "v_mfma_f32_16x16x32_bf16 v[36:39], a[180:183], v[176:179], v[36:39]\n"
    "s_waitcnt lgkmcnt(6)\n"
    "v_cvt_pk_bf16_f32 v212, v212, v213\n"
    "v_mfma_f32_16x16x32_bf16 v[56:59], a[188:191], v[176:179], v[56:59]\n"
    "ds_read_b64_tr_b16 a[176:177], v246 offset:20480\n"
    "v_mfma_f32_16x16x32_bf16 v[56:59], a[184:187], v[172:175], v[56:59]\n"
    "ds_read_b64_tr_b16 a[174:175], v245 offset:55296\n"
    "v_mfma_f32_16x16x32_bf16 v[52:55], a[184:187], v[164:167], v[52:55]\n"
    "v_cvt_pk_bf16_f32 v213, v214, v215\n"
    "v_mfma_f32_16x16x32_bf16 v[52:55], a[188:191], v[168:171], v[52:55]\n"
    "v_exp_f32_e32 v184, v184\n"
    "v_mfma_f32_16x16x32_bf16 v[48:51], a[188:191], v[160:163], v[48:51]\n"
    "ds_read_b64_tr_b16 a[178:179], v246 offset:22528\n"
    "v_mfma_f32_16x16x32_bf16 v[48:51], a[184:187], v[156:159], v[48:51]\n"
    "v_mfma_f32_16x16x32_bf16 v[44:47], a[184:187], v[148:151], v[44:47]\n"
    "v_exp_f32_e32 v185, v185\n"
    "v_mfma_f32_16x16x32_bf16 v[44:47], a[188:191], v[152:155], v[44:47]\n"
    "v_exp_f32_e32 v186, v186\n"
    "v_mfma_f32_16x16x32_bf16 v[40:43], a[188:191], v[144:147], v[40:43]\n"
    "ds_read_b64_tr_b16 a[180:181], v246 offset:53248\n"
    "v_mfma_f32_16x16x32_bf16 v[40:43], a[184:187], v[140:143], v[40:43]\n"
    "s_waitcnt lgkmcnt(7)\n"
    "v_exp_f32_e32 v187, v187\n"
    "v_mfma_f32_16x16x32_bf16 v[60:63], a[160:163], v[140:143], v[60:63]\n"
    "ds_read_b64_tr_b16 a[184:185], v247 offset:20480\n"
    "v_mfma_f32_16x16x32_bf16 v[60:63], a[164:167], v[144:147], v[60:63]\n"
    "ds_read_b64_tr_b16 a[182:183], v246 offset:55296\n"
    "v_mfma_f32_16x16x32_bf16 v[64:67], a[164:167], v[152:155], v[64:67]\n"
    "ds_read_b64_tr_b16 a[186:187], v247 offset:22528\n"
    "v_mfma_f32_16x16x32_bf16 v[64:67], a[160:163], v[148:151], v[64:67]\n"
    "v_cvt_pk_bf16_f32 v182, v184, v185\n"
    "v_mfma_f32_16x16x32_bf16 v[68:71], a[160:163], v[156:159], v[68:71]\n"
    "v_exp_f32_e32 v192, v192\n"
    "v_mfma_f32_16x16x32_bf16 v[68:71], a[164:167], v[160:163], v[68:71]\n"
    "ds_read_b64_tr_b16 a[188:189], v247 offset:53248\n"
    "v_mfma_f32_16x16x32_bf16 v[72:75], a[164:167], v[168:171], v[72:75]\n"
    "ds_read_b64_tr_b16 a[190:191], v247 offset:55296\n"
    "v_mfma_f32_16x16x32_bf16 v[72:75], a[160:163], v[164:167], v[72:75]\n"
    "s_waitcnt vmcnt(9)\n"
    "v_mfma_f32_16x16x32_bf16 v[76:79], a[160:163], v[172:175], v[76:79]\n"
    "s_barrier\n"
    "v_mfma_f32_16x16x32_bf16 v[76:79], a[164:167], v[176:179], v[76:79]\n"
    "s_waitcnt lgkmcnt(7)\n"
    "s_mov_b32 m0, s42\n"
    "v_mfma_f32_16x16x32_bf16 v[96:99], a[172:175], v[176:179], v[96:99]\n"
    "v_exp_f32_e32 v193, v193\n"
    "v_mfma_f32_16x16x32_bf16 v[96:99], a[168:171], v[172:175], v[96:99]\n"
    "v_cvt_pk_bf16_f32 v183, v186, v187\n"
    "v_mfma_f32_16x16x32_bf16 v[92:95], a[168:171], v[164:167], v[92:95]\n"
    "v_exp_f32_e32 v194, v194\n"
    "v_mfma_f32_16x16x32_bf16 v[92:95], a[172:175], v[168:171], v[92:95]\n"
    "ds_read_b128 a[160:163], v240 offset:0\n"
    "v_mfma_f32_16x16x32_bf16 v[88:91], a[172:175], v[160:163], v[88:91]\n"
    "ds_write_addtid_b32 v180 offset:0\n"
    "v_mfma_f32_16x16x32_bf16 v[88:91], a[168:171], v[156:159], v[88:91]\n"
    "v_exp_f32_e32 v195, v195\n"
    "v_mfma_f32_16x16x32_bf16 v[84:87], a[168:171], v[148:151], v[84:87]\n"
    "ds_write_addtid_b32 v181 offset:256\n"
    "v_mfma_f32_16x16x32_bf16 v[84:87], a[172:175], v[152:155], v[84:87]\n"
    "ds_write_addtid_b32 v188 offset:1024\n"
    "v_mfma_f32_16x16x32_bf16 v[80:83], a[172:175], v[144:147], v[80:83]\n"
    "v_cvt_pk_bf16_f32 v190, v192, v193\n"
    "v_mfma_f32_16x16x32_bf16 v[80:83], a[168:171], v[140:143], v[80:83]\n"
    "s_waitcnt lgkmcnt(7)\n"
    "v_exp_f32_e32 v200, v200\n"
    "v_mfma_f32_16x16x32_bf16 v[100:103], a[176:179], v[140:143], v[100:103]\n"
    "ds_read_b128 a[164:167], v241 offset:0\n"
    "v_mfma_f32_16x16x32_bf16 v[100:103], a[180:183], v[144:147], v[100:103]\n"
    "ds_write_addtid_b32 v189 offset:1280\n"
    "v_mfma_f32_16x16x32_bf16 v[104:107], a[180:183], v[152:155], v[104:107]\n"
    "v_exp_f32_e32 v201, v201\n"
    "v_mfma_f32_16x16x32_bf16 v[104:107], a[176:179], v[148:151], v[104:107]\n"
    "ds_read_b128 a[168:171], v240 offset:4096\n"
    "v_mfma_f32_16x16x32_bf16 v[108:111], a[176:179], v[156:159], v[108:111]\n"
    "ds_write_addtid_b32 v196 offset:2048\n"
    "v_mfma_f32_16x16x32_bf16 v[108:111], a[180:183], v[160:163], v[108:111]\n"
    "v_cvt_pk_bf16_f32 v191, v194, v195\n"
    "v_mfma_f32_16x16x32_bf16 v[112:115], a[180:183], v[168:171], v[112:115]\n"
    "ds_write_addtid_b32 v197 offset:2304\n"
    "v_mfma_f32_16x16x32_bf16 v[112:115], a[176:179], v[164:167], v[112:115]\n"
    "v_exp_f32_e32 v202, v202\n"
    "v_mfma_f32_16x16x32_bf16 v[116:119], a[176:179], v[172:175], v[116:119]\n"
    "ds_write_addtid_b32 v204 offset:3072\n"
    "v_mfma_f32_16x16x32_bf16 v[116:119], a[180:183], v[176:179], v[116:119]\n"
    "s_waitcnt lgkmcnt(10)\n"
    "v_exp_f32_e32 v203, v203\n"
    "v_mfma_f32_16x16x32_bf16 v[136:139], a[188:191], v[176:179], v[136:139]\n"
    "ds_read_b128 a[172:175], v241 offset:4096\n"
    "v_mfma_f32_16x16x32_bf16 v[136:139], a[184:187], v[172:175], v[136:139]\n"
    "ds_write_addtid_b32 v205 offset:3328\n"
    "v_cvt_pk_bf16_f32 v198, v200, v201\n"
    "v_mfma_f32_16x16x32_bf16 v[132:135], a[184:187], v[164:167], v[132:135]\n"
    "ds_read_b128 a[176:179], v242 offset:0\n"
    "v_mfma_f32_16x16x32_bf16 v[132:135], a[188:191], v[168:171], v[132:135]\n"
    "ds_write_addtid_b32 v212 offset:4096\n"
    "v_mfma_f32_16x16x32_bf16 v[128:131], a[188:191], v[160:163], v[128:131]\n"
    "v_exp_f32_e32 v208, v208\n"
    "v_mfma_f32_16x16x32_bf16 v[128:131], a[184:187], v[156:159], v[128:131]\n"
    "ds_write_addtid_b32 v213 offset:4352\n"
    "v_mfma_f32_16x16x32_bf16 v[124:127], a[184:187], v[148:151], v[124:127]\n"
    "v_exp_f32_e32 v209, v209\n"
    "v_mfma_f32_16x16x32_bf16 v[124:127], a[188:191], v[152:155], v[124:127]\n"
    "ds_read_b128 a[180:183], v243 offset:0\n"
    "v_mfma_f32_16x16x32_bf16 v[120:123], a[188:191], v[144:147], v[120:123]\n"
    "ds_write_addtid_b32 v182 offset:512\n"
    "v_mfma_f32_16x16x32_bf16 v[120:123], a[184:187], v[140:143], v[120:123]\n"
    "s_waitcnt lgkmcnt(12)\n"
    "v_cvt_pk_bf16_f32 v199, v202, v203\n"
    "v_mfma_f32_16x16x32_bf16 v[172:175], a[160:163], a[128:131], v[236:239]\n"
    "v_mfma_f32_16x16x32_bf16 v[172:175], a[164:167], a[132:135], v[172:175]\n"
    "v_exp_f32_e32 v210, v210\n"
    "v_mfma_f32_16x16x32_bf16 v[164:167], a[164:167], a[100:103], v[232:235]\n"
    "ds_write_addtid_b32 v183 offset:768\n"
    "v_mfma_f32_16x16x32_bf16 v[164:167], a[160:163], a[96:99], v[164:167]\n"
    "ds_read_b128 a[184:187], v242 offset:4096\n"
    "v_mfma_f32_16x16x32_bf16 v[156:159], a[160:163], a[64:67], v[228:231]\n"
    "v_exp_f32_e32 v211, v211\n"
    "v_mfma_f32_16x16x32_bf16 v[156:159], a[164:167], a[68:71], v[156:159]\n"
    "ds_write_addtid_b32 v190 offset:1536\n"
    "v_mfma_f32_16x16x32_bf16 v[148:151], a[164:167], a[36:39], v[224:227]\n"
    "v_cvt_pk_bf16_f32 v206, v208, v209\n"
    "v_mfma_f32_16x16x32_bf16 v[148:151], a[160:163], a[32:35], v[148:151]\n"
    "v_mfma_f32_16x16x32_bf16 v[140:143], a[160:163], a[0:3], v[220:223]\n"
    "ds_write_addtid_b32 v191 offset:1792\n"
    "v_mfma_f32_16x16x32_bf16 v[140:143], a[164:167], a[4:7], v[140:143]\n"
    "s_waitcnt lgkmcnt(10)\n"
    "v_exp_f32_e32 v216, v216\n"
    "v_mfma_f32_16x16x32_bf16 v[140:143], a[168:171], a[8:11], v[140:143]\n"
    "ds_read_b128 a[188:191], v243 offset:4096\n"
    "v_mfma_f32_16x16x32_bf16 v[140:143], a[172:175], a[12:15], v[140:143]\n"
    "v_exp_f32_e32 v217, v217\n"
    "v_mfma_f32_16x16x32_bf16 v[148:151], a[172:175], a[44:47], v[148:151]\n"
    "ds_write_addtid_b32 v198 offset:2560\n"
    "v_mfma_f32_16x16x32_bf16 v[148:151], a[168:171], a[40:43], v[148:151]\n"
    "ds_read_b128 a[160:163], v240 offset:2048\n"
    "v_mfma_f32_16x16x32_bf16 v[156:159], a[168:171], a[72:75], v[156:159]\n"
    "v_cvt_pk_bf16_f32 v207, v210, v211\n"
    "v_mfma_f32_16x16x32_bf16 v[156:159], a[172:175], a[76:79], v[156:159]\n"
    "ds_write_addtid_b32 v199 offset:2816\n"
    "v_mfma_f32_16x16x32_bf16 v[164:167], a[172:175], a[108:111], v[164:167]\n"
    "v_exp_f32_e32 v218, v218\n"
    "v_mfma_f32_16x16x32_bf16 v[164:167], a[168:171], a[104:107], v[164:167]\n"
    "ds_write_addtid_b32 v206 offset:3584\n"
    "v_mfma_f32_16x16x32_bf16 v[172:175], a[168:171], a[136:139], v[172:175]\n"
    "v_mfma_f32_16x16x32_bf16 v[172:175], a[172:175], a[140:143], v[172:175]\n"
    "s_waitcnt lgkmcnt(10)\n"
    "v_exp_f32_e32 v219, v219\n"
    "v_mfma_f32_16x16x32_bf16 v[172:175], a[176:179], a[144:147], v[172:175]\n"
    "ds_read_b128 a[164:167], v241 offset:2048\n"
    "v_mfma_f32_16x16x32_bf16 v[172:175], a[180:183], a[148:151], v[172:175]\n"
    "v_cvt_pk_bf16_f32 v214, v216, v217\n"
    "v_mfma_f32_16x16x32_bf16 v[164:167], a[180:183], a[116:119], v[164:167]\n"
    "ds_write_addtid_b32 v207 offset:3840\n"
    "v_mfma_f32_16x16x32_bf16 v[164:167], a[176:179], a[112:115], v[164:167]\n"
    "ds_read_b128 a[168:171], v240 offset:6144\n"
    "v_mfma_f32_16x16x32_bf16 v[156:159], a[176:179], a[80:83], v[156:159]\n"
    "v_cvt_pk_bf16_f32 v215, v218, v219\n"
    "v_mfma_f32_16x16x32_bf16 v[156:159], a[180:183], a[84:87], v[156:159]\n"
    "ds_write_addtid_b32 v214 offset:4608\n"
    "v_mfma_f32_16x16x32_bf16 v[148:151], a[180:183], a[52:55], v[148:151]\n"
    "ds_write_addtid_b32 v215 offset:4864\n"
    "v_mfma_f32_16x16x32_bf16 v[148:151], a[176:179], a[48:51], v[148:151]\n"
    "v_mfma_f32_16x16x32_bf16 v[140:143], a[176:179], a[16:19], v[140:143]\n"
    "ds_read_b128 a[172:175], v241 offset:6144\n"
    "v_mfma_f32_16x16x32_bf16 v[140:143], a[180:183], a[20:23], v[140:143]\n"
    "s_waitcnt lgkmcnt(10)\n"
    "s_waitcnt lgkmcnt(1)\n"
    "v_mfma_f32_16x16x32_bf16 v[140:143], a[184:187], a[24:27], v[140:143]\n"
    "s_barrier\n"
    "v_mfma_f32_16x16x32_bf16 v[140:143], a[188:191], a[28:31], v[140:143]\n"
    "s_add_u32 m0, s43, 16384\n"
    "v_mfma_f32_16x16x32_bf16 v[148:151], a[188:191], a[60:63], v[148:151]\n"
    "ds_read_b128 a[176:179], v242 offset:2048\n"
    "v_mfma_f32_16x16x32_bf16 v[148:151], a[184:187], a[56:59], v[148:151]\n"
    "buffer_load_dwordx4 v248, s[8:11], s40 offen offset:0 sc1 lds\n"
    "v_mfma_f32_16x16x32_bf16 v[156:159], a[184:187], a[88:91], v[156:159]\n"
    "ds_read2st64_b32 v[184:185], v249 offset0:0 offset1:1\n"
    "v_mfma_f32_16x16x32_bf16 v[156:159], a[188:191], a[92:95], v[156:159]\n"
    "v_mfma_f32_16x16x32_bf16 v[164:167], a[188:191], a[124:127], v[164:167]\n"
    "ds_read2st64_b32 v[186:187], v249 offset0:2 offset1:3\n"
    "v_mfma_f32_16x16x32_bf16 v[164:167], a[184:187], a[120:123], v[164:167]\n"
    "buffer_load_dwordx4 v248, s[8:11], s40 offen offset:1024 sc1 lds\n"
    "v_mfma_f32_16x16x32_bf16 v[172:175], a[184:187], a[152:155], v[172:175]\n"
    "v_mfma_f32_16x16x32_bf16 v[172:175], a[188:191], a[156:159], v[172:175]\n"
    "v_mfma_f32_16x16x32_bf16 v[176:179], a[160:163], a[128:131], v[236:239]\n"
    "ds_read_b128 a[180:183], v243 offset:2048\n"
    "v_mfma_f32_16x16x32_bf16 v[176:179], a[164:167], a[132:135], v[176:179]\n"
    "buffer_load_dwordx4 v248, s[8:11], s40 offen offset:2048 sc1 lds\n"
    "v_mfma_f32_16x16x32_bf16 v[168:171], a[164:167], a[100:103], v[232:235]\n"
    "ds_read2st64_b32 v[192:193], v249 offset0:4 offset1:5\n"
    "v_mfma_f32_16x16x32_bf16 v[168:171], a[160:163], a[96:99], v[168:171]\n"
    "ds_read_b128 a[184:187], v242 offset:6144\n"
    "v_mfma_f32_16x16x32_bf16 v[160:163], a[160:163], a[64:67], v[228:231]\n"
    "ds_read2st64_b32 v[194:195], v249 offset0:6 offset1:7\n"
    "v_mfma_f32_16x16x32_bf16 v[160:163], a[164:167], a[68:71], v[160:163]\n"
    "buffer_load_dwordx4 v248, s[8:11], s40 offen offset:3072 sc1 lds\n"
    "v_mfma_f32_16x16x32_bf16 v[152:155], a[164:167], a[36:39], v[224:227]\n"
    "s_add_u32 m0, s43, 20480\n"
    "v_exp_f32_e32 v140, v140\n"
    "v_mfma_f32_16x16x32_bf16 v[152:155], a[160:163], a[32:35], v[152:155]\n"
    "s_add_u32 s40, s40, 0x8000\n"
    "v_exp_f32_e32 v141, v141\n"
    "v_mfma_f32_16x16x32_bf16 v[144:147], a[160:163], a[0:3], v[220:223]\n"
    "ds_read2st64_b32 v[200:201], v249 offset0:8 offset1:9\n"
    "v_mfma_f32_16x16x32_bf16 v[144:147], a[164:167], a[4:7], v[144:147]\n"
    "s_waitcnt lgkmcnt(8)\n"
    "buffer_load_dwordx4 v248, s[8:11], s41 offen offset:0 sc1 lds\n"
    "v_mfma_f32_16x16x32_bf16 v[144:147], a[168:171], a[8:11], v[144:147]\n"
    "ds_read_b128 a[188:191], v243 offset:6144\n"
    "v_mfma_f32_16x16x32_bf16 v[144:147], a[172:175], a[12:15], v[144:147]\n"
    "ds_read_b64_tr_b16 a[160:161], v244 offset:24576\n"
    "v_mfma_f32_16x16x32_bf16 v[152:155], a[172:175], a[44:47], v[152:155]\n"
    "ds_read2st64_b32 v[202:203], v249 offset0:10 offset1:11\n"
    "v_mfma_f32_16x16x32_bf16 v[152:155], a[168:171], a[40:43], v[152:155]\n"
    "buffer_load_dwordx4 v248, s[8:11], s41 offen offset:1024 sc1 lds\n"
    "v_mfma_f32_16x16x32_bf16 v[160:163], a[168:171], a[72:75], v[160:163]\n"
    "ds_read_b64_tr_b16 a[162:163], v244 offset:26624\n"
    "v_mfma_f32_16x16x32_bf16 v[160:163], a[172:175], a[76:79], v[160:163]\n"
    "v_exp_f32_e32 v142, v142\n"
    "v_mfma_f32_16x16x32_bf16 v[168:171], a[172:175], a[108:111], v[168:171]\n"
    "ds_read2st64_b32 v[208:209], v249 offset0:12 offset1:13\n"
    "v_mfma_f32_16x16x32_bf16 v[168:171], a[168:171], a[104:107], v[168:171]\n"
    "buffer_load_dwordx4 v248, s[8:11], s41 offen offset:2048 sc1 lds\n"
    "v_mfma_f32_16x16x32_bf16 v[176:179], a[168:171], a[136:139], v[176:179]\n"
    "ds_read_b64_tr_b16 a[164:165], v244 offset:57344\n"
    "v_mfma_f32_16x16x32_bf16 v[176:179], a[172:175], a[140:143], v[176:179]\n"
    "s_waitcnt lgkmcnt(10)\n"
    "ds_read2st64_b32 v[210:211], v249 offset0:14 offset1:15\n"
    "v_mfma_f32_16x16x32_bf16 v[176:179], a[176:179], a[144:147], v[176:179]\n"
    "ds_read_b64_tr_b16 a[168:169], v245 offset:24576\n"
    "v_mfma_f32_16x16x32_bf16 v[176:179], a[180:183], a[148:151], v[176:179]\n"
    "buffer_load_dwordx4 v248, s[8:11], s41 offen offset:3072 sc1 lds\n"
    "v_mfma_f32_16x16x32_bf16 v[168:171], a[180:183], a[116:119], v[168:171]\n"
    "ds_read_b64_tr_b16 a[166:167], v244 offset:59392\n"
    "v_mfma_f32_16x16x32_bf16 v[168:171], a[176:179], a[112:115], v[168:171]\n"
    "s_add_u32 s41, s41, 0x8000\n"
    "v_exp_f32_e32 v143, v143\n"
    "v_mfma_f32_16x16x32_bf16 v[160:163], a[176:179], a[80:83], v[160:163]\n"
    "ds_read_b64_tr_b16 a[170:171], v245 offset:26624\n"
    "v_cvt_pk_bf16_f32 v140, v140, v141\n"
    "v_mfma_f32_16x16x32_bf16 v[160:163], a[180:183], a[84:87], v[160:163]\n"
    "ds_read2st64_b32 v[216:217], v249 offset0:16 offset1:17\n"
    "v_mfma_f32_16x16x32_bf16 v[152:155], a[180:183], a[52:55], v[152:155]\n"
    "v_exp_f32_e32 v148, v148\n"
    "v_mfma_f32_16x16x32_bf16 v[152:155], a[176:179], a[48:51], v[152:155]\n"
    "v_exp_f32_e32 v149, v149\n"
    "v_mfma_f32_16x16x32_bf16 v[144:147], a[176:179], a[16:19], v[144:147]\n"
    "ds_read_b64_tr_b16 a[172:173], v245 offset:57344\n"
    "v_cvt_pk_bf16_f32 v141, v142, v143\n"
    "v_mfma_f32_16x16x32_bf16 v[144:147], a[180:183], a[20:23], v[144:147]\n"
    "s_waitcnt lgkmcnt(11)\n"
    "ds_read2st64_b32 v[218:219], v249 offset0:18 offset1:19\n"
    "v_mfma_f32_16x16x32_bf16 v[144:147], a[184:187], a[24:27], v[144:147]\n"
    "ds_read_b64_tr_b16 a[176:177], v246 offset:24576\n"
    "v_mfma_f32_16x16x32_bf16 v[144:147], a[188:191], a[28:31], v[144:147]\n"
    "ds_read_b64_tr_b16 a[174:175], v245 offset:59392\n"
    "v_mfma_f32_16x16x32_bf16 v[152:155], a[188:191], a[60:63], v[152:155]\n"
    "v_exp_f32_e32 v150, v150\n"
    "v_mfma_f32_16x16x32_bf16 v[152:155], a[184:187], a[56:59], v[152:155]\n"
    "v_exp_f32_e32 v151, v151\n"
    "v_mfma_f32_16x16x32_bf16 v[160:163], a[184:187], a[88:91], v[160:163]\n"
    "v_mfma_f32_16x16x32_bf16 v[160:163], a[188:191], a[92:95], v[160:163]\n"
    "ds_read_b64_tr_b16 a[178:179], v246 offset:26624\n"
    "v_mfma_f32_16x16x32_bf16 v[168:171], a[188:191], a[124:127], v[168:171]\n"
    "v_cvt_pk_bf16_f32 v148, v148, v149\n"
    "v_mfma_f32_16x16x32_bf16 v[168:171], a[184:187], a[120:123], v[168:171]\n"
    "v_exp_f32_e32 v156, v156\n"
    "v_mfma_f32_16x16x32_bf16 v[176:179], a[184:187], a[152:155], v[176:179]\n"
    "ds_read_b64_tr_b16 a[180:181], v246 offset:57344\n"
    "v_mfma_f32_16x16x32_bf16 v[176:179], a[188:191], a[156:159], v[176:179]\n"
    "s_waitcnt lgkmcnt(4)\n"
    "v_exp_f32_e32 v157, v157\n"
    "v_mfma_f32_16x16x32_bf16 a[236:239], a[160:163], v[180:183], a[236:239]\n"
    "v_mfma_f32_16x16x32_bf16 a[236:239], a[164:167], v[184:187], a[236:239]\n"
    "ds_read_b64_tr_b16 a[182:183], v246 offset:59392\n"
    "v_mfma_f32_16x16x32_bf16 a[240:243], a[164:167], v[192:195], a[240:243]\n"
    "ds_read_b64_tr_b16 a[184:185], v247 offset:24576\n"
    "v_mfma_f32_16x16x32_bf16 a[240:243], a[160:163], v[188:191], a[240:243]\n"
    "v_cvt_pk_bf16_f32 v149, v150, v151\n"
    "v_mfma_f32_16x16x32_bf16 a[244:247], a[160:163], v[196:199], a[244:247]\n"
    "ds_read_b64_tr_b16 a[186:187], v247 offset:26624\n"
    "v_mfma_f32_16x16x32_bf16 a[244:247], a[164:167], v[200:203], a[244:247]\n"
    "v_exp_f32_e32 v158, v158\n"
    "v_mfma_f32_16x16x32_bf16 a[248:251], a[164:167], v[208:211], a[248:251]\n"
    "v_exp_f32_e32 v159, v159\n"
    "v_mfma_f32_16x16x32_bf16 a[248:251], a[160:163], v[204:207], a[248:251]\n"
    "v_cvt_pk_bf16_f32 v156, v156, v157\n"
    "v_mfma_f32_16x16x32_bf16 a[252:255], a[160:163], v[212:215], a[252:255]\n"
    "ds_read_b64_tr_b16 a[188:189], v247 offset:57344\n"
    "v_mfma_f32_16x16x32_bf16 a[252:255], a[164:167], v[216:219], a[252:255]\n"
    "s_waitcnt lgkmcnt(6)\n"
    "v_exp_f32_e32 v164, v164\n"
    "v_mfma_f32_16x16x32_bf16 v[16:19], a[172:175], v[216:219], v[16:19]\n"
    "ds_read_b64_tr_b16 a[160:161], v244 offset:28672\n"
    "v_mfma_f32_16x16x32_bf16 v[16:19], a[168:171], v[212:215], v[16:19]\n"
    "ds_read_b64_tr_b16 a[190:191], v247 offset:59392\n"
    "v_mfma_f32_16x16x32_bf16 v[12:15], a[168:171], v[204:207], v[12:15]\n"
    "v_exp_f32_e32 v165, v165\n"
    "v_mfma_f32_16x16x32_bf16 v[12:15], a[172:175], v[208:211], v[12:15]\n"
    "v_cvt_pk_bf16_f32 v157, v158, v159\n"
    "v_mfma_f32_16x16x32_bf16 v[8:11], a[172:175], v[200:203], v[8:11]\n"
    "ds_read_b64_tr_b16 a[162:163], v244 offset:30720\n"
    "v_mfma_f32_16x16x32_bf16 v[8:11], a[168:171], v[196:199], v[8:11]\n"
    "v_exp_f32_e32 v166, v166\n"
    "v_mfma_f32_16x16x32_bf16 v[4:7], a[168:171], v[188:191], v[4:7]\n"
    "v_exp_f32_e32 v167, v167\n"
    "v_mfma_f32_16x16x32_bf16 v[4:7], a[172:175], v[192:195], v[4:7]\n"
    "v_cvt_pk_bf16_f32 v164, v164, v165\n"
    "v_mfma_f32_16x16x32_bf16 v[0:3], a[172:175], v[184:187], v[0:3]\n"
    "ds_read_b64_tr_b16 a[164:165], v244 offset:61440\n"
    "v_mfma_f32_16x16x32_bf16 v[0:3], a[168:171], v[180:183], v[0:3]\n"
    "s_waitcnt lgkmcnt(7)\n"
    "v_exp_f32_e32 v172, v172\n"
    "v_mfma_f32_16x16x32_bf16 v[20:23], a[176:179], v[180:183], v[20:23]\n"
    "v_mfma_f32_16x16x32_bf16 v[20:23], a[180:183], v[184:187], v[20:23]\n"
    "ds_read_b64_tr_b16 a[166:167], v244 offset:63488\n"
    "v_mfma_f32_16x16x32_bf16 v[24:27], a[180:183], v[192:195], v[24:27]\n"
    "ds_read_b64_tr_b16 a[168:169], v245 offset:28672\n"
    "v_mfma_f32_16x16x32_bf16 v[24:27], a[176:179], v[188:191], v[24:27]\n"
    "v_exp_f32_e32 v173, v173\n"
    "v_mfma_f32_16x16x32_bf16 v[28:31], a[176:179], v[196:199], v[28:31]\n"
    "ds_read_b64_tr_b16 a[170:171], v245 offset:30720\n"
    "v_mfma_f32_16x16x32_bf16 v[28:31], a[180:183], v[200:203], v[28:31]\n"
    "v_cvt_pk_bf16_f32 v165, v166, v167\n"
    "v_mfma_f32_16x16x32_bf16 v[32:35], a[180:183], v[208:211], v[32:35]\n"
    "v_exp_f32_e32 v174, v174\n"
    "v_mfma_f32_16x16x32_bf16 v[32:35], a[176:179], v[204:207], v[32:35]\n"
    "v_exp_f32_e32 v175, v175\n"
    "v_mfma_f32_16x16x32_bf16 v[36:39], a[176:179], v[212:215], v[36:39]\n"
    "ds_read_b64_tr_b16 a[172:173], v245 offset:61440\n"
    "v_mfma_f32_16x16x32_bf16 v[36:39], a[180:183], v[216:219], v[36:39]\n"
    "s_waitcnt lgkmcnt(6)\n"
    "v_cvt_pk_bf16_f32 v172, v172, v173\n"
    "v_mfma_f32_16x16x32_bf16 v[56:59], a[188:191], v[216:219], v[56:59]\n"
    "ds_read_b64_tr_b16 a[176:177], v246 offset:28672\n"
    "v_mfma_f32_16x16x32_bf16 v[56:59], a[184:187], v[212:215], v[56:59]\n"
    "ds_read_b64_tr_b16 a[174:175], v245 offset:63488\n"
    "v_mfma_f32_16x16x32_bf16 v[52:55], a[184:187], v[204:207], v[52:55]\n"
    "v_cvt_pk_bf16_f32 v173, v174, v175\n"
    "v_mfma_f32_16x16x32_bf16 v[52:55], a[188:191], v[208:211], v[52:55]\n"
    "v_exp_f32_e32 v144, v144\n"
    "v_mfma_f32_16x16x32_bf16 v[48:51], a[188:191], v[200:203], v[48:51]\n"
    "ds_read_b64_tr_b16 a[178:179], v246 offset:30720\n"
    "v_mfma_f32_16x16x32_bf16 v[48:51], a[184:187], v[196:199], v[48:51]\n"
    "v_mfma_f32_16x16x32_bf16 v[44:47], a[184:187], v[188:191], v[44:47]\n"
    "v_exp_f32_e32 v145, v145\n"
    "v_mfma_f32_16x16x32_bf16 v[44:47], a[188:191], v[192:195], v[44:47]\n"
    "v_exp_f32_e32 v146, v146\n"
    "v_mfma_f32_16x16x32_bf16 v[40:43], a[188:191], v[184:187], v[40:43]\n"
    "ds_read_b64_tr_b16 a[180:181], v246 offset:61440\n"
    "v_mfma_f32_16x16x32_bf16 v[40:43], a[184:187], v[180:183], v[40:43]\n"
    "s_waitcnt lgkmcnt(7)\n"
    "v_exp_f32_e32 v147, v147\n"
    "v_mfma_f32_16x16x32_bf16 v[60:63], a[160:163], v[180:183], v[60:63]\n"
    "ds_read_b64_tr_b16 a[184:185], v247 offset:28672\n"
    "v_mfma_f32_16x16x32_bf16 v[60:63], a[164:167], v[184:187], v[60:63]\n"
    "ds_read_b64_tr_b16 a[182:183], v246 offset:63488\n"
    "v_mfma_f32_16x16x32_bf16 v[64:67], a[164:167], v[192:195], v[64:67]\n"
    "ds_read_b64_tr_b16 a[186:187], v247 offset:30720\n"
    "v_mfma_f32_16x16x32_bf16 v[64:67], a[160:163], v[188:191], v[64:67]\n"
    "v_cvt_pk_bf16_f32 v142, v144, v145\n"
    "v_mfma_f32_16x16x32_bf16 v[68:71], a[160:163], v[196:199], v[68:71]\n"
    "v_exp_f32_e32 v152, v152\n"
    "v_mfma_f32_16x16x32_bf16 v[68:71], a[164:167], v[200:203], v[68:71]\n"
    "ds_read_b64_tr_b16 a[188:189], v247 offset:61440\n"
    "v_mfma_f32_16x16x32_bf16 v[72:75], a[164:167], v[208:211], v[72:75]\n"
    "ds_read_b64_tr_b16 a[190:191], v247 offset:63488\n"
    "v_mfma_f32_16x16x32_bf16 v[72:75], a[160:163], v[204:207], v[72:75]\n"
    "s_waitcnt vmcnt(8)\n"
    "v_mfma_f32_16x16x32_bf16 v[76:79], a[160:163], v[212:215], v[76:79]\n"
    "s_barrier\n"
    "v_mfma_f32_16x16x32_bf16 v[76:79], a[164:167], v[216:219], v[76:79]\n"
    "s_waitcnt lgkmcnt(7)\n"
    "s_mov_b32 m0, s42\n"
    "v_mfma_f32_16x16x32_bf16 v[96:99], a[172:175], v[216:219], v[96:99]\n"
    "v_exp_f32_e32 v153, v153\n"
    "v_mfma_f32_16x16x32_bf16 v[96:99], a[168:171], v[212:215], v[96:99]\n"
    "v_cvt_pk_bf16_f32 v143, v146, v147\n"
    "v_mfma_f32_16x16x32_bf16 v[92:95], a[168:171], v[204:207], v[92:95]\n"
    "v_exp_f32_e32 v154, v154\n"
    "v_mfma_f32_16x16x32_bf16 v[92:95], a[172:175], v[208:211], v[92:95]\n"
    "ds_read_b128 a[160:163], v240 offset:8192\n"
    "v_mfma_f32_16x16x32_bf16 v[88:91], a[172:175], v[200:203], v[88:91]\n"
    "ds_write_addtid_b32 v140 offset:0\n"
    "v_mfma_f32_16x16x32_bf16 v[88:91], a[168:171], v[196:199], v[88:91]\n"
    "v_exp_f32_e32 v155, v155\n"
    "v_mfma_f32_16x16x32_bf16 v[84:87], a[168:171], v[188:191], v[84:87]\n"
    "ds_write_addtid_b32 v141 offset:256\n"
    "v_mfma_f32_16x16x32_bf16 v[84:87], a[172:175], v[192:195], v[84:87]\n"
    "ds_write_addtid_b32 v148 offset:1024\n"
    "v_mfma_f32_16x16x32_bf16 v[80:83], a[172:175], v[184:187], v[80:83]\n"
    "v_cvt_pk_bf16_f32 v150, v152, v153\n"
    "v_mfma_f32_16x16x32_bf16 v[80:83], a[168:171], v[180:183], v[80:83]\n"
    "s_waitcnt lgkmcnt(7)\n"
    "v_exp_f32_e32 v160, v160\n"
    "v_mfma_f32_16x16x32_bf16 v[100:103], a[176:179], v[180:183], v[100:103]\n"
    "ds_read_b128 a[164:167], v241 offset:8192\n"
    "v_mfma_f32_16x16x32_bf16 v[100:103], a[180:183], v[184:187], v[100:103]\n"
    "ds_write_addtid_b32 v149 offset:1280\n"
    "v_mfma_f32_16x16x32_bf16 v[104:107], a[180:183], v[192:195], v[104:107]\n"
    "v_exp_f32_e32 v161, v161\n"
    "v_mfma_f32_16x16x32_bf16 v[104:107], a[176:179], v[188:191], v[104:107]\n"
    "ds_read_b128 a[168:171], v240 offset:12288\n"
    "v_mfma_f32_16x16x32_bf16 v[108:111], a[176:179], v[196:199], v[108:111]\n"
    "ds_write_addtid_b32 v156 offset:2048\n"
    "v_mfma_f32_16x16x32_bf16 v[108:111], a[180:183], v[200:203], v[108:111]\n"
    "v_cvt_pk_bf16_f32 v151, v154, v155\n"
    "v_mfma_f32_16x16x32_bf16 v[112:115], a[180:183], v[208:211], v[112:115]\n"
    "ds_write_addtid_b32 v157 offset:2304\n"
    "v_mfma_f32_16x16x32_bf16 v[112:115], a[176:179], v[204:207], v[112:115]\n"
    "v_exp_f32_e32 v162, v162\n"
    "v_mfma_f32_16x16x32_bf16 v[116:119], a[176:179], v[212:215], v[116:119]\n"
    "ds_write_addtid_b32 v164 offset:3072\n"
    "v_mfma_f32_16x16x32_bf16 v[116:119], a[180:183], v[216:219], v[116:119]\n"
    "s_waitcnt lgkmcnt(10)\n"
    "v_exp_f32_e32 v163, v163\n"
    "v_mfma_f32_16x16x32_bf16 v[136:139], a[188:191], v[216:219], v[136:139]\n"
    "ds_read_b128 a[172:175], v241 offset:12288\n"
    "v_mfma_f32_16x16x32_bf16 v[136:139], a[184:187], v[212:215], v[136:139]\n"
    "ds_write_addtid_b32 v165 offset:3328\n"
    "v_cvt_pk_bf16_f32 v158, v160, v161\n"
    "v_mfma_f32_16x16x32_bf16 v[132:135], a[184:187], v[204:207], v[132:135]\n"
    "ds_read_b128 a[176:179], v242 offset:8192\n"
    "v_mfma_f32_16x16x32_bf16 v[132:135], a[188:191], v[208:211], v[132:135]\n"
    "ds_write_addtid_b32 v172 offset:4096\n"
    "v_mfma_f32_16x16x32_bf16 v[128:131], a[188:191], v[200:203], v[128:131]\n"
    "v_exp_f32_e32 v168, v168\n"
    "v_mfma_f32_16x16x32_bf16 v[128:131], a[184:187], v[196:199], v[128:131]\n"
    "ds_write_addtid_b32 v173 offset:4352\n"
    "v_mfma_f32_16x16x32_bf16 v[124:127], a[184:187], v[188:191], v[124:127]\n"
    "v_exp_f32_e32 v169, v169\n"
    "v_mfma_f32_16x16x32_bf16 v[124:127], a[188:191], v[192:195], v[124:127]\n"
    "ds_read_b128 a[180:183], v243 offset:8192\n"
    "v_mfma_f32_16x16x32_bf16 v[120:123], a[188:191], v[184:187], v[120:123]\n"
    "ds_write_addtid_b32 v142 offset:512\n"
    "v_mfma_f32_16x16x32_bf16 v[120:123], a[184:187], v[180:183], v[120:123]\n"
    "s_waitcnt lgkmcnt(12)\n"
    "v_cvt_pk_bf16_f32 v159, v162, v163\n"
    ".Lat_in:\n"
    "v_mfma_f32_16x16x32_bf16 v[212:215], a[160:163], a[128:131], v[236:239]\n"
    "v_mfma_f32_16x16x32_bf16 v[212:215], a[164:167], a[132:135], v[212:215]\n"
    "v_exp_f32_e32 v170, v170\n"
    "v_mfma_f32_16x16x32_bf16 v[204:207], a[164:167], a[100:103], v[232:235]\n"
    "ds_write_addtid_b32 v143 offset:768\n"
    "v_mfma_f32_16x16x32_bf16 v[204:207], a[160:163], a[96:99], v[204:207]\n"
    "ds_read_b128 a[184:187], v242 offset:12288\n"
    "v_mfma_f32_16x16x32_bf16 v[196:199], a[160:163], a[64:67], v[228:231]\n"
    "v_exp_f32_e32 v171, v171\n"
    "v_mfma_f32_16x16x32_bf16 v[196:199], a[164:167], a[68:71], v[196:199]\n"
    "ds_write_addtid_b32 v150 offset:1536\n"
    "v_mfma_f32_16x16x32_bf16 v[188:191], a[164:167], a[36:39], v[224:227]\n"
    "v_cvt_pk_bf16_f32 v166, v168, v169\n"
    "v_mfma_f32_16x16x32_bf16 v[188:191], a[160:163], a[32:35], v[188:191]\n"
    "v_mfma_f32_16x16x32_bf16 v[180:183], a[160:163], a[0:3], v[220:223]\n"
    "ds_write_addtid_b32 v151 offset:1792\n"
    "v_mfma_f32_16x16x32_bf16 v[180:183], a[164:167], a[4:7], v[180:183]\n"
    "s_waitcnt lgkmcnt(10)\n"
    "v_exp_f32_e32 v176, v176\n"
    "v_mfma_f32_16x16x32_bf16 v[180:183], a[168:171], a[8:11], v[180:183]\n"
    "ds_read_b128 a[188:191], v243 offset:12288\n"
    "v_mfma_f32_16x16x32_bf16 v[180:183], a[172:175], a[12:15], v[180:183]\n"
    "v_exp_f32_e32 v177, v177\n"
    "v_mfma_f32_16x16x32_bf16 v[188:191], a[172:175], a[44:47], v[188:191]\n"
    "ds_write_addtid_b32 v158 offset:2560\n"
    "v_mfma_f32_16x16x32_bf16 v[188:191], a[168:171], a[40:43], v[188:191]\n"
    "ds_read_b128 a[160:163], v240 offset:10240\n"
    "v_mfma_f32_16x16x32_bf16 v[196:199], a[168:171], a[72:75], v[196:199]\n"
    "v_cvt_pk_bf16_f32 v167, v170, v171\n"
    "v_mfma_f32_16x16x32_bf16 v[196:199], a[172:175], a[76:79], v[196:199]\n"
    "ds_write_addtid_b32 v159 offset:2816\n"
    "v_mfma_f32_16x16x32_bf16 v[204:207], a[172:175], a[108:111], v[204:207]\n"
    "v_exp_f32_e32 v178, v178\n"
    "v_mfma_f32_16x16x32_bf16 v[204:207], a[168:171], a[104:107], v[204:207]\n"
    "ds_write_addtid_b32 v166 offset:3584\n"
    "v_mfma_f32_16x16x32_bf16 v[212:215], a[168:171], a[136:139], v[212:215]\n"
    "v_mfma_f32_16x16x32_bf16 v[212:215], a[172:175], a[140:143], v[212:215]\n"
    "s_waitcnt lgkmcnt(10)\n"
    "v_exp_f32_e32 v179, v179\n"
    "v_mfma_f32_16x16x32_bf16 v[212:215], a[176:179], a[144:147], v[212:215]\n"
    "ds_read_b128 a[164:167], v241 offset:10240\n"
    "v_mfma_f32_16x16x32_bf16 v[212:215], a[180:183], a[148:151], v[212:215]\n"
    "v_cvt_pk_bf16_f32 v174, v176, v177\n"
    "v_mfma_f32_16x16x32_bf16 v[204:207], a[180:183], a[116:119], v[204:207]\n"
    "ds_write_addtid_b32 v167 offset:3840\n"
    "v_mfma_f32_16x16x32_bf16 v[204:207], a[176:179], a[112:115], v[204:207]\n"
    "ds_read_b128 a[168:171], v240 offset:14336\n"
    "v_mfma_f32_16x16x32_bf16 v[196:199], a[176:179], a[80:83], v[196:199]\n"
    "v_cvt_pk_bf16_f32 v175, v178, v179\n"
    "v_mfma_f32_16x16x32_bf16 v[196:199], a[180:183], a[84:87], v[196:199]\n"
    "ds_write_addtid_b32 v174 offset:4608\n"
    "v_mfma_f32_16x16x32_bf16 v[188:191], a[180:183], a[52:55], v[188:191]\n"
    "ds_write_addtid_b32 v175 offset:4864\n"
    "v_mfma_f32_16x16x32_bf16 v[188:191], a[176:179], a[48:51], v[188:191]\n"
    "v_mfma_f32_16x16x32_bf16 v[180:183], a[176:179], a[16:19], v[180:183]\n"
    "ds_read_b128 a[172:175], v241 offset:14336\n"
    "v_mfma_f32_16x16x32_bf16 v[180:183], a[180:183], a[20:23], v[180:183]\n"
    "s_waitcnt lgkmcnt(10)\n"
    "s_waitcnt lgkmcnt(1)\n"
    "v_mfma_f32_16x16x32_bf16 v[180:183], a[184:187], a[24:27], v[180:183]\n"
    "s_barrier\n"
    "v_mfma_f32_16x16x32_bf16 v[180:183], a[188:191], a[28:31], v[180:183]\n"
    "s_add_u32 m0, s43, 24576\n"
    "v_mfma_f32_16x16x32_bf16 v[188:191], a[188:191], a[60:63], v[188:191]\n"
    "ds_read_b128 a[176:179], v242 offset:10240\n"
    "v_mfma_f32_16x16x32_bf16 v[188:191], a[184:187], a[56:59], v[188:191]\n"
    "buffer_load_dwordx4 v248, s[8:11], s40 offen offset:0 sc1 lds\n"
    "v_mfma_f32_16x16x32_bf16 v[196:199], a[184:187], a[88:91], v[196:199]\n"
    "ds_read2st64_b32 v[144:145], v249 offset0:0 offset1:1\n"
    "v_mfma_f32_16x16x32_bf16 v[196:199], a[188:191], a[92:95], v[196:199]\n"
    "v_mfma_f32_16x16x32_bf16 v[204:207], a[188:191], a[124:127], v[204:207]\n"
    "ds_read2st64_b32 v[146:147], v249 offset0:2 offset1:3\n"
    "v_mfma_f32_16x16x32_bf16 v[204:207], a[184:187], a[120:123], v[204:207]\n"
    "buffer_load_dwordx4 v248, s[8:11], s40 offen offset:1024 sc1 lds\n"
    "v_mfma_f32_16x16x32_bf16 v[212:215], a[184:187], a[152:155], v[212:215]\n"
    "v_mfma_f32_16x16x32_bf16 v[212:215], a[188:191], a[156:159], v[212:215]\n"
    "v_mfma_f32_16x16x32_bf16 v[216:219], a[160:163], a[128:131], v[236:239]\n"
    "ds_read_b128 a[180:183], v243 offset:10240\n"
    "v_mfma_f32_16x16x32_bf16 v[216:219], a[164:167], a[132:135], v[216:219]\n"
    "buffer_load_dwordx4 v248, s[8:11], s40 offen offset:2048 sc1 lds\n"
    "v_mfma_f32_16x16x32_bf16 v[208:211], a[164:167], a[100:103], v[232:235]\n"
    "ds_read2st64_b32 v[152:153], v249 offset0:4 offset1:5\n"
    "v_mfma_f32_16x16x32_bf16 v[208:211], a[160:163], a[96:99], v[208:211]\n"
    "ds_read_b128 a[184:187], v242 offset:14336\n"
    "v_mfma_f32_16x16x32_bf16 v[200:203], a[160:163], a[64:67], v[228:231]\n"
    "ds_read2st64_b32 v[154:155], v249 offset0:6 offset1:7\n"
    "v_mfma_f32_16x16x32_bf16 v[200:203], a[164:167], a[68:71], v[200:203]\n"
    "buffer_load_dwordx4 v248, s[8:11], s40 offen offset:3072 sc1 lds\n"
    "v_mfma_f32_16x16x32_bf16 v[192:195], a[164:167], a[36:39], v[224:227]\n"
    "s_add_u32 m0, s43, 28672\n"
    "v_exp_f32_e32 v180, v180\n"
    "v_mfma_f32_16x16x32_bf16 v[192:195], a[160:163], a[32:35], v[192:195]\n"
    "s_add_u32 s40, s40, 0x8000\n"
    "v_exp_f32_e32 v181, v181\n"
    "v_mfma_f32_16x16x32_bf16 v[184:187], a[160:163], a[0:3], v[220:223]\n"
    "ds_read2st64_b32 v[160:161], v249 offset0:8 offset1:9\n"
    "v_mfma_f32_16x16x32_bf16 v[184:187], a[164:167], a[4:7], v[184:187]\n"
    "s_waitcnt lgkmcnt(8)\n"
    "buffer_load_dwordx4 v248, s[8:11], s41 offen offset:0 sc1 lds\n"
    "v_mfma_f32_16x16x32_bf16 v[184:187], a[168:171], a[8:11], v[184:187]\n"
    "ds_read_b128 a[188:191], v243 offset:14336\n"
    "v_mfma_f32_16x16x32_bf16 v[184:187], a[172:175], a[12:15], v[184:187]\n"
    "ds_read_b64_tr_b16 a[160:161], v244 offset:0\n"
    "v_mfma_f32_16x16x32_bf16 v[192:195], a[172:175], a[44:47], v[192:195]\n"
    "ds_read2st64_b32 v[162:163], v249 offset0:10 offset1:11\n"
    "v_mfma_f32_16x16x32_bf16 v[192:195], a[168:171], a[40:43], v[192:195]\n"
    "buffer_load_dwordx4 v248, s[8:11], s41 offen offset:1024 sc1 lds\n"
    "v_mfma_f32_16x16x32_bf16 v[200:203], a[168:171], a[72:75], v[200:203]\n"
    "ds_read_b64_tr_b16 a[162:163], v244 offset:2048\n"
    "v_mfma_f32_16x16x32_bf16 v[200:203], a[172:175], a[76:79], v[200:203]\n"
    "v_exp_f32_e32 v182, v182\n"
    "v_mfma_f32_16x16x32_bf16 v[208:211], a[172:175], a[108:111], v[208:211]\n"
    "ds_read2st64_b32 v[168:169], v249 offset0:12 offset1:13\n"
    "v_mfma_f32_16x16x32_bf16 v[208:211], a[168:171], a[104:107], v[208:211]\n"
    "buffer_load_dwordx4 v248, s[8:11], s41 offen offset:2048 sc1 lds\n"
    "v_mfma_f32_16x16x32_bf16 v[216:219], a[168:171], a[136:139], v[216:219]\n"
    "ds_read_b64_tr_b16 a[164:165], v244 offset:32768\n"
    "v_mfma_f32_16x16x32_bf16 v[216:219], a[172:175], a[140:143], v[216:219]\n"
    "s_waitcnt lgkmcnt(10)\n"
    "ds_read2st64_b32 v[170:171], v249 offset0:14 offset1:15\n"
    "v_mfma_f32_16x16x32_bf16 v[216:219], a[176:179], a[144:147], v[216:219]\n"
    "ds_read_b64_tr_b16 a[168:169], v245 offset:0\n"
    "v_mfma_f32_16x16x32_bf16 v[216:219], a[180:183], a[148:151], v[216:219]\n"
    "buffer_load_dwordx4 v248, s[8:11], s41 offen offset:3072 sc1 lds\n"
    "v_mfma_f32_16x16x32_bf16 v[208:211], a[180:183], a[116:119], v[208:211]\n"
    "ds_read_b64_tr_b16 a[166:167], v244 offset:34816\n"
    "v_mfma_f32_16x16x32_bf16 v[208:211], a[176:179], a[112:115], v[208:211]\n"
    "s_add_u32 s41, s41, 0x8000\n"
    "v_exp_f32_e32 v183, v183\n"
    "v_mfma_f32_16x16x32_bf16 v[200:203], a[176:179], a[80:83], v[200:203]\n"
    "ds_read_b64_tr_b16 a[170:171], v245 offset:2048\n"
    "v_cvt_pk_bf16_f32 v180, v180, v181\n"
    "v_mfma_f32_16x16x32_bf16 v[200:203], a[180:183], a[84:87], v[200:203]\n"
    "ds_read2st64_b32 v[176:177], v249 offset0:16 offset1:17\n"
    "v_mfma_f32_16x16x32_bf16 v[192:195], a[180:183], a[52:55], v[192:195]\n"
    "v_exp_f32_e32 v188, v188\n"
    "v_mfma_f32_16x16x32_bf16 v[192:195], a[176:179], a[48:51], v[192:195]\n"
    "v_exp_f32_e32 v189, v189\n"
    "v_mfma_f32_16x16x32_bf16 v[184:187], a[176:179], a[16:19], v[184:187]\n"
    "ds_read_b64_tr_b16 a[172:173], v245 offset:32768\n"
    "v_cvt_pk_bf16_f32 v181, v182, v183\n"
    "v_mfma_f32_16x16x32_bf16 v[184:187], a[180:183], a[20:23], v[184:187]\n"
    "s_waitcnt lgkmcnt(11)\n"
    "ds_read2st64_b32 v[178:179], v249 offset0:18 offset1:19\n"
    "v_mfma_f32_16x16x32_bf16 v[184:187], a[184:187], a[24:27], v[184:187]\n"
    "ds_read_b64_tr_b16 a[176:177], v246 offset:0\n"
    "v_mfma_f32_16x16x32_bf16 v[184:187], a[188:191], a[28:31], v[184:187]\n"
    "ds_read_b64_tr_b16 a[174:175], v245 offset:34816\n"
    "v_mfma_f32_16x16x32_bf16 v[192:195], a[188:191], a[60:63], v[192:195]\n"
    "v_exp_f32_e32 v190, v190\n"
    "v_mfma_f32_16x16x32_bf16 v[192:195], a[184:187], a[56:59], v[192:195]\n"
    "v_exp_f32_e32 v191, v191\n"
    "v_mfma_f32_16x16x32_bf16 v[200:203], a[184:187], a[88:91], v[200:203]\n"
    "v_mfma_f32_16x16x32_bf16 v[200:203], a[188:191], a[92:95], v[200:203]\n"
    "ds_read_b64_tr_b16 a[178:179], v246 offset:2048\n"
    "v_mfma_f32_16x16x32_bf16 v[208:211], a[188:191], a[124:127], v[208:211]\n"
    "v_cvt_pk_bf16_f32 v188, v188, v189\n"
    "v_mfma_f32_16x16x32_bf16 v[208:211], a[184:187], a[120:123], v[208:211]\n"
    "v_exp_f32_e32 v196, v196\n"
    "v_mfma_f32_16x16x32_bf16 v[216:219], a[184:187], a[152:155], v[216:219]\n"
    "ds_read_b64_tr_b16 a[180:181], v246 offset:32768\n"
    "v_mfma_f32_16x16x32_bf16 v[216:219], a[188:191], a[156:159], v[216:219]\n"
    "s_waitcnt lgkmcnt(4)\n"
    "v_exp_f32_e32 v197, v197\n"
    "v_mfma_f32_16x16x32_bf16 a[236:239], a[160:163], v[140:143], a[236:239]\n"
    "v_mfma_f32_16x16x32_bf16 a[236:239], a[164:167], v[144:147], a[236:239]\n"
    "ds_read_b64_tr_b16 a[182:183], v246 offset:34816\n"
    "v_mfma_f32_16x16x32_bf16 a[240:243], a[164:167], v[152:155], a[240:243]\n"
    "ds_read_b64_tr_b16 a[184:185], v247 offset:0\n"
    "v_mfma_f32_16x16x32_bf16 a[240:243], a[160:163], v[148:151], a[240:243]\n"
    "v_cvt_pk_bf16_f32 v189, v190, v191\n"
    "v_mfma_f32_16x16x32_bf16 a[244:247], a[160:163], v[156:159], a[244:247]\n"
    "ds_read_b64_tr_b16 a[186:187], v247 offset:2048\n"
    "v_mfma_f32_16x16x32_bf16 a[244:247], a[164:167], v[160:163], a[244:247]\n"
    "v_exp_f32_e32 v198, v198\n"
    "v_mfma_f32_16x16x32_bf16 a[248:251], a[164:167], v[168:171], a[248:251]\n"
    "v_exp_f32_e32 v199, v199\n"
    "v_mfma_f32_16x16x32_bf16 a[248:251], a[160:163], v[164:167], a[248:251]\n"
    "v_cvt_pk_bf16_f32 v196, v196, v197\n"
    "v_mfma_f32_16x16x32_bf16 a[252:255], a[160:163], v[172:175], a[252:255]\n"
    "ds_read_b64_tr_b16 a[188:189], v247 offset:32768\n"
    "v_mfma_f32_16x16x32_bf16 a[252:255], a[164:167], v[176:179], a[252:255]\n"
    "s_waitcnt lgkmcnt(6)\n"
    "v_exp_f32_e32 v204, v204\n"
    "v_mfma_f32_16x16x32_bf16 v[16:19], a[172:175], v[176:179], v[16:19]\n"
    "ds_read_b64_tr_b16 a[160:161], v244 offset:4096\n"
    "v_mfma_f32_16x16x32_bf16 v[16:19], a[168:171], v[172:175], v[16:19]\n"
    "ds_read_b64_tr_b16 a[190:191], v247 offset:34816\n"
    "v_mfma_f32_16x16x32_bf16 v[12:15], a[168:171], v[164:167], v[12:15]\n"
    "v_exp_f32_e32 v205, v205\n"
    "v_mfma_f32_16x16x32_bf16 v[12:15], a[172:175], v[168:171], v[12:15]\n"
    "v_cvt_pk_bf16_f32 v197, v198, v199\n"
    "v_mfma_f32_16x16x32_bf16 v[8:11], a[172:175], v[160:163], v[8:11]\n"
    "ds_read_b64_tr_b16 a[162:163], v244 offset:6144\n"
    "v_mfma_f32_16x16x32_bf16 v[8:11], a[168:171], v[156:159], v[8:11]\n"
    "v_exp_f32_e32 v206, v206\n"
    "v_mfma_f32_16x16x32_bf16 v[4:7], a[168:171], v[148:151], v[4:7]\n"
    "v_exp_f32_e32 v207, v207\n"
    "v_mfma_f32_16x16x32_bf16 v[4:7], a[172:175], v[152:155], v[4:7]\n"
    "v_cvt_pk_bf16_f32 v204, v204, v205\n"
    "v_mfma_f32_16x16x32_bf16 v[0:3], a[172:175], v[144:147], v[0:3]\n"
    "ds_read_b64_tr_b16 a[164:165], v244 offset:36864\n"
    "v_mfma_f32_16x16x32_bf16 v[0:3], a[168:171], v[140:143], v[0:3]\n"
    "s_waitcnt lgkmcnt(7)\n"
    "v_exp_f32_e32 v212, v212\n"
    "v_mfma_f32_16x16x32_bf16 v[20:23], a[176:179], v[140:143], v[20:23]\n"
    "buffer_load_dword v253, off, s[8:11], s82 sc1\n"
    "v_mfma_f32_16x16x32_bf16 v[20:23], a[180:183], v[144:147], v[20:23]\n"
    "ds_read_b64_tr_b16 a[166:167], v244 offset:38912\n"
    "v_mfma_f32_16x16x32_bf16 v[24:27], a[180:183], v[152:155], v[24:27]\n"
    "ds_read_b64_tr_b16 a[168:169], v245 offset:4096\n"
    "v_mfma_f32_16x16x32_bf16 v[24:27], a[176:179], v[148:151], v[24:27]\n"
    "v_exp_f32_e32 v213, v213\n"
    "v_mfma_f32_16x16x32_bf16 v[28:31], a[176:179], v[156:159], v[28:31]\n"
    "ds_read_b64_tr_b16 a[170:171], v245 offset:6144\n"
    "v_mfma_f32_16x16x32_bf16 v[28:31], a[180:183], v[160:163], v[28:31]\n"
    "v_cvt_pk_bf16_f32 v205, v206, v207\n"
    "v_mfma_f32_16x16x32_bf16 v[32:35], a[180:183], v[168:171], v[32:35]\n"
    "v_exp_f32_e32 v214, v214\n"
    "v_mfma_f32_16x16x32_bf16 v[32:35], a[176:179], v[164:167], v[32:35]\n"
    "v_exp_f32_e32 v215, v215\n"
    "v_mfma_f32_16x16x32_bf16 v[36:39], a[176:179], v[172:175], v[36:39]\n"
    "ds_read_b64_tr_b16 a[172:173], v245 offset:36864\n"
    "v_mfma_f32_16x16x32_bf16 v[36:39], a[180:183], v[176:179], v[36:39]\n"
    "s_waitcnt lgkmcnt(6)\n"
    "v_cvt_pk_bf16_f32 v212, v212, v213\n"
    "v_mfma_f32_16x16x32_bf16 v[56:59], a[188:191], v[176:179], v[56:59]\n"
    "ds_read_b64_tr_b16 a[176:177], v246 offset:4096\n"
    "v_mfma_f32_16x16x32_bf16 v[56:59], a[184:187], v[172:175], v[56:59]\n"
    "ds_read_b64_tr_b16 a[174:175], v245 offset:38912\n"
    "v_mfma_f32_16x16x32_bf16 v[52:55], a[184:187], v[164:167], v[52:55]\n"
    "v_cvt_pk_bf16_f32 v213, v214, v215\n"
    "v_mfma_f32_16x16x32_bf16 v[52:55], a[188:191], v[168:171], v[52:55]\n"
    "v_exp_f32_e32 v184, v184\n"
    "v_mfma_f32_16x16x32_bf16 v[48:51], a[188:191], v[160:163], v[48:51]\n"
    "ds_read_b64_tr_b16 a[178:179], v246 offset:6144\n"
    "v_mfma_f32_16x16x32_bf16 v[48:51], a[184:187], v[156:159], v[48:51]\n"
    "v_mfma_f32_16x16x32_bf16 v[44:47], a[184:187], v[148:151], v[44:47]\n"
    "v_exp_f32_e32 v185, v185\n"
    "v_mfma_f32_16x16x32_bf16 v[44:47], a[188:191], v[152:155], v[44:47]\n"
    "v_exp_f32_e32 v186, v186\n"
    "v_mfma_f32_16x16x32_bf16 v[40:43], a[188:191], v[144:147], v[40:43]\n"
    "ds_read_b64_tr_b16 a[180:181], v246 offset:36864\n"
    "v_mfma_f32_16x16x32_bf16 v[40:43], a[184:187], v[140:143], v[40:43]\n"
    "s_waitcnt lgkmcnt(7)\n"
    "v_exp_f32_e32 v187, v187\n"
    "v_mfma_f32_16x16x32_bf16 v[60:63], a[160:163], v[140:143], v[60:63]\n"
    "ds_read_b64_tr_b16 a[184:185], v247 offset:4096\n"
    "v_mfma_f32_16x16x32_bf16 v[60:63], a[164:167], v[144:147], v[60:63]\n"
    "ds_read_b64_tr_b16 a[182:183], v246 offset:38912\n"
    "v_mfma_f32_16x16x32_bf16 v[64:67], a[164:167], v[152:155], v[64:67]\n"
    "ds_read_b64_tr_b16 a[186:187], v247 offset:6144\n"
    "v_mfma_f32_16x16x32_bf16 v[64:67], a[160:163], v[148:151], v[64:67]\n"
    "v_cvt_pk_bf16_f32 v182, v184, v185\n"
    "v_mfma_f32_16x16x32_bf16 v[68:71], a[160:163], v[156:159], v[68:71]\n"
    "v_exp_f32_e32 v192, v192\n"
    "v_mfma_f32_16x16x32_bf16 v[68:71], a[164:167], v[160:163], v[68:71]\n"
    "ds_read_b64_tr_b16 a[188:189], v247 offset:36864\n"
    "v_mfma_f32_16x16x32_bf16 v[72:75], a[164:167], v[168:171], v[72:75]\n"
    "ds_read_b64_tr_b16 a[190:191], v247 offset:38912\n"
    "v_mfma_f32_16x16x32_bf16 v[72:75], a[160:163], v[164:167], v[72:75]\n"
    "s_waitcnt vmcnt(9)\n"
    "v_mfma_f32_16x16x32_bf16 v[76:79], a[160:163], v[172:175], v[76:79]\n"
    "s_barrier\n"
    "v_mfma_f32_16x16x32_bf16 v[76:79], a[164:167], v[176:179], v[76:79]\n"
    "s_waitcnt lgkmcnt(7)\n"
    "s_mov_b32 m0, s42\n"
    "v_mfma_f32_16x16x32_bf16 v[96:99], a[172:175], v[176:179], v[96:99]\n"
    "v_exp_f32_e32 v193, v193\n"
    "v_mfma_f32_16x16x32_bf16 v[96:99], a[168:171], v[172:175], v[96:99]\n"
    "v_cvt_pk_bf16_f32 v183, v186, v187\n"
    "v_mfma_f32_16x16x32_bf16 v[92:95], a[168:171], v[164:167], v[92:95]\n"
    "v_exp_f32_e32 v194, v194\n"
    "v_mfma_f32_16x16x32_bf16 v[92:95], a[172:175], v[168:171], v[92:95]\n"
    "ds_read_b128 a[160:163], v240 offset:16384\n"
    "v_mfma_f32_16x16x32_bf16 v[88:91], a[172:175], v[160:163], v[88:91]\n"
    "ds_write_addtid_b32 v180 offset:0\n"
    "v_mfma_f32_16x16x32_bf16 v[88:91], a[168:171], v[156:159], v[88:91]\n"
    "v_exp_f32_e32 v195, v195\n"
    "v_mfma_f32_16x16x32_bf16 v[84:87], a[168:171], v[148:151], v[84:87]\n"
    "ds_write_addtid_b32 v181 offset:256\n"
    "v_mfma_f32_16x16x32_bf16 v[84:87], a[172:175], v[152:155], v[84:87]\n"
    "ds_write_addtid_b32 v188 offset:1024\n"
    "v_mfma_f32_16x16x32_bf16 v[80:83], a[172:175], v[144:147], v[80:83]\n"
    "v_cvt_pk_bf16_f32 v190, v192, v193\n"
    "v_mfma_f32_16x16x32_bf16 v[80:83], a[168:171], v[140:143], v[80:83]\n"
    "s_waitcnt lgkmcnt(7)\n"
    "v_exp_f32_e32 v200, v200\n"
    "v_mfma_f32_16x16x32_bf16 v[100:103], a[176:179], v[140:143], v[100:103]\n"
    "ds_read_b128 a[164:167], v241 offset:16384\n"
    "v_mfma_f32_16x16x32_bf16 v[100:103], a[180:183], v[144:147], v[100:103]\n"
    "ds_write_addtid_b32 v189 offset:1280\n"
    "v_mfma_f32_16x16x32_bf16 v[104:107], a[180:183], v[152:155], v[104:107]\n"
    "v_exp_f32_e32 v201, v201\n"
    "v_mfma_f32_16x16x32_bf16 v[104:107], a[176:179], v[148:151], v[104:107]\n"
    "ds_read_b128 a[168:171], v240 offset:20480\n"
    "v_mfma_f32_16x16x32_bf16 v[108:111], a[176:179], v[156:159], v[108:111]\n"
    "ds_write_addtid_b32 v196 offset:2048\n"
    "v_mfma_f32_16x16x32_bf16 v[108:111], a[180:183], v[160:163], v[108:111]\n"
    "v_cvt_pk_bf16_f32 v191, v194, v195\n"
    "v_mfma_f32_16x16x32_bf16 v[112:115], a[180:183], v[168:171], v[112:115]\n"
    "ds_write_addtid_b32 v197 offset:2304\n"
    "v_mfma_f32_16x16x32_bf16 v[112:115], a[176:179], v[164:167], v[112:115]\n"
    "v_exp_f32_e32 v202, v202\n"
    "v_mfma_f32_16x16x32_bf16 v[116:119], a[176:179], v[172:175], v[116:119]\n"
    "ds_write_addtid_b32 v204 offset:3072\n"
    "v_mfma_f32_16x16x32_bf16 v[116:119], a[180:183], v[176:179], v[116:119]\n"
    "s_waitcnt lgkmcnt(10)\n"
    "v_exp_f32_e32 v203, v203\n"
    "v_mfma_f32_16x16x32_bf16 v[136:139], a[188:191], v[176:179], v[136:139]\n"
    "ds_read_b128 a[172:175], v241 offset:20480\n"
    "v_mfma_f32_16x16x32_bf16 v[136:139], a[184:187], v[172:175], v[136:139]\n"
    "ds_write_addtid_b32 v205 offset:3328\n"
    "v_cvt_pk_bf16_f32 v198, v200, v201\n"
    "v_mfma_f32_16x16x32_bf16 v[132:135], a[184:187], v[164:167], v[132:135]\n"
    "ds_read_b128 a[176:179], v242 offset:16384\n"
    "v_mfma_f32_16x16x32_bf16 v[132:135], a[188:191], v[168:171], v[132:135]\n"
    "ds_write_addtid_b32 v212 offset:4096\n"
    "v_mfma_f32_16x16x32_bf16 v[128:131], a[188:191], v[160:163], v[128:131]\n"
    "v_exp_f32_e32 v208, v208\n"
    "v_mfma_f32_16x16x32_bf16 v[128:131], a[184:187], v[156:159], v[128:131]\n"
    "ds_write_addtid_b32 v213 offset:4352\n"
    "v_mfma_f32_16x16x32_bf16 v[124:127], a[184:187], v[148:151], v[124:127]\n"
    "v_exp_f32_e32 v209, v209\n"
    "v_mfma_f32_16x16x32_bf16 v[124:127], a[188:191], v[152:155], v[124:127]\n"
    "ds_read_b128 a[180:183], v243 offset:16384\n"
    "v_mfma_f32_16x16x32_bf16 v[120:123], a[188:191], v[144:147], v[120:123]\n"
    "ds_write_addtid_b32 v182 offset:512\n"
    "v_mfma_f32_16x16x32_bf16 v[120:123], a[184:187], v[140:143], v[120:123]\n"
    "s_waitcnt lgkmcnt(12)\n"
    "v_cvt_pk_bf16_f32 v199, v202, v203\n"
    "v_mfma_f32_16x16x32_bf16 v[172:175], a[160:163], a[128:131], v[236:239]\n"
    "v_mfma_f32_16x16x32_bf16 v[172:175], a[164:167], a[132:135], v[172:175]\n"
    "v_exp_f32_e32 v210, v210\n"
    "v_mfma_f32_16x16x32_bf16 v[164:167], a[164:167], a[100:103], v[232:235]\n"
    "ds_write_addtid_b32 v183 offset:768\n"
    "v_mfma_f32_16x16x32_bf16 v[164:167], a[160:163], a[96:99], v[164:167]\n"
    "ds_read_b128 a[184:187], v242 offset:20480\n"
    "v_mfma_f32_16x16x32_bf16 v[156:159], a[160:163], a[64:67], v[228:231]\n"
    "v_exp_f32_e32 v211, v211\n"
    "v_mfma_f32_16x16x32_bf16 v[156:159], a[164:167], a[68:71], v[156:159]\n"
    "ds_write_addtid_b32 v190 offset:1536\n"
    "v_mfma_f32_16x16x32_bf16 v[148:151], a[164:167], a[36:39], v[224:227]\n"
    "v_cvt_pk_bf16_f32 v206, v208, v209\n"
    "v_mfma_f32_16x16x32_bf16 v[148:151], a[160:163], a[32:35], v[148:151]\n"
    "v_mfma_f32_16x16x32_bf16 v[140:143], a[160:163], a[0:3], v[220:223]\n"
    "ds_write_addtid_b32 v191 offset:1792\n"
    "v_mfma_f32_16x16x32_bf16 v[140:143], a[164:167], a[4:7], v[140:143]\n"
    "s_waitcnt lgkmcnt(10)\n"
    "v_exp_f32_e32 v216, v216\n"
    "v_mfma_f32_16x16x32_bf16 v[140:143], a[168:171], a[8:11], v[140:143]\n"
    "ds_read_b128 a[188:191], v243 offset:20480\n"
    "v_mfma_f32_16x16x32_bf16 v[140:143], a[172:175], a[12:15], v[140:143]\n"
    "v_exp_f32_e32 v217, v217\n"
    "v_mfma_f32_16x16x32_bf16 v[148:151], a[172:175], a[44:47], v[148:151]\n"
    "ds_write_addtid_b32 v198 offset:2560\n"
    "v_mfma_f32_16x16x32_bf16 v[148:151], a[168:171], a[40:43], v[148:151]\n"
    "ds_read_b128 a[160:163], v240 offset:18432\n"
    "v_mfma_f32_16x16x32_bf16 v[156:159], a[168:171], a[72:75], v[156:159]\n"
    "v_cvt_pk_bf16_f32 v207, v210, v211\n"
    "v_mfma_f32_16x16x32_bf16 v[156:159], a[172:175], a[76:79], v[156:159]\n"
    "ds_write_addtid_b32 v199 offset:2816\n"
    "v_mfma_f32_16x16x32_bf16 v[164:167], a[172:175], a[108:111], v[164:167]\n"
    "v_exp_f32_e32 v218, v218\n"
    "v_mfma_f32_16x16x32_bf16 v[164:167], a[168:171], a[104:107], v[164:167]\n"
    "ds_write_addtid_b32 v206 offset:3584\n"
    "v_mfma_f32_16x16x32_bf16 v[172:175], a[168:171], a[136:139], v[172:175]\n"
    "v_mfma_f32_16x16x32_bf16 v[172:175], a[172:175], a[140:143], v[172:175]\n"
    "s_waitcnt lgkmcnt(10)\n"
    "v_exp_f32_e32 v219, v219\n"
    "v_mfma_f32_16x16x32_bf16 v[172:175], a[176:179], a[144:147], v[172:175]\n"
    "ds_read_b128 a[164:167], v241 offset:18432\n"
    "v_mfma_f32_16x16x32_bf16 v[172:175], a[180:183], a[148:151], v[172:175]\n"
    "v_cvt_pk_bf16_f32 v214, v216, v217\n"
    "v_mfma_f32_16x16x32_bf16 v[164:167], a[180:183], a[116:119], v[164:167]\n"
    "ds_write_addtid_b32 v207 offset:3840\n"
    "v_mfma_f32_16x16x32_bf16 v[164:167], a[176:179], a[112:115], v[164:167]\n"
    "ds_read_b128 a[168:171], v240 offset:22528\n"
    "v_mfma_f32_16x16x32_bf16 v[156:159], a[176:179], a[80:83], v[156:159]\n"
    "v_cvt_pk_bf16_f32 v215, v218, v219\n"
    "v_mfma_f32_16x16x32_bf16 v[156:159], a[180:183], a[84:87], v[156:159]\n"
    "ds_write_addtid_b32 v214 offset:4608\n"
    "v_mfma_f32_16x16x32_bf16 v[148:151], a[180:183], a[52:55], v[148:151]\n"
    "ds_write_addtid_b32 v215 offset:4864\n"
    "v_mfma_f32_16x16x32_bf16 v[148:151], a[176:179], a[48:51], v[148:151]\n"
    "v_mfma_f32_16x16x32_bf16 v[140:143], a[176:179], a[16:19], v[140:143]\n"
    "ds_read_b128 a[172:175], v241 offset:22528\n"
    "v_mfma_f32_16x16x32_bf16 v[140:143], a[180:183], a[20:23], v[140:143]\n"
    "s_waitcnt lgkmcnt(10)\n"
    "s_waitcnt lgkmcnt(1)\n"
    "v_mfma_f32_16x16x32_bf16 v[140:143], a[184:187], a[24:27], v[140:143]\n"
    "s_barrier\n"
    "v_mfma_f32_16x16x32_bf16 v[140:143], a[188:191], a[28:31], v[140:143]\n"
    "s_add_u32 m0, s43, 0\n"
    "v_mfma_f32_16x16x32_bf16 v[148:151], a[188:191], a[60:63], v[148:151]\n"
    "ds_read_b128 a[176:179], v242 offset:18432\n"
    "v_mfma_f32_16x16x32_bf16 v[148:151], a[184:187], a[56:59], v[148:151]\n"
    "buffer_load_dwordx4 v248, s[8:11], s40 offen offset:0 sc1 lds\n"
    "v_mfma_f32_16x16x32_bf16 v[156:159], a[184:187], a[88:91], v[156:159]\n"
    "ds_read2st64_b32 v[184:185], v249 offset0:0 offset1:1\n"
    "v_mfma_f32_16x16x32_bf16 v[156:159], a[188:191], a[92:95], v[156:159]\n"
    "v_mfma_f32_16x16x32_bf16 v[164:167], a[188:191], a[124:127], v[164:167]\n"
    "ds_read2st64_b32 v[186:187], v249 offset0:2 offset1:3\n"
    "v_mfma_f32_16x16x32_bf16 v[164:167], a[184:187], a[120:123], v[164:167]\n"
    "buffer_load_dwordx4 v248, s[8:11], s40 offen offset:1024 sc1 lds\n"
    "v_mfma_f32_16x16x32_bf16 v[172:175], a[184:187], a[152:155], v[172:175]\n"
    "v_mfma_f32_16x16x32_bf16 v[172:175], a[188:191], a[156:159], v[172:175]\n"
    "v_mfma_f32_16x16x32_bf16 v[176:179], a[160:163], a[128:131], v[236:239]\n"
    "ds_read_b128 a[180:183], v243 offset:18432\n"
    "v_mfma_f32_16x16x32_bf16 v[176:179], a[164:167], a[132:135], v[176:179]\n"
    "buffer_load_dwordx4 v248, s[8:11], s40 offen offset:2048 sc1 lds\n"
    "v_mfma_f32_16x16x32_bf16 v[168:171], a[164:167], a[100:103], v[232:235]\n"
    "ds_read2st64_b32 v[192:193], v249 offset0:4 offset1:5\n"
    "v_mfma_f32_16x16x32_bf16 v[168:171], a[160:163], a[96:99], v[168:171]\n"
    "ds_read_b128 a[184:187], v242 offset:22528\n"
    "v_mfma_f32_16x16x32_bf16 v[160:163], a[160:163], a[64:67], v[228:231]\n"
    "ds_read2st64_b32 v[194:195], v249 offset0:6 offset1:7\n"
    "v_mfma_f32_16x16x32_bf16 v[160:163], a[164:167], a[68:71], v[160:163]\n"
    "buffer_load_dwordx4 v248, s[8:11], s40 offen offset:3072 sc1 lds\n"
    "v_mfma_f32_16x16x32_bf16 v[152:155], a[164:167], a[36:39], v[224:227]\n"
    "s_add_u32 m0, s43, 4096\n"
    "v_exp_f32_e32 v140, v140\n"
    "v_mfma_f32_16x16x32_bf16 v[152:155], a[160:163], a[32:35], v[152:155]\n"
    "s_add_u32 s40, s40, 0x8000\n"
    "v_exp_f32_e32 v141, v141\n"
    "v_mfma_f32_16x16x32_bf16 v[144:147], a[160:163], a[0:3], v[220:223]\n"
    "ds_read2st64_b32 v[200:201], v249 offset0:8 offset1:9\n"
    "v_mfma_f32_16x16x32_bf16 v[144:147], a[164:167], a[4:7], v[144:147]\n"
    "s_waitcnt lgkmcnt(8)\n"
    "buffer_load_dwordx4 v248, s[8:11], s41 offen offset:0 sc1 lds\n"
    "v_mfma_f32_16x16x32_bf16 v[144:147], a[168:171], a[8:11], v[144:147]\n"
    "ds_read_b128 a[188:191], v243 offset:22528\n"
    "v_mfma_f32_16x16x32_bf16 v[144:147], a[172:175], a[12:15], v[144:147]\n"
    "ds_read_b64_tr_b16 a[160:161], v244 offset:8192\n"
    "v_mfma_f32_16x16x32_bf16 v[152:155], a[172:175], a[44:47], v[152:155]\n"
    "ds_read2st64_b32 v[202:203], v249 offset0:10 offset1:11\n"
    "v_mfma_f32_16x16x32_bf16 v[152:155], a[168:171], a[40:43], v[152:155]\n"
    "buffer_load_dwordx4 v248, s[8:11], s41 offen offset:1024 sc1 lds\n"
    "v_mfma_f32_16x16x32_bf16 v[160:163], a[168:171], a[72:75], v[160:163]\n"
    "ds_read_b64_tr_b16 a[162:163], v244 offset:10240\n"
    "v_mfma_f32_16x16x32_bf16 v[160:163], a[172:175], a[76:79], v[160:163]\n"
    "v_exp_f32_e32 v142, v142\n"
    "v_mfma_f32_16x16x32_bf16 v[168:171], a[172:175], a[108:111], v[168:171]\n"
    "ds_read2st64_b32 v[208:209], v249 offset0:12 offset1:13\n"
    "v_mfma_f32_16x16x32_bf16 v[168:171], a[168:171], a[104:107], v[168:171]\n"
    "buffer_load_dwordx4 v248, s[8:11], s41 offen offset:2048 sc1 lds\n"
    "v_mfma_f32_16x16x32_bf16 v[176:179], a[168:171], a[136:139], v[176:179]\n"
    "ds_read_b64_tr_b16 a[164:165], v244 offset:40960\n"
    "v_mfma_f32_16x16x32_bf16 v[176:179], a[172:175], a[140:143], v[176:179]\n"
    "s_waitcnt lgkmcnt(10)\n"
    "ds_read2st64_b32 v[210:211], v249 offset0:14 offset1:15\n"
    "v_mfma_f32_16x16x32_bf16 v[176:179], a[176:179], a[144:147], v[176:179]\n"
    "ds_read_b64_tr_b16 a[168:169], v245 offset:8192\n"
    "v_mfma_f32_16x16x32_bf16 v[176:179], a[180:183], a[148:151], v[176:179]\n"
    "buffer_load_dwordx4 v248, s[8:11], s41 offen offset:3072 sc1 lds\n"
    "v_mfma_f32_16x16x32_bf16 v[168:171], a[180:183], a[116:119], v[168:171]\n"
    "ds_read_b64_tr_b16 a[166:167], v244 offset:43008\n"
    "v_mfma_f32_16x16x32_bf16 v[168:171], a[176:179], a[112:115], v[168:171]\n"
    "s_add_u32 s41, s41, 0x8000\n"
    "v_exp_f32_e32 v143, v143\n"
    "v_mfma_f32_16x16x32_bf16 v[160:163], a[176:179], a[80:83], v[160:163]\n"
    "ds_read_b64_tr_b16 a[170:171], v245 offset:10240\n"
    "v_cvt_pk_bf16_f32 v140, v140, v141\n"
    "v_mfma_f32_16x16x32_bf16 v[160:163], a[180:183], a[84:87], v[160:163]\n"
    "ds_read2st64_b32 v[216:217], v249 offset0:16 offset1:17\n"
    "v_mfma_f32_16x16x32_bf16 v[152:155], a[180:183], a[52:55], v[152:155]\n"
    "v_exp_f32_e32 v148, v148\n"
    "v_mfma_f32_16x16x32_bf16 v[152:155], a[176:179], a[48:51], v[152:155]\n"
    "v_exp_f32_e32 v149, v149\n"
    "v_mfma_f32_16x16x32_bf16 v[144:147], a[176:179], a[16:19], v[144:147]\n"
    "ds_read_b64_tr_b16 a[172:173], v245 offset:40960\n"
    "v_cvt_pk_bf16_f32 v141, v142, v143\n"
    "v_mfma_f32_16x16x32_bf16 v[144:147], a[180:183], a[20:23], v[144:147]\n"
    "s_waitcnt lgkmcnt(11)\n"
    "ds_read2st64_b32 v[218:219], v249 offset0:18 offset1:19\n"
    "v_mfma_f32_16x16x32_bf16 v[144:147], a[184:187], a[24:27], v[144:147]\n"
    "ds_read_b64_tr_b16 a[176:177], v246 offset:8192\n"
    "v_mfma_f32_16x16x32_bf16 v[144:147], a[188:191], a[28:31], v[144:147]\n"
    "ds_read_b64_tr_b16 a[174:175], v245 offset:43008\n"
    "v_mfma_f32_16x16x32_bf16 v[152:155], a[188:191], a[60:63], v[152:155]\n"
    "v_exp_f32_e32 v150, v150\n"
    "v_mfma_f32_16x16x32_bf16 v[152:155], a[184:187], a[56:59], v[152:155]\n"
    "v_exp_f32_e32 v151, v151\n"
    "v_mfma_f32_16x16x32_bf16 v[160:163], a[184:187], a[88:91], v[160:163]\n"
    "s_waitcnt vmcnt(8)\n"
    "v_readfirstlane_b32 s81, v253\n"
    "v_mfma_f32_16x16x32_bf16 v[160:163], a[188:191], a[92:95], v[160:163]\n"
    "ds_read_b64_tr_b16 a[178:179], v246 offset:10240\n"
    "v_mfma_f32_16x16x32_bf16 v[168:171], a[188:191], a[124:127], v[168:171]\n"
    "s_cmp_eq_u32 s81, s39\n"
    "v_cvt_pk_bf16_f32 v148, v148, v149\n"
    "v_mfma_f32_16x16x32_bf16 v[168:171], a[184:187], a[120:123], v[168:171]\n"
    "s_cselect_b32 s88, s83, 2.0\n"
    "v_exp_f32_e32 v156, v156\n"
    "v_mfma_f32_16x16x32_bf16 v[176:179], a[184:187], a[152:155], v[176:179]\n"
    "s_mov_b32 m0, s30\n"
    "ds_read_b64_tr_b16 a[180:181], v246 offset:40960\n"
    "v_mfma_f32_16x16x32_bf16 v[176:179], a[188:191], a[156:159], v[176:179]\n"
    "s_waitcnt lgkmcnt(4)\n"
    "v_exp_f32_e32 v157, v157\n"
    "v_mfma_f32_16x16x32_bf16 a[236:239], a[160:163], v[180:183], a[236:239]\n"
    "buffer_load_dwordx4 v250, s[8:11], s88 offen offset:0 sc1 lds\n"
    "v_mfma_f32_16x16x32_bf16 a[236:239], a[164:167], v[184:187], a[236:239]\n"
    "ds_read_b64_tr_b16 a[182:183], v246 offset:43008\n"
    "v_mfma_f32_16x16x32_bf16 a[240:243], a[164:167], v[192:195], a[240:243]\n"
    "ds_read_b64_tr_b16 a[184:185], v247 offset:8192\n"
    "v_mfma_f32_16x16x32_bf16 a[240:243], a[160:163], v[188:191], a[240:243]\n"
    "v_cvt_pk_bf16_f32 v149, v150, v151\n"
    "v_mfma_f32_16x16x32_bf16 a[244:247], a[160:163], v[196:199], a[244:247]\n"
    "ds_read_b64_tr_b16 a[186:187], v247 offset:10240\n"
    "v_mfma_f32_16x16x32_bf16 a[244:247], a[164:167], v[200:203], a[244:247]\n"
    "v_exp_f32_e32 v158, v158\n"
    "v_mfma_f32_16x16x32_bf16 a[248:251], a[164:167], v[208:211], a[248:251]\n"
    "v_exp_f32_e32 v159, v159\n"
    "v_mfma_f32_16x16x32_bf16 a[248:251], a[160:163], v[204:207], a[248:251]\n"
    "v_cvt_pk_bf16_f32 v156, v156, v157\n"
    "v_mfma_f32_16x16x32_bf16 a[252:255], a[160:163], v[212:215], a[252:255]\n"
    "ds_read_b64_tr_b16 a[188:189], v247 offset:40960\n"
    "v_mfma_f32_16x16x32_bf16 a[252:255], a[164:167], v[216:219], a[252:255]\n"
    "s_waitcnt lgkmcnt(6)\n"
    "v_exp_f32_e32 v164, v164\n"
    "v_mfma_f32_16x16x32_bf16 v[16:19], a[172:175], v[216:219], v[16:19]\n"
    "ds_read_b64_tr_b16 a[160:161], v244 offset:12288\n"
    "v_mfma_f32_16x16x32_bf16 v[16:19], a[168:171], v[212:215], v[16:19]\n"
    "ds_read_b64_tr_b16 a[190:191], v247 offset:43008\n"
    "v_mfma_f32_16x16x32_bf16 v[12:15], a[168:171], v[204:207], v[12:15]\n"
    "v_exp_f32_e32 v165, v165\n"
    "v_mfma_f32_16x16x32_bf16 v[12:15], a[172:175], v[208:211], v[12:15]\n"
    "v_cvt_pk_bf16_f32 v157, v158, v159\n"
    "v_mfma_f32_16x16x32_bf16 v[8:11], a[172:175], v[200:203], v[8:11]\n"
    "ds_read_b64_tr_b16 a[162:163], v244 offset:14336\n"
    "v_mfma_f32_16x16x32_bf16 v[8:11], a[168:171], v[196:199], v[8:11]\n"
    "v_exp_f32_e32 v166, v166\n"
    "v_mfma_f32_16x16x32_bf16 v[4:7], a[168:171], v[188:191], v[4:7]\n"
    "v_exp_f32_e32 v167, v167\n"
    "v_mfma_f32_16x16x32_bf16 v[4:7], a[172:175], v[192:195], v[4:7]\n"
    "v_cvt_pk_bf16_f32 v164, v164, v165\n"
    "v_mfma_f32_16x16x32_bf16 v[0:3], a[172:175], v[184:187], v[0:3]\n"
    "ds_read_b64_tr_b16 a[164:165], v244 offset:45056\n"
    "v_mfma_f32_16x16x32_bf16 v[0:3], a[168:171], v[180:183], v[0:3]\n"
    "s_waitcnt lgkmcnt(7)\n"
    "v_exp_f32_e32 v172, v172\n"
    "v_mfma_f32_16x16x32_bf16 v[20:23], a[176:179], v[180:183], v[20:23]\n"
    "v_mfma_f32_16x16x32_bf16 v[20:23], a[180:183], v[184:187], v[20:23]\n"
    "ds_read_b64_tr_b16 a[166:167], v244 offset:47104\n"
    "v_mfma_f32_16x16x32_bf16 v[24:27], a[180:183], v[192:195], v[24:27]\n"
    "ds_read_b64_tr_b16 a[168:169], v245 offset:12288\n"
    "v_mfma_f32_16x16x32_bf16 v[24:27], a[176:179], v[188:191], v[24:27]\n"
    "v_exp_f32_e32 v173, v173\n"
    "v_mfma_f32_16x16x32_bf16 v[28:31], a[176:179], v[196:199], v[28:31]\n"
    "ds_read_b64_tr_b16 a[170:171], v245 offset:14336\n"
    "v_mfma_f32_16x16x32_bf16 v[28:31], a[180:183], v[200:203], v[28:31]\n"
    "v_cvt_pk_bf16_f32 v165, v166, v167\n"
    "v_mfma_f32_16x16x32_bf16 v[32:35], a[180:183], v[208:211], v[32:35]\n"
    "v_exp_f32_e32 v174, v174\n"
    "v_mfma_f32_16x16x32_bf16 v[32:35], a[176:179], v[204:207], v[32:35]\n"
    "v_exp_f32_e32 v175, v175\n"
    "v_mfma_f32_16x16x32_bf16 v[36:39], a[176:179], v[212:215], v[36:39]\n"
    "ds_read_b64_tr_b16 a[172:173], v245 offset:45056\n"
    "v_mfma_f32_16x16x32_bf16 v[36:39], a[180:183], v[216:219], v[36:39]\n"
    "s_waitcnt lgkmcnt(6)\n"
    "v_cvt_pk_bf16_f32 v172, v172, v173\n"
    "v_mfma_f32_16x16x32_bf16 v[56:59], a[188:191], v[216:219], v[56:59]\n"
    "ds_read_b64_tr_b16 a[176:177], v246 offset:12288\n"
    "v_mfma_f32_16x16x32_bf16 v[56:59], a[184:187], v[212:215], v[56:59]\n"
    "ds_read_b64_tr_b16 a[174:175], v245 offset:47104\n"
    "v_mfma_f32_16x16x32_bf16 v[52:55], a[184:187], v[204:207], v[52:55]\n"
    "v_cvt_pk_bf16_f32 v173, v174, v175\n"
    "v_mfma_f32_16x16x32_bf16 v[52:55], a[188:191], v[208:211], v[52:55]\n"
    "v_exp_f32_e32 v144, v144\n"
    "v_mfma_f32_16x16x32_bf16 v[48:51], a[188:191], v[200:203], v[48:51]\n"
    "ds_read_b64_tr_b16 a[178:179], v246 offset:14336\n"
    "v_mfma_f32_16x16x32_bf16 v[48:51], a[184:187], v[196:199], v[48:51]\n"
    "v_mfma_f32_16x16x32_bf16 v[44:47], a[184:187], v[188:191], v[44:47]\n"
    "v_exp_f32_e32 v145, v145\n"
    "v_mfma_f32_16x16x32_bf16 v[44:47], a[188:191], v[192:195], v[44:47]\n"
    "v_exp_f32_e32 v146, v146\n"
    "v_mfma_f32_16x16x32_bf16 v[40:43], a[188:191], v[184:187], v[40:43]\n"
    "ds_read_b64_tr_b16 a[180:181], v246 offset:45056\n"
    "v_mfma_f32_16x16x32_bf16 v[40:43], a[184:187], v[180:183], v[40:43]\n"
    "s_waitcnt lgkmcnt(7)\n"
    "v_exp_f32_e32 v147, v147\n"
    "v_mfma_f32_16x16x32_bf16 v[60:63], a[160:163], v[180:183], v[60:63]\n"
    "ds_read_b64_tr_b16 a[184:185], v247 offset:12288\n"
    "v_mfma_f32_16x16x32_bf16 v[60:63], a[164:167], v[184:187], v[60:63]\n"
    "ds_read_b64_tr_b16 a[182:183], v246 offset:47104\n"
    "v_mfma_f32_16x16x32_bf16 v[64:67], a[164:167], v[192:195], v[64:67]\n"
    "ds_read_b64_tr_b16 a[186:187], v247 offset:14336\n"
    "v_mfma_f32_16x16x32_bf16 v[64:67], a[160:163], v[188:191], v[64:67]\n"
    "v_cvt_pk_bf16_f32 v142, v144, v145\n"
    "v_mfma_f32_16x16x32_bf16 v[68:71], a[160:163], v[196:199], v[68:71]\n"
    "v_exp_f32_e32 v152, v152\n"
    "v_mfma_f32_16x16x32_bf16 v[68:71], a[164:167], v[200:203], v[68:71]\n"
    "ds_read_b64_tr_b16 a[188:189], v247 offset:45056\n"
    "v_mfma_f32_16x16x32_bf16 v[72:75], a[164:167], v[208:211], v[72:75]\n"
    "ds_read_b64_tr_b16 a[190:191], v247 offset:47104\n"
    "v_mfma_f32_16x16x32_bf16 v[72:75], a[160:163], v[204:207], v[72:75]\n"
    "s_waitcnt vmcnt(10)\n"
    "v_mfma_f32_16x16x32_bf16 v[76:79], a[160:163], v[212:215], v[76:79]\n"
    "s_barrier\n"
    "v_mfma_f32_16x16x32_bf16 v[76:79], a[164:167], v[216:219], v[76:79]\n"
    "s_waitcnt lgkmcnt(7)\n"
    "s_mov_b32 m0, s42\n"
    "v_mfma_f32_16x16x32_bf16 v[96:99], a[172:175], v[216:219], v[96:99]\n"
    "v_exp_f32_e32 v153, v153\n"
    "v_mfma_f32_16x16x32_bf16 v[96:99], a[168:171], v[212:215], v[96:99]\n"
    "v_cvt_pk_bf16_f32 v143, v146, v147\n"
    "v_mfma_f32_16x16x32_bf16 v[92:95], a[168:171], v[204:207], v[92:95]\n"
    "v_exp_f32_e32 v154, v154\n"
    "v_mfma_f32_16x16x32_bf16 v[92:95], a[172:175], v[208:211], v[92:95]\n"
    "ds_read_b128 a[160:163], v240 offset:24576\n"
    "v_mfma_f32_16x16x32_bf16 v[88:91], a[172:175], v[200:203], v[88:91]\n"
    "ds_write_addtid_b32 v140 offset:0\n"
    "v_mfma_f32_16x16x32_bf16 v[88:91], a[168:171], v[196:199], v[88:91]\n"
    "v_exp_f32_e32 v155, v155\n"
    "v_mfma_f32_16x16x32_bf16 v[84:87], a[168:171], v[188:191], v[84:87]\n"
    "ds_write_addtid_b32 v141 offset:256\n"
    "v_mfma_f32_16x16x32_bf16 v[84:87], a[172:175], v[192:195], v[84:87]\n"
    "ds_write_addtid_b32 v148 offset:1024\n"
    "v_mfma_f32_16x16x32_bf16 v[80:83], a[172:175], v[184:187], v[80:83]\n"
    "v_cvt_pk_bf16_f32 v150, v152, v153\n"
    "v_mfma_f32_16x16x32_bf16 v[80:83], a[168:171], v[180:183], v[80:83]\n"
    "s_waitcnt lgkmcnt(7)\n"
    "v_exp_f32_e32 v160, v160\n"
    "v_mfma_f32_16x16x32_bf16 v[100:103], a[176:179], v[180:183], v[100:103]\n"
    "ds_read_b128 a[164:167], v241 offset:24576\n"
    "v_mfma_f32_16x16x32_bf16 v[100:103], a[180:183], v[184:187], v[100:103]\n"
    "ds_write_addtid_b32 v149 offset:1280\n"
    "v_mfma_f32_16x16x32_bf16 v[104:107], a[180:183], v[192:195], v[104:107]\n"
    "v_exp_f32_e32 v161, v161\n"
    "v_mfma_f32_16x16x32_bf16 v[104:107], a[176:179], v[188:191], v[104:107]\n"
    "ds_read_b128 a[168:171], v240 offset:28672\n"
    "v_mfma_f32_16x16x32_bf16 v[108:111], a[176:179], v[196:199], v[108:111]\n"
    "ds_write_addtid_b32 v156 offset:2048\n"
    "v_mfma_f32_16x16x32_bf16 v[108:111], a[180:183], v[200:203], v[108:111]\n"
    "v_cvt_pk_bf16_f32 v151, v154, v155\n"
    "v_mfma_f32_16x16x32_bf16 v[112:115], a[180:183], v[208:211], v[112:115]\n"
    "ds_write_addtid_b32 v157 offset:2304\n"
    "v_mfma_f32_16x16x32_bf16 v[112:115], a[176:179], v[204:207], v[112:115]\n"
    "v_exp_f32_e32 v162, v162\n"
    "v_mfma_f32_16x16x32_bf16 v[116:119], a[176:179], v[212:215], v[116:119]\n"
    "ds_write_addtid_b32 v164 offset:3072\n"
    "v_mfma_f32_16x16x32_bf16 v[116:119], a[180:183], v[216:219], v[116:119]\n"
    "s_waitcnt lgkmcnt(10)\n"
    "v_exp_f32_e32 v163, v163\n"
    "v_mfma_f32_16x16x32_bf16 v[136:139], a[188:191], v[216:219], v[136:139]\n"
    "ds_read_b128 a[172:175], v241 offset:28672\n"
    "v_mfma_f32_16x16x32_bf16 v[136:139], a[184:187], v[212:215], v[136:139]\n"
    "ds_write_addtid_b32 v165 offset:3328\n"
    "v_cvt_pk_bf16_f32 v158, v160, v161\n"
    "v_mfma_f32_16x16x32_bf16 v[132:135], a[184:187], v[204:207], v[132:135]\n"
    "ds_read_b128 a[176:179], v242 offset:24576\n"
    "v_mfma_f32_16x16x32_bf16 v[132:135], a[188:191], v[208:211], v[132:135]\n"
    "ds_write_addtid_b32 v172 offset:4096\n"
    "v_mfma_f32_16x16x32_bf16 v[128:131], a[188:191], v[200:203], v[128:131]\n"
    "v_exp_f32_e32 v168, v168\n"
    "v_mfma_f32_16x16x32_bf16 v[128:131], a[184:187], v[196:199], v[128:131]\n"
    "s_sub_u32 s44, s44, 1\n"
    "ds_write_addtid_b32 v173 offset:4352\n"
    "v_mfma_f32_16x16x32_bf16 v[124:127], a[184:187], v[188:191], v[124:127]\n"
    "s_cmp_eq_u32 s44, s80\n"
    "v_exp_f32_e32 v169, v169\n"
    "v_mfma_f32_16x16x32_bf16 v[124:127], a[188:191], v[192:195], v[124:127]\n"
    "s_cselect_b32 s88, s39, s81\n"
    "ds_read_b128 a[180:183], v243 offset:24576\n"
    "v_mfma_f32_16x16x32_bf16 v[120:123], a[188:191], v[184:187], v[120:123]\n"
    "s_cmp_lg_u32 s88, s39\n"
    "ds_write_addtid_b32 v142 offset:512\n"
    "v_mfma_f32_16x16x32_bf16 v[120:123], a[184:187], v[180:183], v[120:123]\n"
    "s_waitcnt lgkmcnt(12)\n"
    "v_cvt_pk_bf16_f32 v159, v162, v163\n"
    "s_cbranch_scc1 .Lat_loop\n"
    "s_cmp_lg_u32 s44, 0\n"
    "s_cbranch_scc1 .Lat_g1\n"
    ".Lat_tail:\n"
    "v_mfma_f32_16x16x32_bf16 v[212:215], a[160:163], a[128:131], v[236:239]\n"
    "v_and_b32_e32 v189, 0x1f0, v250\n"
    "v_lshlrev_b32_e32 v190, 3, v250\n"
    "v_mfma_f32_16x16x32_bf16 v[212:215], a[164:167], a[132:135], v[212:215]\n"
    "ds_read_b128 a[184:187], v242 offset:28672\n"
    "v_lshrrev_b32_e32 v253, 4, v250\n"
    "v_mfma_f32_16x16x32_bf16 v[204:207], a[164:167], a[100:103], v[232:235]\n"
    "s_mov_b32 s52, 6100992\n"
    "ds_read_b128 a[188:191], v243 offset:28672\n"
    "v_bfe_u32 v251, v253, 4, 2\n"
    "v_mfma_f32_16x16x32_bf16 v[204:207], a[160:163], a[96:99], v[204:207]\n"
    "s_lshr_b32 s81, s34, 6\n"
    "v_lshlrev_b32_e32 v251, 1, v251\n"
    "v_xor_b32_e32 v251, v251, v253\n"
    "v_mfma_f32_16x16x32_bf16 v[196:199], a[160:163], a[64:67], v[228:231]\n"
    "s_lshl_b32 s81, s81, 9\n"
    "ds_write_addtid_b32 v143 offset:768\n"
    "v_and_b32_e32 v251, 7, v251\n"
    "v_mfma_f32_16x16x32_bf16 v[196:199], a[164:167], a[68:71], v[196:199]\n"
    "s_waitcnt lgkmcnt(9)\n"
    "v_exp_f32_e32 v170, v170\n"
    "v_mfma_f32_16x16x32_bf16 v[196:199], a[168:171], a[72:75], v[196:199]\n"
    "s_add_u32 s81, s81, 6074368\n"
    "v_exp_f32_e32 v171, v171\n"
    "v_mfma_f32_16x16x32_bf16 v[196:199], a[172:175], a[76:79], v[196:199]\n"
    "s_lshl_b32 s63, s93, 13\n"
    "ds_write_addtid_b32 v150 offset:1536\n"
    "v_cvt_pk_bf16_f32 v166, v168, v169\n"
    "v_mfma_f32_16x16x32_bf16 v[204:207], a[172:175], a[108:111], v[204:207]\n"
    "s_xor_b32 s45, s63, 0x4000\n"
    "ds_read_b128 a[160:163], v240 offset:26624\n"
    "v_lshlrev_b32_e32 v251, 4, v251\n"
    "v_mfma_f32_16x16x32_bf16 v[204:207], a[168:171], a[104:107], v[204:207]\n"
    "s_mul_i32 s58, s93, 33792\n"
    "v_lshrrev_b32_e32 v253, 3, v253\n"
    "v_lshl_add_u32 v251, v253, 9, v251\n"
    "v_mfma_f32_16x16x32_bf16 v[212:215], a[168:171], a[136:139], v[212:215]\n"
    "s_add_u32 s57, s58, 5513216\n"
    "v_bfe_u32 v255, v250, 2, 4\n"
    "v_mov_b32_e32 v254, s39\n"
    "v_mfma_f32_16x16x32_bf16 v[212:215], a[172:175], a[140:143], v[212:215]\n"
    "s_waitcnt lgkmcnt(6)\n"
    "v_exp_f32_e32 v176, v176\n"
    "v_mfma_f32_16x16x32_bf16 v[212:215], a[176:179], a[144:147], v[212:215]\n"
    "s_add_u32 s58, s58, 5378048\n"
    "ds_read_b128 a[168:171], v240 offset:30720\n"
    "v_lshrrev_b32_e32 v252, 2, v250\n"
    "v_mfma_f32_16x16x32_bf16 v[212:215], a[180:183], a[148:151], v[212:215]\n"
    "s_add_u32 s36, s35, 0x8000\n"
    "v_exp_f32_e32 v177, v177\n"
    "v_mfma_f32_16x16x32_bf16 v[204:207], a[180:183], a[116:119], v[204:207]\n"
    "s_add_u32 s37, s35, 0x10000\n"
    "ds_read_b128 a[164:167], v241 offset:26624\n"
    "v_cvt_pk_bf16_f32 v167, v170, v171\n"
    "v_mfma_f32_16x16x32_bf16 v[204:207], a[176:179], a[112:115], v[204:207]\n"
    "s_cmp_lt_u32 s32, 2\n"
    "s_cselect_b32 s37, s37, 2.0\n"
    "ds_write_addtid_b32 v151 offset:1792\n"
    "v_mfma_f32_16x16x32_bf16 v[196:199], a[176:179], a[80:83], v[196:199]\n"
    "buffer_load_dword v253, v255, s[8:11], s52 offen offset:0 sc1\n"
    "v_mfma_f32_16x16x32_bf16 v[196:199], a[180:183], a[84:87], v[196:199]\n"
    "s_waitcnt lgkmcnt(6)\n"
    "ds_write_addtid_b32 v158 offset:2560\n"
    "v_mfma_f32_16x16x32_bf16 v[196:199], a[184:187], a[88:91], v[196:199]\n"
    "ds_write_addtid_b32 v159 offset:2816\n"
    "ds_write_addtid_b32 v166 offset:3584\n"
    "v_mfma_f32_16x16x32_bf16 v[196:199], a[188:191], a[92:95], v[196:199]\n"
    "v_exp_f32_e32 v178, v178\n"
    "v_mfma_f32_16x16x32_bf16 v[204:207], a[188:191], a[124:127], v[204:207]\n"
    "v_exp_f32_e32 v179, v179\n"
    "v_mfma_f32_16x16x32_bf16 v[204:207], a[184:187], a[120:123], v[204:207]\n"
    "v_cvt_pk_bf16_f32 v174, v176, v177\n"
    "v_cvt_pk_bf16_f32 v175, v178, v179\n"
    "v_mfma_f32_16x16x32_bf16 v[212:215], a[184:187], a[152:155], v[212:215]\n"
    "ds_read_b128 a[172:175], v241 offset:30720\n"
    "ds_write_addtid_b32 v167 offset:3840\n"
    "v_mfma_f32_16x16x32_bf16 v[212:215], a[188:191], a[156:159], v[212:215]\n"
    "s_waitcnt lgkmcnt(6)\n"
    "ds_write_addtid_b32 v174 offset:4608\n"
    "ds_write_addtid_b32 v175 offset:4864\n"
    "v_mfma_f32_16x16x32_bf16 v[216:219], a[160:163], a[128:131], v[236:239]\n"
    "ds_read_b128 a[184:187], v242 offset:30720\n"
    "v_mfma_f32_16x16x32_bf16 v[216:219], a[164:167], a[132:135], v[216:219]\n"
    "ds_read_b128 a[176:179], v242 offset:26624\n"
    "v_mfma_f32_16x16x32_bf16 v[208:211], a[164:167], a[100:103], v[232:235]\n"
    "ds_read_b128 a[180:183], v243 offset:26624\n"
    "v_mfma_f32_16x16x32_bf16 v[208:211], a[160:163], a[96:99], v[208:211]\n"
    "s_waitcnt lgkmcnt(3)\n"
    "v_mfma_f32_16x16x32_bf16 v[200:203], a[160:163], a[64:67], v[228:231]\n"
    "s_barrier\n"
    "v_mfma_f32_16x16x32_bf16 v[200:203], a[164:167], a[68:71], v[200:203]\n"
    "ds_read2st64_b32 v[178:179], v249 offset0:18 offset1:19\n"
    "v_mfma_f32_16x16x32_bf16 v[200:203], a[168:171], a[72:75], v[200:203]\n"
    "s_add_u32 m0, s43, 8192\n"
    "ds_read_b128 a[188:191], v243 offset:30720\n"
    "ds_read2st64_b32 v[176:177], v249 offset0:16 offset1:17\n"
    "v_mfma_f32_16x16x32_bf16 v[200:203], a[172:175], a[76:79], v[200:203]\n"
    "buffer_load_dwordx4 v248, s[8:11], s40 offen offset:0 sc1 lds\n"
    "v_mfma_f32_16x16x32_bf16 v[208:211], a[172:175], a[108:111], v[208:211]\n"
    "ds_read_b64_tr_b16 a[160:161], v244 offset:16384\n"
    "ds_read2st64_b32 v[170:171], v249 offset0:14 offset1:15\n"
    "v_mfma_f32_16x16x32_bf16 v[208:211], a[168:171], a[104:107], v[208:211]\n"
    "ds_read2st64_b32 v[168:169], v249 offset0:12 offset1:13\n"
    "v_mfma_f32_16x16x32_bf16 v[216:219], a[168:171], a[136:139], v[216:219]\n"
    "ds_read2st64_b32 v[162:163], v249 offset0:10 offset1:11\n"
    "v_mfma_f32_16x16x32_bf16 v[216:219], a[172:175], a[140:143], v[216:219]\n"
    "s_waitcnt lgkmcnt(7)\n"
    "ds_read_b64_tr_b16 a[162:163], v244 offset:18432\n"
    "v_mfma_f32_16x16x32_bf16 v[216:219], a[176:179], a[144:147], v[216:219]\n"
    "buffer_load_dwordx4 v248, s[8:11], s40 offen offset:1024 sc1 lds\n"
    "v_mfma_f32_16x16x32_bf16 v[216:219], a[180:183], a[148:151], v[216:219]\n"
    "ds_read_b64_tr_b16 a[164:165], v244 offset:49152\n"
    "ds_read_b64_tr_b16 a[166:167], v244 offset:51200\n"
    "v_mfma_f32_16x16x32_bf16 v[208:211], a[180:183], a[116:119], v[208:211]\n"
    "ds_read2st64_b32 v[160:161], v249 offset0:8 offset1:9\n"
    "v_mfma_f32_16x16x32_bf16 v[208:211], a[176:179], a[112:115], v[208:211]\n"
    "ds_read2st64_b32 v[154:155], v249 offset0:6 offset1:7\n"
    "v_mfma_f32_16x16x32_bf16 v[200:203], a[176:179], a[80:83], v[200:203]\n"
    "ds_read2st64_b32 v[152:153], v249 offset0:4 offset1:5\n"
    "v_mfma_f32_16x16x32_bf16 v[200:203], a[180:183], a[84:87], v[200:203]\n"
    "s_waitcnt lgkmcnt(11)\n"
    "ds_read2st64_b32 v[144:145], v249 offset0:0 offset1:1\n"
    "ds_read2st64_b32 v[146:147], v249 offset0:2 offset1:3\n"
    "v_mfma_f32_16x16x32_bf16 v[200:203], a[184:187], a[88:91], v[200:203]\n"
    "buffer_load_dwordx4 v248, s[8:11], s40 offen offset:2048 sc1 lds\n"
    "v_mfma_f32_16x16x32_bf16 v[200:203], a[188:191], a[92:95], v[200:203]\n"
    "ds_read_b64_tr_b16 a[168:169], v245 offset:16384\n"
    "v_mfma_f32_16x16x32_bf16 v[208:211], a[188:191], a[124:127], v[208:211]\n"
    "ds_read_b64_tr_b16 a[170:171], v245 offset:18432\n"
    "v_mfma_f32_16x16x32_bf16 v[208:211], a[184:187], a[120:123], v[208:211]\n"
    "v_mfma_f32_16x16x32_bf16 v[216:219], a[184:187], a[152:155], v[216:219]\n"
    "ds_read_b64_tr_b16 a[176:177], v246 offset:16384\n"
    "v_mfma_f32_16x16x32_bf16 v[216:219], a[188:191], a[156:159], v[216:219]\n"
    "s_waitcnt lgkmcnt(3)\n"
    "v_exp_f32_e32 v196, v196\n"
    "v_mfma_f32_16x16x32_bf16 a[252:255], a[160:163], v[172:175], a[252:255]\n"
    "buffer_load_dwordx4 v248, s[8:11], s40 offen offset:3072 sc1 lds\n"
    "v_mfma_f32_16x16x32_bf16 a[252:255], a[164:167], v[176:179], a[252:255]\n"
    "s_add_u32 s40, s40, 0x8000\n"
    "ds_read_b64_tr_b16 a[172:173], v245 offset:49152\n"
    "v_mfma_f32_16x16x32_bf16 a[248:251], a[164:167], v[168:171], a[248:251]\n"
    "s_add_u32 m0, s43, 12288\n"
    "ds_read_b64_tr_b16 a[174:175], v245 offset:51200\n"
    "v_mfma_f32_16x16x32_bf16 a[248:251], a[160:163], v[164:167], a[248:251]\n"
    "v_exp_f32_e32 v197, v197\n"
    "v_mfma_f32_16x16x32_bf16 a[244:247], a[160:163], v[156:159], a[244:247]\n"
    "ds_read_b64_tr_b16 a[178:179], v246 offset:18432\n"
    "v_mfma_f32_16x16x32_bf16 a[244:247], a[164:167], v[160:163], a[244:247]\n"
    "buffer_load_dwordx4 v248, s[8:11], s41 offen offset:0 sc1 lds\n"
    "v_mfma_f32_16x16x32_bf16 a[240:243], a[164:167], v[152:155], a[240:243]\n"
    "ds_read_b64_tr_b16 a[180:181], v246 offset:49152\n"
    "v_mfma_f32_16x16x32_bf16 a[240:243], a[160:163], v[148:151], a[240:243]\n"
    "ds_read_b64_tr_b16 a[184:185], v247 offset:16384\n"
    "v_mfma_f32_16x16x32_bf16 a[236:239], a[160:163], v[140:143], a[236:239]\n"
    "ds_read_b64_tr_b16 a[186:187], v247 offset:18432\n"
    "v_mfma_f32_16x16x32_bf16 a[236:239], a[164:167], v[144:147], a[236:239]\n"
    "s_waitcnt lgkmcnt(4)\n"
    "v_exp_f32_e32 v198, v198\n"
    "v_mfma_f32_16x16x32_bf16 v[0:3], a[168:171], v[140:143], v[0:3]\n"
    "buffer_load_dwordx4 v248, s[8:11], s41 offen offset:1024 sc1 lds\n"
    "v_mfma_f32_16x16x32_bf16 v[0:3], a[172:175], v[144:147], v[0:3]\n"
    "v_exp_f32_e32 v199, v199\n"
    "v_mfma_f32_16x16x32_bf16 v[4:7], a[172:175], v[152:155], v[4:7]\n"
    "ds_read_b64_tr_b16 a[182:183], v246 offset:51200\n"
    "v_cvt_pk_bf16_f32 v196, v196, v197\n"
    "v_mfma_f32_16x16x32_bf16 v[4:7], a[168:171], v[148:151], v[4:7]\n"
    "ds_read_b64_tr_b16 a[188:189], v247 offset:49152\n"
    "v_mfma_f32_16x16x32_bf16 v[8:11], a[168:171], v[156:159], v[8:11]\n"
    "v_exp_f32_e32 v204, v204\n"
    "v_mfma_f32_16x16x32_bf16 v[8:11], a[172:175], v[160:163], v[8:11]\n"
    "buffer_load_dwordx4 v248, s[8:11], s41 offen offset:2048 sc1 lds\n"
    "v_mfma_f32_16x16x32_bf16 v[12:15], a[172:175], v[168:171], v[12:15]\n"
    "ds_read_b64_tr_b16 a[190:191], v247 offset:51200\n"
    "v_mfma_f32_16x16x32_bf16 v[12:15], a[168:171], v[164:167], v[12:15]\n"
    "s_waitcnt vmcnt(7)\n"
    "v_mfma_f32_16x16x32_bf16 v[16:19], a[168:171], v[172:175], v[16:19]\n"
    "s_barrier\n"
    "v_mfma_f32_16x16x32_bf16 v[16:19], a[172:175], v[176:179], v[16:19]\n"
    "s_waitcnt lgkmcnt(2)\n"
    "v_add_u32_e32 v249, s45, v252\n"
    "v_add_u32_e32 v252, s63, v252\n"
    "v_mfma_f32_16x16x32_bf16 v[36:39], a[176:179], v[172:175], v[36:39]\n"
    "buffer_store_dword v254, off, s[8:11], s84 offset:0 sc1\n"
    "v_mfma_f32_16x16x32_bf16 v[36:39], a[180:183], v[176:179], v[36:39]\n"
    "s_mul_i32 s45, s49, 63\n"
    "ds_read_b64_tr_b16 a[160:161], v244 offset:20480\n"
    "v_mfma_f32_16x16x32_bf16 v[32:35], a[180:183], v[168:171], v[32:35]\n"
    "s_add_u32 s45, s45, s33\n"
    "v_exp_f32_e32 v205, v205\n"
    "v_mfma_f32_16x16x32_bf16 v[32:35], a[176:179], v[164:167], v[32:35]\n"
    "buffer_load_dwordx4 v248, s[8:11], s41 offen offset:3072 sc1 lds\n"
    "v_mfma_f32_16x16x32_bf16 v[28:31], a[176:179], v[156:159], v[28:31]\n"
    "s_add_u32 s41, s41, 0x8000\n"
    "ds_read_b64_tr_b16 a[162:163], v244 offset:22528\n"
    "v_cmp_ne_u32_e32 vcc, s39, v253\n"
    "v_mfma_f32_16x16x32_bf16 v[28:31], a[180:183], v[160:163], v[28:31]\n"
    "s_mov_b32 m0, s63\n"
    "ds_read_b64_tr_b16 a[168:169], v245 offset:20480\n"
    "v_mfma_f32_16x16x32_bf16 v[24:27], a[180:183], v[152:155], v[24:27]\n"
    "s_mul_i32 s45, s45, 405504\n"
    "ds_read_b64_tr_b16 a[164:165], v244 offset:53248\n"
    "v_mfma_f32_16x16x32_bf16 v[24:27], a[176:179], v[148:151], v[24:27]\n"
    "s_cbranch_vccnz .Lat_wpoll\n"
    ".Lat_wpoll_ret:\n"
    "v_mfma_f32_16x16x32_bf16 v[20:23], a[176:179], v[140:143], v[20:23]\n"
    "s_add_u32 s62, s57, 0\n"
    "ds_read_b64_tr_b16 a[166:167], v244 offset:55296\n"
    "v_mfma_f32_16x16x32_bf16 v[20:23], a[180:183], v[144:147], v[20:23]\n"
    "s_waitcnt lgkmcnt(5)\n"
    "ds_read_b64_tr_b16 a[170:171], v245 offset:22528\n"
    "v_cvt_pk_bf16_f32 v197, v198, v199\n"
    "v_mfma_f32_16x16x32_bf16 v[40:43], a[184:187], v[140:143], v[40:43]\n"
    "buffer_load_dwordx4 a[216:219], v250, s[8:11], s62 offen offset:0 sc1\n"
    "v_mfma_f32_16x16x32_bf16 v[40:43], a[188:191], v[144:147], v[40:43]\n"
    "s_add_u32 s45, s45, 16588800\n"
    "ds_read_b64_tr_b16 a[176:177], v246 offset:20480\n"
    "v_mfma_f32_16x16x32_bf16 v[44:47], a[188:191], v[152:155], v[44:47]\n"
    "s_lshl_b32 s46, s51, 12\n"
    "ds_read_b64_tr_b16 a[178:179], v246 offset:22528\n"
    "v_mfma_f32_16x16x32_bf16 v[44:47], a[184:187], v[148:151], v[44:47]\n"
    "s_lshl_b32 s48, s50, 15\n"
    "ds_read_b64_tr_b16 a[180:181], v246 offset:53248\n"
    "v_mfma_f32_16x16x32_bf16 v[48:51], a[184:187], v[156:159], v[48:51]\n"
    "s_add_u32 s46, s46, s48\n"
    "v_exp_f32_e32 v206, v206\n"
    "v_mfma_f32_16x16x32_bf16 v[48:51], a[188:191], v[160:163], v[48:51]\n"
    "buffer_load_dwordx4 a[220:223], v250, s[8:11], s62 offen offset:1024 sc1\n"
    "v_mfma_f32_16x16x32_bf16 v[52:55], a[188:191], v[168:171], v[52:55]\n"
    "s_cmp_eq_u32 s51, 0\n"
    "s_cselect_b32 s47, 2.0, 0\n"
    "ds_read_b64_tr_b16 a[172:173], v245 offset:53248\n"
    "v_mfma_f32_16x16x32_bf16 v[52:55], a[184:187], v[164:167], v[52:55]\n"
    "s_add_u32 s47, s47, 98304\n"
    "ds_read_b64_tr_b16 a[174:175], v245 offset:55296\n"
    "v_mfma_f32_16x16x32_bf16 v[56:59], a[184:187], v[172:175], v[56:59]\n"
    "s_lshl_b32 s61, s50, 9\n"
    "v_exp_f32_e32 v207, v207\n"
    "v_mfma_f32_16x16x32_bf16 v[56:59], a[188:191], v[176:179], v[56:59]\n"
    "s_waitcnt lgkmcnt(6)\n"
    "ds_read_b64_tr_b16 a[184:185], v247 offset:20480\n"
    "v_mfma_f32_16x16x32_bf16 v[76:79], a[160:163], v[172:175], v[76:79]\n"
    "buffer_load_dwordx4 a[224:227], v250, s[8:11], s62 offen offset:2048 sc1\n"
    "v_mfma_f32_16x16x32_bf16 v[76:79], a[164:167], v[176:179], v[76:79]\n"
    "s_add_u32 s61, s61, s47\n"
    "ds_write_addtid_b32 v196 offset:0\n"
    "v_cvt_pk_bf16_f32 v204, v204, v205\n"
    "v_mfma_f32_16x16x32_bf16 v[72:75], a[164:167], v[168:171], v[72:75]\n"
    "s_add_u32 s48, s32, 3\n"
    "v_exp_f32_e32 v212, v212\n"
    "v_mfma_f32_16x16x32_bf16 v[72:75], a[160:163], v[164:167], v[72:75]\n"
    "s_and_b32 s48, s48, 3\n"
    "ds_read_b64_tr_b16 a[182:183], v246 offset:55296\n"
    "v_mfma_f32_16x16x32_bf16 v[68:71], a[160:163], v[156:159], v[68:71]\n"
    "s_mul_i32 s48, s48, 101376\n"
    "ds_read_b64_tr_b16 a[186:187], v247 offset:22528\n"
    "v_mfma_f32_16x16x32_bf16 v[68:71], a[164:167], v[160:163], v[68:71]\n"
    "buffer_load_dwordx4 a[228:231], v250, s[8:11], s62 offen offset:3072 sc1\n"
    "v_mfma_f32_16x16x32_bf16 v[64:67], a[164:167], v[152:155], v[64:67]\n"
    "s_add_u32 s62, s57, 4096\n"
    "ds_write_addtid_b32 v197 offset:256\n"
    "v_mfma_f32_16x16x32_bf16 v[64:67], a[160:163], v[148:151], v[64:67]\n"
    "s_add_u32 s48, s48, s45\n"
    "v_exp_f32_e32 v213, v213\n"
    "v_mfma_f32_16x16x32_bf16 v[60:63], a[160:163], v[140:143], v[60:63]\n"
    "s_add_u32 s29, s48, s46\n"
    "ds_read_b64_tr_b16 a[188:189], v247 offset:53248\n"
    "v_cvt_pk_bf16_f32 v205, v206, v207\n"
    "v_mfma_f32_16x16x32_bf16 v[60:63], a[164:167], v[144:147], v[60:63]\n"
    "s_waitcnt lgkmcnt(6)\n"
    "v_exp_f32_e32 v214, v214\n"
    "v_mfma_f32_16x16x32_bf16 v[80:83], a[168:171], v[140:143], v[80:83]\n"
    "buffer_load_dwordx4 a[232:235], v250, s[8:11], s62 offen offset:0 sc1\n"
    "v_mfma_f32_16x16x32_bf16 v[80:83], a[172:175], v[144:147], v[80:83]\n"
    "s_add_u32 s29, s29, 8192\n"
    "v_exp_f32_e32 v215, v215\n"
    "v_mfma_f32_16x16x32_bf16 v[84:87], a[172:175], v[152:155], v[84:87]\n"
    "s_add_u32 s89, s48, s61\n"
    "ds_read_b64_tr_b16 a[190:191], v247 offset:55296\n"
    "v_mfma_f32_16x16x32_bf16 v[84:87], a[168:171], v[148:151], v[84:87]\n"
    "s_add_u32 s89, s89, 128\n"
    "ds_write_addtid_b32 v204 offset:1024\n"
    "v_cvt_pk_bf16_f32 v212, v212, v213\n"
    "v_mfma_f32_16x16x32_bf16 v[88:91], a[168:171], v[156:159], v[88:91]\n"
    "s_add_u32 s48, s32, 2\n"
    "ds_read_b128 a[160:163], v240 offset:0\n"
    "v_mfma_f32_16x16x32_bf16 v[88:91], a[172:175], v[160:163], v[88:91]\n"
    "v_mfma_f32_16x16x32_bf16 v[92:95], a[172:175], v[168:171], v[92:95]\n"
    "s_barrier\n"
    "v_mfma_f32_16x16x32_bf16 v[92:95], a[168:171], v[164:167], v[92:95]\n"
    "buffer_load_dwordx4 v[192:195], v250, s[8:11], s62 offen offset:1024 sc1\n"
    "v_mfma_f32_16x16x32_bf16 v[96:99], a[168:171], v[172:175], v[96:99]\n"
    "s_add_u32 m0, s43, 16384\n"
    "ds_read_b128 a[164:167], v241 offset:0\n"
    "v_mfma_f32_16x16x32_bf16 v[96:99], a[172:175], v[176:179], v[96:99]\n"
    "s_waitcnt lgkmcnt(7)\n"
    "v_exp_f32_e32 v200, v200\n"
    "v_mfma_f32_16x16x32_bf16 v[116:119], a[176:179], v[172:175], v[116:119]\n"
    "buffer_load_dwordx4 v248, s[8:11], s40 offen offset:0 sc1 lds\n"
    "v_mfma_f32_16x16x32_bf16 v[116:119], a[180:183], v[176:179], v[116:119]\n"
    "s_mov_b32 m0, s63\n"
    "v_mfma_f32_16x16x32_bf16 v[112:115], a[180:183], v[168:171], v[112:115]\n"
    "s_and_b32 s48, s48, 3\n"
    "v_exp_f32_e32 v201, v201\n"
    "v_mfma_f32_16x16x32_bf16 v[112:115], a[176:179], v[164:167], v[112:115]\n"
    "s_mul_i32 s48, s48, 101376\n"
    "ds_read_b128 a[168:171], v240 offset:4096\n"
    "v_cvt_pk_bf16_f32 v213, v214, v215\n"
    "v_mfma_f32_16x16x32_bf16 v[108:111], a[176:179], v[156:159], v[108:111]\n"
    "s_add_u32 m0, s43, 16384\n"
    "v_exp_f32_e32 v202, v202\n"
    "v_mfma_f32_16x16x32_bf16 v[108:111], a[180:183], v[160:163], v[108:111]\n"
    "buffer_load_dwordx4 v248, s[8:11], s40 offen offset:1024 sc1 lds\n"
    "v_mfma_f32_16x16x32_bf16 v[104:107], a[180:183], v[152:155], v[104:107]\n"
    "s_mov_b32 m0, s63\n"
    "v_exp_f32_e32 v203, v203\n"
    "v_mfma_f32_16x16x32_bf16 v[104:107], a[176:179], v[148:151], v[104:107]\n"
    "s_add_u32 s48, s48, s45\n"
    "ds_write_addtid_b32 v205 offset:1280\n"
    "v_cvt_pk_bf16_f32 v198, v200, v201\n"
    "v_mfma_f32_16x16x32_bf16 v[100:103], a[176:179], v[140:143], v[100:103]\n"
    "s_add_u32 s28, s48, s46\n"
    "v_exp_f32_e32 v208, v208\n"
    "v_mfma_f32_16x16x32_bf16 v[100:103], a[180:183], v[144:147], v[100:103]\n"
    "s_waitcnt lgkmcnt(5)\n"
    "ds_write_addtid_b32 v212 offset:2048\n"
    "v_mfma_f32_16x16x32_bf16 v[120:123], a[184:187], v[140:143], v[120:123]\n"
    "s_add_u32 m0, s43, 16384\n"
    "v_exp_f32_e32 v209, v209\n"
    "v_mfma_f32_16x16x32_bf16 v[120:123], a[188:191], v[144:147], v[120:123]\n"
    "buffer_load_dwordx4 v248, s[8:11], s40 offen offset:2048 sc1 lds\n"
    "v_mfma_f32_16x16x32_bf16 v[124:127], a[188:191], v[152:155], v[124:127]\n"
    "s_mov_b32 m0, s63\n"
    "ds_read_b128 a[172:175], v241 offset:4096\n"
    "v_mfma_f32_16x16x32_bf16 v[124:127], a[184:187], v[148:151], v[124:127]\n"
    "s_add_u32 s28, s28, 16384\n"
    "ds_read_b128 a[176:179], v242 offset:0\n"
    "v_cvt_pk_bf16_f32 v199, v202, v203\n"
    "v_mfma_f32_16x16x32_bf16 v[128:131], a[184:187], v[156:159], v[128:131]\n"
    "s_add_u32 s88, s48, s61\n"
    "v_exp_f32_e32 v210, v210\n"
    "v_mfma_f32_16x16x32_bf16 v[128:131], a[188:191], v[160:163], v[128:131]\n"
    "s_add_u32 m0, s43, 16384\n"
    "v_exp_f32_e32 v211, v211\n"
    "v_mfma_f32_16x16x32_bf16 v[132:135], a[188:191], v[168:171], v[132:135]\n"
    "buffer_load_dwordx4 v248, s[8:11], s40 offen offset:3072 sc1 lds\n"
    "v_mfma_f32_16x16x32_bf16 v[132:135], a[184:187], v[164:167], v[132:135]\n"
    "s_mov_b32 m0, s63\n"
    "ds_read_b128 a[180:183], v243 offset:0\n"
    "v_mfma_f32_16x16x32_bf16 v[136:139], a[184:187], v[172:175], v[136:139]\n"
    "s_add_u32 s88, s88, 256\n"
    "ds_write_addtid_b32 v213 offset:2304\n"
    "v_mfma_f32_16x16x32_bf16 v[136:139], a[188:191], v[176:179], v[136:139]\n"
    "s_waitcnt lgkmcnt(7)\n"
    "v_cvt_pk_bf16_f32 v206, v208, v209\n"
    "v_mfma_f32_16x16x32_bf16 v[172:175], a[160:163], a[128:131], v[236:239]\n"
    "s_add_u32 m0, s43, 20480\n"
    "v_exp_f32_e32 v216, v216\n"
    "v_mfma_f32_16x16x32_bf16 v[172:175], a[164:167], a[132:135], v[172:175]\n"
    "buffer_load_dwordx4 v248, s[8:11], s41 offen offset:0 sc1 lds\n"
    "v_mfma_f32_16x16x32_bf16 v[164:167], a[164:167], a[100:103], v[232:235]\n"
    "s_mov_b32 m0, s63\n"
    "v_exp_f32_e32 v217, v217\n"
    "v_mfma_f32_16x16x32_bf16 v[164:167], a[160:163], a[96:99], v[164:167]\n"
    "s_add_u32 s48, s32, 1\n"
    "ds_write_addtid_b32 v198 offset:512\n"
    "v_mfma_f32_16x16x32_bf16 v[156:159], a[160:163], a[64:67], v[228:231]\n"
    "s_and_b32 s48, s48, 3\n"
    "ds_read_b128 a[184:187], v242 offset:4096\n"
    "v_mfma_f32_16x16x32_bf16 v[156:159], a[164:167], a[68:71], v[156:159]\n"
    "s_waitcnt lgkmcnt(5)\n"
    "ds_write_addtid_b32 v199 offset:768\n"
    "v_mfma_f32_16x16x32_bf16 v[156:159], a[168:171], a[72:75], v[156:159]\n"
    "s_add_u32 m0, s43, 20480\n"
    "ds_read_b128 a[188:191], v243 offset:4096\n"
    "v_cvt_pk_bf16_f32 v207, v210, v211\n"
    "v_mfma_f32_16x16x32_bf16 v[156:159], a[172:175], a[76:79], v[156:159]\n"
    "buffer_load_dwordx4 v248, s[8:11], s41 offen offset:1024 sc1 lds\n"
    "v_mfma_f32_16x16x32_bf16 v[164:167], a[172:175], a[108:111], v[164:167]\n"
    "s_mov_b32 m0, s63\n"
    "v_exp_f32_e32 v218, v218\n"
    "v_mfma_f32_16x16x32_bf16 v[164:167], a[168:171], a[104:107], v[164:167]\n"
    "s_mul_i32 s48, s48, 101376\n"
    "v_exp_f32_e32 v219, v219\n"
    "v_mfma_f32_16x16x32_bf16 v[172:175], a[168:171], a[136:139], v[172:175]\n"
    "s_add_u32 s48, s48, s45\n"
    "ds_write_addtid_b32 v206 offset:1536\n"
    "v_cvt_pk_bf16_f32 v214, v216, v217\n"
    "v_mfma_f32_16x16x32_bf16 v[172:175], a[172:175], a[140:143], v[172:175]\n"
    "s_waitcnt lgkmcnt(6)\n"
    "ds_read_b128 a[160:163], v240 offset:2048\n"
    "v_cvt_pk_bf16_f32 v215, v218, v219\n"
    "v_mfma_f32_16x16x32_bf16 v[172:175], a[176:179], a[144:147], v[172:175]\n"
    "s_add_u32 m0, s43, 20480\n"
    "ds_read_b128 a[168:171], v240 offset:6144\n"
    "v_mfma_f32_16x16x32_bf16 v[172:175], a[180:183], a[148:151], v[172:175]\n"
    "buffer_load_dwordx4 v248, s[8:11], s41 offen offset:2048 sc1 lds\n"
    "v_mfma_f32_16x16x32_bf16 v[164:167], a[180:183], a[116:119], v[164:167]\n"
    "s_mov_b32 m0, s63\n"
    "ds_read_b128 a[164:167], v241 offset:2048\n"
    "v_mfma_f32_16x16x32_bf16 v[164:167], a[176:179], a[112:115], v[164:167]\n"
    "s_add_u32 s82, s48, s46\n"
    "ds_write_addtid_b32 v207 offset:1792\n"
    "v_mfma_f32_16x16x32_bf16 v[156:159], a[176:179], a[80:83], v[156:159]\n"
    "s_add_u32 s82, s82, 24576\n"
    "ds_write_addtid_b32 v214 offset:2560\n"
    "v_mfma_f32_16x16x32_bf16 v[156:159], a[180:183], a[84:87], v[156:159]\n"
    "s_waitcnt lgkmcnt(6)\n"
    "ds_write_addtid_b32 v215 offset:2816\n"
    "v_mfma_f32_16x16x32_bf16 v[156:159], a[184:187], a[88:91], v[156:159]\n"
    "s_add_u32 m0, s43, 20480\n"
    "ds_read_b128 a[176:179], v242 offset:2048\n"
    "v_mfma_f32_16x16x32_bf16 v[156:159], a[188:191], a[92:95], v[156:159]\n"
    "buffer_load_dwordx4 v248, s[8:11], s41 offen offset:3072 sc1 lds\n"
    "v_mfma_f32_16x16x32_bf16 v[164:167], a[188:191], a[124:127], v[164:167]\n"
    "s_mov_b32 m0, s63\n"
    "ds_read_b128 a[180:183], v243 offset:2048\n"
    "v_mfma_f32_16x16x32_bf16 v[164:167], a[184:187], a[120:123], v[164:167]\n"
    "s_add_u32 s87, s48, s61\n"
    "v_exp_f32_e32 v156, v156\n"
    "v_mfma_f32_16x16x32_bf16 v[172:175], a[184:187], a[152:155], v[172:175]\n"
    "s_add_u32 s87, s87, 384\n"
    "ds_read_b128 a[172:175], v241 offset:6144\n"
    "v_mfma_f32_16x16x32_bf16 v[172:175], a[188:191], a[156:159], v[172:175]\n"
    "s_waitcnt lgkmcnt(6)\n"
    "ds_read_b128 a[184:187], v242 offset:6144\n"
    "ds_read_b128 a[188:191], v243 offset:6144\n"
    "v_mfma_f32_16x16x32_bf16 v[176:179], a[160:163], a[128:131], v[236:239]\n"
    "s_mul_i32 s48, s32, 101376\n"
    "v_exp_f32_e32 v157, v157\n"
    "v_mfma_f32_16x16x32_bf16 v[176:179], a[164:167], a[132:135], v[176:179]\n"
    "s_add_u32 s48, s48, s45\n"
    "v_exp_f32_e32 v158, v158\n"
    "v_mfma_f32_16x16x32_bf16 v[168:171], a[164:167], a[100:103], v[232:235]\n"
    "s_add_u32 s59, s48, s46\n"
    "v_exp_f32_e32 v159, v159\n"
    "v_mfma_f32_16x16x32_bf16 v[168:171], a[160:163], a[96:99], v[168:171]\n"
    "s_waitcnt lgkmcnt(5)\n"
    "s_waitcnt vmcnt(14)\n"
    "v_mfma_f32_16x16x32_bf16 v[160:163], a[160:163], a[64:67], v[228:231]\n"
    "s_barrier\n"
    "v_mfma_f32_16x16x32_bf16 v[160:163], a[164:167], a[68:71], v[160:163]\n"
    "s_waitcnt lgkmcnt(2)\n"
    "ds_read2st64_b32 a[196:197], v249 offset0:0 offset1:1\n"
    "ds_read2st64_b32 a[198:199], v249 offset0:2 offset1:3\n"
    "v_mfma_f32_16x16x32_bf16 v[160:163], a[168:171], a[72:75], v[160:163]\n"
    "s_add_u32 s60, s48, s61\n"
    "ds_read_b128 a[160:163], v240 offset:8192\n"
    "v_cvt_pk_bf16_f32 v156, v156, v157\n"
    "v_mfma_f32_16x16x32_bf16 v[160:163], a[172:175], a[76:79], v[160:163]\n"
    "s_and_b32 s48, s32, 1\n"
    "ds_read_b128 a[164:167], v241 offset:8192\n"
    "v_mfma_f32_16x16x32_bf16 v[168:171], a[172:175], a[108:111], v[168:171]\n"
    "s_xor_b32 s48, s48, s50\n"
    "v_exp_f32_e32 v164, v164\n"
    "v_mfma_f32_16x16x32_bf16 v[168:171], a[168:171], a[104:107], v[168:171]\n"
    "s_sub_u32 s61, s32, s48\n"
    "v_exp_f32_e32 v165, v165\n"
    "v_mfma_f32_16x16x32_bf16 v[176:179], a[168:171], a[136:139], v[176:179]\n"
    "s_and_b32 s61, s61, 3\n"
    "ds_read2st64_b32 a[192:193], v252 offset0:0 offset1:1\n"
    "v_cvt_pk_bf16_f32 v157, v158, v159\n"
    "v_mfma_f32_16x16x32_bf16 v[176:179], a[172:175], a[140:143], v[176:179]\n"
    "v_exp_f32_e32 v166, v166\n"
    "v_mfma_f32_16x16x32_bf16 v[176:179], a[176:179], a[144:147], v[176:179]\n"
    "s_add_u32 s61, s61, 8\n"
    "ds_read_b128 a[168:171], v240 offset:12288\n"
    "ds_read2st64_b32 a[194:195], v252 offset0:2 offset1:3\n"
    "v_mfma_f32_16x16x32_bf16 v[176:179], a[180:183], a[148:151], v[176:179]\n"
    "s_mul_i32 s48, s48, 101376\n"
    "v_exp_f32_e32 v167, v167\n"
    "v_mfma_f32_16x16x32_bf16 v[168:171], a[180:183], a[116:119], v[168:171]\n"
    "s_add_u32 s48, s48, s45\n"
    "ds_read_b128 a[172:175], v241 offset:12288\n"
    "v_cvt_pk_bf16_f32 v164, v164, v165\n"
    "v_mfma_f32_16x16x32_bf16 v[168:171], a[176:179], a[112:115], v[168:171]\n"
    "s_lshl_b32 s80, s61, 13\n"
    "v_exp_f32_e32 v172, v172\n"
    "v_mfma_f32_16x16x32_bf16 v[160:163], a[176:179], a[80:83], v[160:163]\n"
    "s_add_u32 s80, s80, s48\n"
    "v_exp_f32_e32 v173, v173\n"
    "v_mfma_f32_16x16x32_bf16 v[160:163], a[180:183], a[84:87], v[160:163]\n"
    "s_waitcnt lgkmcnt(8)\n"
    "ds_read_b128 a[176:179], v242 offset:8192\n"
    "v_cvt_pk_bf16_f32 v165, v166, v167\n"
    "v_mfma_f32_16x16x32_bf16 v[160:163], a[184:187], a[88:91], v[160:163]\n"
    "s_lshl_b32 s46, s51, 12\n"
    "ds_read2st64_b32 a[204:205], v249 offset0:4 offset1:5\n"
    "v_mfma_f32_16x16x32_bf16 v[160:163], a[188:191], a[92:95], v[160:163]\n"
    "s_add_u32 s80, s80, s46\n"
    "v_exp_f32_e32 v174, v174\n"
    "v_mfma_f32_16x16x32_bf16 v[168:171], a[188:191], a[124:127], v[168:171]\n"
    "s_lshl_b32 s86, s61, 7\n"
    "ds_read_b128 a[180:183], v243 offset:8192\n"
    "ds_read2st64_b32 a[206:207], v249 offset0:6 offset1:7\n"
    "v_mfma_f32_16x16x32_bf16 v[168:171], a[184:187], a[120:123], v[168:171]\n"
    "s_add_u32 s86, s86, s48\n"
    "v_exp_f32_e32 v175, v175\n"
    "v_mfma_f32_16x16x32_bf16 v[176:179], a[184:187], a[152:155], v[176:179]\n"
    "s_add_u32 s86, s86, s47\n"
    "ds_write_addtid_b32 v156 offset:3072\n"
    "v_cvt_pk_bf16_f32 v172, v172, v173\n"
    "v_mfma_f32_16x16x32_bf16 v[176:179], a[188:191], a[156:159], v[176:179]\n"
    "s_waitcnt lgkmcnt(9)\n"
    "ds_write_addtid_b32 v157 offset:3328\n"
    "ds_write_addtid_b32 v164 offset:4096\n"
    "v_mfma_f32_16x16x32_bf16 v[180:183], a[160:163], a[128:131], v[236:239]\n"
    "s_lshl_b32 s46, s93, 7\n"
    "v_exp_f32_e32 v160, v160\n"
    "v_mfma_f32_16x16x32_bf16 v[180:183], a[164:167], a[132:135], v[180:183]\n"
    "s_lshl_b32 s55, s93, 15\n"
    "ds_read_b128 a[184:187], v242 offset:12288\n"
    "ds_read_b128 a[188:191], v243 offset:12288\n"
    "v_mfma_f32_16x16x32_bf16 v[148:151], a[164:167], a[100:103], v[232:235]\n"
    "s_sub_u32 s54, s55, 24576\n"
    "v_exp_f32_e32 v161, v161\n"
    "v_mfma_f32_16x16x32_bf16 v[148:151], a[160:163], a[96:99], v[148:151]\n"
    "s_add_u32 s55, s55, 32768\n"
    "ds_read2st64_b32 a[200:201], v252 offset0:4 offset1:5\n"
    "v_cvt_pk_bf16_f32 v173, v174, v175\n"
    "v_mfma_f32_16x16x32_bf16 v[140:143], a[160:163], a[64:67], v[228:231]\n"
    "s_lshl2_add_u32 s38, s34, s93\n"
    "ds_write_addtid_b32 v165 offset:4352\n"
    "v_exp_f32_e32 v162, v162\n"
    "v_mfma_f32_16x16x32_bf16 v[140:143], a[164:167], a[68:71], v[140:143]\n"
    "s_waitcnt lgkmcnt(11)\n"
    "v_exp_f32_e32 v163, v163\n"
    "v_mfma_f32_16x16x32_bf16 v[140:143], a[168:171], a[72:75], v[140:143]\n"
    "s_lshl1_add_u32 s38, s38, s49\n"
    "ds_read2st64_b32 a[202:203], v252 offset0:6 offset1:7\n"
    "v_cvt_pk_bf16_f32 v158, v160, v161\n"
    "v_mfma_f32_16x16x32_bf16 v[140:143], a[172:175], a[76:79], v[140:143]\n"
    "s_lshl_b32 s38, s38, 7\n"
    "v_exp_f32_e32 v168, v168\n"
    "v_mfma_f32_16x16x32_bf16 v[148:151], a[172:175], a[108:111], v[148:151]\n"
    "s_add_u32 s38, s38, 5648384\n"
    "v_exp_f32_e32 v169, v169\n"
    "v_mfma_f32_16x16x32_bf16 v[148:151], a[168:171], a[104:107], v[148:151]\n"
    "s_lshl3_add_u32 s90, s34, s49\n"
    "ds_read_b128 a[160:163], v240 offset:10240\n"
    "v_cvt_pk_bf16_f32 v159, v162, v163\n"
    "v_mfma_f32_16x16x32_bf16 v[180:183], a[168:171], a[136:139], v[180:183]\n"
    "s_lshl2_add_u32 s90, s90, s93\n"
    "ds_read2st64_b32 a[212:213], v249 offset0:8 offset1:9\n"
    "v_exp_f32_e32 v170, v170\n"
    "v_mfma_f32_16x16x32_bf16 v[180:183], a[172:175], a[140:143], v[180:183]\n"
    "s_waitcnt lgkmcnt(11)\n"
    "v_exp_f32_e32 v171, v171\n"
    "v_mfma_f32_16x16x32_bf16 v[180:183], a[176:179], a[144:147], v[180:183]\n"
    "s_lshl2_add_u32 s90, s90, 6041600\n"
    "ds_read_b128 a[164:167], v241 offset:10240\n"
    "ds_read2st64_b32 a[214:215], v249 offset0:10 offset1:11\n"
    "v_mfma_f32_16x16x32_bf16 v[180:183], a[180:183], a[148:151], v[180:183]\n"
    "s_lshl_b32 s45, s51, 15\n"
    "ds_read_b128 a[168:171], v240 offset:14336\n"
    "v_cvt_pk_bf16_f32 v166, v168, v169\n"
    "v_mfma_f32_16x16x32_bf16 v[148:151], a[180:183], a[116:119], v[148:151]\n"
    "s_sub_u32 s45, 24576, s45\n"
    "v_exp_f32_e32 v176, v176\n"
    "v_mfma_f32_16x16x32_bf16 v[148:151], a[176:179], a[112:115], v[148:151]\n"
    "s_add_u32 s47, s46, 6103040\n"
    "v_exp_f32_e32 v177, v177\n"
    "v_mfma_f32_16x16x32_bf16 v[140:143], a[176:179], a[80:83], v[140:143]\n"
    "s_andn2_b32 s48, s38, 0xc00\n"
    "ds_write_addtid_b32 v172 offset:5120\n"
    "v_cvt_pk_bf16_f32 v167, v170, v171\n"
    "v_mfma_f32_16x16x32_bf16 v[140:143], a[180:183], a[84:87], v[140:143]\n"
    "s_waitcnt lgkmcnt(9)\n"
    "ds_read_b128 a[176:179], v242 offset:10240\n"
    "v_exp_f32_e32 v178, v178\n"
    "v_mfma_f32_16x16x32_bf16 v[140:143], a[184:187], a[88:91], v[140:143]\n"
    "s_andn2_b32 s61, s90, 0x18c\n"
    "ds_write_addtid_b32 v173 offset:5376\n"
    "ds_write_addtid_b32 v158 offset:3584\n"
    "v_mfma_f32_16x16x32_bf16 v[140:143], a[188:191], a[92:95], v[140:143]\n"
    "buffer_load_dword v188, v189, s[8:11], s81 offen offset:0 sc1\n"
    "ds_write_addtid_b32 v159 offset:3840\n"
    "v_mfma_f32_16x16x32_bf16 v[148:151], a[188:191], a[124:127], v[148:151]\n"
    "s_add_u32 s31, s46, s35\n"
    "v_exp_f32_e32 v179, v179\n"
    "v_mfma_f32_16x16x32_bf16 v[148:151], a[184:187], a[120:123], v[148:151]\n"
    "s_add_u32 m0, s63, 6144\n"
    "ds_read_b128 a[172:175], v241 offset:14336\n"
    "v_exp_f32_e32 v140, v140\n"
    "v_mfma_f32_16x16x32_bf16 v[180:183], a[184:187], a[152:155], v[180:183]\n"
    "buffer_load_dwordx4 v251, s[12:15], s31 offen offset:0 lds\n"
    "ds_read2st64_b32 a[208:209], v252 offset0:8 offset1:9\n"
    "v_mfma_f32_16x16x32_bf16 v[180:183], a[188:191], a[156:159], v[180:183]\n"
    "s_waitcnt lgkmcnt(9)\n"
    "v_exp_f32_e32 v141, v141\n"
    "v_mfma_f32_16x16x32_bf16 v[184:187], a[160:163], a[128:131], v[236:239]\n"
    "s_mov_b32 m0, s63\n"
    "v_exp_f32_e32 v142, v142\n"
    "v_mfma_f32_16x16x32_bf16 v[184:187], a[164:167], a[132:135], v[184:187]\n"
    "ds_read_b128 a[184:187], v242 offset:14336\n"
    "v_exp_f32_e32 v143, v143\n"
    "v_mfma_f32_16x16x32_bf16 v[152:155], a[164:167], a[100:103], v[232:235]\n"
    "s_add_u32 s31, s31, 0x1000\n"
    "ds_read_b128 a[180:183], v243 offset:10240\n"
    "v_cvt_pk_bf16_f32 v140, v140, v141\n"
    "v_mfma_f32_16x16x32_bf16 v[152:155], a[160:163], a[96:99], v[152:155]\n"
    "s_add_u32 m0, s63, 7168\n"
    "ds_read2st64_b32 a[210:211], v252 offset0:10 offset1:11\n"
    "v_exp_f32_e32 v148, v148\n"
    "v_mfma_f32_16x16x32_bf16 v[144:147], a[160:163], a[64:67], v[228:231]\n"
    "buffer_load_dwordx4 v251, s[12:15], s31 offen offset:0 lds\n"
    "v_mfma_f32_16x16x32_bf16 v[144:147], a[164:167], a[68:71], v[144:147]\n"
    "s_waitcnt lgkmcnt(4)\n"
    "s_waitcnt vmcnt(3)\n"
    "v_mfma_f32_16x16x32_bf16 v[144:147], a[168:171], a[72:75], v[144:147]\n"
    "s_barrier\n"
    "v_mfma_f32_16x16x32_bf16 v[144:147], a[172:175], a[76:79], v[144:147]\n"
    "s_mov_b32 m0, s63\n"
    "v_exp_f32_e32 v149, v149\n"
    "v_mfma_f32_16x16x32_bf16 v[152:155], a[172:175], a[108:111], v[152:155]\n"
    "s_cmp_eq_u32 s93, 1\n"
    "s_cselect_b32 s31, s36, 2.0\n"
    "ds_read_b128 a[188:191], v243 offset:14336\n"
    "v_mfma_f32_16x16x32_bf16 v[152:155], a[168:171], a[104:107], v[152:155]\n"
    "s_cmp_eq_u32 s93, 2\n"
    "s_cselect_b32 s31, s37, s31\n"
    "ds_write_addtid_b32 v166 offset:4608\n"
    "v_mfma_f32_16x16x32_bf16 v[184:187], a[168:171], a[136:139], v[184:187]\n"
    "buffer_load_dword v191, v190, s[12:15], s31 offen offset:0\n"
    "v_mfma_f32_16x16x32_bf16 v[184:187], a[172:175], a[140:143], v[184:187]\n"
    "s_waitcnt lgkmcnt(3)\n"
    "ds_read_b128 a[160:163], v240 offset:16384\n"
    "v_cvt_pk_bf16_f32 v141, v142, v143\n"
    "v_mfma_f32_16x16x32_bf16 v[184:187], a[176:179], a[144:147], v[184:187]\n"
    "s_cmp_eq_u32 s93, 0\n"
    "s_cselect_b32 s31, s35, s31\n"
    "ds_read_b128 a[164:167], v241 offset:16384\n"
    "v_mfma_f32_16x16x32_bf16 v[184:187], a[180:183], a[148:151], v[184:187]\n"
    "s_add_u32 s31, s31, 6103040\n"
    "v_exp_f32_e32 v150, v150\n"
    "v_mfma_f32_16x16x32_bf16 v[152:155], a[180:183], a[116:119], v[152:155]\n"
    "ds_write_addtid_b32 v167 offset:4864\n"
    "v_cvt_pk_bf16_f32 v174, v176, v177\n"
    "v_mfma_f32_16x16x32_bf16 v[152:155], a[176:179], a[112:115], v[152:155]\n"
    "ds_read_b128 a[168:171], v240 offset:20480\n"
    "ds_write_addtid_b32 v140 offset:0\n"
    "v_mfma_f32_16x16x32_bf16 v[144:147], a[176:179], a[80:83], v[144:147]\n"
    "ds_read_b128 a[172:175], v241 offset:20480\n"
    "v_exp_f32_e32 v151, v151\n"
    "v_mfma_f32_16x16x32_bf16 v[144:147], a[180:183], a[84:87], v[144:147]\n"
    "s_waitcnt lgkmcnt(7)\n"
    "ds_write_addtid_b32 v141 offset:256\n"
    "v_cvt_pk_bf16_f32 v148, v148, v149\n"
    "v_mfma_f32_16x16x32_bf16 v[144:147], a[184:187], a[88:91], v[144:147]\n"
    "v_exp_f32_e32 v180, v180\n"
    "v_mfma_f32_16x16x32_bf16 v[144:147], a[188:191], a[92:95], v[144:147]\n"
    "ds_write_addtid_b32 v174 offset:5632\n"
    "v_exp_f32_e32 v181, v181\n"
    "v_mfma_f32_16x16x32_bf16 v[152:155], a[188:191], a[124:127], v[152:155]\n"
    "ds_write_addtid_b32 v148 offset:1024\n"
    "v_cvt_pk_bf16_f32 v149, v150, v151\n"
    "v_mfma_f32_16x16x32_bf16 v[152:155], a[184:187], a[120:123], v[152:155]\n"
    "ds_read_b128 a[176:179], v242 offset:16384\n"
    "ds_write_addtid_b32 v149 offset:1280\n"
    "v_mfma_f32_16x16x32_bf16 v[184:187], a[184:187], a[152:155], v[184:187]\n"
    "v_exp_f32_e32 v182, v182\n"
    "v_mfma_f32_16x16x32_bf16 v[184:187], a[188:191], a[156:159], v[184:187]\n"
    "s_waitcnt lgkmcnt(9)\n"
    "v_cvt_pk_bf16_f32 v180, v180, v181\n"
    "v_exp_f32_e32 v183, v183\n"
    "v_mfma_f32_16x16x32_bf16 v[212:215], a[160:163], a[128:131], v[236:239]\n"
    "ds_read_b128 a[180:183], v243 offset:16384\n"
    "v_exp_f32_e32 v144, v144\n"
    "v_mfma_f32_16x16x32_bf16 v[212:215], a[164:167], a[132:135], v[212:215]\n"
    "ds_read_b128 a[184:187], v242 offset:20480\n"
    "v_cvt_pk_bf16_f32 v175, v178, v179\n"
    "v_mfma_f32_16x16x32_bf16 v[204:207], a[164:167], a[100:103], v[232:235]\n"
    "v_exp_f32_e32 v145, v145\n"
    "v_mfma_f32_16x16x32_bf16 v[204:207], a[160:163], a[96:99], v[204:207]\n"
    "ds_write_addtid_b32 v175 offset:5888\n"
    "v_cvt_pk_bf16_f32 v181, v182, v183\n"
    "v_mfma_f32_16x16x32_bf16 v[196:199], a[160:163], a[64:67], v[228:231]\n"
    "v_exp_f32_e32 v146, v146\n"
    "v_mfma_f32_16x16x32_bf16 v[196:199], a[164:167], a[68:71], v[196:199]\n"
    "s_waitcnt lgkmcnt(8)\n"
    "v_exp_f32_e32 v147, v147\n"
    "v_mfma_f32_16x16x32_bf16 v[196:199], a[168:171], a[72:75], v[196:199]\n"
    "ds_write_addtid_b32 v180 offset:2048\n"
    "v_cvt_pk_bf16_f32 v142, v144, v145\n"
    "v_mfma_f32_16x16x32_bf16 v[196:199], a[172:175], a[76:79], v[196:199]\n"
    "s_waitcnt lgkmcnt(1)\n"
    "v_mfma_f32_16x16x32_bf16 v[204:207], a[172:175], a[108:111], v[204:207]\n"
    "s_barrier\n"
    "v_mfma_f32_16x16x32_bf16 v[204:207], a[168:171], a[104:107], v[204:207]\n"
    "ds_read_b128 a[188:191], v243 offset:20480\n"
    "ds_write_addtid_b32 v181 offset:2304\n"
    "v_mfma_f32_16x16x32_bf16 v[212:215], a[168:171], a[136:139], v[212:215]\n"
    "v_exp_f32_e32 v152, v152\n"
    "v_mfma_f32_16x16x32_bf16 v[212:215], a[172:175], a[140:143], v[212:215]\n"
    "v_exp_f32_e32 v153, v153\n"
    "v_mfma_f32_16x16x32_bf16 v[212:215], a[176:179], a[144:147], v[212:215]\n"
    "ds_read_b128 a[168:171], v240 offset:22528\n"
    "v_cvt_pk_bf16_f32 v143, v146, v147\n"
    "v_mfma_f32_16x16x32_bf16 v[212:215], a[180:183], a[148:151], v[212:215]\n"
    "ds_read_b128 a[160:163], v240 offset:18432\n"
    "ds_read_b128 a[164:167], v241 offset:18432\n"
    "v_mfma_f32_16x16x32_bf16 v[204:207], a[180:183], a[116:119], v[204:207]\n"
    "ds_read_b128 a[172:175], v241 offset:22528\n"
    "v_exp_f32_e32 v154, v154\n"
    "v_mfma_f32_16x16x32_bf16 v[204:207], a[176:179], a[112:115], v[204:207]\n"
    "ds_write_addtid_b32 v142 offset:512\n"
    "v_exp_f32_e32 v155, v155\n"
    "v_mfma_f32_16x16x32_bf16 v[196:199], a[176:179], a[80:83], v[196:199]\n"
    "ds_read2st64_b32 v[160:161], v249 offset0:12 offset1:13\n"
    "v_cvt_pk_bf16_f32 v150, v152, v153\n"
    "v_mfma_f32_16x16x32_bf16 v[196:199], a[180:183], a[84:87], v[196:199]\n"
    "s_waitcnt lgkmcnt(7)\n"
    "ds_read2st64_b32 v[162:163], v249 offset0:14 offset1:15\n"
    "v_exp_f32_e32 v184, v184\n"
    "v_mfma_f32_16x16x32_bf16 v[196:199], a[184:187], a[88:91], v[196:199]\n"
    "ds_read_b128 a[176:179], v242 offset:18432\n"
    "v_exp_f32_e32 v185, v185\n"
    "v_mfma_f32_16x16x32_bf16 v[196:199], a[188:191], a[92:95], v[196:199]\n"
    "ds_read_b128 a[180:183], v243 offset:18432\n"
    "v_cvt_pk_bf16_f32 v151, v154, v155\n"
    "v_mfma_f32_16x16x32_bf16 v[204:207], a[188:191], a[124:127], v[204:207]\n"
    "v_exp_f32_e32 v186, v186\n"
    "v_mfma_f32_16x16x32_bf16 v[204:207], a[184:187], a[120:123], v[204:207]\n"
    "s_waitcnt vmcnt(3)\n"
    "ds_write_addtid_b32 v143 offset:768\n"
    "v_cmp_ne_u32_e32 vcc, s39, v188\n"
    "v_mfma_f32_16x16x32_bf16 v[212:215], a[184:187], a[152:155], v[212:215]\n"
    "ds_read2st64_b32 v[168:169], v249 offset0:16 offset1:17\n"
    "v_exp_f32_e32 v196, v196\n"
    "v_mfma_f32_16x16x32_bf16 v[212:215], a[188:191], a[156:159], v[212:215]\n"
    "s_waitcnt lgkmcnt(8)\n"
    "ds_read2st64_b32 v[170:171], v249 offset0:18 offset1:19\n"
    "v_exp_f32_e32 v197, v197\n"
    "v_mfma_f32_16x16x32_bf16 v[216:219], a[160:163], a[128:131], v[236:239]\n"
    "s_cmp_eq_u64 vcc, 0\n"
    "s_cselect_b32 s31, s31, 2.0\n"
    "s_cselect_b32 s44, 0, 1\n"
    "ds_write_addtid_b32 v150 offset:1536\n"
    "v_mfma_f32_16x16x32_bf16 v[216:219], a[164:167], a[132:135], v[216:219]\n"
    "buffer_load_dword v191, v190, s[8:11], s31 offen offset:0 sc1\n"
    "ds_read_b128 a[184:187], v242 offset:22528\n"
    "v_mfma_f32_16x16x32_bf16 v[208:211], a[164:167], a[100:103], v[232:235]\n"
    "v_exp_f32_e32 v198, v198\n"
    "v_mfma_f32_16x16x32_bf16 v[208:211], a[160:163], a[96:99], v[208:211]\n"
    "ds_write_addtid_b32 v151 offset:1792\n"
    "v_exp_f32_e32 v187, v187\n"
    "v_mfma_f32_16x16x32_bf16 v[200:203], a[160:163], a[64:67], v[228:231]\n"
    "ds_read_b128 a[188:191], v243 offset:22528\n"
    "v_cvt_pk_bf16_f32 v182, v184, v185\n"
    "v_mfma_f32_16x16x32_bf16 v[200:203], a[164:167], a[68:71], v[200:203]\n"
    "s_waitcnt lgkmcnt(12)\n"
    "v_cvt_pk_bf16_f32 v196, v196, v197\n"
    "v_exp_f32_e32 v199, v199\n"
    "v_mfma_f32_16x16x32_bf16 v[200:203], a[168:171], a[72:75], v[200:203]\n"
    "v_exp_f32_e32 v204, v204\n"
    "v_mfma_f32_16x16x32_bf16 v[200:203], a[172:175], a[76:79], v[200:203]\n"
    "ds_read2st64_b32 v[176:177], v249 offset0:20 offset1:21\n"
    "v_exp_f32_e32 v205, v205\n"
    "v_mfma_f32_16x16x32_bf16 v[208:211], a[172:175], a[108:111], v[208:211]\n"
    "ds_read2st64_b32 v[178:179], v249 offset0:22 offset1:23\n"
    "v_and_b32_e32 v248, 0xf0, v250\n"
    "v_lshrrev_b32_e32 v231, 3, v248\n"
    "v_mfma_f32_16x16x32_bf16 v[208:211], a[168:171], a[104:107], v[208:211]\n"
    "ds_read_b64_tr_b16 a[160:161], v244 offset:24576\n"
    "v_cvt_pk_bf16_f32 v197, v198, v199\n"
    "v_mfma_f32_16x16x32_bf16 v[216:219], a[168:171], a[136:139], v[216:219]\n"
    "s_lshr_b32 s31, s35, 8\n"
    "ds_read_b64_tr_b16 a[162:163], v244 offset:26624\n"
    "v_exp_f32_e32 v206, v206\n"
    "v_mfma_f32_16x16x32_bf16 v[216:219], a[172:175], a[140:143], v[216:219]\n"
    "s_waitcnt lgkmcnt(11)\n"
    "ds_read_b64_tr_b16 a[168:169], v245 offset:24576\n"
    "v_cvt_pk_bf16_f32 v183, v186, v187\n"
    "v_mfma_f32_16x16x32_bf16 v[216:219], a[176:179], a[144:147], v[216:219]\n"
    "buffer_load_ushort v228, v231, s[24:27], s31 offen\n"
    "ds_write_addtid_b32 v182 offset:2560\n"
    "v_mfma_f32_16x16x32_bf16 v[216:219], a[180:183], a[148:151], v[216:219]\n"
    "s_lshr_b32 s31, s36, 8\n"
    "ds_read_b64_tr_b16 a[164:165], v244 offset:57344\n"
    "v_exp_f32_e32 v207, v207\n"
    "v_mfma_f32_16x16x32_bf16 v[208:211], a[180:183], a[116:119], v[208:211]\n"
    "ds_read_b64_tr_b16 a[170:171], v245 offset:26624\n"
    "v_cvt_pk_bf16_f32 v204, v204, v205\n"
    "v_mfma_f32_16x16x32_bf16 v[208:211], a[176:179], a[112:115], v[208:211]\n"
    "ds_read_b64_tr_b16 a[172:173], v245 offset:57344\n"
    "v_exp_f32_e32 v212, v212\n"
    "v_mfma_f32_16x16x32_bf16 v[200:203], a[176:179], a[80:83], v[200:203]\n"
    "buffer_load_ushort v229, v231, s[24:27], s31 offen\n"
    "ds_read_b64_tr_b16 a[166:167], v244 offset:59392\n"
    "v_mfma_f32_16x16x32_bf16 v[200:203], a[180:183], a[84:87], v[200:203]\n"
    "s_waitcnt lgkmcnt(10)\n"
    "v_cvt_pk_bf16_f32 v205, v206, v207\n"
    "v_exp_f32_e32 v213, v213\n"
    "v_mfma_f32_16x16x32_bf16 v[200:203], a[184:187], a[88:91], v[200:203]\n"
    "s_lshr_b32 s31, s37, 8\n"
    "ds_write_addtid_b32 v183 offset:2816\n"
    "v_exp_f32_e32 v214, v214\n"
    "v_mfma_f32_16x16x32_bf16 v[200:203], a[188:191], a[92:95], v[200:203]\n"
    "ds_read_b64_tr_b16 a[174:175], v245 offset:59392\n"
    "v_exp_f32_e32 v215, v215\n"
    "v_mfma_f32_16x16x32_bf16 v[208:211], a[188:191], a[124:127], v[208:211]\n"
    "buffer_load_ushort v230, v231, s[24:27], s31 offen\n"
    "ds_read_b64_tr_b16 a[176:177], v246 offset:24576\n"
    "v_mfma_f32_16x16x32_bf16 v[208:211], a[184:187], a[120:123], v[208:211]\n"
    "s_waitcnt lgkmcnt(2)\n"
    "v_mfma_f32_16x16x32_bf16 v[216:219], a[184:187], a[152:155], v[216:219]\n"
    "s_barrier\n"
    "v_mfma_f32_16x16x32_bf16 v[216:219], a[188:191], a[156:159], v[216:219]\n"
    "ds_write_addtid_b32 v196 offset:3072\n"
    "v_cvt_pk_bf16_f32 v212, v212, v213\n"
    "v_mfma_f32_16x16x32_bf16 a[252:255], a[160:163], a[208:211], a[252:255]\n"
    "s_add_u32 s62, s57, 4096\n"
    "ds_write_addtid_b32 v197 offset:3328\n"
    "v_exp_f32_e32 v200, v200\n"
    "v_mfma_f32_16x16x32_bf16 a[252:255], a[164:167], a[212:215], a[252:255]\n"
    "buffer_load_dwordx4 a[64:67], v250, s[8:11], s62 offen offset:2048 sc1\n"
    "ds_read_b64_tr_b16 a[184:185], v246 offset:0\n"
    "v_mfma_f32_16x16x32_bf16 a[248:251], a[164:167], a[204:207], a[248:251]\n"
    "ds_write_addtid_b32 v204 offset:4096\n"
    "v_lshrrev_b32_e32 v231, 2, v250\n"
    "v_mfma_f32_16x16x32_bf16 a[248:251], a[160:163], a[200:203], a[248:251]\n"
    "ds_read_b64_tr_b16 a[178:179], v246 offset:26624\n"
    "ds_read_b64_tr_b16 a[186:187], v246 offset:2048\n"
    "v_mfma_f32_16x16x32_bf16 a[244:247], a[160:163], a[192:195], a[244:247]\n"
    "ds_read_b64_tr_b16 a[180:181], v246 offset:57344\n"
    "ds_read_b64_tr_b16 a[182:183], v246 offset:59392\n"
    "v_mfma_f32_16x16x32_bf16 a[244:247], a[164:167], a[196:199], a[244:247]\n"
    "s_waitcnt lgkmcnt(9)\n"
    "ds_write_addtid_b32 v205 offset:4352\n"
    "v_exp_f32_e32 v201, v201\n"
    "v_mfma_f32_16x16x32_bf16 v[8:11], a[168:171], a[192:195], v[8:11]\n"
    "buffer_load_dwordx4 a[68:71], v250, s[8:11], s62 offen offset:3072 sc1\n"
    "ds_write_addtid_b32 v212 offset:5120\n"
    "v_mfma_f32_16x16x32_bf16 v[8:11], a[172:175], a[196:199], v[8:11]\n"
    "s_add_u32 s62, s57, 8192\n"
    "ds_read_b64_tr_b16 a[160:161], v244 offset:0\n"
    "v_cvt_pk_bf16_f32 v213, v214, v215\n"
    "v_mfma_f32_16x16x32_bf16 v[12:15], a[172:175], a[204:207], v[12:15]\n"
    "ds_read_b64_tr_b16 a[188:189], v246 offset:32768\n"
    "v_exp_f32_e32 v202, v202\n"
    "v_mfma_f32_16x16x32_bf16 v[12:15], a[168:171], a[200:203], v[12:15]\n"
    "ds_write_addtid_b32 v213 offset:5376\n"
    "v_exp_f32_e32 v203, v203\n"
    "v_mfma_f32_16x16x32_bf16 v[16:19], a[168:171], a[208:211], v[16:19]\n"
    "ds_read_b64_tr_b16 a[190:191], v246 offset:34816\n"
    "v_cvt_pk_bf16_f32 v198, v200, v201\n"
    "v_mfma_f32_16x16x32_bf16 v[16:19], a[172:175], a[212:215], v[16:19]\n"
    "s_waitcnt lgkmcnt(6)\n"
    "ds_read_b64_tr_b16 a[168:169], v245 offset:0\n"
    "v_exp_f32_e32 v208, v208\n"
    "v_mfma_f32_16x16x32_bf16 v[36:39], a[176:179], a[208:211], v[36:39]\n"
    "buffer_load_dwordx4 a[72:75], v250, s[8:11], s62 offen offset:0 sc1\n"
    "ds_write_addtid_b32 v198 offset:3584\n"
    "v_mfma_f32_16x16x32_bf16 v[36:39], a[180:183], a[212:215], v[36:39]\n"
    "ds_read2st64_b32 v[144:145], v249 offset0:0 offset1:1\n"
    "v_exp_f32_e32 v209, v209\n"
    "v_mfma_f32_16x16x32_bf16 v[32:35], a[180:183], a[204:207], v[32:35]\n"
    "ds_read2st64_b32 v[146:147], v249 offset0:2 offset1:3\n"
    "ds_read2st64_b32 v[152:153], v249 offset0:4 offset1:5\n"
    "v_cvt_pk_bf16_f32 v199, v202, v203\n"
    "v_mfma_f32_16x16x32_bf16 v[32:35], a[176:179], a[200:203], v[32:35]\n"
    "ds_read_b64_tr_b16 a[162:163], v244 offset:2048\n"
    "ds_read_b64_tr_b16 a[164:165], v244 offset:32768\n"
    "v_mfma_f32_16x16x32_bf16 v[28:31], a[176:179], a[192:195], v[28:31]\n"
    "ds_read_b64_tr_b16 a[166:167], v244 offset:34816\n"
    "v_exp_f32_e32 v210, v210\n"
    "v_mfma_f32_16x16x32_bf16 v[28:31], a[180:183], a[196:199], v[28:31]\n"
    "s_waitcnt lgkmcnt(8)\n"
    "ds_read_b64_tr_b16 a[170:171], v245 offset:2048\n"
    "v_exp_f32_e32 v211, v211\n"
    "v_mfma_f32_16x16x32_bf16 v[28:31], a[184:187], v[156:159], v[28:31]\n"
    "buffer_load_dwordx4 a[76:79], v250, s[8:11], s62 offen offset:1024 sc1\n"
    "ds_read2st64_b32 v[154:155], v249 offset0:6 offset1:7\n"
    "v_mfma_f32_16x16x32_bf16 v[28:31], a[188:191], v[160:163], v[28:31]\n"
    "ds_read_b64_tr_b16 a[172:173], v245 offset:32768\n"
    "ds_read2st64_b32 v[184:185], v249 offset0:8 offset1:9\n"
    "v_mfma_f32_16x16x32_bf16 v[32:35], a[188:191], v[168:171], v[32:35]\n"
    "ds_read2st64_b32 v[186:187], v249 offset0:10 offset1:11\n"
    "v_cvt_pk_bf16_f32 v206, v208, v209\n"
    "v_mfma_f32_16x16x32_bf16 v[32:35], a[184:187], v[164:167], v[32:35]\n"
    "ds_read_b64_tr_b16 a[174:175], v245 offset:34816\n"
    "v_exp_f32_e32 v216, v216\n"
    "v_mfma_f32_16x16x32_bf16 v[36:39], a[184:187], v[172:175], v[36:39]\n"
    "ds_write_addtid_b32 v199 offset:3840\n"
    "v_exp_f32_e32 v217, v217\n"
    "v_mfma_f32_16x16x32_bf16 v[36:39], a[188:191], v[176:179], v[36:39]\n"
    "s_waitcnt lgkmcnt(7)\n"
    "ds_read_b64_tr_b16 a[176:177], v245 offset:8192\n"
    "ds_read_b64_tr_b16 a[178:179], v245 offset:10240\n"
    "v_cvt_pk_bf16_f32 v207, v210, v211\n"
    "v_mfma_f32_16x16x32_bf16 a[252:255], a[160:163], v[172:175], a[252:255]\n"
    "buffer_load_dwordx4 a[80:83], v250, s[8:11], s62 offen offset:2048 sc1\n"
    "ds_write_addtid_b32 v206 offset:4608\n"
    "v_mfma_f32_16x16x32_bf16 a[252:255], a[164:167], v[176:179], a[252:255]\n"
    "ds_read_b64_tr_b16 a[184:185], v244 offset:8192\n"
    "v_exp_f32_e32 v218, v218\n"
    "v_mfma_f32_16x16x32_bf16 a[248:251], a[164:167], v[168:171], a[248:251]\n"
    "ds_read_b64_tr_b16 a[180:181], v245 offset:40960\n"
    "v_exp_f32_e32 v219, v219\n"
    "v_mfma_f32_16x16x32_bf16 a[248:251], a[160:163], v[164:167], a[248:251]\n"
    "ds_write_addtid_b32 v207 offset:4864\n"
    "v_cvt_pk_bf16_f32 v214, v216, v217\n"
    "v_mfma_f32_16x16x32_bf16 a[244:247], a[160:163], v[156:159], a[244:247]\n"
    "ds_read_b64_tr_b16 a[182:183], v245 offset:43008\n"
    "v_cvt_pk_bf16_f32 v215, v218, v219\n"
    "v_mfma_f32_16x16x32_bf16 a[244:247], a[164:167], v[160:163], a[244:247]\n"
    "s_waitcnt lgkmcnt(8)\n"
    "ds_write_addtid_b32 v214 offset:5632\n"
    "v_subrev_u32_e32 v231, 0x80, v231\n"
    "v_mfma_f32_16x16x32_bf16 v[8:11], a[168:171], v[156:159], v[8:11]\n"
    "buffer_load_dwordx4 a[84:87], v250, s[8:11], s62 offen offset:3072 sc1\n"
    "ds_read_b64_tr_b16 a[186:187], v244 offset:10240\n"
    "v_mfma_f32_16x16x32_bf16 v[8:11], a[172:175], v[160:163], v[8:11]\n"
    "s_add_u32 s62, s57, 12288\n"
    "ds_read_b64_tr_b16 a[160:161], v244 offset:16384\n"
    "ds_write_addtid_b32 v215 offset:5888\n"
    "v_mfma_f32_16x16x32_bf16 v[12:15], a[172:175], v[168:171], v[12:15]\n"
    "ds_read_b64_tr_b16 a[162:163], v244 offset:18432\n"
    "ds_read_b64_tr_b16 a[164:165], v244 offset:49152\n"
    "v_mfma_f32_16x16x32_bf16 v[12:15], a[168:171], v[164:167], v[12:15]\n"
    "ds_read_b64_tr_b16 a[188:189], v244 offset:40960\n"
    "ds_read_b64_tr_b16 a[166:167], v244 offset:51200\n"
    "v_mfma_f32_16x16x32_bf16 v[16:19], a[168:171], v[172:175], v[16:19]\n"
    "ds_read_b64_tr_b16 a[190:191], v244 offset:43008\n"
    "v_min_u32_e32 v231, 2.0, v231\n"
    "v_mfma_f32_16x16x32_bf16 v[16:19], a[172:175], v[176:179], v[16:19]\n"
    "s_waitcnt lgkmcnt(9)\n"
    "s_waitcnt lgkmcnt(5)\n"
    "v_mfma_f32_16x16x32_bf16 v[16:19], a[176:179], v[180:183], v[16:19]\n"
    "s_barrier\n"
    "v_mfma_f32_16x16x32_bf16 v[16:19], a[180:183], v[184:187], v[16:19]\n"
    "buffer_load_dwordx4 a[88:91], v250, s[8:11], s62 offen offset:0 sc1\n"
    "ds_read2st64_b32 v[200:201], v249 offset0:12 offset1:13\n"
    "v_mfma_f32_16x16x32_bf16 v[12:15], a[180:183], v[152:155], v[12:15]\n"
    "ds_read2st64_b32 v[202:203], v249 offset0:14 offset1:15\n"
    "v_lshrrev_b32_e32 v252, 4, v250\n"
    "v_mfma_f32_16x16x32_bf16 v[12:15], a[176:179], v[148:151], v[12:15]\n"
    "ds_read2st64_b32 v[208:209], v249 offset0:16 offset1:17\n"
    "v_lshlrev_b32_e32 v239, 5, v248\n"
    "v_mfma_f32_16x16x32_bf16 v[8:11], a[176:179], v[140:143], v[8:11]\n"
    "ds_read_b64_tr_b16 a[168:169], v245 offset:16384\n"
    "ds_read2st64_b32 v[210:211], v249 offset0:18 offset1:19\n"
    "v_mfma_f32_16x16x32_bf16 v[8:11], a[180:183], v[144:147], v[8:11]\n"
    "s_waitcnt lgkmcnt(5)\n"
    "ds_read2st64_b32 v[216:217], v249 offset0:20 offset1:21\n"
    "ds_read2st64_b32 v[218:219], v249 offset0:22 offset1:23\n"
    "v_mfma_f32_16x16x32_bf16 a[244:247], a[184:187], v[140:143], a[244:247]\n"
    "buffer_load_dwordx4 a[92:95], v250, s[8:11], s62 offen offset:1024 sc1\n"
    "v_mfma_f32_16x16x32_bf16 a[244:247], a[188:191], v[144:147], a[244:247]\n"
    "ds_read_b64_tr_b16 a[170:171], v245 offset:18432\n"
    "ds_read_b64_tr_b16 a[172:173], v245 offset:49152\n"
    "v_mfma_f32_16x16x32_bf16 a[248:251], a[188:191], v[152:155], a[248:251]\n"
    "ds_read_b64_tr_b16 a[176:177], v246 offset:8192\n"
    "v_and_or_b32 v239, v252, 48, v239\n"
    "v_mfma_f32_16x16x32_bf16 a[248:251], a[184:187], v[148:151], a[248:251]\n"
    "ds_read_b64_tr_b16 a[174:175], v245 offset:51200\n"
    "v_lshrrev_b32_e32 v248, 2, v248\n"
    "v_mfma_f32_16x16x32_bf16 a[252:255], a[184:187], v[180:183], a[252:255]\n"
    "ds_read_b64_tr_b16 a[178:179], v246 offset:10240\n"
    "v_lshrrev_b32_e32 v253, 4, v252\n"
    "v_mfma_f32_16x16x32_bf16 a[252:255], a[188:191], v[184:187], a[252:255]\n"
    "s_waitcnt lgkmcnt(5)\n"
    "ds_read_b64_tr_b16 a[184:185], v246 offset:16384\n"
    "v_lshl_add_u32 v248, v253, 7, v248\n"
    "v_mfma_f32_16x16x32_bf16 a[252:255], a[160:163], v[212:215], a[252:255]\n"
    "buffer_load_dwordx4 a[96:99], v250, s[8:11], s62 offen offset:2048 sc1\n"
    "v_mfma_f32_16x16x32_bf16 a[252:255], a[164:167], v[216:219], a[252:255]\n"
    "ds_read_b64_tr_b16 a[180:181], v246 offset:40960\n"
    "v_bfe_u32 v255, v252, 2, 2\n"
    "v_mfma_f32_16x16x32_bf16 a[248:251], a[164:167], v[208:211], a[248:251]\n"
    "ds_read_b64_tr_b16 a[186:187], v246 offset:18432\n"
    "v_max_u32_e32 v238, 1, v255\n"
    "v_mfma_f32_16x16x32_bf16 a[248:251], a[160:163], v[204:207], a[248:251]\n"
    "ds_read_b64_tr_b16 a[182:183], v246 offset:43008\n"
    "v_add_u32_e32 v238, s32, v238\n"
    "v_mfma_f32_16x16x32_bf16 a[244:247], a[160:163], v[196:199], a[244:247]\n"
    "v_and_b32_e32 v238, 3, v238\n"
    "v_lshlrev_b32_e32 v238, 10, v238\n"
    "v_mfma_f32_16x16x32_bf16 a[244:247], a[164:167], v[200:203], a[244:247]\n"
    "s_waitcnt lgkmcnt(5)\n"
    "ds_read_b64_tr_b16 a[160:161], v247 offset:24576\n"
    "v_and_b32_e32 v252, 3, v252\n"
    "v_mfma_f32_16x16x32_bf16 v[8:11], a[168:171], v[196:199], v[8:11]\n"
    "buffer_load_dwordx4 a[100:103], v250, s[8:11], s62 offen offset:3072 sc1\n"
    "v_mfma_f32_16x16x32_bf16 v[8:11], a[172:175], v[200:203], v[8:11]\n"
    "s_add_u32 s62, s57, 16384\n"
    "ds_read_b64_tr_b16 a[188:189], v246 offset:49152\n"
    "v_xor_b32_e32 v253, v253, v255\n"
    "v_mfma_f32_16x16x32_bf16 v[12:15], a[172:175], v[208:211], v[12:15]\n"
    "ds_read_b64_tr_b16 a[162:163], v247 offset:26624\n"
    "v_lshl_or_b32 v252, v253, 2, v252\n"
    "v_mfma_f32_16x16x32_bf16 v[12:15], a[168:171], v[204:207], v[12:15]\n"
    "ds_read_b64_tr_b16 a[190:191], v246 offset:51200\n"
    "v_cvt_pk_bf16_f32 v11, v10, v11\n"
    "v_mfma_f32_16x16x32_bf16 v[16:19], a[168:171], v[212:215], v[16:19]\n"
    "v_cvt_pk_bf16_f32 v10, v8, v9\n"
    "v_accvgpr_read_b32 v8, a244\n"
    "v_mfma_f32_16x16x32_bf16 v[16:19], a[172:175], v[216:219], v[16:19]\n"
    "s_waitcnt lgkmcnt(4)\n"
    "v_accvgpr_read_b32 v9, a245\n"
    "v_accvgpr_read_b32 v255, a247\n"
    "v_mfma_f32_16x16x32_bf16 v[36:39], a[176:179], v[180:183], v[36:39]\n"
    "ds_read_b64_tr_b16 a[168:169], v247 offset:0\n"
    "v_cvt_pk_bf16_f32 v8, v8, v9\n"
    "v_mfma_f32_16x16x32_bf16 v[36:39], a[180:183], v[184:187], v[36:39]\n"
    "v_accvgpr_read_b32 v9, a246\n"
    "v_cvt_pk_bf16_f32 v9, v9, v255\n"
    "v_mfma_f32_16x16x32_bf16 v[32:35], a[180:183], v[152:155], v[32:35]\n"
    "buffer_store_dwordx4 v[8:11], v250, s[8:11], s82 offen offset:0 sc1\n"
    "v_mfma_f32_16x16x32_bf16 v[32:35], a[176:179], v[148:151], v[32:35]\n"
    "ds_read_b64_tr_b16 a[164:165], v247 offset:57344\n"
    "ds_read_b64_tr_b16 a[166:167], v247 offset:59392\n"
    "v_mfma_f32_16x16x32_bf16 v[28:31], a[176:179], v[140:143], v[28:31]\n"
    "ds_read_b64_tr_b16 a[170:171], v247 offset:2048\n"
    "v_cvt_pk_bf16_f32 v15, v14, v15\n"
    "v_mfma_f32_16x16x32_bf16 v[28:31], a[180:183], v[144:147], v[28:31]\n"
    "s_waitcnt lgkmcnt(4)\n"
    "ds_read_b64_tr_b16 a[176:177], v247 offset:8192\n"
    "v_mfma_f32_16x16x32_bf16 v[28:31], a[184:187], v[196:199], v[28:31]\n"
    "buffer_load_dwordx4 a[104:107], v250, s[8:11], s62 offen offset:0 sc1\n"
    "v_mfma_f32_16x16x32_bf16 v[28:31], a[188:191], v[200:203], v[28:31]\n"
    "ds_read_b64_tr_b16 a[172:173], v247 offset:32768\n"
    "v_cvt_pk_bf16_f32 v14, v12, v13\n"
    "v_mfma_f32_16x16x32_bf16 v[32:35], a[188:191], v[208:211], v[32:35]\n"
    "ds_read_b64_tr_b16 a[178:179], v247 offset:10240\n"
    "v_accvgpr_read_b32 v12, a248\n"
    "v_mfma_f32_16x16x32_bf16 v[32:35], a[184:187], v[204:207], v[32:35]\n"
    "ds_read_b64_tr_b16 a[174:175], v247 offset:34816\n"
    "v_cvt_pk_bf16_f32 v28, v28, v29\n"
    "v_mfma_f32_16x16x32_bf16 v[36:39], a[184:187], v[212:215], v[36:39]\n"
    "ds_read_b64_tr_b16 a[180:181], v247 offset:40960\n"
    "v_mfma_f32_16x16x32_bf16 v[36:39], a[188:191], v[216:219], v[36:39]\n"
    "s_waitcnt lgkmcnt(6)\n"
    "ds_read_b64_tr_b16 a[182:183], v247 offset:43008\n"
    "v_accvgpr_read_b32 v13, a249\n"
    "v_mfma_f32_16x16x32_bf16 v[56:59], a[160:163], a[208:211], v[56:59]\n"
    "buffer_load_dwordx4 a[108:111], v250, s[8:11], s62 offen offset:1024 sc1\n"
    "v_mfma_f32_16x16x32_bf16 v[56:59], a[164:167], a[212:215], v[56:59]\n"
    "ds_read_b64_tr_b16 a[184:185], v247 offset:16384\n"
    "v_accvgpr_read_b32 v255, a251\n"
    "v_mfma_f32_16x16x32_bf16 v[52:55], a[164:167], a[204:207], v[52:55]\n"
    "v_cvt_pk_bf16_f32 v12, v12, v13\n"
    "v_accvgpr_read_b32 v13, a250\n"
    "v_mfma_f32_16x16x32_bf16 v[52:55], a[160:163], a[200:203], v[52:55]\n"
    "ds_read_b64_tr_b16 a[186:187], v247 offset:18432\n"
    "v_cvt_pk_bf16_f32 v13, v13, v255\n"
    "v_mfma_f32_16x16x32_bf16 v[48:51], a[160:163], a[192:195], v[48:51]\n"
    "buffer_store_dwordx4 v[12:15], v250, s[8:11], s28 offen offset:0 sc1\n"
    "v_mfma_f32_16x16x32_bf16 v[48:51], a[164:167], a[196:199], v[48:51]\n"
    "s_waitcnt lgkmcnt(4)\n"
    "ds_read_b64_tr_b16 a[160:161], v244 offset:28672\n"
    "v_cvt_pk_bf16_f32 v19, v18, v19\n"
    "v_mfma_f32_16x16x32_bf16 v[48:51], a[168:171], v[156:159], v[48:51]\n"
    "s_add_u32 s62, s57, 16384\n"
    "ds_read_b64_tr_b16 a[162:163], v244 offset:30720\n"
    "v_cvt_pk_bf16_f32 v18, v16, v17\n"
    "v_mfma_f32_16x16x32_bf16 v[48:51], a[172:175], v[160:163], v[48:51]\n"
    "buffer_load_dwordx4 a[112:115], v250, s[8:11], s62 offen offset:2048 sc1\n"
    "v_mfma_f32_16x16x32_bf16 v[52:55], a[172:175], v[168:171], v[52:55]\n"
    "ds_read_b64_tr_b16 a[188:189], v247 offset:49152\n"
    "v_accvgpr_read_b32 v16, a252\n"
    "v_mfma_f32_16x16x32_bf16 v[52:55], a[168:171], v[164:167], v[52:55]\n"
    "ds_read_b64_tr_b16 a[164:165], v244 offset:61440\n"
    "v_mfma_f32_16x16x32_bf16 v[56:59], a[168:171], v[172:175], v[56:59]\n"
    "ds_read_b64_tr_b16 a[190:191], v247 offset:51200\n"
    "v_accvgpr_read_b32 v17, a253\n"
    "v_mfma_f32_16x16x32_bf16 v[56:59], a[172:175], v[176:179], v[56:59]\n"
    "s_waitcnt lgkmcnt(7)\n"
    "ds_read_b64_tr_b16 a[166:167], v244 offset:63488\n"
    "v_cvt_pk_bf16_f32 v29, v30, v31\n"
    "v_mfma_f32_16x16x32_bf16 v[56:59], a[176:179], v[180:183], v[56:59]\n"
    "v_mov_b32_e32 v253, 0x3f80\n"
    "v_accvgpr_read_b32 v255, a255\n"
    "v_mfma_f32_16x16x32_bf16 v[56:59], a[180:183], v[184:187], v[56:59]\n"
    "ds_read_b64_tr_b16 a[168:169], v244 offset:4096\n"
    "v_cvt_pk_bf16_f32 v16, v16, v17\n"
    "v_mfma_f32_16x16x32_bf16 v[52:55], a[180:183], v[152:155], v[52:55]\n"
    "ds_read_b64_tr_b16 a[170:171], v244 offset:6144\n"
    "v_cvt_pk_bf16_f32 v32, v32, v33\n"
    "v_mfma_f32_16x16x32_bf16 v[52:55], a[176:179], v[148:151], v[52:55]\n"
    "buffer_load_dwordx4 a[116:119], v250, s[8:11], s62 offen offset:3072 sc1\n"
    "v_mfma_f32_16x16x32_bf16 v[48:51], a[176:179], v[140:143], v[48:51]\n"
    "s_add_u32 s62, s57, 20480\n"
    "v_accvgpr_read_b32 v17, a254\n"
    "v_cvt_pk_bf16_f32 v17, v17, v255\n"
    "v_mfma_f32_16x16x32_bf16 v[48:51], a[180:183], v[144:147], v[48:51]\n"
    "s_waitcnt lgkmcnt(3)\n"
    "ds_read_b64_tr_b16 a[172:173], v244 offset:36864\n"
    "v_mfma_f32_16x16x32_bf16 v[48:51], a[184:187], v[196:199], v[48:51]\n"
    "buffer_store_dwordx4 v[16:19], v250, s[8:11], s29 offen offset:0 sc1\n"
    "v_mfma_f32_16x16x32_bf16 v[48:51], a[188:191], v[200:203], v[48:51]\n"
    "v_mov_b64_e32 v[232:233], 0\n"
    "v_mov_b64_e32 v[236:237], 0\n"
    "v_mfma_f32_16x16x32_bf16 v[52:55], a[188:191], v[208:211], v[52:55]\n"
    "ds_read_b64_tr_b16 a[174:175], v244 offset:38912\n"
    "v_mfma_f32_16x16x32_bf16 v[52:55], a[184:187], v[204:207], v[52:55]\n"
    "ds_read_b64_tr_b16 a[176:177], v244 offset:12288\n"
    "v_mfma_f32_16x16x32_bf16 v[56:59], a[184:187], v[212:215], v[56:59]\n"
    "ds_read_b64_tr_b16 a[178:179], v244 offset:14336\n"
    "v_cvt_pk_bf16_f32 v30, v48, v49\n"
    "v_mfma_f32_16x16x32_bf16 v[56:59], a[188:191], v[216:219], v[56:59]\n"
    "s_waitcnt lgkmcnt(6)\n"
    "ds_read_b64_tr_b16 a[184:185], v244 offset:20480\n"
    "v_mfma_f32_16x16x32_bf16 v[76:79], a[160:163], a[208:211], v[76:79]\n"
    "buffer_load_dwordx4 a[120:123], v250, s[8:11], s62 offen offset:0 sc1\n"
    "v_mfma_f32_16x16x32_bf16 v[76:79], a[164:167], a[212:215], v[76:79]\n"
    "ds_read_b64_tr_b16 a[186:187], v244 offset:22528\n"
    "v_mfma_f32_16x16x32_bf16 v[72:75], a[164:167], a[204:207], v[72:75]\n"
    "ds_read_b64_tr_b16 a[180:181], v244 offset:45056\n"
    "v_cvt_pk_bf16_f32 v36, v36, v37\n"
    "v_mfma_f32_16x16x32_bf16 v[72:75], a[160:163], a[200:203], v[72:75]\n"
    "ds_read_b64_tr_b16 a[182:183], v244 offset:47104\n"
    "v_mfma_f32_16x16x32_bf16 v[68:71], a[160:163], a[192:195], v[68:71]\n"
    "ds_read_b64_tr_b16 a[188:189], v244 offset:53248\n"
    "v_mfma_f32_16x16x32_bf16 v[68:71], a[164:167], a[196:199], v[68:71]\n"
    "s_waitcnt lgkmcnt(7)\n"
    "ds_read_b64_tr_b16 a[190:191], v244 offset:55296\n"
    "v_cvt_pk_bf16_f32 v37, v38, v39\n"
    "v_mfma_f32_16x16x32_bf16 v[68:71], a[168:171], v[156:159], v[68:71]\n"
    "v_cmp_eq_u32_e32 vcc, 0, v252\n"
    "v_cvt_pk_bf16_f32 v31, v50, v51\n"
    "v_mfma_f32_16x16x32_bf16 v[68:71], a[172:175], v[160:163], v[68:71]\n"
    "ds_read_b64_tr_b16 a[160:161], v245 offset:28672\n"
    "v_mfma_f32_16x16x32_bf16 v[72:75], a[172:175], v[168:171], v[72:75]\n"
    "buffer_store_dwordx4 v[28:31], v250, s[8:11], s82 offen offset:1024 sc1\n"
    "v_mfma_f32_16x16x32_bf16 v[72:75], a[168:171], v[164:167], v[72:75]\n"
    "ds_read_b64_tr_b16 a[162:163], v245 offset:30720\n"
    "v_mfma_f32_16x16x32_bf16 v[76:79], a[168:171], v[172:175], v[76:79]\n"
    "ds_read_b64_tr_b16 a[164:165], v245 offset:61440\n"
    "v_mfma_f32_16x16x32_bf16 v[76:79], a[172:175], v[176:179], v[76:79]\n"
    "s_waitcnt lgkmcnt(5)\n"
    "ds_read_b64_tr_b16 a[168:169], v245 offset:4096\n"
    "v_mfma_f32_16x16x32_bf16 v[76:79], a[176:179], v[180:183], v[76:79]\n"
    "buffer_load_dwordx4 a[124:127], v250, s[8:11], s62 offen offset:1024 sc1\n"
    "v_mfma_f32_16x16x32_bf16 v[76:79], a[180:183], v[184:187], v[76:79]\n"
    "v_cvt_pk_bf16_f32 v33, v34, v35\n"
    "v_mfma_f32_16x16x32_bf16 v[72:75], a[180:183], v[152:155], v[72:75]\n"
    "ds_read_b64_tr_b16 a[170:171], v245 offset:6144\n"
    "v_mfma_f32_16x16x32_bf16 v[72:75], a[176:179], v[148:151], v[72:75]\n"
    "ds_read_b64_tr_b16 a[166:167], v245 offset:63488\n"
    "v_mfma_f32_16x16x32_bf16 v[68:71], a[176:179], v[140:143], v[68:71]\n"
    "ds_read_b64_tr_b16 a[172:173], v245 offset:36864\n"
    "v_cndmask_b32_e32 v234, 0, v253, vcc\n"
    "v_mfma_f32_16x16x32_bf16 v[68:71], a[180:183], v[144:147], v[68:71]\n"
    "s_waitcnt lgkmcnt(7)\n"
    "ds_read_b64_tr_b16 a[176:177], v245 offset:12288\n"
    "v_cmp_eq_u32_e32 vcc, 1, v252\n"
    "v_mfma_f32_16x16x32_bf16 v[68:71], a[184:187], v[196:199], v[68:71]\n"
    "ds_read_b64_tr_b16 a[178:179], v245 offset:14336\n"
    "v_cndmask_b32_e64 v234, v234, 1.0, vcc\n"
    "v_mfma_f32_16x16x32_bf16 v[68:71], a[188:191], v[200:203], v[68:71]\n"
    "v_cvt_pk_bf16_f32 v34, v52, v53\n"
    "v_cvt_pk_bf16_f32 v35, v54, v55\n"
    "v_mfma_f32_16x16x32_bf16 v[72:75], a[188:191], v[208:211], v[72:75]\n"
    "v_mfma_f32_16x16x32_bf16 v[72:75], a[184:187], v[204:207], v[72:75]\n"
    "ds_read_b64_tr_b16 a[174:175], v245 offset:38912\n"
    "v_mfma_f32_16x16x32_bf16 v[76:79], a[184:187], v[212:215], v[76:79]\n"
    "buffer_store_dwordx4 v[32:35], v250, s[8:11], s28 offen offset:1024 sc1\n"
    "v_mfma_f32_16x16x32_bf16 v[76:79], a[188:191], v[216:219], v[76:79]\n"
    "s_waitcnt lgkmcnt(4)\n"
    "ds_read_b64_tr_b16 a[184:185], v245 offset:20480\n"
    "v_cvt_pk_bf16_f32 v68, v68, v69\n"
    "v_mfma_f32_16x16x32_bf16 v[96:99], a[160:163], a[208:211], v[96:99]\n"
    "ds_read_b64_tr_b16 a[186:187], v245 offset:22528\n"
    "v_cvt_pk_bf16_f32 v72, v72, v73\n"
    "v_mfma_f32_16x16x32_bf16 v[96:99], a[164:167], a[212:215], v[96:99]\n"
    "buffer_load_dwordx4 a[128:131], v250, s[8:11], s62 offen offset:2048 sc1\n"
    "v_mfma_f32_16x16x32_bf16 v[92:95], a[164:167], a[204:207], v[92:95]\n"
    "ds_read_b64_tr_b16 a[180:181], v245 offset:45056\n"
    "v_cvt_pk_bf16_f32 v76, v76, v77\n"
    "v_mfma_f32_16x16x32_bf16 v[92:95], a[160:163], a[200:203], v[92:95]\n"
    "ds_read_b64_tr_b16 a[188:189], v245 offset:53248\n"
    "v_cvt_pk_bf16_f32 v38, v56, v57\n"
    "v_mfma_f32_16x16x32_bf16 v[88:91], a[160:163], a[192:195], v[88:91]\n"
    "ds_read_b64_tr_b16 a[182:183], v245 offset:47104\n"
    "v_mfma_f32_16x16x32_bf16 v[88:91], a[164:167], a[196:199], v[88:91]\n"
    "s_waitcnt lgkmcnt(5)\n"
    "ds_read_b64_tr_b16 a[160:161], v246 offset:28672\n"
    "v_cmp_eq_u32_e32 vcc, 2, v252\n"
    "v_mfma_f32_16x16x32_bf16 v[88:91], a[168:171], v[156:159], v[88:91]\n"
    "s_nop 0\n"
    "v_cndmask_b32_e32 v235, 0, v253, vcc\n"
    "v_mfma_f32_16x16x32_bf16 v[88:91], a[172:175], v[160:163], v[88:91]\n"
    "ds_read_b64_tr_b16 a[190:191], v245 offset:55296\n"
    "v_mfma_f32_16x16x32_bf16 v[92:95], a[172:175], v[168:171], v[92:95]\n"
    "v_cvt_pk_bf16_f32 v39, v58, v59\n"
    "v_mfma_f32_16x16x32_bf16 v[92:95], a[168:171], v[164:167], v[92:95]\n"
    "buffer_load_dwordx4 a[132:135], v250, s[8:11], s62 offen offset:3072 sc1\n"
    "v_mfma_f32_16x16x32_bf16 v[96:99], a[168:171], v[172:175], v[96:99]\n"
    "s_add_u32 s62, s57, 24576\n"
    "v_cvt_pk_bf16_f32 v73, v74, v75\n"
    "v_mfma_f32_16x16x32_bf16 v[96:99], a[172:175], v[176:179], v[96:99]\n"
    "s_waitcnt lgkmcnt(2)\n"
    "ds_read_b64_tr_b16 a[162:163], v246 offset:30720\n"
    "v_mfma_f32_16x16x32_bf16 v[96:99], a[176:179], v[180:183], v[96:99]\n"
    "buffer_store_dwordx4 v[36:39], v250, s[8:11], s29 offen offset:1024 sc1\n"
    "v_mfma_f32_16x16x32_bf16 v[96:99], a[180:183], v[184:187], v[96:99]\n"
    "ds_read_b64_tr_b16 a[168:169], v246 offset:4096\n"
    "v_mfma_f32_16x16x32_bf16 v[92:95], a[180:183], v[152:155], v[92:95]\n"
    "ds_read_b64_tr_b16 a[164:165], v246 offset:61440\n"
    "v_mfma_f32_16x16x32_bf16 v[92:95], a[176:179], v[148:151], v[92:95]\n"
    "ds_read_b64_tr_b16 a[166:167], v246 offset:63488\n"
    "v_mfma_f32_16x16x32_bf16 v[88:91], a[176:179], v[140:143], v[88:91]\n"
    "ds_read_b64_tr_b16 a[170:171], v246 offset:6144\n"
    "v_cvt_pk_bf16_f32 v69, v70, v71\n"
    "v_mfma_f32_16x16x32_bf16 v[88:91], a[180:183], v[144:147], v[88:91]\n"
    "s_waitcnt lgkmcnt(5)\n"
    "ds_read_b64_tr_b16 a[176:177], v246 offset:12288\n"
    "v_cvt_pk_bf16_f32 v77, v78, v79\n"
    "v_mfma_f32_16x16x32_bf16 v[88:91], a[184:187], v[196:199], v[88:91]\n"
    "buffer_load_dwordx4 a[136:139], v250, s[8:11], s62 offen offset:0 sc1\n"
    "v_mfma_f32_16x16x32_bf16 v[88:91], a[188:191], v[200:203], v[88:91]\n"
    "ds_read_b64_tr_b16 a[172:173], v246 offset:36864\n"
    "v_mfma_f32_16x16x32_bf16 v[92:95], a[188:191], v[208:211], v[92:95]\n"
    "v_mfma_f32_16x16x32_bf16 v[92:95], a[184:187], v[204:207], v[92:95]\n"
    "ds_read_b64_tr_b16 a[174:175], v246 offset:38912\n"
    "v_cvt_pk_bf16_f32 v70, v88, v89\n"
    "v_mfma_f32_16x16x32_bf16 v[96:99], a[184:187], v[212:215], v[96:99]\n"
    "v_mfma_f32_16x16x32_bf16 v[96:99], a[188:191], v[216:219], v[96:99]\n"
    "s_waitcnt lgkmcnt(4)\n"
    "ds_read_b64_tr_b16 a[178:179], v246 offset:14336\n"
    "v_cmp_eq_u32_e32 vcc, 3, v252\n"
    "v_mfma_f32_16x16x32_bf16 v[116:119], a[160:163], a[208:211], v[116:119]\n"
    "ds_read_b64_tr_b16 a[184:185], v246 offset:20480\n"
    "v_cvt_pk_bf16_f32 v71, v90, v91\n"
    "v_mfma_f32_16x16x32_bf16 v[116:119], a[164:167], a[212:215], v[116:119]\n"
    "v_cvt_pk_bf16_f32 v74, v92, v93\n"
    "v_mfma_f32_16x16x32_bf16 v[112:115], a[164:167], a[204:207], v[112:115]\n"
    "buffer_store_dwordx4 v[68:71], v250, s[8:11], s82 offen offset:2048 sc1\n"
    "v_mfma_f32_16x16x32_bf16 v[112:115], a[160:163], a[200:203], v[112:115]\n"
    "ds_read_b64_tr_b16 a[180:181], v246 offset:45056\n"
    "v_mfma_f32_16x16x32_bf16 v[108:111], a[160:163], a[192:195], v[108:111]\n"
    "ds_read_b64_tr_b16 a[182:183], v246 offset:47104\n"
    "v_mfma_f32_16x16x32_bf16 v[108:111], a[164:167], a[196:199], v[108:111]\n"
    "s_waitcnt lgkmcnt(4)\n"
    "ds_read_b64_tr_b16 a[186:187], v246 offset:22528\n"
    "v_mfma_f32_16x16x32_bf16 v[108:111], a[168:171], v[156:159], v[108:111]\n"
    "buffer_load_dwordx4 a[140:143], v250, s[8:11], s62 offen offset:1024 sc1\n"
    "v_mfma_f32_16x16x32_bf16 v[108:111], a[172:175], v[160:163], v[108:111]\n"
    "ds_read_b64_tr_b16 a[188:189], v246 offset:53248\n"
    "v_cndmask_b32_e64 v235, v235, 1.0, vcc\n"
    "v_mfma_f32_16x16x32_bf16 v[112:115], a[172:175], v[168:171], v[112:115]\n"
    "v_mfma_f32_16x16x32_bf16 v[112:115], a[168:171], v[164:167], v[112:115]\n"
    "ds_read_b64_tr_b16 a[160:161], v247 offset:28672\n"
    "v_mfma_f32_16x16x32_bf16 v[116:119], a[168:171], v[172:175], v[116:119]\n"
    "ds_read_b64_tr_b16 a[190:191], v246 offset:55296\n"
    "v_cvt_pk_bf16_f32 v75, v94, v95\n"
    "v_mfma_f32_16x16x32_bf16 v[116:119], a[172:175], v[176:179], v[116:119]\n"
    "s_waitcnt lgkmcnt(4)\n"
    "ds_read_b64_tr_b16 a[162:163], v247 offset:30720\n"
    "v_cvt_pk_bf16_f32 v78, v96, v97\n"
    "v_mfma_f32_16x16x32_bf16 v[116:119], a[176:179], v[180:183], v[116:119]\n"
    "ds_read_b64_tr_b16 a[164:165], v247 offset:61440\n"
    "v_mfma_f32_16x16x32_bf16 v[116:119], a[180:183], v[184:187], v[116:119]\n"
    "v_mfma_f32_16x16x32_bf16 v[112:115], a[180:183], v[152:155], v[112:115]\n"
    "ds_read_b64_tr_b16 a[168:169], v247 offset:4096\n"
    "v_mfma_f32_16x16x32_bf16 v[112:115], a[176:179], v[148:151], v[112:115]\n"
    "ds_read_b64_tr_b16 a[166:167], v247 offset:63488\n"
    "v_mfma_f32_16x16x32_bf16 v[108:111], a[176:179], v[140:143], v[108:111]\n"
    "buffer_store_dwordx4 v[72:75], v250, s[8:11], s28 offen offset:2048 sc1\n"
    "v_mfma_f32_16x16x32_bf16 v[108:111], a[180:183], v[144:147], v[108:111]\n"
    "s_waitcnt lgkmcnt(4)\n"
    "ds_read_b64_tr_b16 a[170:171], v247 offset:6144\n"
    "v_mfma_f32_16x16x32_bf16 v[108:111], a[184:187], v[196:199], v[108:111]\n"
    "ds_read_b64_tr_b16 a[176:177], v247 offset:12288\n"
    "v_mfma_f32_16x16x32_bf16 v[108:111], a[188:191], v[200:203], v[108:111]\n"
    "buffer_load_dwordx4 a[144:147], v250, s[8:11], s62 offen offset:2048 sc1\n"
    "v_mfma_f32_16x16x32_bf16 v[112:115], a[188:191], v[208:211], v[112:115]\n"
    "ds_read_b64_tr_b16 a[172:173], v247 offset:36864\n"
    "v_mfma_f32_16x16x32_bf16 v[112:115], a[184:187], v[204:207], v[112:115]\n"
    "ds_read_b64_tr_b16 a[178:179], v247 offset:14336\n"
    "v_cvt_pk_bf16_f32 v108, v108, v109\n"
    "v_mfma_f32_16x16x32_bf16 v[116:119], a[184:187], v[212:215], v[116:119]\n"
    "ds_read_b64_tr_b16 a[174:175], v247 offset:38912\n"
    "v_mfma_f32_16x16x32_bf16 v[116:119], a[188:191], v[216:219], v[116:119]\n"
    "s_waitcnt lgkmcnt(5)\n"
    "ds_read_b64_tr_b16 a[180:181], v247 offset:45056\n"
    "v_cvt_pk_bf16_f32 v79, v98, v99\n"
    "v_mfma_f32_16x16x32_bf16 v[136:139], a[160:163], a[208:211], v[136:139]\n"
    "s_cmp_lg_u32 s44, 0\n"
    "s_cbranch_scc1 .Lat_glate\n"
    ".Lat_glate_ret:\n"
    "v_mfma_f32_16x16x32_bf16 v[136:139], a[164:167], a[212:215], v[136:139]\n"
    "ds_read_b64_tr_b16 a[184:185], v247 offset:20480\n"
    "v_mfma_f32_16x16x32_bf16 v[132:135], a[164:167], a[204:207], v[132:135]\n"
    "ds_read_b64_tr_b16 a[186:187], v247 offset:22528\n"
    "v_mfma_f32_16x16x32_bf16 v[132:135], a[160:163], a[200:203], v[132:135]\n"
    "buffer_load_dwordx4 a[148:151], v250, s[8:11], s62 offen offset:3072 sc1\n"
    "v_mfma_f32_16x16x32_bf16 v[128:131], a[160:163], a[192:195], v[128:131]\n"
    "s_add_u32 s62, s57, 28672\n"
    "ds_read_b64_tr_b16 a[182:183], v247 offset:47104\n"
    "v_mfma_f32_16x16x32_bf16 v[128:131], a[164:167], a[196:199], v[128:131]\n"
    "s_waitcnt lgkmcnt(4)\n"
    "v_cvt_pk_bf16_f32 v116, v116, v117\n"
    "v_mfma_f32_16x16x32_bf16 v[128:131], a[168:171], v[156:159], v[128:131]\n"
    "buffer_store_dwordx4 v[76:79], v250, s[8:11], s29 offen offset:2048 sc1\n"
    "v_mfma_f32_16x16x32_bf16 v[128:131], a[172:175], v[160:163], v[128:131]\n"
    "ds_read_b64_tr_b16 a[188:189], v247 offset:53248\n"
    "v_mfma_f32_16x16x32_bf16 v[132:135], a[172:175], v[168:171], v[132:135]\n"
    "ds_read_b64_tr_b16 a[190:191], v247 offset:55296\n"
    "v_mfma_f32_16x16x32_bf16 v[132:135], a[168:171], v[164:167], v[132:135]\n"
    "ds_read_b128 a[160:163], v240 offset:24576\n"
    "v_mfma_f32_16x16x32_bf16 v[136:139], a[168:171], v[172:175], v[136:139]\n"
    "ds_read_b128 a[164:167], v241 offset:24576\n"
    "v_mfma_f32_16x16x32_bf16 v[136:139], a[172:175], v[176:179], v[136:139]\n"
    "s_waitcnt lgkmcnt(4)\n"
    "ds_read_b128 a[168:171], v240 offset:28672\n"
    "v_mfma_f32_16x16x32_bf16 v[136:139], a[176:179], v[180:183], v[136:139]\n"
    "buffer_load_dwordx4 a[152:155], v250, s[8:11], s62 offen offset:0 sc1\n"
    "v_mfma_f32_16x16x32_bf16 v[136:139], a[180:183], v[184:187], v[136:139]\n"
    "v_mfma_f32_16x16x32_bf16 v[132:135], a[180:183], v[152:155], v[132:135]\n"
    "v_mfma_f32_16x16x32_bf16 v[132:135], a[176:179], v[148:151], v[132:135]\n"
    "v_mfma_f32_16x16x32_bf16 v[128:131], a[176:179], v[140:143], v[128:131]\n"
    "ds_read_b128 a[172:175], v241 offset:28672\n"
    "v_cvt_pk_bf16_f32 v109, v110, v111\n"
    "v_mfma_f32_16x16x32_bf16 v[128:131], a[180:183], v[144:147], v[128:131]\n"
    "s_waitcnt lgkmcnt(4)\n"
    "ds_read_b128 a[176:179], v242 offset:24576\n"
    "v_mfma_f32_16x16x32_bf16 v[128:131], a[184:187], v[196:199], v[128:131]\n"
    "v_cvt_pk_bf16_f32 v117, v118, v119\n"
    "v_mfma_f32_16x16x32_bf16 v[128:131], a[188:191], v[200:203], v[128:131]\n"
    "v_mfma_f32_16x16x32_bf16 v[132:135], a[188:191], v[208:211], v[132:135]\n"
    "buffer_load_dwordx4 a[156:159], v250, s[8:11], s62 offen offset:1024 sc1\n"
    "v_mfma_f32_16x16x32_bf16 v[132:135], a[184:187], v[204:207], v[132:135]\n"
    "ds_read_b128 a[180:183], v243 offset:24576\n"
    "v_mfma_f32_16x16x32_bf16 v[136:139], a[184:187], v[212:215], v[136:139]\n"
    "v_cvt_pk_bf16_f32 v110, v128, v129\n"
    "v_cvt_pk_bf16_f32 v111, v130, v131\n"
    "v_mfma_f32_16x16x32_bf16 v[136:139], a[188:191], v[216:219], v[136:139]\n"
    "s_waitcnt lgkmcnt(4)\n"
    "ds_read_b128 a[184:187], v242 offset:28672\n"
    "v_mfma_f32_16x16x32_bf16 v[164:167], a[160:163], a[32:35], v[224:227]\n"
    "buffer_store_dwordx4 v[108:111], v250, s[8:11], s82 offen offset:3072 sc1\n"
    "v_mfma_f32_16x16x32_bf16 v[164:167], a[164:167], a[36:39], v[164:167]\n"
    "ds_read_b128 a[188:191], v243 offset:28672\n"
    "v_mfma_f32_16x16x32_bf16 v[156:159], a[164:167], a[4:7], v[220:223]\n"
    "v_mfma_f32_16x16x32_bf16 v[156:159], a[160:163], a[0:3], v[156:159]\n"
    "s_waitcnt lgkmcnt(4)\n"
    "v_cvt_pk_bf16_f32 v112, v112, v113\n"
    "v_mfma_f32_16x16x32_bf16 v[156:159], a[168:171], a[8:11], v[156:159]\n"
    "buffer_store_dword v131, v231, s[8:11], s87 offen offset:0 sc1\n"
    "v_mfma_f32_16x16x32_bf16 v[156:159], a[172:175], a[12:15], v[156:159]\n"
    "ds_read_b128 a[160:163], v240 offset:26624\n"
    "v_cvt_pk_bf16_f32 v113, v114, v115\n"
    "v_mfma_f32_16x16x32_bf16 v[164:167], a[172:175], a[44:47], v[164:167]\n"
    "ds_read_b128 a[164:167], v241 offset:26624\n"
    "v_mfma_f32_16x16x32_bf16 v[164:167], a[168:171], a[40:43], v[164:167]\n"
    "s_waitcnt lgkmcnt(4)\n"
    "ds_read_b128 a[168:171], v240 offset:30720\n"
    "v_mfma_f32_16x16x32_bf16 v[164:167], a[176:179], a[48:51], v[164:167]\n"
    "v_cvt_pk_bf16_f32 v118, v136, v137\n"
    "v_mfma_f32_16x16x32_bf16 v[164:167], a[180:183], a[52:55], v[164:167]\n"
    "buffer_store_dword v135, v231, s[8:11], s88 offen offset:0 sc1\n"
    "v_mfma_f32_16x16x32_bf16 v[156:159], a[180:183], a[20:23], v[156:159]\n"
    "v_cvt_pk_bf16_f32 v114, v132, v133\n"
    "v_cvt_pk_bf16_f32 v119, v138, v139\n"
    "v_mfma_f32_16x16x32_bf16 v[156:159], a[176:179], a[16:19], v[156:159]\n"
    "s_waitcnt lgkmcnt(3)\n"
    "ds_read_b128 a[172:175], v241 offset:30720\n"
    "v_mfma_f32_16x16x32_bf16 v[156:159], a[184:187], a[24:27], v[156:159]\n"
    "ds_read_b128 a[176:179], v242 offset:26624\n"
    "v_cvt_pk_bf16_f32 v115, v134, v135\n"
    "v_mfma_f32_16x16x32_bf16 v[156:159], a[188:191], a[28:31], v[156:159]\n"
    "ds_read_b128 a[180:183], v243 offset:26624\n"
    "v_mfma_f32_16x16x32_bf16 v[164:167], a[188:191], a[60:63], v[164:167]\n"
    "buffer_store_dwordx4 v[112:115], v250, s[8:11], s28 offen offset:3072 sc1\n"
    "v_mfma_f32_16x16x32_bf16 v[164:167], a[184:187], a[56:59], v[164:167]\n"
    "s_waitcnt lgkmcnt(4)\n"
    "v_exp_f32_e32 v156, v156\n"
    "v_mfma_f32_16x16x32_bf16 v[168:171], a[160:163], a[32:35], v[224:227]\n"
    "v_exp_f32_e32 v157, v157\n"
    "v_mfma_f32_16x16x32_bf16 v[168:171], a[164:167], a[36:39], v[168:171]\n"
    "buffer_store_dword v139, v231, s[8:11], s89 offen offset:0 sc1\n"
    "v_mfma_f32_16x16x32_bf16 v[160:163], a[164:167], a[4:7], v[220:223]\n"
    "s_mov_b32 s56, 0\n"
    "v_exp_f32_e32 v158, v158\n"
    "v_mfma_f32_16x16x32_bf16 v[160:163], a[160:163], a[0:3], v[160:163]\n"
    "s_waitcnt lgkmcnt(2)\n"
    "ds_read_b128 a[184:187], v242 offset:30720\n"
    "ds_read_b128 a[188:191], v243 offset:30720\n"
    "v_mfma_f32_16x16x32_bf16 v[160:163], a[168:171], a[8:11], v[160:163]\n"
    "buffer_load_dwordx4 v[180:183], v250, s[8:11], s62 offen offset:2048 sc1\n"
    "v_mfma_f32_16x16x32_bf16 v[160:163], a[172:175], a[12:15], v[160:163]\n"
    "ds_read_b128 a[160:163], v240 offset:0\n"
    "ds_read_b128 a[164:167], v241 offset:0\n"
    "v_mfma_f32_16x16x32_bf16 v[168:171], a[172:175], a[44:47], v[168:171]\n"
    "v_exp_f32_e32 v159, v159\n"
    "v_mfma_f32_16x16x32_bf16 v[168:171], a[168:171], a[40:43], v[168:171]\n"
    "s_waitcnt lgkmcnt(4)\n"
    "ds_read_b128 a[168:171], v240 offset:4096\n"
    "v_cvt_pk_bf16_f32 v156, v156, v157\n"
    "v_mfma_f32_16x16x32_bf16 v[168:171], a[176:179], a[48:51], v[168:171]\n"
    "ds_write_addtid_b32 v156 offset:0\n"
    "v_add_f32_e32 v128, v131, v135\n"
    "v_mfma_f32_16x16x32_bf16 v[168:171], a[180:183], a[52:55], v[168:171]\n"
    "v_min3_f32 v129, v131, v135, v139\n"
    "v_add_f32_e32 v128, v128, v139\n"
    "v_mfma_f32_16x16x32_bf16 v[160:163], a[180:183], a[20:23], v[160:163]\n"
    "ds_read_b128 a[172:175], v241 offset:4096\n"
    "v_mfma_f32_16x16x32_bf16 v[160:163], a[176:179], a[16:19], v[160:163]\n"
    "s_waitcnt lgkmcnt(5)\n"
    "ds_read_b128 a[176:179], v242 offset:0\n"
    "ds_read_b128 a[180:183], v243 offset:0\n"
    "v_mfma_f32_16x16x32_bf16 v[160:163], a[184:187], a[24:27], v[160:163]\n"
    "buffer_store_dwordx4 v[116:119], v250, s[8:11], s29 offen offset:3072 sc1\n"
    "v_mfma_f32_16x16x32_bf16 v[160:163], a[188:191], a[28:31], v[160:163]\n"
    "v_exp_f32_e32 v164, v164\n"
    "v_mfma_f32_16x16x32_bf16 v[168:171], a[188:191], a[60:63], v[168:171]\n"
    "v_exp_f32_e32 v165, v165\n"
    "v_mfma_f32_16x16x32_bf16 v[168:171], a[184:187], a[56:59], v[168:171]\n"
    "s_waitcnt lgkmcnt(5)\n"
    "ds_read_b128 a[184:187], v242 offset:4096\n"
    "v_cvt_pk_bf16_f32 v157, v158, v159\n"
    "v_mfma_f32_16x16x32_bf16 v[204:207], a[160:163], a[32:35], v[224:227]\n"
    "buffer_load_dwordx4 v[184:187], v250, s[8:11], s62 offen offset:3072 sc1\n"
    "v_mfma_f32_16x16x32_bf16 v[204:207], a[164:167], a[36:39], v[204:207]\n"
    "s_add_u32 s62, s58, 0\n"
    "ds_read_b128 a[188:191], v243 offset:4096\n"
    "ds_write_addtid_b32 v157 offset:256\n"
    "v_mfma_f32_16x16x32_bf16 v[196:199], a[164:167], a[4:7], v[220:223]\n"
    "v_exp_f32_e32 v166, v166\n"
    "v_mfma_f32_16x16x32_bf16 v[196:199], a[160:163], a[0:3], v[196:199]\n"
    "s_waitcnt lgkmcnt(5)\n"
    "ds_read_b128 a[160:163], v240 offset:2048\n"
    "v_mfma_f32_16x16x32_bf16 v[196:199], a[168:171], a[8:11], v[196:199]\n"
    "v_exp_f32_e32 v167, v167\n"
    "v_mfma_f32_16x16x32_bf16 v[196:199], a[172:175], a[12:15], v[196:199]\n"
    "ds_read_b128 a[164:167], v241 offset:2048\n"
    "v_cvt_pk_bf16_f32 v164, v164, v165\n"
    "v_mfma_f32_16x16x32_bf16 v[204:207], a[172:175], a[44:47], v[204:207]\n"
    "v_exp_f32_e32 v160, v160\n"
    "v_mfma_f32_16x16x32_bf16 v[204:207], a[168:171], a[40:43], v[204:207]\n"
    "s_waitcnt lgkmcnt(5)\n"
    "v_exp_f32_e32 v161, v161\n"
    "v_mfma_f32_16x16x32_bf16 v[204:207], a[176:179], a[48:51], v[204:207]\n"
    "buffer_load_dwordx4 v[28:31], v250, s[8:11], s62 offen offset:0 sc1\n"
    "v_mfma_f32_16x16x32_bf16 v[204:207], a[180:183], a[52:55], v[204:207]\n"
    "ds_read_b128 a[168:171], v240 offset:6144\n"
    "v_cvt_pk_bf16_f32 v165, v166, v167\n"
    "v_mfma_f32_16x16x32_bf16 v[196:199], a[180:183], a[20:23], v[196:199]\n"
    "ds_write_addtid_b32 v164 offset:1024\n"
    "v_exp_f32_e32 v162, v162\n"
    "v_mfma_f32_16x16x32_bf16 v[196:199], a[176:179], a[16:19], v[196:199]\n"
    "s_waitcnt lgkmcnt(5)\n"
    "ds_read_b128 a[176:179], v242 offset:2048\n"
    "v_exp_f32_e32 v163, v163\n"
    "v_mfma_f32_16x16x32_bf16 v[196:199], a[184:187], a[24:27], v[196:199]\n"
    "ds_read_b128 a[172:175], v241 offset:6144\n"
    "v_cvt_pk_bf16_f32 v158, v160, v161\n"
    "v_mfma_f32_16x16x32_bf16 v[196:199], a[188:191], a[28:31], v[196:199]\n"
    "v_exp_f32_e32 v168, v168\n"
    "v_mfma_f32_16x16x32_bf16 v[204:207], a[188:191], a[60:63], v[204:207]\n"
    "v_exp_f32_e32 v169, v169\n"
    "v_mfma_f32_16x16x32_bf16 v[204:207], a[184:187], a[56:59], v[204:207]\n"
    "s_waitcnt lgkmcnt(4)\n"
    "ds_read_b128 a[180:183], v243 offset:2048\n"
    "v_cvt_pk_bf16_f32 v159, v162, v163\n"
    "v_mfma_f32_16x16x32_bf16 v[208:211], a[160:163], a[32:35], v[224:227]\n"
    "buffer_load_dwordx4 v[48:51], v250, s[8:11], s62 offen offset:1024 sc1\n"
    "ds_write_addtid_b32 v165 offset:1280\n"
    "v_mfma_f32_16x16x32_bf16 v[208:211], a[164:167], a[36:39], v[208:211]\n"
    "ds_write_addtid_b32 v158 offset:512\n"
    "v_exp_f32_e32 v170, v170\n"
    "v_mfma_f32_16x16x32_bf16 v[200:203], a[164:167], a[4:7], v[220:223]\n"
    "ds_read_b128 a[184:187], v242 offset:6144\n"
    "v_exp_f32_e32 v196, v196\n"
    "v_mfma_f32_16x16x32_bf16 v[200:203], a[160:163], a[0:3], v[200:203]\n"
    "s_waitcnt lgkmcnt(4)\n"
    "v_exp_f32_e32 v197, v197\n"
    "v_mfma_f32_16x16x32_bf16 v[200:203], a[168:171], a[8:11], v[200:203]\n"
    "ds_read_b128 a[188:191], v243 offset:6144\n"
    "v_exp_f32_e32 v198, v198\n"
    "v_mfma_f32_16x16x32_bf16 v[200:203], a[172:175], a[12:15], v[200:203]\n"
    "v_exp_f32_e32 v171, v171\n"
    "v_mfma_f32_16x16x32_bf16 v[208:211], a[172:175], a[44:47], v[208:211]\n"
    "v_exp_f32_e32 v199, v199\n"
    "v_mfma_f32_16x16x32_bf16 v[208:211], a[168:171], a[40:43], v[208:211]\n"
    "s_waitcnt lgkmcnt(4)\n"
    "ds_read_b128 a[160:163], v240 offset:8192\n"
    "ds_read_b128 a[164:167], v241 offset:8192\n"
    "v_mfma_f32_16x16x32_bf16 v[208:211], a[176:179], a[48:51], v[208:211]\n"
    "buffer_load_dwordx4 v[32:35], v250, s[8:11], s62 offen offset:2048 sc1\n"
    "ds_read_b128 a[168:171], v240 offset:12288\n"
    "v_mfma_f32_16x16x32_bf16 v[208:211], a[180:183], a[52:55], v[208:211]\n"
    "ds_read_b128 a[172:175], v241 offset:12288\n"
    "v_cvt_pk_bf16_f32 v196, v196, v197\n"
    "v_mfma_f32_16x16x32_bf16 v[200:203], a[180:183], a[20:23], v[200:203]\n"
    "v_exp_f32_e32 v204, v204\n"
    "v_mfma_f32_16x16x32_bf16 v[200:203], a[176:179], a[16:19], v[200:203]\n"
    "s_waitcnt lgkmcnt(4)\n"
    "v_exp_f32_e32 v205, v205\n"
    "v_mfma_f32_16x16x32_bf16 v[200:203], a[184:187], a[24:27], v[200:203]\n"
    "v_cvt_pk_bf16_f32 v197, v198, v199\n"
    "v_exp_f32_e32 v206, v206\n"
    "v_mfma_f32_16x16x32_bf16 v[200:203], a[188:191], a[28:31], v[200:203]\n"
    "ds_read_b128 a[176:179], v242 offset:8192\n"
    "v_cvt_pk_bf16_f32 v166, v168, v169\n"
    "v_mfma_f32_16x16x32_bf16 v[208:211], a[188:191], a[60:63], v[208:211]\n"
    "v_exp_f32_e32 v207, v207\n"
    "v_mfma_f32_16x16x32_bf16 v[208:211], a[184:187], a[56:59], v[208:211]\n"
    "s_waitcnt lgkmcnt(3)\n"
    "ds_read_b128 a[180:183], v243 offset:8192\n"
    "v_cvt_pk_bf16_f32 v204, v204, v205\n"
    "v_mfma_f32_16x16x32_bf16 v[212:215], a[160:163], a[32:35], v[224:227]\n"
    "buffer_load_dwordx4 v[52:55], v250, s[8:11], s62 offen offset:3072 sc1\n"
    "v_mfma_f32_16x16x32_bf16 v[212:215], a[164:167], a[36:39], v[212:215]\n"
    "s_add_u32 s62, s58, 4096\n"
    "v_exp_f32_e32 v200, v200\n"
    "v_mfma_f32_16x16x32_bf16 v[172:175], a[164:167], a[4:7], v[220:223]\n"
    "v_exp_f32_e32 v201, v201\n"
    "v_mfma_f32_16x16x32_bf16 v[172:175], a[160:163], a[0:3], v[172:175]\n"
    "s_waitcnt lgkmcnt(2)\n"
    "ds_read_b128 a[160:163], v240 offset:10240\n"
    "v_cvt_pk_bf16_f32 v205, v206, v207\n"
    "v_mfma_f32_16x16x32_bf16 v[172:175], a[168:171], a[8:11], v[172:175]\n"
    "ds_read_b128 a[184:187], v242 offset:12288\n"
    "ds_read_b128 a[188:191], v243 offset:12288\n"
    "v_mfma_f32_16x16x32_bf16 v[172:175], a[172:175], a[12:15], v[172:175]\n"
    "ds_read_b128 a[164:167], v241 offset:10240\n"
    "v_exp_f32_e32 v202, v202\n"
    "v_mfma_f32_16x16x32_bf16 v[212:215], a[172:175], a[44:47], v[212:215]\n"
    "ds_write_addtid_b32 v159 offset:768\n"
    "v_cvt_pk_bf16_f32 v167, v170, v171\n"
    "v_mfma_f32_16x16x32_bf16 v[212:215], a[168:171], a[40:43], v[212:215]\n"
    "s_waitcnt lgkmcnt(5)\n"
    "ds_write_addtid_b32 v166 offset:1536\n"
    "v_exp_f32_e32 v203, v203\n"
    "v_mfma_f32_16x16x32_bf16 v[212:215], a[176:179], a[48:51], v[212:215]\n"
    "buffer_load_dwordx4 v[36:39], v250, s[8:11], s62 offen offset:0 sc1\n"
    "v_mfma_f32_16x16x32_bf16 v[212:215], a[180:183], a[52:55], v[212:215]\n"
    "ds_read_b128 a[168:171], v240 offset:14336\n"
    "v_cvt_pk_bf16_f32 v198, v200, v201\n"
    "v_mfma_f32_16x16x32_bf16 v[172:175], a[180:183], a[20:23], v[172:175]\n"
    "ds_read_b128 a[172:175], v241 offset:14336\n"
    "v_exp_f32_e32 v208, v208\n"
    "v_mfma_f32_16x16x32_bf16 v[172:175], a[176:179], a[16:19], v[172:175]\n"
    "s_waitcnt lgkmcnt(5)\n"
    "v_exp_f32_e32 v209, v209\n"
    "v_mfma_f32_16x16x32_bf16 v[172:175], a[184:187], a[24:27], v[172:175]\n"
    "ds_read_b128 a[176:179], v242 offset:10240\n"
    "v_cvt_pk_bf16_f32 v199, v202, v203\n"
    "v_mfma_f32_16x16x32_bf16 v[172:175], a[188:191], a[28:31], v[172:175]\n"
    "ds_read_b128 a[180:183], v243 offset:10240\n"
    "ds_write_addtid_b32 v167 offset:1792\n"
    "v_mfma_f32_16x16x32_bf16 v[212:215], a[188:191], a[60:63], v[212:215]\n"
    "v_exp_f32_e32 v210, v210\n"
    "v_mfma_f32_16x16x32_bf16 v[212:215], a[184:187], a[56:59], v[212:215]\n"
    "s_waitcnt lgkmcnt(7)\n"
    "ds_read_b128 a[184:187], v242 offset:14336\n"
    "v_exp_f32_e32 v172, v172\n"
    "v_mfma_f32_16x16x32_bf16 v[216:219], a[160:163], a[32:35], v[224:227]\n"
    "buffer_load_dwordx4 v[56:59], v250, s[8:11], s62 offen offset:1024 sc1\n"
    "ds_read_b128 a[188:191], v243 offset:14336\n"
    "v_mfma_f32_16x16x32_bf16 v[216:219], a[164:167], a[36:39], v[216:219]\n"
    "s_waitcnt lgkmcnt(2)\n"
    "v_mfma_f32_16x16x32_bf16 v[176:179], a[164:167], a[4:7], v[220:223]\n"
    "s_barrier\n"
    "v_mfma_f32_16x16x32_bf16 v[176:179], a[160:163], a[0:3], v[176:179]\n"
    "ds_read_b64_tr_b16 a[160:161], v244 offset:24576\n"
    "ds_read2st64_b32 v[160:161], v249 offset0:0 offset1:1\n"
    "v_mfma_f32_16x16x32_bf16 v[176:179], a[168:171], a[8:11], v[176:179]\n"
    "ds_read_b64_tr_b16 a[162:163], v244 offset:26624\n"
    "v_exp_f32_e32 v173, v173\n"
    "v_mfma_f32_16x16x32_bf16 v[176:179], a[172:175], a[12:15], v[176:179]\n"
    "ds_read2st64_b32 v[162:163], v249 offset0:2 offset1:3\n"
    "v_exp_f32_e32 v174, v174\n"
    "v_mfma_f32_16x16x32_bf16 v[216:219], a[172:175], a[44:47], v[216:219]\n"
    "ds_read_b64_tr_b16 a[164:165], v244 offset:57344\n"
    "ds_read_b64_tr_b16 a[166:167], v244 offset:59392\n"
    "v_mfma_f32_16x16x32_bf16 v[216:219], a[168:171], a[40:43], v[216:219]\n"
    "ds_read2st64_b32 v[168:169], v249 offset0:4 offset1:5\n"
    "ds_read2st64_b32 v[170:171], v249 offset0:6 offset1:7\n"
    "v_mfma_f32_16x16x32_bf16 v[216:219], a[176:179], a[48:51], v[216:219]\n"
    "s_waitcnt vmcnt(7)\n"
    "buffer_store_dword v254, v231, s[8:11], s38 offen offset:0 sc1\n"
    "v_mfma_f32_16x16x32_bf16 v[216:219], a[180:183], a[52:55], v[216:219]\n"
    "v_exp_f32_e32 v175, v175\n"
    "v_mfma_f32_16x16x32_bf16 v[176:179], a[180:183], a[20:23], v[176:179]\n"
    "ds_read_b64_tr_b16 a[168:169], v245 offset:24576\n"
    "ds_read_b64_tr_b16 a[170:171], v245 offset:26624\n"
    "v_mfma_f32_16x16x32_bf16 v[176:179], a[176:179], a[16:19], v[176:179]\n"
    "s_waitcnt lgkmcnt(10)\n"
    "ds_read_b64_tr_b16 a[172:173], v245 offset:57344\n"
    "v_cvt_pk_bf16_f32 v172, v172, v173\n"
    "v_mfma_f32_16x16x32_bf16 v[176:179], a[184:187], a[24:27], v[176:179]\n"
    "buffer_load_dwordx4 v[68:71], v250, s[8:11], s62 offen offset:2048 sc1\n"
    "ds_read_b64_tr_b16 a[174:175], v245 offset:59392\n"
    "v_mfma_f32_16x16x32_bf16 v[176:179], a[188:191], a[28:31], v[176:179]\n"
    "ds_read_b64_tr_b16 a[176:177], v246 offset:24576\n"
    "v_exp_f32_e32 v211, v211\n"
    "v_mfma_f32_16x16x32_bf16 v[216:219], a[188:191], a[60:63], v[216:219]\n"
    "ds_read_b64_tr_b16 a[178:179], v246 offset:26624\n"
    "ds_read_b64_tr_b16 a[180:181], v246 offset:57344\n"
    "v_mfma_f32_16x16x32_bf16 v[216:219], a[184:187], a[56:59], v[216:219]\n"
    "s_waitcnt lgkmcnt(7)\n"
    "v_exp_f32_e32 v212, v212\n"
    "v_mfma_f32_16x16x32_bf16 a[240:243], a[160:163], v[164:167], a[240:243]\n"
    "ds_read_b64_tr_b16 a[182:183], v246 offset:59392\n"
    "ds_read_b64_tr_b16 a[184:185], v247 offset:24576\n"
    "v_mfma_f32_16x16x32_bf16 a[240:243], a[164:167], v[168:171], a[240:243]\n"
    "ds_write_addtid_b32 v196 offset:2048\n"
    "v_exp_f32_e32 v213, v213\n"
    "v_mfma_f32_16x16x32_bf16 a[236:239], a[164:167], v[160:163], a[236:239]\n"
    "ds_read_b64_tr_b16 a[186:187], v247 offset:26624\n"
    "ds_read_b64_tr_b16 a[188:189], v247 offset:57344\n"
    "v_mfma_f32_16x16x32_bf16 a[236:239], a[160:163], v[156:159], a[236:239]\n"
    "s_waitcnt lgkmcnt(8)\n"
    "ds_read_b64_tr_b16 a[190:191], v247 offset:59392\n"
    "v_cvt_pk_bf16_f32 v173, v174, v175\n"
    "v_mfma_f32_16x16x32_bf16 v[0:3], a[168:171], v[156:159], v[0:3]\n"
    "buffer_load_dwordx4 v[88:91], v250, s[8:11], s62 offen offset:3072 sc1\n"
    "ds_write_addtid_b32 v172 offset:4096\n"
    "v_mfma_f32_16x16x32_bf16 v[0:3], a[172:175], v[160:163], v[0:3]\n"
    "s_add_u32 s62, s58, 8192\n"
    "ds_read_b64_tr_b16 a[160:161], v244 offset:28672\n"
    "ds_read_b64_tr_b16 a[162:163], v244 offset:30720\n"
    "v_mfma_f32_16x16x32_bf16 v[4:7], a[172:175], v[168:171], v[4:7]\n"
    "ds_read_b64_tr_b16 a[164:165], v244 offset:61440\n"
    "v_exp_f32_e32 v214, v214\n"
    "v_mfma_f32_16x16x32_bf16 v[4:7], a[168:171], v[164:167], v[4:7]\n"
    "s_waitcnt lgkmcnt(9)\n"
    "ds_read_b64_tr_b16 a[166:167], v244 offset:63488\n"
    "ds_read_b64_tr_b16 a[168:169], v245 offset:28672\n"
    "v_mfma_f32_16x16x32_bf16 v[24:27], a[176:179], v[164:167], v[24:27]\n"
    "ds_read_b64_tr_b16 a[170:171], v245 offset:30720\n"
    "v_exp_f32_e32 v215, v215\n"
    "v_mfma_f32_16x16x32_bf16 v[24:27], a[180:183], v[168:171], v[24:27]\n"
    "ds_read_b64_tr_b16 a[172:173], v245 offset:61440\n"
    "v_cvt_pk_bf16_f32 v212, v212, v213\n"
    "v_mfma_f32_16x16x32_bf16 v[20:23], a[180:183], v[160:163], v[20:23]\n"
    "v_exp_f32_e32 v176, v176\n"
    "v_mfma_f32_16x16x32_bf16 v[20:23], a[176:179], v[156:159], v[20:23]\n"
    "s_waitcnt lgkmcnt(8)\n"
    "ds_read_b64_tr_b16 a[174:175], v245 offset:63488\n"
    "v_exp_f32_e32 v177, v177\n"
    "v_mfma_f32_16x16x32_bf16 v[40:43], a[184:187], v[156:159], v[40:43]\n"
    "buffer_load_dwordx4 v[72:75], v250, s[8:11], s62 offen offset:0 sc1\n"
    "v_mfma_f32_16x16x32_bf16 v[40:43], a[188:191], v[160:163], v[40:43]\n"
    "ds_read_b64_tr_b16 a[176:177], v246 offset:28672\n"
    "ds_read_b64_tr_b16 a[178:179], v246 offset:30720\n"
    "v_mfma_f32_16x16x32_bf16 v[44:47], a[188:191], v[168:171], v[44:47]\n"
    "ds_write_addtid_b32 v197 offset:2304\n"
    "ds_write_addtid_b32 v204 offset:3072\n"
    "v_mfma_f32_16x16x32_bf16 v[44:47], a[184:187], v[164:167], v[44:47]\n"
    "s_waitcnt lgkmcnt(8)\n"
    "ds_read_b64_tr_b16 a[180:181], v246 offset:61440\n"
    "ds_write_addtid_b32 v173 offset:4352\n"
    "v_mfma_f32_16x16x32_bf16 v[64:67], a[160:163], v[164:167], v[64:67]\n"
    "ds_read_b64_tr_b16 a[182:183], v246 offset:63488\n"
    "ds_read_b64_tr_b16 a[184:185], v247 offset:28672\n"
    "v_mfma_f32_16x16x32_bf16 v[64:67], a[164:167], v[168:171], v[64:67]\n"
    "ds_read_b64_tr_b16 a[186:187], v247 offset:30720\n"
    "v_cvt_pk_bf16_f32 v213, v214, v215\n"
    "v_mfma_f32_16x16x32_bf16 v[60:63], a[164:167], v[160:163], v[60:63]\n"
    "ds_write_addtid_b32 v205 offset:3328\n"
    "ds_write_addtid_b32 v198 offset:2560\n"
    "v_mfma_f32_16x16x32_bf16 v[60:63], a[160:163], v[156:159], v[60:63]\n"
    "s_waitcnt lgkmcnt(11)\n"
    "ds_read_b64_tr_b16 a[188:189], v247 offset:61440\n"
    "ds_read_b64_tr_b16 a[190:191], v247 offset:63488\n"
    "v_mfma_f32_16x16x32_bf16 v[80:83], a[168:171], v[156:159], v[80:83]\n"
    "buffer_load_dwordx4 v[92:95], v250, s[8:11], s62 offen offset:1024 sc1\n"
    "v_mfma_f32_16x16x32_bf16 v[80:83], a[172:175], v[160:163], v[80:83]\n"
    "ds_write_addtid_b32 v212 offset:5120\n"
    "v_cvt_pk_bf16_f32 v206, v208, v209\n"
    "v_mfma_f32_16x16x32_bf16 v[84:87], a[172:175], v[168:171], v[84:87]\n"
    "ds_read_b128 a[160:163], v240 offset:16384\n"
    "ds_read_b128 a[164:167], v241 offset:16384\n"
    "v_mfma_f32_16x16x32_bf16 v[84:87], a[168:171], v[164:167], v[84:87]\n"
    "s_waitcnt lgkmcnt(9)\n"
    "v_mfma_f32_16x16x32_bf16 v[104:107], a[176:179], v[164:167], v[104:107]\n"
    "s_barrier\n"
    "v_mfma_f32_16x16x32_bf16 v[104:107], a[180:183], v[168:171], v[104:107]\n"
    "ds_write_addtid_b32 v213 offset:5376\n"
    "v_exp_f32_e32 v178, v178\n"
    "v_mfma_f32_16x16x32_bf16 v[100:103], a[180:183], v[160:163], v[100:103]\n"
    "s_add_u32 s31, s47, s35\n"
    "ds_read_b128 a[168:171], v240 offset:20480\n"
    "ds_write_addtid_b32 v199 offset:2816\n"
    "v_mfma_f32_16x16x32_bf16 v[100:103], a[176:179], v[156:159], v[100:103]\n"
    "s_waitcnt lgkmcnt(6)\n"
    "ds_read_b128 a[172:175], v241 offset:20480\n"
    "ds_write_addtid_b32 v206 offset:3584\n"
    "v_mfma_f32_16x16x32_bf16 v[120:123], a[184:187], v[156:159], v[120:123]\n"
    "s_add_u32 m0, s55, 24576\n"
    "v_exp_f32_e32 v179, v179\n"
    "v_mfma_f32_16x16x32_bf16 v[120:123], a[188:191], v[160:163], v[120:123]\n"
    "buffer_load_dwordx4 v251, s[8:11], s31 offen offset:0 sc1 lds\n"
    "v_mfma_f32_16x16x32_bf16 v[124:127], a[188:191], v[168:171], v[124:127]\n"
    "s_mov_b32 m0, s63\n"
    "v_cvt_pk_bf16_f32 v207, v210, v211\n"
    "v_cvt_pk_bf16_f32 v174, v176, v177\n"
    "v_mfma_f32_16x16x32_bf16 v[124:127], a[184:187], v[164:167], v[124:127]\n"
    "s_waitcnt lgkmcnt(5)\n"
    "ds_read_b128 a[176:179], v242 offset:16384\n"
    "ds_read_b128 a[180:183], v243 offset:16384\n"
    "v_mfma_f32_16x16x32_bf16 v[148:151], a[160:163], a[32:35], v[224:227]\n"
    "buffer_load_dwordx4 v[76:79], v250, s[8:11], s62 offen offset:2048 sc1\n"
    "v_mfma_f32_16x16x32_bf16 v[148:151], a[164:167], a[36:39], v[148:151]\n"
    "s_add_u32 s31, s31, 0x1000\n"
    "v_exp_f32_e32 v216, v216\n"
    "v_mfma_f32_16x16x32_bf16 v[140:143], a[164:167], a[4:7], v[220:223]\n"
    "s_add_u32 m0, s55, 25600\n"
    "ds_read_b128 a[184:187], v242 offset:20480\n"
    "ds_read_b128 a[188:191], v243 offset:20480\n"
    "v_mfma_f32_16x16x32_bf16 v[140:143], a[160:163], a[0:3], v[140:143]\n"
    "s_waitcnt lgkmcnt(5)\n"
    "v_exp_f32_e32 v217, v217\n"
    "v_mfma_f32_16x16x32_bf16 v[140:143], a[168:171], a[8:11], v[140:143]\n"
    "buffer_load_dwordx4 v251, s[8:11], s31 offen offset:0 sc1 lds\n"
    "v_mfma_f32_16x16x32_bf16 v[140:143], a[172:175], a[12:15], v[140:143]\n"
    "s_mov_b32 m0, s63\n"
    "ds_read_b128 a[160:163], v240 offset:18432\n"
    "v_cvt_pk_bf16_f32 v175, v178, v179\n"
    "v_mfma_f32_16x16x32_bf16 v[148:151], a[172:175], a[44:47], v[148:151]\n"
    "v_exp_f32_e32 v218, v218\n"
    "v_mfma_f32_16x16x32_bf16 v[148:151], a[168:171], a[40:43], v[148:151]\n"
    "s_waitcnt lgkmcnt(3)\n"
    "ds_write_addtid_b32 v174 offset:4608\n"
    "v_exp_f32_e32 v219, v219\n"
    "v_mfma_f32_16x16x32_bf16 v[148:151], a[176:179], a[48:51], v[148:151]\n"
    "s_add_u32 s31, s47, s36\n"
    "ds_read_b128 a[164:167], v241 offset:18432\n"
    "ds_write_addtid_b32 v175 offset:4864\n"
    "v_mfma_f32_16x16x32_bf16 v[148:151], a[180:183], a[52:55], v[148:151]\n"
    "s_add_u32 m0, s55, 26624\n"
    "ds_read_b128 a[168:171], v240 offset:22528\n"
    "v_cvt_pk_bf16_f32 v214, v216, v217\n"
    "v_mfma_f32_16x16x32_bf16 v[140:143], a[180:183], a[20:23], v[140:143]\n"
    "buffer_load_dwordx4 v251, s[8:11], s31 offen offset:0 sc1 lds\n"
    "v_mfma_f32_16x16x32_bf16 v[140:143], a[176:179], a[16:19], v[140:143]\n"
    "s_waitcnt lgkmcnt(5)\n"
    "ds_read_b128 a[176:179], v242 offset:18432\n"
    "ds_read_b128 a[180:183], v243 offset:18432\n"
    "v_mfma_f32_16x16x32_bf16 v[140:143], a[184:187], a[24:27], v[140:143]\n"
    "s_mov_b32 m0, s63\n"
    "ds_read_b128 a[172:175], v241 offset:22528\n"
    "v_cvt_pk_bf16_f32 v215, v218, v219\n"
    "v_mfma_f32_16x16x32_bf16 v[140:143], a[188:191], a[28:31], v[140:143]\n"
    "buffer_load_dword v253, v238, s[8:11], s48 offen offset:0 sc1\n"
    "ds_write_addtid_b32 v214 offset:5632\n"
    "v_mfma_f32_16x16x32_bf16 v[148:151], a[188:191], a[60:63], v[148:151]\n"
    "s_add_u32 s31, s31, 0x1000\n"
    "ds_write_addtid_b32 v207 offset:3840\n"
    "ds_write_addtid_b32 v215 offset:5888\n"
    "v_mfma_f32_16x16x32_bf16 v[148:151], a[184:187], a[56:59], v[148:151]\n"
    "s_waitcnt lgkmcnt(8)\n"
    "ds_read_b128 a[184:187], v242 offset:22528\n"
    "ds_read_b128 a[188:191], v243 offset:22528\n"
    "v_mfma_f32_16x16x32_bf16 v[152:155], a[160:163], a[32:35], v[224:227]\n"
    "s_add_u32 m0, s55, 27648\n"
    "v_exp_f32_e32 v140, v140\n"
    "v_mfma_f32_16x16x32_bf16 v[152:155], a[164:167], a[36:39], v[152:155]\n"
    "s_waitcnt lgkmcnt(3)\n"
    "s_waitcnt lgkmcnt(2)\n"
    "v_mfma_f32_16x16x32_bf16 v[144:147], a[164:167], a[4:7], v[220:223]\n"
    "s_barrier\n"
    "v_mfma_f32_16x16x32_bf16 v[144:147], a[160:163], a[0:3], v[144:147]\n"
    "ds_read2st64_b32 v[200:201], v249 offset0:8 offset1:9\n"
    "v_exp_f32_e32 v141, v141\n"
    "v_mfma_f32_16x16x32_bf16 v[144:147], a[168:171], a[8:11], v[144:147]\n"
    "buffer_load_dwordx4 v251, s[8:11], s31 offen offset:0 sc1 lds\n"
    "ds_read_b64_tr_b16 a[160:161], v244 offset:0\n"
    "v_mfma_f32_16x16x32_bf16 v[144:147], a[172:175], a[12:15], v[144:147]\n"
    "s_mov_b32 m0, s63\n"
    "ds_read_b64_tr_b16 a[162:163], v244 offset:2048\n"
    "ds_read_b64_tr_b16 a[164:165], v244 offset:32768\n"
    "ds_read2st64_b32 v[202:203], v249 offset0:10 offset1:11\n"
    "v_mfma_f32_16x16x32_bf16 v[152:155], a[172:175], a[44:47], v[152:155]\n"
    "s_add_u32 s87, s59, 8192\n"
    "ds_read_b64_tr_b16 a[166:167], v244 offset:34816\n"
    "v_exp_f32_e32 v142, v142\n"
    "v_mfma_f32_16x16x32_bf16 v[152:155], a[168:171], a[40:43], v[152:155]\n"
    "ds_read_b64_tr_b16 a[168:169], v245 offset:0\n"
    "v_exp_f32_e32 v143, v143\n"
    "v_mfma_f32_16x16x32_bf16 v[152:155], a[176:179], a[48:51], v[152:155]\n"
    "s_add_u32 s31, s47, s37\n"
    "ds_read2st64_b32 v[208:209], v249 offset0:12 offset1:13\n"
    "ds_read2st64_b32 v[210:211], v249 offset0:14 offset1:15\n"
    "v_mfma_f32_16x16x32_bf16 v[152:155], a[180:183], a[52:55], v[152:155]\n"
    "s_add_u32 m0, s55, 28672\n"
    "ds_read_b64_tr_b16 a[170:171], v245 offset:2048\n"
    "v_cvt_pk_bf16_f32 v140, v140, v141\n"
    "v_mfma_f32_16x16x32_bf16 v[144:147], a[180:183], a[20:23], v[144:147]\n"
    "buffer_load_dwordx4 v251, s[8:11], s31 offen offset:0 sc1 lds\n"
    "ds_read_b64_tr_b16 a[172:173], v245 offset:32768\n"
    "v_mfma_f32_16x16x32_bf16 v[144:147], a[176:179], a[16:19], v[144:147]\n"
    "s_waitcnt lgkmcnt(11)\n"
    "ds_read_b64_tr_b16 a[174:175], v245 offset:34816\n"
    "v_exp_f32_e32 v148, v148\n"
    "v_mfma_f32_16x16x32_bf16 v[144:147], a[184:187], a[24:27], v[144:147]\n"
    "s_mov_b32 m0, s63\n"
    "ds_read_b64_tr_b16 a[176:177], v246 offset:0\n"
    "v_exp_f32_e32 v149, v149\n"
    "v_mfma_f32_16x16x32_bf16 v[144:147], a[188:191], a[28:31], v[144:147]\n"
    "s_add_u32 s88, s59, 16384\n"
    "ds_read_b64_tr_b16 a[178:179], v246 offset:2048\n"
    "v_cvt_pk_bf16_f32 v141, v142, v143\n"
    "v_mfma_f32_16x16x32_bf16 v[152:155], a[188:191], a[60:63], v[152:155]\n"
    "s_add_u32 s31, s31, 0x1000\n"
    "ds_read_b64_tr_b16 a[180:181], v246 offset:32768\n"
    "v_exp_f32_e32 v150, v150\n"
    "v_mfma_f32_16x16x32_bf16 v[152:155], a[184:187], a[56:59], v[152:155]\n"
    "s_waitcnt lgkmcnt(6)\n"
    "ds_read_b64_tr_b16 a[182:183], v246 offset:34816\n"
    "ds_write_addtid_b32 v140 offset:0\n"
    "v_mfma_f32_16x16x32_bf16 a[240:243], a[160:163], v[204:207], a[240:243]\n"
    "s_add_u32 m0, s55, 29696\n"
    "ds_read_b64_tr_b16 a[184:185], v247 offset:0\n"
    "v_exp_f32_e32 v151, v151\n"
    "v_mfma_f32_16x16x32_bf16 a[240:243], a[164:167], v[208:211], a[240:243]\n"
    "buffer_load_dwordx4 v251, s[8:11], s31 offen offset:0 sc1 lds\n"
    "ds_read_b64_tr_b16 a[186:187], v247 offset:2048\n"
    "v_mfma_f32_16x16x32_bf16 a[236:239], a[164:167], v[200:203], a[236:239]\n"
    "s_mov_b32 m0, s63\n"
    "ds_read_b64_tr_b16 a[188:189], v247 offset:32768\n"
    "v_cvt_pk_bf16_f32 v148, v148, v149\n"
    "v_mfma_f32_16x16x32_bf16 a[236:239], a[160:163], v[196:199], a[236:239]\n"
    "s_waitcnt lgkmcnt(8)\n"
    "ds_read_b64_tr_b16 a[160:161], v244 offset:4096\n"
    "v_exp_f32_e32 v144, v144\n"
    "v_mfma_f32_16x16x32_bf16 v[0:3], a[168:171], v[196:199], v[0:3]\n"
    "s_add_u32 s89, s59, 24576\n"
    "ds_read_b64_tr_b16 a[190:191], v247 offset:34816\n"
    "ds_write_addtid_b32 v141 offset:256\n"
    "v_mfma_f32_16x16x32_bf16 v[0:3], a[172:175], v[200:203], v[0:3]\n"
    "s_add_u32 s31, s46, s36\n"
    "ds_write_addtid_b32 v148 offset:1024\n"
    "v_exp_f32_e32 v145, v145\n"
    "v_mfma_f32_16x16x32_bf16 v[4:7], a[172:175], v[208:211], v[4:7]\n"
    "s_add_u32 m0, s55, 30720\n"
    "ds_read_b64_tr_b16 a[162:163], v244 offset:6144\n"
    "v_cvt_pk_bf16_f32 v149, v150, v151\n"
    "v_mfma_f32_16x16x32_bf16 v[4:7], a[168:171], v[204:207], v[4:7]\n"
    "s_waitcnt lgkmcnt(9)\n"
    "ds_read_b64_tr_b16 a[164:165], v244 offset:36864\n"
    "ds_read_b64_tr_b16 a[166:167], v244 offset:38912\n"
    "v_mfma_f32_16x16x32_bf16 v[24:27], a[176:179], v[204:207], v[24:27]\n"
    "buffer_load_dwordx4 v251, s[12:15], s31 offen offset:0 lds\n"
    "ds_read_b64_tr_b16 a[168:169], v245 offset:4096\n"
    "v_mfma_f32_16x16x32_bf16 v[24:27], a[180:183], v[208:211], v[24:27]\n"
    "s_mov_b32 m0, s63\n"
    "ds_read_b64_tr_b16 a[170:171], v245 offset:6144\n"
    "v_exp_f32_e32 v146, v146\n"
    "v_mfma_f32_16x16x32_bf16 v[20:23], a[180:183], v[200:203], v[20:23]\n"
    "ds_read_b64_tr_b16 a[172:173], v245 offset:36864\n"
    "ds_read_b64_tr_b16 a[174:175], v245 offset:38912\n"
    "v_mfma_f32_16x16x32_bf16 v[20:23], a[176:179], v[196:199], v[20:23]\n"
    "s_waitcnt lgkmcnt(9)\n"
    "ds_read_b64_tr_b16 a[176:177], v246 offset:4096\n"
    "ds_read2st64_b32 v[176:177], v249 offset0:16 offset1:17\n"
    "v_mfma_f32_16x16x32_bf16 v[40:43], a[184:187], v[196:199], v[40:43]\n"
    "s_add_u32 s31, s31, 0x1000\n"
    "ds_read2st64_b32 v[178:179], v249 offset0:18 offset1:19\n"
    "v_exp_f32_e32 v147, v147\n"
    "v_mfma_f32_16x16x32_bf16 v[40:43], a[188:191], v[200:203], v[40:43]\n"
    "s_add_u32 m0, s55, 31744\n"
    "ds_read_b64_tr_b16 a[178:179], v246 offset:6144\n"
    "ds_read2st64_b32 v[216:217], v249 offset0:20 offset1:21\n"
    "v_mfma_f32_16x16x32_bf16 v[44:47], a[188:191], v[208:211], v[44:47]\n"
    "buffer_load_dwordx4 v251, s[12:15], s31 offen offset:0 lds\n"
    "ds_read_b64_tr_b16 a[180:181], v246 offset:36864\n"
    "v_mfma_f32_16x16x32_bf16 v[44:47], a[184:187], v[204:207], v[44:47]\n"
    "s_waitcnt lgkmcnt(10)\n"
    "ds_read_b64_tr_b16 a[182:183], v246 offset:38912\n"
    "ds_read2st64_b32 v[218:219], v249 offset0:22 offset1:23\n"
    "v_mfma_f32_16x16x32_bf16 v[64:67], a[160:163], v[204:207], v[64:67]\n"
    "s_mov_b32 m0, s63\n"
    "ds_read_b64_tr_b16 a[184:185], v247 offset:4096\n"
    "v_cvt_pk_bf16_f32 v142, v144, v145\n"
    "v_mfma_f32_16x16x32_bf16 v[64:67], a[164:167], v[208:211], v[64:67]\n"
    "s_waitcnt vmcnt(5)\n"
    "ds_read_b64_tr_b16 a[186:187], v247 offset:6144\n"
    "v_subrev_u32_e32 v253, s39, v253\n"
    "v_subrev_u32_e32 v253, 0, v253\n"
    "v_mfma_f32_16x16x32_bf16 v[60:63], a[164:167], v[200:203], v[60:63]\n"
    "ds_read_b64_tr_b16 a[188:189], v247 offset:36864\n"
    "v_cmp_le_u32_e32 vcc, 1, v253\n"
    "v_min_u32_e32 v220, 0x13c, v248\n"
    "v_mfma_f32_16x16x32_bf16 v[60:63], a[160:163], v[196:199], v[60:63]\n"
    "s_waitcnt lgkmcnt(11)\n"
    "ds_read_b64_tr_b16 a[190:191], v247 offset:38912\n"
    "v_exp_f32_e32 v152, v152\n"
    "v_mfma_f32_16x16x32_bf16 v[80:83], a[168:171], v[196:199], v[80:83]\n"
    "ds_read_b64_tr_b16 a[160:161], v244 offset:12288\n"
    "ds_read_b64_tr_b16 a[162:163], v244 offset:14336\n"
    "ds_read_b64_tr_b16 a[164:165], v244 offset:45056\n"
    "v_mfma_f32_16x16x32_bf16 v[80:83], a[172:175], v[200:203], v[80:83]\n"
    "s_cbranch_vccnz .Lat_k\n"
    ".Lat_kr:\n"
    "s_cmp_lg_u32 s56, 0\n"
    "s_cbranch_scc1 .Lat_kx\n"
    ".Lat_kxr:\n"
    "v_mfma_f32_16x16x32_bf16 v[84:87], a[172:175], v[208:211], v[84:87]\n"
    "buffer_load_dwordx4 a[244:247], v250, s[8:11], s87 offen offset:0 sc1\n"
    "ds_read_b64_tr_b16 a[166:167], v244 offset:47104\n"
    "v_mfma_f32_16x16x32_bf16 v[84:87], a[168:171], v[204:207], v[84:87]\n"
    "s_waitcnt lgkmcnt(9)\n"
    "ds_write_addtid_b32 v149 offset:1280\n"
    "v_exp_f32_e32 v153, v153\n"
    "v_mfma_f32_16x16x32_bf16 v[104:107], a[176:179], v[204:207], v[104:107]\n"
    "v_cvt_pk_bf16_f32 v143, v146, v147\n"
    "v_exp_f32_e32 v154, v154\n"
    "v_mfma_f32_16x16x32_bf16 v[104:107], a[180:183], v[208:211], v[104:107]\n"
    "buffer_load_dwordx4 v[96:99], v250, s[8:11], s62 offen offset:3072 sc1\n"
    "ds_read_b64_tr_b16 a[168:169], v245 offset:12288\n"
    "v_mfma_f32_16x16x32_bf16 v[100:103], a[180:183], v[200:203], v[100:103]\n"
    "s_add_u32 s62, s58, 12288\n"
    "ds_read_b64_tr_b16 a[170:171], v245 offset:14336\n"
    "ds_read_b64_tr_b16 a[172:173], v245 offset:45056\n"
    "v_mfma_f32_16x16x32_bf16 v[100:103], a[176:179], v[196:199], v[100:103]\n"
    "s_waitcnt lgkmcnt(8)\n"
    "ds_read_b64_tr_b16 a[176:177], v246 offset:12288\n"
    "ds_write_addtid_b32 v142 offset:512\n"
    "v_mfma_f32_16x16x32_bf16 v[120:123], a[184:187], v[196:199], v[120:123]\n"
    "ds_read_b64_tr_b16 a[174:175], v245 offset:47104\n"
    "ds_read_b64_tr_b16 a[178:179], v246 offset:14336\n"
    "v_mfma_f32_16x16x32_bf16 v[120:123], a[188:191], v[200:203], v[120:123]\n"
    "ds_write_addtid_b32 v143 offset:768\n"
    "v_exp_f32_e32 v155, v155\n"
    "v_mfma_f32_16x16x32_bf16 v[124:127], a[188:191], v[208:211], v[124:127]\n"
    "buffer_load_dwordx4 a[248:251], v250, s[8:11], s88 offen offset:0 sc1\n"
    "ds_read_b64_tr_b16 a[180:181], v246 offset:45056\n"
    "v_mfma_f32_16x16x32_bf16 v[124:127], a[184:187], v[204:207], v[124:127]\n"
    "s_waitcnt lgkmcnt(10)\n"
    "ds_read_b64_tr_b16 a[184:185], v247 offset:12288\n"
    "v_cvt_pk_bf16_f32 v150, v152, v153\n"
    "v_mfma_f32_16x16x32_bf16 v[64:67], a[160:163], v[212:215], v[64:67]\n"
    "ds_read_b64_tr_b16 a[182:183], v246 offset:47104\n"
    "v_cvt_pk_bf16_f32 v151, v154, v155\n"
    "v_mfma_f32_16x16x32_bf16 v[64:67], a[164:167], v[216:219], v[64:67]\n"
    "buffer_load_dwordx4 v[108:111], v250, s[8:11], s62 offen offset:0 sc1\n"
    "ds_read_b64_tr_b16 a[186:187], v247 offset:14336\n"
    "v_mfma_f32_16x16x32_bf16 v[60:63], a[164:167], v[176:179], v[60:63]\n"
    "ds_read_b64_tr_b16 a[188:189], v247 offset:45056\n"
    "ds_read_b64_tr_b16 a[190:191], v247 offset:47104\n"
    "v_mfma_f32_16x16x32_bf16 v[60:63], a[160:163], v[172:175], v[60:63]\n"
    "s_waitcnt lgkmcnt(8)\n"
    "ds_write_addtid_b32 v150 offset:1536\n"
    "ds_write_addtid_b32 v151 offset:1792\n"
    "v_mfma_f32_16x16x32_bf16 v[80:83], a[168:171], v[172:175], v[80:83]\n"
    "ds_read_b64_tr_b16 a[160:161], v244 offset:8192\n"
    "v_add_u32_e32 v240, s45, v240\n"
    "v_mfma_f32_16x16x32_bf16 v[80:83], a[172:175], v[176:179], v[80:83]\n"
    "ds_read_b64_tr_b16 a[162:163], v244 offset:10240\n"
    "ds_read_b64_tr_b16 a[164:165], v244 offset:40960\n"
    "v_mfma_f32_16x16x32_bf16 v[84:87], a[172:175], v[216:219], v[84:87]\n"
    "buffer_load_dwordx4 a[252:255], v250, s[8:11], s89 offen offset:0 sc1\n"
    "ds_read_b64_tr_b16 a[166:167], v244 offset:43008\n"
    "v_mfma_f32_16x16x32_bf16 v[84:87], a[168:171], v[212:215], v[84:87]\n"
    "s_waitcnt lgkmcnt(9)\n"
    "s_waitcnt lgkmcnt(4)\n"
    "s_waitcnt vmcnt(7)\n"
    "v_mfma_f32_16x16x32_bf16 v[104:107], a[176:179], v[212:215], v[104:107]\n"
    "s_barrier\n"
    "v_mfma_f32_16x16x32_bf16 v[104:107], a[180:183], v[216:219], v[104:107]\n"
    "buffer_load_dwordx4 v[112:115], v250, s[8:11], s62 offen offset:1024 sc1\n"
    "ds_read2st64_b32 v[144:145], v249 offset0:0 offset1:1\n"
    "v_mfma_f32_16x16x32_bf16 v[100:103], a[180:183], v[176:179], v[100:103]\n"
    "s_add_u32 s31, s46, s37\n"
    "ds_read2st64_b32 v[146:147], v249 offset0:2 offset1:3\n"
    "ds_read2st64_b32 v[152:153], v249 offset0:4 offset1:5\n"
    "v_mfma_f32_16x16x32_bf16 v[100:103], a[176:179], v[172:175], v[100:103]\n"
    "ds_read_b64_tr_b16 a[168:169], v244 offset:16384\n"
    "ds_read_b64_tr_b16 a[170:171], v244 offset:18432\n"
    "v_mfma_f32_16x16x32_bf16 v[120:123], a[184:187], v[172:175], v[120:123]\n"
    "s_add_u32 m0, s55, 4096\n"
    "ds_read_b64_tr_b16 a[172:173], v244 offset:49152\n"
    "ds_read_b64_tr_b16 a[174:175], v244 offset:51200\n"
    "v_mfma_f32_16x16x32_bf16 v[120:123], a[188:191], v[176:179], v[120:123]\n"
    "buffer_load_dwordx4 v251, s[12:15], s31 offen offset:0 lds\n"
    "ds_read2st64_b32 v[154:155], v249 offset0:6 offset1:7\n"
    "v_mfma_f32_16x16x32_bf16 v[124:127], a[188:191], v[216:219], v[124:127]\n"
    "s_mov_b32 m0, s63\n"
    "ds_read_b64_tr_b16 a[176:177], v245 offset:8192\n"
    "ds_read_b64_tr_b16 a[178:179], v245 offset:10240\n"
    "v_mfma_f32_16x16x32_bf16 v[124:127], a[184:187], v[212:215], v[124:127]\n"
    "s_waitcnt lgkmcnt(10)\n"
    "ds_read_b64_tr_b16 a[180:181], v245 offset:40960\n"
    "ds_read_b64_tr_b16 a[182:183], v245 offset:43008\n"
    "v_mfma_f32_16x16x32_bf16 a[240:243], a[160:163], v[212:215], a[240:243]\n"
    "buffer_load_dwordx4 v[8:11], v250, s[8:11], s87 offen offset:1024 sc1\n"
    "v_mfma_f32_16x16x32_bf16 a[240:243], a[164:167], v[216:219], a[240:243]\n"
    "s_add_u32 s31, s31, 0x1000\n"
    "ds_read_b64_tr_b16 a[184:185], v245 offset:16384\n"
    "v_xor_b32_e32 v241, 64, v240\n"
    "v_mfma_f32_16x16x32_bf16 a[236:239], a[164:167], v[176:179], a[236:239]\n"
    "s_add_u32 m0, s55, 5120\n"
    "ds_read_b64_tr_b16 a[186:187], v245 offset:18432\n"
    "ds_read_b64_tr_b16 a[188:189], v245 offset:49152\n"
    "v_mfma_f32_16x16x32_bf16 a[236:239], a[160:163], v[172:175], a[236:239]\n"
    "s_waitcnt lgkmcnt(7)\n"
    "ds_read_b64_tr_b16 a[190:191], v245 offset:51200\n"
    "v_add_u32_e32 v242, 0x10000, v240\n"
    "v_mfma_f32_16x16x32_bf16 a[236:239], a[168:171], v[140:143], a[236:239]\n"
    "buffer_load_dwordx4 v251, s[12:15], s31 offen offset:0 lds\n"
    "v_mfma_f32_16x16x32_bf16 a[236:239], a[172:175], v[144:147], a[236:239]\n"
    "s_mov_b32 m0, s63\n"
    "ds_read_b64_tr_b16 a[160:161], v246 offset:8192\n"
    "v_add_u32_e32 v243, 0x10000, v241\n"
    "v_mfma_f32_16x16x32_bf16 a[240:243], a[172:175], v[152:155], a[240:243]\n"
    "ds_read_b64_tr_b16 a[162:163], v246 offset:10240\n"
    "ds_read_b64_tr_b16 a[164:165], v246 offset:40960\n"
    "v_mfma_f32_16x16x32_bf16 a[240:243], a[168:171], v[148:151], a[240:243]\n"
    "s_waitcnt lgkmcnt(7)\n"
    "ds_read_b64_tr_b16 a[166:167], v246 offset:43008\n"
    "v_add_u32_e32 v252, s54, v240\n"
    "v_mfma_f32_16x16x32_bf16 v[4:7], a[176:179], v[212:215], v[4:7]\n"
    "buffer_load_dwordx4 v[12:15], v250, s[8:11], s88 offen offset:1024 sc1\n"
    "v_mfma_f32_16x16x32_bf16 v[4:7], a[180:183], v[216:219], v[4:7]\n"
    "ds_read_b64_tr_b16 a[168:169], v246 offset:16384\n"
    "v_mfma_f32_16x16x32_bf16 v[0:3], a[180:183], v[176:179], v[0:3]\n"
    "ds_read_b64_tr_b16 a[170:171], v246 offset:18432\n"
    "v_xor_b32_e32 v255, 64, v252\n"
    "v_mfma_f32_16x16x32_bf16 v[0:3], a[176:179], v[172:175], v[0:3]\n"
    "s_waitcnt lgkmcnt(6)\n"
    "ds_read_b64_tr_b16 a[172:173], v246 offset:49152\n"
    "ds_read_b64_tr_b16 a[174:175], v246 offset:51200\n"
    "v_mfma_f32_16x16x32_bf16 v[0:3], a[184:187], v[140:143], v[0:3]\n"
    "buffer_load_dwordx4 v[132:135], v250, s[8:11], s62 offen offset:2048 sc1\n"
    "v_mfma_f32_16x16x32_bf16 v[0:3], a[188:191], v[144:147], v[0:3]\n"
    "ds_read_b64_tr_b16 a[176:177], v247 offset:8192\n"
    "ds_read_b64_tr_b16 a[178:179], v247 offset:10240\n"
    "v_mfma_f32_16x16x32_bf16 v[4:7], a[188:191], v[152:155], v[4:7]\n"
    "ds_read_b64_tr_b16 a[180:181], v247 offset:40960\n"
    "v_lshlrev_b32_e32 v228, 16, v228\n"
    "v_mfma_f32_16x16x32_bf16 v[4:7], a[184:187], v[148:151], v[4:7]\n"
    "s_waitcnt lgkmcnt(7)\n"
    "ds_read_b64_tr_b16 a[182:183], v247 offset:43008\n"
    "v_lshlrev_b32_e32 v229, 16, v229\n"
    "v_mfma_f32_16x16x32_bf16 v[24:27], a[160:163], v[212:215], v[24:27]\n"
    "buffer_load_dwordx4 v[16:19], v250, s[8:11], s89 offen offset:1024 sc1\n"
    "v_mfma_f32_16x16x32_bf16 v[24:27], a[164:167], v[216:219], v[24:27]\n"
    "ds_read_b64_tr_b16 a[184:185], v247 offset:16384\n"
    "v_cvt_pk_bf16_f32 v7, v6, v7\n"
    "v_mfma_f32_16x16x32_bf16 v[20:23], a[164:167], v[176:179], v[20:23]\n"
    "ds_read_b64_tr_b16 a[186:187], v247 offset:18432\n"
    "ds_read_b64_tr_b16 a[188:189], v247 offset:49152\n"
    "v_mfma_f32_16x16x32_bf16 v[20:23], a[160:163], v[172:175], v[20:23]\n"
    "s_waitcnt lgkmcnt(7)\n"
    "ds_read_b64_tr_b16 a[190:191], v247 offset:51200\n"
    "ds_read_b64_tr_b16 a[160:161], v244 offset:20480\n"
    "v_mfma_f32_16x16x32_bf16 v[20:23], a[168:171], v[140:143], v[20:23]\n"
    "buffer_load_dwordx4 v[116:119], v250, s[8:11], s62 offen offset:3072 sc1\n"
    "v_mfma_f32_16x16x32_bf16 v[20:23], a[172:175], v[144:147], v[20:23]\n"
    "s_add_u32 s62, s58, 16384\n"
    "ds_read_b64_tr_b16 a[162:163], v244 offset:22528\n"
    "ds_read_b64_tr_b16 a[164:165], v244 offset:53248\n"
    "v_mfma_f32_16x16x32_bf16 v[24:27], a[172:175], v[152:155], v[24:27]\n"
    "ds_read_b64_tr_b16 a[166:167], v244 offset:55296\n"
    "v_cvt_pk_bf16_f32 v6, v4, v5\n"
    "v_mfma_f32_16x16x32_bf16 v[24:27], a[168:171], v[148:151], v[24:27]\n"
    "s_waitcnt lgkmcnt(8)\n"
    "ds_read_b64_tr_b16 a[168:169], v245 offset:20480\n"
    "ds_read_b64_tr_b16 a[170:171], v245 offset:22528\n"
    "v_mfma_f32_16x16x32_bf16 v[44:47], a[176:179], v[212:215], v[44:47]\n"
    "buffer_load_dwordx4 a[192:195], v250, s[8:11], s87 offen offset:2048 sc1\n"
    "v_mfma_f32_16x16x32_bf16 v[44:47], a[180:183], v[216:219], v[44:47]\n"
    "ds_read_b64_tr_b16 a[172:173], v245 offset:53248\n"
    "ds_read_b64_tr_b16 a[174:175], v245 offset:55296\n"
    "v_mfma_f32_16x16x32_bf16 v[40:43], a[180:183], v[176:179], v[40:43]\n"
    "v_accvgpr_read_b32 v4, a240\n"
    "v_accvgpr_read_b32 v5, a241\n"
    "v_mfma_f32_16x16x32_bf16 v[40:43], a[176:179], v[172:175], v[40:43]\n"
    "s_waitcnt lgkmcnt(8)\n"
    "ds_read_b64_tr_b16 a[176:177], v246 offset:20480\n"
    "v_lshlrev_b32_e32 v230, 16, v230\n"
    "v_mfma_f32_16x16x32_bf16 v[40:43], a[184:187], v[140:143], v[40:43]\n"
    "buffer_load_dwordx4 v[136:139], v250, s[8:11], s62 offen offset:0 sc1\n"
    "ds_read_b64_tr_b16 a[178:179], v246 offset:22528\n"
    "v_mfma_f32_16x16x32_bf16 v[40:43], a[188:191], v[144:147], v[40:43]\n"
    "ds_read_b64_tr_b16 a[180:181], v246 offset:53248\n"
    "ds_read_b64_tr_b16 a[182:183], v246 offset:55296\n"
    "v_mfma_f32_16x16x32_bf16 v[44:47], a[188:191], v[152:155], v[44:47]\n"
    "v_accvgpr_read_b32 v225, a243\n"
    "v_cvt_pk_bf16_f32 v4, v4, v5\n"
    "v_mfma_f32_16x16x32_bf16 v[44:47], a[184:187], v[148:151], v[44:47]\n"
    "s_waitcnt lgkmcnt(8)\n"
    "ds_read_b64_tr_b16 a[184:185], v247 offset:20480\n"
    "ds_read_b64_tr_b16 a[186:187], v247 offset:22528\n"
    "v_mfma_f32_16x16x32_bf16 v[64:67], a[160:163], v[148:151], v[64:67]\n"
    "buffer_load_dwordx4 a[196:199], v250, s[8:11], s88 offen offset:2048 sc1\n"
    "v_mfma_f32_16x16x32_bf16 v[64:67], a[164:167], v[152:155], v[64:67]\n"
    "s_waitcnt lgkmcnt(6)\n"
    "ds_read_b64_tr_b16 a[188:189], v247 offset:53248\n"
    "ds_read_b64_tr_b16 a[190:191], v247 offset:55296\n"
    "v_mfma_f32_16x16x32_bf16 v[84:87], a[172:175], v[152:155], v[84:87]\n"
    "ds_read_b128 a[48:51], v240 offset:0\n"
    "v_accvgpr_read_b32 v5, a242\n"
    "v_mfma_f32_16x16x32_bf16 v[84:87], a[168:171], v[148:151], v[84:87]\n"
    "s_waitcnt lgkmcnt(5)\n"
    "ds_read_b128 a[52:55], v241 offset:0\n"
    "v_cvt_pk_bf16_f32 v5, v5, v225\n"
    "v_mfma_f32_16x16x32_bf16 v[104:107], a[176:179], v[148:151], v[104:107]\n"
    "buffer_store_dwordx4 v[4:7], v250, s[8:11], s80 offen offset:0 sc1\n"
    "v_mfma_f32_16x16x32_bf16 v[104:107], a[180:183], v[152:155], v[104:107]\n"
    "s_waitcnt lgkmcnt(2)\n"
    "v_cvt_pk_bf16_f32 v24, v24, v25\n"
    "v_cvt_pk_bf16_f32 v25, v26, v27\n"
    "v_mfma_f32_16x16x32_bf16 v[124:127], a[188:191], v[152:155], v[124:127]\n"
    "ds_read_b128 a[56:59], v240 offset:32768\n"
    "v_cvt_pk_bf16_f32 v26, v44, v45\n"
    "v_mfma_f32_16x16x32_bf16 v[124:127], a[184:187], v[148:151], v[124:127]\n"
    "ds_read_b128 a[60:63], v241 offset:32768\n"
    "v_cvt_pk_bf16_f32 v27, v46, v47\n"
    "v_mfma_f32_16x16x32_bf16 v[60:63], a[164:167], v[144:147], v[60:63]\n"
    "buffer_load_dwordx4 a[200:203], v250, s[8:11], s89 offen offset:2048 sc1\n"
    "v_mfma_f32_16x16x32_bf16 v[60:63], a[160:163], v[140:143], v[60:63]\n"
    "buffer_store_dwordx4 v[24:27], v250, s[8:11], s80 offen offset:1024 sc1\n"
    "v_mfma_f32_16x16x32_bf16 v[80:83], a[168:171], v[140:143], v[80:83]\n"
    "v_cvt_pk_bf16_f32 v64, v64, v65\n"
    "v_cvt_pk_bf16_f32 v65, v66, v67\n"
    "v_mfma_f32_16x16x32_bf16 v[80:83], a[172:175], v[144:147], v[80:83]\n"
    "v_cvt_pk_bf16_f32 v66, v84, v85\n"
    "v_cvt_pk_bf16_f32 v67, v86, v87\n"
    "v_mfma_f32_16x16x32_bf16 v[100:103], a[180:183], v[144:147], v[100:103]\n"
    "buffer_store_dwordx4 v[64:67], v250, s[8:11], s80 offen offset:2048 sc1\n"
    "v_mfma_f32_16x16x32_bf16 v[100:103], a[176:179], v[140:143], v[100:103]\n"
    "s_mov_b32 s45, 0x71800000\n"
    "v_cvt_pk_bf16_f32 v104, v104, v105\n"
    "v_cvt_pk_bf16_f32 v105, v106, v107\n"
    "v_mfma_f32_16x16x32_bf16 v[120:123], a[184:187], v[140:143], v[120:123]\n"
    "v_cvt_pk_bf16_f32 v106, v124, v125\n"
    "v_cvt_pk_bf16_f32 v107, v126, v127\n"
    "v_mfma_f32_16x16x32_bf16 v[120:123], a[188:191], v[144:147], v[120:123]\n"
    "s_waitcnt lgkmcnt(2)\n"
    "buffer_store_dwordx4 v[104:107], v250, s[8:11], s80 offen offset:3072 sc1\n"
    "v_mfma_f32_16x16x32_bf16 v[156:159], a[216:219], a[48:51], 0\n"
    "buffer_load_dwordx4 a[204:207], v250, s[8:11], s87 offen offset:3072 sc1\n"
    "ds_read_b128 a[160:163], v242 offset:0\n"
    "v_mfma_f32_16x16x32_bf16 v[160:163], a[220:223], a[48:51], 0\n"
    "ds_read_b128 a[164:167], v243 offset:0\n"
    "buffer_store_dword v127, v231, s[8:11], s86 offen offset:0 sc1\n"
    "v_mfma_f32_16x16x32_bf16 v[164:167], a[224:227], a[48:51], 0\n"
    "ds_read_b128 a[168:171], v242 offset:32768\n"
    "v_mfma_f32_16x16x32_bf16 v[168:171], a[228:231], a[48:51], 0\n"
    "ds_read_b128 a[172:175], v243 offset:32768\n"
    "v_add_f32_e32 v225, v123, v127\n"
    "v_mfma_f32_16x16x32_bf16 v[156:159], a[232:235], a[52:55], v[156:159]\n"
    "ds_read_b128 a[176:179], v240 offset:2048\n"
    "v_min3_f32 v129, v129, v123, v127\n"
    "v_mfma_f32_16x16x32_bf16 v[160:163], v[192:195], a[52:55], v[160:163]\n"
    "ds_read_b128 a[180:183], v241 offset:2048\n"
    "v_add_f32_e32 v225, v225, v128\n"
    "v_mfma_f32_16x16x32_bf16 v[164:167], a[64:67], a[52:55], v[164:167]\n"
    "ds_read_b128 a[184:187], v240 offset:34816\n"
    "v_mov_b32_e32 v227, v123\n"
    "v_mfma_f32_16x16x32_bf16 v[168:171], a[68:71], a[52:55], v[168:171]\n"
    "s_waitcnt lgkmcnt(7)\n"
    "ds_read_b128 a[188:191], v241 offset:34816\n"
    "v_cmp_nlt_f32_e32 vcc, 0x12800000, v129\n"
    "v_mfma_f32_16x16x32_bf16 v[156:159], a[72:75], a[56:59], v[156:159]\n"
    "buffer_load_dwordx4 a[208:211], v250, s[8:11], s88 offen offset:3072 sc1\n"
    "ds_read_b128 a[48:51], v242 offset:2048\n"
    "v_mfma_f32_16x16x32_bf16 v[160:163], a[76:79], a[56:59], v[160:163]\n"
    "s_add_u32 s62, s58, 16384\n"
    "v_cmp_nlt_f32_e64 s[28:29], v225, s45\n"
    "v_mfma_f32_16x16x32_bf16 v[164:167], a[80:83], a[56:59], v[164:167]\n"
    "buffer_load_dwordx4 a[0:3], v250, s[8:11], s62 offen offset:1024 sc1\n"
    "v_mfma_f32_16x16x32_bf16 v[168:171], a[84:87], a[56:59], v[168:171]\n"
    "s_or_b32 s45, s29, vcc_hi\n"
    "v_mfma_f32_16x16x32_bf16 v[156:159], a[88:91], a[60:63], v[156:159]\n"
    "s_lshr_b32 s45, s45, 16\n"
    "s_cselect_b32 s45, s51, 0\n"
    "ds_read_b128 a[52:55], v243 offset:2048\n"
    "v_mfma_f32_16x16x32_bf16 v[160:163], a[92:95], a[60:63], v[160:163]\n"
    "s_andn2_b32 s45, s45, s49\n"
    "v_mfma_f32_16x16x32_bf16 v[164:167], a[96:99], a[60:63], v[164:167]\n"
    "s_add_u32 s45, s45, s39\n"
    "v_mfma_f32_16x16x32_bf16 v[168:171], a[100:103], a[60:63], v[168:171]\n"
    "s_waitcnt lgkmcnt(8)\n"
    "v_mov_b32_e32 v254, s45\n"
    "v_mfma_f32_16x16x32_bf16 v[156:159], a[104:107], a[160:163], v[156:159]\n"
    "buffer_load_dwordx4 a[212:215], v250, s[8:11], s89 offen offset:3072 sc1\n"
    "ds_read_b128 a[56:59], v242 offset:34816\n"
    "v_mfma_f32_16x16x32_bf16 v[160:163], a[108:111], a[160:163], v[160:163]\n"
    "s_lshl1_add_u32 s55, s50, s51\n"
    "v_mfma_f32_16x16x32_bf16 v[164:167], a[112:115], a[160:163], v[164:167]\n"
    "s_lshl_b32 s55, s55, 12\n"
    "v_mfma_f32_16x16x32_bf16 v[168:171], a[116:119], a[160:163], v[168:171]\n"
    "s_xor_b32 s31, s93, 2\n"
    "v_mfma_f32_16x16x32_bf16 v[156:159], a[120:123], a[164:167], v[156:159]\n"
    "s_lshl_b32 s31, s31, 8\n"
    "ds_read_b128 a[60:63], v243 offset:34816\n"
    "v_mfma_f32_16x16x32_bf16 v[160:163], a[124:127], a[164:167], v[160:163]\n"
    "s_add_u32 s31, s31, 0x6000\n"
    "v_mfma_f32_16x16x32_bf16 v[164:167], a[128:131], a[164:167], v[164:167]\n"
    "s_mul_i32 s45, s49, 252\n"
    "v_mfma_f32_16x16x32_bf16 v[168:171], a[132:135], a[164:167], v[168:171]\n"
    "s_waitcnt lgkmcnt(8)\n"
    "v_mfma_f32_16x16x32_bf16 v[156:159], a[136:139], a[168:171], v[156:159]\n"
    "buffer_load_dword v220, v220, s[8:11], s60 offen offset:192 sc1\n"
    "ds_read_b128 a[160:163], v240 offset:4096\n"
    "v_mfma_f32_16x16x32_bf16 v[160:163], a[140:143], a[168:171], v[160:163]\n"
    "s_add_u32 s45, s45, s34\n"
    "v_mfma_f32_16x16x32_bf16 v[164:167], a[144:147], a[168:171], v[164:167]\n"
    "s_mul_i32 s45, s45, 101376\n"
    "v_mfma_f32_16x16x32_bf16 v[168:171], a[148:151], a[168:171], v[168:171]\n"
    "s_add_u32 s45, s45, 16654336\n"
    "v_mfma_f32_16x16x32_bf16 v[156:159], a[152:155], a[172:175], v[156:159]\n"
    "s_lshl_b32 s47, s93, 11\n"
    "ds_read_b128 a[164:167], v241 offset:4096\n"
    "v_mfma_f32_16x16x32_bf16 v[160:163], a[156:159], a[172:175], v[160:163]\n"
    "s_add_u32 s54, s47, s45\n"
    "v_and_b32_e32 v251, 0x7ff, v252\n"
    "v_mfma_f32_16x16x32_bf16 v[164:167], v[180:183], a[172:175], v[164:167]\n"
    "s_add_u32 s45, s45, 32768\n"
    "v_add_u32_e32 v251, s63, v251\n"
    "v_mfma_f32_16x16x32_bf16 v[168:171], v[184:187], a[172:175], v[168:171]\n"
    "s_waitcnt lgkmcnt(8)\n"
    "v_mfma_f32_16x16x32_bf16 v[196:199], a[216:219], a[176:179], 0\n"
    "s_cmp_lt_u32 s32, 2\n"
    "s_cselect_b32 s54, s54, 2.0\n"
    "s_cselect_b32 s45, s45, 2.0\n"
    "ds_read_b128 a[168:171], v240 offset:36864\n"
    "v_mfma_f32_16x16x32_bf16 v[200:203], a[220:223], a[176:179], 0\n"
    "s_add_u32 s80, s35, s46\n"
    "v_mfma_f32_16x16x32_bf16 v[204:207], a[224:227], a[176:179], 0\n"
    "s_add_u32 s82, s36, s46\n"
    "v_mul_f32_e32 v156, v156, v228\n"
    "v_mul_f32_e32 v157, v157, v228\n"
    "v_mfma_f32_16x16x32_bf16 v[208:211], a[228:231], a[176:179], 0\n"
    "s_add_u32 s28, s37, s46\n"
    "v_mul_f32_e32 v158, v158, v228\n"
    "v_mul_f32_e32 v159, v159, v228\n"
    "v_mfma_f32_16x16x32_bf16 v[196:199], a[232:235], a[180:183], v[196:199]\n"
    "ds_read_b128 a[172:175], v241 offset:36864\n"
    "v_mul_f32_e32 v160, v160, v228\n"
    "v_mfma_f32_16x16x32_bf16 v[200:203], v[192:195], a[180:183], v[200:203]\n"
    "v_add_u32_e32 v244, s55, v250\n"
    "v_mul_f32_e32 v161, v161, v228\n"
    "v_mfma_f32_16x16x32_bf16 v[204:207], a[64:67], a[180:183], v[204:207]\n"
    "v_lshrrev_b32_e32 v247, 2, v250\n"
    "v_mfma_f32_16x16x32_bf16 v[208:211], a[68:71], a[180:183], v[208:211]\n"
    "s_waitcnt lgkmcnt(8)\n"
    "v_mul_f32_e32 v162, v162, v228\n"
    "v_mfma_f32_16x16x32_bf16 v[196:199], a[72:75], a[184:187], v[196:199]\n"
    "ds_read_b128 a[176:179], v242 offset:4096\n"
    "v_mfma_f32_16x16x32_bf16 v[200:203], a[76:79], a[184:187], v[200:203]\n"
    "v_xor_b32_e32 v249, 64, v251\n"
    "v_mfma_f32_16x16x32_bf16 v[204:207], a[80:83], a[184:187], v[204:207]\n"
    "s_waitcnt vmcnt(4)\n"
    "buffer_store_dword v254, off, s[8:11], s90 offset:0 sc1\n"
    "v_mfma_f32_16x16x32_bf16 v[208:211], a[84:87], a[184:187], v[208:211]\n"
    "v_add_u32_e32 v247, s31, v247\n"
    "v_mul_f32_e32 v163, v163, v228\n"
    "v_mfma_f32_16x16x32_bf16 v[196:199], a[88:91], a[188:191], v[196:199]\n"
    "ds_read_b128 a[180:183], v243 offset:4096\n"
    "v_add_u32_e32 v246, s47, v250\n"
    "v_mfma_f32_16x16x32_bf16 v[200:203], a[92:95], a[188:191], v[200:203]\n"
    "v_and_b32_e32 v245, 0x603f, v247\n"
    "v_bfe_u32 v226, v250, 2, 4\n"
    "v_mfma_f32_16x16x32_bf16 v[204:207], a[96:99], a[188:191], v[204:207]\n"
    "v_mul_f32_e32 v164, v164, v228\n"
    "v_mfma_f32_16x16x32_bf16 v[208:211], a[100:103], a[188:191], v[208:211]\n"
    "s_waitcnt lgkmcnt(8)\n"
    "v_bfe_u32 v225, v250, 6, 2\n"
    "v_lshl_or_b32 v226, v225, 7, v226\n"
    "v_mfma_f32_16x16x32_bf16 v[196:199], a[104:107], a[48:51], v[196:199]\n"
    "ds_read_b128 a[184:187], v242 offset:36864\n"
    "v_mfma_f32_16x16x32_bf16 v[200:203], a[108:111], a[48:51], v[200:203]\n"
    "v_and_b32_e32 v225, 0x60ff, v247\n"
    "v_mul_f32_e32 v165, v165, v228\n"
    "v_mfma_f32_16x16x32_bf16 v[204:207], a[112:115], a[48:51], v[204:207]\n"
    "buffer_load_dwordx4 a[4:7], v250, s[8:11], s62 offen offset:2048 sc1\n"
    "v_mfma_f32_16x16x32_bf16 v[208:211], a[116:119], a[48:51], v[208:211]\n"
    "s_waitcnt vmcnt(20)\n"
    "v_mul_f32_e32 v166, v166, v228\n"
    "v_mfma_f32_16x16x32_bf16 v[196:199], a[120:123], a[52:55], v[196:199]\n"
    "ds_read_b128 a[188:191], v243 offset:36864\n"
    "v_mfma_f32_16x16x32_bf16 v[200:203], a[124:127], a[52:55], v[200:203]\n"
    "v_mul_f32_e32 v167, v167, v228\n"
    "v_mfma_f32_16x16x32_bf16 v[204:207], a[128:131], a[52:55], v[204:207]\n"
    "buffer_load_dwordx4 a[8:11], v250, s[8:11], s62 offen offset:3072 sc1\n"
    "v_mfma_f32_16x16x32_bf16 v[208:211], a[132:135], a[52:55], v[208:211]\n"
    "s_waitcnt lgkmcnt(8)\n"
    "v_mul_f32_e32 v168, v168, v228\n"
    "v_mfma_f32_16x16x32_bf16 v[196:199], a[136:139], a[56:59], v[196:199]\n"
    "s_add_u32 s62, s58, 20480\n"
    "v_mfma_f32_16x16x32_bf16 v[200:203], a[140:143], a[56:59], v[200:203]\n"
    "v_mul_f32_e32 v169, v169, v228\n"
    "v_mul_f32_e32 v170, v170, v228\n"
    "v_mfma_f32_16x16x32_bf16 v[204:207], a[144:147], a[56:59], v[204:207]\n"
    "buffer_load_dwordx4 a[12:15], v250, s[8:11], s62 offen offset:0 sc1\n"
    "v_mfma_f32_16x16x32_bf16 v[208:211], a[148:151], a[56:59], v[208:211]\n"
    "v_mul_f32_e32 v171, v171, v228\n"
    "v_mfma_f32_16x16x32_bf16 v[196:199], a[152:155], a[60:63], v[196:199]\n"
    "ds_read_b128 a[48:51], v251 offset:6144\n"
    "v_mfma_f32_16x16x32_bf16 v[200:203], a[156:159], a[60:63], v[200:203]\n"
    "ds_read_b128 a[52:55], v249 offset:6144\n"
    "v_mfma_f32_16x16x32_bf16 v[204:207], v[180:183], a[60:63], v[204:207]\n"
    "s_barrier\n"
    "v_mfma_f32_16x16x32_bf16 v[208:211], v[184:187], a[60:63], v[208:211]\n"
    "s_waitcnt vmcnt(4)\n"
    "buffer_load_dwordx4 a[16:19], v250, s[8:11], s62 offen offset:1024 sc1\n"
    "v_mfma_f32_16x16x32_bf16 a[236:239], v[234:237], a[244:247], a[236:239]\n"
    "v_mfma_f32_16x16x32_bf16 v[0:3], v[232:235], a[244:247], v[0:3]\n"
    "v_cmp_le_u32_e32 vcc, 0x300, v250\n"
    "v_mfma_f32_16x16x32_bf16 a[236:239], v[234:237], a[248:251], a[236:239]\n"
    "v_cndmask_b32_e32 v227, v220, v227, vcc\n"
    "v_mfma_f32_16x16x32_bf16 v[0:3], v[232:235], a[248:251], v[0:3]\n"
    "buffer_load_dwordx4 a[20:23], v250, s[8:11], s62 offen offset:2048 sc1\n"
    "v_mfma_f32_16x16x32_bf16 a[236:239], v[234:237], a[252:255], a[236:239]\n"
    "v_mfma_f32_16x16x32_bf16 v[0:3], v[232:235], a[252:255], v[0:3]\n"
    "ds_write_b32 v247, v227 offset:0\n"
    "v_mfma_f32_16x16x32_bf16 v[20:23], v[234:237], v[8:11], v[20:23]\n"
    "v_mfma_f32_16x16x32_bf16 v[40:43], v[232:235], v[8:11], v[40:43]\n"
    "buffer_load_dwordx4 a[24:27], v250, s[8:11], s62 offen offset:3072 sc1\n"
    "v_mfma_f32_16x16x32_bf16 v[20:23], v[234:237], v[12:15], v[20:23]\n"
    "s_add_u32 s62, s58, 24576\n"
    "v_accvgpr_read_b32 v64, a236\n"
    "v_accvgpr_read_b32 v65, a237\n"
    "v_mfma_f32_16x16x32_bf16 v[40:43], v[232:235], v[12:15], v[40:43]\n"
    "v_accvgpr_read_b32 v66, a238\n"
    "v_accvgpr_read_b32 v67, a239\n"
    "v_mfma_f32_16x16x32_bf16 v[20:23], v[234:237], v[16:19], v[20:23]\n"
    "v_cvt_pk_bf16_f32 v64, v64, v65\n"
    "v_cvt_pk_bf16_f32 v65, v66, v67\n"
    "v_mfma_f32_16x16x32_bf16 v[40:43], v[232:235], v[16:19], v[40:43]\n"
    "buffer_load_dwordx4 a[28:31], v250, s[8:11], s62 offen offset:0 sc1\n"
    "v_mfma_f32_16x16x32_bf16 v[60:63], v[234:237], a[192:195], v[60:63]\n"
    "v_cvt_pk_bf16_f32 v66, v0, v1\n"
    "v_cvt_pk_bf16_f32 v67, v2, v3\n"
    "v_mfma_f32_16x16x32_bf16 v[80:83], v[232:235], a[192:195], v[80:83]\n"
    "ds_write_b128 v244, v[64:67] offset:0\n"
    "v_mfma_f32_16x16x32_bf16 v[60:63], v[234:237], a[196:199], v[60:63]\n"
    "v_mfma_f32_16x16x32_bf16 v[80:83], v[232:235], a[196:199], v[80:83]\n"
    "buffer_load_dwordx4 a[32:35], v250, s[8:11], s62 offen offset:1024 sc1\n"
    "v_mfma_f32_16x16x32_bf16 v[60:63], v[234:237], a[200:203], v[60:63]\n"
    "v_cvt_pk_bf16_f32 v20, v20, v21\n"
    "v_cvt_pk_bf16_f32 v21, v22, v23\n"
    "v_mfma_f32_16x16x32_bf16 v[80:83], v[232:235], a[200:203], v[80:83]\n"
    "v_cvt_pk_bf16_f32 v22, v40, v41\n"
    "v_cvt_pk_bf16_f32 v23, v42, v43\n"
    "v_mfma_f32_16x16x32_bf16 v[100:103], v[234:237], a[204:207], v[100:103]\n"
    "v_mfma_f32_16x16x32_bf16 v[120:123], v[232:235], a[204:207], v[120:123]\n"
    "buffer_load_dwordx4 a[36:39], v250, s[8:11], s62 offen offset:2048 sc1\n"
    "v_mfma_f32_16x16x32_bf16 v[100:103], v[234:237], a[208:211], v[100:103]\n"
    "ds_write_b128 v244, v[20:23] offset:1024\n"
    "v_mfma_f32_16x16x32_bf16 v[120:123], v[232:235], a[208:211], v[120:123]\n"
    "v_cvt_pk_bf16_f32 v60, v60, v61\n"
    "v_cvt_pk_bf16_f32 v61, v62, v63\n"
    "v_mfma_f32_16x16x32_bf16 v[100:103], v[234:237], a[212:215], v[100:103]\n"
    "v_mfma_f32_16x16x32_bf16 v[120:123], v[232:235], a[212:215], v[120:123]\n"
    "s_waitcnt lgkmcnt(11)\n"
    "v_cvt_pk_bf16_f32 v62, v80, v81\n"
    "v_cvt_pk_bf16_f32 v63, v82, v83\n"
    "v_mfma_f32_16x16x32_bf16 v[172:175], a[216:219], a[160:163], 0\n"
    "buffer_load_dword v253, v226, s[8:11], s61 offen offset:0 sc1\n"
    "v_mfma_f32_16x16x32_bf16 v[176:179], a[220:223], a[160:163], 0\n"
    "ds_write_b128 v244, v[60:63] offset:2048\n"
    "v_mfma_f32_16x16x32_bf16 v[212:215], a[224:227], a[160:163], 0\n"
    "buffer_load_dwordx4 a[40:43], v250, s[8:11], s62 offen offset:3072 sc1\n"
    "v_mfma_f32_16x16x32_bf16 v[216:219], a[228:231], a[160:163], 0\n"
    "s_add_u32 s62, s58, 28672\n"
    "v_cvt_pk_bf16_f32 v100, v100, v101\n"
    "v_cvt_pk_bf16_f32 v101, v102, v103\n"
    "v_mfma_f32_16x16x32_bf16 v[172:175], a[232:235], a[164:167], v[172:175]\n"
    "v_cvt_pk_bf16_f32 v102, v120, v121\n"
    "v_cvt_pk_bf16_f32 v103, v122, v123\n"
    "v_mfma_f32_16x16x32_bf16 v[176:179], v[192:195], a[164:167], v[176:179]\n"
    "ds_write_b128 v244, v[100:103] offset:3072\n"
    "v_mfma_f32_16x16x32_bf16 v[212:215], a[64:67], a[164:167], v[212:215]\n"
    "buffer_load_dwordx4 a[44:47], v250, s[8:11], s62 offen offset:0 sc1\n"
    "v_mfma_f32_16x16x32_bf16 v[216:219], a[68:71], a[164:167], v[216:219]\n"
    "s_waitcnt lgkmcnt(12)\n"
    "v_mfma_f32_16x16x32_bf16 v[172:175], a[72:75], a[168:171], v[172:175]\n"
    "v_mfma_f32_16x16x32_bf16 v[176:179], a[76:79], a[168:171], v[176:179]\n"
    "s_waitcnt lgkmcnt(0)\n"
    "v_mfma_f32_16x16x32_bf16 v[212:215], a[80:83], a[168:171], v[212:215]\n"
    "s_barrier\n"
    "v_mfma_f32_16x16x32_bf16 v[216:219], a[84:87], a[168:171], v[216:219]\n"
    "buffer_load_dwordx4 a[240:243], v250, s[8:11], s62 offen offset:1024 sc1\n"
    "v_mfma_f32_16x16x32_bf16 v[172:175], a[88:91], a[172:175], v[172:175]\n"
    "ds_read_b128 a[244:247], v250 offset:0\n"
    "v_mfma_f32_16x16x32_bf16 v[176:179], a[92:95], a[172:175], v[176:179]\n"
    "ds_read_b128 a[248:251], v250 offset:1024\n"
    "v_mfma_f32_16x16x32_bf16 v[212:215], a[96:99], a[172:175], v[212:215]\n"
    "ds_read_b128 a[192:195], v250 offset:2048\n"
    "v_mfma_f32_16x16x32_bf16 v[216:219], a[100:103], a[172:175], v[216:219]\n"
    "ds_read_b128 a[196:199], v250 offset:3072\n"
    "v_mfma_f32_16x16x32_bf16 v[172:175], a[104:107], a[176:179], v[172:175]\n"
    "buffer_load_dwordx4 v[4:7], v250, s[8:11], s62 offen offset:2048 sc1\n"
    "v_mfma_f32_16x16x32_bf16 v[176:179], a[108:111], a[176:179], v[176:179]\n"
    "ds_read_b128 a[200:203], v250 offset:4096\n"
    "v_mfma_f32_16x16x32_bf16 v[212:215], a[112:115], a[176:179], v[212:215]\n"
    "ds_read_b128 a[204:207], v250 offset:5120\n"
    "v_mfma_f32_16x16x32_bf16 v[216:219], a[116:119], a[176:179], v[216:219]\n"
    "ds_read_b128 a[208:211], v250 offset:6144\n"
    "v_mfma_f32_16x16x32_bf16 v[172:175], a[120:123], a[180:183], v[172:175]\n"
    "buffer_load_dwordx4 v[24:27], v250, s[8:11], s62 offen offset:3072 sc1\n"
    "v_mfma_f32_16x16x32_bf16 v[176:179], a[124:127], a[180:183], v[176:179]\n"
    "v_mul_f32_e32 v196, v196, v229\n"
    "v_mul_f32_e32 v197, v197, v229\n"
    "v_mfma_f32_16x16x32_bf16 v[212:215], a[128:131], a[180:183], v[212:215]\n"
    "ds_read_b128 a[212:215], v250 offset:7168\n"
    "v_mfma_f32_16x16x32_bf16 v[216:219], a[132:135], a[180:183], v[216:219]\n"
    "ds_read2_b32 v[220:221], v245 offset0:0 offset1:16\n"
    "v_mfma_f32_16x16x32_bf16 v[172:175], a[136:139], a[184:187], v[172:175]\n"
    "ds_read2_b32 v[222:223], v245 offset0:32 offset1:48\n"
    "v_mfma_f32_16x16x32_bf16 v[176:179], a[140:143], a[184:187], v[176:179]\n"
    "ds_read2_b32 v[80:81], v245 offset0:64 offset1:80\n"
    "v_mfma_f32_16x16x32_bf16 v[212:215], a[144:147], a[184:187], v[212:215]\n"
    "v_mul_f32_e32 v198, v198, v229\n"
    "v_mul_f32_e32 v199, v199, v229\n"
    "v_mfma_f32_16x16x32_bf16 v[216:219], a[148:151], a[184:187], v[216:219]\n"
    "ds_read2_b32 v[82:83], v245 offset0:96 offset1:112\n"
    "v_mfma_f32_16x16x32_bf16 v[172:175], a[152:155], a[188:191], v[172:175]\n"
    "v_mul_f32_e32 v200, v200, v229\n"
    "v_mfma_f32_16x16x32_bf16 v[176:179], a[156:159], a[188:191], v[176:179]\n"
    "v_mfma_f32_16x16x32_bf16 v[212:215], v[180:183], a[188:191], v[212:215]\n"
    "v_mul_f32_e32 v201, v201, v229\n"
    "v_mfma_f32_16x16x32_bf16 v[216:219], v[184:187], a[188:191], v[216:219]\n"
    "v_mul_f32_e32 v202, v202, v229\n"
    "v_mfma_f32_16x16x32_bf16 v[156:159], v[234:237], a[48:51], v[156:159]\n"
    "v_mul_f32_e32 v203, v203, v229\n"
    "v_mfma_f32_16x16x32_bf16 v[160:163], v[232:235], a[48:51], v[160:163]\n"
    "ds_read_b128 a[64:67], v250 offset:8192\n"
    "v_mul_f32_e32 v204, v204, v229\n"
    "v_mfma_f32_16x16x32_bf16 v[164:167], v[234:237], a[52:55], v[164:167]\n"
    "ds_read_b128 a[68:71], v250 offset:9216\n"
    "v_mfma_f32_16x16x32_bf16 v[168:171], v[232:235], a[52:55], v[168:171]\n"
    "s_waitcnt lgkmcnt(12)\n"
    "ds_read_b128 a[72:75], v250 offset:10240\n"
    "v_mfma_f32_16x16x32_bf16 v[140:143], v[28:31], a[244:247], 0\n"
    "ds_read_b128 a[76:79], v250 offset:11264\n"
    "v_mfma_f32_16x16x32_bf16 v[144:147], v[48:51], a[244:247], 0\n"
    "v_mul_f32_e32 v205, v205, v229\n"
    "v_mul_f32_e32 v206, v206, v229\n"
    "v_mfma_f32_16x16x32_bf16 v[148:151], v[32:35], a[244:247], 0\n"
    "v_mul_f32_e32 v207, v207, v229\n"
    "v_mfma_f32_16x16x32_bf16 v[152:155], v[52:55], a[244:247], 0\n"
    "s_waitcnt vmcnt(5)\n"
    "v_subrev_u32_e32 v253, s39, v253\n"
    "v_cmp_lt_u32_e32 vcc, 1, v253\n"
    "v_mfma_f32_16x16x32_bf16 v[140:143], v[36:39], a[248:251], v[140:143]\n"
    "ds_read_b128 a[80:83], v250 offset:12288\n"
    "v_mfma_f32_16x16x32_bf16 v[144:147], v[56:59], a[248:251], v[144:147]\n"
    "ds_read_b128 a[84:87], v250 offset:13312\n"
    "v_mul_f32_e32 v208, v208, v229\n"
    "v_mfma_f32_16x16x32_bf16 v[148:151], v[68:71], a[248:251], v[148:151]\n"
    "s_cbranch_vccnz .Lat_ppoll\n"
    ".Lat_pok:\n"
    "v_cmp_eq_u32_e32 vcc, 1, v253\n"
    "v_mfma_f32_16x16x32_bf16 v[152:155], v[88:91], a[248:251], v[152:155]\n"
    "s_waitcnt lgkmcnt(9)\n"
    "buffer_load_dwordx4 a[128:131], v250, s[8:11], s54 offen offset:0 sc1\n"
    "v_mfma_f32_16x16x32_bf16 v[140:143], v[72:75], a[192:195], v[140:143]\n"
    "v_add_f32_e32 v220, v220, v221\n"
    "v_mfma_f32_16x16x32_bf16 v[144:147], v[92:95], a[192:195], v[144:147]\n"
    "s_cbranch_vccnz .Lat_redo\n"
    "v_mfma_f32_16x16x32_bf16 v[148:151], v[76:79], a[192:195], v[148:151]\n"
    "buffer_load_dwordx4 a[132:135], v250, s[8:11], s54 offen offset:1024 sc1\n"
    "v_mfma_f32_16x16x32_bf16 v[152:155], v[96:99], a[192:195], v[152:155]\n"
    "s_add_u32 s54, s54, 8192\n"
    "ds_read_b128 a[88:91], v250 offset:14336\n"
    "v_mul_f32_e32 v209, v209, v229\n"
    "v_mfma_f32_16x16x32_bf16 v[140:143], v[108:111], a[196:199], v[140:143]\n"
    "buffer_load_dwordx4 a[136:139], v250, s[8:11], s54 offen offset:0 sc1\n"
    "v_mfma_f32_16x16x32_bf16 v[144:147], v[112:115], a[196:199], v[144:147]\n"
    "ds_read_b128 a[92:95], v250 offset:15360\n"
    "v_mfma_f32_16x16x32_bf16 v[148:151], v[132:135], a[196:199], v[148:151]\n"
    "s_waitcnt vmcnt(3)\n"
    "buffer_load_dwordx4 a[140:143], v250, s[8:11], s54 offen offset:1024 sc1\n"
    "v_mfma_f32_16x16x32_bf16 v[152:155], v[116:119], a[196:199], v[152:155]\n"
    "s_add_u32 s54, s54, 8192\n"
    "s_waitcnt lgkmcnt(10)\n"
    "v_mfma_f32_16x16x32_bf16 v[140:143], v[136:139], a[200:203], v[140:143]\n"
    "buffer_load_dwordx4 a[144:147], v250, s[8:11], s54 offen offset:0 sc1\n"
    "v_mfma_f32_16x16x32_bf16 v[144:147], a[0:3], a[200:203], v[144:147]\n"
    "ds_read_b128 a[56:59], v252 offset:30720\n"
    "v_mfma_f32_16x16x32_bf16 v[148:151], a[4:7], a[200:203], v[148:151]\n"
    "buffer_load_dwordx4 a[148:151], v250, s[8:11], s54 offen offset:1024 sc1\n"
    "v_mfma_f32_16x16x32_bf16 v[152:155], a[8:11], a[200:203], v[152:155]\n"
    "s_add_u32 s54, s54, 8192\n"
    "v_mul_f32_e32 v210, v210, v229\n"
    "v_add_f32_e32 v222, v222, v223\n"
    "v_mfma_f32_16x16x32_bf16 v[140:143], a[12:15], a[204:207], v[140:143]\n"
    "buffer_load_dwordx4 a[152:155], v250, s[8:11], s54 offen offset:0 sc1\n"
    "v_mfma_f32_16x16x32_bf16 v[144:147], a[16:19], a[204:207], v[144:147]\n"
    "ds_read_b128 a[60:63], v255 offset:30720\n"
    "v_mfma_f32_16x16x32_bf16 v[148:151], a[20:23], a[204:207], v[148:151]\n"
    "buffer_load_dwordx4 a[156:159], v250, s[8:11], s54 offen offset:1024 sc1\n"
    "v_mfma_f32_16x16x32_bf16 v[152:155], a[24:27], a[204:207], v[152:155]\n"
    "s_waitcnt lgkmcnt(10)\n"
    "v_mul_f32_e32 v211, v211, v229\n"
    "v_mfma_f32_16x16x32_bf16 v[140:143], a[28:31], a[208:211], v[140:143]\n"
    "buffer_load_dword v227, v248, s[8:11], s45 offen offset:1088 sc1\n"
    "v_mfma_f32_16x16x32_bf16 v[144:147], a[32:35], a[208:211], v[144:147]\n"
    "v_add_f32_e32 v220, v220, v222\n"
    "v_mfma_f32_16x16x32_bf16 v[148:151], a[36:39], a[208:211], v[148:151]\n"
    "v_rcp_f32_e32 v220, v220\n"
    "v_mfma_f32_16x16x32_bf16 v[152:155], a[40:43], a[208:211], v[152:155]\n"
    "v_add_f32_e32 v80, v80, v81\n"
    "v_add_f32_e32 v82, v82, v83\n"
    "v_mfma_f32_16x16x32_bf16 v[140:143], a[44:47], a[212:215], v[140:143]\n"
    "v_mul_f32_e32 v172, v172, v230\n"
    "v_add_f32_e32 v80, v80, v82\n"
    "v_mfma_f32_16x16x32_bf16 v[144:147], a[240:243], a[212:215], v[144:147]\n"
    "v_rcp_f32_e32 v80, v80\n"
    "v_mfma_f32_16x16x32_bf16 v[148:151], v[4:7], a[212:215], v[148:151]\n"
    "ds_read_b128 a[160:163], v252 offset:4096\n"
    "v_mfma_f32_16x16x32_bf16 v[152:155], v[24:27], a[212:215], v[152:155]\n"
    "s_waitcnt lgkmcnt(9)\n"
    "ds_read_b128 a[164:167], v255 offset:4096\n"
    "v_mfma_f32_16x16x32_bf16 v[180:183], v[28:31], a[64:67], 0\n"
    "v_mul_f32_e32 v173, v173, v230\n"
    "v_mfma_f32_16x16x32_bf16 v[184:187], v[48:51], a[64:67], 0\n"
    "v_fma_f32 v140, v140, v220, v156\n"
    "v_fma_f32 v141, v141, v220, v157\n"
    "v_mfma_f32_16x16x32_bf16 v[188:191], v[32:35], a[64:67], 0\n"
    "v_mfma_f32_16x16x32_bf16 v[192:195], v[52:55], a[64:67], 0\n"
    "v_fma_f32 v142, v142, v220, v158\n"
    "v_mfma_f32_16x16x32_bf16 v[180:183], v[36:39], a[68:71], v[180:183]\n"
    "v_fma_f32 v143, v143, v220, v159\n"
    "v_mfma_f32_16x16x32_bf16 v[184:187], v[56:59], a[68:71], v[184:187]\n"
    "v_fma_f32 v144, v144, v220, v160\n"
    "v_fma_f32 v145, v145, v220, v161\n"
    "v_mfma_f32_16x16x32_bf16 v[188:191], v[68:71], a[68:71], v[188:191]\n"
    "v_fma_f32 v146, v146, v220, v162\n"
    "v_mfma_f32_16x16x32_bf16 v[192:195], v[88:91], a[68:71], v[192:195]\n"
    "s_waitcnt lgkmcnt(8)\n"
    "v_fma_f32 v147, v147, v220, v163\n"
    "v_mfma_f32_16x16x32_bf16 v[180:183], v[72:75], a[72:75], v[180:183]\n"
    "v_mul_f32_e32 v174, v174, v230\n"
    "v_mfma_f32_16x16x32_bf16 v[184:187], v[92:95], a[72:75], v[184:187]\n"
    "v_cvt_pk_bf16_f32 v140, v140, v141\n"
    "v_cvt_pk_bf16_f32 v141, v142, v143\n"
    "v_mfma_f32_16x16x32_bf16 v[188:191], v[76:79], a[72:75], v[188:191]\n"
    "v_cvt_pk_bf16_f32 v142, v144, v145\n"
    "v_mfma_f32_16x16x32_bf16 v[192:195], v[96:99], a[72:75], v[192:195]\n"
    "v_cvt_pk_bf16_f32 v143, v146, v147\n"
    "v_mfma_f32_16x16x32_bf16 v[180:183], v[108:111], a[76:79], v[180:183]\n"
    "buffer_store_dwordx4 v[140:143], v239, s[16:19], s80 offen offset:0 sc0 sc1\n"
    "v_mfma_f32_16x16x32_bf16 v[184:187], v[112:115], a[76:79], v[184:187]\n"
    "v_mul_f32_e32 v175, v175, v230\n"
    "v_mfma_f32_16x16x32_bf16 v[188:191], v[132:135], a[76:79], v[188:191]\n"
    "v_fma_f32 v148, v148, v220, v164\n"
    "v_mfma_f32_16x16x32_bf16 v[192:195], v[116:119], a[76:79], v[192:195]\n"
    "s_waitcnt lgkmcnt(6)\n"
    "v_fma_f32 v149, v149, v220, v165\n"
    "v_fma_f32 v150, v150, v220, v166\n"
    "v_mfma_f32_16x16x32_bf16 v[180:183], v[136:139], a[80:83], v[180:183]\n"
    "v_fma_f32 v151, v151, v220, v167\n"
    "v_mfma_f32_16x16x32_bf16 v[184:187], a[0:3], a[80:83], v[184:187]\n"
    "v_fma_f32 v152, v152, v220, v168\n"
    "v_mfma_f32_16x16x32_bf16 v[188:191], a[4:7], a[80:83], v[188:191]\n"
    "v_fma_f32 v153, v153, v220, v169\n"
    "v_fma_f32 v154, v154, v220, v170\n"
    "v_mfma_f32_16x16x32_bf16 v[192:195], a[8:11], a[80:83], v[192:195]\n"
    "v_fma_f32 v155, v155, v220, v171\n"
    "v_cvt_pk_bf16_f32 v148, v148, v149\n"
    "v_mfma_f32_16x16x32_bf16 v[180:183], a[12:15], a[84:87], v[180:183]\n"
    "v_cvt_pk_bf16_f32 v149, v150, v151\n"
    "v_cvt_pk_bf16_f32 v150, v152, v153\n"
    "v_mfma_f32_16x16x32_bf16 v[184:187], a[16:19], a[84:87], v[184:187]\n"
    "v_cvt_pk_bf16_f32 v151, v154, v155\n"
    "v_mfma_f32_16x16x32_bf16 v[188:191], a[20:23], a[84:87], v[188:191]\n"
    "buffer_store_dwordx4 v[148:151], v239, s[16:19], s80 offen offset:64 sc0 sc1\n"
    "v_mfma_f32_16x16x32_bf16 v[192:195], a[24:27], a[84:87], v[192:195]\n"
    "s_waitcnt lgkmcnt(4)\n"
    "v_mfma_f32_16x16x32_bf16 v[180:183], a[28:31], a[88:91], v[180:183]\n"
    "v_mul_f32_e32 v176, v176, v230\n"
    "v_mul_f32_e32 v177, v177, v230\n"
    "v_mfma_f32_16x16x32_bf16 v[184:187], a[32:35], a[88:91], v[184:187]\n"
    "v_mul_f32_e32 v178, v178, v230\n"
    "v_mul_f32_e32 v179, v179, v230\n"
    "v_mfma_f32_16x16x32_bf16 v[188:191], a[36:39], a[88:91], v[188:191]\n"
    "v_mul_f32_e32 v212, v212, v230\n"
    "v_mfma_f32_16x16x32_bf16 v[192:195], a[40:43], a[88:91], v[192:195]\n"
    "s_waitcnt vmcnt(9)\n"
    "v_mul_f32_e32 v213, v213, v230\n"
    "v_mfma_f32_16x16x32_bf16 v[64:67], v[234:237], a[128:131], 0\n"
    "v_mfma_f32_16x16x32_bf16 v[84:87], v[232:235], a[128:131], 0\n"
    "v_mfma_f32_16x16x32_bf16 v[104:107], v[234:237], a[132:135], 0\n"
    "v_mul_f32_e32 v214, v214, v230\n"
    "v_mfma_f32_16x16x32_bf16 v[124:127], v[232:235], a[132:135], 0\n"
    "s_waitcnt vmcnt(7)\n"
    "v_mfma_f32_16x16x32_bf16 v[64:67], v[234:237], a[136:139], v[64:67]\n"
    "v_mfma_f32_16x16x32_bf16 v[84:87], v[232:235], a[136:139], v[84:87]\n"
    "v_mul_f32_e32 v215, v215, v230\n"
    "v_mfma_f32_16x16x32_bf16 v[104:107], v[234:237], a[140:143], v[104:107]\n"
    "v_mul_f32_e32 v216, v216, v230\n"
    "v_mfma_f32_16x16x32_bf16 v[124:127], v[232:235], a[140:143], v[124:127]\n"
    "s_waitcnt vmcnt(5)\n"
    "v_mul_f32_e32 v217, v217, v230\n"
    "v_mfma_f32_16x16x32_bf16 v[64:67], v[234:237], a[144:147], v[64:67]\n"
    "v_mul_f32_e32 v218, v218, v230\n"
    "v_mfma_f32_16x16x32_bf16 v[84:87], v[232:235], a[144:147], v[84:87]\n"
    "v_mul_f32_e32 v219, v219, v230\n"
    "v_mfma_f32_16x16x32_bf16 v[104:107], v[234:237], a[148:151], v[104:107]\n"
    "v_mfma_f32_16x16x32_bf16 v[124:127], v[232:235], a[148:151], v[124:127]\n"
    "s_waitcnt vmcnt(3)\n"
    "v_mfma_f32_16x16x32_bf16 v[64:67], v[234:237], a[152:155], v[64:67]\n"
    "s_waitcnt vmcnt(2)\n"
    "ds_write_b32 v225, v227 offset:512\n"
    "v_mfma_f32_16x16x32_bf16 v[84:87], v[232:235], a[152:155], v[84:87]\n"
    "v_mfma_f32_16x16x32_bf16 v[104:107], v[234:237], a[156:159], v[104:107]\n"
    "v_mfma_f32_16x16x32_bf16 v[124:127], v[232:235], a[156:159], v[124:127]\n"
    "v_cvt_pk_bf16_f32 v64, v64, v65\n"
    "v_cvt_pk_bf16_f32 v65, v66, v67\n"
    "v_mfma_f32_16x16x32_bf16 v[180:183], a[44:47], a[92:95], v[180:183]\n"
    "v_cvt_pk_bf16_f32 v66, v84, v85\n"
    "v_cvt_pk_bf16_f32 v67, v86, v87\n"
    "v_mfma_f32_16x16x32_bf16 v[184:187], a[240:243], a[92:95], v[184:187]\n"
    "ds_write_b128 v246, v[64:67] offset:16384\n"
    "v_mfma_f32_16x16x32_bf16 v[188:191], v[4:7], a[92:95], v[188:191]\n"
    "v_cvt_pk_bf16_f32 v104, v104, v105\n"
    "v_cvt_pk_bf16_f32 v105, v106, v107\n"
    "v_mfma_f32_16x16x32_bf16 v[192:195], v[24:27], a[92:95], v[192:195]\n"
    "s_waitcnt lgkmcnt(4)\n"
    "v_cvt_pk_bf16_f32 v106, v124, v125\n"
    "v_cvt_pk_bf16_f32 v107, v126, v127\n"
    "v_mfma_f32_16x16x32_bf16 v[196:199], v[234:237], a[56:59], v[196:199]\n"
    "ds_write_b128 v246, v[104:107] offset:17408\n"
    "v_mfma_f32_16x16x32_bf16 v[200:203], v[232:235], a[56:59], v[200:203]\n"
    "v_mfma_f32_16x16x32_bf16 v[204:207], v[234:237], a[60:63], v[204:207]\n"
    "s_waitcnt lgkmcnt(0)\n"
    "v_mfma_f32_16x16x32_bf16 v[208:211], v[232:235], a[60:63], v[208:211]\n"
    "s_barrier\n"
    "v_mfma_f32_16x16x32_bf16 v[172:175], v[234:237], a[160:163], v[172:175]\n"
    "ds_read_b128 a[96:99], v250 offset:16384\n"
    "v_mfma_f32_16x16x32_bf16 v[176:179], v[232:235], a[160:163], v[176:179]\n"
    "ds_read_b128 a[100:103], v250 offset:17408\n"
    "v_mfma_f32_16x16x32_bf16 v[212:215], v[234:237], a[164:167], v[212:215]\n"
    "ds_read_b128 a[104:107], v250 offset:18432\n"
    "ds_read_b128 a[108:111], v250 offset:19456\n"
    "v_fma_f32 v180, v180, v80, v196\n"
    "v_mfma_f32_16x16x32_bf16 v[216:219], v[232:235], a[164:167], v[216:219]\n"
    "s_waitcnt lgkmcnt(2)\n"
    "ds_read_b128 a[112:115], v250 offset:20480\n"
    "ds_read_b128 a[116:119], v250 offset:21504\n"
    "v_mfma_f32_16x16x32_bf16 v[0:3], v[28:31], a[96:99], 0\n"
    "ds_read_b128 a[120:123], v250 offset:22528\n"
    "ds_read_b128 a[124:127], v250 offset:23552\n"
    "v_mfma_f32_16x16x32_bf16 v[20:23], v[48:51], a[96:99], 0\n"
    "ds_read2_b32 v[220:221], v245 offset0:128 offset1:144\n"
    "ds_read2_b32 v[222:223], v245 offset0:160 offset1:176\n"
    "v_mfma_f32_16x16x32_bf16 v[0:3], v[36:39], a[100:103], v[0:3]\n"
    "v_fma_f32 v181, v181, v80, v197\n"
    "v_fma_f32 v182, v182, v80, v198\n"
    "v_mfma_f32_16x16x32_bf16 v[20:23], v[56:59], a[100:103], v[20:23]\n"
    "s_waitcnt lgkmcnt(6)\n"
    "v_fma_f32 v183, v183, v80, v199\n"
    "v_fma_f32 v184, v184, v80, v200\n"
    "v_mfma_f32_16x16x32_bf16 v[0:3], v[72:75], a[104:107], v[0:3]\n"
    "s_or_b32 s45, s34, s93\n"
    "s_cselect_b32 s45, 2.0, 6101760\n"
    "v_fma_f32 v185, v185, v80, v201\n"
    "v_mfma_f32_16x16x32_bf16 v[20:23], v[92:95], a[104:107], v[20:23]\n"
    "v_fma_f32 v186, v186, v80, v202\n"
    "v_fma_f32 v187, v187, v80, v203\n"
    "v_mfma_f32_16x16x32_bf16 v[0:3], v[108:111], a[108:111], v[0:3]\n"
    "v_cvt_pk_bf16_f32 v180, v180, v181\n"
    "v_cvt_pk_bf16_f32 v181, v182, v183\n"
    "v_mfma_f32_16x16x32_bf16 v[20:23], v[112:115], a[108:111], v[20:23]\n"
    "s_waitcnt lgkmcnt(4)\n"
    "v_cvt_pk_bf16_f32 v182, v184, v185\n"
    "v_cvt_pk_bf16_f32 v183, v186, v187\n"
    "v_mfma_f32_16x16x32_bf16 v[0:3], v[136:139], a[112:115], v[0:3]\n"
    "buffer_store_dwordx4 v[180:183], v239, s[16:19], s82 offen offset:0 sc0 sc1\n"
    "v_mfma_f32_16x16x32_bf16 v[20:23], a[0:3], a[112:115], v[20:23]\n"
    "v_fma_f32 v188, v188, v80, v204\n"
    "v_fma_f32 v189, v189, v80, v205\n"
    "v_mfma_f32_16x16x32_bf16 v[0:3], a[12:15], a[116:119], v[0:3]\n"
    "v_fma_f32 v190, v190, v80, v206\n"
    "v_fma_f32 v191, v191, v80, v207\n"
    "v_mfma_f32_16x16x32_bf16 v[20:23], a[16:19], a[116:119], v[20:23]\n"
    "s_waitcnt lgkmcnt(0)\n"
    "v_fma_f32 v192, v192, v80, v208\n"
    "v_fma_f32 v193, v193, v80, v209\n"
    "v_fma_f32 v194, v194, v80, v210\n"
    "v_mfma_f32_16x16x32_bf16 v[0:3], a[28:31], a[120:123], v[0:3]\n"
    "v_fma_f32 v195, v195, v80, v211\n"
    "v_cvt_pk_bf16_f32 v188, v188, v189\n"
    "v_mfma_f32_16x16x32_bf16 v[20:23], a[32:35], a[120:123], v[20:23]\n"
    "v_cvt_pk_bf16_f32 v189, v190, v191\n"
    "v_cvt_pk_bf16_f32 v190, v192, v193\n"
    "v_cvt_pk_bf16_f32 v191, v194, v195\n"
    "v_mfma_f32_16x16x32_bf16 v[0:3], a[44:47], a[124:127], v[0:3]\n"
    "buffer_store_dwordx4 v[188:191], v239, s[16:19], s82 offen offset:64 sc0 sc1\n"
    "v_mfma_f32_16x16x32_bf16 v[20:23], a[240:243], a[124:127], v[20:23]\n"
    "v_add_f32_e32 v220, v220, v221\n"
    "v_add_f32_e32 v222, v222, v223\n"
    "v_mfma_f32_16x16x32_bf16 v[40:43], v[32:35], a[96:99], 0\n"
    "v_add_f32_e32 v220, v220, v222\n"
    "v_mfma_f32_16x16x32_bf16 v[60:63], v[52:55], a[96:99], 0\n"
    "v_rcp_f32_e32 v220, v220\n"
    "v_mfma_f32_16x16x32_bf16 v[40:43], v[68:71], a[100:103], v[40:43]\n"
    "v_fma_f32 v0, v0, v220, v172\n"
    "v_mfma_f32_16x16x32_bf16 v[60:63], v[88:91], a[100:103], v[60:63]\n"
    "v_fma_f32 v1, v1, v220, v173\n"
    "v_fma_f32 v2, v2, v220, v174\n"
    "v_mfma_f32_16x16x32_bf16 v[40:43], v[76:79], a[104:107], v[40:43]\n"
    "v_fma_f32 v3, v3, v220, v175\n"
    "v_mfma_f32_16x16x32_bf16 v[60:63], v[96:99], a[104:107], v[60:63]\n"
    "v_fma_f32 v20, v20, v220, v176\n"
    "v_fma_f32 v21, v21, v220, v177\n"
    "v_mfma_f32_16x16x32_bf16 v[40:43], v[132:135], a[108:111], v[40:43]\n"
    "v_fma_f32 v22, v22, v220, v178\n"
    "v_fma_f32 v23, v23, v220, v179\n"
    "v_mfma_f32_16x16x32_bf16 v[60:63], v[116:119], a[108:111], v[60:63]\n"
    "v_cvt_pk_bf16_f32 v0, v0, v1\n"
    "v_cvt_pk_bf16_f32 v1, v2, v3\n"
    "v_mfma_f32_16x16x32_bf16 v[40:43], a[4:7], a[112:115], v[40:43]\n"
    "v_cvt_pk_bf16_f32 v2, v20, v21\n"
    "v_cvt_pk_bf16_f32 v3, v22, v23\n"
    "v_mfma_f32_16x16x32_bf16 v[60:63], a[8:11], a[112:115], v[60:63]\n"
    "buffer_store_dwordx4 v[0:3], v239, s[16:19], s28 offen offset:0 sc0 sc1\n"
    "v_mfma_f32_16x16x32_bf16 v[40:43], a[20:23], a[116:119], v[40:43]\n"
    "v_mfma_f32_16x16x32_bf16 v[60:63], a[24:27], a[116:119], v[60:63]\n"
    "v_mfma_f32_16x16x32_bf16 v[40:43], a[36:39], a[120:123], v[40:43]\n"
    "v_mfma_f32_16x16x32_bf16 v[60:63], a[40:43], a[120:123], v[60:63]\n"
    "v_mfma_f32_16x16x32_bf16 v[40:43], v[4:7], a[124:127], v[40:43]\n"
    "v_mfma_f32_16x16x32_bf16 v[60:63], v[24:27], a[124:127], v[60:63]\n"
    "v_mov_b32_e32 v254, s39\n"
    "s_nop 2\n"
    "v_fma_f32 v40, v40, v220, v212\n"
    "v_fma_f32 v41, v41, v220, v213\n"
    "v_fma_f32 v42, v42, v220, v214\n"
    "v_fma_f32 v43, v43, v220, v215\n"
    "v_fma_f32 v60, v60, v220, v216\n"
    "v_fma_f32 v61, v61, v220, v217\n"
    "v_fma_f32 v62, v62, v220, v218\n"
    "v_fma_f32 v63, v63, v220, v219\n"
    "v_cvt_pk_bf16_f32 v40, v40, v41\n"
    "v_cvt_pk_bf16_f32 v41, v42, v43\n"
    "v_cvt_pk_bf16_f32 v42, v60, v61\n"
    "v_cvt_pk_bf16_f32 v43, v62, v63\n"
    "buffer_store_dwordx4 v[40:43], v239, s[16:19], s28 offen offset:64 sc0 sc1\n"
    "buffer_store_dword v254, off, s[8:11], s45 offset:0 sc1\n"
    "s_endpgm\n"
    ".Lat_g1:\n"
    "s_cmp_eq_u32 s85, 0\n"
    "s_cbranch_scc1 .Lat_loop\n"
    "s_cmp_eq_u32 s81, s39\n"
    "s_cbranch_scc1 .Lat_g1b\n"
    "s_mov_b32 s53, 0x400000\n"
    ".Lat_lp:\n"
    "s_sleep 1\n"
    "buffer_load_dword v253, off, s[8:11], s82 sc1\n"
    "s_waitcnt vmcnt(0)\n"
    "v_readfirstlane_b32 s81, v253\n"
    "s_sub_u32 s53, s53, 1\n"
    "s_cbranch_scc1 .Lat_lpx\n"
    "s_nop 3\n"
    "s_cmp_lg_u32 s81, s39\n"
    "s_cbranch_scc1 .Lat_lp\n"
    ".Lat_lpx:\n"
    "s_mov_b32 m0, s30\n"
    "s_nop 0\n"
    "buffer_load_dwordx4 v250, s[8:11], s83 offen offset:0 sc1 lds\n"
    "s_mov_b32 m0, s42\n"
    ".Lat_g1b:\n"
    "v_lshrrev_b32_e32 v228, 2, v250\n"
    "v_add_u32_e32 v254, s30, v228\n"
    "s_waitcnt vmcnt(0)\n"
    "ds_read_b32 v254, v254 offset:960\n"
    "v_lshrrev_b32_e32 v228, 9, v250\n"
    "v_lshl_add_u32 v251, v228, 2, s30\n"
    "v_and_b32_e32 v252, 0xf0, v250\n"
    "v_lshl_add_u32 v252, v228, 8, v252\n"
    "v_bfe_u32 v229, v250, 8, 1\n"
    "v_lshl_add_u32 v252, v229, 3, v252\n"
    "v_and_b32_e32 v239, 0x1f0, v250\n"
    "v_bfe_u32 v228, v250, 2, 4\n"
    "s_mov_b32 s88, 0xcedc\n"
    "v_bfe_u32 v238, s88, v228, 4\n"
    "v_mul_u32_u24_e32 v238, 0x1111, v238\n"
    "v_mov_b64_e32 v[224:225], 0\n"
    "v_mov_b64_e32 v[226:227], 0\n"
    "s_mov_b32 s85, 0\n"
    "s_mov_b32 s82, 2.0\n"
    "s_mov_b32 s83, 2.0\n"
    "s_mov_b32 s89, 4\n"
    "s_waitcnt lgkmcnt(0)\n"
    "v_readlane_b32 s88, v254, 0\n"
    "v_readlane_b32 s87, v254, 1\n"
    "v_readlane_b32 s84, v254, 2\n"
    "v_readlane_b32 s86, v254, 4\n"
    "s_nop 3\n"
    "s_min_u32 s88, s88, s44\n"
    "s_sub_u32 s80, s44, s88\n"
    "s_cmp_eq_u32 s88, 0\n"
    "s_cbranch_scc1 .Lat_g2\n"
    "s_branch .Lat_gloop\n"
    ".p2align 6\n"
    ".Lat_gloop:\n"
    "v_mfma_f32_16x16x32_bf16 v[212:215], a[160:163], a[128:131], v[220:223]\n"
    "ds_write_addtid_b32 v143 offset:768\n"
    "v_mfma_f32_16x16x32_bf16 v[212:215], a[164:167], a[132:135], v[212:215]\n"
    "v_exp_f32_e32 v170, v170\n"
    "v_mfma_f32_16x16x32_bf16 v[204:207], a[164:167], a[100:103], v[220:223]\n"
    "ds_write_addtid_b32 v150 offset:1536\n"
    "v_mfma_f32_16x16x32_bf16 v[204:207], a[160:163], a[96:99], v[204:207]\n"
    "ds_read_b128 a[184:187], v242 offset:28672\n"
    "v_mfma_f32_16x16x32_bf16 v[196:199], a[160:163], a[64:67], v[220:223]\n"
    "v_exp_f32_e32 v171, v171\n"
    "v_mfma_f32_16x16x32_bf16 v[196:199], a[164:167], a[68:71], v[196:199]\n"
    "ds_write_addtid_b32 v151 offset:1792\n"
    "v_cvt_pk_bf16_f32 v166, v168, v169\n"
    "v_mfma_f32_16x16x32_bf16 v[188:191], a[164:167], a[36:39], v[220:223]\n"
    "v_exp_f32_e32 v176, v176\n"
    "v_mfma_f32_16x16x32_bf16 v[188:191], a[160:163], a[32:35], v[188:191]\n"
    "v_exp_f32_e32 v177, v177\n"
    "v_mfma_f32_16x16x32_bf16 v[180:183], a[160:163], a[0:3], v[220:223]\n"
    "ds_write_addtid_b32 v158 offset:2560\n"
    "v_cvt_pk_bf16_f32 v167, v170, v171\n"
    "v_mfma_f32_16x16x32_bf16 v[180:183], a[164:167], a[4:7], v[180:183]\n"
    "s_waitcnt vmcnt(20)\n"
    "v_exp_f32_e32 v178, v178\n"
    "v_smfmac_f32_16x16x64_bf16 v[224:227], a[232:235], a[192:199], v238\n"
    "s_waitcnt lgkmcnt(10)\n"
    "ds_read_b128 a[188:191], v243 offset:28672\n"
    "v_mfma_f32_16x16x32_bf16 v[180:183], a[168:171], a[8:11], v[180:183]\n"
    "s_bitcmp1_b32 s86, 0\n"
    "ds_read_b128 a[160:163], v240 offset:26624\n"
    "v_mfma_f32_16x16x32_bf16 v[180:183], a[172:175], a[12:15], v[180:183]\n"
    "s_cselect_b64 exec, -1, 0\n"
    "v_cvt_pk_bf16_f32 v236, v224, v225\n"
    "v_cvt_pk_bf16_f32 v237, v226, v227\n"
    "v_mfma_f32_16x16x32_bf16 v[188:191], a[172:175], a[44:47], v[188:191]\n"
    "s_cselect_b32 s88, 512, 0\n"
    "v_mov_b64_e32 v[224:225], 0\n"
    "v_mov_b64_e32 v[226:227], 0\n"
    "v_mfma_f32_16x16x32_bf16 v[188:191], a[168:171], a[40:43], v[188:191]\n"
    "buffer_store_dwordx2 v[236:237], v252, s[8:11], s87 offen sc1\n"
    "v_mfma_f32_16x16x32_bf16 v[196:199], a[168:171], a[72:75], v[196:199]\n"
    "s_mov_b64 exec, -1\n"
    "ds_read2_b32 v[228:229], v251 offset0:0 offset1:2\n"
    "v_mfma_f32_16x16x32_bf16 v[196:199], a[172:175], a[76:79], v[196:199]\n"
    "s_add_u32 s87, s87, s88\n"
    "ds_read2_b32 v[230:231], v251 offset0:4 offset1:6\n"
    "v_mfma_f32_16x16x32_bf16 v[204:207], a[172:175], a[108:111], v[204:207]\n"
    "ds_read_b32 v232, v251 offset:32\n"
    "v_mfma_f32_16x16x32_bf16 v[204:207], a[168:171], a[104:107], v[204:207]\n"
    "v_exp_f32_e32 v179, v179\n"
    "v_mfma_f32_16x16x32_bf16 v[212:215], a[168:171], a[136:139], v[212:215]\n"
    "ds_write_addtid_b32 v159 offset:2816\n"
    "v_cvt_pk_bf16_f32 v174, v176, v177\n"
    "v_mfma_f32_16x16x32_bf16 v[212:215], a[172:175], a[140:143], v[212:215]\n"
    "s_waitcnt lgkmcnt(13)\n"
    "ds_read_b128 a[164:167], v241 offset:26624\n"
    "v_mfma_f32_16x16x32_bf16 v[212:215], a[176:179], a[144:147], v[212:215]\n"
    "ds_write_addtid_b32 v166 offset:3584\n"
    "v_cvt_pk_bf16_f32 v175, v178, v179\n"
    "v_mfma_f32_16x16x32_bf16 v[212:215], a[180:183], a[148:151], v[212:215]\n"
    "ds_read_b128 a[168:171], v240 offset:30720\n"
    "v_mfma_f32_16x16x32_bf16 v[204:207], a[180:183], a[116:119], v[204:207]\n"
    "s_waitcnt lgkmcnt(6)\n"
    "ds_write_addtid_b32 v167 offset:3840\n"
    "v_mad_u32_u16 v234, v228, s90, v239 op_sel:[0,0,0,0]\n"
    "v_mfma_f32_16x16x32_bf16 v[204:207], a[176:179], a[112:115], v[204:207]\n"
    "buffer_load_dwordx4 a[192:195], v234, s[20:23], 0 offen sc1\n"
    "v_mad_u32_u16 v235, v228, s90, v239 op_sel:[1,0,0,0]\n"
    "v_mfma_f32_16x16x32_bf16 v[196:199], a[176:179], a[80:83], v[196:199]\n"
    "ds_write_addtid_b32 v174 offset:4608\n"
    "v_mfma_f32_16x16x32_bf16 v[196:199], a[180:183], a[84:87], v[196:199]\n"
    "ds_write_addtid_b32 v175 offset:4864\n"
    "v_mfma_f32_16x16x32_bf16 v[188:191], a[180:183], a[52:55], v[188:191]\n"
    "buffer_load_dwordx4 a[196:199], v235, s[20:23], 0 offen sc1\n"
    "v_mfma_f32_16x16x32_bf16 v[188:191], a[176:179], a[48:51], v[188:191]\n"
    "v_mfma_f32_16x16x32_bf16 v[180:183], a[176:179], a[16:19], v[180:183]\n"
    "s_waitcnt lgkmcnt(0)\n"
    "v_mfma_f32_16x16x32_bf16 v[180:183], a[180:183], a[20:23], v[180:183]\n"
    "s_barrier\n"
    "v_mfma_f32_16x16x32_bf16 v[180:183], a[184:187], a[24:27], v[180:183]\n"
    "s_add_u32 m0, s43, 8192\n"
    "ds_read_b128 a[172:175], v241 offset:30720\n"
    "v_mfma_f32_16x16x32_bf16 v[180:183], a[188:191], a[28:31], v[180:183]\n"
    "ds_read2st64_b32 v[144:145], v249 offset0:0 offset1:1\n"
    "v_mfma_f32_16x16x32_bf16 v[188:191], a[188:191], a[60:63], v[188:191]\n"
    "buffer_load_dwordx4 v248, s[8:11], s40 offen offset:0 sc1 lds\n"
    "v_mfma_f32_16x16x32_bf16 v[188:191], a[184:187], a[56:59], v[188:191]\n"
    "ds_read_b128 a[176:179], v242 offset:26624\n"
    "v_mfma_f32_16x16x32_bf16 v[196:199], a[184:187], a[88:91], v[196:199]\n"
    "v_mfma_f32_16x16x32_bf16 v[196:199], a[188:191], a[92:95], v[196:199]\n"
    "v_mfma_f32_16x16x32_bf16 v[204:207], a[188:191], a[124:127], v[204:207]\n"
    "buffer_load_dwordx4 v248, s[8:11], s40 offen offset:1024 sc1 lds\n"
    "v_mfma_f32_16x16x32_bf16 v[204:207], a[184:187], a[120:123], v[204:207]\n"
    "ds_read2st64_b32 v[146:147], v249 offset0:2 offset1:3\n"
    "v_mfma_f32_16x16x32_bf16 v[212:215], a[184:187], a[152:155], v[212:215]\n"
    "v_mfma_f32_16x16x32_bf16 v[212:215], a[188:191], a[156:159], v[212:215]\n"
    "ds_read_b128 a[180:183], v243 offset:26624\n"
    "v_mfma_f32_16x16x32_bf16 v[216:219], a[160:163], a[128:131], v[220:223]\n"
    "buffer_load_dwordx4 v248, s[8:11], s40 offen offset:2048 sc1 lds\n"
    "v_mfma_f32_16x16x32_bf16 v[216:219], a[164:167], a[132:135], v[216:219]\n"
    "ds_read2st64_b32 v[152:153], v249 offset0:4 offset1:5\n"
    "v_mfma_f32_16x16x32_bf16 v[208:211], a[164:167], a[100:103], v[220:223]\n"
    "v_exp_f32_e32 v180, v180\n"
    "v_mfma_f32_16x16x32_bf16 v[208:211], a[160:163], a[96:99], v[208:211]\n"
    "ds_read_b128 a[184:187], v242 offset:30720\n"
    "v_mfma_f32_16x16x32_bf16 v[200:203], a[160:163], a[64:67], v[220:223]\n"
    "buffer_load_dwordx4 v248, s[8:11], s40 offen offset:3072 sc1 lds\n"
    "v_mfma_f32_16x16x32_bf16 v[200:203], a[164:167], a[68:71], v[200:203]\n"
    "s_add_u32 m0, s43, 12288\n"
    "ds_read2st64_b32 v[154:155], v249 offset0:6 offset1:7\n"
    "v_mfma_f32_16x16x32_bf16 v[192:195], a[164:167], a[36:39], v[220:223]\n"
    "s_add_u32 s40, s40, 0x8000\n"
    "ds_read2st64_b32 v[160:161], v249 offset0:8 offset1:9\n"
    "v_mfma_f32_16x16x32_bf16 v[192:195], a[160:163], a[32:35], v[192:195]\n"
    "ds_read2st64_b32 v[162:163], v249 offset0:10 offset1:11\n"
    "v_mfma_f32_16x16x32_bf16 v[184:187], a[160:163], a[0:3], v[220:223]\n"
    "buffer_load_dwordx4 v248, s[8:11], s41 offen offset:0 sc1 lds\n"
    "v_mfma_f32_16x16x32_bf16 v[184:187], a[164:167], a[4:7], v[184:187]\n"
    "s_waitcnt vmcnt(17)\n"
    "ds_read_b128 a[188:191], v243 offset:30720\n"
    "v_smfmac_f32_16x16x64_bf16 v[224:227], a[232:235], a[200:207], v238\n"
    "s_waitcnt lgkmcnt(10)\n"
    "v_mad_u32_u16 v234, v229, s90, v239 op_sel:[0,0,0,0]\n"
    "v_mfma_f32_16x16x32_bf16 v[184:187], a[168:171], a[8:11], v[184:187]\n"
    "s_bitcmp1_b32 s86, 1\n"
    "buffer_load_dwordx4 a[200:203], v234, s[20:23], 0 offen sc1\n"
    "v_mad_u32_u16 v235, v229, s90, v239 op_sel:[1,0,0,0]\n"
    "v_mfma_f32_16x16x32_bf16 v[184:187], a[172:175], a[12:15], v[184:187]\n"
    "s_cselect_b64 exec, -1, 0\n"
    "v_cvt_pk_bf16_f32 v236, v224, v225\n"
    "v_cvt_pk_bf16_f32 v237, v226, v227\n"
    "v_mfma_f32_16x16x32_bf16 v[192:195], a[172:175], a[44:47], v[192:195]\n"
    "s_cselect_b32 s88, 512, 0\n"
    "v_mov_b64_e32 v[224:225], 0\n"
    "v_mov_b64_e32 v[226:227], 0\n"
    "v_mfma_f32_16x16x32_bf16 v[192:195], a[168:171], a[40:43], v[192:195]\n"
    "buffer_store_dwordx2 v[236:237], v252, s[8:11], s87 offen sc1\n"
    "v_mfma_f32_16x16x32_bf16 v[200:203], a[168:171], a[72:75], v[200:203]\n"
    "s_mov_b64 exec, -1\n"
    "ds_read_b64_tr_b16 a[160:161], v244 offset:16384\n"
    "v_mfma_f32_16x16x32_bf16 v[200:203], a[172:175], a[76:79], v[200:203]\n"
    "s_add_u32 s87, s87, s88\n"
    "ds_read_b64_tr_b16 a[162:163], v244 offset:18432\n"
    "v_mfma_f32_16x16x32_bf16 v[208:211], a[172:175], a[108:111], v[208:211]\n"
    "buffer_load_dwordx4 v248, s[8:11], s41 offen offset:1024 sc1 lds\n"
    "v_mfma_f32_16x16x32_bf16 v[208:211], a[168:171], a[104:107], v[208:211]\n"
    "ds_read_b64_tr_b16 a[164:165], v244 offset:49152\n"
    "v_mfma_f32_16x16x32_bf16 v[216:219], a[168:171], a[136:139], v[216:219]\n"
    "ds_read2st64_b32 v[168:169], v249 offset0:12 offset1:13\n"
    "v_mfma_f32_16x16x32_bf16 v[216:219], a[172:175], a[140:143], v[216:219]\n"
    "s_waitcnt lgkmcnt(10)\n"
    "ds_read2st64_b32 v[170:171], v249 offset0:14 offset1:15\n"
    "v_mfma_f32_16x16x32_bf16 v[216:219], a[176:179], a[144:147], v[216:219]\n"
    "buffer_load_dwordx4 v248, s[8:11], s41 offen offset:2048 sc1 lds\n"
    "v_mfma_f32_16x16x32_bf16 v[216:219], a[180:183], a[148:151], v[216:219]\n"
    "ds_read_b64_tr_b16 a[166:167], v244 offset:51200\n"
    "v_mfma_f32_16x16x32_bf16 v[208:211], a[180:183], a[116:119], v[208:211]\n"
    "ds_read_b64_tr_b16 a[168:169], v245 offset:16384\n"
    "v_mfma_f32_16x16x32_bf16 v[208:211], a[176:179], a[112:115], v[208:211]\n"
    "v_exp_f32_e32 v181, v181\n"
    "v_mfma_f32_16x16x32_bf16 v[200:203], a[176:179], a[80:83], v[200:203]\n"
    "buffer_load_dwordx4 v248, s[8:11], s41 offen offset:3072 sc1 lds\n"
    "v_mfma_f32_16x16x32_bf16 v[200:203], a[180:183], a[84:87], v[200:203]\n"
    "ds_read_b64_tr_b16 a[170:171], v245 offset:18432\n"
    "v_mfma_f32_16x16x32_bf16 v[192:195], a[180:183], a[52:55], v[192:195]\n"
    "s_add_u32 s41, s41, 0x8000\n"
    "ds_read2st64_b32 v[176:177], v249 offset0:16 offset1:17\n"
    "v_mfma_f32_16x16x32_bf16 v[192:195], a[176:179], a[48:51], v[192:195]\n"
    "buffer_load_dwordx4 a[204:207], v235, s[20:23], 0 offen sc1\n"
    "v_mfma_f32_16x16x32_bf16 v[184:187], a[176:179], a[16:19], v[184:187]\n"
    "ds_read_b64_tr_b16 a[172:173], v245 offset:49152\n"
    "v_mfma_f32_16x16x32_bf16 v[184:187], a[180:183], a[20:23], v[184:187]\n"
    "s_waitcnt lgkmcnt(10)\n"
    "ds_read2st64_b32 v[178:179], v249 offset0:18 offset1:19\n"
    "v_mfma_f32_16x16x32_bf16 v[184:187], a[184:187], a[24:27], v[184:187]\n"
    "ds_read_b64_tr_b16 a[176:177], v246 offset:16384\n"
    "v_mfma_f32_16x16x32_bf16 v[184:187], a[188:191], a[28:31], v[184:187]\n"
    "ds_read_b64_tr_b16 a[174:175], v245 offset:51200\n"
    "v_mfma_f32_16x16x32_bf16 v[192:195], a[188:191], a[60:63], v[192:195]\n"
    "v_exp_f32_e32 v182, v182\n"
    "v_mfma_f32_16x16x32_bf16 v[192:195], a[184:187], a[56:59], v[192:195]\n"
    "v_exp_f32_e32 v183, v183\n"
    "v_mfma_f32_16x16x32_bf16 v[200:203], a[184:187], a[88:91], v[200:203]\n"
    "ds_read_b64_tr_b16 a[178:179], v246 offset:18432\n"
    "v_cvt_pk_bf16_f32 v180, v180, v181\n"
    "v_mfma_f32_16x16x32_bf16 v[200:203], a[188:191], a[92:95], v[200:203]\n"
    "v_exp_f32_e32 v188, v188\n"
    "v_mfma_f32_16x16x32_bf16 v[208:211], a[188:191], a[124:127], v[208:211]\n"
    "v_exp_f32_e32 v189, v189\n"
    "v_mfma_f32_16x16x32_bf16 v[208:211], a[184:187], a[120:123], v[208:211]\n"
    "v_cvt_pk_bf16_f32 v181, v182, v183\n"
    "v_mfma_f32_16x16x32_bf16 v[216:219], a[184:187], a[152:155], v[216:219]\n"
    "ds_read_b64_tr_b16 a[180:181], v246 offset:49152\n"
    "v_mfma_f32_16x16x32_bf16 v[216:219], a[188:191], a[156:159], v[216:219]\n"
    "s_waitcnt vmcnt(20)\n"
    "ds_read_b64_tr_b16 a[182:183], v246 offset:51200\n"
    "v_smfmac_f32_16x16x64_bf16 v[224:227], a[232:235], a[208:215], v238\n"
    "s_waitcnt lgkmcnt(5)\n"
    "v_mad_u32_u16 v234, v230, s90, v239 op_sel:[0,0,0,0]\n"
    "v_mfma_f32_16x16x32_bf16 a[236:239], a[160:163], v[140:143], a[236:239]\n"
    "s_bitcmp1_b32 s86, 2\n"
    "buffer_load_dwordx4 a[208:211], v234, s[20:23], 0 offen sc1\n"
    "v_mad_u32_u16 v235, v230, s90, v239 op_sel:[1,0,0,0]\n"
    "v_mfma_f32_16x16x32_bf16 a[236:239], a[164:167], v[144:147], a[236:239]\n"
    "s_cselect_b64 exec, -1, 0\n"
    "v_cvt_pk_bf16_f32 v236, v224, v225\n"
    "v_cvt_pk_bf16_f32 v237, v226, v227\n"
    "v_mfma_f32_16x16x32_bf16 a[240:243], a[164:167], v[152:155], a[240:243]\n"
    "s_cselect_b32 s88, 512, 0\n"
    "v_mov_b64_e32 v[224:225], 0\n"
    "v_mov_b64_e32 v[226:227], 0\n"
    "v_mfma_f32_16x16x32_bf16 a[240:243], a[160:163], v[148:151], a[240:243]\n"
    "buffer_store_dwordx2 v[236:237], v252, s[8:11], s87 offen sc1\n"
    "v_mfma_f32_16x16x32_bf16 a[244:247], a[160:163], v[156:159], a[244:247]\n"
    "s_mov_b64 exec, -1\n"
    "ds_read_b64_tr_b16 a[184:185], v247 offset:16384\n"
    "v_mfma_f32_16x16x32_bf16 a[244:247], a[164:167], v[160:163], a[244:247]\n"
    "s_add_u32 s87, s87, s88\n"
    "ds_read_b64_tr_b16 a[186:187], v247 offset:18432\n"
    "v_mfma_f32_16x16x32_bf16 a[248:251], a[164:167], v[168:171], a[248:251]\n"
    "buffer_load_dwordx4 a[212:215], v235, s[20:23], 0 offen sc1\n"
    "v_mfma_f32_16x16x32_bf16 a[248:251], a[160:163], v[164:167], a[248:251]\n"
    "ds_read_b64_tr_b16 a[188:189], v247 offset:49152\n"
    "v_mfma_f32_16x16x32_bf16 a[252:255], a[160:163], v[172:175], a[252:255]\n"
    "v_exp_f32_e32 v190, v190\n"
    "v_mfma_f32_16x16x32_bf16 a[252:255], a[164:167], v[176:179], a[252:255]\n"
    "s_waitcnt lgkmcnt(6)\n"
    "v_exp_f32_e32 v191, v191\n"
    "v_mfma_f32_16x16x32_bf16 v[16:19], a[172:175], v[176:179], v[16:19]\n"
    "ds_read_b64_tr_b16 a[190:191], v247 offset:51200\n"
    "v_cvt_pk_bf16_f32 v188, v188, v189\n"
    "v_mfma_f32_16x16x32_bf16 v[16:19], a[168:171], v[172:175], v[16:19]\n"
    "ds_read_b64_tr_b16 a[160:161], v244 offset:20480\n"
    "v_mfma_f32_16x16x32_bf16 v[12:15], a[168:171], v[164:167], v[12:15]\n"
    "v_exp_f32_e32 v196, v196\n"
    "v_mfma_f32_16x16x32_bf16 v[12:15], a[172:175], v[168:171], v[12:15]\n"
    "v_exp_f32_e32 v197, v197\n"
    "v_mfma_f32_16x16x32_bf16 v[8:11], a[172:175], v[160:163], v[8:11]\n"
    "ds_read_b64_tr_b16 a[162:163], v244 offset:22528\n"
    "v_cvt_pk_bf16_f32 v189, v190, v191\n"
    "v_mfma_f32_16x16x32_bf16 v[8:11], a[168:171], v[156:159], v[8:11]\n"
    "v_exp_f32_e32 v198, v198\n"
    "v_mfma_f32_16x16x32_bf16 v[4:7], a[168:171], v[148:151], v[4:7]\n"
    "v_exp_f32_e32 v199, v199\n"
    "v_mfma_f32_16x16x32_bf16 v[4:7], a[172:175], v[152:155], v[4:7]\n"
    "v_cvt_pk_bf16_f32 v196, v196, v197\n"
    "v_mfma_f32_16x16x32_bf16 v[0:3], a[172:175], v[144:147], v[0:3]\n"
    "ds_read_b64_tr_b16 a[164:165], v244 offset:53248\n"
    "v_mfma_f32_16x16x32_bf16 v[0:3], a[168:171], v[140:143], v[0:3]\n"
    "s_waitcnt lgkmcnt(7)\n"
    "v_exp_f32_e32 v204, v204\n"
    "v_mfma_f32_16x16x32_bf16 v[20:23], a[176:179], v[140:143], v[20:23]\n"
    "ds_read_b64_tr_b16 a[168:169], v245 offset:20480\n"
    "v_mfma_f32_16x16x32_bf16 v[20:23], a[180:183], v[144:147], v[20:23]\n"
    "ds_read_b64_tr_b16 a[166:167], v244 offset:55296\n"
    "v_mfma_f32_16x16x32_bf16 v[24:27], a[180:183], v[152:155], v[24:27]\n"
    "v_exp_f32_e32 v205, v205\n"
    "v_mfma_f32_16x16x32_bf16 v[24:27], a[176:179], v[148:151], v[24:27]\n"
    "v_cvt_pk_bf16_f32 v197, v198, v199\n"
    "v_mfma_f32_16x16x32_bf16 v[28:31], a[176:179], v[156:159], v[28:31]\n"
    "ds_read_b64_tr_b16 a[170:171], v245 offset:22528\n"
    "v_mfma_f32_16x16x32_bf16 v[28:31], a[180:183], v[160:163], v[28:31]\n"
    "v_exp_f32_e32 v206, v206\n"
    "v_mfma_f32_16x16x32_bf16 v[32:35], a[180:183], v[168:171], v[32:35]\n"
    "v_exp_f32_e32 v207, v207\n"
    "v_mfma_f32_16x16x32_bf16 v[32:35], a[176:179], v[164:167], v[32:35]\n"
    "v_cvt_pk_bf16_f32 v204, v204, v205\n"
    "v_mfma_f32_16x16x32_bf16 v[36:39], a[176:179], v[172:175], v[36:39]\n"
    "ds_read_b64_tr_b16 a[172:173], v245 offset:53248\n"
    "v_mfma_f32_16x16x32_bf16 v[36:39], a[180:183], v[176:179], v[36:39]\n"
    "s_waitcnt vmcnt(20)\n"
    "ds_read_b64_tr_b16 a[174:175], v245 offset:55296\n"
    "v_smfmac_f32_16x16x64_bf16 v[224:227], a[232:235], a[216:223], v238\n"
    "s_waitcnt lgkmcnt(8)\n"
    "v_mad_u32_u16 v234, v231, s90, v239 op_sel:[0,0,0,0]\n"
    "v_mfma_f32_16x16x32_bf16 v[56:59], a[188:191], v[176:179], v[56:59]\n"
    "s_bitcmp1_b32 s86, 3\n"
    "buffer_load_dwordx4 a[216:219], v234, s[20:23], 0 offen sc1\n"
    "v_mad_u32_u16 v235, v231, s90, v239 op_sel:[1,0,0,0]\n"
    "v_mfma_f32_16x16x32_bf16 v[56:59], a[184:187], v[172:175], v[56:59]\n"
    "s_cselect_b64 exec, -1, 0\n"
    "v_cvt_pk_bf16_f32 v236, v224, v225\n"
    "v_cvt_pk_bf16_f32 v237, v226, v227\n"
    "v_mfma_f32_16x16x32_bf16 v[52:55], a[184:187], v[164:167], v[52:55]\n"
    "s_cselect_b32 s88, 512, 0\n"
    "v_mov_b64_e32 v[224:225], 0\n"
    "v_mov_b64_e32 v[226:227], 0\n"
    "v_mfma_f32_16x16x32_bf16 v[52:55], a[188:191], v[168:171], v[52:55]\n"
    "buffer_store_dwordx2 v[236:237], v252, s[8:11], s87 offen sc1\n"
    "v_mfma_f32_16x16x32_bf16 v[48:51], a[188:191], v[160:163], v[48:51]\n"
    "s_mov_b64 exec, -1\n"
    "ds_read_b64_tr_b16 a[176:177], v246 offset:20480\n"
    "v_mfma_f32_16x16x32_bf16 v[48:51], a[184:187], v[156:159], v[48:51]\n"
    "s_add_u32 s87, s87, s88\n"
    "ds_read_b64_tr_b16 a[178:179], v246 offset:22528\n"
    "v_mfma_f32_16x16x32_bf16 v[44:47], a[184:187], v[148:151], v[44:47]\n"
    "buffer_load_dwordx4 a[220:223], v235, s[20:23], 0 offen sc1\n"
    "v_mfma_f32_16x16x32_bf16 v[44:47], a[188:191], v[152:155], v[44:47]\n"
    "ds_read_b64_tr_b16 a[180:181], v246 offset:53248\n"
    "v_mfma_f32_16x16x32_bf16 v[40:43], a[188:191], v[144:147], v[40:43]\n"
    "v_exp_f32_e32 v212, v212\n"
    "v_mfma_f32_16x16x32_bf16 v[40:43], a[184:187], v[140:143], v[40:43]\n"
    "s_waitcnt lgkmcnt(6)\n"
    "v_exp_f32_e32 v213, v213\n"
    "v_mfma_f32_16x16x32_bf16 v[60:63], a[160:163], v[140:143], v[60:63]\n"
    "ds_read_b64_tr_b16 a[182:183], v246 offset:55296\n"
    "v_cvt_pk_bf16_f32 v205, v206, v207\n"
    "v_mfma_f32_16x16x32_bf16 v[60:63], a[164:167], v[144:147], v[60:63]\n"
    "ds_read_b64_tr_b16 a[184:185], v247 offset:20480\n"
    "v_mfma_f32_16x16x32_bf16 v[64:67], a[164:167], v[152:155], v[64:67]\n"
    "ds_read_b64_tr_b16 a[186:187], v247 offset:22528\n"
    "v_mfma_f32_16x16x32_bf16 v[64:67], a[160:163], v[148:151], v[64:67]\n"
    "v_exp_f32_e32 v214, v214\n"
    "v_mfma_f32_16x16x32_bf16 v[68:71], a[160:163], v[156:159], v[68:71]\n"
    "v_exp_f32_e32 v215, v215\n"
    "v_mfma_f32_16x16x32_bf16 v[68:71], a[164:167], v[160:163], v[68:71]\n"
    "ds_read_b64_tr_b16 a[188:189], v247 offset:53248\n"
    "v_cvt_pk_bf16_f32 v212, v212, v213\n"
    "v_mfma_f32_16x16x32_bf16 v[72:75], a[164:167], v[168:171], v[72:75]\n"
    "ds_read_b64_tr_b16 a[190:191], v247 offset:55296\n"
    "v_cvt_pk_bf16_f32 v213, v214, v215\n"
    "v_mfma_f32_16x16x32_bf16 v[72:75], a[160:163], v[164:167], v[72:75]\n"
    "s_waitcnt vmcnt(30)\n"
    "v_mfma_f32_16x16x32_bf16 v[76:79], a[160:163], v[172:175], v[76:79]\n"
    "s_barrier\n"
    "v_mfma_f32_16x16x32_bf16 v[76:79], a[164:167], v[176:179], v[76:79]\n"
    "s_waitcnt lgkmcnt(8)\n"
    "s_mov_b32 m0, s42\n"
    "v_mfma_f32_16x16x32_bf16 v[96:99], a[172:175], v[176:179], v[96:99]\n"
    "v_exp_f32_e32 v184, v184\n"
    "v_mfma_f32_16x16x32_bf16 v[96:99], a[168:171], v[172:175], v[96:99]\n"
    "v_exp_f32_e32 v185, v185\n"
    "v_mfma_f32_16x16x32_bf16 v[92:95], a[168:171], v[164:167], v[92:95]\n"
    "v_exp_f32_e32 v186, v186\n"
    "v_mfma_f32_16x16x32_bf16 v[92:95], a[172:175], v[168:171], v[92:95]\n"
    "ds_read_b128 a[160:163], v240 offset:0\n"
    "ds_write_addtid_b32 v180 offset:0\n"
    "v_mfma_f32_16x16x32_bf16 v[88:91], a[172:175], v[160:163], v[88:91]\n"
    "v_exp_f32_e32 v187, v187\n"
    "v_mfma_f32_16x16x32_bf16 v[88:91], a[168:171], v[156:159], v[88:91]\n"
    "ds_write_addtid_b32 v181 offset:256\n"
    "v_cvt_pk_bf16_f32 v182, v184, v185\n"
    "v_mfma_f32_16x16x32_bf16 v[84:87], a[168:171], v[148:151], v[84:87]\n"
    "v_exp_f32_e32 v192, v192\n"
    "v_mfma_f32_16x16x32_bf16 v[84:87], a[172:175], v[152:155], v[84:87]\n"
    "v_exp_f32_e32 v193, v193\n"
    "v_mfma_f32_16x16x32_bf16 v[80:83], a[172:175], v[144:147], v[80:83]\n"
    "ds_write_addtid_b32 v188 offset:1024\n"
    "v_cvt_pk_bf16_f32 v183, v186, v187\n"
    "v_mfma_f32_16x16x32_bf16 v[80:83], a[168:171], v[140:143], v[80:83]\n"
    "s_waitcnt vmcnt(20)\n"
    "ds_read_b128 a[164:167], v241 offset:0\n"
    "ds_write_addtid_b32 v189 offset:1280\n"
    "v_smfmac_f32_16x16x64_bf16 v[224:227], a[232:235], a[224:231], v238\n"
    "s_waitcnt lgkmcnt(10)\n"
    "ds_write_addtid_b32 v196 offset:2048\n"
    "v_mad_u32_u16 v234, v232, s90, v239 op_sel:[0,0,0,0]\n"
    "v_mfma_f32_16x16x32_bf16 v[100:103], a[176:179], v[140:143], v[100:103]\n"
    "s_bitcmp1_b32 s86, 4\n"
    "buffer_load_dwordx4 a[224:227], v234, s[20:23], 0 offen sc1\n"
    "v_mad_u32_u16 v235, v232, s90, v239 op_sel:[1,0,0,0]\n"
    "v_mfma_f32_16x16x32_bf16 v[100:103], a[180:183], v[144:147], v[100:103]\n"
    "s_cselect_b64 exec, -1, 0\n"
    "v_cvt_pk_bf16_f32 v236, v224, v225\n"
    "v_cvt_pk_bf16_f32 v237, v226, v227\n"
    "v_mfma_f32_16x16x32_bf16 v[104:107], a[180:183], v[152:155], v[104:107]\n"
    "s_cselect_b32 s88, 512, 0\n"
    "v_mov_b64_e32 v[224:225], 0\n"
    "v_mov_b64_e32 v[226:227], 0\n"
    "v_mfma_f32_16x16x32_bf16 v[104:107], a[176:179], v[148:151], v[104:107]\n"
    "buffer_store_dwordx2 v[236:237], v252, s[8:11], s87 offen sc1\n"
    "v_mfma_f32_16x16x32_bf16 v[108:111], a[176:179], v[156:159], v[108:111]\n"
    "s_mov_b64 exec, -1\n"
    "ds_read_b128 a[168:171], v240 offset:4096\n"
    "ds_write_addtid_b32 v197 offset:2304\n"
    "v_mfma_f32_16x16x32_bf16 v[108:111], a[180:183], v[160:163], v[108:111]\n"
    "s_add_u32 s87, s87, s88\n"
    "v_exp_f32_e32 v194, v194\n"
    "v_mfma_f32_16x16x32_bf16 v[112:115], a[180:183], v[168:171], v[112:115]\n"
    "buffer_load_dwordx4 a[228:231], v235, s[20:23], 0 offen sc1\n"
    "ds_write_addtid_b32 v204 offset:3072\n"
    "v_mfma_f32_16x16x32_bf16 v[112:115], a[176:179], v[164:167], v[112:115]\n"
    "ds_write_addtid_b32 v205 offset:3328\n"
    "v_mfma_f32_16x16x32_bf16 v[116:119], a[176:179], v[172:175], v[116:119]\n"
    "v_exp_f32_e32 v195, v195\n"
    "v_mfma_f32_16x16x32_bf16 v[116:119], a[180:183], v[176:179], v[116:119]\n"
    "s_waitcnt lgkmcnt(11)\n"
    "ds_read_b128 a[172:175], v241 offset:4096\n"
    "v_cvt_pk_bf16_f32 v190, v192, v193\n"
    "v_mfma_f32_16x16x32_bf16 v[136:139], a[188:191], v[176:179], v[136:139]\n"
    "v_exp_f32_e32 v200, v200\n"
    "v_mfma_f32_16x16x32_bf16 v[136:139], a[184:187], v[172:175], v[136:139]\n"
    "v_exp_f32_e32 v201, v201\n"
    "v_mfma_f32_16x16x32_bf16 v[132:135], a[184:187], v[164:167], v[132:135]\n"
    "ds_read_b128 a[176:179], v242 offset:0\n"
    "v_cvt_pk_bf16_f32 v191, v194, v195\n"
    "v_mfma_f32_16x16x32_bf16 v[132:135], a[188:191], v[168:171], v[132:135]\n"
    "v_exp_f32_e32 v202, v202\n"
    "v_mfma_f32_16x16x32_bf16 v[128:131], a[188:191], v[160:163], v[128:131]\n"
    "v_exp_f32_e32 v203, v203\n"
    "v_mfma_f32_16x16x32_bf16 v[128:131], a[184:187], v[156:159], v[128:131]\n"
    "ds_write_addtid_b32 v212 offset:4096\n"
    "v_cvt_pk_bf16_f32 v198, v200, v201\n"
    "v_mfma_f32_16x16x32_bf16 v[124:127], a[184:187], v[148:151], v[124:127]\n"
    "v_exp_f32_e32 v208, v208\n"
    "v_mfma_f32_16x16x32_bf16 v[124:127], a[188:191], v[152:155], v[124:127]\n"
    "ds_read_b128 a[180:183], v243 offset:0\n"
    "ds_write_addtid_b32 v213 offset:4352\n"
    "v_mfma_f32_16x16x32_bf16 v[120:123], a[188:191], v[144:147], v[120:123]\n"
    "v_exp_f32_e32 v209, v209\n"
    "v_mfma_f32_16x16x32_bf16 v[120:123], a[184:187], v[140:143], v[120:123]\n"
    "s_waitcnt lgkmcnt(11)\n"
    "ds_write_addtid_b32 v182 offset:512\n"
    "v_cvt_pk_bf16_f32 v199, v202, v203\n"
    "v_mfma_f32_16x16x32_bf16 v[172:175], a[160:163], a[128:131], v[220:223]\n"
    "ds_write_addtid_b32 v183 offset:768\n"
    "v_mfma_f32_16x16x32_bf16 v[172:175], a[164:167], a[132:135], v[172:175]\n"
    "v_exp_f32_e32 v210, v210\n"
    "v_mfma_f32_16x16x32_bf16 v[164:167], a[164:167], a[100:103], v[220:223]\n"
    "ds_write_addtid_b32 v190 offset:1536\n"
    "v_mfma_f32_16x16x32_bf16 v[164:167], a[160:163], a[96:99], v[164:167]\n"
    "ds_read_b128 a[184:187], v242 offset:4096\n"
    "v_mfma_f32_16x16x32_bf16 v[156:159], a[160:163], a[64:67], v[220:223]\n"
    "v_exp_f32_e32 v211, v211\n"
    "v_mfma_f32_16x16x32_bf16 v[156:159], a[164:167], a[68:71], v[156:159]\n"
    "ds_write_addtid_b32 v191 offset:1792\n"
    "v_cvt_pk_bf16_f32 v206, v208, v209\n"
    "v_mfma_f32_16x16x32_bf16 v[148:151], a[164:167], a[36:39], v[220:223]\n"
    "v_exp_f32_e32 v216, v216\n"
    "v_mfma_f32_16x16x32_bf16 v[148:151], a[160:163], a[32:35], v[148:151]\n"
    "v_exp_f32_e32 v217, v217\n"
    "v_mfma_f32_16x16x32_bf16 v[140:143], a[160:163], a[0:3], v[220:223]\n"
    "ds_write_addtid_b32 v198 offset:2560\n"
    "v_cvt_pk_bf16_f32 v207, v210, v211\n"
    "v_mfma_f32_16x16x32_bf16 v[140:143], a[164:167], a[4:7], v[140:143]\n"
    "s_waitcnt vmcnt(20)\n"
    "v_exp_f32_e32 v218, v218\n"
    "v_smfmac_f32_16x16x64_bf16 v[224:227], a[232:235], a[192:199], v238\n"
    "s_waitcnt lgkmcnt(10)\n"
    "ds_read_b128 a[188:191], v243 offset:4096\n"
    "v_mfma_f32_16x16x32_bf16 v[140:143], a[168:171], a[8:11], v[140:143]\n"
    "s_bitcmp1_b32 s86, 5\n"
    "ds_read_b128 a[160:163], v240 offset:2048\n"
    "v_mfma_f32_16x16x32_bf16 v[140:143], a[172:175], a[12:15], v[140:143]\n"
    "s_cselect_b64 exec, -1, 0\n"
    "v_cvt_pk_bf16_f32 v236, v224, v225\n"
    "v_cvt_pk_bf16_f32 v237, v226, v227\n"
    "v_mfma_f32_16x16x32_bf16 v[148:151], a[172:175], a[44:47], v[148:151]\n"
    "s_cselect_b32 s88, 512, 0\n"
    "v_mov_b64_e32 v[224:225], 0\n"
    "v_mov_b64_e32 v[226:227], 0\n"
    "v_mfma_f32_16x16x32_bf16 v[148:151], a[168:171], a[40:43], v[148:151]\n"
    "buffer_store_dwordx2 v[236:237], v252, s[8:11], s87 offen sc1\n"
    "v_mfma_f32_16x16x32_bf16 v[156:159], a[168:171], a[72:75], v[156:159]\n"
    "s_mov_b64 exec, -1\n"
    "ds_read2_b32 v[228:229], v251 offset0:10 offset1:12\n"
    "v_mfma_f32_16x16x32_bf16 v[156:159], a[172:175], a[76:79], v[156:159]\n"
    "s_add_u32 s87, s87, s88\n"
    "ds_read2_b32 v[230:231], v251 offset0:14 offset1:16\n"
    "v_mfma_f32_16x16x32_bf16 v[164:167], a[172:175], a[108:111], v[164:167]\n"
    "ds_read_b32 v232, v251 offset:72\n"
    "v_mfma_f32_16x16x32_bf16 v[164:167], a[168:171], a[104:107], v[164:167]\n"
    "v_exp_f32_e32 v219, v219\n"
    "v_mfma_f32_16x16x32_bf16 v[172:175], a[168:171], a[136:139], v[172:175]\n"
    "ds_write_addtid_b32 v199 offset:2816\n"
    "v_cvt_pk_bf16_f32 v214, v216, v217\n"
    "v_mfma_f32_16x16x32_bf16 v[172:175], a[172:175], a[140:143], v[172:175]\n"
    "s_waitcnt lgkmcnt(13)\n"
    "ds_read_b128 a[164:167], v241 offset:2048\n"
    "v_mfma_f32_16x16x32_bf16 v[172:175], a[176:179], a[144:147], v[172:175]\n"
    "ds_write_addtid_b32 v206 offset:3584\n"
    "v_cvt_pk_bf16_f32 v215, v218, v219\n"
    "v_mfma_f32_16x16x32_bf16 v[172:175], a[180:183], a[148:151], v[172:175]\n"
    "ds_read_b128 a[168:171], v240 offset:6144\n"
    "v_mfma_f32_16x16x32_bf16 v[164:167], a[180:183], a[116:119], v[164:167]\n"
    "s_waitcnt lgkmcnt(6)\n"
    "ds_write_addtid_b32 v207 offset:3840\n"
    "v_mad_u32_u16 v234, v228, s90, v239 op_sel:[0,0,0,0]\n"
    "v_mfma_f32_16x16x32_bf16 v[164:167], a[176:179], a[112:115], v[164:167]\n"
    "buffer_load_dwordx4 a[192:195], v234, s[20:23], 0 offen sc1\n"
    "v_mad_u32_u16 v235, v228, s90, v239 op_sel:[1,0,0,0]\n"
    "v_mfma_f32_16x16x32_bf16 v[156:159], a[176:179], a[80:83], v[156:159]\n"
    "ds_write_addtid_b32 v214 offset:4608\n"
    "v_mfma_f32_16x16x32_bf16 v[156:159], a[180:183], a[84:87], v[156:159]\n"
    "ds_write_addtid_b32 v215 offset:4864\n"
    "v_mfma_f32_16x16x32_bf16 v[148:151], a[180:183], a[52:55], v[148:151]\n"
    "buffer_load_dwordx4 a[196:199], v235, s[20:23], 0 offen sc1\n"
    "v_mfma_f32_16x16x32_bf16 v[148:151], a[176:179], a[48:51], v[148:151]\n"
    "v_mfma_f32_16x16x32_bf16 v[140:143], a[176:179], a[16:19], v[140:143]\n"
    "s_waitcnt lgkmcnt(0)\n"
    "v_mfma_f32_16x16x32_bf16 v[140:143], a[180:183], a[20:23], v[140:143]\n"
    "s_barrier\n"
    "v_mfma_f32_16x16x32_bf16 v[140:143], a[184:187], a[24:27], v[140:143]\n"
    "s_add_u32 m0, s43, 16384\n"
    "ds_read_b128 a[172:175], v241 offset:6144\n"
    "v_mfma_f32_16x16x32_bf16 v[140:143], a[188:191], a[28:31], v[140:143]\n"
    "ds_read2st64_b32 v[184:185], v249 offset0:0 offset1:1\n"
    "v_mfma_f32_16x16x32_bf16 v[148:151], a[188:191], a[60:63], v[148:151]\n"
    "buffer_load_dwordx4 v248, s[8:11], s40 offen offset:0 sc1 lds\n"
    "v_mfma_f32_16x16x32_bf16 v[148:151], a[184:187], a[56:59], v[148:151]\n"
    "ds_read_b128 a[176:179], v242 offset:2048\n"
    "v_mfma_f32_16x16x32_bf16 v[156:159], a[184:187], a[88:91], v[156:159]\n"
    "v_mfma_f32_16x16x32_bf16 v[156:159], a[188:191], a[92:95], v[156:159]\n"
    "v_mfma_f32_16x16x32_bf16 v[164:167], a[188:191], a[124:127], v[164:167]\n"
    "buffer_load_dwordx4 v248, s[8:11], s40 offen offset:1024 sc1 lds\n"
    "v_mfma_f32_16x16x32_bf16 v[164:167], a[184:187], a[120:123], v[164:167]\n"
    "ds_read2st64_b32 v[186:187], v249 offset0:2 offset1:3\n"
    "v_mfma_f32_16x16x32_bf16 v[172:175], a[184:187], a[152:155], v[172:175]\n"
    "v_mfma_f32_16x16x32_bf16 v[172:175], a[188:191], a[156:159], v[172:175]\n"
    "ds_read_b128 a[180:183], v243 offset:2048\n"
    "v_mfma_f32_16x16x32_bf16 v[176:179], a[160:163], a[128:131], v[220:223]\n"
    "buffer_load_dwordx4 v248, s[8:11], s40 offen offset:2048 sc1 lds\n"
    "v_mfma_f32_16x16x32_bf16 v[176:179], a[164:167], a[132:135], v[176:179]\n"
    "ds_read2st64_b32 v[192:193], v249 offset0:4 offset1:5\n"
    "v_mfma_f32_16x16x32_bf16 v[168:171], a[164:167], a[100:103], v[220:223]\n"
    "v_exp_f32_e32 v140, v140\n"
    "v_mfma_f32_16x16x32_bf16 v[168:171], a[160:163], a[96:99], v[168:171]\n"
    "ds_read_b128 a[184:187], v242 offset:6144\n"
    "v_mfma_f32_16x16x32_bf16 v[160:163], a[160:163], a[64:67], v[220:223]\n"
    "buffer_load_dwordx4 v248, s[8:11], s40 offen offset:3072 sc1 lds\n"
    "v_mfma_f32_16x16x32_bf16 v[160:163], a[164:167], a[68:71], v[160:163]\n"
    "s_add_u32 m0, s43, 20480\n"
    "ds_read2st64_b32 v[194:195], v249 offset0:6 offset1:7\n"
    "v_mfma_f32_16x16x32_bf16 v[152:155], a[164:167], a[36:39], v[220:223]\n"
    "s_add_u32 s40, s40, 0x8000\n"
    "ds_read2st64_b32 v[200:201], v249 offset0:8 offset1:9\n"
    "v_mfma_f32_16x16x32_bf16 v[152:155], a[160:163], a[32:35], v[152:155]\n"
    "ds_read2st64_b32 v[202:203], v249 offset0:10 offset1:11\n"
    "v_mfma_f32_16x16x32_bf16 v[144:147], a[160:163], a[0:3], v[220:223]\n"
    "buffer_load_dwordx4 v248, s[8:11], s41 offen offset:0 sc1 lds\n"
    "v_mfma_f32_16x16x32_bf16 v[144:147], a[164:167], a[4:7], v[144:147]\n"
    "s_waitcnt vmcnt(17)\n"
    "ds_read_b128 a[188:191], v243 offset:6144\n"
    "v_smfmac_f32_16x16x64_bf16 v[224:227], a[232:235], a[200:207], v238\n"
    "s_waitcnt lgkmcnt(10)\n"
    "v_mad_u32_u16 v234, v229, s90, v239 op_sel:[0,0,0,0]\n"
    "v_mfma_f32_16x16x32_bf16 v[144:147], a[168:171], a[8:11], v[144:147]\n"
    "s_bitcmp1_b32 s86, 6\n"
    "buffer_load_dwordx4 a[200:203], v234, s[20:23], 0 offen sc1\n"
    "v_mad_u32_u16 v235, v229, s90, v239 op_sel:[1,0,0,0]\n"
    "v_mfma_f32_16x16x32_bf16 v[144:147], a[172:175], a[12:15], v[144:147]\n"
    "s_cselect_b64 exec, -1, 0\n"
    "v_cvt_pk_bf16_f32 v236, v224, v225\n"
    "v_cvt_pk_bf16_f32 v237, v226, v227\n"
    "v_mfma_f32_16x16x32_bf16 v[152:155], a[172:175], a[44:47], v[152:155]\n"
    "s_cselect_b32 s88, 512, 0\n"
    "v_mov_b64_e32 v[224:225], 0\n"
    "v_mov_b64_e32 v[226:227], 0\n"
    "v_mfma_f32_16x16x32_bf16 v[152:155], a[168:171], a[40:43], v[152:155]\n"
    "buffer_store_dwordx2 v[236:237], v252, s[8:11], s87 offen sc1\n"
    "v_mfma_f32_16x16x32_bf16 v[160:163], a[168:171], a[72:75], v[160:163]\n"
    "s_mov_b64 exec, -1\n"
    "ds_read_b64_tr_b16 a[160:161], v244 offset:24576\n"
    "v_mfma_f32_16x16x32_bf16 v[160:163], a[172:175], a[76:79], v[160:163]\n"
    "s_add_u32 s87, s87, s88\n"
    "ds_read_b64_tr_b16 a[162:163], v244 offset:26624\n"
    "v_mfma_f32_16x16x32_bf16 v[168:171], a[172:175], a[108:111], v[168:171]\n"
    "buffer_load_dwordx4 v248, s[8:11], s41 offen offset:1024 sc1 lds\n"
    "v_mfma_f32_16x16x32_bf16 v[168:171], a[168:171], a[104:107], v[168:171]\n"
    "ds_read_b64_tr_b16 a[164:165], v244 offset:57344\n"
    "v_mfma_f32_16x16x32_bf16 v[176:179], a[168:171], a[136:139], v[176:179]\n"
    "ds_read2st64_b32 v[208:209], v249 offset0:12 offset1:13\n"
    "v_mfma_f32_16x16x32_bf16 v[176:179], a[172:175], a[140:143], v[176:179]\n"
    "s_waitcnt lgkmcnt(10)\n"
    "ds_read2st64_b32 v[210:211], v249 offset0:14 offset1:15\n"
    "v_mfma_f32_16x16x32_bf16 v[176:179], a[176:179], a[144:147], v[176:179]\n"
    "buffer_load_dwordx4 v248, s[8:11], s41 offen offset:2048 sc1 lds\n"
    "v_mfma_f32_16x16x32_bf16 v[176:179], a[180:183], a[148:151], v[176:179]\n"
    "ds_read_b64_tr_b16 a[166:167], v244 offset:59392\n"
    "v_mfma_f32_16x16x32_bf16 v[168:171], a[180:183], a[116:119], v[168:171]\n"
    "ds_read_b64_tr_b16 a[168:169], v245 offset:24576\n"
    "v_mfma_f32_16x16x32_bf16 v[168:171], a[176:179], a[112:115], v[168:171]\n"
    "v_exp_f32_e32 v141, v141\n"
    "v_mfma_f32_16x16x32_bf16 v[160:163], a[176:179], a[80:83], v[160:163]\n"
    "buffer_load_dwordx4 v248, s[8:11], s41 offen offset:3072 sc1 lds\n"
    "v_mfma_f32_16x16x32_bf16 v[160:163], a[180:183], a[84:87], v[160:163]\n"
    "ds_read_b64_tr_b16 a[170:171], v245 offset:26624\n"
    "v_mfma_f32_16x16x32_bf16 v[152:155], a[180:183], a[52:55], v[152:155]\n"
    "s_add_u32 s41, s41, 0x8000\n"
    "ds_read2st64_b32 v[216:217], v249 offset0:16 offset1:17\n"
    "v_mfma_f32_16x16x32_bf16 v[152:155], a[176:179], a[48:51], v[152:155]\n"
    "buffer_load_dwordx4 a[204:207], v235, s[20:23], 0 offen sc1\n"
    "v_mfma_f32_16x16x32_bf16 v[144:147], a[176:179], a[16:19], v[144:147]\n"
    "ds_read_b64_tr_b16 a[172:173], v245 offset:57344\n"
    "v_mfma_f32_16x16x32_bf16 v[144:147], a[180:183], a[20:23], v[144:147]\n"
    "s_waitcnt lgkmcnt(10)\n"
    "ds_read2st64_b32 v[218:219], v249 offset0:18 offset1:19\n"
    "v_mfma_f32_16x16x32_bf16 v[144:147], a[184:187], a[24:27], v[144:147]\n"
    "ds_read_b64_tr_b16 a[176:177], v246 offset:24576\n"
    "v_mfma_f32_16x16x32_bf16 v[144:147], a[188:191], a[28:31], v[144:147]\n"
    "ds_read_b64_tr_b16 a[174:175], v245 offset:59392\n"
    "v_mfma_f32_16x16x32_bf16 v[152:155], a[188:191], a[60:63], v[152:155]\n"
    "v_exp_f32_e32 v142, v142\n"
    "v_mfma_f32_16x16x32_bf16 v[152:155], a[184:187], a[56:59], v[152:155]\n"
    "v_exp_f32_e32 v143, v143\n"
    "v_mfma_f32_16x16x32_bf16 v[160:163], a[184:187], a[88:91], v[160:163]\n"
    "ds_read_b64_tr_b16 a[178:179], v246 offset:26624\n"
    "v_cvt_pk_bf16_f32 v140, v140, v141\n"
    "v_mfma_f32_16x16x32_bf16 v[160:163], a[188:191], a[92:95], v[160:163]\n"
    "v_exp_f32_e32 v148, v148\n"
    "v_mfma_f32_16x16x32_bf16 v[168:171], a[188:191], a[124:127], v[168:171]\n"
    "v_exp_f32_e32 v149, v149\n"
    "v_mfma_f32_16x16x32_bf16 v[168:171], a[184:187], a[120:123], v[168:171]\n"
    "v_cvt_pk_bf16_f32 v141, v142, v143\n"
    "v_mfma_f32_16x16x32_bf16 v[176:179], a[184:187], a[152:155], v[176:179]\n"
    "ds_read_b64_tr_b16 a[180:181], v246 offset:57344\n"
    "v_mfma_f32_16x16x32_bf16 v[176:179], a[188:191], a[156:159], v[176:179]\n"
    "s_waitcnt vmcnt(20)\n"
    "ds_read_b64_tr_b16 a[182:183], v246 offset:59392\n"
    "v_smfmac_f32_16x16x64_bf16 v[224:227], a[232:235], a[208:215], v238\n"
    "s_waitcnt lgkmcnt(5)\n"
    "v_mad_u32_u16 v234, v230, s90, v239 op_sel:[0,0,0,0]\n"
    "v_mfma_f32_16x16x32_bf16 a[236:239], a[160:163], v[180:183], a[236:239]\n"
    "s_bitcmp1_b32 s86, 7\n"
    "buffer_load_dwordx4 a[208:211], v234, s[20:23], 0 offen sc1\n"
    "v_mad_u32_u16 v235, v230, s90, v239 op_sel:[1,0,0,0]\n"
    "v_mfma_f32_16x16x32_bf16 a[236:239], a[164:167], v[184:187], a[236:239]\n"
    "s_cselect_b64 exec, -1, 0\n"
    "v_cvt_pk_bf16_f32 v236, v224, v225\n"
    "v_cvt_pk_bf16_f32 v237, v226, v227\n"
    "v_mfma_f32_16x16x32_bf16 a[240:243], a[164:167], v[192:195], a[240:243]\n"
    "s_cselect_b32 s88, 512, 0\n"
    "v_mov_b64_e32 v[224:225], 0\n"
    "v_mov_b64_e32 v[226:227], 0\n"
    "v_mfma_f32_16x16x32_bf16 a[240:243], a[160:163], v[188:191], a[240:243]\n"
    "buffer_store_dwordx2 v[236:237], v252, s[8:11], s87 offen sc1\n"
    "v_mfma_f32_16x16x32_bf16 a[244:247], a[160:163], v[196:199], a[244:247]\n"
    "s_mov_b64 exec, -1\n"
    "ds_read_b64_tr_b16 a[184:185], v247 offset:24576\n"
    "v_mfma_f32_16x16x32_bf16 a[244:247], a[164:167], v[200:203], a[244:247]\n"
    "s_add_u32 s87, s87, s88\n"
    "ds_read_b64_tr_b16 a[186:187], v247 offset:26624\n"
    "v_mfma_f32_16x16x32_bf16 a[248:251], a[164:167], v[208:211], a[248:251]\n"
    "buffer_load_dwordx4 a[212:215], v235, s[20:23], 0 offen sc1\n"
    "v_mfma_f32_16x16x32_bf16 a[248:251], a[160:163], v[204:207], a[248:251]\n"
    "ds_read_b64_tr_b16 a[188:189], v247 offset:57344\n"
    "v_mfma_f32_16x16x32_bf16 a[252:255], a[160:163], v[212:215], a[252:255]\n"
    "v_exp_f32_e32 v150, v150\n"
    "v_mfma_f32_16x16x32_bf16 a[252:255], a[164:167], v[216:219], a[252:255]\n"
    "s_waitcnt lgkmcnt(6)\n"
    "v_exp_f32_e32 v151, v151\n"
    "v_mfma_f32_16x16x32_bf16 v[16:19], a[172:175], v[216:219], v[16:19]\n"
    "ds_read_b64_tr_b16 a[190:191], v247 offset:59392\n"
    "v_cvt_pk_bf16_f32 v148, v148, v149\n"
    "v_mfma_f32_16x16x32_bf16 v[16:19], a[168:171], v[212:215], v[16:19]\n"
    "ds_read_b64_tr_b16 a[160:161], v244 offset:28672\n"
    "v_mfma_f32_16x16x32_bf16 v[12:15], a[168:171], v[204:207], v[12:15]\n"
    "v_exp_f32_e32 v156, v156\n"
    "v_mfma_f32_16x16x32_bf16 v[12:15], a[172:175], v[208:211], v[12:15]\n"
    "v_exp_f32_e32 v157, v157\n"
    "v_mfma_f32_16x16x32_bf16 v[8:11], a[172:175], v[200:203], v[8:11]\n"
    "ds_read_b64_tr_b16 a[162:163], v244 offset:30720\n"
    "v_cvt_pk_bf16_f32 v149, v150, v151\n"
    "v_mfma_f32_16x16x32_bf16 v[8:11], a[168:171], v[196:199], v[8:11]\n"
    "v_exp_f32_e32 v158, v158\n"
    "v_mfma_f32_16x16x32_bf16 v[4:7], a[168:171], v[188:191], v[4:7]\n"
    "v_exp_f32_e32 v159, v159\n"
    "v_mfma_f32_16x16x32_bf16 v[4:7], a[172:175], v[192:195], v[4:7]\n"
    "v_cvt_pk_bf16_f32 v156, v156, v157\n"
    "v_mfma_f32_16x16x32_bf16 v[0:3], a[172:175], v[184:187], v[0:3]\n"
    "ds_read_b64_tr_b16 a[164:165], v244 offset:61440\n"
    "v_mfma_f32_16x16x32_bf16 v[0:3], a[168:171], v[180:183], v[0:3]\n"
    "s_waitcnt lgkmcnt(7)\n"
    "v_exp_f32_e32 v164, v164\n"
    "v_mfma_f32_16x16x32_bf16 v[20:23], a[176:179], v[180:183], v[20:23]\n"
    "ds_read_b64_tr_b16 a[168:169], v245 offset:28672\n"
    "v_mfma_f32_16x16x32_bf16 v[20:23], a[180:183], v[184:187], v[20:23]\n"
    "ds_read_b64_tr_b16 a[166:167], v244 offset:63488\n"
    "v_mfma_f32_16x16x32_bf16 v[24:27], a[180:183], v[192:195], v[24:27]\n"
    "v_exp_f32_e32 v165, v165\n"
    "v_mfma_f32_16x16x32_bf16 v[24:27], a[176:179], v[188:191], v[24:27]\n"
    "v_cvt_pk_bf16_f32 v157, v158, v159\n"
    "v_mfma_f32_16x16x32_bf16 v[28:31], a[176:179], v[196:199], v[28:31]\n"
    "ds_read_b64_tr_b16 a[170:171], v245 offset:30720\n"
    "v_mfma_f32_16x16x32_bf16 v[28:31], a[180:183], v[200:203], v[28:31]\n"
    "v_exp_f32_e32 v166, v166\n"
    "v_mfma_f32_16x16x32_bf16 v[32:35], a[180:183], v[208:211], v[32:35]\n"
    "v_exp_f32_e32 v167, v167\n"
    "v_mfma_f32_16x16x32_bf16 v[32:35], a[176:179], v[204:207], v[32:35]\n"
    "v_cvt_pk_bf16_f32 v164, v164, v165\n"
    "v_mfma_f32_16x16x32_bf16 v[36:39], a[176:179], v[212:215], v[36:39]\n"
    "ds_read_b64_tr_b16 a[172:173], v245 offset:61440\n"
    "v_mfma_f32_16x16x32_bf16 v[36:39], a[180:183], v[216:219], v[36:39]\n"
    "s_waitcnt vmcnt(20)\n"
    "ds_read_b64_tr_b16 a[174:175], v245 offset:63488\n"
    "v_smfmac_f32_16x16x64_bf16 v[224:227], a[232:235], a[216:223], v238\n"
    "s_waitcnt lgkmcnt(8)\n"
    "v_mad_u32_u16 v234, v231, s90, v239 op_sel:[0,0,0,0]\n"
    "v_mfma_f32_16x16x32_bf16 v[56:59], a[188:191], v[216:219], v[56:59]\n"
    "s_bitcmp1_b32 s86, 8\n"
    "buffer_load_dwordx4 a[216:219], v234, s[20:23], 0 offen sc1\n"
    "v_mad_u32_u16 v235, v231, s90, v239 op_sel:[1,0,0,0]\n"
    "v_mfma_f32_16x16x32_bf16 v[56:59], a[184:187], v[212:215], v[56:59]\n"
    "s_cselect_b64 exec, -1, 0\n"
    "v_cvt_pk_bf16_f32 v236, v224, v225\n"
    "v_cvt_pk_bf16_f32 v237, v226, v227\n"
    "v_mfma_f32_16x16x32_bf16 v[52:55], a[184:187], v[204:207], v[52:55]\n"
    "s_cselect_b32 s88, 512, 0\n"
    "v_mov_b64_e32 v[224:225], 0\n"
    "v_mov_b64_e32 v[226:227], 0\n"
    "v_mfma_f32_16x16x32_bf16 v[52:55], a[188:191], v[208:211], v[52:55]\n"
    "buffer_store_dwordx2 v[236:237], v252, s[8:11], s87 offen sc1\n"
    "v_mfma_f32_16x16x32_bf16 v[48:51], a[188:191], v[200:203], v[48:51]\n"
    "s_mov_b64 exec, -1\n"
    "ds_read_b64_tr_b16 a[176:177], v246 offset:28672\n"
    "v_mfma_f32_16x16x32_bf16 v[48:51], a[184:187], v[196:199], v[48:51]\n"
    "s_add_u32 s87, s87, s88\n"
    "ds_read_b64_tr_b16 a[178:179], v246 offset:30720\n"
    "v_mfma_f32_16x16x32_bf16 v[44:47], a[184:187], v[188:191], v[44:47]\n"
    "buffer_load_dwordx4 a[220:223], v235, s[20:23], 0 offen sc1\n"
    "v_mfma_f32_16x16x32_bf16 v[44:47], a[188:191], v[192:195], v[44:47]\n"
    "ds_read_b64_tr_b16 a[180:181], v246 offset:61440\n"
    "v_mfma_f32_16x16x32_bf16 v[40:43], a[188:191], v[184:187], v[40:43]\n"
    "v_exp_f32_e32 v172, v172\n"
    "v_mfma_f32_16x16x32_bf16 v[40:43], a[184:187], v[180:183], v[40:43]\n"
    "s_waitcnt lgkmcnt(6)\n"
    "v_exp_f32_e32 v173, v173\n"
    "v_mfma_f32_16x16x32_bf16 v[60:63], a[160:163], v[180:183], v[60:63]\n"
    "ds_read_b64_tr_b16 a[182:183], v246 offset:63488\n"
    "v_cvt_pk_bf16_f32 v165, v166, v167\n"
    "v_mfma_f32_16x16x32_bf16 v[60:63], a[164:167], v[184:187], v[60:63]\n"
    "ds_read_b64_tr_b16 a[184:185], v247 offset:28672\n"
    "v_mfma_f32_16x16x32_bf16 v[64:67], a[164:167], v[192:195], v[64:67]\n"
    "ds_read_b64_tr_b16 a[186:187], v247 offset:30720\n"
    "v_mfma_f32_16x16x32_bf16 v[64:67], a[160:163], v[188:191], v[64:67]\n"
    "v_exp_f32_e32 v174, v174\n"
    "v_mfma_f32_16x16x32_bf16 v[68:71], a[160:163], v[196:199], v[68:71]\n"
    "v_exp_f32_e32 v175, v175\n"
    "v_mfma_f32_16x16x32_bf16 v[68:71], a[164:167], v[200:203], v[68:71]\n"
    "ds_read_b64_tr_b16 a[188:189], v247 offset:61440\n"
    "v_cvt_pk_bf16_f32 v172, v172, v173\n"
    "v_mfma_f32_16x16x32_bf16 v[72:75], a[164:167], v[208:211], v[72:75]\n"
    "ds_read_b64_tr_b16 a[190:191], v247 offset:63488\n"
    "v_cvt_pk_bf16_f32 v173, v174, v175\n"
    "v_mfma_f32_16x16x32_bf16 v[72:75], a[160:163], v[204:207], v[72:75]\n"
    "s_waitcnt vmcnt(30)\n"
    "v_mfma_f32_16x16x32_bf16 v[76:79], a[160:163], v[212:215], v[76:79]\n"
    "s_barrier\n"
    "v_mfma_f32_16x16x32_bf16 v[76:79], a[164:167], v[216:219], v[76:79]\n"
    "s_waitcnt lgkmcnt(8)\n"
    "s_mov_b32 m0, s42\n"
    "v_mfma_f32_16x16x32_bf16 v[96:99], a[172:175], v[216:219], v[96:99]\n"
    "v_exp_f32_e32 v144, v144\n"
    "v_mfma_f32_16x16x32_bf16 v[96:99], a[168:171], v[212:215], v[96:99]\n"
    "v_exp_f32_e32 v145, v145\n"
    "v_mfma_f32_16x16x32_bf16 v[92:95], a[168:171], v[204:207], v[92:95]\n"
    "v_exp_f32_e32 v146, v146\n"
    "v_mfma_f32_16x16x32_bf16 v[92:95], a[172:175], v[208:211], v[92:95]\n"
    "ds_read_b128 a[160:163], v240 offset:8192\n"
    "ds_write_addtid_b32 v140 offset:0\n"
    "v_mfma_f32_16x16x32_bf16 v[88:91], a[172:175], v[200:203], v[88:91]\n"
    "v_exp_f32_e32 v147, v147\n"
    "v_mfma_f32_16x16x32_bf16 v[88:91], a[168:171], v[196:199], v[88:91]\n"
    "ds_write_addtid_b32 v141 offset:256\n"
    "v_cvt_pk_bf16_f32 v142, v144, v145\n"
    "v_mfma_f32_16x16x32_bf16 v[84:87], a[168:171], v[188:191], v[84:87]\n"
    "v_exp_f32_e32 v152, v152\n"
    "v_mfma_f32_16x16x32_bf16 v[84:87], a[172:175], v[192:195], v[84:87]\n"
    "v_exp_f32_e32 v153, v153\n"
    "v_mfma_f32_16x16x32_bf16 v[80:83], a[172:175], v[184:187], v[80:83]\n"
    "ds_write_addtid_b32 v148 offset:1024\n"
    "v_cvt_pk_bf16_f32 v143, v146, v147\n"
    "v_mfma_f32_16x16x32_bf16 v[80:83], a[168:171], v[180:183], v[80:83]\n"
    "s_waitcnt vmcnt(20)\n"
    "ds_read_b128 a[164:167], v241 offset:8192\n"
    "ds_write_addtid_b32 v149 offset:1280\n"
    "v_smfmac_f32_16x16x64_bf16 v[224:227], a[232:235], a[224:231], v238\n"
    "s_waitcnt lgkmcnt(10)\n"
    "ds_write_addtid_b32 v156 offset:2048\n"
    "v_mad_u32_u16 v234, v232, s90, v239 op_sel:[0,0,0,0]\n"
    "v_mfma_f32_16x16x32_bf16 v[100:103], a[176:179], v[180:183], v[100:103]\n"
    "s_bitcmp1_b32 s86, 9\n"
    "buffer_load_dwordx4 a[224:227], v234, s[20:23], 0 offen sc1\n"
    "v_mad_u32_u16 v235, v232, s90, v239 op_sel:[1,0,0,0]\n"
    "v_mfma_f32_16x16x32_bf16 v[100:103], a[180:183], v[184:187], v[100:103]\n"
    "s_cselect_b64 exec, -1, 0\n"
    "v_cvt_pk_bf16_f32 v236, v224, v225\n"
    "v_cvt_pk_bf16_f32 v237, v226, v227\n"
    "v_mfma_f32_16x16x32_bf16 v[104:107], a[180:183], v[192:195], v[104:107]\n"
    "s_cselect_b32 s88, 512, 0\n"
    "v_mov_b64_e32 v[224:225], 0\n"
    "v_mov_b64_e32 v[226:227], 0\n"
    "v_mfma_f32_16x16x32_bf16 v[104:107], a[176:179], v[188:191], v[104:107]\n"
    "buffer_store_dwordx2 v[236:237], v252, s[8:11], s87 offen sc1\n"
    "v_mfma_f32_16x16x32_bf16 v[108:111], a[176:179], v[196:199], v[108:111]\n"
    "s_mov_b64 exec, -1\n"
    "ds_read_b128 a[168:171], v240 offset:12288\n"
    "ds_write_addtid_b32 v157 offset:2304\n"
    "v_mfma_f32_16x16x32_bf16 v[108:111], a[180:183], v[200:203], v[108:111]\n"
    "s_add_u32 s87, s87, s88\n"
    "v_exp_f32_e32 v154, v154\n"
    "v_mfma_f32_16x16x32_bf16 v[112:115], a[180:183], v[208:211], v[112:115]\n"
    "buffer_load_dwordx4 a[228:231], v235, s[20:23], 0 offen sc1\n"
    "ds_write_addtid_b32 v164 offset:3072\n"
    "v_mfma_f32_16x16x32_bf16 v[112:115], a[176:179], v[204:207], v[112:115]\n"
    "ds_write_addtid_b32 v165 offset:3328\n"
    "v_mfma_f32_16x16x32_bf16 v[116:119], a[176:179], v[212:215], v[116:119]\n"
    "v_exp_f32_e32 v155, v155\n"
    "v_mfma_f32_16x16x32_bf16 v[116:119], a[180:183], v[216:219], v[116:119]\n"
    "s_waitcnt lgkmcnt(11)\n"
    "ds_read_b128 a[172:175], v241 offset:12288\n"
    "v_cvt_pk_bf16_f32 v150, v152, v153\n"
    "v_mfma_f32_16x16x32_bf16 v[136:139], a[188:191], v[216:219], v[136:139]\n"
    "v_exp_f32_e32 v160, v160\n"
    "v_mfma_f32_16x16x32_bf16 v[136:139], a[184:187], v[212:215], v[136:139]\n"
    "v_exp_f32_e32 v161, v161\n"
    "v_mfma_f32_16x16x32_bf16 v[132:135], a[184:187], v[204:207], v[132:135]\n"
    "ds_read_b128 a[176:179], v242 offset:8192\n"
    "v_cvt_pk_bf16_f32 v151, v154, v155\n"
    "v_mfma_f32_16x16x32_bf16 v[132:135], a[188:191], v[208:211], v[132:135]\n"
    "v_exp_f32_e32 v162, v162\n"
    "v_mfma_f32_16x16x32_bf16 v[128:131], a[188:191], v[200:203], v[128:131]\n"
    "v_exp_f32_e32 v163, v163\n"
    "v_mfma_f32_16x16x32_bf16 v[128:131], a[184:187], v[196:199], v[128:131]\n"
    "ds_write_addtid_b32 v172 offset:4096\n"
    "v_cvt_pk_bf16_f32 v158, v160, v161\n"
    "v_mfma_f32_16x16x32_bf16 v[124:127], a[184:187], v[188:191], v[124:127]\n"
    "v_exp_f32_e32 v168, v168\n"
    "v_mfma_f32_16x16x32_bf16 v[124:127], a[188:191], v[192:195], v[124:127]\n"
    "ds_read_b128 a[180:183], v243 offset:8192\n"
    "ds_write_addtid_b32 v173 offset:4352\n"
    "v_mfma_f32_16x16x32_bf16 v[120:123], a[188:191], v[184:187], v[120:123]\n"
    "v_exp_f32_e32 v169, v169\n"
    "v_mfma_f32_16x16x32_bf16 v[120:123], a[184:187], v[180:183], v[120:123]\n"
    "s_waitcnt lgkmcnt(11)\n"
    "ds_write_addtid_b32 v142 offset:512\n"
    "v_cvt_pk_bf16_f32 v159, v162, v163\n"
    "v_mfma_f32_16x16x32_bf16 v[212:215], a[160:163], a[128:131], v[220:223]\n"
    "ds_write_addtid_b32 v143 offset:768\n"
    "v_mfma_f32_16x16x32_bf16 v[212:215], a[164:167], a[132:135], v[212:215]\n"
    "v_exp_f32_e32 v170, v170\n"
    "v_mfma_f32_16x16x32_bf16 v[204:207], a[164:167], a[100:103], v[220:223]\n"
    "ds_write_addtid_b32 v150 offset:1536\n"
    "v_mfma_f32_16x16x32_bf16 v[204:207], a[160:163], a[96:99], v[204:207]\n"
    "ds_read_b128 a[184:187], v242 offset:12288\n"
    "v_mfma_f32_16x16x32_bf16 v[196:199], a[160:163], a[64:67], v[220:223]\n"
    "v_exp_f32_e32 v171, v171\n"
    "v_mfma_f32_16x16x32_bf16 v[196:199], a[164:167], a[68:71], v[196:199]\n"
    "ds_write_addtid_b32 v151 offset:1792\n"
    "v_cvt_pk_bf16_f32 v166, v168, v169\n"
    "v_mfma_f32_16x16x32_bf16 v[188:191], a[164:167], a[36:39], v[220:223]\n"
    "v_exp_f32_e32 v176, v176\n"
    "v_mfma_f32_16x16x32_bf16 v[188:191], a[160:163], a[32:35], v[188:191]\n"
    "v_exp_f32_e32 v177, v177\n"
    "v_mfma_f32_16x16x32_bf16 v[180:183], a[160:163], a[0:3], v[220:223]\n"
    "ds_write_addtid_b32 v158 offset:2560\n"
    "v_cvt_pk_bf16_f32 v167, v170, v171\n"
    "v_mfma_f32_16x16x32_bf16 v[180:183], a[164:167], a[4:7], v[180:183]\n"
    "s_waitcnt vmcnt(20)\n"
    "v_exp_f32_e32 v178, v178\n"
    "v_smfmac_f32_16x16x64_bf16 v[224:227], a[232:235], a[192:199], v238\n"
    "s_waitcnt lgkmcnt(10)\n"
    "ds_read_b128 a[188:191], v243 offset:12288\n"
    "v_mfma_f32_16x16x32_bf16 v[180:183], a[168:171], a[8:11], v[180:183]\n"
    "s_bitcmp1_b32 s86, 10\n"
    "ds_read_b128 a[160:163], v240 offset:10240\n"
    "v_mfma_f32_16x16x32_bf16 v[180:183], a[172:175], a[12:15], v[180:183]\n"
    "s_cselect_b64 exec, -1, 0\n"
    "v_cvt_pk_bf16_f32 v236, v224, v225\n"
    "v_cvt_pk_bf16_f32 v237, v226, v227\n"
    "v_mfma_f32_16x16x32_bf16 v[188:191], a[172:175], a[44:47], v[188:191]\n"
    "s_cselect_b32 s88, 512, 0\n"
    "v_mov_b64_e32 v[224:225], 0\n"
    "v_mov_b64_e32 v[226:227], 0\n"
    "v_mfma_f32_16x16x32_bf16 v[188:191], a[168:171], a[40:43], v[188:191]\n"
    "buffer_store_dwordx2 v[236:237], v252, s[8:11], s87 offen sc1\n"
    "v_mfma_f32_16x16x32_bf16 v[196:199], a[168:171], a[72:75], v[196:199]\n"
    "s_mov_b64 exec, -1\n"
    "ds_read2_b32 v[228:229], v251 offset0:20 offset1:22\n"
    "v_mfma_f32_16x16x32_bf16 v[196:199], a[172:175], a[76:79], v[196:199]\n"
    "s_add_u32 s87, s87, s88\n"
    "ds_read2_b32 v[230:231], v251 offset0:24 offset1:26\n"
    "v_mfma_f32_16x16x32_bf16 v[204:207], a[172:175], a[108:111], v[204:207]\n"
    "ds_read_b32 v232, v251 offset:112\n"
    "v_mfma_f32_16x16x32_bf16 v[204:207], a[168:171], a[104:107], v[204:207]\n"
    "v_exp_f32_e32 v179, v179\n"
    "v_mfma_f32_16x16x32_bf16 v[212:215], a[168:171], a[136:139], v[212:215]\n"
    "ds_write_addtid_b32 v159 offset:2816\n"
    "v_cvt_pk_bf16_f32 v174, v176, v177\n"
    "v_mfma_f32_16x16x32_bf16 v[212:215], a[172:175], a[140:143], v[212:215]\n"
    "s_waitcnt lgkmcnt(13)\n"
    "ds_read_b128 a[164:167], v241 offset:10240\n"
    "v_mfma_f32_16x16x32_bf16 v[212:215], a[176:179], a[144:147], v[212:215]\n"
    "ds_write_addtid_b32 v166 offset:3584\n"
    "v_cvt_pk_bf16_f32 v175, v178, v179\n"
    "v_mfma_f32_16x16x32_bf16 v[212:215], a[180:183], a[148:151], v[212:215]\n"
    "ds_read_b128 a[168:171], v240 offset:14336\n"
    "v_mfma_f32_16x16x32_bf16 v[204:207], a[180:183], a[116:119], v[204:207]\n"
    "s_waitcnt lgkmcnt(6)\n"
    "ds_write_addtid_b32 v167 offset:3840\n"
    "v_mad_u32_u16 v234, v228, s90, v239 op_sel:[0,0,0,0]\n"
    "v_mfma_f32_16x16x32_bf16 v[204:207], a[176:179], a[112:115], v[204:207]\n"
    "buffer_load_dwordx4 a[192:195], v234, s[20:23], 0 offen sc1\n"
    "v_mad_u32_u16 v235, v228, s90, v239 op_sel:[1,0,0,0]\n"
    "v_mfma_f32_16x16x32_bf16 v[196:199], a[176:179], a[80:83], v[196:199]\n"
    "ds_write_addtid_b32 v174 offset:4608\n"
    "v_mfma_f32_16x16x32_bf16 v[196:199], a[180:183], a[84:87], v[196:199]\n"
    "ds_write_addtid_b32 v175 offset:4864\n"
    "v_mfma_f32_16x16x32_bf16 v[188:191], a[180:183], a[52:55], v[188:191]\n"
    "buffer_load_dwordx4 a[196:199], v235, s[20:23], 0 offen sc1\n"
    "v_mfma_f32_16x16x32_bf16 v[188:191], a[176:179], a[48:51], v[188:191]\n"
    "v_mfma_f32_16x16x32_bf16 v[180:183], a[176:179], a[16:19], v[180:183]\n"
    "s_waitcnt lgkmcnt(0)\n"
    "v_mfma_f32_16x16x32_bf16 v[180:183], a[180:183], a[20:23], v[180:183]\n"
    "s_barrier\n"
    "v_mfma_f32_16x16x32_bf16 v[180:183], a[184:187], a[24:27], v[180:183]\n"
    "s_add_u32 m0, s43, 24576\n"
    "ds_read_b128 a[172:175], v241 offset:14336\n"
    "v_mfma_f32_16x16x32_bf16 v[180:183], a[188:191], a[28:31], v[180:183]\n"
    "ds_read2st64_b32 v[144:145], v249 offset0:0 offset1:1\n"
    "v_mfma_f32_16x16x32_bf16 v[188:191], a[188:191], a[60:63], v[188:191]\n"
    "buffer_load_dwordx4 v248, s[8:11], s40 offen offset:0 sc1 lds\n"
    "v_mfma_f32_16x16x32_bf16 v[188:191], a[184:187], a[56:59], v[188:191]\n"
    "ds_read_b128 a[176:179], v242 offset:10240\n"
    "v_mfma_f32_16x16x32_bf16 v[196:199], a[184:187], a[88:91], v[196:199]\n"
    "v_mfma_f32_16x16x32_bf16 v[196:199], a[188:191], a[92:95], v[196:199]\n"
    "v_mfma_f32_16x16x32_bf16 v[204:207], a[188:191], a[124:127], v[204:207]\n"
    "buffer_load_dwordx4 v248, s[8:11], s40 offen offset:1024 sc1 lds\n"
    "v_mfma_f32_16x16x32_bf16 v[204:207], a[184:187], a[120:123], v[204:207]\n"
    "ds_read2st64_b32 v[146:147], v249 offset0:2 offset1:3\n"
    "v_mfma_f32_16x16x32_bf16 v[212:215], a[184:187], a[152:155], v[212:215]\n"
    "v_mfma_f32_16x16x32_bf16 v[212:215], a[188:191], a[156:159], v[212:215]\n"
    "ds_read_b128 a[180:183], v243 offset:10240\n"
    "v_mfma_f32_16x16x32_bf16 v[216:219], a[160:163], a[128:131], v[220:223]\n"
    "buffer_load_dwordx4 v248, s[8:11], s40 offen offset:2048 sc1 lds\n"
    "v_mfma_f32_16x16x32_bf16 v[216:219], a[164:167], a[132:135], v[216:219]\n"
    "ds_read2st64_b32 v[152:153], v249 offset0:4 offset1:5\n"
    "v_mfma_f32_16x16x32_bf16 v[208:211], a[164:167], a[100:103], v[220:223]\n"
    "v_exp_f32_e32 v180, v180\n"
    "v_mfma_f32_16x16x32_bf16 v[208:211], a[160:163], a[96:99], v[208:211]\n"
    "ds_read_b128 a[184:187], v242 offset:14336\n"
    "v_mfma_f32_16x16x32_bf16 v[200:203], a[160:163], a[64:67], v[220:223]\n"
    "buffer_load_dwordx4 v248, s[8:11], s40 offen offset:3072 sc1 lds\n"
    "v_mfma_f32_16x16x32_bf16 v[200:203], a[164:167], a[68:71], v[200:203]\n"
    "s_add_u32 m0, s43, 28672\n"
    "ds_read2st64_b32 v[154:155], v249 offset0:6 offset1:7\n"
    "v_mfma_f32_16x16x32_bf16 v[192:195], a[164:167], a[36:39], v[220:223]\n"
    "s_add_u32 s40, s40, 0x8000\n"
    "ds_read2st64_b32 v[160:161], v249 offset0:8 offset1:9\n"
    "v_mfma_f32_16x16x32_bf16 v[192:195], a[160:163], a[32:35], v[192:195]\n"
    "ds_read2st64_b32 v[162:163], v249 offset0:10 offset1:11\n"
    "v_mfma_f32_16x16x32_bf16 v[184:187], a[160:163], a[0:3], v[220:223]\n"
    "buffer_load_dwordx4 v248, s[8:11], s41 offen offset:0 sc1 lds\n"
    "v_mfma_f32_16x16x32_bf16 v[184:187], a[164:167], a[4:7], v[184:187]\n"
    "s_waitcnt vmcnt(17)\n"
    "ds_read_b128 a[188:191], v243 offset:14336\n"
    "v_smfmac_f32_16x16x64_bf16 v[224:227], a[232:235], a[200:207], v238\n"
    "s_waitcnt lgkmcnt(10)\n"
    "v_mad_u32_u16 v234, v229, s90, v239 op_sel:[0,0,0,0]\n"
    "v_mfma_f32_16x16x32_bf16 v[184:187], a[168:171], a[8:11], v[184:187]\n"
    "s_bitcmp1_b32 s86, 11\n"
    "buffer_load_dwordx4 a[200:203], v234, s[20:23], 0 offen sc1\n"
    "v_mad_u32_u16 v235, v229, s90, v239 op_sel:[1,0,0,0]\n"
    "v_mfma_f32_16x16x32_bf16 v[184:187], a[172:175], a[12:15], v[184:187]\n"
    "s_cselect_b64 exec, -1, 0\n"
    "v_cvt_pk_bf16_f32 v236, v224, v225\n"
    "v_cvt_pk_bf16_f32 v237, v226, v227\n"
    "v_mfma_f32_16x16x32_bf16 v[192:195], a[172:175], a[44:47], v[192:195]\n"
    "s_cselect_b32 s88, 512, 0\n"
    "v_mov_b64_e32 v[224:225], 0\n"
    "v_mov_b64_e32 v[226:227], 0\n"
    "v_mfma_f32_16x16x32_bf16 v[192:195], a[168:171], a[40:43], v[192:195]\n"
    "buffer_store_dwordx2 v[236:237], v252, s[8:11], s87 offen sc1\n"
    "v_mfma_f32_16x16x32_bf16 v[200:203], a[168:171], a[72:75], v[200:203]\n"
    "s_mov_b64 exec, -1\n"
    "ds_read_b64_tr_b16 a[160:161], v244 offset:0\n"
    "v_mfma_f32_16x16x32_bf16 v[200:203], a[172:175], a[76:79], v[200:203]\n"
    "s_add_u32 s87, s87, s88\n"
    "ds_read_b64_tr_b16 a[162:163], v244 offset:2048\n"
    "v_mfma_f32_16x16x32_bf16 v[208:211], a[172:175], a[108:111], v[208:211]\n"
    "buffer_load_dwordx4 v248, s[8:11], s41 offen offset:1024 sc1 lds\n"
    "v_mfma_f32_16x16x32_bf16 v[208:211], a[168:171], a[104:107], v[208:211]\n"
    "ds_read_b64_tr_b16 a[164:165], v244 offset:32768\n"
    "v_mfma_f32_16x16x32_bf16 v[216:219], a[168:171], a[136:139], v[216:219]\n"
    "ds_read2st64_b32 v[168:169], v249 offset0:12 offset1:13\n"
    "v_mfma_f32_16x16x32_bf16 v[216:219], a[172:175], a[140:143], v[216:219]\n"
    "s_waitcnt lgkmcnt(10)\n"
    "ds_read2st64_b32 v[170:171], v249 offset0:14 offset1:15\n"
    "v_mfma_f32_16x16x32_bf16 v[216:219], a[176:179], a[144:147], v[216:219]\n"
    "buffer_load_dwordx4 v248, s[8:11], s41 offen offset:2048 sc1 lds\n"
    "v_mfma_f32_16x16x32_bf16 v[216:219], a[180:183], a[148:151], v[216:219]\n"
    "ds_read_b64_tr_b16 a[166:167], v244 offset:34816\n"
    "v_mfma_f32_16x16x32_bf16 v[208:211], a[180:183], a[116:119], v[208:211]\n"
    "ds_read_b64_tr_b16 a[168:169], v245 offset:0\n"
    "v_mfma_f32_16x16x32_bf16 v[208:211], a[176:179], a[112:115], v[208:211]\n"
    "v_exp_f32_e32 v181, v181\n"
    "v_mfma_f32_16x16x32_bf16 v[200:203], a[176:179], a[80:83], v[200:203]\n"
    "buffer_load_dwordx4 v248, s[8:11], s41 offen offset:3072 sc1 lds\n"
    "v_mfma_f32_16x16x32_bf16 v[200:203], a[180:183], a[84:87], v[200:203]\n"
    "ds_read_b64_tr_b16 a[170:171], v245 offset:2048\n"
    "v_mfma_f32_16x16x32_bf16 v[192:195], a[180:183], a[52:55], v[192:195]\n"
    "s_add_u32 s41, s41, 0x8000\n"
    "ds_read2st64_b32 v[176:177], v249 offset0:16 offset1:17\n"
    "v_mfma_f32_16x16x32_bf16 v[192:195], a[176:179], a[48:51], v[192:195]\n"
    "buffer_load_dwordx4 a[204:207], v235, s[20:23], 0 offen sc1\n"
    "v_mfma_f32_16x16x32_bf16 v[184:187], a[176:179], a[16:19], v[184:187]\n"
    "ds_read_b64_tr_b16 a[172:173], v245 offset:32768\n"
    "v_mfma_f32_16x16x32_bf16 v[184:187], a[180:183], a[20:23], v[184:187]\n"
    "s_waitcnt lgkmcnt(10)\n"
    "ds_read2st64_b32 v[178:179], v249 offset0:18 offset1:19\n"
    "v_mfma_f32_16x16x32_bf16 v[184:187], a[184:187], a[24:27], v[184:187]\n"
    "ds_read_b64_tr_b16 a[176:177], v246 offset:0\n"
    "v_mfma_f32_16x16x32_bf16 v[184:187], a[188:191], a[28:31], v[184:187]\n"
    "ds_read_b64_tr_b16 a[174:175], v245 offset:34816\n"
    "v_mfma_f32_16x16x32_bf16 v[192:195], a[188:191], a[60:63], v[192:195]\n"
    "v_exp_f32_e32 v182, v182\n"
    "v_mfma_f32_16x16x32_bf16 v[192:195], a[184:187], a[56:59], v[192:195]\n"
    "v_exp_f32_e32 v183, v183\n"
    "v_mfma_f32_16x16x32_bf16 v[200:203], a[184:187], a[88:91], v[200:203]\n"
    "ds_read_b64_tr_b16 a[178:179], v246 offset:2048\n"
    "v_cvt_pk_bf16_f32 v180, v180, v181\n"
    "v_mfma_f32_16x16x32_bf16 v[200:203], a[188:191], a[92:95], v[200:203]\n"
    "v_exp_f32_e32 v188, v188\n"
    "v_mfma_f32_16x16x32_bf16 v[208:211], a[188:191], a[124:127], v[208:211]\n"
    "v_exp_f32_e32 v189, v189\n"
    "v_mfma_f32_16x16x32_bf16 v[208:211], a[184:187], a[120:123], v[208:211]\n"
    "v_cvt_pk_bf16_f32 v181, v182, v183\n"
    "v_mfma_f32_16x16x32_bf16 v[216:219], a[184:187], a[152:155], v[216:219]\n"
    "ds_read_b64_tr_b16 a[180:181], v246 offset:32768\n"
    "v_mfma_f32_16x16x32_bf16 v[216:219], a[188:191], a[156:159], v[216:219]\n"
    "s_waitcnt vmcnt(20)\n"
    "ds_read_b64_tr_b16 a[182:183], v246 offset:34816\n"
    "v_smfmac_f32_16x16x64_bf16 v[224:227], a[232:235], a[208:215], v238\n"
    "s_waitcnt lgkmcnt(5)\n"
    "v_mad_u32_u16 v234, v230, s90, v239 op_sel:[0,0,0,0]\n"
    "v_mfma_f32_16x16x32_bf16 a[236:239], a[160:163], v[140:143], a[236:239]\n"
    "s_bitcmp1_b32 s86, 12\n"
    "buffer_load_dwordx4 a[208:211], v234, s[20:23], 0 offen sc1\n"
    "v_mad_u32_u16 v235, v230, s90, v239 op_sel:[1,0,0,0]\n"
    "v_mfma_f32_16x16x32_bf16 a[236:239], a[164:167], v[144:147], a[236:239]\n"
    "s_cselect_b64 exec, -1, 0\n"
    "v_cvt_pk_bf16_f32 v236, v224, v225\n"
    "v_cvt_pk_bf16_f32 v237, v226, v227\n"
    "v_mfma_f32_16x16x32_bf16 a[240:243], a[164:167], v[152:155], a[240:243]\n"
    "s_cselect_b32 s88, 512, 0\n"
    "v_mov_b64_e32 v[224:225], 0\n"
    "v_mov_b64_e32 v[226:227], 0\n"
    "v_mfma_f32_16x16x32_bf16 a[240:243], a[160:163], v[148:151], a[240:243]\n"
    "buffer_store_dwordx2 v[236:237], v252, s[8:11], s87 offen sc1\n"
    "v_mfma_f32_16x16x32_bf16 a[244:247], a[160:163], v[156:159], a[244:247]\n"
    "s_mov_b64 exec, -1\n"
    "ds_read_b64_tr_b16 a[184:185], v247 offset:0\n"
    "v_mfma_f32_16x16x32_bf16 a[244:247], a[164:167], v[160:163], a[244:247]\n"
    "s_add_u32 s87, s87, s88\n"
    "ds_read_b64_tr_b16 a[186:187], v247 offset:2048\n"
    "v_mfma_f32_16x16x32_bf16 a[248:251], a[164:167], v[168:171], a[248:251]\n"
    "buffer_load_dwordx4 a[212:215], v235, s[20:23], 0 offen sc1\n"
    "v_mfma_f32_16x16x32_bf16 a[248:251], a[160:163], v[164:167], a[248:251]\n"
    "ds_read_b64_tr_b16 a[188:189], v247 offset:32768\n"
    "v_mfma_f32_16x16x32_bf16 a[252:255], a[160:163], v[172:175], a[252:255]\n"
    "v_exp_f32_e32 v190, v190\n"
    "v_mfma_f32_16x16x32_bf16 a[252:255], a[164:167], v[176:179], a[252:255]\n"
    "s_waitcnt lgkmcnt(6)\n"
    "v_exp_f32_e32 v191, v191\n"
    "v_mfma_f32_16x16x32_bf16 v[16:19], a[172:175], v[176:179], v[16:19]\n"
    "ds_read_b64_tr_b16 a[190:191], v247 offset:34816\n"
    "v_cvt_pk_bf16_f32 v188, v188, v189\n"
    "v_mfma_f32_16x16x32_bf16 v[16:19], a[168:171], v[172:175], v[16:19]\n"
    "ds_read_b64_tr_b16 a[160:161], v244 offset:4096\n"
    "v_mfma_f32_16x16x32_bf16 v[12:15], a[168:171], v[164:167], v[12:15]\n"
    "v_exp_f32_e32 v196, v196\n"
    "v_mfma_f32_16x16x32_bf16 v[12:15], a[172:175], v[168:171], v[12:15]\n"
    "v_exp_f32_e32 v197, v197\n"
    "v_mfma_f32_16x16x32_bf16 v[8:11], a[172:175], v[160:163], v[8:11]\n"
    "ds_read_b64_tr_b16 a[162:163], v244 offset:6144\n"
    "v_cvt_pk_bf16_f32 v189, v190, v191\n"
    "v_mfma_f32_16x16x32_bf16 v[8:11], a[168:171], v[156:159], v[8:11]\n"
    "v_exp_f32_e32 v198, v198\n"
    "v_mfma_f32_16x16x32_bf16 v[4:7], a[168:171], v[148:151], v[4:7]\n"
    "v_exp_f32_e32 v199, v199\n"
    "v_mfma_f32_16x16x32_bf16 v[4:7], a[172:175], v[152:155], v[4:7]\n"
    "v_cvt_pk_bf16_f32 v196, v196, v197\n"
    "v_mfma_f32_16x16x32_bf16 v[0:3], a[172:175], v[144:147], v[0:3]\n"
    "ds_read_b64_tr_b16 a[164:165], v244 offset:36864\n"
    "v_mfma_f32_16x16x32_bf16 v[0:3], a[168:171], v[140:143], v[0:3]\n"
    "s_waitcnt lgkmcnt(7)\n"
    "v_exp_f32_e32 v204, v204\n"
    "v_mfma_f32_16x16x32_bf16 v[20:23], a[176:179], v[140:143], v[20:23]\n"
    "ds_read_b64_tr_b16 a[168:169], v245 offset:4096\n"
    "v_mfma_f32_16x16x32_bf16 v[20:23], a[180:183], v[144:147], v[20:23]\n"
    "ds_read_b64_tr_b16 a[166:167], v244 offset:38912\n"
    "v_mfma_f32_16x16x32_bf16 v[24:27], a[180:183], v[152:155], v[24:27]\n"
    "v_exp_f32_e32 v205, v205\n"
    "v_mfma_f32_16x16x32_bf16 v[24:27], a[176:179], v[148:151], v[24:27]\n"
    "v_cvt_pk_bf16_f32 v197, v198, v199\n"
    "v_mfma_f32_16x16x32_bf16 v[28:31], a[176:179], v[156:159], v[28:31]\n"
    "ds_read_b64_tr_b16 a[170:171], v245 offset:6144\n"
    "v_mfma_f32_16x16x32_bf16 v[28:31], a[180:183], v[160:163], v[28:31]\n"
    "v_exp_f32_e32 v206, v206\n"
    "v_mfma_f32_16x16x32_bf16 v[32:35], a[180:183], v[168:171], v[32:35]\n"
    "v_exp_f32_e32 v207, v207\n"
    "v_mfma_f32_16x16x32_bf16 v[32:35], a[176:179], v[164:167], v[32:35]\n"
    "v_cvt_pk_bf16_f32 v204, v204, v205\n"
    "v_mfma_f32_16x16x32_bf16 v[36:39], a[176:179], v[172:175], v[36:39]\n"
    "ds_read_b64_tr_b16 a[172:173], v245 offset:36864\n"
    "v_mfma_f32_16x16x32_bf16 v[36:39], a[180:183], v[176:179], v[36:39]\n"
    "s_waitcnt vmcnt(20)\n"
    "ds_read_b64_tr_b16 a[174:175], v245 offset:38912\n"
    "v_smfmac_f32_16x16x64_bf16 v[224:227], a[232:235], a[216:223], v238\n"
    "s_waitcnt lgkmcnt(8)\n"
    "v_mad_u32_u16 v234, v231, s90, v239 op_sel:[0,0,0,0]\n"
    "v_mfma_f32_16x16x32_bf16 v[56:59], a[188:191], v[176:179], v[56:59]\n"
    "s_bitcmp1_b32 s86, 13\n"
    "buffer_load_dwordx4 a[216:219], v234, s[20:23], 0 offen sc1\n"
    "v_mad_u32_u16 v235, v231, s90, v239 op_sel:[1,0,0,0]\n"
    "v_mfma_f32_16x16x32_bf16 v[56:59], a[184:187], v[172:175], v[56:59]\n"
    "s_cselect_b64 exec, -1, 0\n"
    "v_cvt_pk_bf16_f32 v236, v224, v225\n"
    "v_cvt_pk_bf16_f32 v237, v226, v227\n"
    "v_mfma_f32_16x16x32_bf16 v[52:55], a[184:187], v[164:167], v[52:55]\n"
    "s_cselect_b32 s88, 512, 0\n"
    "v_mov_b64_e32 v[224:225], 0\n"
    "v_mov_b64_e32 v[226:227], 0\n"
    "v_mfma_f32_16x16x32_bf16 v[52:55], a[188:191], v[168:171], v[52:55]\n"
    "buffer_store_dwordx2 v[236:237], v252, s[8:11], s87 offen sc1\n"
    "v_mfma_f32_16x16x32_bf16 v[48:51], a[188:191], v[160:163], v[48:51]\n"
    "s_mov_b64 exec, -1\n"
    "ds_read_b64_tr_b16 a[176:177], v246 offset:4096\n"
    "v_mfma_f32_16x16x32_bf16 v[48:51], a[184:187], v[156:159], v[48:51]\n"
    "s_add_u32 s87, s87, s88\n"
    "ds_read_b64_tr_b16 a[178:179], v246 offset:6144\n"
    "v_mfma_f32_16x16x32_bf16 v[44:47], a[184:187], v[148:151], v[44:47]\n"
    "buffer_load_dwordx4 a[220:223], v235, s[20:23], 0 offen sc1\n"
    "v_mfma_f32_16x16x32_bf16 v[44:47], a[188:191], v[152:155], v[44:47]\n"
    "ds_read_b64_tr_b16 a[180:181], v246 offset:36864\n"
    "v_mfma_f32_16x16x32_bf16 v[40:43], a[188:191], v[144:147], v[40:43]\n"
    "v_exp_f32_e32 v212, v212\n"
    "v_mfma_f32_16x16x32_bf16 v[40:43], a[184:187], v[140:143], v[40:43]\n"
    "s_waitcnt lgkmcnt(6)\n"
    "v_exp_f32_e32 v213, v213\n"
    "v_mfma_f32_16x16x32_bf16 v[60:63], a[160:163], v[140:143], v[60:63]\n"
    "ds_read_b64_tr_b16 a[182:183], v246 offset:38912\n"
    "v_cvt_pk_bf16_f32 v205, v206, v207\n"
    "v_mfma_f32_16x16x32_bf16 v[60:63], a[164:167], v[144:147], v[60:63]\n"
    "ds_read_b64_tr_b16 a[184:185], v247 offset:4096\n"
    "v_mfma_f32_16x16x32_bf16 v[64:67], a[164:167], v[152:155], v[64:67]\n"
    "ds_read_b64_tr_b16 a[186:187], v247 offset:6144\n"
    "v_mfma_f32_16x16x32_bf16 v[64:67], a[160:163], v[148:151], v[64:67]\n"
    "v_exp_f32_e32 v214, v214\n"
    "v_mfma_f32_16x16x32_bf16 v[68:71], a[160:163], v[156:159], v[68:71]\n"
    "v_exp_f32_e32 v215, v215\n"
    "v_mfma_f32_16x16x32_bf16 v[68:71], a[164:167], v[160:163], v[68:71]\n"
    "ds_read_b64_tr_b16 a[188:189], v247 offset:36864\n"
    "v_cvt_pk_bf16_f32 v212, v212, v213\n"
    "v_mfma_f32_16x16x32_bf16 v[72:75], a[164:167], v[168:171], v[72:75]\n"
    "ds_read_b64_tr_b16 a[190:191], v247 offset:38912\n"
    "v_cvt_pk_bf16_f32 v213, v214, v215\n"
    "v_mfma_f32_16x16x32_bf16 v[72:75], a[160:163], v[164:167], v[72:75]\n"
    "s_waitcnt vmcnt(30)\n"
    "v_mfma_f32_16x16x32_bf16 v[76:79], a[160:163], v[172:175], v[76:79]\n"
    "s_barrier\n"
    "v_mfma_f32_16x16x32_bf16 v[76:79], a[164:167], v[176:179], v[76:79]\n"
    "s_waitcnt lgkmcnt(8)\n"
    "s_mov_b32 m0, s42\n"
    "v_mfma_f32_16x16x32_bf16 v[96:99], a[172:175], v[176:179], v[96:99]\n"
    "v_exp_f32_e32 v184, v184\n"
    "v_mfma_f32_16x16x32_bf16 v[96:99], a[168:171], v[172:175], v[96:99]\n"
    "v_exp_f32_e32 v185, v185\n"
    "v_mfma_f32_16x16x32_bf16 v[92:95], a[168:171], v[164:167], v[92:95]\n"
    "v_exp_f32_e32 v186, v186\n"
    "v_mfma_f32_16x16x32_bf16 v[92:95], a[172:175], v[168:171], v[92:95]\n"
    "ds_read_b128 a[160:163], v240 offset:16384\n"
    "ds_write_addtid_b32 v180 offset:0\n"
    "v_mfma_f32_16x16x32_bf16 v[88:91], a[172:175], v[160:163], v[88:91]\n"
    "v_exp_f32_e32 v187, v187\n"
    "v_mfma_f32_16x16x32_bf16 v[88:91], a[168:171], v[156:159], v[88:91]\n"
    "ds_write_addtid_b32 v181 offset:256\n"
    "v_cvt_pk_bf16_f32 v182, v184, v185\n"
    "v_mfma_f32_16x16x32_bf16 v[84:87], a[168:171], v[148:151], v[84:87]\n"
    "v_exp_f32_e32 v192, v192\n"
    "v_mfma_f32_16x16x32_bf16 v[84:87], a[172:175], v[152:155], v[84:87]\n"
    "v_exp_f32_e32 v193, v193\n"
    "v_mfma_f32_16x16x32_bf16 v[80:83], a[172:175], v[144:147], v[80:83]\n"
    "ds_write_addtid_b32 v188 offset:1024\n"
    "v_cvt_pk_bf16_f32 v183, v186, v187\n"
    "v_mfma_f32_16x16x32_bf16 v[80:83], a[168:171], v[140:143], v[80:83]\n"
    "s_waitcnt vmcnt(20)\n"
    "ds_read_b128 a[164:167], v241 offset:16384\n"
    "ds_write_addtid_b32 v189 offset:1280\n"
    "v_smfmac_f32_16x16x64_bf16 v[224:227], a[232:235], a[224:231], v238\n"
    "s_waitcnt lgkmcnt(10)\n"
    "ds_write_addtid_b32 v196 offset:2048\n"
    "v_mad_u32_u16 v234, v232, s90, v239 op_sel:[0,0,0,0]\n"
    "v_mfma_f32_16x16x32_bf16 v[100:103], a[176:179], v[140:143], v[100:103]\n"
    "s_bitcmp1_b32 s86, 14\n"
    "buffer_load_dwordx4 a[224:227], v234, s[20:23], 0 offen sc1\n"
    "v_mad_u32_u16 v235, v232, s90, v239 op_sel:[1,0,0,0]\n"
    "v_mfma_f32_16x16x32_bf16 v[100:103], a[180:183], v[144:147], v[100:103]\n"
    "s_cselect_b64 exec, -1, 0\n"
    "v_cvt_pk_bf16_f32 v236, v224, v225\n"
    "v_cvt_pk_bf16_f32 v237, v226, v227\n"
    "v_mfma_f32_16x16x32_bf16 v[104:107], a[180:183], v[152:155], v[104:107]\n"
    "s_cselect_b32 s88, 512, 0\n"
    "v_mov_b64_e32 v[224:225], 0\n"
    "v_mov_b64_e32 v[226:227], 0\n"
    "v_mfma_f32_16x16x32_bf16 v[104:107], a[176:179], v[148:151], v[104:107]\n"
    "buffer_store_dwordx2 v[236:237], v252, s[8:11], s87 offen sc1\n"
    "v_mfma_f32_16x16x32_bf16 v[108:111], a[176:179], v[156:159], v[108:111]\n"
    "s_mov_b64 exec, -1\n"
    "ds_read_b128 a[168:171], v240 offset:20480\n"
    "ds_write_addtid_b32 v197 offset:2304\n"
    "v_mfma_f32_16x16x32_bf16 v[108:111], a[180:183], v[160:163], v[108:111]\n"
    "s_add_u32 s87, s87, s88\n"
    "v_exp_f32_e32 v194, v194\n"
    "v_mfma_f32_16x16x32_bf16 v[112:115], a[180:183], v[168:171], v[112:115]\n"
    "buffer_load_dwordx4 a[228:231], v235, s[20:23], 0 offen sc1\n"
    "ds_write_addtid_b32 v204 offset:3072\n"
    "v_mfma_f32_16x16x32_bf16 v[112:115], a[176:179], v[164:167], v[112:115]\n"
    "ds_write_addtid_b32 v205 offset:3328\n"
    "v_mfma_f32_16x16x32_bf16 v[116:119], a[176:179], v[172:175], v[116:119]\n"
    "v_exp_f32_e32 v195, v195\n"
    "v_mfma_f32_16x16x32_bf16 v[116:119], a[180:183], v[176:179], v[116:119]\n"
    "s_waitcnt lgkmcnt(11)\n"
    "ds_read_b128 a[172:175], v241 offset:20480\n"
    "v_cvt_pk_bf16_f32 v190, v192, v193\n"
    "v_mfma_f32_16x16x32_bf16 v[136:139], a[188:191], v[176:179], v[136:139]\n"
    "v_exp_f32_e32 v200, v200\n"
    "v_mfma_f32_16x16x32_bf16 v[136:139], a[184:187], v[172:175], v[136:139]\n"
    "v_exp_f32_e32 v201, v201\n"
    "v_mfma_f32_16x16x32_bf16 v[132:135], a[184:187], v[164:167], v[132:135]\n"
    "ds_read_b128 a[176:179], v242 offset:16384\n"
    "v_cvt_pk_bf16_f32 v191, v194, v195\n"
    "v_mfma_f32_16x16x32_bf16 v[132:135], a[188:191], v[168:171], v[132:135]\n"
    "v_exp_f32_e32 v202, v202\n"
    "v_mfma_f32_16x16x32_bf16 v[128:131], a[188:191], v[160:163], v[128:131]\n"
    "v_exp_f32_e32 v203, v203\n"
    "v_mfma_f32_16x16x32_bf16 v[128:131], a[184:187], v[156:159], v[128:131]\n"
    "ds_write_addtid_b32 v212 offset:4096\n"
    "v_cvt_pk_bf16_f32 v198, v200, v201\n"
    "v_mfma_f32_16x16x32_bf16 v[124:127], a[184:187], v[148:151], v[124:127]\n"
    "v_exp_f32_e32 v208, v208\n"
    "v_mfma_f32_16x16x32_bf16 v[124:127], a[188:191], v[152:155], v[124:127]\n"
    "ds_read_b128 a[180:183], v243 offset:16384\n"
    "ds_write_addtid_b32 v213 offset:4352\n"
    "v_mfma_f32_16x16x32_bf16 v[120:123], a[188:191], v[144:147], v[120:123]\n"
    "v_exp_f32_e32 v209, v209\n"
    "v_mfma_f32_16x16x32_bf16 v[120:123], a[184:187], v[140:143], v[120:123]\n"
    "s_waitcnt lgkmcnt(11)\n"
    "ds_write_addtid_b32 v182 offset:512\n"
    "v_cvt_pk_bf16_f32 v199, v202, v203\n"
    "v_mfma_f32_16x16x32_bf16 v[172:175], a[160:163], a[128:131], v[220:223]\n"
    "ds_write_addtid_b32 v183 offset:768\n"
    "v_mfma_f32_16x16x32_bf16 v[172:175], a[164:167], a[132:135], v[172:175]\n"
    "v_exp_f32_e32 v210, v210\n"
    "v_mfma_f32_16x16x32_bf16 v[164:167], a[164:167], a[100:103], v[220:223]\n"
    "ds_write_addtid_b32 v190 offset:1536\n"
    "v_mfma_f32_16x16x32_bf16 v[164:167], a[160:163], a[96:99], v[164:167]\n"
    "ds_read_b128 a[184:187], v242 offset:20480\n"
    "v_mfma_f32_16x16x32_bf16 v[156:159], a[160:163], a[64:67], v[220:223]\n"
    "v_exp_f32_e32 v211, v211\n"
    "v_mfma_f32_16x16x32_bf16 v[156:159], a[164:167], a[68:71], v[156:159]\n"
    "ds_write_addtid_b32 v191 offset:1792\n"
    "v_cvt_pk_bf16_f32 v206, v208, v209\n"
    "v_mfma_f32_16x16x32_bf16 v[148:151], a[164:167], a[36:39], v[220:223]\n"
    "v_exp_f32_e32 v216, v216\n"
    "v_mfma_f32_16x16x32_bf16 v[148:151], a[160:163], a[32:35], v[148:151]\n"
    "v_exp_f32_e32 v217, v217\n"
    "v_mfma_f32_16x16x32_bf16 v[140:143], a[160:163], a[0:3], v[220:223]\n"
    "ds_write_addtid_b32 v198 offset:2560\n"
    "v_cvt_pk_bf16_f32 v207, v210, v211\n"
    "v_mfma_f32_16x16x32_bf16 v[140:143], a[164:167], a[4:7], v[140:143]\n"
    "s_waitcnt vmcnt(20)\n"
    "v_exp_f32_e32 v218, v218\n"
    "v_smfmac_f32_16x16x64_bf16 v[224:227], a[232:235], a[192:199], v238\n"
    "s_waitcnt lgkmcnt(10)\n"
    "ds_read_b128 a[188:191], v243 offset:20480\n"
    "v_mfma_f32_16x16x32_bf16 v[140:143], a[168:171], a[8:11], v[140:143]\n"
    "s_bitcmp1_b32 s86, 15\n"
    "ds_read_b128 a[160:163], v240 offset:18432\n"
    "v_mfma_f32_16x16x32_bf16 v[140:143], a[172:175], a[12:15], v[140:143]\n"
    "s_cselect_b64 exec, -1, 0\n"
    "v_cvt_pk_bf16_f32 v236, v224, v225\n"
    "v_cvt_pk_bf16_f32 v237, v226, v227\n"
    "v_mfma_f32_16x16x32_bf16 v[148:151], a[172:175], a[44:47], v[148:151]\n"
    "s_cselect_b32 s88, 512, 0\n"
    "v_mov_b64_e32 v[224:225], 0\n"
    "v_mov_b64_e32 v[226:227], 0\n"
    "v_mfma_f32_16x16x32_bf16 v[148:151], a[168:171], a[40:43], v[148:151]\n"
    "buffer_store_dwordx2 v[236:237], v252, s[8:11], s87 offen sc1\n"
    "v_mfma_f32_16x16x32_bf16 v[156:159], a[168:171], a[72:75], v[156:159]\n"
    "s_mov_b64 exec, -1\n"
    "ds_read2_b32 v[228:229], v251 offset0:30 offset1:32\n"
    "v_mfma_f32_16x16x32_bf16 v[156:159], a[172:175], a[76:79], v[156:159]\n"
    "s_add_u32 s87, s87, s88\n"
    "ds_read2_b32 v[230:231], v251 offset0:34 offset1:36\n"
    "v_mfma_f32_16x16x32_bf16 v[164:167], a[172:175], a[108:111], v[164:167]\n"
    "ds_read_b32 v232, v251 offset:152\n"
    "v_mfma_f32_16x16x32_bf16 v[164:167], a[168:171], a[104:107], v[164:167]\n"
    "v_exp_f32_e32 v219, v219\n"
    "v_mfma_f32_16x16x32_bf16 v[172:175], a[168:171], a[136:139], v[172:175]\n"
    "ds_write_addtid_b32 v199 offset:2816\n"
    "v_cvt_pk_bf16_f32 v214, v216, v217\n"
    "v_mfma_f32_16x16x32_bf16 v[172:175], a[172:175], a[140:143], v[172:175]\n"
    "s_waitcnt lgkmcnt(13)\n"
    "ds_read_b128 a[164:167], v241 offset:18432\n"
    "v_mfma_f32_16x16x32_bf16 v[172:175], a[176:179], a[144:147], v[172:175]\n"
    "ds_write_addtid_b32 v206 offset:3584\n"
    "v_cvt_pk_bf16_f32 v215, v218, v219\n"
    "v_mfma_f32_16x16x32_bf16 v[172:175], a[180:183], a[148:151], v[172:175]\n"
    "ds_read_b128 a[168:171], v240 offset:22528\n"
    "v_mfma_f32_16x16x32_bf16 v[164:167], a[180:183], a[116:119], v[164:167]\n"
    "s_waitcnt lgkmcnt(6)\n"
    "ds_write_addtid_b32 v207 offset:3840\n"
    "v_mad_u32_u16 v234, v228, s90, v239 op_sel:[0,0,0,0]\n"
    "v_mfma_f32_16x16x32_bf16 v[164:167], a[176:179], a[112:115], v[164:167]\n"
    "buffer_load_dwordx4 a[192:195], v234, s[20:23], 0 offen sc1\n"
    "v_mad_u32_u16 v235, v228, s90, v239 op_sel:[1,0,0,0]\n"
    "v_mfma_f32_16x16x32_bf16 v[156:159], a[176:179], a[80:83], v[156:159]\n"
    "ds_write_addtid_b32 v214 offset:4608\n"
    "v_add_u32_e32 v251, 160, v251\n"
    "v_mfma_f32_16x16x32_bf16 v[156:159], a[180:183], a[84:87], v[156:159]\n"
    "ds_write_addtid_b32 v215 offset:4864\n"
    "v_mfma_f32_16x16x32_bf16 v[148:151], a[180:183], a[52:55], v[148:151]\n"
    "buffer_load_dwordx4 a[196:199], v235, s[20:23], 0 offen sc1\n"
    "v_mfma_f32_16x16x32_bf16 v[148:151], a[176:179], a[48:51], v[148:151]\n"
    "v_mfma_f32_16x16x32_bf16 v[140:143], a[176:179], a[16:19], v[140:143]\n"
    "s_waitcnt lgkmcnt(0)\n"
    "v_mfma_f32_16x16x32_bf16 v[140:143], a[180:183], a[20:23], v[140:143]\n"
    "s_barrier\n"
    "v_mfma_f32_16x16x32_bf16 v[140:143], a[184:187], a[24:27], v[140:143]\n"
    "s_add_u32 m0, s43, 0\n"
    "ds_read_b128 a[172:175], v241 offset:22528\n"
    "v_mfma_f32_16x16x32_bf16 v[140:143], a[188:191], a[28:31], v[140:143]\n"
    "ds_read2st64_b32 v[184:185], v249 offset0:0 offset1:1\n"
    "v_mfma_f32_16x16x32_bf16 v[148:151], a[188:191], a[60:63], v[148:151]\n"
    "buffer_load_dwordx4 v248, s[8:11], s40 offen offset:0 sc1 lds\n"
    "v_mfma_f32_16x16x32_bf16 v[148:151], a[184:187], a[56:59], v[148:151]\n"
    "ds_read_b128 a[176:179], v242 offset:18432\n"
    "v_mfma_f32_16x16x32_bf16 v[156:159], a[184:187], a[88:91], v[156:159]\n"
    "v_mfma_f32_16x16x32_bf16 v[156:159], a[188:191], a[92:95], v[156:159]\n"
    "v_mfma_f32_16x16x32_bf16 v[164:167], a[188:191], a[124:127], v[164:167]\n"
    "buffer_load_dwordx4 v248, s[8:11], s40 offen offset:1024 sc1 lds\n"
    "v_mfma_f32_16x16x32_bf16 v[164:167], a[184:187], a[120:123], v[164:167]\n"
    "ds_read2st64_b32 v[186:187], v249 offset0:2 offset1:3\n"
    "v_mfma_f32_16x16x32_bf16 v[172:175], a[184:187], a[152:155], v[172:175]\n"
    "v_mfma_f32_16x16x32_bf16 v[172:175], a[188:191], a[156:159], v[172:175]\n"
    "ds_read_b128 a[180:183], v243 offset:18432\n"
    "v_mfma_f32_16x16x32_bf16 v[176:179], a[160:163], a[128:131], v[220:223]\n"
    "buffer_load_dwordx4 v248, s[8:11], s40 offen offset:2048 sc1 lds\n"
    "v_mfma_f32_16x16x32_bf16 v[176:179], a[164:167], a[132:135], v[176:179]\n"
    "ds_read2st64_b32 v[192:193], v249 offset0:4 offset1:5\n"
    "v_mfma_f32_16x16x32_bf16 v[168:171], a[164:167], a[100:103], v[220:223]\n"
    "v_exp_f32_e32 v140, v140\n"
    "v_mfma_f32_16x16x32_bf16 v[168:171], a[160:163], a[96:99], v[168:171]\n"
    "ds_read_b128 a[184:187], v242 offset:22528\n"
    "v_mfma_f32_16x16x32_bf16 v[160:163], a[160:163], a[64:67], v[220:223]\n"
    "buffer_load_dwordx4 v248, s[8:11], s40 offen offset:3072 sc1 lds\n"
    "v_mfma_f32_16x16x32_bf16 v[160:163], a[164:167], a[68:71], v[160:163]\n"
    "s_add_u32 m0, s43, 4096\n"
    "ds_read2st64_b32 v[194:195], v249 offset0:6 offset1:7\n"
    "v_mfma_f32_16x16x32_bf16 v[152:155], a[164:167], a[36:39], v[220:223]\n"
    "s_add_u32 s40, s40, 0x8000\n"
    "ds_read2st64_b32 v[200:201], v249 offset0:8 offset1:9\n"
    "v_mfma_f32_16x16x32_bf16 v[152:155], a[160:163], a[32:35], v[152:155]\n"
    "ds_read2st64_b32 v[202:203], v249 offset0:10 offset1:11\n"
    "v_mfma_f32_16x16x32_bf16 v[144:147], a[160:163], a[0:3], v[220:223]\n"
    "buffer_load_dwordx4 v248, s[8:11], s41 offen offset:0 sc1 lds\n"
    "v_mfma_f32_16x16x32_bf16 v[144:147], a[164:167], a[4:7], v[144:147]\n"
    "s_waitcnt vmcnt(17)\n"
    "ds_read_b128 a[188:191], v243 offset:22528\n"
    "v_smfmac_f32_16x16x64_bf16 v[224:227], a[232:235], a[200:207], v238\n"
    "s_waitcnt lgkmcnt(10)\n"
    "v_mad_u32_u16 v234, v229, s90, v239 op_sel:[0,0,0,0]\n"
    "v_mfma_f32_16x16x32_bf16 v[144:147], a[168:171], a[8:11], v[144:147]\n"
    "s_bitcmp1_b32 s86, 16\n"
    "buffer_load_dwordx4 a[200:203], v234, s[20:23], 0 offen sc1\n"
    "v_mad_u32_u16 v235, v229, s90, v239 op_sel:[1,0,0,0]\n"
    "v_mfma_f32_16x16x32_bf16 v[144:147], a[172:175], a[12:15], v[144:147]\n"
    "s_cselect_b64 exec, -1, 0\n"
    "v_cvt_pk_bf16_f32 v236, v224, v225\n"
    "v_cvt_pk_bf16_f32 v237, v226, v227\n"
    "v_mfma_f32_16x16x32_bf16 v[152:155], a[172:175], a[44:47], v[152:155]\n"
    "s_cselect_b32 s88, 512, 0\n"
    "v_mov_b64_e32 v[224:225], 0\n"
    "v_mov_b64_e32 v[226:227], 0\n"
    "v_mfma_f32_16x16x32_bf16 v[152:155], a[168:171], a[40:43], v[152:155]\n"
    "buffer_store_dwordx2 v[236:237], v252, s[8:11], s87 offen sc1\n"
    "v_mfma_f32_16x16x32_bf16 v[160:163], a[168:171], a[72:75], v[160:163]\n"
    "s_mov_b64 exec, -1\n"
    "ds_read_b64_tr_b16 a[160:161], v244 offset:8192\n"
    "v_mfma_f32_16x16x32_bf16 v[160:163], a[172:175], a[76:79], v[160:163]\n"
    "s_add_u32 s87, s87, s88\n"
    "ds_read_b64_tr_b16 a[162:163], v244 offset:10240\n"
    "v_mfma_f32_16x16x32_bf16 v[168:171], a[172:175], a[108:111], v[168:171]\n"
    "buffer_load_dwordx4 v248, s[8:11], s41 offen offset:1024 sc1 lds\n"
    "v_mfma_f32_16x16x32_bf16 v[168:171], a[168:171], a[104:107], v[168:171]\n"
    "ds_read_b64_tr_b16 a[164:165], v244 offset:40960\n"
    "v_mfma_f32_16x16x32_bf16 v[176:179], a[168:171], a[136:139], v[176:179]\n"
    "ds_read2st64_b32 v[208:209], v249 offset0:12 offset1:13\n"
    "v_mfma_f32_16x16x32_bf16 v[176:179], a[172:175], a[140:143], v[176:179]\n"
    "s_waitcnt lgkmcnt(10)\n"
    "ds_read2st64_b32 v[210:211], v249 offset0:14 offset1:15\n"
    "v_mfma_f32_16x16x32_bf16 v[176:179], a[176:179], a[144:147], v[176:179]\n"
    "buffer_load_dwordx4 v248, s[8:11], s41 offen offset:2048 sc1 lds\n"
    "v_mfma_f32_16x16x32_bf16 v[176:179], a[180:183], a[148:151], v[176:179]\n"
    "ds_read_b64_tr_b16 a[166:167], v244 offset:43008\n"
    "v_mfma_f32_16x16x32_bf16 v[168:171], a[180:183], a[116:119], v[168:171]\n"
    "ds_read_b64_tr_b16 a[168:169], v245 offset:8192\n"
    "v_mfma_f32_16x16x32_bf16 v[168:171], a[176:179], a[112:115], v[168:171]\n"
    "v_exp_f32_e32 v141, v141\n"
    "v_mfma_f32_16x16x32_bf16 v[160:163], a[176:179], a[80:83], v[160:163]\n"
    "buffer_load_dwordx4 v248, s[8:11], s41 offen offset:3072 sc1 lds\n"
    "v_mfma_f32_16x16x32_bf16 v[160:163], a[180:183], a[84:87], v[160:163]\n"
    "ds_read_b64_tr_b16 a[170:171], v245 offset:10240\n"
    "v_mfma_f32_16x16x32_bf16 v[152:155], a[180:183], a[52:55], v[152:155]\n"
    "s_add_u32 s41, s41, 0x8000\n"
    "ds_read2st64_b32 v[216:217], v249 offset0:16 offset1:17\n"
    "v_mfma_f32_16x16x32_bf16 v[152:155], a[176:179], a[48:51], v[152:155]\n"
    "buffer_load_dwordx4 a[204:207], v235, s[20:23], 0 offen sc1\n"
    "v_mfma_f32_16x16x32_bf16 v[144:147], a[176:179], a[16:19], v[144:147]\n"
    "ds_read_b64_tr_b16 a[172:173], v245 offset:40960\n"
    "v_mfma_f32_16x16x32_bf16 v[144:147], a[180:183], a[20:23], v[144:147]\n"
    "s_waitcnt lgkmcnt(10)\n"
    "ds_read2st64_b32 v[218:219], v249 offset0:18 offset1:19\n"
    "v_mfma_f32_16x16x32_bf16 v[144:147], a[184:187], a[24:27], v[144:147]\n"
    "ds_read_b64_tr_b16 a[176:177], v246 offset:8192\n"
    "v_mfma_f32_16x16x32_bf16 v[144:147], a[188:191], a[28:31], v[144:147]\n"
    "ds_read_b64_tr_b16 a[174:175], v245 offset:43008\n"
    "v_mfma_f32_16x16x32_bf16 v[152:155], a[188:191], a[60:63], v[152:155]\n"
    "v_exp_f32_e32 v142, v142\n"
    "v_mfma_f32_16x16x32_bf16 v[152:155], a[184:187], a[56:59], v[152:155]\n"
    "v_exp_f32_e32 v143, v143\n"
    "v_mfma_f32_16x16x32_bf16 v[160:163], a[184:187], a[88:91], v[160:163]\n"
    "ds_read_b64_tr_b16 a[178:179], v246 offset:10240\n"
    "v_cvt_pk_bf16_f32 v140, v140, v141\n"
    "v_mfma_f32_16x16x32_bf16 v[160:163], a[188:191], a[92:95], v[160:163]\n"
    "v_exp_f32_e32 v148, v148\n"
    "v_mfma_f32_16x16x32_bf16 v[168:171], a[188:191], a[124:127], v[168:171]\n"
    "v_exp_f32_e32 v149, v149\n"
    "v_mfma_f32_16x16x32_bf16 v[168:171], a[184:187], a[120:123], v[168:171]\n"
    "v_cvt_pk_bf16_f32 v141, v142, v143\n"
    "v_mfma_f32_16x16x32_bf16 v[176:179], a[184:187], a[152:155], v[176:179]\n"
    "ds_read_b64_tr_b16 a[180:181], v246 offset:40960\n"
    "v_mfma_f32_16x16x32_bf16 v[176:179], a[188:191], a[156:159], v[176:179]\n"
    "s_waitcnt vmcnt(20)\n"
    "ds_read_b64_tr_b16 a[182:183], v246 offset:43008\n"
    "v_smfmac_f32_16x16x64_bf16 v[224:227], a[232:235], a[208:215], v238\n"
    "s_waitcnt lgkmcnt(5)\n"
    "v_mad_u32_u16 v234, v230, s90, v239 op_sel:[0,0,0,0]\n"
    "v_mfma_f32_16x16x32_bf16 a[236:239], a[160:163], v[180:183], a[236:239]\n"
    "s_bitcmp1_b32 s86, 17\n"
    "buffer_load_dwordx4 a[208:211], v234, s[20:23], 0 offen sc1\n"
    "v_mad_u32_u16 v235, v230, s90, v239 op_sel:[1,0,0,0]\n"
    "v_mfma_f32_16x16x32_bf16 a[236:239], a[164:167], v[184:187], a[236:239]\n"
    "s_cselect_b64 exec, -1, 0\n"
    "v_cvt_pk_bf16_f32 v236, v224, v225\n"
    "v_cvt_pk_bf16_f32 v237, v226, v227\n"
    "v_mfma_f32_16x16x32_bf16 a[240:243], a[164:167], v[192:195], a[240:243]\n"
    "s_cselect_b32 s88, 512, 0\n"
    "v_mov_b64_e32 v[224:225], 0\n"
    "v_mov_b64_e32 v[226:227], 0\n"
    "v_mfma_f32_16x16x32_bf16 a[240:243], a[160:163], v[188:191], a[240:243]\n"
    "buffer_store_dwordx2 v[236:237], v252, s[8:11], s87 offen sc1\n"
    "v_mfma_f32_16x16x32_bf16 a[244:247], a[160:163], v[196:199], a[244:247]\n"
    "s_mov_b64 exec, -1\n"
    "ds_read_b64_tr_b16 a[184:185], v247 offset:8192\n"
    "v_mfma_f32_16x16x32_bf16 a[244:247], a[164:167], v[200:203], a[244:247]\n"
    "s_add_u32 s87, s87, s88\n"
    "ds_read_b64_tr_b16 a[186:187], v247 offset:10240\n"
    "v_mfma_f32_16x16x32_bf16 a[248:251], a[164:167], v[208:211], a[248:251]\n"
    "buffer_load_dwordx4 a[212:215], v235, s[20:23], 0 offen sc1\n"
    "v_mfma_f32_16x16x32_bf16 a[248:251], a[160:163], v[204:207], a[248:251]\n"
    "ds_read_b64_tr_b16 a[188:189], v247 offset:40960\n"
    "v_mfma_f32_16x16x32_bf16 a[252:255], a[160:163], v[212:215], a[252:255]\n"
    "v_exp_f32_e32 v150, v150\n"
    "v_mfma_f32_16x16x32_bf16 a[252:255], a[164:167], v[216:219], a[252:255]\n"
    "s_waitcnt lgkmcnt(6)\n"
    "v_exp_f32_e32 v151, v151\n"
    "v_mfma_f32_16x16x32_bf16 v[16:19], a[172:175], v[216:219], v[16:19]\n"
    "ds_read_b64_tr_b16 a[190:191], v247 offset:43008\n"
    "v_cvt_pk_bf16_f32 v148, v148, v149\n"
    "v_mfma_f32_16x16x32_bf16 v[16:19], a[168:171], v[212:215], v[16:19]\n"
    "ds_read_b64_tr_b16 a[160:161], v244 offset:12288\n"
    "v_mfma_f32_16x16x32_bf16 v[12:15], a[168:171], v[204:207], v[12:15]\n"
    "v_exp_f32_e32 v156, v156\n"
    "v_mfma_f32_16x16x32_bf16 v[12:15], a[172:175], v[208:211], v[12:15]\n"
    "v_exp_f32_e32 v157, v157\n"
    "v_mfma_f32_16x16x32_bf16 v[8:11], a[172:175], v[200:203], v[8:11]\n"
    "ds_read_b64_tr_b16 a[162:163], v244 offset:14336\n"
    "v_cvt_pk_bf16_f32 v149, v150, v151\n"
    "v_mfma_f32_16x16x32_bf16 v[8:11], a[168:171], v[196:199], v[8:11]\n"
    "v_exp_f32_e32 v158, v158\n"
    "v_mfma_f32_16x16x32_bf16 v[4:7], a[168:171], v[188:191], v[4:7]\n"
    "v_exp_f32_e32 v159, v159\n"
    "v_mfma_f32_16x16x32_bf16 v[4:7], a[172:175], v[192:195], v[4:7]\n"
    "v_cvt_pk_bf16_f32 v156, v156, v157\n"
    "v_mfma_f32_16x16x32_bf16 v[0:3], a[172:175], v[184:187], v[0:3]\n"
    "ds_read_b64_tr_b16 a[164:165], v244 offset:45056\n"
    "v_mfma_f32_16x16x32_bf16 v[0:3], a[168:171], v[180:183], v[0:3]\n"
    "s_waitcnt lgkmcnt(7)\n"
    "v_exp_f32_e32 v164, v164\n"
    "v_mfma_f32_16x16x32_bf16 v[20:23], a[176:179], v[180:183], v[20:23]\n"
    "ds_read_b64_tr_b16 a[168:169], v245 offset:12288\n"
    "v_mfma_f32_16x16x32_bf16 v[20:23], a[180:183], v[184:187], v[20:23]\n"
    "ds_read_b64_tr_b16 a[166:167], v244 offset:47104\n"
    "v_mfma_f32_16x16x32_bf16 v[24:27], a[180:183], v[192:195], v[24:27]\n"
    "v_exp_f32_e32 v165, v165\n"
    "v_mfma_f32_16x16x32_bf16 v[24:27], a[176:179], v[188:191], v[24:27]\n"
    "v_cvt_pk_bf16_f32 v157, v158, v159\n"
    "v_mfma_f32_16x16x32_bf16 v[28:31], a[176:179], v[196:199], v[28:31]\n"
    "ds_read_b64_tr_b16 a[170:171], v245 offset:14336\n"
    "v_mfma_f32_16x16x32_bf16 v[28:31], a[180:183], v[200:203], v[28:31]\n"
    "v_exp_f32_e32 v166, v166\n"
    "v_mfma_f32_16x16x32_bf16 v[32:35], a[180:183], v[208:211], v[32:35]\n"
    "v_exp_f32_e32 v167, v167\n"
    "v_mfma_f32_16x16x32_bf16 v[32:35], a[176:179], v[204:207], v[32:35]\n"
    "v_cvt_pk_bf16_f32 v164, v164, v165\n"
    "v_mfma_f32_16x16x32_bf16 v[36:39], a[176:179], v[212:215], v[36:39]\n"
    "ds_read_b64_tr_b16 a[172:173], v245 offset:45056\n"
    "v_mfma_f32_16x16x32_bf16 v[36:39], a[180:183], v[216:219], v[36:39]\n"
    "s_waitcnt vmcnt(20)\n"
    "ds_read_b64_tr_b16 a[174:175], v245 offset:47104\n"
    "v_smfmac_f32_16x16x64_bf16 v[224:227], a[232:235], a[216:223], v238\n"
    "s_waitcnt lgkmcnt(8)\n"
    "v_mad_u32_u16 v234, v231, s90, v239 op_sel:[0,0,0,0]\n"
    "v_mfma_f32_16x16x32_bf16 v[56:59], a[188:191], v[216:219], v[56:59]\n"
    "s_bitcmp1_b32 s86, 18\n"
    "buffer_load_dwordx4 a[216:219], v234, s[20:23], 0 offen sc1\n"
    "v_mad_u32_u16 v235, v231, s90, v239 op_sel:[1,0,0,0]\n"
    "v_mfma_f32_16x16x32_bf16 v[56:59], a[184:187], v[212:215], v[56:59]\n"
    "s_cselect_b64 exec, -1, 0\n"
    "v_cvt_pk_bf16_f32 v236, v224, v225\n"
    "v_cvt_pk_bf16_f32 v237, v226, v227\n"
    "v_mfma_f32_16x16x32_bf16 v[52:55], a[184:187], v[204:207], v[52:55]\n"
    "s_cselect_b32 s88, 512, 0\n"
    "v_mov_b64_e32 v[224:225], 0\n"
    "v_mov_b64_e32 v[226:227], 0\n"
    "v_mfma_f32_16x16x32_bf16 v[52:55], a[188:191], v[208:211], v[52:55]\n"
    "buffer_store_dwordx2 v[236:237], v252, s[8:11], s87 offen sc1\n"
    "v_mfma_f32_16x16x32_bf16 v[48:51], a[188:191], v[200:203], v[48:51]\n"
    "s_mov_b64 exec, -1\n"
    "ds_read_b64_tr_b16 a[176:177], v246 offset:12288\n"
    "v_mfma_f32_16x16x32_bf16 v[48:51], a[184:187], v[196:199], v[48:51]\n"
    "s_add_u32 s87, s87, s88\n"
    "ds_read_b64_tr_b16 a[178:179], v246 offset:14336\n"
    "v_mfma_f32_16x16x32_bf16 v[44:47], a[184:187], v[188:191], v[44:47]\n"
    "buffer_load_dwordx4 a[220:223], v235, s[20:23], 0 offen sc1\n"
    "v_mfma_f32_16x16x32_bf16 v[44:47], a[188:191], v[192:195], v[44:47]\n"
    "ds_read_b64_tr_b16 a[180:181], v246 offset:45056\n"
    "v_mfma_f32_16x16x32_bf16 v[40:43], a[188:191], v[184:187], v[40:43]\n"
    "v_exp_f32_e32 v172, v172\n"
    "v_mfma_f32_16x16x32_bf16 v[40:43], a[184:187], v[180:183], v[40:43]\n"
    "s_waitcnt lgkmcnt(6)\n"
    "v_exp_f32_e32 v173, v173\n"
    "v_mfma_f32_16x16x32_bf16 v[60:63], a[160:163], v[180:183], v[60:63]\n"
    "ds_read_b64_tr_b16 a[182:183], v246 offset:47104\n"
    "v_cvt_pk_bf16_f32 v165, v166, v167\n"
    "v_mfma_f32_16x16x32_bf16 v[60:63], a[164:167], v[184:187], v[60:63]\n"
    "ds_read_b64_tr_b16 a[184:185], v247 offset:12288\n"
    "v_mfma_f32_16x16x32_bf16 v[64:67], a[164:167], v[192:195], v[64:67]\n"
    "ds_read_b64_tr_b16 a[186:187], v247 offset:14336\n"
    "v_mfma_f32_16x16x32_bf16 v[64:67], a[160:163], v[188:191], v[64:67]\n"
    "v_exp_f32_e32 v174, v174\n"
    "v_mfma_f32_16x16x32_bf16 v[68:71], a[160:163], v[196:199], v[68:71]\n"
    "v_exp_f32_e32 v175, v175\n"
    "v_mfma_f32_16x16x32_bf16 v[68:71], a[164:167], v[200:203], v[68:71]\n"
    "ds_read_b64_tr_b16 a[188:189], v247 offset:45056\n"
    "v_cvt_pk_bf16_f32 v172, v172, v173\n"
    "v_mfma_f32_16x16x32_bf16 v[72:75], a[164:167], v[208:211], v[72:75]\n"
    "ds_read_b64_tr_b16 a[190:191], v247 offset:47104\n"
    "v_cvt_pk_bf16_f32 v173, v174, v175\n"
    "v_mfma_f32_16x16x32_bf16 v[72:75], a[160:163], v[204:207], v[72:75]\n"
    "s_waitcnt vmcnt(30)\n"
    "v_mfma_f32_16x16x32_bf16 v[76:79], a[160:163], v[212:215], v[76:79]\n"
    "s_barrier\n"
    "v_mfma_f32_16x16x32_bf16 v[76:79], a[164:167], v[216:219], v[76:79]\n"
    "s_waitcnt lgkmcnt(8)\n"
    "s_mov_b32 m0, s42\n"
    "v_mfma_f32_16x16x32_bf16 v[96:99], a[172:175], v[216:219], v[96:99]\n"
    "v_exp_f32_e32 v144, v144\n"
    "v_mfma_f32_16x16x32_bf16 v[96:99], a[168:171], v[212:215], v[96:99]\n"
    "v_exp_f32_e32 v145, v145\n"
    "v_mfma_f32_16x16x32_bf16 v[92:95], a[168:171], v[204:207], v[92:95]\n"
    "v_exp_f32_e32 v146, v146\n"
    "v_mfma_f32_16x16x32_bf16 v[92:95], a[172:175], v[208:211], v[92:95]\n"
    "ds_read_b128 a[160:163], v240 offset:24576\n"
    "ds_write_addtid_b32 v140 offset:0\n"
    "v_mfma_f32_16x16x32_bf16 v[88:91], a[172:175], v[200:203], v[88:91]\n"
    "v_exp_f32_e32 v147, v147\n"
    "v_mfma_f32_16x16x32_bf16 v[88:91], a[168:171], v[196:199], v[88:91]\n"
    "ds_write_addtid_b32 v141 offset:256\n"
    "v_cvt_pk_bf16_f32 v142, v144, v145\n"
    "v_mfma_f32_16x16x32_bf16 v[84:87], a[168:171], v[188:191], v[84:87]\n"
    "v_exp_f32_e32 v152, v152\n"
    "v_mfma_f32_16x16x32_bf16 v[84:87], a[172:175], v[192:195], v[84:87]\n"
    "v_exp_f32_e32 v153, v153\n"
    "v_mfma_f32_16x16x32_bf16 v[80:83], a[172:175], v[184:187], v[80:83]\n"
    "ds_write_addtid_b32 v148 offset:1024\n"
    "v_cvt_pk_bf16_f32 v143, v146, v147\n"
    "v_mfma_f32_16x16x32_bf16 v[80:83], a[168:171], v[180:183], v[80:83]\n"
    "s_waitcnt vmcnt(20)\n"
    "ds_read_b128 a[164:167], v241 offset:24576\n"
    "ds_write_addtid_b32 v149 offset:1280\n"
    "v_smfmac_f32_16x16x64_bf16 v[224:227], a[232:235], a[224:231], v238\n"
    "s_waitcnt lgkmcnt(10)\n"
    "ds_write_addtid_b32 v156 offset:2048\n"
    "v_mad_u32_u16 v234, v232, s90, v239 op_sel:[0,0,0,0]\n"
    "v_mfma_f32_16x16x32_bf16 v[100:103], a[176:179], v[180:183], v[100:103]\n"
    "s_bitcmp1_b32 s86, 19\n"
    "buffer_load_dwordx4 a[224:227], v234, s[20:23], 0 offen sc1\n"
    "v_mad_u32_u16 v235, v232, s90, v239 op_sel:[1,0,0,0]\n"
    "v_mfma_f32_16x16x32_bf16 v[100:103], a[180:183], v[184:187], v[100:103]\n"
    "s_cselect_b64 exec, -1, 0\n"
    "v_cvt_pk_bf16_f32 v236, v224, v225\n"
    "v_cvt_pk_bf16_f32 v237, v226, v227\n"
    "v_mfma_f32_16x16x32_bf16 v[104:107], a[180:183], v[192:195], v[104:107]\n"
    "s_cselect_b32 s88, 512, 0\n"
    "v_mov_b64_e32 v[224:225], 0\n"
    "v_mov_b64_e32 v[226:227], 0\n"
    "v_mfma_f32_16x16x32_bf16 v[104:107], a[176:179], v[188:191], v[104:107]\n"
    "buffer_store_dwordx2 v[236:237], v252, s[8:11], s87 offen sc1\n"
    "v_mfma_f32_16x16x32_bf16 v[108:111], a[176:179], v[196:199], v[108:111]\n"
    "s_mov_b64 exec, -1\n"
    "ds_read_b128 a[168:171], v240 offset:28672\n"
    "ds_write_addtid_b32 v157 offset:2304\n"
    "v_mfma_f32_16x16x32_bf16 v[108:111], a[180:183], v[200:203], v[108:111]\n"
    "s_add_u32 s87, s87, s88\n"
    "v_exp_f32_e32 v154, v154\n"
    "v_mfma_f32_16x16x32_bf16 v[112:115], a[180:183], v[208:211], v[112:115]\n"
    "s_add_u32 s89, s89, 1\n"
    "buffer_load_dwordx4 a[228:231], v235, s[20:23], 0 offen sc1\n"
    "ds_write_addtid_b32 v164 offset:3072\n"
    "v_mfma_f32_16x16x32_bf16 v[112:115], a[176:179], v[204:207], v[112:115]\n"
    "ds_write_addtid_b32 v165 offset:3328\n"
    "v_readlane_b32 s86, v254, s89\n"
    "v_mfma_f32_16x16x32_bf16 v[116:119], a[176:179], v[212:215], v[116:119]\n"
    "v_exp_f32_e32 v155, v155\n"
    "v_mfma_f32_16x16x32_bf16 v[116:119], a[180:183], v[216:219], v[116:119]\n"
    "s_waitcnt lgkmcnt(11)\n"
    "ds_read_b128 a[172:175], v241 offset:28672\n"
    "v_cvt_pk_bf16_f32 v150, v152, v153\n"
    "v_mfma_f32_16x16x32_bf16 v[136:139], a[188:191], v[216:219], v[136:139]\n"
    "v_exp_f32_e32 v160, v160\n"
    "v_mfma_f32_16x16x32_bf16 v[136:139], a[184:187], v[212:215], v[136:139]\n"
    "v_exp_f32_e32 v161, v161\n"
    "v_mfma_f32_16x16x32_bf16 v[132:135], a[184:187], v[204:207], v[132:135]\n"
    "ds_read_b128 a[176:179], v242 offset:24576\n"
    "v_cvt_pk_bf16_f32 v151, v154, v155\n"
    "v_mfma_f32_16x16x32_bf16 v[132:135], a[188:191], v[208:211], v[132:135]\n"
    "v_exp_f32_e32 v162, v162\n"
    "v_mfma_f32_16x16x32_bf16 v[128:131], a[188:191], v[200:203], v[128:131]\n"
    "v_exp_f32_e32 v163, v163\n"
    "v_mfma_f32_16x16x32_bf16 v[128:131], a[184:187], v[196:199], v[128:131]\n"
    "ds_write_addtid_b32 v172 offset:4096\n"
    "v_cvt_pk_bf16_f32 v158, v160, v161\n"
    "v_mfma_f32_16x16x32_bf16 v[124:127], a[184:187], v[188:191], v[124:127]\n"
    "v_exp_f32_e32 v168, v168\n"
    "v_mfma_f32_16x16x32_bf16 v[124:127], a[188:191], v[192:195], v[124:127]\n"
    "s_sub_u32 s44, s44, 1\n"
    "ds_read_b128 a[180:183], v243 offset:24576\n"
    "ds_write_addtid_b32 v173 offset:4352\n"
    "v_mfma_f32_16x16x32_bf16 v[120:123], a[188:191], v[184:187], v[120:123]\n"
    "s_cmp_lg_u32 s44, s80\n"
    "v_exp_f32_e32 v169, v169\n"
    "v_mfma_f32_16x16x32_bf16 v[120:123], a[184:187], v[180:183], v[120:123]\n"
    "s_waitcnt lgkmcnt(11)\n"
    "ds_write_addtid_b32 v142 offset:512\n"
    "v_cvt_pk_bf16_f32 v159, v162, v163\n"
    "s_cbranch_scc1 .Lat_gloop\n"
    ".Lat_g2:\n"
    "v_mov_b64_e32 v[224:225], v[220:221]\n"
    "v_mov_b64_e32 v[226:227], v[220:221]\n"
    "v_mov_b64_e32 v[228:229], v[220:221]\n"
    "v_mov_b64_e32 v[230:231], v[220:221]\n"
    "v_mov_b64_e32 v[232:233], v[220:221]\n"
    "v_mov_b64_e32 v[234:235], v[220:221]\n"
    "v_mov_b64_e32 v[236:237], v[220:221]\n"
    "v_mov_b64_e32 v[238:239], v[220:221]\n"
    "s_mov_b32 s80, 0\n"
    "s_waitcnt lgkmcnt(0)\n"
    "s_cmp_lg_u32 s44, 0\n"
    "s_cbranch_scc1 .Lat_loop\n"
    "s_branch .Lat_tail\n"
    ".Lat_wpoll:\n"
    "s_mov_b32 s53, 0x400000\n"
    ".Lat_wpoll_l:\n"
    "s_sleep 1\n"
    "buffer_load_dword v253, v255, s[8:11], s52 offen offset:0 sc1\n"
    "s_waitcnt vmcnt(0)\n"
    "v_cmp_ne_u32_e32 vcc, s39, v253\n"
    "s_sub_u32 s53, s53, 1\n"
    "s_cbranch_scc1 .Lat_wpoll_ret\n"
    "s_nop 2\n"
    "s_cbranch_vccnz .Lat_wpoll_l\n"
    "s_branch .Lat_wpoll_ret\n"
    ".Lat_glate:\n"
    "s_mov_b32 s53, 0x400000\n"
    ".Lat_glate_l:\n"
    "s_sleep 2\n"
    "buffer_load_dword v188, v189, s[8:11], s81 offen offset:0 sc1\n"
    "s_waitcnt vmcnt(0)\n"
    "v_cmp_ne_u32_e32 vcc, s39, v188\n"
    "s_sub_u32 s53, s53, 1\n"
    "s_cbranch_scc1 .Lat_glate_ret\n"
    "s_nop 2\n"
    "s_cbranch_vccnz .Lat_glate_l\n"
    "s_branch .Lat_glate_ret\n"
    ".Lat_k:\n"
    "s_mov_b32 s53, 0x400000\n"
    ".Lat_kl:\n"
    "buffer_load_dword v253, v238, s[8:11], s48 offen offset:0 sc1\n"
    "s_waitcnt vmcnt(0)\n"
    "v_subrev_u32_e32 v253, s39, v253\n"
    "v_subrev_u32_e32 v253, 0, v253\n"
    "v_cmp_le_u32_e32 vcc, 1, v253\n"
    "s_sub_u32 s53, s53, 1\n"
    "s_cbranch_scc1 .Lat_kr\n"
    "s_nop 2\n"
    "s_cbranch_vccnz .Lat_kl\n"
    "s_branch .Lat_kr\n"
    ".Lat_kx:\n"
    "s_branch .Lat_kxr\n"
    ".Lat_ppoll:\n"
    "s_mov_b32 s53, 0x400000\n"
    ".Lat_ppl:\n"
    "buffer_load_dword v253, v226, s[8:11], s61 offen offset:0 sc1\n"
    "s_waitcnt vmcnt(0)\n"
    "v_subrev_u32_e32 v253, s39, v253\n"
    "v_cmp_lt_u32_e32 vcc, 1, v253\n"
    "s_sub_u32 s53, s53, 1\n"
    "s_cbranch_scc1 .Lat_pok\n"
    "s_nop 2\n"
    "s_cbranch_vccnz .Lat_ppl\n"
    "s_branch .Lat_pok\n"
    ".Lat_bin:\n"
    "s_movk_i32 s45, -2560\n"
    "s_mov_b32 s48, 81920\n"
    "v_mov_b32_e32 v172, 1\n"
    "s_waitcnt vmcnt(0)\n"
    "v_mul_u32_u24_e32 v160, 0x199a, v144\n"
    "v_mul_u32_u24_e32 v161, 0x199a, v145\n"
    "v_mul_u32_u24_e32 v162, 0x199a, v146\n"
    "v_mul_u32_u24_e32 v163, 0x199a, v147\n"
    "v_mul_u32_u24_e32 v164, 0x199a, v148\n"
    "v_bfe_u32 v160, v160, 24, 2\n"
    "v_bfe_u32 v161, v161, 24, 2\n"
    "v_bfe_u32 v162, v162, 24, 2\n"
    "v_bfe_u32 v163, v163, 24, 2\n"
    "v_bfe_u32 v164, v164, 24, 2\n"
    "v_lshl_add_u32 v166, v160, 2, s48\n"
    "v_lshl_add_u32 v167, v161, 2, s48\n"
    "v_lshl_add_u32 v168, v162, 2, s48\n"
    "v_lshl_add_u32 v169, v163, 2, s48\n"
    "v_lshl_add_u32 v170, v164, 2, s48\n"
    "ds_add_rtn_u32 v166, v166, v172 offset:4096\n"
    "ds_add_rtn_u32 v167, v167, v172 offset:4096\n"
    "ds_add_rtn_u32 v168, v168, v172 offset:4096\n"
    "ds_add_rtn_u32 v169, v169, v172 offset:4096\n"
    "ds_add_rtn_u32 v170, v170, v172 offset:4096\n"
    "v_mad_i32_i24 v144, v160, s45, v144\n"
    "v_mad_i32_i24 v145, v161, s45, v145\n"
    "v_mad_i32_i24 v146, v162, s45, v146\n"
    "v_mad_i32_i24 v147, v163, s45, v147\n"
    "v_mad_i32_i24 v148, v164, s45, v148\n"
    "v_lshl_or_b32 v144, v144, 18, v152\n"
    "v_lshl_or_b32 v145, v145, 18, v153\n"
    "v_lshl_or_b32 v146, v146, 18, v154\n"
    "v_lshl_or_b32 v147, v147, 18, v155\n"
    "v_lshl_or_b32 v148, v148, 18, v156\n"
    "v_lshl_add_u32 v160, v160, 10, s48\n"
    "v_lshl_add_u32 v161, v161, 10, s48\n"
    "v_lshl_add_u32 v162, v162, 10, s48\n"
    "v_lshl_add_u32 v163, v163, 10, s48\n"
    "v_lshl_add_u32 v164, v164, 10, s48\n"
    "s_branch .Lat_bin_ret\n"
    ".Lat_bin2:\n"
    "s_waitcnt lgkmcnt(0)\n"
    "v_min_i32_e32 v166, 254, v166\n"
    "v_min_i32_e32 v167, 254, v167\n"
    "v_min_i32_e32 v168, 254, v168\n"
    "v_min_i32_e32 v169, 254, v169\n"
    "v_min_i32_e32 v170, 254, v170\n"
    "v_lshl_add_u32 v160, v166, 2, v160\n"
    "v_lshl_add_u32 v161, v167, 2, v161\n"
    "v_lshl_add_u32 v162, v168, 2, v162\n"
    "v_lshl_add_u32 v163, v169, 2, v163\n"
    "v_lshl_add_u32 v164, v170, 2, v164\n"
    "ds_write_b32 v160, v144 offset:4\n"
    "ds_write_b32 v161, v145 offset:4\n"
    "ds_write_b32 v162, v146 offset:4\n"
    "ds_write_b32 v163, v147 offset:4\n"
    "ds_write_b32 v164, v148 offset:4\n"
    "s_branch .Lat_bin2_ret\n"
    ".Lat_apoll:\n"
    "s_mov_b32 s53, 0x400000\n"
    ".Lat_apoll_l:\n"
    "s_sleep 1\n"
    "buffer_load_dword v101, v100, s[8:11], 0 offen offset:0 sc1\n"
    "s_waitcnt vmcnt(0)\n"
    "v_cmp_ne_u32_e32 vcc, s39, v101\n"
    "s_sub_u32 s53, s53, 1\n"
    "s_cbranch_scc1 .Lat_apoll_ret\n"
    "s_nop 2\n"
    "s_cbranch_vccnz .Lat_apoll_l\n"
    "s_branch .Lat_apoll_ret\n"
    ".Lat_h1poll:\n"
    "s_mov_b32 s53, 0x400000\n"
    ".Lat_h1poll_l:\n"
    "s_sleep 1\n"
    "buffer_load_dword v80, v83, s[8:11], s59 offen offset:0 sc1\n"
    "s_waitcnt vmcnt(0)\n"
    "v_cmp_ne_u32_e32 vcc, s39, v80\n"
    "s_sub_u32 s53, s53, 1\n"
    "s_cbranch_scc1 .Lat_h1poll_ret\n"
    "s_nop 2\n"
    "s_cbranch_vccnz .Lat_h1poll_l\n"
    "s_branch .Lat_h1poll_ret\n"
    ".Lat_qpoll:\n"
    "s_mov_b32 s53, 0x400000\n"
    ".Lat_qpoll_l:\n"
    "s_sleep 0\n"
    "buffer_load_dword v254, v253, s[8:11], s47 offen offset:0 sc1\n"
    "s_waitcnt vmcnt(0)\n"
    "v_cmp_ne_u32_e32 vcc, s39, v254\n"
    "s_sub_u32 s53, s53, 1\n"
    "s_cbranch_scc1 .Lat_qpoll_ret\n"
    "s_nop 2\n"
    "s_cbranch_vccnz .Lat_qpoll_l\n"
    "s_branch .Lat_qpoll_ret\n"
    ".Lat_hpoll:\n"
    "s_mov_b32 s53, 0x400000\n"
    ".Lat_hpoll_l:\n"
    "s_sleep 1\n"
    "buffer_load_dword v253, v250, s[8:11], s59 offen offset:0 sc1\n"
    "buffer_load_dword v254, v252, s[8:11], s59 offen offset:0 sc1\n"
    "s_waitcnt vmcnt(0)\n"
    "v_xor_b32_e32 v254, s39, v254\n"
    "v_xor_b32_e32 v253, s39, v253\n"
    "v_or_b32_e32 v253, v253, v254\n"
    "v_cmp_ne_u32_e32 vcc, 0, v253\n"
    "s_sub_u32 s53, s53, 1\n"
    "s_cbranch_scc1 .Lat_hpoll_ret\n"
    "s_nop 2\n"
    "s_cbranch_vccnz .Lat_hpoll_l\n"
    "s_branch .Lat_hpoll_ret\n"
    ".Lat_redo:\n"
    "s_waitcnt vmcnt(0) lgkmcnt(0)\n"
    "s_barrier\n"
    "s_mov_b32 s49, 1\n"
    "s_mov_b32 s80, 0\n"
    "s_mov_b32 s82, 2.0\n"
    "v_lshrrev_b32_e32 v224, 4, v250\n"
    "s_and_b32 s50, s93, 1\n"
    "s_lshr_b32 s51, s93, 1\n"
    "v_lshrrev_b32_e32 v225, 3, v224\n"
    "v_and_b32_e32 v226, 6, v225\n"
    "v_and_b32_e32 v227, 7, v224\n"
    "v_xor_b32_e32 v226, v226, v227\n"
    "v_lshlrev_b32_e32 v226, 4, v226\n"
    "v_and_b32_e32 v227, 4, v225\n"
    "v_add_u32_e32 v227, v227, v225\n"
    "v_lshl_add_u32 v251, v227, 9, v226\n"
    "v_add_u32_e32 v252, 0x1c00, v251\n"
    "v_and_b32_e32 v227, 1, v225\n"
    "v_lshl_add_u32 v248, v227, 9, v226\n"
    "v_lshrrev_b32_e32 v227, 1, v225\n"
    "v_lshl_add_u32 v248, v227, 12, v248\n"
    "s_mul_i32 s43, s50, 3\n"
    "s_xor_b32 s43, s43, s51\n"
    "s_lshl_b32 s43, s43, 15\n"
    "s_add_u32 s43, s43, 32768\n"
    "s_mul_i32 s42, s93, 5120\n"
    "s_lshl_b32 s47, s50, 8\n"
    "s_mul_i32 s40, s32, 1277952\n"
    "s_lshl_b32 s41, s51, 14\n"
    "s_add_u32 s40, s40, s41\n"
    "s_add_u32 s40, s40, s47\n"
    "s_add_u32 s41, s40, 128\n"
    "v_bfe_u32 v225, v224, 2, 3\n"
    "v_lshlrev_b32_e32 v244, 7, v225\n"
    "v_lshrrev_b32_e32 v226, 5, v224\n"
    "v_lshl_add_u32 v244, v226, 10, v244\n"
    "v_and_b32_e32 v225, 6, v225\n"
    "v_bfe_u32 v226, v224, 1, 1\n"
    "v_xor_b32_e32 v225, v225, v226\n"
    "v_lshl_add_u32 v244, v225, 4, v244\n"
    "v_and_b32_e32 v226, 1, v224\n"
    "v_lshl_add_u32 v244, v226, 3, v244\n"
    "s_lshl_b32 s47, s51, 16\n"
    "s_add_u32 s47, s47, 32768\n"
    "v_add_u32_e32 v244, s47, v244\n"
    "v_xor_b32_e32 v245, 32, v244\n"
    "v_xor_b32_e32 v246, 64, v244\n"
    "v_xor_b32_e32 v247, 0x60, v244\n"
    "v_and_b32_e32 v225, 7, v224\n"
    "v_lshlrev_b32_e32 v227, 7, v225\n"
    "v_bfe_u32 v226, v224, 3, 1\n"
    "v_lshl_add_u32 v227, v226, 10, v227\n"
    "v_and_b32_e32 v225, 6, v224\n"
    "v_lshrrev_b32_e32 v226, 4, v224\n"
    "v_xor_b32_e32 v225, v225, v226\n"
    "v_lshl_add_u32 v227, v225, 4, v227\n"
    "s_lshl_b32 s47, s51, 15\n"
    "s_add_u32 s48, s47, 32768\n"
    "s_sub_u32 s47, 131072, s47\n"
    "v_add_u32_e32 v240, s48, v227\n"
    "v_xor_b32_e32 v241, 64, v240\n"
    "v_add_u32_e32 v242, s47, v227\n"
    "v_xor_b32_e32 v243, 64, v242\n"
    "s_xor_b32 s47, s93, 2\n"
    "s_mul_i32 s47, s47, 5120\n"
    "v_lshl_add_u32 v249, v224, 2, s47\n"
    "v_and_b32_e32 v252, 0x30, v250\n"
    "v_add_u32_e32 v252, 0x400, v252\n"
    "s_mul_i32 s59, s32, 999\n"
    "s_andn2_b32 s59, s59, 63\n"
    "s_sub_u32 s48, 3968, s59\n"
    "v_cmp_le_u32_e32 vcc, 0x80, v250\n"
    "v_mov_b32_e32 v80, s48\n"
    "s_add_u32 s59, s59, 6076416\n"
    "v_cndmask_b32_e32 v83, v250, v80, vcc\n"
    "v_bfe_u32 v253, v250, 4, 2\n"
    "v_max_u32_e32 v253, 1, v253\n"
    "v_add_u32_e32 v253, s32, v253\n"
    "v_and_b32_e32 v253, 3, v253\n"
    "v_lshlrev_b32_e32 v253, 9, v253\n"
    "v_mov_b32_e32 v84, s39\n"
    "v_lshrrev_b32_e32 v86, 2, v250\n"
    "v_min_u32_e32 v86, 124, v86\n"
    "s_lshl2_add_u32 s58, s34, s93\n"
    "s_lshl_b32 s58, s58, 7\n"
    "s_add_u32 s58, s58, 5910528\n"
    "s_lshl_b32 s45, s50, 13\n"
    "v_add_u32_e32 v85, s45, v250\n"
    "s_lshl3_add_u32 s45, s45, 49152\n"
    "v_add_u32_e32 v255, s45, v250\n"
    "v_lshrrev_b32_e32 v105, 7, v250\n"
    "v_and_b32_e32 v104, 0x70, v251\n"
    "v_lshl_add_u32 v104, v105, 9, v104\n"
    "v_add_u32_e32 v105, 0xc00, v104\n"
    "v_add_u32_e32 v106, 0x1800, v104\n"
    "v_add_u32_e32 v107, 0x2400, v104\n"
    "s_lshl_b32 s57, s93, 7\n"
    "s_lshl4_add_u32 s56, s57, 24576\n"
    "s_add_u32 s57, s57, 5111808\n"
    "s_mul_i32 s45, s51, 32768\n"
    "s_add_u32 s45, s45, 8192\n"
    "v_subrev_u32_e32 v46, s45, v240\n"
    "v_subrev_u32_e32 v47, s45, v241\n"
    "s_mul_i32 s45, s51, 61440\n"
    "s_add_u32 s45, s45, 8192\n"
    "v_subrev_u32_e32 v4, s45, v244\n"
    "v_subrev_u32_e32 v5, s45, v245\n"
    "v_subrev_u32_e32 v6, s45, v246\n"
    "v_subrev_u32_e32 v7, s45, v247\n"
    "v_accvgpr_write_b32 a244, 0\n"
    "v_accvgpr_write_b32 a245, 0\n"
    "v_accvgpr_write_b32 a246, 0\n"
    "v_accvgpr_write_b32 a247, 0\n"
    "v_accvgpr_write_b32 a248, 0\n"
    "v_accvgpr_write_b32 a249, 0\n"
    "v_accvgpr_write_b32 a250, 0\n"
    "v_accvgpr_write_b32 a251, 0\n"
    "v_accvgpr_write_b32 a252, 0\n"
    "v_accvgpr_write_b32 a253, 0\n"
    "v_accvgpr_write_b32 a254, 0\n"
    "v_accvgpr_write_b32 a255, 0\n"
    "v_mov_b64_e32 v[8:9], 0\n"
    "v_mov_b64_e32 v[10:11], 0\n"
    "v_mov_b64_e32 v[12:13], 0\n"
    "v_mov_b64_e32 v[14:15], 0\n"
    "v_mov_b64_e32 v[16:17], 0\n"
    "v_mov_b64_e32 v[18:19], 0\n"
    "v_mov_b64_e32 v[28:29], 0\n"
    "v_mov_b64_e32 v[30:31], 0\n"
    "v_mov_b64_e32 v[32:33], 0\n"
    "v_mov_b64_e32 v[34:35], 0\n"
    "v_mov_b64_e32 v[36:37], 0\n"
    "v_mov_b64_e32 v[38:39], 0\n"
    "v_mov_b64_e32 v[48:49], 0\n"
    "v_mov_b64_e32 v[50:51], 0\n"
    "v_mov_b64_e32 v[52:53], 0\n"
    "v_mov_b64_e32 v[54:55], 0\n"
    "v_mov_b64_e32 v[56:57], 0\n"
    "v_mov_b64_e32 v[58:59], 0\n"
    "v_mov_b64_e32 v[68:69], 0\n"
    "v_mov_b64_e32 v[70:71], 0\n"
    "v_mov_b64_e32 v[72:73], 0\n"
    "v_mov_b64_e32 v[74:75], 0\n"
    "v_mov_b64_e32 v[76:77], 0\n"
    "v_mov_b64_e32 v[78:79], 0\n"
    "v_mov_b64_e32 v[88:89], 0\n"
    "v_mov_b64_e32 v[90:91], 0\n"
    "v_mov_b64_e32 v[92:93], 0\n"
    "v_mov_b64_e32 v[94:95], 0\n"
    "v_mov_b64_e32 v[96:97], 0\n"
    "v_mov_b64_e32 v[98:99], 0\n"
    "v_mov_b64_e32 v[108:109], 0\n"
    "v_mov_b64_e32 v[110:111], 0\n"
    "v_mov_b64_e32 v[112:113], 0\n"
    "v_mov_b64_e32 v[114:115], 0\n"
    "v_mov_b64_e32 v[116:117], 0\n"
    "v_mov_b64_e32 v[118:119], 0\n"
    "v_mov_b64_e32 v[128:129], 0\n"
    "v_mov_b64_e32 v[130:131], 0\n"
    "v_mov_b64_e32 v[132:133], 0\n"
    "v_mov_b64_e32 v[134:135], 0\n"
    "v_mov_b64_e32 v[136:137], 0\n"
    "v_mov_b64_e32 v[138:139], 0\n"
    "s_mov_b32 m0, s56\n"
    "s_nop 0\n"
    "buffer_load_dwordx4 v104, s[8:11], s57 offen offset:0 sc1 lds\n"
    "buffer_load_dwordx4 v105, s[8:11], s57 offen offset:1024 sc1 lds\n"
    "s_add_u32 m0, s43, 0\n"
    "s_nop 0\n"
    "buffer_load_dwordx4 v104, s[8:11], s40 offen offset:0 sc1 lds\n"
    "buffer_load_dwordx4 v105, s[8:11], s40 offen offset:1024 sc1 lds\n"
    "buffer_load_dwordx4 v106, s[8:11], s40 offen offset:2048 sc1 lds\n"
    "buffer_load_dwordx4 v107, s[8:11], s40 offen offset:3072 sc1 lds\n"
    "s_add_u32 s40, s40, 0x8000\n"
    "s_add_u32 m0, s43, 4096\n"
    "s_nop 0\n"
    "buffer_load_dwordx4 v104, s[8:11], s41 offen offset:0 sc1 lds\n"
    "buffer_load_dwordx4 v105, s[8:11], s41 offen offset:1024 sc1 lds\n"
    "buffer_load_dwordx4 v106, s[8:11], s41 offen offset:2048 sc1 lds\n"
    "buffer_load_dwordx4 v107, s[8:11], s41 offen offset:3072 sc1 lds\n"
    "s_add_u32 s41, s41, 0x8000\n"
    "s_add_u32 m0, s43, 8192\n"
    "s_nop 0\n"
    "buffer_load_dwordx4 v104, s[8:11], s40 offen offset:0 sc1 lds\n"
    "buffer_load_dwordx4 v105, s[8:11], s40 offen offset:1024 sc1 lds\n"
    "buffer_load_dwordx4 v106, s[8:11], s40 offen offset:2048 sc1 lds\n"
    "buffer_load_dwordx4 v107, s[8:11], s40 offen offset:3072 sc1 lds\n"
    "s_add_u32 s40, s40, 0x8000\n"
    "s_add_u32 m0, s43, 12288\n"
    "s_nop 0\n"
    "buffer_load_dwordx4 v104, s[8:11], s41 offen offset:0 sc1 lds\n"
    "buffer_load_dwordx4 v105, s[8:11], s41 offen offset:1024 sc1 lds\n"
    "buffer_load_dwordx4 v106, s[8:11], s41 offen offset:2048 sc1 lds\n"
    "buffer_load_dwordx4 v107, s[8:11], s41 offen offset:3072 sc1 lds\n"
    "s_add_u32 s41, s41, 0x8000\n"
    "s_waitcnt vmcnt(0)\n"
    "s_branch .Lat_p2\n"
    ".Lat_maxpass:\n"
    "s_waitcnt lgkmcnt(0)\n"
    "s_mul_i32 s48, s33, 81920\n"
    "s_add_u32 s48, s48, 11345920\n"
    "s_add_u32 s45, s32, 0\n"
    "s_and_b32 s45, s45, 3\n"
    "s_lshl2_add_u32 s45, s50, s45\n"
    "s_lshl_b32 s45, s45, 13\n"
    "s_add_u32 s45, s45, s48\n"
    "s_add_u32 s46, s45, 4096\n"
    "buffer_load_dwordx4 a[0:3], v250, s[8:11], s45 offen offset:0 sc1\n"
    "buffer_load_dwordx4 a[4:7], v250, s[8:11], s45 offen offset:1024 sc1\n"
    "buffer_load_dwordx4 a[8:11], v250, s[8:11], s45 offen offset:2048 sc1\n"
    "buffer_load_dwordx4 a[12:15], v250, s[8:11], s45 offen offset:3072 sc1\n"
    "buffer_load_dwordx4 a[16:19], v250, s[8:11], s46 offen offset:0 sc1\n"
    "buffer_load_dwordx4 a[20:23], v250, s[8:11], s46 offen offset:1024 sc1\n"
    "buffer_load_dwordx4 a[24:27], v250, s[8:11], s46 offen offset:2048 sc1\n"
    "buffer_load_dwordx4 a[28:31], v250, s[8:11], s46 offen offset:3072 sc1\n"
    "s_and_b32 s45, s32, 1\n"
    "s_xor_b32 s45, s45, s50\n"
    "s_add_u32 s45, s45, 8\n"
    "s_lshl_b32 s45, s45, 13\n"
    "s_add_u32 s45, s45, s48\n"
    "s_add_u32 s46, s45, 4096\n"
    "buffer_load_dwordx4 a[32:35], v250, s[8:11], s45 offen offset:0 sc1\n"
    "buffer_load_dwordx4 a[36:39], v250, s[8:11], s45 offen offset:1024 sc1\n"
    "buffer_load_dwordx4 a[40:43], v250, s[8:11], s45 offen offset:2048 sc1\n"
    "buffer_load_dwordx4 a[44:47], v250, s[8:11], s45 offen offset:3072 sc1\n"
    "buffer_load_dwordx4 a[48:51], v250, s[8:11], s46 offen offset:0 sc1\n"
    "buffer_load_dwordx4 a[52:55], v250, s[8:11], s46 offen offset:1024 sc1\n"
    "buffer_load_dwordx4 a[56:59], v250, s[8:11], s46 offen offset:2048 sc1\n"
    "buffer_load_dwordx4 a[60:63], v250, s[8:11], s46 offen offset:3072 sc1\n"
    "s_add_u32 s45, s32, 1\n"
    "s_and_b32 s45, s45, 3\n"
    "s_lshl2_add_u32 s45, s50, s45\n"
    "s_lshl_b32 s45, s45, 13\n"
    "s_add_u32 s45, s45, s48\n"
    "s_add_u32 s46, s45, 4096\n"
    "buffer_load_dwordx4 a[64:67], v250, s[8:11], s45 offen offset:0 sc1\n"
    "buffer_load_dwordx4 a[68:71], v250, s[8:11], s45 offen offset:1024 sc1\n"
    "buffer_load_dwordx4 a[72:75], v250, s[8:11], s45 offen offset:2048 sc1\n"
    "buffer_load_dwordx4 a[76:79], v250, s[8:11], s45 offen offset:3072 sc1\n"
    "buffer_load_dwordx4 a[80:83], v250, s[8:11], s46 offen offset:0 sc1\n"
    "buffer_load_dwordx4 a[84:87], v250, s[8:11], s46 offen offset:1024 sc1\n"
    "buffer_load_dwordx4 a[88:91], v250, s[8:11], s46 offen offset:2048 sc1\n"
    "buffer_load_dwordx4 a[92:95], v250, s[8:11], s46 offen offset:3072 sc1\n"
    "s_add_u32 s45, s32, 2\n"
    "s_and_b32 s45, s45, 3\n"
    "s_lshl2_add_u32 s45, s50, s45\n"
    "s_lshl_b32 s45, s45, 13\n"
    "s_add_u32 s45, s45, s48\n"
    "s_add_u32 s46, s45, 4096\n"
    "buffer_load_dwordx4 a[96:99], v250, s[8:11], s45 offen offset:0 sc1\n"
    "buffer_load_dwordx4 a[100:103], v250, s[8:11], s45 offen offset:1024 sc1\n"
    "buffer_load_dwordx4 a[104:107], v250, s[8:11], s45 offen offset:2048 sc1\n"
    "buffer_load_dwordx4 a[108:111], v250, s[8:11], s45 offen offset:3072 sc1\n"
    "buffer_load_dwordx4 a[112:115], v250, s[8:11], s46 offen offset:0 sc1\n"
    "buffer_load_dwordx4 a[116:119], v250, s[8:11], s46 offen offset:1024 sc1\n"
    "buffer_load_dwordx4 a[120:123], v250, s[8:11], s46 offen offset:2048 sc1\n"
    "buffer_load_dwordx4 a[124:127], v250, s[8:11], s46 offen offset:3072 sc1\n"
    "s_add_u32 s45, s32, 3\n"
    "s_and_b32 s45, s45, 3\n"
    "s_lshl2_add_u32 s45, s50, s45\n"
    "s_lshl_b32 s45, s45, 13\n"
    "s_add_u32 s45, s45, s48\n"
    "s_add_u32 s46, s45, 4096\n"
    "buffer_load_dwordx4 a[128:131], v250, s[8:11], s45 offen offset:0 sc1\n"
    "buffer_load_dwordx4 a[132:135], v250, s[8:11], s45 offen offset:1024 sc1\n"
    "buffer_load_dwordx4 a[136:139], v250, s[8:11], s45 offen offset:2048 sc1\n"
    "buffer_load_dwordx4 a[140:143], v250, s[8:11], s45 offen offset:3072 sc1\n"
    "buffer_load_dwordx4 a[144:147], v250, s[8:11], s46 offen offset:0 sc1\n"
    "buffer_load_dwordx4 a[148:151], v250, s[8:11], s46 offen offset:1024 sc1\n"
    "buffer_load_dwordx4 a[152:155], v250, s[8:11], s46 offen offset:2048 sc1\n"
    "buffer_load_dwordx4 a[156:159], v250, s[8:11], s46 offen offset:3072 sc1\n"
    "s_waitcnt vmcnt(0)\n"
    "v_mov_b32_e32 v200, 0xff800000\n"
    "v_mov_b32_e32 v201, 0xff800000\n"
    "v_mov_b32_e32 v202, 0xff800000\n"
    "v_mov_b32_e32 v203, 0xff800000\n"
    "v_mov_b32_e32 v204, 0xff800000\n"
    "s_lshl_b32 s45, s51, 14\n"
    "s_lshl_b32 s46, s50, 8\n"
    "s_add_u32 s45, s45, s46\n"
    "s_add_u32 s61, s46, 5103616\n"
    "s_movk_i32 s48, 157\n"
    ".Lat_mp_loop:\n"
    "s_min_u32 s45, s45, s61\n"
    "s_add_u32 s46, s45, 128\n"
    "s_add_u32 m0, s43, 16384\n"
    "s_nop 0\n"
    "buffer_load_dwordx4 v248, s[8:11], s45 offen offset:0 sc1 lds\n"
    "buffer_load_dwordx4 v248, s[8:11], s45 offen offset:1024 sc1 lds\n"
    "buffer_load_dwordx4 v248, s[8:11], s45 offen offset:2048 sc1 lds\n"
    "buffer_load_dwordx4 v248, s[8:11], s45 offen offset:3072 sc1 lds\n"
    "s_add_u32 m0, s43, 20480\n"
    "s_nop 0\n"
    "buffer_load_dwordx4 v248, s[8:11], s46 offen offset:0 sc1 lds\n"
    "buffer_load_dwordx4 v248, s[8:11], s46 offen offset:1024 sc1 lds\n"
    "buffer_load_dwordx4 v248, s[8:11], s46 offen offset:2048 sc1 lds\n"
    "buffer_load_dwordx4 v248, s[8:11], s46 offen offset:3072 sc1 lds\n"
    "s_waitcnt vmcnt(0)\n"
    "s_barrier\n"
    "ds_read_b128 a[160:163], v240 offset:16384\n"
    "ds_read_b128 a[164:167], v241 offset:16384\n"
    "ds_read_b128 a[168:171], v240 offset:20480\n"
    "ds_read_b128 a[172:175], v241 offset:20480\n"
    "ds_read_b128 a[176:179], v242 offset:16384\n"
    "ds_read_b128 a[180:183], v243 offset:16384\n"
    "ds_read_b128 a[184:187], v242 offset:20480\n"
    "ds_read_b128 a[188:191], v243 offset:20480\n"
    "s_waitcnt lgkmcnt(0)\n"
    "v_mfma_f32_16x16x32_bf16 v[180:183], a[160:163], a[0:3], 0\n"
    "v_mfma_f32_16x16x32_bf16 v[184:187], a[160:163], a[32:35], 0\n"
    "v_mfma_f32_16x16x32_bf16 v[188:191], a[160:163], a[64:67], 0\n"
    "v_mfma_f32_16x16x32_bf16 v[192:195], a[160:163], a[96:99], 0\n"
    "v_mfma_f32_16x16x32_bf16 v[196:199], a[160:163], a[128:131], 0\n"
    "v_mfma_f32_16x16x32_bf16 v[180:183], a[164:167], a[4:7], v[180:183]\n"
    "v_mfma_f32_16x16x32_bf16 v[184:187], a[164:167], a[36:39], v[184:187]\n"
    "v_mfma_f32_16x16x32_bf16 v[188:191], a[164:167], a[68:71], v[188:191]\n"
    "v_mfma_f32_16x16x32_bf16 v[192:195], a[164:167], a[100:103], v[192:195]\n"
    "v_mfma_f32_16x16x32_bf16 v[196:199], a[164:167], a[132:135], v[196:199]\n"
    "v_mfma_f32_16x16x32_bf16 v[180:183], a[168:171], a[8:11], v[180:183]\n"
    "v_mfma_f32_16x16x32_bf16 v[184:187], a[168:171], a[40:43], v[184:187]\n"
    "v_mfma_f32_16x16x32_bf16 v[188:191], a[168:171], a[72:75], v[188:191]\n"
    "v_mfma_f32_16x16x32_bf16 v[192:195], a[168:171], a[104:107], v[192:195]\n"
    "v_mfma_f32_16x16x32_bf16 v[196:199], a[168:171], a[136:139], v[196:199]\n"
    "v_mfma_f32_16x16x32_bf16 v[180:183], a[172:175], a[12:15], v[180:183]\n"
    "v_mfma_f32_16x16x32_bf16 v[184:187], a[172:175], a[44:47], v[184:187]\n"
    "v_mfma_f32_16x16x32_bf16 v[188:191], a[172:175], a[76:79], v[188:191]\n"
    "v_mfma_f32_16x16x32_bf16 v[192:195], a[172:175], a[108:111], v[192:195]\n"
    "v_mfma_f32_16x16x32_bf16 v[196:199], a[172:175], a[140:143], v[196:199]\n"
    "v_mfma_f32_16x16x32_bf16 v[180:183], a[176:179], a[16:19], v[180:183]\n"
    "v_mfma_f32_16x16x32_bf16 v[184:187], a[176:179], a[48:51], v[184:187]\n"
    "v_mfma_f32_16x16x32_bf16 v[188:191], a[176:179], a[80:83], v[188:191]\n"
    "v_mfma_f32_16x16x32_bf16 v[192:195], a[176:179], a[112:115], v[192:195]\n"
    "v_mfma_f32_16x16x32_bf16 v[196:199], a[176:179], a[144:147], v[196:199]\n"
    "v_mfma_f32_16x16x32_bf16 v[180:183], a[180:183], a[20:23], v[180:183]\n"
    "v_mfma_f32_16x16x32_bf16 v[184:187], a[180:183], a[52:55], v[184:187]\n"
    "v_mfma_f32_16x16x32_bf16 v[188:191], a[180:183], a[84:87], v[188:191]\n"
    "v_mfma_f32_16x16x32_bf16 v[192:195], a[180:183], a[116:119], v[192:195]\n"
    "v_mfma_f32_16x16x32_bf16 v[196:199], a[180:183], a[148:151], v[196:199]\n"
    "v_mfma_f32_16x16x32_bf16 v[180:183], a[184:187], a[24:27], v[180:183]\n"
    "v_mfma_f32_16x16x32_bf16 v[184:187], a[184:187], a[56:59], v[184:187]\n"
    "v_mfma_f32_16x16x32_bf16 v[188:191], a[184:187], a[88:91], v[188:191]\n"
    "v_mfma_f32_16x16x32_bf16 v[192:195], a[184:187], a[120:123], v[192:195]\n"
    "v_mfma_f32_16x16x32_bf16 v[196:199], a[184:187], a[152:155], v[196:199]\n"
    "v_mfma_f32_16x16x32_bf16 v[180:183], a[188:191], a[28:31], v[180:183]\n"
    "v_mfma_f32_16x16x32_bf16 v[184:187], a[188:191], a[60:63], v[184:187]\n"
    "v_mfma_f32_16x16x32_bf16 v[188:191], a[188:191], a[92:95], v[188:191]\n"
    "v_mfma_f32_16x16x32_bf16 v[192:195], a[188:191], a[124:127], v[192:195]\n"
    "v_mfma_f32_16x16x32_bf16 v[196:199], a[188:191], a[156:159], v[196:199]\n"
    "s_nop 15\n"
    "v_max3_f32 v200, v200, v180, v181\n"
    "v_max3_f32 v200, v200, v182, v183\n"
    "v_max3_f32 v201, v201, v184, v185\n"
    "v_max3_f32 v201, v201, v186, v187\n"
    "v_max3_f32 v202, v202, v188, v189\n"
    "v_max3_f32 v202, v202, v190, v191\n"
    "v_max3_f32 v203, v203, v192, v193\n"
    "v_max3_f32 v203, v203, v194, v195\n"
    "v_max3_f32 v204, v204, v196, v197\n"
    "v_max3_f32 v204, v204, v198, v199\n"
    "s_nop 3\n"
    "ds_read_b128 a[160:163], v240 offset:18432\n"
    "ds_read_b128 a[164:167], v241 offset:18432\n"
    "ds_read_b128 a[168:171], v240 offset:22528\n"
    "ds_read_b128 a[172:175], v241 offset:22528\n"
    "ds_read_b128 a[176:179], v242 offset:18432\n"
    "ds_read_b128 a[180:183], v243 offset:18432\n"
    "ds_read_b128 a[184:187], v242 offset:22528\n"
    "ds_read_b128 a[188:191], v243 offset:22528\n"
    "s_waitcnt lgkmcnt(0)\n"
    "v_mfma_f32_16x16x32_bf16 v[180:183], a[160:163], a[0:3], 0\n"
    "v_mfma_f32_16x16x32_bf16 v[184:187], a[160:163], a[32:35], 0\n"
    "v_mfma_f32_16x16x32_bf16 v[188:191], a[160:163], a[64:67], 0\n"
    "v_mfma_f32_16x16x32_bf16 v[192:195], a[160:163], a[96:99], 0\n"
    "v_mfma_f32_16x16x32_bf16 v[196:199], a[160:163], a[128:131], 0\n"
    "v_mfma_f32_16x16x32_bf16 v[180:183], a[164:167], a[4:7], v[180:183]\n"
    "v_mfma_f32_16x16x32_bf16 v[184:187], a[164:167], a[36:39], v[184:187]\n"
    "v_mfma_f32_16x16x32_bf16 v[188:191], a[164:167], a[68:71], v[188:191]\n"
    "v_mfma_f32_16x16x32_bf16 v[192:195], a[164:167], a[100:103], v[192:195]\n"
    "v_mfma_f32_16x16x32_bf16 v[196:199], a[164:167], a[132:135], v[196:199]\n"
    "v_mfma_f32_16x16x32_bf16 v[180:183], a[168:171], a[8:11], v[180:183]\n"
    "v_mfma_f32_16x16x32_bf16 v[184:187], a[168:171], a[40:43], v[184:187]\n"
    "v_mfma_f32_16x16x32_bf16 v[188:191], a[168:171], a[72:75], v[188:191]\n"
    "v_mfma_f32_16x16x32_bf16 v[192:195], a[168:171], a[104:107], v[192:195]\n"
    "v_mfma_f32_16x16x32_bf16 v[196:199], a[168:171], a[136:139], v[196:199]\n"
    "v_mfma_f32_16x16x32_bf16 v[180:183], a[172:175], a[12:15], v[180:183]\n"
    "v_mfma_f32_16x16x32_bf16 v[184:187], a[172:175], a[44:47], v[184:187]\n"
    "v_mfma_f32_16x16x32_bf16 v[188:191], a[172:175], a[76:79], v[188:191]\n"
    "v_mfma_f32_16x16x32_bf16 v[192:195], a[172:175], a[108:111], v[192:195]\n"
    "v_mfma_f32_16x16x32_bf16 v[196:199], a[172:175], a[140:143], v[196:199]\n"
    "v_mfma_f32_16x16x32_bf16 v[180:183], a[176:179], a[16:19], v[180:183]\n"
    "v_mfma_f32_16x16x32_bf16 v[184:187], a[176:179], a[48:51], v[184:187]\n"
    "v_mfma_f32_16x16x32_bf16 v[188:191], a[176:179], a[80:83], v[188:191]\n"
    "v_mfma_f32_16x16x32_bf16 v[192:195], a[176:179], a[112:115], v[192:195]\n"
    "v_mfma_f32_16x16x32_bf16 v[196:199], a[176:179], a[144:147], v[196:199]\n"
    "v_mfma_f32_16x16x32_bf16 v[180:183], a[180:183], a[20:23], v[180:183]\n"
    "v_mfma_f32_16x16x32_bf16 v[184:187], a[180:183], a[52:55], v[184:187]\n"
    "v_mfma_f32_16x16x32_bf16 v[188:191], a[180:183], a[84:87], v[188:191]\n"
    "v_mfma_f32_16x16x32_bf16 v[192:195], a[180:183], a[116:119], v[192:195]\n"
    "v_mfma_f32_16x16x32_bf16 v[196:199], a[180:183], a[148:151], v[196:199]\n"
    "v_mfma_f32_16x16x32_bf16 v[180:183], a[184:187], a[24:27], v[180:183]\n"
    "v_mfma_f32_16x16x32_bf16 v[184:187], a[184:187], a[56:59], v[184:187]\n"
    "v_mfma_f32_16x16x32_bf16 v[188:191], a[184:187], a[88:91], v[188:191]\n"
    "v_mfma_f32_16x16x32_bf16 v[192:195], a[184:187], a[120:123], v[192:195]\n"
    "v_mfma_f32_16x16x32_bf16 v[196:199], a[184:187], a[152:155], v[196:199]\n"
    "v_mfma_f32_16x16x32_bf16 v[180:183], a[188:191], a[28:31], v[180:183]\n"
    "v_mfma_f32_16x16x32_bf16 v[184:187], a[188:191], a[60:63], v[184:187]\n"
    "v_mfma_f32_16x16x32_bf16 v[188:191], a[188:191], a[92:95], v[188:191]\n"
    "v_mfma_f32_16x16x32_bf16 v[192:195], a[188:191], a[124:127], v[192:195]\n"
    "v_mfma_f32_16x16x32_bf16 v[196:199], a[188:191], a[156:159], v[196:199]\n"
    "s_nop 15\n"
    "v_max3_f32 v200, v200, v180, v181\n"
    "v_max3_f32 v200, v200, v182, v183\n"
    "v_max3_f32 v201, v201, v184, v185\n"
    "v_max3_f32 v201, v201, v186, v187\n"
    "v_max3_f32 v202, v202, v188, v189\n"
    "v_max3_f32 v202, v202, v190, v191\n"
    "v_max3_f32 v203, v203, v192, v193\n"
    "v_max3_f32 v203, v203, v194, v195\n"
    "v_max3_f32 v204, v204, v196, v197\n"
    "v_max3_f32 v204, v204, v198, v199\n"
    "s_nop 3\n"
    "s_barrier\n"
    "s_add_u32 s45, s45, 0x8000\n"
    "s_sub_u32 s48, s48, 1\n"
    "s_cmp_lg_u32 s48, 0\n"
    "s_cbranch_scc1 .Lat_mp_loop\n"
    "s_mov_b32 m0, s42\n"
    "s_nop 1\n"
    "ds_write_addtid_b32 v200 offset:0\n"
    "ds_write_addtid_b32 v201 offset:256\n"
    "ds_write_addtid_b32 v202 offset:512\n"
    "ds_write_addtid_b32 v203 offset:768\n"
    "ds_write_addtid_b32 v204 offset:1024\n"
    "s_waitcnt lgkmcnt(0)\n"
    "s_barrier\n"
    "ds_read_b32 v180, v249 offset:0\n"
    "ds_read_b32 v184, v249 offset:256\n"
    "ds_read_b32 v188, v249 offset:512\n"
    "ds_read_b32 v192, v249 offset:768\n"
    "ds_read_b32 v196, v249 offset:1024\n"
    "s_waitcnt lgkmcnt(0)\n"
    "v_max_f32_e32 v205, v200, v180\n"
    "v_mov_b32_e32 v206, v205\n"
    "s_nop 1\n"
    "v_permlane16_swap_b32_e32 v205, v206\n"
    "s_nop 0\n"
    "v_max_f32_e32 v205, v205, v206\n"
    "v_mov_b32_e32 v206, v205\n"
    "s_nop 1\n"
    "v_permlane32_swap_b32_e32 v205, v206\n"
    "s_nop 0\n"
    "v_max_f32_e32 v205, v205, v206\n"
    "v_add_f32_e32 v205, 0x42980000, v205\n"
    "v_xor_b32_e32 v220, 0x80000000, v205\n"
    "v_xor_b32_e32 v221, 0x80000000, v205\n"
    "v_xor_b32_e32 v222, 0x80000000, v205\n"
    "v_xor_b32_e32 v223, 0x80000000, v205\n"
    "v_max_f32_e32 v205, v201, v184\n"
    "v_mov_b32_e32 v206, v205\n"
    "s_nop 1\n"
    "v_permlane16_swap_b32_e32 v205, v206\n"
    "s_nop 0\n"
    "v_max_f32_e32 v205, v205, v206\n"
    "v_mov_b32_e32 v206, v205\n"
    "s_nop 1\n"
    "v_permlane32_swap_b32_e32 v205, v206\n"
    "s_nop 0\n"
    "v_max_f32_e32 v205, v205, v206\n"
    "v_add_f32_e32 v205, 0x42980000, v205\n"
    "v_xor_b32_e32 v224, 0x80000000, v205\n"
    "v_xor_b32_e32 v225, 0x80000000, v205\n"
    "v_xor_b32_e32 v226, 0x80000000, v205\n"
    "v_xor_b32_e32 v227, 0x80000000, v205\n"
    "v_max_f32_e32 v205, v202, v188\n"
    "v_mov_b32_e32 v206, v205\n"
    "s_nop 1\n"
    "v_permlane16_swap_b32_e32 v205, v206\n"
    "s_nop 0\n"
    "v_max_f32_e32 v205, v205, v206\n"
    "v_mov_b32_e32 v206, v205\n"
    "s_nop 1\n"
    "v_permlane32_swap_b32_e32 v205, v206\n"
    "s_nop 0\n"
    "v_max_f32_e32 v205, v205, v206\n"
    "v_add_f32_e32 v205, 0x42980000, v205\n"
    "v_xor_b32_e32 v228, 0x80000000, v205\n"
    "v_xor_b32_e32 v229, 0x80000000, v205\n"
    "v_xor_b32_e32 v230, 0x80000000, v205\n"
    "v_xor_b32_e32 v231, 0x80000000, v205\n"
    "v_max_f32_e32 v205, v203, v192\n"
    "v_mov_b32_e32 v206, v205\n"
    "s_nop 1\n"
    "v_permlane16_swap_b32_e32 v205, v206\n"
    "s_nop 0\n"
    "v_max_f32_e32 v205, v205, v206\n"
    "v_mov_b32_e32 v206, v205\n"
    "s_nop 1\n"
    "v_permlane32_swap_b32_e32 v205, v206\n"
    "s_nop 0\n"
    "v_max_f32_e32 v205, v205, v206\n"
    "v_add_f32_e32 v205, 0x42980000, v205\n"
    "v_xor_b32_e32 v232, 0x80000000, v205\n"
    "v_xor_b32_e32 v233, 0x80000000, v205\n"
    "v_xor_b32_e32 v234, 0x80000000, v205\n"
    "v_xor_b32_e32 v235, 0x80000000, v205\n"
    "v_max_f32_e32 v205, v204, v196\n"
    "v_mov_b32_e32 v206, v205\n"
    "s_nop 1\n"
    "v_permlane16_swap_b32_e32 v205, v206\n"
    "s_nop 0\n"
    "v_max_f32_e32 v205, v205, v206\n"
    "v_mov_b32_e32 v206, v205\n"
    "s_nop 1\n"
    "v_permlane32_swap_b32_e32 v205, v206\n"
    "s_nop 0\n"
    "v_max_f32_e32 v205, v205, v206\n"
    "v_add_f32_e32 v205, 0x42980000, v205\n"
    "v_xor_b32_e32 v236, 0x80000000, v205\n"
    "v_xor_b32_e32 v237, 0x80000000, v205\n"
    "v_xor_b32_e32 v238, 0x80000000, v205\n"
    "v_xor_b32_e32 v239, 0x80000000, v205\n"
    "s_nop 1\n"
    "s_barrier\n"
    "s_mul_i32 s48, s33, 81920\n"
    "s_add_u32 s48, s48, 11345920\n"
    "s_lshl_b32 s46, s93, 11\n"
    "s_add_u32 s48, s48, s46\n"
    "s_add_u32 s46, s46, 49152\n"
    "s_and_b32 s45, s32, 1\n"
    "s_xor_b32 s45, s45, 8\n"
    "s_lshl_b32 s45, s45, 13\n"
    "s_add_u32 s45, s45, s48\n"
    "s_add_u32 m0, s46, 0\n"
    "s_nop 0\n"
    "buffer_load_dwordx4 v250, s[8:11], s45 offen offset:0 sc1 lds\n"
    "buffer_load_dwordx4 v250, s[8:11], s45 offen offset:1024 sc1 lds\n"
    "s_branch .Lat_maxpass_ret\n"
    ".Lat_out:\n"
    "s_waitcnt lgkmcnt(0)\n"
    "s_mov_b64 s[2:3], s[0:1]\n"
    "s_mov_b64 %0, s[2:3]\n"
    "s_mov_b32 %1, s92\n"
    "s_mov_b32 %2, s93\n"
    : "=s"(ka), "=s"(b), "=s"(wid) : "0"(__builtin_amdgcn_kernarg_segment_ptr()), "1"(blockIdx.x)
    : "v0", "v1", "v2", "v3", "v4", "v5", "v6", "v7", "v8", "v9", "v10", "v11", "v12", "v13", "v14", "v15", "v16", "v17", "v18", "v19", "v20", "v21", "v22", "v23", "v24", "v25", "v26", "v27", "v28", "v29", "v30", "v31", "v32", "v33", "v34", "v35", "v36", "v37", "v38", "v39", "v40", "v41", "v42", "v43", "v44", "v45", "v46", "v47", "v48", "v49", "v50", "v51", "v52", "v53", "v54", "v55", "v56", "v57", "v58", "v59", "v60", "v61", "v62", "v63", "v64", "v65", "v66", "v67", "v68", "v69", "v70", "v71", "v72", "v73", "v74", "v75", "v76", "v77", "v78", "v79", "v80", "v81", "v82", "v83", "v84", "v85", "v86", "v87", "v88", "v89", "v90", "v91", "v92", "v93", "v94", "v95", "v96", "v97", "v98", "v99", "v100", "v101", "v102", "v103", "v104", "v105", "v106", "v107", "v108", "v109", "v110", "v111", "v112", "v113", "v114", "v115", "v116", "v117", "v118", "v119", "v120", "v121", "v122", "v123", "v124", "v125", "v126", "v127", "v128", "v129", "v130", "v131", "v132", "v133", "v134", "v135", "v136", "v137", "v138", "v139", "v140", "v141", "v142", "v143", "v144", "v145", "v146", "v147", "v148", "v149", "v150", "v151", "v152", "v153", "v154", "v155", "v156", "v157", "v158", "v159", "v160", "v161", "v162", "v163", "v164", "v165", "v166", "v167", "v168", "v169", "v170", "v171", "v172", "v173", "v174", "v175", "v176", "v177", "v178", "v179", "v180", "v181", "v182", "v183", "v184", "v185", "v186", "v187", "v188", "v189", "v190", "v191", "v192", "v193", "v194", "v195", "v196", "v197", "v198", "v199", "v200", "v201", "v202", "v203", "v204", "v205", "v206", "v207", "v208", "v209", "v210", "v211", "v212", "v213", "v214", "v215", "v216", "v217", "v218", "v219", "v220", "v221", "v222", "v223", "v224", "v225", "v226", "v227", "v228", "v229", "v230", "v231", "v232", "v233", "v234", "v235", "v236", "v237", "v238", "v239", "v240", "v241", "v242", "v243", "v244", "v245", "v246", "v247", "v248", "v249", "v250", "v251", "v252", "v253", "v254", "v255", "a0", "a1", "a2", "a3", "a4", "a5", "a6", "a7", "a8", "a9", "a10", "a11", "a12", "a13", "a14", "a15", "a16", "a17", "a18", "a19", "a20", "a21", "a22", "a23", "a24", "a25", "a26", "a27", "a28", "a29", "a30", "a31", "a32", "a33", "a34", "a35", "a36", "a37", "a38", "a39", "a40", "a41", "a42", "a43", "a44", "a45", "a46", "a47", "a48", "a49", "a50", "a51", "a52", "a53", "a54", "a55", "a56", "a57", "a58", "a59", "a60", "a61", "a62", "a63", "a64", "a65", "a66", "a67", "a68", "a69", "a70", "a71", "a72", "a73", "a74", "a75", "a76", "a77", "a78", "a79", "a80", "a81", "a82", "a83", "a84", "a85", "a86", "a87", "a88", "a89", "a90", "a91", "a92", "a93", "a94", "a95", "a96", "a97", "a98", "a99", "a100", "a101", "a102", "a103", "a104", "a105", "a106", "a107", "a108", "a109", "a110", "a111", "a112", "a113", "a114", "a115", "a116", "a117", "a118", "a119", "a120", "a121", "a122", "a123", "a124", "a125", "a126", "a127", "a128", "a129", "a130", "a131", "a132", "a133", "a134", "a135", "a136", "a137", "a138", "a139", "a140", "a141", "a142", "a143", "a144", "a145", "a146", "a147", "a148", "a149", "a150", "a151", "a152", "a153", "a154", "a155", "a156", "a157", "a158", "a159", "a160", "a161", "a162", "a163", "a164", "a165", "a166", "a167", "a168", "a169", "a170", "a171", "a172", "a173", "a174", "a175", "a176", "a177", "a178", "a179", "a180", "a181", "a182", "a183", "a184", "a185", "a186", "a187", "a188", "a189", "a190", "a191", "a192", "a193", "a194", "a195", "a196", "a197", "a198", "a199", "a200", "a201", "a202", "a203", "a204", "a205", "a206", "a207", "a208", "a209", "a210", "a211", "a212", "a213", "a214", "a215", "a216", "a217", "a218", "a219", "a220", "a221", "a222", "a223", "a224", "a225", "a226", "a227", "a228", "a229", "a230", "a231", "a232", "a233", "a234", "a235", "a236", "a237", "a238", "a239", "a240", "a241", "a242", "a243", "a244", "a245", "a246", "a247", "a248", "a249", "a250", "a251", "a252", "a253", "a254", "a255", "s2", "s3", "s4", "s5", "s6", "s7", "s8", "s9", "s10", "s11", "s12", "s13", "s14", "s15", "s16", "s17", "s18", "s19", "s20", "s21", "s22", "s23", "s24", "s25", "s26", "s27", "s28", "s29", "s30", "s31", "s32", "s33", "s34", "s35", "s36", "s37", "s38", "s39", "s40", "s41", "s42", "s43", "s44", "s45", "s46", "s47", "s48", "s49", "s50", "s51", "s52", "s53", "s54", "s55", "s56", "s57", "s58", "s59", "s60", "s61", "s62", "s63", "s80", "s81", "s82", "s83", "s84", "s85", "s86", "s87", "s88", "s89", "s90", "s91", "s92", "s93", "s94", "s95", "vcc", "m0", "scc", "memory");
  typedef __attribute__((address_space(1))) void* gp;
  helper(lds, b >> 1 & 3, wid, (const int*)(gp)ka[2], (const int*)(gp)ka[4], (const u16*)(gp)ka[8], (const u16*)(gp)ka[9], (const u16*)(gp)ka[10], (char*)(gp)ka[3]);
}

extern "C" void kernel_launch(void* const* d_in, const int* in_sizes, int n_in, void* d_out, int out_size, void* d_ws,
                              size_t ws_size, hipStream_t stream) {
  const u16* x = (const u16*)d_in[0];
  const int* src = (const int*)d_in[1];
  const int* dst = (const int*)d_in[2];
  const u16* deg_inv = (const u16*)d_in[3];
  const u16* wmsg = (const u16*)d_in[4];
  const u16* wq = (const u16*)d_in[5];
  const u16* wk = (const u16*)d_in[6];
  const u16* wv = (const u16*)d_in[7];
  const u16* wout = (const u16*)d_in[8];
  char* ws = (char*)d_ws;
  hipFuncSetAttribute((const void*)gps_kernel, hipFuncAttributeMaxDynamicSharedMemorySize, 163840);
  gps_kernel<<<256, 256, 163840, stream>>>(wq, wk, src, ws, dst, x, (u16*)d_out, deg_inv, wv, wmsg, wout);
}
```

```cpp
#include <hip/hip_runtime.h>
#include <hip/hip_bf16.h>
#include <stdint.h>

typedef unsigned short u16;
typedef __attribute__((ext_vector_type(8))) short bf16x8;
typedef __attribute__((ext_vector_type(4))) short bf16x4;
typedef __attribute__((ext_vector_type(4))) float f32x4;
typedef __attribute__((ext_vector_type(2))) float f32x2;
typedef __attribute__((ext_vector_type(4))) unsigned u32x4;
typedef __attribute__((ext_vector_type(2))) unsigned u32x2;
#define DI __device__ __forceinline__

constexpr int NN = 10000, DD = 256, EE = 160000;
constexpr int NATT = 252, NSP = 4, SPN = 2560;
constexpr int GU = 5, GNB = 5; static_assert(GU * GNB <= 30, "list size");

constexpr unsigned WS_H = 0;
constexpr unsigned WS_AF = WS_H + 10240 * 512;
constexpr unsigned WS_WV = WS_AF + 135168;
constexpr unsigned WS_WM = WS_WV + 135168;
constexpr unsigned WS_XF = WS_WM + 135168;
constexpr unsigned WS_GF = WS_XF + 3328 * 128;
constexpr unsigned WS_HF = WS_GF + 2048;
constexpr unsigned WS_PF = WS_HF + 4096;
constexpr unsigned WS_NF = WS_PF + 4096;
constexpr unsigned WS_WF = WS_NF + 16384;
constexpr unsigned WS_LF = WS_WF + 256;
constexpr unsigned WS_EP = WS_LF + 512;
constexpr unsigned WS_G = WS_EP + 256 + 1024;
constexpr unsigned WS_QP = WS_G + 10240 * 512;
constexpr unsigned WS_XB = WS_QP + 10240 * 512;
constexpr unsigned WS_LS = WS_XB + 2 * NATT * 101376;
constexpr unsigned WS_BL = WS_LS + 524288;
constexpr unsigned WS_DBG = WS_BL + 64 * 260 * 64;
constexpr unsigned WS_END = WS_DBG + 1024 * 128 + 8192;

DI float bf2f(unsigned x) { return __uint_as_float(x << 16); }
DI float bfhi(unsigned x) { return __uint_as_float(x & 0xffff0000u); }
DI unsigned pk2(float lo, float hi) {
  typedef __attribute__((ext_vector_type(2))) __bf16 bf2_t;
  const bf2_t r = __builtin_convertvector((f32x2){lo, hi}, bf2_t);
  return __builtin_bit_cast(unsigned, r);
}
typedef __attribute__((address_space(3))) bf16x4* lds_bf16x4_p;
DI bf16x4 tr_read(unsigned p) { return __builtin_amdgcn_ds_read_tr16_b64_v4i16((lds_bf16x4_p)p); }
DI bf16x8 as_bf(u32x4 v) { return __builtin_bit_cast(bf16x8, v); }
#define SB() __builtin_amdgcn_sched_barrier(0)
#define MFMA16(a, b, c) __builtin_amdgcn_mfma_f32_16x16x32_bf16(a, b, c, 0, 0, 0)

typedef __amdgpu_buffer_rsrc_t srd_t;
DI void drain() { asm volatile("s_waitcnt vmcnt(0)" ::: "memory"); }
DI void st_flag(srd_t w, unsigned off, unsigned tag) { __builtin_amdgcn_raw_buffer_store_b32(tag, w, off, 0, 16); }
DI void wait_flag(srd_t w, unsigned off, unsigned tag) {
#pragma unroll 1
  for (int it = 0; it < (1 << 22); ++it) {
    unsigned v;
    asm volatile("buffer_load_dword %0, %1, %2, 0 offen sc1\n s_waitcnt vmcnt(0)" : "=v"(v) : "v"(off), "s"(w) : "memory");
    if (__ballot(v != tag) == 0) break;
    __builtin_amdgcn_s_sleep(2);
    asm volatile("" ::: "memory");
  }
  asm volatile("" ::: "memory");
}

DI void prod_store(const f32x4 (&acc)[2][2], int mat, int ks, int J, int lane, srd_t wsrd) {
#pragma unroll
  for (int s = 0; s < 2; ++s) {
    u32x4 o = {pk2(acc[0][s][0], acc[0][s][1]), pk2(acc[0][s][2], acc[0][s][3]), pk2(acc[1][s][0], acc[1][s][1]), pk2(acc[1][s][2], acc[1][s][3])};
    if (ks == 7 && lane >= 48) o[3] &= 0xffffu;
    const int T = 2 * J + s;
    const int at = mat * 135168 + ks * 4096 + (T & 3) * 1024;
    __builtin_amdgcn_raw_buffer_store_b128(o, wsrd, WS_WV + (T >> 2) * 33792 + at + lane * 16, 0, 16);
  }
}
DI void corr(unsigned* dl, const unsigned (&lr)[16]) {
  float a0 = 0.f, a1 = 0.f;
#pragma unroll
  for (int r = 0; r < 16; ++r) { a0 += bf2f(lr[r] & 0xffffu); a1 += bfhi(lr[r]); }
  dl[0] = pk2(a0 * -0.0625f, a1 * -0.0625f);
  dl[128] = pk2((bf2f(lr[15] & 0xffffu) - 0.25f * a0) * (1.f / 3.f), (bfhi(lr[15]) - 0.25f * a1) * (1.f / 3.f));
}
DI void product_load(char* lds, int wid, int lane, int job, const u16* __restrict__ wv, const u16* __restrict__ wm, const u16* __restrict__ wo) {
  const unsigned ldsb = (unsigned)(size_t)lds;
  const int r = lane >> 3, sw = ((lane & 7) ^ (r & 6)) * 16 + wid * 128;
  const unsigned vp = (8 * (r >> 2) + (r & 3)) * 512 + sw, vn = r * 512 + sw;
  auto image = [&](int n, const u16* w, int row0, bool nat) {
#pragma unroll
    for (int i = 0; i < 8; ++i) {
      const int a = i & 3;
      asm volatile("s_mov_b32 m0, %2\n s_nop 0\n global_load_lds_dwordx4 %0, %1"
                   :: "v"(nat ? vn : vp), "s"(w + (row0 + (i >> 2) * 32 + (nat ? 8 * a : 16 * (a & 1) + 4 * (a >> 1))) * DD),
                      "s"(ldsb + n * 32768 + (i >> 2) * 16384 + wid * 4096 + a * 1024) : "m0", "memory");
    }
  };
  image(2, wv, (job >> 2) * 64, true);
  image(3, wm, (job >> 2) * 64, false);
#pragma unroll
  for (int k = 0; k < 8; ++k)
    asm volatile("s_mov_b32 m0, %2\n s_nop 0\n global_load_lds_dwordx4 %0, %1"
                 :: "v"((lane >> 3) * 512 + (lane & 7) * 16), "s"(wo + (k * 32 + wid * 8) * DD + (job & 3) * 64), "s"(ldsb + k * 4096 + wid * 1024) : "m0", "memory");
}
DI void product_w(char* lds, int wid, int lane, int job, srd_t wsrd) {
  const int m = lane & 15, g = lane >> 4;
  const int ks = 2 * (job >> 2) + (wid >> 1), J = 2 * (job & 3) + (wid & 1);
  const unsigned ldsb = (unsigned)(size_t)lds;
  unsigned* dl = reinterpret_cast<unsigned*>(lds + 0x20000);
  const bool hi = (m >> 3) != 0;
  const unsigned ra = (m >> 3) * 1024 + (m & 7) * 128 + (g ^ (m & 6)) * 16;
  auto frag = [&](int n, int tl, int t, int k) {
    return *reinterpret_cast<const bf16x8*>(lds + n * 32768 + tl * 16384 + (k >> 1) * 4096 + t * 2048 + (ra ^ ((k & 1) * 64)));
  };
  f32x4 av[2][2] = {}, am[2][2] = {}, ad[2] = {};
  const unsigned tb = ldsb + (8 * g + ((lane >> 2) & 3)) * 128 + (wid & 1) * 64 + (lane & 3) * 16;
  bf16x8 d0[8], d1[8], d2[8], rr[8][2], vf[8][2], mf[8][2];
#pragma unroll
  for (int kk = 0; kk < 9; ++kk) {
    if (kk < 8) {
      d0[kk] = *reinterpret_cast<const bf16x8*>(dl + 512 + 16 * kk + 4 * g); d1[kk] = *reinterpret_cast<const bf16x8*>(dl + 512 + (ks == 7 ? 128 : 0) + 16 * kk + 4 * g);
      d2[kk] = *reinterpret_cast<const bf16x8*>(dl + 768 + (ks == 7 && hi ? 128 : 0) + 16 * kk + 4 * g);
#pragma unroll
      for (int s = 0; s < 2; ++s) {
        rr[kk][s] = __builtin_shufflevector(tr_read(tb + kk * 4096 + s * 8), tr_read(tb + kk * 4096 + s * 8 + 512), 0, 1, 2, 3, 4, 5, 6, 7);
        vf[kk][s] = frag(2, wid >> 1, s, kk); mf[kk][s] = frag(3, wid >> 1, s, kk);
      }
    }
    SB();
    if (kk) {
      const int k = kk - 1;
#pragma unroll
      for (int s = 0; s < 2; ++s) {
        av[0][s] = MFMA16(d0[k], rr[k][s], av[0][s]); av[1][s] = MFMA16(d1[k], rr[k][s], av[1][s]); ad[s] = MFMA16(d2[k], rr[k][s], ad[s]);
#pragma unroll
        for (int t = 0; t < 2; ++t) { av[t][s] = MFMA16(vf[k][t], rr[k][s], av[t][s]); am[t][s] = MFMA16(mf[k][t], rr[k][s], am[t][s]); }
      }
    }
    SB();
  }
#pragma unroll
  for (int t = 0; t < 2; ++t)
#pragma unroll
    for (int s = 0; s < 2; ++s) am[t][s] += ad[s];
  prod_store(av, 0, ks, J, lane, wsrd);
  prod_store(am, 1, ks, J, lane, wsrd);
}

DI void helper(char* lds, int j, int wid, const int* __restrict__ src, const int* __restrict__ dst,
               const u16* __restrict__ wv, const u16* __restrict__ wm, const u16* __restrict__ wo, char* ws) {
  unsigned* img = reinterpret_cast<unsigned*>(lds);
  char* cntb = lds + 0x20000;
  int* cnt = reinterpret_cast<int*>(cntb);
  unsigned* ub = reinterpret_cast<unsigned*>(lds + 0x22c00);
  int* red = reinterpret_cast<int*>(ub + 256);
  u16* dg = reinterpret_cast<u16*>(lds + 0x23400);
  u16* tmp = reinterpret_cast<u16*>(lds + 0x25000) + wid * 512;
  int lane; asm volatile("v_mbcnt_lo_u32_b32 %0, -1, 0\n\tv_mbcnt_hi_u32_b32 %0, -1, %0" : "=v"(lane));
  const int tid = wid * 64 + lane, lo = j * SPN, nn_ = min(SPN, NN - lo);
  const srd_t wsrd = __builtin_amdgcn_make_buffer_rsrc(ws, 0, WS_END, 0x00020000);
  const unsigned tag = __builtin_amdgcn_raw_buffer_load_b32(wsrd, WS_EP, 0, 0) + 4u;
  auto touch = [&](unsigned at) { asm volatile("s_mov_b32 m0, 0x27c00\n s_nop 0\n buffer_load_dword %0, %1, 0 offen sc1 lds" :: "v"(at), "s"(wsrd) : "m0", "memory"); };
  touch(lane < 16 ? min(WS_GF + (lane * 16 + wid * 4 + j) * 128, WS_EP) : 0x40000000u);
  for (int i = tid; i < 8192; i += 256) { const unsigned f = (i & 63) >= 60 ? 0u : ~0u; reinterpret_cast<u32x4*>(img)[i] = (u32x4){f, f, f, f}; }
  for (int i = tid; i < SPN + 64; i += 256) cnt[i] = 0;
  ub[tid] = 0u;
  if (tid < 8) red[tid] = 0;
  touch(WS_XF + 262144 + (j * 256 + tid) * 128);
  wait_flag(wsrd, WS_NF + j * 4096 + min(tid, 249) * 16, tag);
  __syncthreads();
  const unsigned dum = (SPN + lane) << 18;
  auto fetch = [&](int k) {
    return __builtin_amdgcn_raw_buffer_load_b128(wsrd, WS_BL + (k < 48 ? tid * 16 + (k & 7) * 4096 : (tid >> 2) * 128 + (tid & 3) * 16 + (k & 3) * 8192 + (k & 4) * 16), (j * 8 + (k >> 3)) * 33280, 16);
  };
  auto clean = [&](u32x4& e, int k) {
#pragma unroll
    for (int r = 0; r < 4; ++r) e[r] = (k < 48 ? (k & 7) == 7 && tid >= 208 : (k & 3) == 3 && tid >= 232) ? dum : min(e[r], dum);
  };
  auto slot = [&](unsigned e) { return reinterpret_cast<int*>(cntb + (e >> 16)); };
  constexpr int NL = 52;
  u32x4 ent[NL];
#pragma unroll
  for (int k = 0; k < NL; ++k) ent[k] = fetch(k);
  SB();
#pragma unroll
  for (int k = 0; k < NL; ++k) {
    clean(ent[k], k);
    int* p[4];
#pragma unroll
    for (int r = 0; r < 4; ++r) p[r] = slot(ent[k][r]);
    SB();
#pragma unroll
    for (int r = 0; r < 4; ++r) atomicAdd(p[r], 2);
    SB();
  }
  unsigned use = 0;
  const unsigned dum3 = (tid & 3) == 3 ? dum : 0;
#pragma unroll
  for (int k = 0; k < 4; ++k) use |= __ballot(ent[NL - 4 + k][3] < dum3) ? 1u << k : 0u;
  if (use)
#pragma unroll 1
    for (int k = NL; k < 64; ++k)
      if (use >> (k - NL) & 1) {
        u32x4 e = fetch(k); clean(e, k);
#pragma unroll
        for (int r = 0; r < 4; ++r) atomicAdd(slot(e[r]), 2);
        if (__ballot(e[3] < dum3)) { use |= 16u << (k - NL); if (k >= 60) red[7] = 1; }
      }
  __syncthreads();
  int inc = 0, nu[10], cn[10], cm = 0;
  auto units = [&](int c) { return max(1, (c + 6) >> 3); };
#pragma unroll
  for (int i = 0; i < 10; i += 2) { const u32x2 v = *reinterpret_cast<u32x2*>(cnt + 10 * tid + i); cn[i] = v[0]; cn[i + 1] = v[1]; }
  const int nup = units(cnt[max(10 * tid - 1, 0)]);
  const bool act = 10 * tid < nn_;
#pragma unroll
  for (int i = 0; i < 10; ++i) { cm = max(cm, cn[i]); nu[i] = act ? units(cn[i]) : 0; inc += nu[i]; }
  const int own = inc;
  inc += __builtin_amdgcn_update_dpp(0, inc, 0x111, 15, 15, true); inc += __builtin_amdgcn_update_dpp(0, inc, 0x112, 15, 15, true);
  inc += __builtin_amdgcn_update_dpp(0, inc, 0x114, 15, 15, true); inc += __builtin_amdgcn_update_dpp(0, inc, 0x118, 15, 15, true);
  inc += __builtin_amdgcn_update_dpp(0, inc, 0x142, 10, 15, false); inc += __builtin_amdgcn_update_dpp(0, inc, 0x143, 12, 15, false);
  if (lane == 63) red[wid] = inc;
  const bool srt = __ballot(cm > 64);
  __syncthreads();
  const u32x4 rd = *reinterpret_cast<u32x4*>(red);
  const float rs_ = 64.f / (float)(rd[0] + rd[1] + rd[2] + rd[3]);
  inc += (wid > 0 ? rd[0] : 0) + (wid > 1 ? rd[1] : 0) + (wid > 2 ? rd[2] : 0) - own;
  int wv_[10];
  {
    int o = inc, wp = tid ? min(127, (int)((float)(2 * o - nup) * rs_)) : 128;
#pragma unroll
    for (int i = 0; i < 10; ++i) {
      wv_[i] = min(127, (int)((float)(2 * o + nu[i]) * rs_));
      ub[act && wv_[i] != wp ? wv_[i] : 128 + lane] = (unsigned)o << 12 | (unsigned)(10 * tid + i);
      wp = wv_[i]; o += nu[i];
    }
  }
  __syncthreads();
  {
    int u0[10], top = 0;
#pragma unroll
    for (int i = 0; i < 10; ++i) u0[i] = ub[wv_[i]] >> 12;
#pragma unroll
    for (int i = 0; i < 10; ++i) {
      const int lu = inc - u0[i], e = lu + nu[i] - 1 + GU, q = min(e / (4 * GU), GNB);
      u0[i] = wv_[i] * 1024 + lu * 8; top = max(top, q);
      atomicOr(img + wv_[i] * 256 + 244 + q, act ? 1u << (e - q * 4 * GU) : 0u);
      inc += nu[i];
    }
#pragma unroll
    for (int i = 0; i < 10; i += 2) *reinterpret_cast<u32x2*>(cnt + 10 * tid + i) = (u32x2){(unsigned)u0[i], (unsigned)u0[i + 1]};
    if (tid < 64) cnt[SPN + tid] = 0x30000;
    if (act && top == GNB) red[7] = 1;
  }
  __syncthreads();
  const bool big_ = red[7];
  if (tid < 128) {
    unsigned nb = 0;
#pragma unroll
    for (int q = 0; q < GNB; ++q) nb = img[tid * 256 + 244 + q] ? q + 1 : nb;
    *reinterpret_cast<u32x4*>(img + tid * 256 + 240) = (u32x4){big_ ? 0u : nb, WS_G + (lo + (ub[tid] & 4095u)) * 512, big_ || (tid & 3) ? 0x40000000u : WS_GF + j * 512 + tid * 4, 0u};
  }
  if (!big_) {
    const unsigned dmp = 0x27000 + lane * 4;
    typedef __attribute__((address_space(3))) u16* lds_u16_p;
    unsigned p[2][8];
#pragma unroll
    for (int k = 0; k < NL + 2; k += 2) {
      if (k < NL) {
        int* q[8];
#pragma unroll
        for (int r = 0; r < 8; ++r) q[r] = slot(ent[k + (r >> 2)][r & 3]);
        SB();
#pragma unroll
        for (int r = 0; r < 8; ++r) p[k >> 1 & 1][r] = atomicAdd(q[r], 2);
        SB();
      }
      if (k) {
#pragma unroll
        for (int r = 0; r < 8; ++r) p[~k >> 1 & 1][r] = min(p[~k >> 1 & 1][r], dmp);
        SB();
#pragma unroll
        for (int r = 0; r < 8; ++r) *(lds_u16_p)p[~k >> 1 & 1][r] = ent[k - 2 + (r >> 2)][r & 3];
        SB();
      }
    }
    if (use)
#pragma unroll 1
      for (int k = NL; k < 64; ++k)
        if (use >> (k - NL) & 1) {
          u32x4 e = fetch(k); clean(e, k);
#pragma unroll
          for (int r = 0; r < 4; ++r) *(lds_u16_p)min((unsigned)atomicAdd(slot(e[r]), 2), dmp) = e[r];
        }
    __syncthreads();
    if (srt) {
#pragma unroll
      for (int i = 0; i < 10; ++i) dg[10 * tid + i] = cn[i] >> 1;
      for (unsigned long long big = __ballot(cm > 64); big; big &= big - 1)
      for (int n = 10 * (wid * 64 + __builtin_ctzll(big)), n1 = n + 10; n < n1; ++n) {
        const int d = __builtin_amdgcn_readfirstlane(dg[n]);
        if (d <= 32) continue;
        u16* f = reinterpret_cast<u16*>(lds + cnt[n]) - d;
        for (int i = lane; i < d + 8; i += 64) tmp[i] = i < d ? f[i] : 0xffff;
        __builtin_amdgcn_wave_barrier();
        for (int i = lane; i < d; i += 64) {
          const unsigned key = tmp[i] << 9 | i; int p_ = 0;
          for (int q = 0; q < d; q += 8) {
            const u32x4 v = *reinterpret_cast<u32x4*>(tmp + q);
#pragma unroll
            for (int e = 0; e < 4; ++e) p_ += (((v[e] & 0xffffu) << 9 | (q + 2 * e)) < key) + (((v[e] >> 16) << 9 | (q + 2 * e + 1)) < key);
          }
          f[p_] = key >> 9;
        }
        __builtin_amdgcn_wave_barrier();
      }
    }
  }
  __syncthreads();
#pragma unroll 8
  for (int k = 0; k < 32; ++k)
    __builtin_amdgcn_raw_buffer_store_b128(reinterpret_cast<u32x4*>(img)[k * 256 + tid], wsrd, WS_LS + j * 131072 + (k * 256 + tid) * 16, 0, 16);
  unsigned lr[16];
#pragma unroll
  for (int r = 0; r < 16; ++r) lr[r] = reinterpret_cast<const unsigned*>((wid >= 2 ? wm : wv) + (240 + r) * DD)[tid & 127];
  wait_flag(wsrd, WS_HF + min(tid, NATT - 1) * 16, tag);
  drain();
  __syncthreads();
  if (tid == 0) st_flag(wsrd, WS_LF + j * 128, tag);
  touch(WS_XF + (j * 256 + tid) * 256);
  touch(wid ? 0x40000000u : WS_GF - 32768 + (j * 64 + lane) * 128);
  corr(reinterpret_cast<unsigned*>(lds + 0x20000) + 512 + (wid >> 1) * 256 + (tid & 127), lr);
#pragma unroll 1
  for (int job = 4 * j; job < 4 * j + 4; ++job) {
    product_load(lds, wid, lane, job, wv, wm, wo);
    drain();
    __syncthreads();
    product_w(lds, wid, lane, job, wsrd);
    __syncthreads();
  }
  drain();
  __syncthreads();
  if (tid == 0) st_flag(wsrd, WS_WF + j * 4, tag);
  if (big_) {
    const srd_t msrd = __builtin_amdgcn_make_buffer_rsrc(ws + WS_H, 0, 10240 * 512, 0x00020000);
    bf16x8 b1;
#pragma unroll
    for (int e = 0; e < 8; ++e) b1[e] = (lane & 15) == e + ((lane >> 1) & 8) ? 0x3f80 : 0;
    for (int n = wid; n < nn_; n += 4) {
      f32x4 acc = {};
      for (int e0 = 0; e0 < EE; e0 += 64) {
        unsigned long long m = __ballot(dst[e0 + lane] == lo + n);
        while (m) {
          const int e = e0 + __builtin_ctzll(m); m &= m - 1;
          const unsigned vo = lane < 32 ? (unsigned)src[e] * 512 + lane * 16 : 0x7fffff00u;
          acc = MFMA16(b1, as_bf(__builtin_amdgcn_raw_buffer_load_b128(msrd, vo, 0, 16)), acc);
        }
      }
      __builtin_amdgcn_raw_buffer_store_b64((u32x2){pk2(acc[0], acc[1]), pk2(acc[2], acc[3])}, wsrd,
                                            WS_G + (lo + n) * 512 + (lane >> 5) * 256 + (lane & 15) * 16 + (lane & 16) / 2, 0, 16);
    }
    drain();
    __syncthreads();
    if (tid < 32) st_flag(wsrd, WS_GF + j * 512 + tid * 16, tag);
  }
}

constexpr unsigned CODE_BYTES = 77000;
extern "C" __global__ __launch_bounds__(256, 1) void gps_kernel(const u16* wq, const u16* wk, const int* src, char* ws, const int* dst, const u16* x, u16* out,
                                                                const u16* deg_inv, const u16* wv, const u16* wm, const u16* wo) {
  extern __shared__ __attribute__((aligned(16))) char lds[];
  static_assert(WS_END == 69410816 && WS_EP == 6101760, "workspace layout of the generated code");
  typedef void* const __attribute__((address_space(4)))* arg_p;
  arg_p ka; int b, wid;
  asm volatile(
    ".Lat_0:\n"
    "s_mov_b32 s92, s2\n"
    "s_load_dwordx2 s[2:3], s[0:1], 0x0\n"
    "s_load_dwordx4 s[4:7], s[0:1], 0x8\n"
    "s_load_dwordx8 s[8:15], s[0:1], 0x18\n"
    "s_load_dwordx2 s[28:29], s[0:1], 0x38\n"
    "s_getpc_b64 s[30:31]\n"
    "v_readfirstlane_b32 s93, v0\n"
    "s_bfe_u32 s45, s92, 0x40003\n"
    "v_lshlrev_b32_e32 v1, 5, v0\n"
    "s_lshl_b32 s45, s45, 13\n"
    "s_mov_b32 m0, 0x27f00\n"
    "v_add_u32_e32 v1, s45, v1\n"
    "v_min_u32_e32 v1, 76488, v1\n"
    "global_load_lds_dword v1, s[30:31]\n"
    "s_and_b32 s45, s92, 249\n"
    "s_lshr_b32 s93, s93, 6\n"
    "s_cmp_eq_u32 s45, 249\n"
    "s_cbranch_scc1 .Lat_out\n"
    "v_mbcnt_lo_u32_b32 v24, -1, 0\n"
    "v_mbcnt_hi_u32_b32 v24, -1, v24\n"
    "s_and_b32 s50, s93, 1\n"
    "s_lshr_b32 s51, s93, 1\n"
    "v_lshrrev_b32_e32 v25, 3, v24\n"
    "v_and_b32_e32 v26, 6, v25\n"
    "v_and_b32_e32 v27, 7, v24\n"
    "v_xor_b32_e32 v26, v26, v27\n"
    "v_lshlrev_b32_e32 v26, 4, v26\n"
    "v_and_b32_e32 v27, 4, v25\n"
    "v_add_u32_e32 v27, v27, v25\n"
    "v_lshl_add_u32 v251, v27, 9, v26\n"
    "v_add_u32_e32 v252, 0x1c00, v251\n"
    "s_mov_b32 s22, 0x20000\n"
    "s_mov_b32 s23, 0x20000\n"
    "s_mov_b32 s26, 0x20000\n"
    "s_mov_b32 s27, 0x20000\n"
    "s_lshr_b32 s36, s92, 7\n"
    "s_mul_i32 s36, s36, 3\n"
    "s_add_u32 s36, s36, s92\n"
    "s_and_b32 s36, s36, 127\n"
    "s_lshl_b32 s45, s93, 7\n"
    "s_lshl_b32 s46, s93, 12\n"
    "s_and_b32 s47, s36, 7\n"
    "s_lshl_b32 s47, s47, 14\n"
    "s_mov_b32 m0, s46\n"
    "s_add_u32 s47, s47, s45\n"
    "s_and_b32 s34, s92, 7\n"
    "s_lshl_b32 s34, s34, 5\n"
    "s_lshr_b32 s32, s92, 3\n"
    "s_add_u32 s34, s34, s32\n"
    "s_lshl_b32 s32, s34, 6\n"
    "s_or_b32 s32, s32, 63\n"
    "s_and_b32 s32, s32, 255\n"
    "s_cmp_ge_u32 s34, 252\n"
    "s_cselect_b32 s34, s32, s34\n"
    "s_and_b32 s32, s34, 3\n"
    "s_lshr_b32 s33, s34, 2\n"
    "s_or_b32 s38, s33, 16\n"
    "s_cmp_eq_u32 s38, 62\n"
    "s_cselect_b32 s38, 16, 0\n"
    "s_xor_b32 s33, s33, s38\n"
    "s_lshl2_add_u32 s34, s33, s32\n"
    "s_mul_i32 s38, s33, 81920\n"
    "s_lshl_b32 s35, s32, 13\n"
    "s_and_b32 s55, s35, 0x2000\n"
    "s_add_u32 s35, s35, s38\n"
    "s_add_u32 s55, s55, s38\n"
    "s_mov_b32 s39, 6101760\n"
    "s_waitcnt lgkmcnt(0)\n"
    "s_mov_b64 s[20:21], s[2:3]\n"
    "s_mov_b64 s[24:25], s[4:5]\n"
    "s_load_dword s39, s[8:9], s39\n"
    "buffer_load_dwordx4 v251, s[20:23], s47 offen offset:0 lds\n"
    "s_add_u32 s48, s47, 1024\n"
    "buffer_load_dwordx4 v252, s[20:23], s47 offen offset:3072 lds\n"
    "s_add_u32 s47, s47, 0x1800\n"
    "buffer_load_dwordx4 v251, s[20:23], s48 offen offset:1024 lds\n"
    "buffer_load_dwordx4 v251, s[20:23], s47 offen offset:2048 lds\n"
    "s_lshr_b32 s47, s36, 4\n"
    "s_lshl_b32 s47, s47, 14\n"
    "s_and_b32 s48, s36, 8\n"
    "s_lshl_b32 s48, s48, 8\n"
    "s_add_u32 s47, s47, s48\n"
    "s_add_u32 m0, s46, 0x4000\n"
    "s_add_u32 s47, s47, s45\n"
    "buffer_load_dwordx4 v251, s[24:27], s47 offen offset:0 lds\n"
    "buffer_load_dwordx4 v252, s[24:27], s47 offen offset:1024 lds\n"
    "s_add_u32 s47, s45, 120832\n"
    "buffer_load_dwordx4 v251, s[24:27], s47 offen offset:2048 lds\n"
    "s_add_u32 s47, s47, 1024\n"
    "buffer_load_dwordx4 v251, s[24:27], s47 offen offset:3072 lds\n"
    "s_mov_b64 s[16:17], s[14:15]\n"
    "s_mov_b64 s[86:87], s[10:11]\n"
    "s_mov_b32 s14, 5120000\n"
    "s_mov_b32 s15, 0x20000\n"
    "s_lshl_b32 s45, s93, 2\n"
    "s_add_u32 s54, s35, 0x8000\n"
    "s_add_u32 s55, s55, 0x10000\n"
    "v_lshlrev_b32_e32 v250, 4, v24\n"
    "v_lshrrev_b32_e32 v28, 4, v24\n"
    "v_add_u32_e32 v28, s45, v28\n"
    "v_and_b32_e32 v104, 0xf0, v250\n"
    "v_lshl_add_u32 v104, v28, 9, v104\n"
    "buffer_load_dwordx4 v[0:3], v104, s[12:15], s35 offen offset:0\n"
    "buffer_load_dwordx4 v[4:7], v104, s[12:15], s35 offen offset:256\n"
    "s_cmp_lt_u32 s32, 2\n"
    "s_cselect_b32 s60, s55, 2.0\n"
    "buffer_load_dwordx4 v[8:11], v104, s[12:15], s54 offen offset:0\n"
    "buffer_load_dwordx4 v[12:15], v104, s[12:15], s54 offen offset:256\n"
    "buffer_load_dwordx4 v[16:19], v104, s[12:15], s55 offen offset:0\n"
    "buffer_load_dwordx4 v[20:23], v104, s[12:15], s55 offen offset:256\n"
    "s_min_u32 s46, s34, 249\n"
    "s_mul_i32 s46, s46, 2560\n"
    "s_lshl_b32 s47, s50, 10\n"
    "s_add_u32 s47, s47, s46\n"
    "v_add_u32_e32 v108, s47, v250\n"
    "s_lshl_b32 s47, s50, 8\n"
    "s_add_u32 s47, s47, s46\n"
    "s_add_u32 s47, s47, 2048\n"
    "v_lshl_add_u32 v109, v24, 2, s47\n"
    "s_cmp_lg_u32 s51, 0\n"
    "s_cselect_b64 exec, -1, 1\n"
    "global_load_dwordx4 v[144:147], v108, s[86:87]\n"
    "global_load_dword v148, v109, s[86:87]\n"
    "global_load_dwordx4 v[152:155], v108, s[6:7]\n"
    "global_load_dword v156, v109, s[6:7]\n"
    "s_mov_b64 exec, -1\n"
    "s_mov_b32 s10, 69410816\n"
    "s_mov_b32 s11, 0x20000\n"
    "s_mov_b32 s49, 0\n"
    "v_and_b32_e32 v25, 7, v24\n"
    "v_lshlrev_b32_e32 v27, 7, v25\n"
    "v_bfe_u32 v26, v24, 3, 1\n"
    "v_lshl_add_u32 v27, v26, 10, v27\n"
    "v_and_b32_e32 v25, 6, v24\n"
    "v_lshrrev_b32_e32 v26, 4, v24\n"
    "v_xor_b32_e32 v25, v25, v26\n"
    "v_lshl_add_u32 v240, v25, 4, v27\n"
    "s_lshl_b32 s48, s50, 11\n"
    "v_xor_b32_e32 v241, 64, v240\n"
    "v_add_u32_e32 v72, s48, v240\n"
    "v_add_u32_e32 v73, s48, v241\n"
    "s_lshl_b32 s47, s93, 10\n"
    "v_add_u32_e32 v108, s47, v250\n"
    "v_lshrrev_b32_e32 v109, 7, v108\n"
    "v_mul_u32_u24_e32 v109, 0x8200, v109\n"
    "v_and_b32_e32 v32, 0x70, v250\n"
    "v_or_b32_e32 v109, v109, v32\n"
    "v_add_u32_e32 v108, 81920, v108\n"
    "v_mov_b64_e32 v[32:33], -1\n"
    "v_mov_b64_e32 v[34:35], -1\n"
    "ds_write_b128 v108, v[32:35] offset:0\n"
    "ds_write_b128 v108, v[32:35] offset:16\n"
    "v_and_b32_e32 v29, 6, v28\n"
    "v_xor_b32_e32 v29, v29, v24\n"
    "v_and_b32_e32 v29, 7, v29\n"
    "v_lshlrev_b32_e32 v105, 7, v28\n"
    "v_lshl_add_u32 v105, v29, 4, v105\n"
    "v_bfe_u32 v29, v24, 3, 1\n"
    "v_subrev_u32_e32 v29, s32, v29\n"
    "v_and_b32_e32 v29, 3, v29\n"
    "v_lshl_add_u32 v105, v29, 11, v105\n"
    "v_add_u32_e32 v105, 32768, v105\n"
    "v_xor_b32_e32 v106, 0x1000, v105\n"
    "v_and_b32_e32 v29, 15, v24\n"
    "v_cmp_eq_u32_e32 vcc, 15, v29\n"
    "v_mov_b32_e32 v107, 0xffff\n"
    "s_mov_b32 s58, 0x3f803f80\n"
    "s_mov_b32 s57, 0x43c00000\n"
    "v_cndmask_b32_e32 v107, -1, v107, vcc\n"
    "s_mov_b32 s56, 0xc3c00000\n"
    "s_mov_b32 s62, 0xc000c000\n"
    "s_mov_b32 s63, 0xc000c000\n"
    "s_lshl_b32 s47, s93, 10\n"
    "s_add_u32 s47, s47, 6080512\n"
    "v_add_u32_e32 v100, s47, v250\n"
    "v_and_b32_e32 v29, 15, v24\n"
    "s_cmp_ge_u32 s36, 112\n"
    "s_cselect_b32 s47, 8, 16\n"
    "v_cmp_le_u32_e32 vcc, s47, v29\n"
    "v_mov_b32_e32 v64, 0xbd80bd80\n"
    "v_mov_b32_e32 v65, 0xbdabbdab\n"
    "v_mov_b32_e32 v66, 0x392b392b\n"
    "v_cndmask_b32_e32 v64, v64, v65, vcc\n"
    "v_cndmask_b32_e32 v66, 0, v66, vcc\n"
    "s_cselect_b32 s47, 56, 64\n"
    "v_cmp_le_u32_e32 vcc, s47, v24\n"
    "v_mov_b32_e32 v65, 0x3e80bdab\n"
    "v_mov_b32_e32 v67, 0x392b\n"
    "v_cndmask_b32_e32 v65, v64, v65, vcc\n"
    "v_cndmask_b32_e32 v67, v66, v67, vcc\n"
    "s_cmp_ge_u32 s36, 120\n"
    "s_cselect_b32 s47, 15, 16\n"
    "v_cmp_ne_u32_e32 vcc, s47, v29\n"
    "v_mov_b32_e32 v68, 0x3db8aa3b\n"
    "s_lshr_b32 s47, s36, 5\n"
    "v_cndmask_b32_e32 v68, 0, v68, vcc\n"
    "s_waitcnt vmcnt(10)\n"
    "s_waitcnt lgkmcnt(0)\n"
    "s_barrier\n"
    "s_add_u32 s39, s39, 4\n"
    "s_cmp_lg_u32 s51, 0\n"
    "s_cbranch_scc1 .Lat_noj2\n"
    "ds_read_b128 v[112:115], v72 offset:0\n"
    "ds_read_b128 v[208:211], v240 offset:18432\n"
    "ds_read_b128 v[176:179], v240 offset:16384\n"
    "ds_read_b128 v[116:119], v73 offset:0\n"
    "ds_read_b128 v[212:215], v241 offset:18432\n"
    "ds_read_b128 v[180:183], v241 offset:16384\n"
    "ds_read_b128 v[120:123], v72 offset:4096\n"
    "ds_read_b128 v[216:219], v240 offset:22528\n"
    "ds_read_b128 v[184:187], v240 offset:20480\n"
    "ds_read_b128 v[124:127], v73 offset:4096\n"
    "ds_read_b128 v[220:223], v241 offset:22528\n"
    "ds_read_b128 v[188:191], v241 offset:20480\n"
    "ds_read_b128 v[128:131], v72 offset:8192\n"
    "ds_read_b128 v[224:227], v240 offset:26624\n"
    "ds_read_b128 v[192:195], v240 offset:24576\n"
    "s_waitcnt lgkmcnt(12)\n"
    "v_mfma_f32_16x16x32_bf16 v[56:59], v[208:211], v[112:115], 0\n"
    "ds_read_b128 v[132:135], v73 offset:8192\n"
    "ds_read_b128 v[228:231], v241 offset:26624\n"
    "v_mfma_f32_16x16x32_bf16 v[48:51], v[112:115], v[176:179], 0\n"
    "ds_read_b128 v[196:199], v241 offset:24576\n"
    "s_waitcnt lgkmcnt(12)\n"
    "v_mfma_f32_16x16x32_bf16 v[56:59], v[212:215], v[116:119], v[56:59]\n"
    "ds_read_b128 v[136:139], v72 offset:12288\n"
    "ds_read_b128 v[232:235], v240 offset:30720\n"
    "v_mfma_f32_16x16x32_bf16 v[48:51], v[116:119], v[180:183], v[48:51]\n"
    "ds_read_b128 v[200:203], v240 offset:28672\n"
    "s_waitcnt lgkmcnt(12)\n"
    "v_mfma_f32_16x16x32_bf16 v[56:59], v[216:219], v[120:123], v[56:59]\n"
    "ds_read_b128 v[140:143], v73 offset:12288\n"
    "ds_read_b128 v[236:239], v241 offset:30720\n"
    "v_mfma_f32_16x16x32_bf16 v[48:51], v[120:123], v[184:187], v[48:51]\n"
    "ds_read_b128 v[204:207], v241 offset:28672\n"
    "s_waitcnt lgkmcnt(12)\n"
    "v_mfma_f32_16x16x32_bf16 v[56:59], v[220:223], v[124:127], v[56:59]\n"
    "s_mul_i32 s47, s47, 33792\n"
    "s_and_b32 s48, s36, 7\n"
    "s_lshl_b32 s48, s48, 11\n"
    "v_mfma_f32_16x16x32_bf16 v[48:51], v[124:127], v[188:191], v[48:51]\n"
    "s_add_u32 s47, s47, s48\n"
    "s_and_b32 s48, s36, 8\n"
    "s_lshl_b32 s48, s48, 7\n"
    "s_waitcnt lgkmcnt(9)\n"
    "v_mfma_f32_16x16x32_bf16 v[56:59], v[224:227], v[128:131], v[56:59]\n"
    "s_add_u32 s47, s47, s48\n"
    "s_and_b32 s48, s36, 16\n"
    "s_lshl_b32 s48, s48, 10\n"
    "v_mfma_f32_16x16x32_bf16 v[48:51], v[128:131], v[192:195], v[48:51]\n"
    "s_add_u32 s47, s47, s48\n"
    "s_add_u32 s52, s47, 5242880\n"
    "s_lshl_b32 s48, s50, 9\n"
    "s_waitcnt lgkmcnt(6)\n"
    "v_mfma_f32_16x16x32_bf16 v[56:59], v[228:231], v[132:135], v[56:59]\n"
    "s_add_u32 s52, s52, s48\n"
    "v_and_b32_e32 v74, 0x1f0, v250\n"
    "v_lshrrev_b32_e32 v29, 5, v24\n"
    "v_mfma_f32_16x16x32_bf16 v[48:51], v[132:135], v[196:199], v[48:51]\n"
    "v_lshl_add_u32 v74, v29, 3, v74\n"
    "s_lshl1_add_u32 s53, s36, s50\n"
    "s_lshl4_add_u32 s53, s53, 6080512\n"
    "s_waitcnt lgkmcnt(3)\n"
    "v_mfma_f32_16x16x32_bf16 v[56:59], v[232:235], v[136:139], v[56:59]\n"
    "s_cmp_lg_u32 s51, 0\n"
    "v_mfma_f32_16x16x32_bf16 v[48:51], v[136:139], v[200:203], v[48:51]\n"
    "s_waitcnt lgkmcnt(0)\n"
    "v_mfma_f32_16x16x32_bf16 v[56:59], v[236:239], v[140:143], v[56:59]\n"
    "v_mfma_f32_16x16x32_bf16 v[48:51], v[140:143], v[204:207], v[48:51]\n"
    "s_nop 3\n"
    "v_cvt_pk_bf16_f32 v56, v56, v57\n"
    "v_cvt_pk_bf16_f32 v57, v58, v59\n"
    "s_nop 1\n"
    "v_mfma_f32_16x16x16_bf16 v[48:51], v[56:57], v[64:65], v[48:51]\n"
    "v_mfma_f32_16x16x16_bf16 v[48:51], v[56:57], v[66:67], v[48:51]\n"
    "s_nop 7\n"
    "v_mul_f32_e32 v48, v48, v68\n"
    "v_mul_f32_e32 v49, v49, v68\n"
    "v_mul_f32_e32 v50, v50, v68\n"
    "v_mul_f32_e32 v51, v51, v68\n"
    "v_cvt_pk_bf16_f32 v48, v48, v49\n"
    "v_cvt_pk_bf16_f32 v49, v50, v51\n"
    "buffer_store_dwordx2 v[48:49], v74, s[8:11], s52 offen offset:0 sc1\n"
    "s_branch .Lat_ln\n"
    ".Lat_noj2:\n"
    "s_cmp_lg_u32 s93, 3\n"
    "s_cbranch_scc1 .Lat_wn10\n"
    "s_call_b64 s[2:3], .Lat_w0\n"
    "s_call_b64 s[2:3], .Lat_w2\n"
    "s_call_b64 s[2:3], .Lat_w4\n"
    ".Lat_wn10:\n"
    "s_cmp_lg_u32 s93, 2\n"
    "s_cbranch_scc1 .Lat_wn11\n"
    "s_call_b64 s[2:3], .Lat_w1\n"
    "s_call_b64 s[2:3], .Lat_w3\n"
    "s_call_b64 s[2:3], .Lat_w5\n"
    ".Lat_wn11:\n"
    "s_cmp_lg_u32 s51, 0\n"
    ".Lat_ln:\n"
    "v_mov_b32_e32 v102, s39\n"
    "s_waitcnt vmcnt(4)\n"
    "v_dot2_f32_bf16 v24, v0, s58, 0\n"
    "v_dot2_f32_bf16 v32, v8, s58, 0\n"
    "v_dot2_f32_bf16 v40, v16, s58, 0\n"
    "v_dot2_f32_bf16 v25, v4, s58, 0\n"
    "v_dot2_f32_bf16 v33, v12, s58, 0\n"
    "v_dot2_f32_bf16 v41, v20, s58, 0\n"
    "v_dot2_f32_bf16 v26, v0, v0, 0\n"
    "v_dot2_f32_bf16 v34, v8, v8, 0\n"
    "v_dot2_f32_bf16 v42, v16, v16, 0\n"
    "v_dot2_f32_bf16 v27, v4, v4, 0\n"
    "v_dot2_f32_bf16 v35, v12, v12, 0\n"
    "v_dot2_f32_bf16 v43, v20, v20, 0\n"
    "v_dot2_f32_bf16 v24, v1, s58, v24\n"
    "v_dot2_f32_bf16 v32, v9, s58, v32\n"
    "v_dot2_f32_bf16 v40, v17, s58, v40\n"
    "v_dot2_f32_bf16 v25, v5, s58, v25\n"
    "v_dot2_f32_bf16 v33, v13, s58, v33\n"
    "v_dot2_f32_bf16 v41, v21, s58, v41\n"
    "v_dot2_f32_bf16 v26, v1, v1, v26\n"
    "v_dot2_f32_bf16 v34, v9, v9, v34\n"
    "v_dot2_f32_bf16 v42, v17, v17, v42\n"
    "v_dot2_f32_bf16 v27, v5, v5, v27\n"
    "v_dot2_f32_bf16 v35, v13, v13, v35\n"
    "v_dot2_f32_bf16 v43, v21, v21, v43\n"
    "v_dot2_f32_bf16 v24, v2, s58, v24\n"
    "v_dot2_f32_bf16 v32, v10, s58, v32\n"
    "v_dot2_f32_bf16 v40, v18, s58, v40\n"
    "v_dot2_f32_bf16 v25, v6, s58, v25\n"
    "v_dot2_f32_bf16 v33, v14, s58, v33\n"
    "v_dot2_f32_bf16 v41, v22, s58, v41\n"
    "v_dot2_f32_bf16 v26, v2, v2, v26\n"
    "v_dot2_f32_bf16 v34, v10, v10, v34\n"
    "v_dot2_f32_bf16 v42, v18, v18, v42\n"
    "v_dot2_f32_bf16 v27, v6, v6, v27\n"
    "v_dot2_f32_bf16 v35, v14, v14, v35\n"
    "v_dot2_f32_bf16 v43, v22, v22, v43\n"
    "v_dot2_f32_bf16 v24, v3, s58, v24\n"
    "v_dot2_f32_bf16 v32, v11, s58, v32\n"
    "v_dot2_f32_bf16 v40, v19, s58, v40\n"
    "v_dot2_f32_bf16 v25, v7, s58, v25\n"
    "v_dot2_f32_bf16 v33, v15, s58, v33\n"
    "v_dot2_f32_bf16 v41, v23, s58, v41\n"
    "v_dot2_f32_bf16 v26, v3, v3, v26\n"
    "v_dot2_f32_bf16 v34, v11, v11, v34\n"
    "v_dot2_f32_bf16 v42, v19, v19, v42\n"
    "v_dot2_f32_bf16 v27, v7, v7, v27\n"
    "v_dot2_f32_bf16 v35, v15, v15, v35\n"
    "v_dot2_f32_bf16 v43, v23, v23, v43\n"
    "v_and_b32_e32 v30, 0xffff0000, v7\n"
    "v_and_b32_e32 v38, 0xffff0000, v15\n"
    "v_and_b32_e32 v46, 0xffff0000, v23\n"
    "v_add_f32_e32 v24, v24, v25\n"
    "v_add_f32_e32 v32, v32, v33\n"
    "v_add_f32_e32 v40, v40, v41\n"
    "v_add_f32_e32 v26, v26, v27\n"
    "v_add_f32_e32 v34, v34, v35\n"
    "v_add_f32_e32 v42, v42, v43\n"
    "v_add_f32_dpp v25, v25, v25 quad_perm:[1,0,3,2] row_mask:0xf bank_mask:0xf bound_ctrl:1\n"
    "v_lshlrev_b32_e32 v48, 16, v0\n"
    "v_add_f32_dpp v33, v33, v33 quad_perm:[1,0,3,2] row_mask:0xf bank_mask:0xf bound_ctrl:1\n"
    "v_and_b32_e32 v49, 0xffff0000, v0\n"
    "v_add_f32_dpp v41, v41, v41 quad_perm:[1,0,3,2] row_mask:0xf bank_mask:0xf bound_ctrl:1\n"
    "v_lshlrev_b32_e32 v50, 16, v1\n"
    "v_mov_b32_dpp v30, v30 quad_perm:[3,3,3,3] row_mask:0xf bank_mask:0xf bound_ctrl:1\n"
    "v_and_b32_e32 v51, 0xffff0000, v1\n"
    "v_mov_b32_dpp v38, v38 quad_perm:[3,3,3,3] row_mask:0xf bank_mask:0xf bound_ctrl:1\n"
    "v_lshlrev_b32_e32 v52, 16, v2\n"
    "v_mov_b32_dpp v46, v46 quad_perm:[3,3,3,3] row_mask:0xf bank_mask:0xf bound_ctrl:1\n"
    "v_and_b32_e32 v53, 0xffff0000, v2\n"
    "v_add_f32_dpp v24, v24, v24 quad_perm:[1,0,3,2] row_mask:0xf bank_mask:0xf bound_ctrl:1\n"
    "v_lshlrev_b32_e32 v54, 16, v3\n"
    "v_add_f32_dpp v32, v32, v32 quad_perm:[1,0,3,2] row_mask:0xf bank_mask:0xf bound_ctrl:1\n"
    "v_and_b32_e32 v55, 0xffff0000, v3\n"
    "v_add_f32_dpp v40, v40, v40 quad_perm:[1,0,3,2] row_mask:0xf bank_mask:0xf bound_ctrl:1\n"
    "v_lshlrev_b32_e32 v56, 16, v4\n"
    "v_add_f32_dpp v26, v26, v26 quad_perm:[1,0,3,2] row_mask:0xf bank_mask:0xf bound_ctrl:1\n"
    "v_and_b32_e32 v57, 0xffff0000, v4\n"
    "v_add_f32_dpp v34, v34, v34 quad_perm:[1,0,3,2] row_mask:0xf bank_mask:0xf bound_ctrl:1\n"
    "v_lshlrev_b32_e32 v58, 16, v5\n"
    "s_cbranch_scc1 .Lat_nojf\n"
    "s_waitcnt vmcnt(0)\n"
    "buffer_store_dword v102, off, s[8:11], s53 offset:0 sc1\n"
    ".Lat_nojf:\n"
    "v_add_f32_dpp v42, v42, v42 quad_perm:[1,0,3,2] row_mask:0xf bank_mask:0xf bound_ctrl:1\n"
    "v_and_b32_e32 v59, 0xffff0000, v5\n"
    "v_add_f32_dpp v24, v24, v24 quad_perm:[2,3,0,1] row_mask:0xf bank_mask:0xf bound_ctrl:1\n"
    "v_lshlrev_b32_e32 v60, 16, v6\n"
    "v_add_f32_dpp v32, v32, v32 quad_perm:[2,3,0,1] row_mask:0xf bank_mask:0xf bound_ctrl:1\n"
    "v_and_b32_e32 v61, 0xffff0000, v6\n"
    "v_add_f32_dpp v40, v40, v40 quad_perm:[2,3,0,1] row_mask:0xf bank_mask:0xf bound_ctrl:1\n"
    "v_lshlrev_b32_e32 v62, 16, v7\n"
    "v_add_f32_dpp v26, v26, v26 quad_perm:[2,3,0,1] row_mask:0xf bank_mask:0xf bound_ctrl:1\n"
    "v_and_b32_e32 v63, 0xffff0000, v7\n"
    "v_add_f32_dpp v34, v34, v34 quad_perm:[2,3,0,1] row_mask:0xf bank_mask:0xf bound_ctrl:1\n"
    "v_lshlrev_b32_e32 v64, 16, v8\n"
    "v_add_f32_dpp v42, v42, v42 quad_perm:[2,3,0,1] row_mask:0xf bank_mask:0xf bound_ctrl:1\n"
    "v_and_b32_e32 v65, 0xffff0000, v8\n"
    "v_add_f32_dpp v24, v24, v24 row_half_mirror row_mask:0xf bank_mask:0xf bound_ctrl:1\n"
    "v_lshlrev_b32_e32 v66, 16, v9\n"
    "v_add_f32_dpp v32, v32, v32 row_half_mirror row_mask:0xf bank_mask:0xf bound_ctrl:1\n"
    "v_and_b32_e32 v67, 0xffff0000, v9\n"
    "v_add_f32_dpp v40, v40, v40 row_half_mirror row_mask:0xf bank_mask:0xf bound_ctrl:1\n"
    "v_lshlrev_b32_e32 v68, 16, v10\n"
    "v_add_f32_dpp v26, v26, v26 row_half_mirror row_mask:0xf bank_mask:0xf bound_ctrl:1\n"
    "v_and_b32_e32 v69, 0xffff0000, v10\n"
    "v_add_f32_dpp v34, v34, v34 row_half_mirror row_mask:0xf bank_mask:0xf bound_ctrl:1\n"
    "v_lshlrev_b32_e32 v70, 16, v11\n"
    "v_add_f32_dpp v42, v42, v42 row_half_mirror row_mask:0xf bank_mask:0xf bound_ctrl:1\n"
    "v_and_b32_e32 v71, 0xffff0000, v11\n"
    "v_add_f32_dpp v24, v24, v24 row_mirror row_mask:0xf bank_mask:0xf bound_ctrl:1\n"
    "v_lshlrev_b32_e32 v72, 16, v12\n"
    "v_add_f32_dpp v32, v32, v32 row_mirror row_mask:0xf bank_mask:0xf bound_ctrl:1\n"
    "v_and_b32_e32 v73, 0xffff0000, v12\n"
    "v_add_f32_dpp v40, v40, v40 row_mirror row_mask:0xf bank_mask:0xf bound_ctrl:1\n"
    "v_lshlrev_b32_e32 v74, 16, v13\n"
    "v_add_f32_dpp v26, v26, v26 row_mirror row_mask:0xf bank_mask:0xf bound_ctrl:1\n"
    "v_and_b32_e32 v75, 0xffff0000, v13\n"
    "v_add_f32_dpp v34, v34, v34 row_mirror row_mask:0xf bank_mask:0xf bound_ctrl:1\n"
    "v_lshlrev_b32_e32 v76, 16, v14\n"
    "v_add_f32_dpp v42, v42, v42 row_mirror row_mask:0xf bank_mask:0xf bound_ctrl:1\n"
    "v_and_b32_e32 v77, 0xffff0000, v14\n"
    "v_mul_f32_e32 v24, 0x3b800000, v24\n"
    "v_lshlrev_b32_e32 v78, 16, v15\n"
    "v_mul_f32_e32 v32, 0x3b800000, v32\n"
    "v_and_b32_e32 v79, 0xffff0000, v15\n"
    "v_mul_f32_e32 v40, 0x3b800000, v40\n"
    "v_lshlrev_b32_e32 v80, 16, v16\n"
    "v_mul_f32_e32 v25, 0.25, v25\n"
    "v_and_b32_e32 v81, 0xffff0000, v16\n"
    "v_mul_f32_e32 v33, 0.25, v33\n"
    "v_lshlrev_b32_e32 v82, 16, v17\n"
    "v_mul_f32_e32 v41, 0.25, v41\n"
    "v_and_b32_e32 v83, 0xffff0000, v17\n"
    "v_mul_f32_e64 v27, v24, -v24\n"
    "v_lshlrev_b32_e32 v84, 16, v18\n"
    "v_mul_f32_e64 v35, v32, -v32\n"
    "v_and_b32_e32 v85, 0xffff0000, v18\n"
    "v_mul_f32_e64 v43, v40, -v40\n"
    "v_lshlrev_b32_e32 v86, 16, v19\n"
    "v_sub_f32_e32 v25, v25, v30\n"
    "v_and_b32_e32 v87, 0xffff0000, v19\n"
    "v_sub_f32_e32 v33, v33, v38\n"
    "v_lshlrev_b32_e32 v88, 16, v20\n"
    "v_sub_f32_e32 v41, v41, v46\n"
    "v_and_b32_e32 v89, 0xffff0000, v20\n"
    "v_fmac_f32_e32 v27, 0x3b800000, v26\n"
    "v_lshlrev_b32_e32 v90, 16, v21\n"
    "v_fmac_f32_e32 v35, 0x3b800000, v34\n"
    "v_and_b32_e32 v91, 0xffff0000, v21\n"
    "v_fmac_f32_e32 v43, 0x3b800000, v42\n"
    "v_lshlrev_b32_e32 v92, 16, v22\n"
    "v_mul_f32_e32 v25, 0x3eaaaaab, v25\n"
    "v_and_b32_e32 v93, 0xffff0000, v22\n"
    "v_mul_f32_e32 v33, 0x3eaaaaab, v33\n"
    "v_lshlrev_b32_e32 v94, 16, v23\n"
    "v_mul_f32_e32 v41, 0x3eaaaaab, v41\n"
    "v_and_b32_e32 v95, 0xffff0000, v23\n"
    "v_max_f32_e32 v27, 0, v27\n"
    "v_max_f32_e32 v35, 0, v35\n"
    "v_max_f32_e32 v43, 0, v43\n"
    "s_cbranch_scc0 .Lat_bin\n"
    "s_movk_i32 s45, -2560\n"
    "s_mov_b32 s48, 81920\n"
    "v_mov_b32_e32 v172, 1\n"
    "s_waitcnt vmcnt(0)\n"
    "v_mul_u32_u24_e32 v160, 0x199a, v144\n"
    "v_mul_u32_u24_e32 v161, 0x199a, v145\n"
    "v_mul_u32_u24_e32 v162, 0x199a, v146\n"
    "v_mul_u32_u24_e32 v163, 0x199a, v147\n"
    "v_mul_u32_u24_e32 v164, 0x199a, v148\n"
    "v_bfe_u32 v160, v160, 24, 2\n"
    "v_bfe_u32 v161, v161, 24, 2\n"
    "v_bfe_u32 v162, v162, 24, 2\n"
    "v_bfe_u32 v163, v163, 24, 2\n"
    "v_bfe_u32 v164, v164, 24, 2\n"
    "v_lshl_add_u32 v166, v160, 2, s48\n"
    "v_lshl_add_u32 v167, v161, 2, s48\n"
    "v_lshl_add_u32 v168, v162, 2, s48\n"
    "v_lshl_add_u32 v169, v163, 2, s48\n"
    "v_lshl_add_u32 v170, v164, 2, s48\n"
    "ds_add_rtn_u32 v166, v166, v172 offset:4096\n"
    "ds_add_rtn_u32 v167, v167, v172 offset:4096\n"
    "ds_add_rtn_u32 v168, v168, v172 offset:4096\n"
    "ds_add_rtn_u32 v169, v169, v172 offset:4096\n"
    "ds_add_rtn_u32 v170, v170, v172 offset:4096\n"
    "v_mad_i32_i24 v144, v160, s45, v144\n"
    "v_mad_i32_i24 v145, v161, s45, v145\n"
    "v_mad_i32_i24 v146, v162, s45, v146\n"
    "v_mad_i32_i24 v147, v163, s45, v147\n"
    "v_mad_i32_i24 v148, v164, s45, v148\n"
    "v_lshl_or_b32 v144, v144, 18, v152\n"
    "v_lshl_or_b32 v145, v145, 18, v153\n"
    "v_lshl_or_b32 v146, v146, 18, v154\n"
    "v_lshl_or_b32 v147, v147, 18, v155\n"
    "v_lshl_or_b32 v148, v148, 18, v156\n"
    "v_lshl_add_u32 v160, v160, 10, s48\n"
    "v_lshl_add_u32 v161, v161, 10, s48\n"
    "v_lshl_add_u32 v162, v162, 10, s48\n"
    "v_lshl_add_u32 v163, v163, 10, s48\n"
    "v_lshl_add_u32 v164, v164, 10, s48\n"
    ".Lat_bin:\n"
    "v_cndmask_b32_e64 v25, v24, v25, s[62:63]\n"
    "v_cndmask_b32_e64 v33, v32, v33, s[62:63]\n"
    "v_cndmask_b32_e64 v41, v40, v41, s[62:63]\n"
    "v_add_f32_e32 v27, 0x3727c5ac, v27\n"
    "v_add_f32_e32 v35, 0x3727c5ac, v35\n"
    "v_add_f32_e32 v43, 0x3727c5ac, v43\n"
    "v_rsq_f32_e32 v28, v27\n"
    "v_rsq_f32_e32 v36, v35\n"
    "v_rsq_f32_e32 v44, v43\n"
    "v_fma_f32 v29, -v24, v28, s57\n"
    "v_fma_f32 v37, -v32, v36, s57\n"
    "v_fma_f32 v45, -v40, v44, s57\n"
    "v_fma_f32 v31, -v25, v28, s57\n"
    "v_fma_f32 v39, -v33, v36, s57\n"
    "v_fma_f32 v47, -v41, v44, s57\n"
    "v_sub_f32_e32 v24, v29, v31\n"
    "v_sub_f32_e32 v32, v37, v39\n"
    "v_sub_f32_e32 v40, v45, v47\n"
    "v_pk_fma_f32 v[48:49], v[48:49], v[28:29], v[28:29] op_sel:[0,0,1] op_sel_hi:[1,0,1]\n"
    "v_pk_fma_f32 v[50:51], v[50:51], v[28:29], v[28:29] op_sel:[0,0,1] op_sel_hi:[1,0,1]\n"
    "v_pk_fma_f32 v[52:53], v[52:53], v[28:29], v[28:29] op_sel:[0,0,1] op_sel_hi:[1,0,1]\n"
    "v_pk_fma_f32 v[54:55], v[54:55], v[28:29], v[28:29] op_sel:[0,0,1] op_sel_hi:[1,0,1]\n"
    "v_pk_fma_f32 v[56:57], v[56:57], v[28:29], v[30:31] op_sel:[0,0,1] op_sel_hi:[1,0,1]\n"
    "v_pk_fma_f32 v[58:59], v[58:59], v[28:29], v[30:31] op_sel:[0,0,1] op_sel_hi:[1,0,1]\n"
    "v_pk_fma_f32 v[60:61], v[60:61], v[28:29], v[30:31] op_sel:[0,0,1] op_sel_hi:[1,0,1]\n"
    "v_pk_fma_f32 v[62:63], v[62:63], v[28:29], v[30:31] op_sel:[0,0,1] op_sel_hi:[1,0,1]\n"
    "v_pk_add_f32 v[48:49], v[48:49], s[56:57] op_sel_hi:[1,0]\n"
    "v_pk_add_f32 v[50:51], v[50:51], s[56:57] op_sel_hi:[1,0]\n"
    "v_pk_add_f32 v[52:53], v[52:53], s[56:57] op_sel_hi:[1,0]\n"
    "v_pk_add_f32 v[54:55], v[54:55], s[56:57] op_sel_hi:[1,0]\n"
    "v_pk_add_f32 v[56:57], v[56:57], s[56:57] op_sel_hi:[1,0]\n"
    "v_pk_add_f32 v[58:59], v[58:59], s[56:57] op_sel_hi:[1,0]\n"
    "v_pk_add_f32 v[60:61], v[60:61], s[56:57] op_sel_hi:[1,0]\n"
    "v_pk_add_f32 v[62:63], v[62:63], s[56:57] op_sel_hi:[1,0]\n"
    "v_cvt_pk_bf16_f32 v0, v48, v49\n"
    "v_cvt_pk_bf16_f32 v1, v50, v51\n"
    "v_cvt_pk_bf16_f32 v2, v52, v53\n"
    "v_cvt_pk_bf16_f32 v3, v54, v55\n"
    "v_cvt_pk_bf16_f32 v4, v56, v57\n"
    "v_cvt_pk_bf16_f32 v5, v58, v59\n"
    "v_cvt_pk_bf16_f32 v6, v60, v61\n"
    "v_cvt_pk_bf16_f32 v7, v62, v63\n"
    "buffer_store_dwordx4 v[0:3], v104, s[8:11], s35 offen offset:0 sc1\n"
    "v_bfi_b32 v7, v107, v7, 1.0\n"
    "ds_write_b128 v105, v[0:3] offset:0\n"
    "buffer_store_dwordx4 v[4:7], v104, s[8:11], s35 offen offset:256 sc1\n"
    "v_pk_fma_f32 v[64:65], v[64:65], v[36:37], v[36:37] op_sel:[0,0,1] op_sel_hi:[1,0,1]\n"
    "v_pk_fma_f32 v[66:67], v[66:67], v[36:37], v[36:37] op_sel:[0,0,1] op_sel_hi:[1,0,1]\n"
    "v_pk_fma_f32 v[68:69], v[68:69], v[36:37], v[36:37] op_sel:[0,0,1] op_sel_hi:[1,0,1]\n"
    "v_pk_fma_f32 v[70:71], v[70:71], v[36:37], v[36:37] op_sel:[0,0,1] op_sel_hi:[1,0,1]\n"
    "v_pk_fma_f32 v[72:73], v[72:73], v[36:37], v[38:39] op_sel:[0,0,1] op_sel_hi:[1,0,1]\n"
    "v_pk_fma_f32 v[74:75], v[74:75], v[36:37], v[38:39] op_sel:[0,0,1] op_sel_hi:[1,0,1]\n"
    "v_pk_fma_f32 v[76:77], v[76:77], v[36:37], v[38:39] op_sel:[0,0,1] op_sel_hi:[1,0,1]\n"
    "v_pk_fma_f32 v[78:79], v[78:79], v[36:37], v[38:39] op_sel:[0,0,1] op_sel_hi:[1,0,1]\n"
    "v_pk_add_f32 v[64:65], v[64:65], s[56:57] op_sel_hi:[1,0]\n"
    "v_pk_add_f32 v[66:67], v[66:67], s[56:57] op_sel_hi:[1,0]\n"
    "v_pk_add_f32 v[68:69], v[68:69], s[56:57] op_sel_hi:[1,0]\n"
    "v_pk_add_f32 v[70:71], v[70:71], s[56:57] op_sel_hi:[1,0]\n"
    "v_pk_add_f32 v[72:73], v[72:73], s[56:57] op_sel_hi:[1,0]\n"
    "v_pk_add_f32 v[74:75], v[74:75], s[56:57] op_sel_hi:[1,0]\n"
    "v_pk_add_f32 v[76:77], v[76:77], s[56:57] op_sel_hi:[1,0]\n"
    "v_pk_add_f32 v[78:79], v[78:79], s[56:57] op_sel_hi:[1,0]\n"
    "v_cvt_pk_bf16_f32 v8, v64, v65\n"
    "v_cvt_pk_bf16_f32 v9, v66, v67\n"
    "v_cvt_pk_bf16_f32 v10, v68, v69\n"
    "v_cvt_pk_bf16_f32 v11, v70, v71\n"
    "v_cvt_pk_bf16_f32 v12, v72, v73\n"
    "v_cvt_pk_bf16_f32 v13, v74, v75\n"
    "v_cvt_pk_bf16_f32 v14, v76, v77\n"
    "v_cvt_pk_bf16_f32 v15, v78, v79\n"
    "buffer_store_dwordx4 v[8:11], v104, s[8:11], s54 offen offset:0 sc1\n"
    "v_bfi_b32 v15, v107, v15, 1.0\n"
    "ds_write_b128 v105, v[8:11] offset:8192\n"
    "buffer_store_dwordx4 v[12:15], v104, s[8:11], s54 offen offset:256 sc1\n"
    "v_pk_fma_f32 v[80:81], v[80:81], v[44:45], v[44:45] op_sel:[0,0,1] op_sel_hi:[1,0,1]\n"
    "v_pk_fma_f32 v[82:83], v[82:83], v[44:45], v[44:45] op_sel:[0,0,1] op_sel_hi:[1,0,1]\n"
    "v_pk_fma_f32 v[84:85], v[84:85], v[44:45], v[44:45] op_sel:[0,0,1] op_sel_hi:[1,0,1]\n"
    "v_pk_fma_f32 v[86:87], v[86:87], v[44:45], v[44:45] op_sel:[0,0,1] op_sel_hi:[1,0,1]\n"
    "v_pk_fma_f32 v[88:89], v[88:89], v[44:45], v[46:47] op_sel:[0,0,1] op_sel_hi:[1,0,1]\n"
    "v_pk_fma_f32 v[90:91], v[90:91], v[44:45], v[46:47] op_sel:[0,0,1] op_sel_hi:[1,0,1]\n"
    "v_pk_fma_f32 v[92:93], v[92:93], v[44:45], v[46:47] op_sel:[0,0,1] op_sel_hi:[1,0,1]\n"
    "v_pk_fma_f32 v[94:95], v[94:95], v[44:45], v[46:47] op_sel:[0,0,1] op_sel_hi:[1,0,1]\n"
    "v_pk_add_f32 v[80:81], v[80:81], s[56:57] op_sel_hi:[1,0]\n"
    "v_pk_add_f32 v[82:83], v[82:83], s[56:57] op_sel_hi:[1,0]\n"
    "v_pk_add_f32 v[84:85], v[84:85], s[56:57] op_sel_hi:[1,0]\n"
    "v_pk_add_f32 v[86:87], v[86:87], s[56:57] op_sel_hi:[1,0]\n"
    "v_pk_add_f32 v[88:89], v[88:89], s[56:57] op_sel_hi:[1,0]\n"
    "v_pk_add_f32 v[90:91], v[90:91], s[56:57] op_sel_hi:[1,0]\n"
    "v_pk_add_f32 v[92:93], v[92:93], s[56:57] op_sel_hi:[1,0]\n"
    "v_pk_add_f32 v[94:95], v[94:95], s[56:57] op_sel_hi:[1,0]\n"
    "v_cvt_pk_bf16_f32 v16, v80, v81\n"
    "v_cvt_pk_bf16_f32 v17, v82, v83\n"
    "v_cvt_pk_bf16_f32 v18, v84, v85\n"
    "v_cvt_pk_bf16_f32 v19, v86, v87\n"
    "v_cvt_pk_bf16_f32 v20, v88, v89\n"
    "v_cvt_pk_bf16_f32 v21, v90, v91\n"
    "v_cvt_pk_bf16_f32 v22, v92, v93\n"
    "v_cvt_pk_bf16_f32 v23, v94, v95\n"
    "buffer_store_dwordx4 v[16:19], v104, s[8:11], s60 offen offset:0 sc1\n"
    "v_bfi_b32 v23, v107, v23, 1.0\n"
    "ds_write_b128 v105, v[16:19] offset:16384\n"
    "buffer_store_dwordx4 v[20:23], v104, s[8:11], s60 offen offset:256 sc1\n"
    "v_pk_add_f32 v[56:57], v[56:57], v[24:25] op_sel_hi:[1,0]\n"
    "v_pk_add_f32 v[58:59], v[58:59], v[24:25] op_sel_hi:[1,0]\n"
    "v_pk_add_f32 v[60:61], v[60:61], v[24:25] op_sel_hi:[1,0]\n"
    "v_pk_add_f32 v[62:63], v[62:63], v[24:25] op_sel_hi:[1,0]\n"
    "v_cvt_pk_bf16_f32 v56, v56, v57\n"
    "v_cvt_pk_bf16_f32 v57, v58, v59\n"
    "v_cvt_pk_bf16_f32 v58, v60, v61\n"
    "v_cvt_pk_bf16_f32 v59, v62, v63\n"
    "ds_write_b128 v106, v[56:59] offset:0\n"
    "v_pk_add_f32 v[72:73], v[72:73], v[32:33] op_sel_hi:[1,0]\n"
    "v_pk_add_f32 v[74:75], v[74:75], v[32:33] op_sel_hi:[1,0]\n"
    "v_pk_add_f32 v[76:77], v[76:77], v[32:33] op_sel_hi:[1,0]\n"
    "v_pk_add_f32 v[78:79], v[78:79], v[32:33] op_sel_hi:[1,0]\n"
    "v_cvt_pk_bf16_f32 v72, v72, v73\n"
    "v_cvt_pk_bf16_f32 v73, v74, v75\n"
    "v_cvt_pk_bf16_f32 v74, v76, v77\n"
    ".long 0xbedf00ff\n"
    ".Lat_w0:\n"
    "s_setpc_b64 s[2:3]\n"
    "v_cvt_pk_bf16_f32 v75, v78, v79\n"
    "ds_write_b128 v106, v[72:75] offset:8192\n"
    "v_pk_add_f32 v[88:89], v[88:89], v[40:41] op_sel_hi:[1,0]\n"
    "v_pk_add_f32 v[90:91], v[90:91], v[40:41] op_sel_hi:[1,0]\n"
    "v_pk_add_f32 v[92:93], v[92:93], v[40:41] op_sel_hi:[1,0]\n"
    "v_pk_add_f32 v[94:95], v[94:95], v[40:41] op_sel_hi:[1,0]\n"
    "v_cvt_pk_bf16_f32 v88, v88, v89\n"
    "v_cvt_pk_bf16_f32 v89, v90, v91\n"
    "v_cvt_pk_bf16_f32 v90, v92, v93\n"
    "v_cvt_pk_bf16_f32 v91, v94, v95\n"
    "ds_write_b128 v106, v[88:91] offset:16384\n"
    "s_cbranch_scc0 .Lat_bin2\n"
    "s_waitcnt lgkmcnt(0)\n"
    "v_min_i32_e32 v166, 254, v166\n"
    "v_min_i32_e32 v167, 254, v167\n"
    "v_min_i32_e32 v168, 254, v168\n"
    "v_min_i32_e32 v169, 254, v169\n"
    "v_min_i32_e32 v170, 254, v170\n"
    "v_lshl_add_u32 v160, v166, 2, v160\n"
    "v_lshl_add_u32 v161, v167, 2, v161\n"
    "v_lshl_add_u32 v162, v168, 2, v162\n"
    "v_lshl_add_u32 v163, v169, 2, v163\n"
    "v_lshl_add_u32 v164, v170, 2, v164\n"
    "ds_write_b32 v160, v144 offset:4\n"
    "ds_write_b32 v161, v145 offset:4\n"
    "ds_write_b32 v162, v146 offset:4\n"
    "ds_write_b32 v163, v147 offset:4\n"
    "ds_write_b32 v164, v148 offset:4\n"
    ".Lat_bin2:\n"
    "s_waitcnt lgkmcnt(0)\n"
    "s_barrier\n"
    "ds_read_b128 a[0:3], v240 offset:32768\n"
    "ds_read_b128 a[32:35], v240 offset:40960\n"
    "ds_read_b128 a[64:67], v240 offset:49152\n"
    "ds_read_b128 a[4:7], v241 offset:32768\n"
    "ds_read_b128 a[36:39], v241 offset:40960\n"
    "ds_read_b128 a[68:71], v241 offset:49152\n"
    "ds_read_b128 a[8:11], v240 offset:34816\n"
    "ds_read_b128 a[40:43], v240 offset:43008\n"
    "ds_read_b128 a[72:75], v240 offset:51200\n"
    "ds_read_b128 a[12:15], v241 offset:34816\n"
    "ds_read_b128 a[44:47], v241 offset:43008\n"
    "ds_read_b128 a[76:79], v241 offset:51200\n"
    "buffer_load_dword v101, v100, s[8:11], 0 offen offset:0 sc1\n"
    "ds_read_b128 a[16:19], v240 offset:36864\n"
    "ds_read_b128 a[48:51], v240 offset:45056\n"
    "ds_read_b128 a[80:83], v240 offset:53248\n"
    "ds_read_b128 a[20:23], v241 offset:36864\n"
    "ds_read_b128 a[52:55], v241 offset:45056\n"
    "ds_read_b128 a[84:87], v241 offset:53248\n"
    "ds_read_b128 a[24:27], v240 offset:38912\n"
    "ds_read_b128 a[56:59], v240 offset:47104\n"
    "ds_read_b128 a[88:91], v240 offset:55296\n"
    "ds_read_b128 a[28:31], v241 offset:38912\n"
    "ds_read_b128 a[60:63], v241 offset:47104\n"
    "ds_read_b128 a[92:95], v241 offset:55296\n"
    "ds_read_b128 v[40:43], v108 offset:0\n"
    "s_mul_i32 s52, s93, 33792\n"
    "s_add_u32 s52, s52, 5242880\n"
    "s_lshl_b32 s61, s32, 12\n"
    "s_lshl4_add_u32 s46, s34, 6076416\n"
    "s_cmp_eq_u32 s93, 0\n"
    "s_cselect_b32 s46, s46, 2.0\n"
    "s_lshl_b32 s48, s34, 7\n"
    "s_add_u32 s48, s48, 68206592\n"
    "s_lshl_b32 s47, s93, 12\n"
    "s_waitcnt vmcnt(1)\n"
    "s_waitcnt lgkmcnt(0)\n"
    "s_barrier\n"
    "buffer_store_dword v102, off, s[8:11], s46 offset:0 sc1\n"
    "buffer_store_dwordx4 v[40:43], v109, s[8:11], s48 offen offset:0 sc1\n"
    "s_lshl4_add_u32 s46, s34, s47\n"
    "s_add_u32 s46, s46, 6084608\n"
    "s_waitcnt vmcnt(2)\n"
    "v_cmp_ne_u32_e32 vcc, s39, v101\n"
    "s_nop 4\n"
    "s_cbranch_vccz .Lat_apoll_ret\n"
    ".Lat_apoll:\n"
    "s_mov_b32 s53, 0x400000\n"
    ".Lat_apoll_l:\n"
    "s_sleep 1\n"
    "buffer_load_dword v101, v100, s[8:11], 0 offen offset:0 sc1\n"
    "s_waitcnt vmcnt(0)\n"
    "v_cmp_ne_u32_e32 vcc, s39, v101\n"
    "s_sub_u32 s53, s53, 1\n"
    "s_cbranch_scc1 .Lat_apoll_ret\n"
    "s_nop 2\n"
    "s_cbranch_vccnz .Lat_apoll_l\n"
    ".Lat_apoll_ret:\n"
    "s_add_u32 s47, s52, s61\n"
    "buffer_load_dwordx4 v[112:115], v250, s[8:11], s47 offen offset:0 sc1\n"
    "buffer_load_dwordx4 v[116:119], v250, s[8:11], s47 offen offset:1024 sc1\n"
    "buffer_load_dwordx4 v[128:131], v250, s[8:11], s47 offen offset:2048 sc1\n"
    "s_add_u32 s61, s61, 0x1000\n"
    "buffer_load_dwordx4 v[132:135], v250, s[8:11], s47 offen offset:3072 sc1\n"
    "s_bitset0_b32 s61, 14\n"
    "s_add_u32 s47, s52, s61\n"
    "buffer_load_dwordx4 v[144:147], v250, s[8:11], s47 offen offset:0 sc1\n"
    "buffer_load_dwordx4 v[148:151], v250, s[8:11], s47 offen offset:1024 sc1\n"
    "buffer_load_dwordx4 v[160:163], v250, s[8:11], s47 offen offset:2048 sc1\n"
    "s_add_u32 s61, s61, 0x1000\n"
    "buffer_load_dwordx4 v[164:167], v250, s[8:11], s47 offen offset:3072 sc1\n"
    "s_bitset0_b32 s61, 14\n"
    "s_add_u32 s47, s52, s61\n"
    "buffer_load_dwordx4 v[176:179], v250, s[8:11], s47 offen offset:0 sc1\n"
    "buffer_load_dwordx4 v[180:183], v250, s[8:11], s47 offen offset:1024 sc1\n"
    "buffer_load_dwordx4 v[192:195], v250, s[8:11], s47 offen offset:2048 sc1\n"
    "s_add_u32 s61, s61, 0x1000\n"
    "v_lshrrev_b32_e32 v8, 4, v250\n"
    "v_and_b32_e32 v10, 0x70, v251\n"
    "v_bfe_u32 v9, v8, 3, 1\n"
    "v_lshl_add_u32 v248, v9, 9, v10\n"
    "v_lshrrev_b32_e32 v9, 4, v8\n"
    "v_lshl_add_u32 v248, v9, 12, v248\n"
    "s_mul_i32 s43, s50, 3\n"
    "s_xor_b32 s43, s43, s51\n"
    "s_lshl_b32 s43, s43, 15\n"
    "s_add_u32 s43, s43, 32768\n"
    "s_mul_i32 s42, s93, 5120\n"
    "s_lshl_b32 s45, s50, 8\n"
    "s_mul_i32 s40, s32, 1277952\n"
    "s_lshl_b32 s41, s51, 14\n"
    "s_add_u32 s40, s40, s41\n"
    "s_add_u32 s40, s40, s45\n"
    "s_add_u32 s41, s40, 128\n"
    "v_bfe_u32 v9, v8, 2, 3\n"
    "v_lshlrev_b32_e32 v244, 7, v9\n"
    "v_lshrrev_b32_e32 v10, 5, v8\n"
    "v_lshl_add_u32 v244, v10, 10, v244\n"
    "v_and_b32_e32 v9, 6, v9\n"
    "v_bfe_u32 v10, v8, 1, 1\n"
    "v_xor_b32_e32 v9, v9, v10\n"
    "v_lshl_add_u32 v244, v9, 4, v244\n"
    "v_and_b32_e32 v10, 1, v8\n"
    "v_lshl_add_u32 v244, v10, 3, v244\n"
    "s_lshl_b32 s45, s51, 16\n"
    "s_add_u32 s45, s45, 32768\n"
    "v_add_u32_e32 v244, s45, v244\n"
    "v_xor_b32_e32 v245, 32, v244\n"
    "v_xor_b32_e32 v246, 64, v244\n"
    "v_xor_b32_e32 v247, 0x60, v244\n"
    "s_lshl_b32 s45, s51, 15\n"
    "s_add_u32 s48, s45, 32768\n"
    "s_sub_u32 s45, 131072, s45\n"
    "v_add_u32_e32 v242, s45, v240\n"
    "v_add_u32_e32 v240, s48, v240\n"
    "v_xor_b32_e32 v241, 64, v240\n"
    "v_xor_b32_e32 v243, 64, v242\n"
    "s_xor_b32 s45, s93, 2\n"
    "s_mul_i32 s45, s45, 5120\n"
    "v_lshl_add_u32 v249, v8, 2, s45\n"
    "s_lshl_b32 s45, s93, 11\n"
    "v_add_u32_e32 v99, s45, v250\n"
    "v_add_u32_e32 v98, 11345920, v99\n"
    "v_and_b32_e32 v252, 0x30, v250\n"
    "v_add_u32_e32 v252, 0x400, v252\n"
    "s_mul_i32 s59, s32, 999\n"
    "s_andn2_b32 s59, s59, 63\n"
    "s_sub_u32 s48, 3968, s59\n"
    "v_cmp_le_u32_e32 vcc, 0x80, v250\n"
    "v_mov_b32_e32 v80, s48\n"
    "s_add_u32 s59, s59, 6076416\n"
    "v_cndmask_b32_e32 v83, v250, v80, vcc\n"
    "v_bfe_u32 v253, v250, 4, 2\n"
    "v_max_u32_e32 v253, 1, v253\n"
    "v_add_u32_e32 v253, s32, v253\n"
    "v_and_b32_e32 v253, 3, v253\n"
    "v_lshlrev_b32_e32 v253, 9, v253\n"
    "v_mov_b32_e32 v84, s39\n"
    "v_lshrrev_b32_e32 v86, 2, v250\n"
    "v_min_u32_e32 v86, 124, v86\n"
    "s_lshl2_add_u32 s58, s34, s93\n"
    "s_lshl_b32 s58, s58, 7\n"
    "s_add_u32 s58, s58, 5910528\n"
    "s_lshl_b32 s45, s50, 13\n"
    "v_add_u32_e32 v85, s45, v250\n"
    "s_lshl3_add_u32 s45, s45, 49152\n"
    "v_add_u32_e32 v255, s45, v250\n"
    "v_lshrrev_b32_e32 v105, 7, v250\n"
    "v_and_b32_e32 v104, 0x70, v251\n"
    "v_lshl_add_u32 v104, v105, 9, v104\n"
    "v_add_u32_e32 v105, 0xc00, v104\n"
    "v_add_u32_e32 v106, 0x1800, v104\n"
    "v_add_u32_e32 v107, 0x2400, v104\n"
    "s_lshl_b32 s57, s93, 7\n"
    "s_lshl4_add_u32 s56, s57, 24576\n"
    "s_add_u32 s57, s57, 5111808\n"
    "s_mul_i32 s45, s51, 32768\n"
    "s_add_u32 s45, s45, 8192\n"
    "v_subrev_u32_e32 v46, s45, v240\n"
    "v_subrev_u32_e32 v47, s45, v241\n"
    "s_mul_i32 s45, s51, 61440\n"
    "s_add_u32 s45, s45, 8192\n"
    "v_subrev_u32_e32 v4, s45, v244\n"
    "v_subrev_u32_e32 v5, s45, v245\n"
    "v_subrev_u32_e32 v6, s45, v246\n"
    "v_subrev_u32_e32 v7, s45, v247\n"
    "s_waitcnt vmcnt(9)\n"
    "v_mfma_f32_16x16x32_bf16 v[24:27], v[112:115], a[0:3], 0\n"
    "buffer_load_dwordx4 v[196:199], v250, s[8:11], s47 offen offset:3072 sc1\n"
    "s_bitset0_b32 s61, 14\n"
    "v_mfma_f32_16x16x32_bf16 v[28:31], v[116:119], a[0:3], 0\n"
    "v_accvgpr_write_b32 a244, 0\n"
    "v_mfma_f32_16x16x32_bf16 v[48:51], v[112:115], a[32:35], 0\n"
    "v_accvgpr_write_b32 a245, 0\n"
    "v_mfma_f32_16x16x32_bf16 v[52:55], v[116:119], a[32:35], 0\n"
    "s_add_u32 s47, s52, s61\n"
    "buffer_load_dwordx4 v[208:211], v250, s[8:11], s47 offen offset:0 sc1\n"
    "v_mfma_f32_16x16x32_bf16 v[64:67], v[112:115], a[64:67], 0\n"
    "v_bfe_u32 v0, v250, 4, 2\n"
    "v_mfma_f32_16x16x32_bf16 v[68:71], v[116:119], a[64:67], 0\n"
    "s_mov_b32 s18, 5120000\n"
    "v_accvgpr_write_b32 a246, 0\n"
    "s_waitcnt vmcnt(9)\n"
    "v_mfma_f32_16x16x32_bf16 v[24:27], v[128:131], a[4:7], v[24:27]\n"
    "buffer_load_dwordx4 v[212:215], v250, s[8:11], s47 offen offset:1024 sc1\n"
    "s_add_u32 s52, s52, 0x4000\n"
    "v_mfma_f32_16x16x32_bf16 v[28:31], v[132:135], a[4:7], v[28:31]\n"
    "buffer_store_dword v102, off, s[8:11], s46 offset:0 sc1\n"
    "v_accvgpr_write_b32 a247, 0\n"
    "v_mfma_f32_16x16x32_bf16 v[48:51], v[128:131], a[36:39], v[48:51]\n"
    "v_bfe_u32 v1, v250, 6, 2\n"
    "v_accvgpr_write_b32 a248, 0\n"
    "v_mfma_f32_16x16x32_bf16 v[52:55], v[132:135], a[36:39], v[52:55]\n"
    "buffer_load_dwordx4 v[224:227], v250, s[8:11], s47 offen offset:2048 sc1\n"
    "s_add_u32 s61, s61, 0x1000\n"
    "v_mfma_f32_16x16x32_bf16 v[64:67], v[128:131], a[68:71], v[64:67]\n"
    "v_mov_b32_e32 v2, 0x3f80\n"
    "v_accvgpr_write_b32 a249, 0\n"
    "v_mfma_f32_16x16x32_bf16 v[68:71], v[132:135], a[68:71], v[68:71]\n"
    "s_mov_b32 s19, 0x20000\n"
    "s_waitcnt vmcnt(10)\n"
    "v_mfma_f32_16x16x32_bf16 v[24:27], v[144:147], a[8:11], v[24:27]\n"
    "buffer_load_dwordx4 v[228:231], v250, s[8:11], s47 offen offset:3072 sc1\n"
    "s_bitset0_b32 s61, 14\n"
    "v_mfma_f32_16x16x32_bf16 v[28:31], v[148:151], a[8:11], v[28:31]\n"
    "v_accvgpr_write_b32 a250, 0\n"
    "v_mfma_f32_16x16x32_bf16 v[48:51], v[144:147], a[40:43], v[48:51]\n"
    "s_mov_b64 s[20:21], s[8:9]\n"
    "v_accvgpr_write_b32 a251, 0\n"
    "v_mfma_f32_16x16x32_bf16 v[52:55], v[148:151], a[40:43], v[52:55]\n"
    "s_add_u32 s47, s52, s61\n"
    "buffer_load_dwordx4 v[120:123], v250, s[8:11], s47 offen offset:0 sc1\n"
    "v_mfma_f32_16x16x32_bf16 v[64:67], v[144:147], a[72:75], v[64:67]\n"
    "v_cmp_eq_u32_e32 vcc, 3, v0\n"
    "s_nop 1\n"
    "v_cndmask_b32_e64 v2, v2, 1.0, vcc\n"
    "v_mfma_f32_16x16x32_bf16 v[68:71], v[148:151], a[72:75], v[68:71]\n"
    "v_accvgpr_write_b32 a252, 0\n"
    "s_waitcnt vmcnt(10)\n"
    "v_mfma_f32_16x16x32_bf16 v[24:27], v[160:163], a[12:15], v[24:27]\n"
    "buffer_load_dwordx4 v[124:127], v250, s[8:11], s47 offen offset:1024 sc1\n"
    "v_accvgpr_write_b32 a253, 0\n"
    "v_mfma_f32_16x16x32_bf16 v[28:31], v[164:167], a[12:15], v[28:31]\n"
    "s_mov_b32 s22, 5242880\n"
    "v_accvgpr_write_b32 a254, 0\n"
    "v_mfma_f32_16x16x32_bf16 v[48:51], v[160:163], a[44:47], v[48:51]\n"
    "v_cmp_eq_u32_e32 vcc, 0, v1\n"
    "s_nop 1\n"
    "v_cndmask_b32_e32 v3, 0, v2, vcc\n"
    "v_mfma_f32_16x16x32_bf16 v[52:55], v[164:167], a[44:47], v[52:55]\n"
    "buffer_load_dwordx4 v[136:139], v250, s[8:11], s47 offen offset:2048 sc1\n"
    "s_add_u32 s61, s61, 0x1000\n"
    "v_mfma_f32_16x16x32_bf16 v[64:67], v[160:163], a[76:79], v[64:67]\n"
    "v_accvgpr_write_b32 a232, v3\n"
    "v_accvgpr_write_b32 a255, 0\n"
    "v_mfma_f32_16x16x32_bf16 v[68:71], v[164:167], a[76:79], v[68:71]\n"
    "s_mov_b64 s[24:25], s[28:29]\n"
    "v_accvgpr_write_b32 a192, 0\n"
    "s_waitcnt vmcnt(10)\n"
    "v_mfma_f32_16x16x32_bf16 v[24:27], v[176:179], a[16:19], v[24:27]\n"
    "buffer_load_dwordx4 v[140:143], v250, s[8:11], s47 offen offset:3072 sc1\n"
    "s_bitset0_b32 s61, 14\n"
    "v_mfma_f32_16x16x32_bf16 v[28:31], v[180:183], a[16:19], v[28:31]\n"
    "v_accvgpr_write_b32 a193, 0\n"
    "v_mfma_f32_16x16x32_bf16 v[48:51], v[176:179], a[48:51], v[48:51]\n"
    "v_cmp_eq_u32_e32 vcc, 1, v1\n"
    "s_nop 1\n"
    "v_cndmask_b32_e32 v3, 0, v2, vcc\n"
    "v_mfma_f32_16x16x32_bf16 v[52:55], v[180:183], a[48:51], v[52:55]\n"
    "s_add_u32 s47, s52, s61\n"
    "buffer_load_dwordx4 v[152:155], v250, s[8:11], s47 offen offset:0 sc1\n"
    "v_mfma_f32_16x16x32_bf16 v[64:67], v[176:179], a[80:83], v[64:67]\n"
    "buffer_load_dword v80, v83, s[8:11], s59 offen offset:0 sc1\n"
    "v_accvgpr_write_b32 a194, 0\n"
    "v_mfma_f32_16x16x32_bf16 v[68:71], v[180:183], a[80:83], v[68:71]\n"
    "s_movk_i32 s26, 20000\n"
    "v_accvgpr_write_b32 a195, 0\n"
    "s_waitcnt vmcnt(11)\n"
    "v_mfma_f32_16x16x32_bf16 v[24:27], v[192:195], a[20:23], v[24:27]\n"
    "buffer_load_dwordx4 v[156:159], v250, s[8:11], s47 offen offset:1024 sc1\n"
    "v_accvgpr_write_b32 a196, 0\n"
    "v_mfma_f32_16x16x32_bf16 v[28:31], v[196:199], a[20:23], v[28:31]\n"
    "v_accvgpr_write_b32 a233, v3\n"
    "v_mfma_f32_16x16x32_bf16 v[48:51], v[192:195], a[52:55], v[48:51]\n"
    "s_bfe_u32 s88, s92, 0x20001\n"
    "v_accvgpr_write_b32 a197, 0\n"
    "v_mfma_f32_16x16x32_bf16 v[52:55], v[196:199], a[52:55], v[52:55]\n"
    "buffer_load_dwordx4 v[168:171], v250, s[8:11], s47 offen offset:2048 sc1\n"
    "s_add_u32 s61, s61, 0x1000\n"
    "v_mfma_f32_16x16x32_bf16 v[64:67], v[192:195], a[84:87], v[64:67]\n"
    "v_cmp_eq_u32_e32 vcc, 2, v1\n"
    "s_nop 1\n"
    "v_cndmask_b32_e32 v3, 0, v2, vcc\n"
    "v_mfma_f32_16x16x32_bf16 v[68:71], v[196:199], a[84:87], v[68:71]\n"
    "v_accvgpr_write_b32 a198, 0\n"
    "s_waitcnt vmcnt(11)\n"
    "v_mfma_f32_16x16x32_bf16 v[24:27], v[208:211], a[24:27], v[24:27]\n"
    "buffer_load_dwordx4 v[172:175], v250, s[8:11], s47 offen offset:3072 sc1\n"
    "s_bitset0_b32 s61, 14\n"
    "v_mfma_f32_16x16x32_bf16 v[28:31], v[212:215], a[24:27], v[28:31]\n"
    "s_lshl_b32 s82, s88, 7\n"
    "v_accvgpr_write_b32 a199, 0\n"
    "v_mfma_f32_16x16x32_bf16 v[48:51], v[208:211], a[56:59], v[48:51]\n"
    "v_accvgpr_write_b32 a234, v3\n"
    "v_accvgpr_write_b32 a200, 0\n"
    "v_mfma_f32_16x16x32_bf16 v[52:55], v[212:215], a[56:59], v[52:55]\n"
    "s_add_u32 s47, s52, s61\n"
    "buffer_load_dwordx4 v[184:187], v250, s[8:11], s47 offen offset:0 sc1\n"
    "v_mfma_f32_16x16x32_bf16 v[64:67], v[208:211], a[88:91], v[64:67]\n"
    "v_cmp_eq_u32_e32 vcc, 3, v1\n"
    "s_nop 1\n"
    "v_cndmask_b32_e32 v3, 0, v2, vcc\n"
    "v_mfma_f32_16x16x32_bf16 v[68:71], v[212:215], a[88:91], v[68:71]\n"
    "s_add_u32 s82, s82, 6101248\n"
    "v_accvgpr_write_b32 a201, 0\n"
    "s_waitcnt vmcnt(10)\n"
    "v_mfma_f32_16x16x32_bf16 v[24:27], v[224:227], a[28:31], v[24:27]\n"
    "buffer_load_dwordx4 v[188:191], v250, s[8:11], s47 offen offset:1024 sc1\n"
    "v_accvgpr_write_b32 a202, 0\n"
    "v_mfma_f32_16x16x32_bf16 v[28:31], v[228:231], a[28:31], v[28:31]\n"
    "v_accvgpr_write_b32 a235, v3\n"
    "v_accvgpr_write_b32 a203, 0\n"
    "v_mfma_f32_16x16x32_bf16 v[48:51], v[224:227], a[60:63], v[48:51]\n"
    "s_lshl_b32 s83, s88, 17\n"
    "v_accvgpr_write_b32 a204, 0\n"
    "v_mfma_f32_16x16x32_bf16 v[52:55], v[228:231], a[60:63], v[52:55]\n"
    "buffer_load_dwordx4 v[200:203], v250, s[8:11], s47 offen offset:2048 sc1\n"
    "s_add_u32 s61, s61, 0x1000\n"
    "v_mfma_f32_16x16x32_bf16 v[64:67], v[224:227], a[92:95], v[64:67]\n"
    "s_and_b32 s89, s34, 31\n"
    "v_accvgpr_write_b32 a205, 0\n"
    "v_mfma_f32_16x16x32_bf16 v[68:71], v[228:231], a[92:95], v[68:71]\n"
    "s_lshl2_add_u32 s89, s89, s93\n"
    "v_accvgpr_write_b32 a206, 0\n"
    "s_waitcnt vmcnt(10)\n"
    "v_mfma_f32_16x16x32_bf16 v[32:35], v[120:123], a[0:3], 0\n"
    "buffer_load_dwordx4 v[204:207], v250, s[8:11], s47 offen offset:3072 sc1\n"
    "s_bitset0_b32 s61, 14\n"
    "v_mfma_f32_16x16x32_bf16 v[36:39], v[124:127], a[0:3], 0\n"
    "v_cvt_pk_bf16_f32 v24, v24, v25\n"
    "v_accvgpr_write_b32 a207, 0\n"
    "v_mfma_f32_16x16x32_bf16 v[56:59], v[120:123], a[32:35], 0\n"
    "v_cvt_pk_bf16_f32 v25, v26, v27\n"
    "v_accvgpr_write_b32 a208, 0\n"
    "v_mfma_f32_16x16x32_bf16 v[60:63], v[124:127], a[32:35], 0\n"
    "s_add_u32 s47, s52, s61\n"
    "buffer_load_dwordx4 v[216:219], v250, s[8:11], s47 offen offset:0 sc1\n"
    "v_mfma_f32_16x16x32_bf16 v[72:75], v[120:123], a[64:67], 0\n"
    "v_cvt_pk_bf16_f32 v26, v28, v29\n"
    "v_cvt_pk_bf16_f32 v27, v30, v31\n"
    "v_mfma_f32_16x16x32_bf16 v[76:79], v[124:127], a[64:67], 0\n"
    "buffer_store_dwordx4 v[24:27], v98, s[8:11], s35 offen offset:0 sc1\n"
    "v_accvgpr_write_b32 a209, 0\n"
    "s_waitcnt vmcnt(11)\n"
    "v_mfma_f32_16x16x32_bf16 v[32:35], v[136:139], a[4:7], v[32:35]\n"
    "buffer_load_dwordx4 v[220:223], v250, s[8:11], s47 offen offset:1024 sc1\n"
    "s_add_u32 s52, s52, 0x4000\n"
    "v_mfma_f32_16x16x32_bf16 v[36:39], v[140:143], a[4:7], v[36:39]\n"
    "v_cvt_pk_bf16_f32 v48, v48, v49\n"
    "v_accvgpr_write_b32 a210, 0\n"
    "v_mfma_f32_16x16x32_bf16 v[56:59], v[136:139], a[36:39], v[56:59]\n"
    "v_cvt_pk_bf16_f32 v49, v50, v51\n"
    "v_cvt_pk_bf16_f32 v50, v52, v53\n"
    "v_mfma_f32_16x16x32_bf16 v[60:63], v[140:143], a[36:39], v[60:63]\n"
    "buffer_load_dwordx4 v[232:235], v250, s[8:11], s47 offen offset:2048 sc1\n"
    "s_add_u32 s61, s61, 0x1000\n"
    "s_waitcnt vmcnt(11)\n"
    "v_cmp_ne_u32_e32 vcc, s39, v80\n"
    "v_mfma_f32_16x16x32_bf16 v[72:75], v[136:139], a[68:71], v[72:75]\n"
    "v_cvt_pk_bf16_f32 v51, v54, v55\n"
    "buffer_store_dwordx4 v[48:51], v98, s[8:11], s54 offen offset:0 sc1\n"
    "v_mfma_f32_16x16x32_bf16 v[76:79], v[140:143], a[68:71], v[76:79]\n"
    "v_cvt_pk_bf16_f32 v64, v64, v65\n"
    "v_cvt_pk_bf16_f32 v65, v66, v67\n"
    "s_waitcnt vmcnt(11)\n"
    "v_mfma_f32_16x16x32_bf16 v[32:35], v[152:155], a[8:11], v[32:35]\n"
    "buffer_load_dwordx4 v[236:239], v250, s[8:11], s47 offen offset:3072 sc1\n"
    "s_bitset0_b32 s61, 14\n"
    "v_mfma_f32_16x16x32_bf16 v[36:39], v[156:159], a[8:11], v[36:39]\n"
    "s_cbranch_vccnz .Lat_h1poll\n"
    ".Lat_h1poll_ret:\n"
    "s_mov_b32 m0, s56\n"
    "v_mfma_f32_16x16x32_bf16 v[56:59], v[152:155], a[40:43], v[56:59]\n"
    "buffer_load_dwordx4 v104, s[8:11], s57 offen offset:0 sc1 lds\n"
    "v_cvt_pk_bf16_f32 v66, v68, v69\n"
    "v_mfma_f32_16x16x32_bf16 v[60:63], v[156:159], a[40:43], v[60:63]\n"
    "v_cvt_pk_bf16_f32 v67, v70, v71\n"
    "buffer_store_dwordx4 v[64:67], v98, s[8:11], s60 offen offset:0 sc1\n"
    "v_mfma_f32_16x16x32_bf16 v[72:75], v[152:155], a[72:75], v[72:75]\n"
    "s_lshl_b32 s89, s89, 10\n"
    "v_accvgpr_write_b32 a211, 0\n"
    "v_mfma_f32_16x16x32_bf16 v[76:79], v[156:159], a[72:75], v[76:79]\n"
    "s_add_u32 s83, s83, s89\n"
    "v_accvgpr_write_b32 a212, 0\n"
    "s_waitcnt vmcnt(12)\n"
    "v_mfma_f32_16x16x32_bf16 v[32:35], v[168:171], a[12:15], v[32:35]\n"
    "buffer_load_dwordx4 v105, s[8:11], s57 offen offset:1024 sc1 lds\n"
    "v_accvgpr_write_b32 a213, 0\n"
    "v_mfma_f32_16x16x32_bf16 v[36:39], v[172:175], a[12:15], v[36:39]\n"
    "s_add_u32 s83, s83, 67682304\n"
    "v_accvgpr_write_b32 a214, 0\n"
    "v_mfma_f32_16x16x32_bf16 v[56:59], v[168:171], a[44:47], v[56:59]\n"
    "ds_write_b128 v99, v[24:27] offset:0\n"
    "v_accvgpr_write_b32 a215, 0\n"
    "v_mfma_f32_16x16x32_bf16 v[60:63], v[172:175], a[44:47], v[60:63]\n"
    "s_add_u32 m0, s43, 0\n"
    "v_accvgpr_write_b32 a216, 0\n"
    "v_mfma_f32_16x16x32_bf16 v[72:75], v[168:171], a[76:79], v[72:75]\n"
    "buffer_load_dwordx4 v104, s[8:11], s40 offen offset:0 sc1 lds\n"
    "v_accvgpr_write_b32 a217, 0\n"
    "v_mfma_f32_16x16x32_bf16 v[76:79], v[172:175], a[76:79], v[76:79]\n"
    "v_accvgpr_write_b32 a218, 0\n"
    "s_waitcnt vmcnt(12)\n"
    "v_mfma_f32_16x16x32_bf16 v[32:35], v[184:187], a[16:19], v[32:35]\n"
    "s_lshl_b32 s30, s93, 10\n"
    "v_mfma_f32_16x16x32_bf16 v[36:39], v[188:191], a[16:19], v[36:39]\n"
    "v_accvgpr_write_b32 a219, 0\n"
    "v_mfma_f32_16x16x32_bf16 v[56:59], v[184:187], a[48:51], v[56:59]\n"
    "buffer_load_dwordx4 v105, s[8:11], s40 offen offset:1024 sc1 lds\n"
    "v_mfma_f32_16x16x32_bf16 v[60:63], v[188:191], a[48:51], v[60:63]\n"
    "v_accvgpr_write_b32 a220, 0\n"
    "v_mfma_f32_16x16x32_bf16 v[72:75], v[184:187], a[80:83], v[72:75]\n"
    "s_add_u32 s30, s30, 20480\n"
    "v_mfma_f32_16x16x32_bf16 v[76:79], v[188:191], a[80:83], v[76:79]\n"
    "ds_write_b128 v99, v[48:51] offset:8192\n"
    "v_accvgpr_write_b32 a221, 0\n"
    "s_waitcnt vmcnt(11)\n"
    "v_mfma_f32_16x16x32_bf16 v[32:35], v[200:203], a[20:23], v[32:35]\n"
    "buffer_load_dwordx4 v106, s[8:11], s40 offen offset:2048 sc1 lds\n"
    "v_mfma_f32_16x16x32_bf16 v[36:39], v[204:207], a[20:23], v[36:39]\n"
    "v_accvgpr_write_b32 a222, 0\n"
    "v_mfma_f32_16x16x32_bf16 v[56:59], v[200:203], a[52:55], v[56:59]\n"
    "v_accvgpr_write_b32 a223, 0\n"
    "v_mfma_f32_16x16x32_bf16 v[60:63], v[204:207], a[52:55], v[60:63]\n"
    "s_bitcmp0_b32 s92, 0\n"
    "s_cselect_b32 s85, 1, 0\n"
    "s_cselect_b32 s80, 5, 0\n"
    "v_mfma_f32_16x16x32_bf16 v[72:75], v[200:203], a[84:87], v[72:75]\n"
    "buffer_load_dwordx4 v107, s[8:11], s40 offen offset:3072 sc1 lds\n"
    "v_mfma_f32_16x16x32_bf16 v[76:79], v[204:207], a[84:87], v[76:79]\n"
    "v_accvgpr_write_b32 a224, 0\n"
    "s_waitcnt vmcnt(10)\n"
    "v_mfma_f32_16x16x32_bf16 v[32:35], v[216:219], a[24:27], v[32:35]\n"
    "s_bitcmp0_b32 s92, 0\n"
    "s_cselect_b32 s82, s82, 2.0\n"
    "s_cselect_b32 s83, s83, 2.0\n"
    "v_mfma_f32_16x16x32_bf16 v[36:39], v[220:223], a[24:27], v[36:39]\n"
    "s_add_u32 s40, s40, 0x8000\n"
    "v_mfma_f32_16x16x32_bf16 v[56:59], v[216:219], a[56:59], v[56:59]\n"
    "ds_write_b128 v99, v[64:67] offset:49152\n"
    "v_mfma_f32_16x16x32_bf16 v[60:63], v[220:223], a[56:59], v[60:63]\n"
    "v_accvgpr_write_b32 a225, 0\n"
    "v_mfma_f32_16x16x32_bf16 v[72:75], v[216:219], a[88:91], v[72:75]\n"
    "v_accvgpr_write_b32 a226, 0\n"
    "v_mfma_f32_16x16x32_bf16 v[76:79], v[220:223], a[88:91], v[76:79]\n"
    "s_mov_b32 s84, 2.0\n"
    "s_waitcnt vmcnt(7)\n"
    "v_mfma_f32_16x16x32_bf16 v[32:35], v[232:235], a[28:31], v[32:35]\n"
    "v_accvgpr_write_b32 a227, 0\n"
    "v_mfma_f32_16x16x32_bf16 v[36:39], v[236:239], a[28:31], v[36:39]\n"
    "v_accvgpr_write_b32 a228, 0\n"
    "v_mfma_f32_16x16x32_bf16 v[56:59], v[232:235], a[60:63], v[56:59]\n"
    "v_accvgpr_write_b32 a229, 0\n"
    "v_mfma_f32_16x16x32_bf16 v[60:63], v[236:239], a[60:63], v[60:63]\n"
    "v_accvgpr_write_b32 a230, 0\n"
    "v_mfma_f32_16x16x32_bf16 v[72:75], v[232:235], a[92:95], v[72:75]\n"
    "s_movk_i32 s90, 512\n"
    "v_mfma_f32_16x16x32_bf16 v[76:79], v[236:239], a[92:95], v[76:79]\n"
    "v_accvgpr_write_b32 a231, 0\n"
    "v_cvt_pk_bf16_f32 v32, v32, v33\n"
    "v_cvt_pk_bf16_f32 v33, v34, v35\n"
    "v_cvt_pk_bf16_f32 v34, v36, v37\n"
    "v_cvt_pk_bf16_f32 v35, v38, v39\n"
    "ds_write_b128 v99, v[32:35] offset:1024\n"
    "buffer_store_dwordx4 v[32:35], v98, s[8:11], s35 offen offset:1024 sc1\n"
    "v_cvt_pk_bf16_f32 v56, v56, v57\n"
    "v_cvt_pk_bf16_f32 v57, v58, v59\n"
    "v_cvt_pk_bf16_f32 v58, v60, v61\n"
    "v_cvt_pk_bf16_f32 v59, v62, v63\n"
    "ds_write_b128 v99, v[56:59] offset:9216\n"
    "buffer_store_dwordx4 v[56:59], v98, s[8:11], s54 offen offset:1024 sc1\n"
    "v_cvt_pk_bf16_f32 v72, v72, v73\n"
    "v_cvt_pk_bf16_f32 v73, v74, v75\n"
    "v_cvt_pk_bf16_f32 v74, v76, v77\n"
    "v_cvt_pk_bf16_f32 v75, v78, v79\n"
    "ds_write_b128 v99, v[72:75] offset:50176\n"
    "buffer_store_dwordx4 v[72:75], v98, s[8:11], s60 offen offset:1024 sc1\n"
    "v_mov_b64_e32 v[8:9], 0\n"
    "v_mov_b64_e32 v[10:11], 0\n"
    "s_add_u32 m0, s43, 4096\n"
    "v_mov_b64_e32 v[12:13], 0\n"
    "v_mov_b64_e32 v[14:15], 0\n"
    "buffer_load_dwordx4 v104, s[8:11], s41 offen offset:0 sc1 lds\n"
    "v_mov_b64_e32 v[16:17], 0\n"
    "v_mov_b64_e32 v[18:19], 0\n"
    "buffer_load_dwordx4 v105, s[8:11], s41 offen offset:1024 sc1 lds\n"
    "v_mov_b64_e32 v[28:29], 0\n"
    "v_mov_b64_e32 v[30:31], 0\n"
    "buffer_load_dwordx4 v106, s[8:11], s41 offen offset:2048 sc1 lds\n"
    "v_mov_b64_e32 v[32:33], 0\n"
    "v_mov_b64_e32 v[34:35], 0\n"
    "buffer_load_dwordx4 v107, s[8:11], s41 offen offset:3072 sc1 lds\n"
    "v_mov_b64_e32 v[36:37], 0\n"
    "v_mov_b64_e32 v[38:39], 0\n"
    "s_add_u32 s41, s41, 0x8000\n"
    "v_mov_b64_e32 v[48:49], 0\n"
    "v_mov_b64_e32 v[50:51], 0\n"
    "v_mov_b64_e32 v[52:53], 0\n"
    "v_mov_b64_e32 v[54:55], 0\n"
    "v_mov_b64_e32 v[56:57], 0\n"
    "v_mov_b64_e32 v[58:59], 0\n"
    "v_mov_b64_e32 v[68:69], 0\n"
    "v_mov_b64_e32 v[70:71], 0\n"
    "v_mov_b64_e32 v[72:73], 0\n"
    "v_mov_b64_e32 v[74:75], 0\n"
    "v_mov_b64_e32 v[76:77], 0\n"
    "v_mov_b64_e32 v[78:79], 0\n"
    "s_cmp_lg_u32 s93, 3\n"
    "s_cbranch_scc1 .Lat_wn40\n"
    "s_call_b64 s[2:3], .Lat_w6\n"
    "s_call_b64 s[2:3], .Lat_w10\n"
    ".Lat_wn40:\n"
    "s_cmp_lg_u32 s93, 2\n"
    "s_cbranch_scc1 .Lat_wn41\n"
    "s_call_b64 s[2:3], .Lat_w7\n"
    "s_call_b64 s[2:3], .Lat_w11\n"
    ".Lat_wn41:\n"
    "s_cmp_lg_u32 s93, 1\n"
    "s_cbranch_scc1 .Lat_wn42\n"
    "s_call_b64 s[2:3], .Lat_w8\n"
    "s_call_b64 s[2:3], .Lat_w12\n"
    ".Lat_wn42:\n"
    "s_cmp_lg_u32 s93, 0\n"
    "s_cbranch_scc1 .Lat_wn43\n"
    "s_call_b64 s[2:3], .Lat_w9\n"
    "s_call_b64 s[2:3], .Lat_w13\n"
    ".Lat_wn43:\n"
    "s_waitcnt vmcnt(4)\n"
    "buffer_store_dword v84, v86, s[8:11], s58 offen offset:0 sc1\n"
    "v_mov_b64_e32 v[88:89], 0\n"
    "v_mov_b64_e32 v[90:91], 0\n"
    "s_add_u32 m0, s43, 8192\n"
    "v_mov_b64_e32 v[92:93], 0\n"
    "v_mov_b64_e32 v[94:95], 0\n"
    "buffer_load_dwordx4 v104, s[8:11], s40 offen offset:0 sc1 lds\n"
    "v_mov_b64_e32 v[96:97], 0\n"
    "v_mov_b64_e32 v[98:99], 0\n"
    "buffer_load_dwordx4 v105, s[8:11], s40 offen offset:1024 sc1 lds\n"
    "v_mov_b64_e32 v[108:109], 0\n"
    "v_mov_b64_e32 v[110:111], 0\n"
    "buffer_load_dwordx4 v106, s[8:11], s40 offen offset:2048 sc1 lds\n"
    "v_mov_b64_e32 v[112:113], 0\n"
    "v_mov_b64_e32 v[114:115], 0\n"
    "buffer_load_dwordx4 v107, s[8:11], s40 offen offset:3072 sc1 lds\n"
    "v_mov_b64_e32 v[116:117], 0\n"
    "v_mov_b64_e32 v[118:119], 0\n"
    "s_add_u32 s40, s40, 0x8000\n"
    "v_mov_b64_e32 v[128:129], 0\n"
    "v_mov_b64_e32 v[130:131], 0\n"
    "s_add_u32 m0, s43, 12288\n"
    "v_mov_b64_e32 v[132:133], 0\n"
    "v_mov_b64_e32 v[134:135], 0\n"
    "buffer_load_dwordx4 v104, s[8:11], s41 offen offset:0 sc1 lds\n"
    "v_mov_b64_e32 v[136:137], 0\n"
    "v_mov_b64_e32 v[138:139], 0\n"
    "buffer_load_dwordx4 v105, s[8:11], s41 offen offset:1024 sc1 lds\n"
    "buffer_load_dwordx4 v106, s[8:11], s41 offen offset:2048 sc1 lds\n"
    "buffer_load_dwordx4 v107, s[8:11], s41 offen offset:3072 sc1 lds\n"
    "s_add_u32 s41, s41, 0x8000\n"
    ".Lat_p2:\n"
    "v_mov_b32_e32 v220, 0xc2540000\n"
    "v_mov_b32_e32 v221, 0xc2540000\n"
    "v_mov_b64_e32 v[222:223], v[220:221]\n"
    "v_mov_b64_e32 v[224:225], v[220:221]\n"
    "v_mov_b64_e32 v[226:227], v[220:221]\n"
    "v_mov_b64_e32 v[228:229], v[220:221]\n"
    "v_mov_b64_e32 v[230:231], v[220:221]\n"
    "v_mov_b64_e32 v[232:233], v[220:221]\n"
    "v_mov_b64_e32 v[234:235], v[220:221]\n"
    "v_mov_b64_e32 v[236:237], v[220:221]\n"
    "v_mov_b64_e32 v[238:239], v[220:221]\n"
    "s_andn2_b32 s47, s58, 0x600\n"
    "s_mov_b32 s44, 9\n"
    "s_waitcnt vmcnt(9)\n"
    "s_waitcnt lgkmcnt(0)\n"
    "s_barrier\n"
    "s_cmp_lg_u32 s49, 0\n"
    "s_cbranch_scc1 .Lat_maxpass\n"
    "ds_read_b128 a[0:3], v85 offset:0\n"
    "ds_read_b128 a[4:7], v85 offset:1024\n"
    "ds_read_b128 a[8:11], v85 offset:2048\n"
    "ds_read_b128 a[12:15], v85 offset:3072\n"
    "ds_read_b128 a[16:19], v85 offset:4096\n"
    "ds_read_b128 a[20:23], v85 offset:5120\n"
    "ds_read_b128 a[24:27], v85 offset:6144\n"
    "ds_read_b128 a[28:31], v85 offset:7168\n"
    "ds_read_b128 a[32:35], v255 offset:0\n"
    "ds_read_b128 a[36:39], v255 offset:1024\n"
    "ds_read_b128 a[40:43], v255 offset:2048\n"
    "ds_read_b128 a[44:47], v255 offset:3072\n"
    "ds_read_b128 a[48:51], v255 offset:4096\n"
    "ds_read_b128 a[52:55], v255 offset:5120\n"
    "ds_read_b128 a[56:59], v255 offset:6144\n"
    "ds_read_b128 a[60:63], v255 offset:7168\n"
    ".Lat_maxpass_ret:\n"
    "ds_read_b128 a[160:163], v46 offset:0\n"
    "ds_read_b128 a[164:167], v47 offset:0\n"
    "ds_read_b128 a[168:171], v46 offset:2048\n"
    "ds_read_b128 a[172:175], v47 offset:2048\n"
    "ds_read_b128 a[176:179], v46 offset:4096\n"
    "ds_read_b128 a[180:183], v47 offset:4096\n"
    "ds_read_b128 a[184:187], v46 offset:6144\n"
    "ds_read_b128 a[188:191], v47 offset:6144\n"
    "s_mul_i32 s48, s33, 81920\n"
    "s_add_u32 s48, s48, 11345920\n"
    "s_lshl_b32 s46, s93, 11\n"
    "s_add_u32 s48, s48, s46\n"
    "s_add_u32 s46, s46, 49152\n"
    "buffer_load_dword v254, v253, s[8:11], s47 offen offset:0 sc1\n"
    "s_waitcnt lgkmcnt(4)\n"
    "v_mfma_f32_16x16x32_bf16 v[212:215], a[160:163], a[0:3], v[220:223]\n"
    ".long 0xbedf00ff\n"
    ".Lat_w1:\n"
    "s_setpc_b64 s[2:3]\n"
    "s_and_b32 s45, s32, 1\n"
    "ds_read_b64_tr_b16 v[140:141], v4 offset:0\n"
    "v_mfma_f32_16x16x32_bf16 v[216:219], a[160:163], a[32:35], v[224:227]\n"
    "s_xor_b32 s45, s45, 9\n"
    "ds_read_b64_tr_b16 v[142:143], v5 offset:0\n"
    "v_mfma_f32_16x16x32_bf16 v[212:215], a[164:167], a[4:7], v[212:215]\n"
    "s_lshl_b32 s45, s45, 13\n"
    "ds_read_b64_tr_b16 v[144:145], v6 offset:0\n"
    "v_mfma_f32_16x16x32_bf16 v[216:219], a[164:167], a[36:39], v[216:219]\n"
    "s_add_u32 s45, s45, s48\n"
    "ds_read_b64_tr_b16 v[146:147], v7 offset:0\n"
    "v_mfma_f32_16x16x32_bf16 v[212:215], a[168:171], a[8:11], v[212:215]\n"
    "s_add_u32 s54, s32, 1\n"
    "ds_read_b64_tr_b16 v[148:149], v4 offset:2048\n"
    "v_mfma_f32_16x16x32_bf16 v[216:219], a[168:171], a[40:43], v[216:219]\n"
    "s_and_b32 s54, s54, 3\n"
    "ds_read_b64_tr_b16 v[150:151], v5 offset:2048\n"
    "v_mfma_f32_16x16x32_bf16 v[212:215], a[172:175], a[12:15], v[212:215]\n"
    "s_lshl_b32 s54, s54, 13\n"
    "ds_read_b64_tr_b16 v[152:153], v6 offset:2048\n"
    "v_mfma_f32_16x16x32_bf16 v[216:219], a[172:175], a[44:47], v[216:219]\n"
    "s_add_u32 s54, s54, s48\n"
    "ds_read_b64_tr_b16 v[154:155], v7 offset:2048\n"
    "s_waitcnt lgkmcnt(8)\n"
    "v_mfma_f32_16x16x32_bf16 v[212:215], a[176:179], a[16:19], v[212:215]\n"
    "s_add_u32 s55, s32, 2\n"
    "ds_read_b128 v[180:183], v240 offset:2048\n"
    "v_mfma_f32_16x16x32_bf16 v[216:219], a[176:179], a[48:51], v[216:219]\n"
    "s_and_b32 s55, s55, 3\n"
    "ds_read_b128 v[184:187], v241 offset:2048\n"
    "v_mfma_f32_16x16x32_bf16 v[212:215], a[180:183], a[20:23], v[212:215]\n"
    "s_lshl_b32 s55, s55, 13\n"
    "ds_read_b128 v[188:191], v240 offset:6144\n"
    "v_mfma_f32_16x16x32_bf16 v[216:219], a[180:183], a[52:55], v[216:219]\n"
    "s_add_u32 s55, s55, s48\n"
    "ds_read_b128 v[192:195], v241 offset:6144\n"
    "v_mfma_f32_16x16x32_bf16 v[212:215], a[184:187], a[24:27], v[212:215]\n"
    "s_add_u32 s60, s32, 3\n"
    "ds_read_b128 v[196:199], v242 offset:2048\n"
    "v_mfma_f32_16x16x32_bf16 v[216:219], a[184:187], a[56:59], v[216:219]\n"
    "s_and_b32 s60, s60, 3\n"
    "ds_read_b128 v[200:203], v243 offset:2048\n"
    "v_mfma_f32_16x16x32_bf16 v[212:215], a[188:191], a[28:31], v[212:215]\n"
    "s_lshl_b32 s60, s60, 13\n"
    "ds_read_b128 v[204:207], v242 offset:6144\n"
    "v_mfma_f32_16x16x32_bf16 v[216:219], a[188:191], a[60:63], v[216:219]\n"
    "s_add_u32 s60, s60, s48\n"
    "ds_read_b128 v[208:211], v243 offset:6144\n"
    "s_lshr_b32 s61, s32, 1\n"
    "s_or_b32 s61, s61, s50\n"
    "s_sub_u32 s61, s61, 1\n"
    "s_add_u32 m0, s46, 65536\n"
    "s_waitcnt vmcnt(0)\n"
    "v_cmp_ne_u32_e32 vcc, s39, v254\n"
    "ds_read_b128 a[160:163], v240 offset:0\n"
    "ds_read_b128 a[164:167], v241 offset:0\n"
    "ds_read_b128 a[168:171], v240 offset:4096\n"
    "ds_read_b128 a[172:175], v241 offset:4096\n"
    "ds_read_b128 a[176:179], v242 offset:0\n"
    "s_cbranch_vccz .Lat_qpoll_ret\n"
    ".Lat_qpoll:\n"
    "s_mov_b32 s53, 0x400000\n"
    ".Lat_qpoll_l:\n"
    "s_sleep 0\n"
    "buffer_load_dword v254, v253, s[8:11], s47 offen offset:0 sc1\n"
    "s_waitcnt vmcnt(0)\n"
    "v_cmp_ne_u32_e32 vcc, s39, v254\n"
    "s_sub_u32 s53, s53, 1\n"
    "s_cbranch_scc1 .Lat_qpoll_ret\n"
    "s_nop 2\n"
    "s_cbranch_vccnz .Lat_qpoll_l\n"
    ".Lat_qpoll_ret:\n"
    "buffer_load_dwordx4 v250, s[8:11], s45 offen offset:0 sc1 lds\n"
    "buffer_load_dwordx4 v250, s[8:11], s45 offen offset:1024 sc1 lds\n"
    "s_add_u32 m0, s46, 8192\n"
    "ds_read_b128 a[180:183], v243 offset:0\n"
    "s_nop 0\n"
    "ds_read_b128 a[184:187], v242 offset:4096\n"
    "buffer_load_dwordx4 v250, s[8:11], s54 offen offset:0 sc1 lds\n"
    "ds_read_b128 a[188:191], v243 offset:4096\n"
    "buffer_load_dwordx4 v250, s[8:11], s54 offen offset:1024 sc1 lds\n"
    "v_exp_f32_e32 v212, v212\n"
    "s_add_u32 m0, s46, 73728\n"
    "v_exp_f32_e32 v213, v213\n"
    "s_add_u32 s54, s54, 0x8000\n"
    "v_exp_f32_e32 v214, v214\n"
    "buffer_load_dwordx4 v250, s[8:11], s54 offen offset:0 sc1 lds\n"
    "v_exp_f32_e32 v215, v215\n"
    "buffer_load_dwordx4 v250, s[8:11], s54 offen offset:1024 sc1 lds\n"
    "v_exp_f32_e32 v216, v216\n"
    "s_add_u32 m0, s46, 32768\n"
    "v_exp_f32_e32 v217, v217\n"
    "s_nop 0\n"
    "v_exp_f32_e32 v218, v218\n"
    "buffer_load_dwordx4 v250, s[8:11], s55 offen offset:0 sc1 lds\n"
    "v_exp_f32_e32 v219, v219\n"
    "buffer_load_dwordx4 v250, s[8:11], s55 offen offset:1024 sc1 lds\n"
    "v_cvt_pk_bf16_f32 v212, v212, v213\n"
    "v_cvt_pk_bf16_f32 v213, v214, v215\n"
    "v_cvt_pk_bf16_f32 v216, v216, v217\n"
    "v_cvt_pk_bf16_f32 v217, v218, v219\n"
    "v_and_b32_e32 v216, s61, v216\n"
    "v_and_b32_e32 v217, s61, v217\n"
    "s_waitcnt lgkmcnt(15)\n"
    "v_mfma_f32_16x16x16_bf16 a[236:239], v[140:141], v[212:213], 0\n"
    "s_add_u32 m0, s46, 98304\n"
    "v_mfma_f32_16x16x16_bf16 v[0:3], v[142:143], v[212:213], 0\n"
    "s_add_u32 s55, s55, 0x8000\n"
    "v_mfma_f32_16x16x16_bf16 v[20:23], v[144:145], v[212:213], 0\n"
    "buffer_load_dwordx4 v250, s[8:11], s55 offen offset:0 sc1 lds\n"
    "v_mfma_f32_16x16x16_bf16 v[40:43], v[146:147], v[212:213], 0\n"
    "buffer_load_dwordx4 v250, s[8:11], s55 offen offset:1024 sc1 lds\n"
    "v_mfma_f32_16x16x16_bf16 v[60:63], v[148:149], v[212:213], 0\n"
    "s_add_u32 m0, s46, 40960\n"
    "v_mfma_f32_16x16x16_bf16 v[80:83], v[150:151], v[212:213], 0\n"
    "buffer_load_dwordx4 v250, s[8:11], s60 offen offset:0 sc1 lds\n"
    "v_mfma_f32_16x16x16_bf16 v[100:103], v[152:153], v[212:213], 0\n"
    "buffer_load_dwordx4 v250, s[8:11], s60 offen offset:1024 sc1 lds\n"
    "v_mfma_f32_16x16x16_bf16 v[120:123], v[154:155], v[212:213], 0\n"
    "s_add_u32 m0, s46, 106496\n"
    "v_mfma_f32_16x16x16_bf16 a[240:243], v[140:141], v[216:217], 0\n"
    "s_add_u32 s60, s60, 0x8000\n"
    "v_mfma_f32_16x16x16_bf16 v[4:7], v[142:143], v[216:217], 0\n"
    "buffer_load_dwordx4 v250, s[8:11], s60 offen offset:0 sc1 lds\n"
    "v_mfma_f32_16x16x16_bf16 v[24:27], v[144:145], v[216:217], 0\n"
    "buffer_load_dwordx4 v250, s[8:11], s60 offen offset:1024 sc1 lds\n"
    "v_mfma_f32_16x16x16_bf16 v[44:47], v[146:147], v[216:217], 0\n"
    "v_mfma_f32_16x16x16_bf16 v[64:67], v[148:149], v[216:217], 0\n"
    "buffer_load_dword v253, v250, s[8:11], s59 offen offset:0 sc1\n"
    "v_mfma_f32_16x16x16_bf16 v[84:87], v[150:151], v[216:217], 0\n"
    "v_mfma_f32_16x16x16_bf16 v[104:107], v[152:153], v[216:217], 0\n"
    "v_mfma_f32_16x16x16_bf16 v[124:127], v[154:155], v[216:217], 0\n"
    "s_waitcnt lgkmcnt(0)\n"
    "buffer_load_dword v254, v252, s[8:11], s59 offen offset:0 sc1\n"
    "v_mfma_f32_16x16x32_bf16 v[140:143], a[160:163], a[0:3], v[220:223]\n"
    "v_mfma_f32_16x16x32_bf16 v[140:143], a[164:167], a[4:7], v[140:143]\n"
    "v_mfma_f32_16x16x32_bf16 v[140:143], a[168:171], a[8:11], v[140:143]\n"
    "v_mfma_f32_16x16x32_bf16 v[140:143], a[172:175], a[12:15], v[140:143]\n"
    "v_mfma_f32_16x16x32_bf16 v[140:143], a[176:179], a[16:19], v[140:143]\n"
    "v_mfma_f32_16x16x32_bf16 v[140:143], a[180:183], a[20:23], v[140:143]\n"
    "v_mfma_f32_16x16x32_bf16 v[140:143], a[184:187], a[24:27], v[140:143]\n"
    "v_mfma_f32_16x16x32_bf16 v[140:143], a[188:191], a[28:31], v[140:143]\n"
    "v_mfma_f32_16x16x32_bf16 v[144:147], v[180:183], a[0:3], v[220:223]\n"
    "v_mfma_f32_16x16x32_bf16 v[144:147], v[184:187], a[4:7], v[144:147]\n"
    "s_waitcnt vmcnt(10)\n"
    "s_barrier\n"
    "v_mfma_f32_16x16x32_bf16 v[144:147], v[188:191], a[8:11], v[144:147]\n"
    "s_mov_b32 m0, s42\n"
    "ds_read_b128 a[32:35], v255 offset:0\n"
    "v_exp_f32_e32 v140, v140\n"
    "v_mfma_f32_16x16x32_bf16 v[144:147], v[192:195], a[12:15], v[144:147]\n"
    "ds_read_b128 a[36:39], v255 offset:1024\n"
    "v_exp_f32_e32 v141, v141\n"
    "v_mfma_f32_16x16x32_bf16 v[144:147], v[196:199], a[16:19], v[144:147]\n"
    "ds_read_b128 a[40:43], v255 offset:2048\n"
    "ds_read_b128 a[44:47], v255 offset:3072\n"
    "v_mfma_f32_16x16x32_bf16 v[144:147], v[200:203], a[20:23], v[144:147]\n"
    "ds_read_b128 a[48:51], v255 offset:4096\n"
    "ds_read_b128 a[52:55], v255 offset:5120\n"
    "v_mfma_f32_16x16x32_bf16 v[144:147], v[204:207], a[24:27], v[144:147]\n"
    "ds_read_b128 a[56:59], v255 offset:6144\n"
    "ds_read_b128 a[60:63], v255 offset:7168\n"
    "v_mfma_f32_16x16x32_bf16 v[144:147], v[208:211], a[28:31], v[144:147]\n"
    "s_waitcnt lgkmcnt(4)\n"
    "v_exp_f32_e32 v142, v142\n"
    "v_mfma_f32_16x16x32_bf16 v[148:151], a[160:163], a[32:35], v[224:227]\n"
    "ds_read_b128 a[64:67], v255 offset:8192\n"
    "v_exp_f32_e32 v143, v143\n"
    "v_mfma_f32_16x16x32_bf16 v[148:151], a[164:167], a[36:39], v[148:151]\n"
    "ds_read_b128 a[68:71], v255 offset:9216\n"
    "v_cvt_pk_bf16_f32 v140, v140, v141\n"
    "v_mfma_f32_16x16x32_bf16 v[148:151], a[168:171], a[40:43], v[148:151]\n"
    "ds_read_b128 a[72:75], v255 offset:10240\n"
    "v_cvt_pk_bf16_f32 v141, v142, v143\n"
    "v_mfma_f32_16x16x32_bf16 v[148:151], a[172:175], a[44:47], v[148:151]\n"
    "s_waitcnt lgkmcnt(3)\n"
    "v_exp_f32_e32 v144, v144\n"
    "v_mfma_f32_16x16x32_bf16 v[148:151], a[176:179], a[48:51], v[148:151]\n"
    "ds_read_b128 a[76:79], v255 offset:11264\n"
    "ds_write_addtid_b32 v140 offset:0\n"
    "v_mfma_f32_16x16x32_bf16 v[148:151], a[180:183], a[52:55], v[148:151]\n"
    "ds_read_b128 a[80:83], v255 offset:12288\n"
    "v_exp_f32_e32 v145, v145\n"
    "v_mfma_f32_16x16x32_bf16 v[148:151], a[184:187], a[56:59], v[148:151]\n"
    "v_exp_f32_e32 v146, v146\n"
    "v_mfma_f32_16x16x32_bf16 v[148:151], a[188:191], a[60:63], v[148:151]\n"
    "ds_read_b128 a[84:87], v255 offset:13312\n"
    "v_exp_f32_e32 v147, v147\n"
    "v_mfma_f32_16x16x32_bf16 v[152:155], v[180:183], a[32:35], v[224:227]\n"
    "ds_read_b128 a[88:91], v255 offset:14336\n"
    "v_cvt_pk_bf16_f32 v142, v144, v145\n"
    "v_mfma_f32_16x16x32_bf16 v[152:155], v[184:187], a[36:39], v[152:155]\n"
    "ds_read_b128 a[92:95], v255 offset:15360\n"
    "v_cvt_pk_bf16_f32 v143, v146, v147\n"
    "v_mfma_f32_16x16x32_bf16 v[152:155], v[188:191], a[40:43], v[152:155]\n"
    "v_exp_f32_e32 v148, v148\n"
    "v_mfma_f32_16x16x32_bf16 v[152:155], v[192:195], a[44:47], v[152:155]\n"
    "v_exp_f32_e32 v149, v149\n"
    "v_mfma_f32_16x16x32_bf16 v[152:155], v[196:199], a[48:51], v[152:155]\n"
    "v_exp_f32_e32 v150, v150\n"
    "v_mfma_f32_16x16x32_bf16 v[152:155], v[200:203], a[52:55], v[152:155]\n"
    "v_exp_f32_e32 v151, v151\n"
    "v_mfma_f32_16x16x32_bf16 v[152:155], v[204:207], a[56:59], v[152:155]\n"
    "s_waitcnt vmcnt(2)\n"
    "s_barrier\n"
    "v_mfma_f32_16x16x32_bf16 v[152:155], v[208:211], a[60:63], v[152:155]\n"
    "s_waitcnt lgkmcnt(5)\n"
    "ds_read_b128 a[96:99], v255 offset:32768\n"
    "ds_write_addtid_b32 v141 offset:256\n"
    "v_mfma_f32_16x16x32_bf16 v[156:159], a[160:163], a[64:67], v[228:231]\n"
    "ds_read_b128 a[100:103], v255 offset:33792\n"
    "ds_write_addtid_b32 v142 offset:512\n"
    "v_mfma_f32_16x16x32_bf16 v[156:159], a[164:167], a[68:71], v[156:159]\n"
    "ds_read_b128 a[104:107], v255 offset:34816\n"
    "v_mfma_f32_16x16x32_bf16 v[156:159], a[168:171], a[72:75], v[156:159]\n"
    "ds_read_b128 a[108:111], v255 offset:35840\n"
    "v_mfma_f32_16x16x32_bf16 v[156:159], a[172:175], a[76:79], v[156:159]\n"
    "s_waitcnt lgkmcnt(6)\n"
    "ds_read_b128 a[112:115], v255 offset:36864\n"
    "v_cvt_pk_bf16_f32 v148, v148, v149\n"
    "v_mfma_f32_16x16x32_bf16 v[156:159], a[176:179], a[80:83], v[156:159]\n"
    "ds_read_b128 a[116:119], v255 offset:37888\n"
    "v_cvt_pk_bf16_f32 v149, v150, v151\n"
    "v_mfma_f32_16x16x32_bf16 v[156:159], a[180:183], a[84:87], v[156:159]\n"
    "ds_read_b128 a[120:123], v255 offset:38912\n"
    "v_exp_f32_e32 v152, v152\n"
    "v_mfma_f32_16x16x32_bf16 v[156:159], a[184:187], a[88:91], v[156:159]\n"
    "ds_read_b128 a[124:127], v255 offset:39936\n"
    "v_exp_f32_e32 v153, v153\n"
    "v_mfma_f32_16x16x32_bf16 v[156:159], a[188:191], a[92:95], v[156:159]\n"
    "ds_read_b128 a[128:131], v255 offset:40960\n"
    "v_exp_f32_e32 v154, v154\n"
    "v_mfma_f32_16x16x32_bf16 v[160:163], v[180:183], a[64:67], v[228:231]\n"
    "ds_read_b128 a[132:135], v255 offset:41984\n"
    "v_exp_f32_e32 v155, v155\n"
    "v_mfma_f32_16x16x32_bf16 v[160:163], v[184:187], a[68:71], v[160:163]\n"
    "ds_read_b128 a[136:139], v255 offset:43008\n"
    "v_cvt_pk_bf16_f32 v150, v152, v153\n"
    "v_mfma_f32_16x16x32_bf16 v[160:163], v[188:191], a[72:75], v[160:163]\n"
    "ds_read_b128 a[140:143], v255 offset:44032\n"
    "v_cvt_pk_bf16_f32 v151, v154, v155\n"
    "v_mfma_f32_16x16x32_bf16 v[160:163], v[192:195], a[76:79], v[160:163]\n"
    "ds_read_b128 a[144:147], v255 offset:45056\n"
    "ds_read_b128 a[148:151], v255 offset:46080\n"
    "v_mfma_f32_16x16x32_bf16 v[160:163], v[196:199], a[80:83], v[160:163]\n"
    "ds_read_b128 a[152:155], v255 offset:47104\n"
    "ds_read_b128 a[156:159], v255 offset:48128\n"
    "v_mfma_f32_16x16x32_bf16 v[160:163], v[200:203], a[84:87], v[160:163]\n"
    "s_waitcnt vmcnt(0)\n"
    "ds_write_addtid_b32 v148 offset:1024\n"
    "v_xor_b32_e32 v254, s39, v254\n"
    "v_xor_b32_e32 v253, s39, v253\n"
    "v_mfma_f32_16x16x32_bf16 v[160:163], v[204:207], a[88:91], v[160:163]\n"
    "ds_write_addtid_b32 v149 offset:1280\n"
    "v_or_b32_e32 v253, v253, v254\n"
    "v_cmp_ne_u32_e32 vcc, 0, v253\n"
    "v_mfma_f32_16x16x32_bf16 v[160:163], v[208:211], a[92:95], v[160:163]\n"
    "s_waitcnt lgkmcnt(14)\n"
    "v_exp_f32_e32 v156, v156\n"
    "v_mfma_f32_16x16x32_bf16 v[164:167], a[160:163], a[96:99], v[232:235]\n"
    "v_exp_f32_e32 v157, v157\n"
    "v_mfma_f32_16x16x32_bf16 v[164:167], a[164:167], a[100:103], v[164:167]\n"
    "v_exp_f32_e32 v158, v158\n"
    "v_mfma_f32_16x16x32_bf16 v[164:167], a[168:171], a[104:107], v[164:167]\n"
    "s_cbranch_vccnz .Lat_hpoll\n"
    ".Lat_hpoll_ret:\n"
    "s_waitcnt lgkmcnt(2)\n"
    "s_waitcnt vmcnt(17)\n"
    "s_barrier\n"
    "v_mfma_f32_16x16x32_bf16 v[164:167], a[172:175], a[108:111], v[164:167]\n"
    "v_exp_f32_e32 v159, v159\n"
    "v_mfma_f32_16x16x32_bf16 v[164:167], a[176:179], a[112:115], v[164:167]\n"
    "s_add_u32 m0, s43, 16384\n"
    "v_cvt_pk_bf16_f32 v156, v156, v157\n"
    "v_mfma_f32_16x16x32_bf16 v[164:167], a[180:183], a[116:119], v[164:167]\n"
    "buffer_load_dwordx4 v248, s[8:11], s40 offen offset:0 sc1 lds\n"
    "v_cvt_pk_bf16_f32 v157, v158, v159\n"
    "v_mfma_f32_16x16x32_bf16 v[164:167], a[184:187], a[120:123], v[164:167]\n"
    "v_exp_f32_e32 v160, v160\n"
    "v_mfma_f32_16x16x32_bf16 v[164:167], a[188:191], a[124:127], v[164:167]\n"
    "v_exp_f32_e32 v161, v161\n"
    "v_mfma_f32_16x16x32_bf16 v[168:171], v[180:183], a[96:99], v[232:235]\n"
    "buffer_load_dwordx4 v248, s[8:11], s40 offen offset:1024 sc1 lds\n"
    "v_mfma_f32_16x16x32_bf16 v[168:171], v[184:187], a[100:103], v[168:171]\n"
    "v_exp_f32_e32 v162, v162\n"
    "v_mfma_f32_16x16x32_bf16 v[168:171], v[188:191], a[104:107], v[168:171]\n"
    "v_exp_f32_e32 v163, v163\n"
    "v_mfma_f32_16x16x32_bf16 v[168:171], v[192:195], a[108:111], v[168:171]\n"
    "buffer_load_dwordx4 v248, s[8:11], s40 offen offset:2048 sc1 lds\n"
    "v_mfma_f32_16x16x32_bf16 v[168:171], v[196:199], a[112:115], v[168:171]\n"
    "v_cvt_pk_bf16_f32 v158, v160, v161\n"
    "v_cvt_pk_bf16_f32 v159, v162, v163\n"
    "v_mfma_f32_16x16x32_bf16 v[168:171], v[200:203], a[116:119], v[168:171]\n"
    "v_exp_f32_e32 v164, v164\n"
    "v_mfma_f32_16x16x32_bf16 v[168:171], v[204:207], a[120:123], v[168:171]\n"
    "buffer_load_dwordx4 v248, s[8:11], s40 offen offset:3072 sc1 lds\n"
    "v_mfma_f32_16x16x32_bf16 v[168:171], v[208:211], a[124:127], v[168:171]\n"
    "s_add_u32 s40, s40, 0x8000\n"
    "v_exp_f32_e32 v165, v165\n"
    "v_mfma_f32_16x16x32_bf16 v[172:175], a[160:163], a[128:131], v[236:239]\n"
    "s_add_u32 m0, s43, 20480\n"
    "v_exp_f32_e32 v166, v166\n"
    "v_mfma_f32_16x16x32_bf16 v[172:175], a[164:167], a[132:135], v[172:175]\n"
    "buffer_load_dwordx4 v248, s[8:11], s41 offen offset:0 sc1 lds\n"
    "ds_read_b128 a[160:163], v240 offset:8192\n"
    "v_mfma_f32_16x16x32_bf16 v[172:175], a[168:171], a[136:139], v[172:175]\n"
    "ds_read_b128 a[164:167], v241 offset:8192\n"
    "v_mfma_f32_16x16x32_bf16 v[172:175], a[172:175], a[140:143], v[172:175]\n"
    "ds_read_b128 a[168:171], v240 offset:12288\n"
    "v_mfma_f32_16x16x32_bf16 v[172:175], a[176:179], a[144:147], v[172:175]\n"
    "buffer_load_dwordx4 v248, s[8:11], s41 offen offset:1024 sc1 lds\n"
    "ds_read_b128 a[172:175], v241 offset:12288\n"
    "v_mfma_f32_16x16x32_bf16 v[172:175], a[180:183], a[148:151], v[172:175]\n"
    "ds_read_b128 a[176:179], v242 offset:8192\n"
    "v_exp_f32_e32 v167, v167\n"
    "v_mfma_f32_16x16x32_bf16 v[172:175], a[184:187], a[152:155], v[172:175]\n"
    "ds_read_b128 a[180:183], v243 offset:8192\n"
    "v_cvt_pk_bf16_f32 v164, v164, v165\n"
    "v_mfma_f32_16x16x32_bf16 v[172:175], a[188:191], a[156:159], v[172:175]\n"
    "buffer_load_dwordx4 v248, s[8:11], s41 offen offset:2048 sc1 lds\n"
    "v_cvt_pk_bf16_f32 v165, v166, v167\n"
    "v_mfma_f32_16x16x32_bf16 v[176:179], v[180:183], a[128:131], v[236:239]\n"
    "v_exp_f32_e32 v168, v168\n"
    "v_mfma_f32_16x16x32_bf16 v[176:179], v[184:187], a[132:135], v[176:179]\n"
    "v_exp_f32_e32 v169, v169\n"
    "v_mfma_f32_16x16x32_bf16 v[176:179], v[188:191], a[136:139], v[176:179]\n"
    "buffer_load_dwordx4 v248, s[8:11], s41 offen offset:3072 sc1 lds\n"
    "v_mfma_f32_16x16x32_bf16 v[176:179], v[192:195], a[140:143], v[176:179]\n"
    "s_mov_b32 m0, s42\n"
    "s_add_u32 s41, s41, 0x8000\n"
    "v_exp_f32_e32 v172, v172\n"
    "v_mfma_f32_16x16x32_bf16 v[176:179], v[196:199], a[144:147], v[176:179]\n"
    "ds_write_addtid_b32 v156 offset:2048\n"
    "v_exp_f32_e32 v173, v173\n"
    "v_mfma_f32_16x16x32_bf16 v[176:179], v[200:203], a[148:151], v[176:179]\n"
    "ds_write_addtid_b32 v157 offset:2304\n"
    "v_exp_f32_e32 v174, v174\n"
    "v_mfma_f32_16x16x32_bf16 v[176:179], v[204:207], a[152:155], v[176:179]\n"
    "ds_write_addtid_b32 v164 offset:3072\n"
    "v_exp_f32_e32 v175, v175\n"
    "v_mfma_f32_16x16x32_bf16 v[176:179], v[208:211], a[156:159], v[176:179]\n"
    "ds_write_addtid_b32 v165 offset:3328\n"
    "v_cvt_pk_bf16_f32 v172, v172, v173\n"
    "v_cvt_pk_bf16_f32 v173, v174, v175\n"
    "ds_write_addtid_b32 v172 offset:4096\n"
    "ds_write_addtid_b32 v173 offset:4352\n"
    "s_waitcnt lgkmcnt(10)\n"
    "s_branch .Lat_in\n"
    ".fill 253, 4, 0xbf800000\n"
    ".long 0xbedf00ff\n"
    ".Lat_w2:\n"
    "s_setpc_b64 s[2:3]\n"
    ".Lat_loop:\n"
    "v_mfma_f32_16x16x32_bf16 v[212:215], a[160:163], a[128:131], v[236:239]\n"
    "v_mfma_f32_16x16x32_bf16 v[212:215], a[164:167], a[132:135], v[212:215]\n"
    "v_exp_f32_e32 v170, v170\n"
    "v_mfma_f32_16x16x32_bf16 v[204:207], a[164:167], a[100:103], v[232:235]\n"
    "ds_write_addtid_b32 v143 offset:768\n"
    "v_mfma_f32_16x16x32_bf16 v[204:207], a[160:163], a[96:99], v[204:207]\n"
    "ds_read_b128 a[184:187], v242 offset:28672\n"
    "v_mfma_f32_16x16x32_bf16 v[196:199], a[160:163], a[64:67], v[228:231]\n"
    "v_exp_f32_e32 v171, v171\n"
    "v_mfma_f32_16x16x32_bf16 v[196:199], a[164:167], a[68:71], v[196:199]\n"
    "ds_write_addtid_b32 v150 offset:1536\n"
    "v_mfma_f32_16x16x32_bf16 v[188:191], a[164:167], a[36:39], v[224:227]\n"
    "v_cvt_pk_bf16_f32 v166, v168, v169\n"
    "v_mfma_f32_16x16x32_bf16 v[188:191], a[160:163], a[32:35], v[188:191]\n"
    "v_mfma_f32_16x16x32_bf16 v[180:183], a[160:163], a[0:3], v[220:223]\n"
    "ds_write_addtid_b32 v151 offset:1792\n"
    "v_mfma_f32_16x16x32_bf16 v[180:183], a[164:167], a[4:7], v[180:183]\n"
    "s_waitcnt lgkmcnt(10)\n"
    "v_exp_f32_e32 v176, v176\n"
    "v_mfma_f32_16x16x32_bf16 v[180:183], a[168:171], a[8:11], v[180:183]\n"
    "ds_read_b128 a[188:191], v243 offset:28672\n"
    "v_mfma_f32_16x16x32_bf16 v[180:183], a[172:175], a[12:15], v[180:183]\n"
    "v_exp_f32_e32 v177, v177\n"
    "v_mfma_f32_16x16x32_bf16 v[188:191], a[172:175], a[44:47], v[188:191]\n"
    "ds_write_addtid_b32 v158 offset:2560\n"
    "v_mfma_f32_16x16x32_bf16 v[188:191], a[168:171], a[40:43], v[188:191]\n"
    "ds_read_b128 a[160:163], v240 offset:26624\n"
    "v_mfma_f32_16x16x32_bf16 v[196:199], a[168:171], a[72:75], v[196:199]\n"
    "v_cvt_pk_bf16_f32 v167, v170, v171\n"
    "v_mfma_f32_16x16x32_bf16 v[196:199], a[172:175], a[76:79], v[196:199]\n"
    "ds_write_addtid_b32 v159 offset:2816\n"
    "v_mfma_f32_16x16x32_bf16 v[204:207], a[172:175], a[108:111], v[204:207]\n"
    "v_exp_f32_e32 v178, v178\n"
    "v_mfma_f32_16x16x32_bf16 v[204:207], a[168:171], a[104:107], v[204:207]\n"
    "ds_write_addtid_b32 v166 offset:3584\n"
    "v_mfma_f32_16x16x32_bf16 v[212:215], a[168:171], a[136:139], v[212:215]\n"
    "v_mfma_f32_16x16x32_bf16 v[212:215], a[172:175], a[140:143], v[212:215]\n"
    "s_waitcnt lgkmcnt(10)\n"
    "v_exp_f32_e32 v179, v179\n"
    "v_mfma_f32_16x16x32_bf16 v[212:215], a[176:179], a[144:147], v[212:215]\n"
    "ds_read_b128 a[164:167], v241 offset:26624\n"
    "v_mfma_f32_16x16x32_bf16 v[212:215], a[180:183], a[148:151], v[212:215]\n"
    "v_cvt_pk_bf16_f32 v174, v176, v177\n"
    "v_mfma_f32_16x16x32_bf16 v[204:207], a[180:183], a[116:119], v[204:207]\n"
    "ds_write_addtid_b32 v167 offset:3840\n"
    "v_mfma_f32_16x16x32_bf16 v[204:207], a[176:179], a[112:115], v[204:207]\n"
    "ds_read_b128 a[168:171], v240 offset:30720\n"
    "v_mfma_f32_16x16x32_bf16 v[196:199], a[176:179], a[80:83], v[196:199]\n"
    "v_cvt_pk_bf16_f32 v175, v178, v179\n"
    "v_mfma_f32_16x16x32_bf16 v[196:199], a[180:183], a[84:87], v[196:199]\n"
    "ds_write_addtid_b32 v174 offset:4608\n"
    "v_mfma_f32_16x16x32_bf16 v[188:191], a[180:183], a[52:55], v[188:191]\n"
    "ds_write_addtid_b32 v175 offset:4864\n"
    "v_mfma_f32_16x16x32_bf16 v[188:191], a[176:179], a[48:51], v[188:191]\n"
    "v_mfma_f32_16x16x32_bf16 v[180:183], a[176:179], a[16:19], v[180:183]\n"
    "ds_read_b128 a[172:175], v241 offset:30720\n"
    "v_mfma_f32_16x16x32_bf16 v[180:183], a[180:183], a[20:23], v[180:183]\n"
    "s_waitcnt lgkmcnt(10)\n"
    "s_waitcnt lgkmcnt(1)\n"
    "v_mfma_f32_16x16x32_bf16 v[180:183], a[184:187], a[24:27], v[180:183]\n"
    "s_barrier\n"
    "v_mfma_f32_16x16x32_bf16 v[180:183], a[188:191], a[28:31], v[180:183]\n"
    "s_add_u32 m0, s43, 8192\n"
    "v_mfma_f32_16x16x32_bf16 v[188:191], a[188:191], a[60:63], v[188:191]\n"
    "ds_read_b128 a[176:179], v242 offset:26624\n"
    "v_mfma_f32_16x16x32_bf16 v[188:191], a[184:187], a[56:59], v[188:191]\n"
    "buffer_load_dwordx4 v248, s[8:11], s40 offen offset:0 sc1 lds\n"
    "v_mfma_f32_16x16x32_bf16 v[196:199], a[184:187], a[88:91], v[196:199]\n"
    "ds_read2st64_b32 v[144:145], v249 offset0:0 offset1:1\n"
    "v_mfma_f32_16x16x32_bf16 v[196:199], a[188:191], a[92:95], v[196:199]\n"
    "v_mfma_f32_16x16x32_bf16 v[204:207], a[188:191], a[124:127], v[204:207]\n"
    "ds_read2st64_b32 v[146:147], v249 offset0:2 offset1:3\n"
    "v_mfma_f32_16x16x32_bf16 v[204:207], a[184:187], a[120:123], v[204:207]\n"
    "buffer_load_dwordx4 v248, s[8:11], s40 offen offset:1024 sc1 lds\n"
    "v_mfma_f32_16x16x32_bf16 v[212:215], a[184:187], a[152:155], v[212:215]\n"
    "v_mfma_f32_16x16x32_bf16 v[212:215], a[188:191], a[156:159], v[212:215]\n"
    "v_mfma_f32_16x16x32_bf16 v[216:219], a[160:163], a[128:131], v[236:239]\n"
    "ds_read_b128 a[180:183], v243 offset:26624\n"
    "v_mfma_f32_16x16x32_bf16 v[216:219], a[164:167], a[132:135], v[216:219]\n"
    "buffer_load_dwordx4 v248, s[8:11], s40 offen offset:2048 sc1 lds\n"
    "v_mfma_f32_16x16x32_bf16 v[208:211], a[164:167], a[100:103], v[232:235]\n"
    "ds_read2st64_b32 v[152:153], v249 offset0:4 offset1:5\n"
    "v_mfma_f32_16x16x32_bf16 v[208:211], a[160:163], a[96:99], v[208:211]\n"
    "ds_read_b128 a[184:187], v242 offset:30720\n"
    "v_mfma_f32_16x16x32_bf16 v[200:203], a[160:163], a[64:67], v[228:231]\n"
    "ds_read2st64_b32 v[154:155], v249 offset0:6 offset1:7\n"
    "v_mfma_f32_16x16x32_bf16 v[200:203], a[164:167], a[68:71], v[200:203]\n"
    "buffer_load_dwordx4 v248, s[8:11], s40 offen offset:3072 sc1 lds\n"
    "v_mfma_f32_16x16x32_bf16 v[192:195], a[164:167], a[36:39], v[224:227]\n"
    "s_add_u32 m0, s43, 12288\n"
    "v_exp_f32_e32 v180, v180\n"
    "v_mfma_f32_16x16x32_bf16 v[192:195], a[160:163], a[32:35], v[192:195]\n"
    "s_add_u32 s40, s40, 0x8000\n"
    "v_exp_f32_e32 v181, v181\n"
    "v_mfma_f32_16x16x32_bf16 v[184:187], a[160:163], a[0:3], v[220:223]\n"
    "ds_read2st64_b32 v[160:161], v249 offset0:8 offset1:9\n"
    "v_mfma_f32_16x16x32_bf16 v[184:187], a[164:167], a[4:7], v[184:187]\n"
    "s_waitcnt lgkmcnt(8)\n"
    "buffer_load_dwordx4 v248, s[8:11], s41 offen offset:0 sc1 lds\n"
    "v_mfma_f32_16x16x32_bf16 v[184:187], a[168:171], a[8:11], v[184:187]\n"
    ".long 0xbedf00ff\n"
    ".Lat_w3:\n"
    "s_setpc_b64 s[2:3]\n"
    "ds_read_b128 a[188:191], v243 offset:30720\n"
    "v_mfma_f32_16x16x32_bf16 v[184:187], a[172:175], a[12:15], v[184:187]\n"
    "ds_read_b64_tr_b16 a[160:161], v244 offset:16384\n"
    "v_mfma_f32_16x16x32_bf16 v[192:195], a[172:175], a[44:47], v[192:195]\n"
    "ds_read2st64_b32 v[162:163], v249 offset0:10 offset1:11\n"
    "v_mfma_f32_16x16x32_bf16 v[192:195], a[168:171], a[40:43], v[192:195]\n"
    "buffer_load_dwordx4 v248, s[8:11], s41 offen offset:1024 sc1 lds\n"
    "v_mfma_f32_16x16x32_bf16 v[200:203], a[168:171], a[72:75], v[200:203]\n"
    "ds_read_b64_tr_b16 a[162:163], v244 offset:18432\n"
    "v_mfma_f32_16x16x32_bf16 v[200:203], a[172:175], a[76:79], v[200:203]\n"
    "v_exp_f32_e32 v182, v182\n"
    "v_mfma_f32_16x16x32_bf16 v[208:211], a[172:175], a[108:111], v[208:211]\n"
    "ds_read2st64_b32 v[168:169], v249 offset0:12 offset1:13\n"
    "v_mfma_f32_16x16x32_bf16 v[208:211], a[168:171], a[104:107], v[208:211]\n"
    "buffer_load_dwordx4 v248, s[8:11], s41 offen offset:2048 sc1 lds\n"
    "v_mfma_f32_16x16x32_bf16 v[216:219], a[168:171], a[136:139], v[216:219]\n"
    "ds_read_b64_tr_b16 a[164:165], v244 offset:49152\n"
    "v_mfma_f32_16x16x32_bf16 v[216:219], a[172:175], a[140:143], v[216:219]\n"
    "s_waitcnt lgkmcnt(10)\n"
    "ds_read2st64_b32 v[170:171], v249 offset0:14 offset1:15\n"
    "v_mfma_f32_16x16x32_bf16 v[216:219], a[176:179], a[144:147], v[216:219]\n"
    "ds_read_b64_tr_b16 a[168:169], v245 offset:16384\n"
    "v_mfma_f32_16x16x32_bf16 v[216:219], a[180:183], a[148:151], v[216:219]\n"
    "buffer_load_dwordx4 v248, s[8:11], s41 offen offset:3072 sc1 lds\n"
    "v_mfma_f32_16x16x32_bf16 v[208:211], a[180:183], a[116:119], v[208:211]\n"
    "ds_read_b64_tr_b16 a[166:167], v244 offset:51200\n"
    "v_mfma_f32_16x16x32_bf16 v[208:211], a[176:179], a[112:115], v[208:211]\n"
    "s_add_u32 s41, s41, 0x8000\n"
    "v_exp_f32_e32 v183, v183\n"
    "v_mfma_f32_16x16x32_bf16 v[200:203], a[176:179], a[80:83], v[200:203]\n"
    "ds_read_b64_tr_b16 a[170:171], v245 offset:18432\n"
    "v_cvt_pk_bf16_f32 v180, v180, v181\n"
    "v_mfma_f32_16x16x32_bf16 v[200:203], a[180:183], a[84:87], v[200:203]\n"
    "ds_read2st64_b32 v[176:177], v249 offset0:16 offset1:17\n"
    "v_mfma_f32_16x16x32_bf16 v[192:195], a[180:183], a[52:55], v[192:195]\n"
    "v_exp_f32_e32 v188, v188\n"
    "v_mfma_f32_16x16x32_bf16 v[192:195], a[176:179], a[48:51], v[192:195]\n"
    "v_exp_f32_e32 v189, v189\n"
    "v_mfma_f32_16x16x32_bf16 v[184:187], a[176:179], a[16:19], v[184:187]\n"
    "ds_read_b64_tr_b16 a[172:173], v245 offset:49152\n"
    "v_cvt_pk_bf16_f32 v181, v182, v183\n"
    "v_mfma_f32_16x16x32_bf16 v[184:187], a[180:183], a[20:23], v[184:187]\n"
    "s_waitcnt lgkmcnt(11)\n"
    "ds_read2st64_b32 v[178:179], v249 offset0:18 offset1:19\n"
    "v_mfma_f32_16x16x32_bf16 v[184:187], a[184:187], a[24:27], v[184:187]\n"
    "ds_read_b64_tr_b16 a[176:177], v246 offset:16384\n"
    "v_mfma_f32_16x16x32_bf16 v[184:187], a[188:191], a[28:31], v[184:187]\n"
    "ds_read_b64_tr_b16 a[174:175], v245 offset:51200\n"
    "v_mfma_f32_16x16x32_bf16 v[192:195], a[188:191], a[60:63], v[192:195]\n"
    "v_exp_f32_e32 v190, v190\n"
    "v_mfma_f32_16x16x32_bf16 v[192:195], a[184:187], a[56:59], v[192:195]\n"
    "v_exp_f32_e32 v191, v191\n"
    "v_mfma_f32_16x16x32_bf16 v[200:203], a[184:187], a[88:91], v[200:203]\n"
    "v_mfma_f32_16x16x32_bf16 v[200:203], a[188:191], a[92:95], v[200:203]\n"
    "ds_read_b64_tr_b16 a[178:179], v246 offset:18432\n"
    "v_mfma_f32_16x16x32_bf16 v[208:211], a[188:191], a[124:127], v[208:211]\n"
    "v_cvt_pk_bf16_f32 v188, v188, v189\n"
    "v_mfma_f32_16x16x32_bf16 v[208:211], a[184:187], a[120:123], v[208:211]\n"
    "v_exp_f32_e32 v196, v196\n"
    "v_mfma_f32_16x16x32_bf16 v[216:219], a[184:187], a[152:155], v[216:219]\n"
    "ds_read_b64_tr_b16 a[180:181], v246 offset:49152\n"
    "v_mfma_f32_16x16x32_bf16 v[216:219], a[188:191], a[156:159], v[216:219]\n"
    "s_waitcnt lgkmcnt(4)\n"
    "v_exp_f32_e32 v197, v197\n"
    "v_mfma_f32_16x16x32_bf16 a[236:239], a[160:163], v[140:143], a[236:239]\n"
    "v_mfma_f32_16x16x32_bf16 a[236:239], a[164:167], v[144:147], a[236:239]\n"
    "ds_read_b64_tr_b16 a[182:183], v246 offset:51200\n"
    "v_mfma_f32_16x16x32_bf16 a[240:243], a[164:167], v[152:155], a[240:243]\n"
    "ds_read_b64_tr_b16 a[184:185], v247 offset:16384\n"
    "v_mfma_f32_16x16x32_bf16 a[240:243], a[160:163], v[148:151], a[240:243]\n"
    "v_cvt_pk_bf16_f32 v189, v190, v191\n"
    "v_mfma_f32_16x16x32_bf16 a[244:247], a[160:163], v[156:159], a[244:247]\n"
    "ds_read_b64_tr_b16 a[186:187], v247 offset:18432\n"
    "v_mfma_f32_16x16x32_bf16 a[244:247], a[164:167], v[160:163], a[244:247]\n"
    "v_exp_f32_e32 v198, v198\n"
    "v_mfma_f32_16x16x32_bf16 a[248:251], a[164:167], v[168:171], a[248:251]\n"
    "v_exp_f32_e32 v199, v199\n"
    "v_mfma_f32_16x16x32_bf16 a[248:251], a[160:163], v[164:167], a[248:251]\n"
    "v_cvt_pk_bf16_f32 v196, v196, v197\n"
    "v_mfma_f32_16x16x32_bf16 a[252:255], a[160:163], v[172:175], a[252:255]\n"
    "ds_read_b64_tr_b16 a[188:189], v247 offset:49152\n"
    "v_mfma_f32_16x16x32_bf16 a[252:255], a[164:167], v[176:179], a[252:255]\n"
    "s_waitcnt lgkmcnt(6)\n"
    "v_exp_f32_e32 v204, v204\n"
    "v_mfma_f32_16x16x32_bf16 v[16:19], a[172:175], v[176:179], v[16:19]\n"
    "ds_read_b64_tr_b16 a[160:161], v244 offset:20480\n"
    "v_mfma_f32_16x16x32_bf16 v[16:19], a[168:171], v[172:175], v[16:19]\n"
    "ds_read_b64_tr_b16 a[190:191], v247 offset:51200\n"
    "v_mfma_f32_16x16x32_bf16 v[12:15], a[168:171], v[164:167], v[12:15]\n"
    "v_exp_f32_e32 v205, v205\n"
    "v_mfma_f32_16x16x32_bf16 v[12:15], a[172:175], v[168:171], v[12:15]\n"
    "v_cvt_pk_bf16_f32 v197, v198, v199\n"
    "v_mfma_f32_16x16x32_bf16 v[8:11], a[172:175], v[160:163], v[8:11]\n"
    "ds_read_b64_tr_b16 a[162:163], v244 offset:22528\n"
    "v_mfma_f32_16x16x32_bf16 v[8:11], a[168:171], v[156:159], v[8:11]\n"
    "v_exp_f32_e32 v206, v206\n"
    "v_mfma_f32_16x16x32_bf16 v[4:7], a[168:171], v[148:151], v[4:7]\n"
    "v_exp_f32_e32 v207, v207\n"
    "v_mfma_f32_16x16x32_bf16 v[4:7], a[172:175], v[152:155], v[4:7]\n"
    "v_cvt_pk_bf16_f32 v204, v204, v205\n"
    "v_mfma_f32_16x16x32_bf16 v[0:3], a[172:175], v[144:147], v[0:3]\n"
    "ds_read_b64_tr_b16 a[164:165], v244 offset:53248\n"
    "v_mfma_f32_16x16x32_bf16 v[0:3], a[168:171], v[140:143], v[0:3]\n"
    "s_waitcnt lgkmcnt(7)\n"
    "v_exp_f32_e32 v212, v212\n"
    "v_mfma_f32_16x16x32_bf16 v[20:23], a[176:179], v[140:143], v[20:23]\n"
    "v_mfma_f32_16x16x32_bf16 v[20:23], a[180:183], v[144:147], v[20:23]\n"
    "ds_read_b64_tr_b16 a[166:167], v244 offset:55296\n"
    "v_mfma_f32_16x16x32_bf16 v[24:27], a[180:183], v[152:155], v[24:27]\n"
    "ds_read_b64_tr_b16 a[168:169], v245 offset:20480\n"
    "v_mfma_f32_16x16x32_bf16 v[24:27], a[176:179], v[148:151], v[24:27]\n"
    "v_exp_f32_e32 v213, v213\n"
    "v_mfma_f32_16x16x32_bf16 v[28:31], a[176:179], v[156:159], v[28:31]\n"
    "ds_read_b64_tr_b16 a[170:171], v245 offset:22528\n"
    "v_mfma_f32_16x16x32_bf16 v[28:31], a[180:183], v[160:163], v[28:31]\n"
    "v_cvt_pk_bf16_f32 v205, v206, v207\n"
    "v_mfma_f32_16x16x32_bf16 v[32:35], a[180:183], v[168:171], v[32:35]\n"
    "v_exp_f32_e32 v214, v214\n"
    "v_mfma_f32_16x16x32_bf16 v[32:35], a[176:179], v[164:167], v[32:35]\n"
    "v_exp_f32_e32 v215, v215\n"
    "v_mfma_f32_16x16x32_bf16 v[36:39], a[176:179], v[172:175], v[36:39]\n"
    "ds_read_b64_tr_b16 a[172:173], v245 offset:53248\n"
    "v_mfma_f32_16x16x32_bf16 v[36:39], a[180:183], v[176:179], v[36:39]\n"
    "s_waitcnt lgkmcnt(6)\n"
    "v_cvt_pk_bf16_f32 v212, v212, v213\n"
    "v_mfma_f32_16x16x32_bf16 v[56:59], a[188:191], v[176:179], v[56:59]\n"
    "ds_read_b64_tr_b16 a[176:177], v246 offset:20480\n"
    "v_mfma_f32_16x16x32_bf16 v[56:59], a[184:187], v[172:175], v[56:59]\n"
    "ds_read_b64_tr_b16 a[174:175], v245 offset:55296\n"
    "v_mfma_f32_16x16x32_bf16 v[52:55], a[184:187], v[164:167], v[52:55]\n"
    "v_cvt_pk_bf16_f32 v213, v214, v215\n"
    "v_mfma_f32_16x16x32_bf16 v[52:55], a[188:191], v[168:171], v[52:55]\n"
    "v_exp_f32_e32 v184, v184\n"
    "v_mfma_f32_16x16x32_bf16 v[48:51], a[188:191], v[160:163], v[48:51]\n"
    "ds_read_b64_tr_b16 a[178:179], v246 offset:22528\n"
    "v_mfma_f32_16x16x32_bf16 v[48:51], a[184:187], v[156:159], v[48:51]\n"
    "v_mfma_f32_16x16x32_bf16 v[44:47], a[184:187], v[148:151], v[44:47]\n"
    "v_exp_f32_e32 v185, v185\n"
    "v_mfma_f32_16x16x32_bf16 v[44:47], a[188:191], v[152:155], v[44:47]\n"
    "v_exp_f32_e32 v186, v186\n"
    "v_mfma_f32_16x16x32_bf16 v[40:43], a[188:191], v[144:147], v[40:43]\n"
    "ds_read_b64_tr_b16 a[180:181], v246 offset:53248\n"
    "v_mfma_f32_16x16x32_bf16 v[40:43], a[184:187], v[140:143], v[40:43]\n"
    "s_waitcnt lgkmcnt(7)\n"
    "v_exp_f32_e32 v187, v187\n"
    "v_mfma_f32_16x16x32_bf16 v[60:63], a[160:163], v[140:143], v[60:63]\n"
    "ds_read_b64_tr_b16 a[184:185], v247 offset:20480\n"
    "v_mfma_f32_16x16x32_bf16 v[60:63], a[164:167], v[144:147], v[60:63]\n"
    "ds_read_b64_tr_b16 a[182:183], v246 offset:55296\n"
    "v_mfma_f32_16x16x32_bf16 v[64:67], a[164:167], v[152:155], v[64:67]\n"
    "ds_read_b64_tr_b16 a[186:187], v247 offset:22528\n"
    "v_mfma_f32_16x16x32_bf16 v[64:67], a[160:163], v[148:151], v[64:67]\n"
    "v_cvt_pk_bf16_f32 v182, v184, v185\n"
    "v_mfma_f32_16x16x32_bf16 v[68:71], a[160:163], v[156:159], v[68:71]\n"
    "v_exp_f32_e32 v192, v192\n"
    "v_mfma_f32_16x16x32_bf16 v[68:71], a[164:167], v[160:163], v[68:71]\n"
    "ds_read_b64_tr_b16 a[188:189], v247 offset:53248\n"
    "v_mfma_f32_16x16x32_bf16 v[72:75], a[164:167], v[168:171], v[72:75]\n"
    "ds_read_b64_tr_b16 a[190:191], v247 offset:55296\n"
    "v_mfma_f32_16x16x32_bf16 v[72:75], a[160:163], v[164:167], v[72:75]\n"
    "s_waitcnt vmcnt(9)\n"
    "v_mfma_f32_16x16x32_bf16 v[76:79], a[160:163], v[172:175], v[76:79]\n"
    "s_barrier\n"
    "v_mfma_f32_16x16x32_bf16 v[76:79], a[164:167], v[176:179], v[76:79]\n"
    "s_waitcnt lgkmcnt(7)\n"
    "s_mov_b32 m0, s42\n"
    "v_mfma_f32_16x16x32_bf16 v[96:99], a[172:175], v[176:179], v[96:99]\n"
    "v_exp_f32_e32 v193, v193\n"
    "v_mfma_f32_16x16x32_bf16 v[96:99], a[168:171], v[172:175], v[96:99]\n"
    "v_cvt_pk_bf16_f32 v183, v186, v187\n"
    "v_mfma_f32_16x16x32_bf16 v[92:95], a[168:171], v[164:167], v[92:95]\n"
    "v_exp_f32_e32 v194, v194\n"
    "v_mfma_f32_16x16x32_bf16 v[92:95], a[172:175], v[168:171], v[92:95]\n"
    "ds_read_b128 a[160:163], v240 offset:0\n"
    "v_mfma_f32_16x16x32_bf16 v[88:91], a[172:175], v[160:163], v[88:91]\n"
    "ds_write_addtid_b32 v180 offset:0\n"
    "v_mfma_f32_16x16x32_bf16 v[88:91], a[168:171], v[156:159], v[88:91]\n"
    "v_exp_f32_e32 v195, v195\n"
    "v_mfma_f32_16x16x32_bf16 v[84:87], a[168:171], v[148:151], v[84:87]\n"
    "ds_write_addtid_b32 v181 offset:256\n"
    "v_mfma_f32_16x16x32_bf16 v[84:87], a[172:175], v[152:155], v[84:87]\n"
    "ds_write_addtid_b32 v188 offset:1024\n"
    "v_mfma_f32_16x16x32_bf16 v[80:83], a[172:175], v[144:147], v[80:83]\n"
    "v_cvt_pk_bf16_f32 v190, v192, v193\n"
    "v_mfma_f32_16x16x32_bf16 v[80:83], a[168:171], v[140:143], v[80:83]\n"
    "s_waitcnt lgkmcnt(7)\n"
    "v_exp_f32_e32 v200, v200\n"
    "v_mfma_f32_16x16x32_bf16 v[100:103], a[176:179], v[140:143], v[100:103]\n"
    "ds_read_b128 a[164:167], v241 offset:0\n"
    "v_mfma_f32_16x16x32_bf16 v[100:103], a[180:183], v[144:147], v[100:103]\n"
    "ds_write_addtid_b32 v189 offset:1280\n"
    "v_mfma_f32_16x16x32_bf16 v[104:107], a[180:183], v[152:155], v[104:107]\n"
    "v_exp_f32_e32 v201, v201\n"
    "v_mfma_f32_16x16x32_bf16 v[104:107], a[176:179], v[148:151], v[104:107]\n"
    "ds_read_b128 a[168:171], v240 offset:4096\n"
    "v_mfma_f32_16x16x32_bf16 v[108:111], a[176:179], v[156:159], v[108:111]\n"
    "ds_write_addtid_b32 v196 offset:2048\n"
    "v_mfma_f32_16x16x32_bf16 v[108:111], a[180:183], v[160:163], v[108:111]\n"
    "v_cvt_pk_bf16_f32 v191, v194, v195\n"
    "v_mfma_f32_16x16x32_bf16 v[112:115], a[180:183], v[168:171], v[112:115]\n"
    "ds_write_addtid_b32 v197 offset:2304\n"
    "v_mfma_f32_16x16x32_bf16 v[112:115], a[176:179], v[164:167], v[112:115]\n"
    "v_exp_f32_e32 v202, v202\n"
    "v_mfma_f32_16x16x32_bf16 v[116:119], a[176:179], v[172:175], v[116:119]\n"
    "ds_write_addtid_b32 v204 offset:3072\n"
    "v_mfma_f32_16x16x32_bf16 v[116:119], a[180:183], v[176:179], v[116:119]\n"
    "s_waitcnt lgkmcnt(10)\n"
    "v_exp_f32_e32 v203, v203\n"
    "v_mfma_f32_16x16x32_bf16 v[136:139], a[188:191], v[176:179], v[136:139]\n"
    "ds_read_b128 a[172:175], v241 offset:4096\n"
    "v_mfma_f32_16x16x32_bf16 v[136:139], a[184:187], v[172:175], v[136:139]\n"
    "ds_write_addtid_b32 v205 offset:3328\n"
    "v_cvt_pk_bf16_f32 v198, v200, v201\n"
    "v_mfma_f32_16x16x32_bf16 v[132:135], a[184:187], v[164:167], v[132:135]\n"
    "ds_read_b128 a[176:179], v242 offset:0\n"
    "v_mfma_f32_16x16x32_bf16 v[132:135], a[188:191], v[168:171], v[132:135]\n"
    "ds_write_addtid_b32 v212 offset:4096\n"
    "v_mfma_f32_16x16x32_bf16 v[128:131], a[188:191], v[160:163], v[128:131]\n"
    "v_exp_f32_e32 v208, v208\n"
    "v_mfma_f32_16x16x32_bf16 v[128:131], a[184:187], v[156:159], v[128:131]\n"
    "ds_write_addtid_b32 v213 offset:4352\n"
    "v_mfma_f32_16x16x32_bf16 v[124:127], a[184:187], v[148:151], v[124:127]\n"
    "v_exp_f32_e32 v209, v209\n"
    "v_mfma_f32_16x16x32_bf16 v[124:127], a[188:191], v[152:155], v[124:127]\n"
    "ds_read_b128 a[180:183], v243 offset:0\n"
    "v_mfma_f32_16x16x32_bf16 v[120:123], a[188:191], v[144:147], v[120:123]\n"
    "ds_write_addtid_b32 v182 offset:512\n"
    "v_mfma_f32_16x16x32_bf16 v[120:123], a[184:187], v[140:143], v[120:123]\n"
    "s_waitcnt lgkmcnt(12)\n"
    "v_cvt_pk_bf16_f32 v199, v202, v203\n"
    "v_mfma_f32_16x16x32_bf16 v[172:175], a[160:163], a[128:131], v[236:239]\n"
    "v_mfma_f32_16x16x32_bf16 v[172:175], a[164:167], a[132:135], v[172:175]\n"
    "v_exp_f32_e32 v210, v210\n"
    "v_mfma_f32_16x16x32_bf16 v[164:167], a[164:167], a[100:103], v[232:235]\n"
    "ds_write_addtid_b32 v183 offset:768\n"
    "v_mfma_f32_16x16x32_bf16 v[164:167], a[160:163], a[96:99], v[164:167]\n"
    "ds_read_b128 a[184:187], v242 offset:4096\n"
    "v_mfma_f32_16x16x32_bf16 v[156:159], a[160:163], a[64:67], v[228:231]\n"
    "v_exp_f32_e32 v211, v211\n"
    "v_mfma_f32_16x16x32_bf16 v[156:159], a[164:167], a[68:71], v[156:159]\n"
    "ds_write_addtid_b32 v190 offset:1536\n"
    "v_mfma_f32_16x16x32_bf16 v[148:151], a[164:167], a[36:39], v[224:227]\n"
    "v_cvt_pk_bf16_f32 v206, v208, v209\n"
    "v_mfma_f32_16x16x32_bf16 v[148:151], a[160:163], a[32:35], v[148:151]\n"
    "v_mfma_f32_16x16x32_bf16 v[140:143], a[160:163], a[0:3], v[220:223]\n"
    "ds_write_addtid_b32 v191 offset:1792\n"
    "v_mfma_f32_16x16x32_bf16 v[140:143], a[164:167], a[4:7], v[140:143]\n"
    "s_waitcnt lgkmcnt(10)\n"
    "v_exp_f32_e32 v216, v216\n"
    "v_mfma_f32_16x16x32_bf16 v[140:143], a[168:171], a[8:11], v[140:143]\n"
    "ds_read_b128 a[188:191], v243 offset:4096\n"
    "v_mfma_f32_16x16x32_bf16 v[140:143], a[172:175], a[12:15], v[140:143]\n"
    "v_exp_f32_e32 v217, v217\n"
    "v_mfma_f32_16x16x32_bf16 v[148:151], a[172:175], a[44:47], v[148:151]\n"
    "ds_write_addtid_b32 v198 offset:2560\n"
    "v_mfma_f32_16x16x32_bf16 v[148:151], a[168:171], a[40:43], v[148:151]\n"
    "ds_read_b128 a[160:163], v240 offset:2048\n"
    "v_mfma_f32_16x16x32_bf16 v[156:159], a[168:171], a[72:75], v[156:159]\n"
    "v_cvt_pk_bf16_f32 v207, v210, v211\n"
    "v_mfma_f32_16x16x32_bf16 v[156:159], a[172:175], a[76:79], v[156:159]\n"
    "ds_write_addtid_b32 v199 offset:2816\n"
    "v_mfma_f32_16x16x32_bf16 v[164:167], a[172:175], a[108:111], v[164:167]\n"
    "v_exp_f32_e32 v218, v218\n"
    "v_mfma_f32_16x16x32_bf16 v[164:167], a[168:171], a[104:107], v[164:167]\n"
    "ds_write_addtid_b32 v206 offset:3584\n"
    "v_mfma_f32_16x16x32_bf16 v[172:175], a[168:171], a[136:139], v[172:175]\n"
    "v_mfma_f32_16x16x32_bf16 v[172:175], a[172:175], a[140:143], v[172:175]\n"
    "s_waitcnt lgkmcnt(10)\n"
    "v_exp_f32_e32 v219, v219\n"
    "v_mfma_f32_16x16x32_bf16 v[172:175], a[176:179], a[144:147], v[172:175]\n"
    "ds_read_b128 a[164:167], v241 offset:2048\n"
    "v_mfma_f32_16x16x32_bf16 v[172:175], a[180:183], a[148:151], v[172:175]\n"
    "v_cvt_pk_bf16_f32 v214, v216, v217\n"
    "v_mfma_f32_16x16x32_bf16 v[164:167], a[180:183], a[116:119], v[164:167]\n"
    "ds_write_addtid_b32 v207 offset:3840\n"
    "v_mfma_f32_16x16x32_bf16 v[164:167], a[176:179], a[112:115], v[164:167]\n"
    "ds_read_b128 a[168:171], v240 offset:6144\n"
    "v_mfma_f32_16x16x32_bf16 v[156:159], a[176:179], a[80:83], v[156:159]\n"
    "v_cvt_pk_bf16_f32 v215, v218, v219\n"
    "v_mfma_f32_16x16x32_bf16 v[156:159], a[180:183], a[84:87], v[156:159]\n"
    "ds_write_addtid_b32 v214 offset:4608\n"
    "v_mfma_f32_16x16x32_bf16 v[148:151], a[180:183], a[52:55], v[148:151]\n"
    "ds_write_addtid_b32 v215 offset:4864\n"
    "v_mfma_f32_16x16x32_bf16 v[148:151], a[176:179], a[48:51], v[148:151]\n"
    "v_mfma_f32_16x16x32_bf16 v[140:143], a[176:179], a[16:19], v[140:143]\n"
    "ds_read_b128 a[172:175], v241 offset:6144\n"
    "v_mfma_f32_16x16x32_bf16 v[140:143], a[180:183], a[20:23], v[140:143]\n"
    "s_waitcnt lgkmcnt(10)\n"
    "s_waitcnt lgkmcnt(1)\n"
    "v_mfma_f32_16x16x32_bf16 v[140:143], a[184:187], a[24:27], v[140:143]\n"
    "s_barrier\n"
    "v_mfma_f32_16x16x32_bf16 v[140:143], a[188:191], a[28:31], v[140:143]\n"
    "s_add_u32 m0, s43, 16384\n"
    "v_mfma_f32_16x16x32_bf16 v[148:151], a[188:191], a[60:63], v[148:151]\n"
    "ds_read_b128 a[176:179], v242 offset:2048\n"
    "v_mfma_f32_16x16x32_bf16 v[148:151], a[184:187], a[56:59], v[148:151]\n"
    "buffer_load_dwordx4 v248, s[8:11], s40 offen offset:0 sc1 lds\n"
    "v_mfma_f32_16x16x32_bf16 v[156:159], a[184:187], a[88:91], v[156:159]\n"
    "ds_read2st64_b32 v[184:185], v249 offset0:0 offset1:1\n"
    "v_mfma_f32_16x16x32_bf16 v[156:159], a[188:191], a[92:95], v[156:159]\n"
    "v_mfma_f32_16x16x32_bf16 v[164:167], a[188:191], a[124:127], v[164:167]\n"
    "ds_read2st64_b32 v[186:187], v249 offset0:2 offset1:3\n"
    "v_mfma_f32_16x16x32_bf16 v[164:167], a[184:187], a[120:123], v[164:167]\n"
    "buffer_load_dwordx4 v248, s[8:11], s40 offen offset:1024 sc1 lds\n"
    "v_mfma_f32_16x16x32_bf16 v[172:175], a[184:187], a[152:155], v[172:175]\n"
    "v_mfma_f32_16x16x32_bf16 v[172:175], a[188:191], a[156:159], v[172:175]\n"
    "v_mfma_f32_16x16x32_bf16 v[176:179], a[160:163], a[128:131], v[236:239]\n"
    "ds_read_b128 a[180:183], v243 offset:2048\n"
    "v_mfma_f32_16x16x32_bf16 v[176:179], a[164:167], a[132:135], v[176:179]\n"
    "buffer_load_dwordx4 v248, s[8:11], s40 offen offset:2048 sc1 lds\n"
    "v_mfma_f32_16x16x32_bf16 v[168:171], a[164:167], a[100:103], v[232:235]\n"
    "ds_read2st64_b32 v[192:193], v249 offset0:4 offset1:5\n"
    "v_mfma_f32_16x16x32_bf16 v[168:171], a[160:163], a[96:99], v[168:171]\n"
    "ds_read_b128 a[184:187], v242 offset:6144\n"
    "v_mfma_f32_16x16x32_bf16 v[160:163], a[160:163], a[64:67], v[228:231]\n"
    "ds_read2st64_b32 v[194:195], v249 offset0:6 offset1:7\n"
    "v_mfma_f32_16x16x32_bf16 v[160:163], a[164:167], a[68:71], v[160:163]\n"
    "buffer_load_dwordx4 v248, s[8:11], s40 offen offset:3072 sc1 lds\n"
    "v_mfma_f32_16x16x32_bf16 v[152:155], a[164:167], a[36:39], v[224:227]\n"
    "s_add_u32 m0, s43, 20480\n"
    "v_exp_f32_e32 v140, v140\n"
    "v_mfma_f32_16x16x32_bf16 v[152:155], a[160:163], a[32:35], v[152:155]\n"
    "s_add_u32 s40, s40, 0x8000\n"
    "v_exp_f32_e32 v141, v141\n"
    "v_mfma_f32_16x16x32_bf16 v[144:147], a[160:163], a[0:3], v[220:223]\n"
    "ds_read2st64_b32 v[200:201], v249 offset0:8 offset1:9\n"
    "v_mfma_f32_16x16x32_bf16 v[144:147], a[164:167], a[4:7], v[144:147]\n"
    "s_waitcnt lgkmcnt(8)\n"
    "buffer_load_dwordx4 v248, s[8:11], s41 offen offset:0 sc1 lds\n"
    "v_mfma_f32_16x16x32_bf16 v[144:147], a[168:171], a[8:11], v[144:147]\n"
    "ds_read_b128 a[188:191], v243 offset:6144\n"
    "v_mfma_f32_16x16x32_bf16 v[144:147], a[172:175], a[12:15], v[144:147]\n"
    "ds_read_b64_tr_b16 a[160:161], v244 offset:24576\n"
    "v_mfma_f32_16x16x32_bf16 v[152:155], a[172:175], a[44:47], v[152:155]\n"
    "ds_read2st64_b32 v[202:203], v249 offset0:10 offset1:11\n"
    "v_mfma_f32_16x16x32_bf16 v[152:155], a[168:171], a[40:43], v[152:155]\n"
    "buffer_load_dwordx4 v248, s[8:11], s41 offen offset:1024 sc1 lds\n"
    "v_mfma_f32_16x16x32_bf16 v[160:163], a[168:171], a[72:75], v[160:163]\n"
    "ds_read_b64_tr_b16 a[162:163], v244 offset:26624\n"
    "v_mfma_f32_16x16x32_bf16 v[160:163], a[172:175], a[76:79], v[160:163]\n"
    "v_exp_f32_e32 v142, v142\n"
    "v_mfma_f32_16x16x32_bf16 v[168:171], a[172:175], a[108:111], v[168:171]\n"
    "ds_read2st64_b32 v[208:209], v249 offset0:12 offset1:13\n"
    "v_mfma_f32_16x16x32_bf16 v[168:171], a[168:171], a[104:107], v[168:171]\n"
    "buffer_load_dwordx4 v248, s[8:11], s41 offen offset:2048 sc1 lds\n"
    "v_mfma_f32_16x16x32_bf16 v[176:179], a[168:171], a[136:139], v[176:179]\n"
    "ds_read_b64_tr_b16 a[164:165], v244 offset:57344\n"
    "v_mfma_f32_16x16x32_bf16 v[176:179], a[172:175], a[140:143], v[176:179]\n"
    "s_waitcnt lgkmcnt(10)\n"
    "ds_read2st64_b32 v[210:211], v249 offset0:14 offset1:15\n"
    "v_mfma_f32_16x16x32_bf16 v[176:179], a[176:179], a[144:147], v[176:179]\n"
    "ds_read_b64_tr_b16 a[168:169], v245 offset:24576\n"
    "v_mfma_f32_16x16x32_bf16 v[176:179], a[180:183], a[148:151], v[176:179]\n"
    "buffer_load_dwordx4 v248, s[8:11], s41 offen offset:3072 sc1 lds\n"
    "v_mfma_f32_16x16x32_bf16 v[168:171], a[180:183], a[116:119], v[168:171]\n"
    "ds_read_b64_tr_b16 a[166:167], v244 offset:59392\n"
    "v_mfma_f32_16x16x32_bf16 v[168:171], a[176:179], a[112:115], v[168:171]\n"
    "s_add_u32 s41, s41, 0x8000\n"
    "v_exp_f32_e32 v143, v143\n"
    "v_mfma_f32_16x16x32_bf16 v[160:163], a[176:179], a[80:83], v[160:163]\n"
    "ds_read_b64_tr_b16 a[170:171], v245 offset:26624\n"
    "v_cvt_pk_bf16_f32 v140, v140, v141\n"
    "v_mfma_f32_16x16x32_bf16 v[160:163], a[180:183], a[84:87], v[160:163]\n"
    "ds_read2st64_b32 v[216:217], v249 offset0:16 offset1:17\n"
    "v_mfma_f32_16x16x32_bf16 v[152:155], a[180:183], a[52:55], v[152:155]\n"
    "v_exp_f32_e32 v148, v148\n"
    "v_mfma_f32_16x16x32_bf16 v[152:155], a[176:179], a[48:51], v[152:155]\n"
    "v_exp_f32_e32 v149, v149\n"
    "v_mfma_f32_16x16x32_bf16 v[144:147], a[176:179], a[16:19], v[144:147]\n"
    "ds_read_b64_tr_b16 a[172:173], v245 offset:57344\n"
    "v_cvt_pk_bf16_f32 v141, v142, v143\n"
    "v_mfma_f32_16x16x32_bf16 v[144:147], a[180:183], a[20:23], v[144:147]\n"
    "s_waitcnt lgkmcnt(11)\n"
    "ds_read2st64_b32 v[218:219], v249 offset0:18 offset1:19\n"
    "v_mfma_f32_16x16x32_bf16 v[144:147], a[184:187], a[24:27], v[144:147]\n"
    "ds_read_b64_tr_b16 a[176:177], v246 offset:24576\n"
    "v_mfma_f32_16x16x32_bf16 v[144:147], a[188:191], a[28:31], v[144:147]\n"
    "ds_read_b64_tr_b16 a[174:175], v245 offset:59392\n"
    "v_mfma_f32_16x16x32_bf16 v[152:155], a[188:191], a[60:63], v[152:155]\n"
    "v_exp_f32_e32 v150, v150\n"
    "v_mfma_f32_16x16x32_bf16 v[152:155], a[184:187], a[56:59], v[152:155]\n"
    "v_exp_f32_e32 v151, v151\n"
    "v_mfma_f32_16x16x32_bf16 v[160:163], a[184:187], a[88:91], v[160:163]\n"
    "v_mfma_f32_16x16x32_bf16 v[160:163], a[188:191], a[92:95], v[160:163]\n"
    "ds_read_b64_tr_b16 a[178:179], v246 offset:26624\n"
    "v_mfma_f32_16x16x32_bf16 v[168:171], a[188:191], a[124:127], v[168:171]\n"
    "v_cvt_pk_bf16_f32 v148, v148, v149\n"
    "v_mfma_f32_16x16x32_bf16 v[168:171], a[184:187], a[120:123], v[168:171]\n"
    "v_exp_f32_e32 v156, v156\n"
    "v_mfma_f32_16x16x32_bf16 v[176:179], a[184:187], a[152:155], v[176:179]\n"
    "ds_read_b64_tr_b16 a[180:181], v246 offset:57344\n"
    "v_mfma_f32_16x16x32_bf16 v[176:179], a[188:191], a[156:159], v[176:179]\n"
    "s_waitcnt lgkmcnt(4)\n"
    "v_exp_f32_e32 v157, v157\n"
    "v_mfma_f32_16x16x32_bf16 a[236:239], a[160:163], v[180:183], a[236:239]\n"
    "v_mfma_f32_16x16x32_bf16 a[236:239], a[164:167], v[184:187], a[236:239]\n"
    "ds_read_b64_tr_b16 a[182:183], v246 offset:59392\n"
    "v_mfma_f32_16x16x32_bf16 a[240:243], a[164:167], v[192:195], a[240:243]\n"
    "ds_read_b64_tr_b16 a[184:185], v247 offset:24576\n"
    "v_mfma_f32_16x16x32_bf16 a[240:243], a[160:163], v[188:191], a[240:243]\n"
    "v_cvt_pk_bf16_f32 v149, v150, v151\n"
    "v_mfma_f32_16x16x32_bf16 a[244:247], a[160:163], v[196:199], a[244:247]\n"
    "ds_read_b64_tr_b16 a[186:187], v247 offset:26624\n"
    "v_mfma_f32_16x16x32_bf16 a[244:247], a[164:167], v[200:203], a[244:247]\n"
    "v_exp_f32_e32 v158, v158\n"
    "v_mfma_f32_16x16x32_bf16 a[248:251], a[164:167], v[208:211], a[248:251]\n"
    "v_exp_f32_e32 v159, v159\n"
    "v_mfma_f32_16x16x32_bf16 a[248:251], a[160:163], v[204:207], a[248:251]\n"
    "v_cvt_pk_bf16_f32 v156, v156, v157\n"
    "v_mfma_f32_16x16x32_bf16 a[252:255], a[160:163], v[212:215], a[252:255]\n"
    "ds_read_b64_tr_b16 a[188:189], v247 offset:57344\n"
    "v_mfma_f32_16x16x32_bf16 a[252:255], a[164:167], v[216:219], a[252:255]\n"
    "s_waitcnt lgkmcnt(6)\n"
    "v_exp_f32_e32 v164, v164\n"
    "v_mfma_f32_16x16x32_bf16 v[16:19], a[172:175], v[216:219], v[16:19]\n"
    "ds_read_b64_tr_b16 a[160:161], v244 offset:28672\n"
    "v_mfma_f32_16x16x32_bf16 v[16:19], a[168:171], v[212:215], v[16:19]\n"
    "ds_read_b64_tr_b16 a[190:191], v247 offset:59392\n"
    "v_mfma_f32_16x16x32_bf16 v[12:15], a[168:171], v[204:207], v[12:15]\n"
    "v_exp_f32_e32 v165, v165\n"
    "v_mfma_f32_16x16x32_bf16 v[12:15], a[172:175], v[208:211], v[12:15]\n"
    "v_cvt_pk_bf16_f32 v157, v158, v159\n"
    "v_mfma_f32_16x16x32_bf16 v[8:11], a[172:175], v[200:203], v[8:11]\n"
    "ds_read_b64_tr_b16 a[162:163], v244 offset:30720\n"
    "v_mfma_f32_16x16x32_bf16 v[8:11], a[168:171], v[196:199], v[8:11]\n"
    "v_exp_f32_e32 v166, v166\n"
    "v_mfma_f32_16x16x32_bf16 v[4:7], a[168:171], v[188:191], v[4:7]\n"
    "v_exp_f32_e32 v167, v167\n"
    "v_mfma_f32_16x16x32_bf16 v[4:7], a[172:175], v[192:195], v[4:7]\n"
    "v_cvt_pk_bf16_f32 v164, v164, v165\n"
    "v_mfma_f32_16x16x32_bf16 v[0:3], a[172:175], v[184:187], v[0:3]\n"
    "ds_read_b64_tr_b16 a[164:165], v244 offset:61440\n"
    "v_mfma_f32_16x16x32_bf16 v[0:3], a[168:171], v[180:183], v[0:3]\n"
    "s_waitcnt lgkmcnt(7)\n"
    "v_exp_f32_e32 v172, v172\n"
    "v_mfma_f32_16x16x32_bf16 v[20:23], a[176:179], v[180:183], v[20:23]\n"
    "v_mfma_f32_16x16x32_bf16 v[20:23], a[180:183], v[184:187], v[20:23]\n"
    "ds_read_b64_tr_b16 a[166:167], v244 offset:63488\n"
    "v_mfma_f32_16x16x32_bf16 v[24:27], a[180:183], v[192:195], v[24:27]\n"
    "ds_read_b64_tr_b16 a[168:169], v245 offset:28672\n"
    "v_mfma_f32_16x16x32_bf16 v[24:27], a[176:179], v[188:191], v[24:27]\n"
    "v_exp_f32_e32 v173, v173\n"
    "v_mfma_f32_16x16x32_bf16 v[28:31], a[176:179], v[196:199], v[28:31]\n"
    "ds_read_b64_tr_b16 a[170:171], v245 offset:30720\n"
    "v_mfma_f32_16x16x32_bf16 v[28:31], a[180:183], v[200:203], v[28:31]\n"
    "v_cvt_pk_bf16_f32 v165, v166, v167\n"
    "v_mfma_f32_16x16x32_bf16 v[32:35], a[180:183], v[208:211], v[32:35]\n"
    "v_exp_f32_e32 v174, v174\n"
    "v_mfma_f32_16x16x32_bf16 v[32:35], a[176:179], v[204:207], v[32:35]\n"
    "v_exp_f32_e32 v175, v175\n"
    "v_mfma_f32_16x16x32_bf16 v[36:39], a[176:179], v[212:215], v[36:39]\n"
    "ds_read_b64_tr_b16 a[172:173], v245 offset:61440\n"
    "v_mfma_f32_16x16x32_bf16 v[36:39], a[180:183], v[216:219], v[36:39]\n"
    "s_waitcnt lgkmcnt(6)\n"
    "v_cvt_pk_bf16_f32 v172, v172, v173\n"
    "v_mfma_f32_16x16x32_bf16 v[56:59], a[188:191], v[216:219], v[56:59]\n"
    "ds_read_b64_tr_b16 a[176:177], v246 offset:28672\n"
    "v_mfma_f32_16x16x32_bf16 v[56:59], a[184:187], v[212:215], v[56:59]\n"
    "ds_read_b64_tr_b16 a[174:175], v245 offset:63488\n"
    "v_mfma_f32_16x16x32_bf16 v[52:55], a[184:187], v[204:207], v[52:55]\n"
    "v_cvt_pk_bf16_f32 v173, v174, v175\n"
    "v_mfma_f32_16x16x32_bf16 v[52:55], a[188:191], v[208:211], v[52:55]\n"
    "v_exp_f32_e32 v144, v144\n"
    "v_mfma_f32_16x16x32_bf16 v[48:51], a[188:191], v[200:203], v[48:51]\n"
    "ds_read_b64_tr_b16 a[178:179], v246 offset:30720\n"
    "v_mfma_f32_16x16x32_bf16 v[48:51], a[184:187], v[196:199], v[48:51]\n"
    "v_mfma_f32_16x16x32_bf16 v[44:47], a[184:187], v[188:191], v[44:47]\n"
    "v_exp_f32_e32 v145, v145\n"
    "v_mfma_f32_16x16x32_bf16 v[44:47], a[188:191], v[192:195], v[44:47]\n"
    "v_exp_f32_e32 v146, v146\n"
    "v_mfma_f32_16x16x32_bf16 v[40:43], a[188:191], v[184:187], v[40:43]\n"
    "ds_read_b64_tr_b16 a[180:181], v246 offset:61440\n"
    "v_mfma_f32_16x16x32_bf16 v[40:43], a[184:187], v[180:183], v[40:43]\n"
    "s_waitcnt lgkmcnt(7)\n"
    "v_exp_f32_e32 v147, v147\n"
    "v_mfma_f32_16x16x32_bf16 v[60:63], a[160:163], v[180:183], v[60:63]\n"
    "ds_read_b64_tr_b16 a[184:185], v247 offset:28672\n"
    "v_mfma_f32_16x16x32_bf16 v[60:63], a[164:167], v[184:187], v[60:63]\n"
    "ds_read_b64_tr_b16 a[182:183], v246 offset:63488\n"
    "v_mfma_f32_16x16x32_bf16 v[64:67], a[164:167], v[192:195], v[64:67]\n"
    "ds_read_b64_tr_b16 a[186:187], v247 offset:30720\n"
    "v_mfma_f32_16x16x32_bf16 v[64:67], a[160:163], v[188:191], v[64:67]\n"
    "v_cvt_pk_bf16_f32 v142, v144, v145\n"
    "v_mfma_f32_16x16x32_bf16 v[68:71], a[160:163], v[196:199], v[68:71]\n"
    "v_exp_f32_e32 v152, v152\n"
    "v_mfma_f32_16x16x32_bf16 v[68:71], a[164:167], v[200:203], v[68:71]\n"
    "ds_read_b64_tr_b16 a[188:189], v247 offset:61440\n"
    "v_mfma_f32_16x16x32_bf16 v[72:75], a[164:167], v[208:211], v[72:75]\n"
    "ds_read_b64_tr_b16 a[190:191], v247 offset:63488\n"
    "v_mfma_f32_16x16x32_bf16 v[72:75], a[160:163], v[204:207], v[72:75]\n"
    "s_waitcnt vmcnt(8)\n"
    "v_mfma_f32_16x16x32_bf16 v[76:79], a[160:163], v[212:215], v[76:79]\n"
    "s_barrier\n"
    "v_mfma_f32_16x16x32_bf16 v[76:79], a[164:167], v[216:219], v[76:79]\n"
    "s_waitcnt lgkmcnt(7)\n"
    "s_mov_b32 m0, s42\n"
    "v_mfma_f32_16x16x32_bf16 v[96:99], a[172:175], v[216:219], v[96:99]\n"
    "v_exp_f32_e32 v153, v153\n"
    "v_mfma_f32_16x16x32_bf16 v[96:99], a[168:171], v[212:215], v[96:99]\n"
    "v_cvt_pk_bf16_f32 v143, v146, v147\n"
    "v_mfma_f32_16x16x32_bf16 v[92:95], a[168:171], v[204:207], v[92:95]\n"
    "v_exp_f32_e32 v154, v154\n"
    "v_mfma_f32_16x16x32_bf16 v[92:95], a[172:175], v[208:211], v[92:95]\n"
    "ds_read_b128 a[160:163], v240 offset:8192\n"
    "v_mfma_f32_16x16x32_bf16 v[88:91], a[172:175], v[200:203], v[88:91]\n"
    "ds_write_addtid_b32 v140 offset:0\n"
    "v_mfma_f32_16x16x32_bf16 v[88:91], a[168:171], v[196:199], v[88:91]\n"
    "v_exp_f32_e32 v155, v155\n"
    "v_mfma_f32_16x16x32_bf16 v[84:87], a[168:171], v[188:191], v[84:87]\n"
    "ds_write_addtid_b32 v141 offset:256\n"
    "v_mfma_f32_16x16x32_bf16 v[84:87], a[172:175], v[192:195], v[84:87]\n"
    "ds_write_addtid_b32 v148 offset:1024\n"
    "v_mfma_f32_16x16x32_bf16 v[80:83], a[172:175], v[184:187], v[80:83]\n"
    "v_cvt_pk_bf16_f32 v150, v152, v153\n"
    "v_mfma_f32_16x16x32_bf16 v[80:83], a[168:171], v[180:183], v[80:83]\n"
    "s_waitcnt lgkmcnt(7)\n"
    "v_exp_f32_e32 v160, v160\n"
    "v_mfma_f32_16x16x32_bf16 v[100:103], a[176:179], v[180:183], v[100:103]\n"
    "ds_read_b128 a[164:167], v241 offset:8192\n"
    "v_mfma_f32_16x16x32_bf16 v[100:103], a[180:183], v[184:187], v[100:103]\n"
    "ds_write_addtid_b32 v149 offset:1280\n"
    "v_mfma_f32_16x16x32_bf16 v[104:107], a[180:183], v[192:195], v[104:107]\n"
    "v_exp_f32_e32 v161, v161\n"
    "v_mfma_f32_16x16x32_bf16 v[104:107], a[176:179], v[188:191], v[104:107]\n"
    "ds_read_b128 a[168:171], v240 offset:12288\n"
    "v_mfma_f32_16x16x32_bf16 v[108:111], a[176:179], v[196:199], v[108:111]\n"
    "ds_write_addtid_b32 v156 offset:2048\n"
    "v_mfma_f32_16x16x32_bf16 v[108:111], a[180:183], v[200:203], v[108:111]\n"
    "v_cvt_pk_bf16_f32 v151, v154, v155\n"
    "v_mfma_f32_16x16x32_bf16 v[112:115], a[180:183], v[208:211], v[112:115]\n"
    "ds_write_addtid_b32 v157 offset:2304\n"
    "v_mfma_f32_16x16x32_bf16 v[112:115], a[176:179], v[204:207], v[112:115]\n"
    "v_exp_f32_e32 v162, v162\n"
    "v_mfma_f32_16x16x32_bf16 v[116:119], a[176:179], v[212:215], v[116:119]\n"
    "ds_write_addtid_b32 v164 offset:3072\n"
    "v_mfma_f32_16x16x32_bf16 v[116:119], a[180:183], v[216:219], v[116:119]\n"
    "s_waitcnt lgkmcnt(10)\n"
    "v_exp_f32_e32 v163, v163\n"
    "v_mfma_f32_16x16x32_bf16 v[136:139], a[188:191], v[216:219], v[136:139]\n"
    "ds_read_b128 a[172:175], v241 offset:12288\n"
    "v_mfma_f32_16x16x32_bf16 v[136:139], a[184:187], v[212:215], v[136:139]\n"
    "ds_write_addtid_b32 v165 offset:3328\n"
    "v_cvt_pk_bf16_f32 v158, v160, v161\n"
    "v_mfma_f32_16x16x32_bf16 v[132:135], a[184:187], v[204:207], v[132:135]\n"
    "ds_read_b128 a[176:179], v242 offset:8192\n"
    "v_mfma_f32_16x16x32_bf16 v[132:135], a[188:191], v[208:211], v[132:135]\n"
    "ds_write_addtid_b32 v172 offset:4096\n"
    "v_mfma_f32_16x16x32_bf16 v[128:131], a[188:191], v[200:203], v[128:131]\n"
    "v_exp_f32_e32 v168, v168\n"
    "v_mfma_f32_16x16x32_bf16 v[128:131], a[184:187], v[196:199], v[128:131]\n"
    "ds_write_addtid_b32 v173 offset:4352\n"
    "v_mfma_f32_16x16x32_bf16 v[124:127], a[184:187], v[188:191], v[124:127]\n"
    "v_exp_f32_e32 v169, v169\n"
    "v_mfma_f32_16x16x32_bf16 v[124:127], a[188:191], v[192:195], v[124:127]\n"
    "ds_read_b128 a[180:183], v243 offset:8192\n"
    "v_mfma_f32_16x16x32_bf16 v[120:123], a[188:191], v[184:187], v[120:123]\n"
    "ds_write_addtid_b32 v142 offset:512\n"
    "v_mfma_f32_16x16x32_bf16 v[120:123], a[184:187], v[180:183], v[120:123]\n"
    "s_waitcnt lgkmcnt(12)\n"
    "v_cvt_pk_bf16_f32 v159, v162, v163\n"
    ".Lat_in:\n"
    "v_mfma_f32_16x16x32_bf16 v[212:215], a[160:163], a[128:131], v[236:239]\n"
    ".long 0xbedf00ff\n"
    ".Lat_w4:\n"
    "s_setpc_b64 s[2:3]\n"
    "v_mfma_f32_16x16x32_bf16 v[212:215], a[164:167], a[132:135], v[212:215]\n"
    "v_exp_f32_e32 v170, v170\n"
    "v_mfma_f32_16x16x32_bf16 v[204:207], a[164:167], a[100:103], v[232:235]\n"
    "ds_write_addtid_b32 v143 offset:768\n"
    "v_mfma_f32_16x16x32_bf16 v[204:207], a[160:163], a[96:99], v[204:207]\n"
    "ds_read_b128 a[184:187], v242 offset:12288\n"
    "v_mfma_f32_16x16x32_bf16 v[196:199], a[160:163], a[64:67], v[228:231]\n"
    "v_exp_f32_e32 v171, v171\n"
    "v_mfma_f32_16x16x32_bf16 v[196:199], a[164:167], a[68:71], v[196:199]\n"
    "ds_write_addtid_b32 v150 offset:1536\n"
    "v_mfma_f32_16x16x32_bf16 v[188:191], a[164:167], a[36:39], v[224:227]\n"
    "v_cvt_pk_bf16_f32 v166, v168, v169\n"
    "v_mfma_f32_16x16x32_bf16 v[188:191], a[160:163], a[32:35], v[188:191]\n"
    "v_mfma_f32_16x16x32_bf16 v[180:183], a[160:163], a[0:3], v[220:223]\n"
    "ds_write_addtid_b32 v151 offset:1792\n"
    "v_mfma_f32_16x16x32_bf16 v[180:183], a[164:167], a[4:7], v[180:183]\n"
    "s_waitcnt lgkmcnt(10)\n"
    "v_exp_f32_e32 v176, v176\n"
    "v_mfma_f32_16x16x32_bf16 v[180:183], a[168:171], a[8:11], v[180:183]\n"
    "ds_read_b128 a[188:191], v243 offset:12288\n"
    "v_mfma_f32_16x16x32_bf16 v[180:183], a[172:175], a[12:15], v[180:183]\n"
    "v_exp_f32_e32 v177, v177\n"
    "v_mfma_f32_16x16x32_bf16 v[188:191], a[172:175], a[44:47], v[188:191]\n"
    "ds_write_addtid_b32 v158 offset:2560\n"
    "v_mfma_f32_16x16x32_bf16 v[188:191], a[168:171], a[40:43], v[188:191]\n"
    "ds_read_b128 a[160:163], v240 offset:10240\n"
    "v_mfma_f32_16x16x32_bf16 v[196:199], a[168:171], a[72:75], v[196:199]\n"
    "v_cvt_pk_bf16_f32 v167, v170, v171\n"
    "v_mfma_f32_16x16x32_bf16 v[196:199], a[172:175], a[76:79], v[196:199]\n"
    "ds_write_addtid_b32 v159 offset:2816\n"
    "v_mfma_f32_16x16x32_bf16 v[204:207], a[172:175], a[108:111], v[204:207]\n"
    "v_exp_f32_e32 v178, v178\n"
    "v_mfma_f32_16x16x32_bf16 v[204:207], a[168:171], a[104:107], v[204:207]\n"
    "ds_write_addtid_b32 v166 offset:3584\n"
    "v_mfma_f32_16x16x32_bf16 v[212:215], a[168:171], a[136:139], v[212:215]\n"
    "v_mfma_f32_16x16x32_bf16 v[212:215], a[172:175], a[140:143], v[212:215]\n"
    "s_waitcnt lgkmcnt(10)\n"
    "v_exp_f32_e32 v179, v179\n"
    "v_mfma_f32_16x16x32_bf16 v[212:215], a[176:179], a[144:147], v[212:215]\n"
    "ds_read_b128 a[164:167], v241 offset:10240\n"
    "v_mfma_f32_16x16x32_bf16 v[212:215], a[180:183], a[148:151], v[212:215]\n"
    "v_cvt_pk_bf16_f32 v174, v176, v177\n"
    "v_mfma_f32_16x16x32_bf16 v[204:207], a[180:183], a[116:119], v[204:207]\n"
    "ds_write_addtid_b32 v167 offset:3840\n"
    "v_mfma_f32_16x16x32_bf16 v[204:207], a[176:179], a[112:115], v[204:207]\n"
    "ds_read_b128 a[168:171], v240 offset:14336\n"
    "v_mfma_f32_16x16x32_bf16 v[196:199], a[176:179], a[80:83], v[196:199]\n"
    "v_cvt_pk_bf16_f32 v175, v178, v179\n"
    "v_mfma_f32_16x16x32_bf16 v[196:199], a[180:183], a[84:87], v[196:199]\n"
    "ds_write_addtid_b32 v174 offset:4608\n"
    "v_mfma_f32_16x16x32_bf16 v[188:191], a[180:183], a[52:55], v[188:191]\n"
    "ds_write_addtid_b32 v175 offset:4864\n"
    "v_mfma_f32_16x16x32_bf16 v[188:191], a[176:179], a[48:51], v[188:191]\n"
    "v_mfma_f32_16x16x32_bf16 v[180:183], a[176:179], a[16:19], v[180:183]\n"
    "ds_read_b128 a[172:175], v241 offset:14336\n"
    "v_mfma_f32_16x16x32_bf16 v[180:183], a[180:183], a[20:23], v[180:183]\n"
    "s_waitcnt lgkmcnt(10)\n"
    "s_waitcnt lgkmcnt(1)\n"
    "v_mfma_f32_16x16x32_bf16 v[180:183], a[184:187], a[24:27], v[180:183]\n"
    "s_barrier\n"
    "v_mfma_f32_16x16x32_bf16 v[180:183], a[188:191], a[28:31], v[180:183]\n"
    "s_add_u32 m0, s43, 24576\n"
    "v_mfma_f32_16x16x32_bf16 v[188:191], a[188:191], a[60:63], v[188:191]\n"
    "ds_read_b128 a[176:179], v242 offset:10240\n"
    "v_mfma_f32_16x16x32_bf16 v[188:191], a[184:187], a[56:59], v[188:191]\n"
    "buffer_load_dwordx4 v248, s[8:11], s40 offen offset:0 sc1 lds\n"
    "v_mfma_f32_16x16x32_bf16 v[196:199], a[184:187], a[88:91], v[196:199]\n"
    "ds_read2st64_b32 v[144:145], v249 offset0:0 offset1:1\n"
    "v_mfma_f32_16x16x32_bf16 v[196:199], a[188:191], a[92:95], v[196:199]\n"
    "v_mfma_f32_16x16x32_bf16 v[204:207], a[188:191], a[124:127], v[204:207]\n"
    "ds_read2st64_b32 v[146:147], v249 offset0:2 offset1:3\n"
    "v_mfma_f32_16x16x32_bf16 v[204:207], a[184:187], a[120:123], v[204:207]\n"
    "buffer_load_dwordx4 v248, s[8:11], s40 offen offset:1024 sc1 lds\n"
    "v_mfma_f32_16x16x32_bf16 v[212:215], a[184:187], a[152:155], v[212:215]\n"
    "v_mfma_f32_16x16x32_bf16 v[212:215], a[188:191], a[156:159], v[212:215]\n"
    "v_mfma_f32_16x16x32_bf16 v[216:219], a[160:163], a[128:131], v[236:239]\n"
    "ds_read_b128 a[180:183], v243 offset:10240\n"
    "v_mfma_f32_16x16x32_bf16 v[216:219], a[164:167], a[132:135], v[216:219]\n"
    "buffer_load_dwordx4 v248, s[8:11], s40 offen offset:2048 sc1 lds\n"
    "v_mfma_f32_16x16x32_bf16 v[208:211], a[164:167], a[100:103], v[232:235]\n"
    "ds_read2st64_b32 v[152:153], v249 offset0:4 offset1:5\n"
    "v_mfma_f32_16x16x32_bf16 v[208:211], a[160:163], a[96:99], v[208:211]\n"
    "ds_read_b128 a[184:187], v242 offset:14336\n"
    "v_mfma_f32_16x16x32_bf16 v[200:203], a[160:163], a[64:67], v[228:231]\n"
    "ds_read2st64_b32 v[154:155], v249 offset0:6 offset1:7\n"
    "v_mfma_f32_16x16x32_bf16 v[200:203], a[164:167], a[68:71], v[200:203]\n"
    "buffer_load_dwordx4 v248, s[8:11], s40 offen offset:3072 sc1 lds\n"
    "v_mfma_f32_16x16x32_bf16 v[192:195], a[164:167], a[36:39], v[224:227]\n"
    "s_add_u32 m0, s43, 28672\n"
    "v_exp_f32_e32 v180, v180\n"
    "v_mfma_f32_16x16x32_bf16 v[192:195], a[160:163], a[32:35], v[192:195]\n"
    "s_add_u32 s40, s40, 0x8000\n"
    "v_exp_f32_e32 v181, v181\n"
    "v_mfma_f32_16x16x32_bf16 v[184:187], a[160:163], a[0:3], v[220:223]\n"
    "ds_read2st64_b32 v[160:161], v249 offset0:8 offset1:9\n"
    "v_mfma_f32_16x16x32_bf16 v[184:187], a[164:167], a[4:7], v[184:187]\n"
    "s_waitcnt lgkmcnt(8)\n"
    "buffer_load_dwordx4 v248, s[8:11], s41 offen offset:0 sc1 lds\n"
    "v_mfma_f32_16x16x32_bf16 v[184:187], a[168:171], a[8:11], v[184:187]\n"
    "ds_read_b128 a[188:191], v243 offset:14336\n"
    "v_mfma_f32_16x16x32_bf16 v[184:187], a[172:175], a[12:15], v[184:187]\n"
    "ds_read_b64_tr_b16 a[160:161], v244 offset:0\n"
    "v_mfma_f32_16x16x32_bf16 v[192:195], a[172:175], a[44:47], v[192:195]\n"
    "ds_read2st64_b32 v[162:163], v249 offset0:10 offset1:11\n"
    "v_mfma_f32_16x16x32_bf16 v[192:195], a[168:171], a[40:43], v[192:195]\n"
    "buffer_load_dwordx4 v248, s[8:11], s41 offen offset:1024 sc1 lds\n"
    "v_mfma_f32_16x16x32_bf16 v[200:203], a[168:171], a[72:75], v[200:203]\n"
    "ds_read_b64_tr_b16 a[162:163], v244 offset:2048\n"
    "v_mfma_f32_16x16x32_bf16 v[200:203], a[172:175], a[76:79], v[200:203]\n"
    "v_exp_f32_e32 v182, v182\n"
    "v_mfma_f32_16x16x32_bf16 v[208:211], a[172:175], a[108:111], v[208:211]\n"
    "ds_read2st64_b32 v[168:169], v249 offset0:12 offset1:13\n"
    "v_mfma_f32_16x16x32_bf16 v[208:211], a[168:171], a[104:107], v[208:211]\n"
    "buffer_load_dwordx4 v248, s[8:11], s41 offen offset:2048 sc1 lds\n"
    "v_mfma_f32_16x16x32_bf16 v[216:219], a[168:171], a[136:139], v[216:219]\n"
    "ds_read_b64_tr_b16 a[164:165], v244 offset:32768\n"
    "v_mfma_f32_16x16x32_bf16 v[216:219], a[172:175], a[140:143], v[216:219]\n"
    "s_waitcnt lgkmcnt(10)\n"
    "ds_read2st64_b32 v[170:171], v249 offset0:14 offset1:15\n"
    "v_mfma_f32_16x16x32_bf16 v[216:219], a[176:179], a[144:147], v[216:219]\n"
    "ds_read_b64_tr_b16 a[168:169], v245 offset:0\n"
    "v_mfma_f32_16x16x32_bf16 v[216:219], a[180:183], a[148:151], v[216:219]\n"
    "buffer_load_dwordx4 v248, s[8:11], s41 offen offset:3072 sc1 lds\n"
    "v_mfma_f32_16x16x32_bf16 v[208:211], a[180:183], a[116:119], v[208:211]\n"
    "ds_read_b64_tr_b16 a[166:167], v244 offset:34816\n"
    "v_mfma_f32_16x16x32_bf16 v[208:211], a[176:179], a[112:115], v[208:211]\n"
    "s_add_u32 s41, s41, 0x8000\n"
    "v_exp_f32_e32 v183, v183\n"
    "v_mfma_f32_16x16x32_bf16 v[200:203], a[176:179], a[80:83], v[200:203]\n"
    "ds_read_b64_tr_b16 a[170:171], v245 offset:2048\n"
    "v_cvt_pk_bf16_f32 v180, v180, v181\n"
    "v_mfma_f32_16x16x32_bf16 v[200:203], a[180:183], a[84:87], v[200:203]\n"
    "ds_read2st64_b32 v[176:177], v249 offset0:16 offset1:17\n"
    "v_mfma_f32_16x16x32_bf16 v[192:195], a[180:183], a[52:55], v[192:195]\n"
    "v_exp_f32_e32 v188, v188\n"
    "v_mfma_f32_16x16x32_bf16 v[192:195], a[176:179], a[48:51], v[192:195]\n"
    "v_exp_f32_e32 v189, v189\n"
    "v_mfma_f32_16x16x32_bf16 v[184:187], a[176:179], a[16:19], v[184:187]\n"
    "ds_read_b64_tr_b16 a[172:173], v245 offset:32768\n"
    "v_cvt_pk_bf16_f32 v181, v182, v183\n"
    "v_mfma_f32_16x16x32_bf16 v[184:187], a[180:183], a[20:23], v[184:187]\n"
    "s_waitcnt lgkmcnt(11)\n"
    "ds_read2st64_b32 v[178:179], v249 offset0:18 offset1:19\n"
    "v_mfma_f32_16x16x32_bf16 v[184:187], a[184:187], a[24:27], v[184:187]\n"
    "ds_read_b64_tr_b16 a[176:177], v246 offset:0\n"
    "v_mfma_f32_16x16x32_bf16 v[184:187], a[188:191], a[28:31], v[184:187]\n"
    "ds_read_b64_tr_b16 a[174:175], v245 offset:34816\n"
    "v_mfma_f32_16x16x32_bf16 v[192:195], a[188:191], a[60:63], v[192:195]\n"
    "v_exp_f32_e32 v190, v190\n"
    "v_mfma_f32_16x16x32_bf16 v[192:195], a[184:187], a[56:59], v[192:195]\n"
    "v_exp_f32_e32 v191, v191\n"
    "v_mfma_f32_16x16x32_bf16 v[200:203], a[184:187], a[88:91], v[200:203]\n"
    "v_mfma_f32_16x16x32_bf16 v[200:203], a[188:191], a[92:95], v[200:203]\n"
    "ds_read_b64_tr_b16 a[178:179], v246 offset:2048\n"
    "v_mfma_f32_16x16x32_bf16 v[208:211], a[188:191], a[124:127], v[208:211]\n"
    "v_cvt_pk_bf16_f32 v188, v188, v189\n"
    "v_mfma_f32_16x16x32_bf16 v[208:211], a[184:187], a[120:123], v[208:211]\n"
    "v_exp_f32_e32 v196, v196\n"
    "v_mfma_f32_16x16x32_bf16 v[216:219], a[184:187], a[152:155], v[216:219]\n"
    "ds_read_b64_tr_b16 a[180:181], v246 offset:32768\n"
    "v_mfma_f32_16x16x32_bf16 v[216:219], a[188:191], a[156:159], v[216:219]\n"
    "s_waitcnt lgkmcnt(4)\n"
    "v_exp_f32_e32 v197, v197\n"
    "v_mfma_f32_16x16x32_bf16 a[236:239], a[160:163], v[140:143], a[236:239]\n"
    "v_mfma_f32_16x16x32_bf16 a[236:239], a[164:167], v[144:147], a[236:239]\n"
    "ds_read_b64_tr_b16 a[182:183], v246 offset:34816\n"
    "v_mfma_f32_16x16x32_bf16 a[240:243], a[164:167], v[152:155], a[240:243]\n"
    "ds_read_b64_tr_b16 a[184:185], v247 offset:0\n"
    "v_mfma_f32_16x16x32_bf16 a[240:243], a[160:163], v[148:151], a[240:243]\n"
    "v_cvt_pk_bf16_f32 v189, v190, v191\n"
    "v_mfma_f32_16x16x32_bf16 a[244:247], a[160:163], v[156:159], a[244:247]\n"
    "ds_read_b64_tr_b16 a[186:187], v247 offset:2048\n"
    "v_mfma_f32_16x16x32_bf16 a[244:247], a[164:167], v[160:163], a[244:247]\n"
    "v_exp_f32_e32 v198, v198\n"
    "v_mfma_f32_16x16x32_bf16 a[248:251], a[164:167], v[168:171], a[248:251]\n"
    "v_exp_f32_e32 v199, v199\n"
    "v_mfma_f32_16x16x32_bf16 a[248:251], a[160:163], v[164:167], a[248:251]\n"
    "v_cvt_pk_bf16_f32 v196, v196, v197\n"
    "v_mfma_f32_16x16x32_bf16 a[252:255], a[160:163], v[172:175], a[252:255]\n"
    "ds_read_b64_tr_b16 a[188:189], v247 offset:32768\n"
    "v_mfma_f32_16x16x32_bf16 a[252:255], a[164:167], v[176:179], a[252:255]\n"
    "s_waitcnt lgkmcnt(6)\n"
    "v_exp_f32_e32 v204, v204\n"
    "v_mfma_f32_16x16x32_bf16 v[16:19], a[172:175], v[176:179], v[16:19]\n"
    "ds_read_b64_tr_b16 a[160:161], v244 offset:4096\n"
    "v_mfma_f32_16x16x32_bf16 v[16:19], a[168:171], v[172:175], v[16:19]\n"
    "ds_read_b64_tr_b16 a[190:191], v247 offset:34816\n"
    "v_mfma_f32_16x16x32_bf16 v[12:15], a[168:171], v[164:167], v[12:15]\n"
    "v_exp_f32_e32 v205, v205\n"
    "v_mfma_f32_16x16x32_bf16 v[12:15], a[172:175], v[168:171], v[12:15]\n"
    "v_cvt_pk_bf16_f32 v197, v198, v199\n"
    "v_mfma_f32_16x16x32_bf16 v[8:11], a[172:175], v[160:163], v[8:11]\n"
    "ds_read_b64_tr_b16 a[162:163], v244 offset:6144\n"
    "v_mfma_f32_16x16x32_bf16 v[8:11], a[168:171], v[156:159], v[8:11]\n"
    "v_exp_f32_e32 v206, v206\n"
    "v_mfma_f32_16x16x32_bf16 v[4:7], a[168:171], v[148:151], v[4:7]\n"
    "v_exp_f32_e32 v207, v207\n"
    "v_mfma_f32_16x16x32_bf16 v[4:7], a[172:175], v[152:155], v[4:7]\n"
    "v_cvt_pk_bf16_f32 v204, v204, v205\n"
    "v_mfma_f32_16x16x32_bf16 v[0:3], a[172:175], v[144:147], v[0:3]\n"
    "ds_read_b64_tr_b16 a[164:165], v244 offset:36864\n"
    "v_mfma_f32_16x16x32_bf16 v[0:3], a[168:171], v[140:143], v[0:3]\n"
    "s_waitcnt lgkmcnt(7)\n"
    "v_exp_f32_e32 v212, v212\n"
    "v_mfma_f32_16x16x32_bf16 v[20:23], a[176:179], v[140:143], v[20:23]\n"
    "buffer_load_dword v253, off, s[8:11], s82 sc1\n"
    "v_mfma_f32_16x16x32_bf16 v[20:23], a[180:183], v[144:147], v[20:23]\n"
    "ds_read_b64_tr_b16 a[166:167], v244 offset:38912\n"
    "v_mfma_f32_16x16x32_bf16 v[24:27], a[180:183], v[152:155], v[24:27]\n"
    "ds_read_b64_tr_b16 a[168:169], v245 offset:4096\n"
    "v_mfma_f32_16x16x32_bf16 v[24:27], a[176:179], v[148:151], v[24:27]\n"
    "v_exp_f32_e32 v213, v213\n"
    "v_mfma_f32_16x16x32_bf16 v[28:31], a[176:179], v[156:159], v[28:31]\n"
    "ds_read_b64_tr_b16 a[170:171], v245 offset:6144\n"
    "v_mfma_f32_16x16x32_bf16 v[28:31], a[180:183], v[160:163], v[28:31]\n"
    "v_cvt_pk_bf16_f32 v205, v206, v207\n"
    "v_mfma_f32_16x16x32_bf16 v[32:35], a[180:183], v[168:171], v[32:35]\n"
    "v_exp_f32_e32 v214, v214\n"
    "v_mfma_f32_16x16x32_bf16 v[32:35], a[176:179], v[164:167], v[32:35]\n"
    "v_exp_f32_e32 v215, v215\n"
    "v_mfma_f32_16x16x32_bf16 v[36:39], a[176:179], v[172:175], v[36:39]\n"
    "ds_read_b64_tr_b16 a[172:173], v245 offset:36864\n"
    "v_mfma_f32_16x16x32_bf16 v[36:39], a[180:183], v[176:179], v[36:39]\n"
    "s_waitcnt lgkmcnt(6)\n"
    "v_cvt_pk_bf16_f32 v212, v212, v213\n"
    "v_mfma_f32_16x16x32_bf16 v[56:59], a[188:191], v[176:179], v[56:59]\n"
    "ds_read_b64_tr_b16 a[176:177], v246 offset:4096\n"
    "v_mfma_f32_16x16x32_bf16 v[56:59], a[184:187], v[172:175], v[56:59]\n"
    "ds_read_b64_tr_b16 a[174:175], v245 offset:38912\n"
    "v_mfma_f32_16x16x32_bf16 v[52:55], a[184:187], v[164:167], v[52:55]\n"
    "v_cvt_pk_bf16_f32 v213, v214, v215\n"
    "v_mfma_f32_16x16x32_bf16 v[52:55], a[188:191], v[168:171], v[52:55]\n"
    "v_exp_f32_e32 v184, v184\n"
    "v_mfma_f32_16x16x32_bf16 v[48:51], a[188:191], v[160:163], v[48:51]\n"
    "ds_read_b64_tr_b16 a[178:179], v246 offset:6144\n"
    "v_mfma_f32_16x16x32_bf16 v[48:51], a[184:187], v[156:159], v[48:51]\n"
    "v_mfma_f32_16x16x32_bf16 v[44:47], a[184:187], v[148:151], v[44:47]\n"
    "v_exp_f32_e32 v185, v185\n"
    "v_mfma_f32_16x16x32_bf16 v[44:47], a[188:191], v[152:155], v[44:47]\n"
    "v_exp_f32_e32 v186, v186\n"
    "v_mfma_f32_16x16x32_bf16 v[40:43], a[188:191], v[144:147], v[40:43]\n"
    "ds_read_b64_tr_b16 a[180:181], v246 offset:36864\n"
    "v_mfma_f32_16x16x32_bf16 v[40:43], a[184:187], v[140:143], v[40:43]\n"
    "s_waitcnt lgkmcnt(7)\n"
    "v_exp_f32_e32 v187, v187\n"
    "v_mfma_f32_16x16x32_bf16 v[60:63], a[160:163], v[140:143], v[60:63]\n"
    "ds_read_b64_tr_b16 a[184:185], v247 offset:4096\n"
    "v_mfma_f32_16x16x32_bf16 v[60:63], a[164:167], v[144:147], v[60:63]\n"
    "ds_read_b64_tr_b16 a[182:183], v246 offset:38912\n"
    "v_mfma_f32_16x16x32_bf16 v[64:67], a[164:167], v[152:155], v[64:67]\n"
    "ds_read_b64_tr_b16 a[186:187], v247 offset:6144\n"
    "v_mfma_f32_16x16x32_bf16 v[64:67], a[160:163], v[148:151], v[64:67]\n"
    "v_cvt_pk_bf16_f32 v182, v184, v185\n"
    "v_mfma_f32_16x16x32_bf16 v[68:71], a[160:163], v[156:159], v[68:71]\n"
    "v_exp_f32_e32 v192, v192\n"
    "v_mfma_f32_16x16x32_bf16 v[68:71], a[164:167], v[160:163], v[68:71]\n"
    "ds_read_b64_tr_b16 a[188:189], v247 offset:36864\n"
    "v_mfma_f32_16x16x32_bf16 v[72:75], a[164:167], v[168:171], v[72:75]\n"
    "ds_read_b64_tr_b16 a[190:191], v247 offset:38912\n"
    "v_mfma_f32_16x16x32_bf16 v[72:75], a[160:163], v[164:167], v[72:75]\n"
    "s_waitcnt vmcnt(9)\n"
    "v_mfma_f32_16x16x32_bf16 v[76:79], a[160:163], v[172:175], v[76:79]\n"
    "s_barrier\n"
    "v_mfma_f32_16x16x32_bf16 v[76:79], a[164:167], v[176:179], v[76:79]\n"
    "s_waitcnt lgkmcnt(7)\n"
    "s_mov_b32 m0, s42\n"
    "v_mfma_f32_16x16x32_bf16 v[96:99], a[172:175], v[176:179], v[96:99]\n"
    "v_exp_f32_e32 v193, v193\n"
    "v_mfma_f32_16x16x32_bf16 v[96:99], a[168:171], v[172:175], v[96:99]\n"
    "v_cvt_pk_bf16_f32 v183, v186, v187\n"
    "v_mfma_f32_16x16x32_bf16 v[92:95], a[168:171], v[164:167], v[92:95]\n"
    "v_exp_f32_e32 v194, v194\n"
    "v_mfma_f32_16x16x32_bf16 v[92:95], a[172:175], v[168:171], v[92:95]\n"
    "ds_read_b128 a[160:163], v240 offset:16384\n"
    "v_mfma_f32_16x16x32_bf16 v[88:91], a[172:175], v[160:163], v[88:91]\n"
    "ds_write_addtid_b32 v180 offset:0\n"
    "v_mfma_f32_16x16x32_bf16 v[88:91], a[168:171], v[156:159], v[88:91]\n"
    "v_exp_f32_e32 v195, v195\n"
    "v_mfma_f32_16x16x32_bf16 v[84:87], a[168:171], v[148:151], v[84:87]\n"
    "ds_write_addtid_b32 v181 offset:256\n"
    "v_mfma_f32_16x16x32_bf16 v[84:87], a[172:175], v[152:155], v[84:87]\n"
    "ds_write_addtid_b32 v188 offset:1024\n"
    "v_mfma_f32_16x16x32_bf16 v[80:83], a[172:175], v[144:147], v[80:83]\n"
    "v_cvt_pk_bf16_f32 v190, v192, v193\n"
    "v_mfma_f32_16x16x32_bf16 v[80:83], a[168:171], v[140:143], v[80:83]\n"
    "s_waitcnt lgkmcnt(7)\n"
    "v_exp_f32_e32 v200, v200\n"
    "v_mfma_f32_16x16x32_bf16 v[100:103], a[176:179], v[140:143], v[100:103]\n"
    "ds_read_b128 a[164:167], v241 offset:16384\n"
    "v_mfma_f32_16x16x32_bf16 v[100:103], a[180:183], v[144:147], v[100:103]\n"
    "ds_write_addtid_b32 v189 offset:1280\n"
    "v_mfma_f32_16x16x32_bf16 v[104:107], a[180:183], v[152:155], v[104:107]\n"
    "v_exp_f32_e32 v201, v201\n"
    "v_mfma_f32_16x16x32_bf16 v[104:107], a[176:179], v[148:151], v[104:107]\n"
    "ds_read_b128 a[168:171], v240 offset:20480\n"
    "v_mfma_f32_16x16x32_bf16 v[108:111], a[176:179], v[156:159], v[108:111]\n"
    "ds_write_addtid_b32 v196 offset:2048\n"
    "v_mfma_f32_16x16x32_bf16 v[108:111], a[180:183], v[160:163], v[108:111]\n"
    "v_cvt_pk_bf16_f32 v191, v194, v195\n"
    "v_mfma_f32_16x16x32_bf16 v[112:115], a[180:183], v[168:171], v[112:115]\n"
    "ds_write_addtid_b32 v197 offset:2304\n"
    "v_mfma_f32_16x16x32_bf16 v[112:115], a[176:179], v[164:167], v[112:115]\n"
    "v_exp_f32_e32 v202, v202\n"
    "v_mfma_f32_16x16x32_bf16 v[116:119], a[176:179], v[172:175], v[116:119]\n"
    "ds_write_addtid_b32 v204 offset:3072\n"
    "v_mfma_f32_16x16x32_bf16 v[116:119], a[180:183], v[176:179], v[116:119]\n"
    "s_waitcnt lgkmcnt(10)\n"
    "v_exp_f32_e32 v203, v203\n"
    "v_mfma_f32_16x16x32_bf16 v[136:139], a[188:191], v[176:179], v[136:139]\n"
    "ds_read_b128 a[172:175], v241 offset:20480\n"
    "v_mfma_f32_16x16x32_bf16 v[136:139], a[184:187], v[172:175], v[136:139]\n"
    "ds_write_addtid_b32 v205 offset:3328\n"
    "v_cvt_pk_bf16_f32 v198, v200, v201\n"
    "v_mfma_f32_16x16x32_bf16 v[132:135], a[184:187], v[164:167], v[132:135]\n"
    "ds_read_b128 a[176:179], v242 offset:16384\n"
    "v_mfma_f32_16x16x32_bf16 v[132:135], a[188:191], v[168:171], v[132:135]\n"
    "ds_write_addtid_b32 v212 offset:4096\n"
    "v_mfma_f32_16x16x32_bf16 v[128:131], a[188:191], v[160:163], v[128:131]\n"
    "v_exp_f32_e32 v208, v208\n"
    "v_mfma_f32_16x16x32_bf16 v[128:131], a[184:187], v[156:159], v[128:131]\n"
    "ds_write_addtid_b32 v213 offset:4352\n"
    "v_mfma_f32_16x16x32_bf16 v[124:127], a[184:187], v[148:151], v[124:127]\n"
    "v_exp_f32_e32 v209, v209\n"
    "v_mfma_f32_16x16x32_bf16 v[124:127], a[188:191], v[152:155], v[124:127]\n"
    "ds_read_b128 a[180:183], v243 offset:16384\n"
    "v_mfma_f32_16x16x32_bf16 v[120:123], a[188:191], v[144:147], v[120:123]\n"
    "ds_write_addtid_b32 v182 offset:512\n"
    "v_mfma_f32_16x16x32_bf16 v[120:123], a[184:187], v[140:143], v[120:123]\n"
    "s_waitcnt lgkmcnt(12)\n"
    "v_cvt_pk_bf16_f32 v199, v202, v203\n"
    "v_mfma_f32_16x16x32_bf16 v[172:175], a[160:163], a[128:131], v[236:239]\n"
    "v_mfma_f32_16x16x32_bf16 v[172:175], a[164:167], a[132:135], v[172:175]\n"
    "v_exp_f32_e32 v210, v210\n"
    "v_mfma_f32_16x16x32_bf16 v[164:167], a[164:167], a[100:103], v[232:235]\n"
    "ds_write_addtid_b32 v183 offset:768\n"
    "v_mfma_f32_16x16x32_bf16 v[164:167], a[160:163], a[96:99], v[164:167]\n"
    "ds_read_b128 a[184:187], v242 offset:20480\n"
    "v_mfma_f32_16x16x32_bf16 v[156:159], a[160:163], a[64:67], v[228:231]\n"
    "v_exp_f32_e32 v211, v211\n"
    "v_mfma_f32_16x16x32_bf16 v[156:159], a[164:167], a[68:71], v[156:159]\n"
    "ds_write_addtid_b32 v190 offset:1536\n"
    "v_mfma_f32_16x16x32_bf16 v[148:151], a[164:167], a[36:39], v[224:227]\n"
    "v_cvt_pk_bf16_f32 v206, v208, v209\n"
    "v_mfma_f32_16x16x32_bf16 v[148:151], a[160:163], a[32:35], v[148:151]\n"
    "v_mfma_f32_16x16x32_bf16 v[140:143], a[160:163], a[0:3], v[220:223]\n"
    "ds_write_addtid_b32 v191 offset:1792\n"
    "v_mfma_f32_16x16x32_bf16 v[140:143], a[164:167], a[4:7], v[140:143]\n"
    "s_waitcnt lgkmcnt(10)\n"
    "v_exp_f32_e32 v216, v216\n"
    "v_mfma_f32_16x16x32_bf16 v[140:143], a[168:171], a[8:11], v[140:143]\n"
    "ds_read_b128 a[188:191], v243 offset:20480\n"
    "v_mfma_f32_16x16x32_bf16 v[140:143], a[172:175], a[12:15], v[140:143]\n"
    "v_exp_f32_e32 v217, v217\n"
    "v_mfma_f32_16x16x32_bf16 v[148:151], a[172:175], a[44:47], v[148:151]\n"
    "ds_write_addtid_b32 v198 offset:2560\n"
    "v_mfma_f32_16x16x32_bf16 v[148:151], a[168:171], a[40:43], v[148:151]\n"
    "ds_read_b128 a[160:163], v240 offset:18432\n"
    "v_mfma_f32_16x16x32_bf16 v[156:159], a[168:171], a[72:75], v[156:159]\n"
    "v_cvt_pk_bf16_f32 v207, v210, v211\n"
    "v_mfma_f32_16x16x32_bf16 v[156:159], a[172:175], a[76:79], v[156:159]\n"
    "ds_write_addtid_b32 v199 offset:2816\n"
    "v_mfma_f32_16x16x32_bf16 v[164:167], a[172:175], a[108:111], v[164:167]\n"
    "v_exp_f32_e32 v218, v218\n"
    "v_mfma_f32_16x16x32_bf16 v[164:167], a[168:171], a[104:107], v[164:167]\n"
    "ds_write_addtid_b32 v206 offset:3584\n"
    "v_mfma_f32_16x16x32_bf16 v[172:175], a[168:171], a[136:139], v[172:175]\n"
    "v_mfma_f32_16x16x32_bf16 v[172:175], a[172:175], a[140:143], v[172:175]\n"
    "s_waitcnt lgkmcnt(10)\n"
    "v_exp_f32_e32 v219, v219\n"
    "v_mfma_f32_16x16x32_bf16 v[172:175], a[176:179], a[144:147], v[172:175]\n"
    "ds_read_b128 a[164:167], v241 offset:18432\n"
    "v_mfma_f32_16x16x32_bf16 v[172:175], a[180:183], a[148:151], v[172:175]\n"
    "v_cvt_pk_bf16_f32 v214, v216, v217\n"
    "v_mfma_f32_16x16x32_bf16 v[164:167], a[180:183], a[116:119], v[164:167]\n"
    "ds_write_addtid_b32 v207 offset:3840\n"
    "v_mfma_f32_16x16x32_bf16 v[164:167], a[176:179], a[112:115], v[164:167]\n"
    "ds_read_b128 a[168:171], v240 offset:22528\n"
    "v_mfma_f32_16x16x32_bf16 v[156:159], a[176:179], a[80:83], v[156:159]\n"
    "v_cvt_pk_bf16_f32 v215, v218, v219\n"
    "v_mfma_f32_16x16x32_bf16 v[156:159], a[180:183], a[84:87], v[156:159]\n"
    "ds_write_addtid_b32 v214 offset:4608\n"
    "v_mfma_f32_16x16x32_bf16 v[148:151], a[180:183], a[52:55], v[148:151]\n"
    "ds_write_addtid_b32 v215 offset:4864\n"
    "v_mfma_f32_16x16x32_bf16 v[148:151], a[176:179], a[48:51], v[148:151]\n"
    "v_mfma_f32_16x16x32_bf16 v[140:143], a[176:179], a[16:19], v[140:143]\n"
    "ds_read_b128 a[172:175], v241 offset:22528\n"
    "v_mfma_f32_16x16x32_bf16 v[140:143], a[180:183], a[20:23], v[140:143]\n"
    "s_waitcnt lgkmcnt(10)\n"
    "s_waitcnt lgkmcnt(1)\n"
    "v_mfma_f32_16x16x32_bf16 v[140:143], a[184:187], a[24:27], v[140:143]\n"
    "s_barrier\n"
    "v_mfma_f32_16x16x32_bf16 v[140:143], a[188:191], a[28:31], v[140:143]\n"
    "s_add_u32 m0, s43, 0\n"
    "v_mfma_f32_16x16x32_bf16 v[148:151], a[188:191], a[60:63], v[148:151]\n"
    "ds_read_b128 a[176:179], v242 offset:18432\n"
    "v_mfma_f32_16x16x32_bf16 v[148:151], a[184:187], a[56:59], v[148:151]\n"
    "buffer_load_dwordx4 v248, s[8:11], s40 offen offset:0 sc1 lds\n"
    "v_mfma_f32_16x16x32_bf16 v[156:159], a[184:187], a[88:91], v[156:159]\n"
    "ds_read2st64_b32 v[184:185], v249 offset0:0 offset1:1\n"
    "v_mfma_f32_16x16x32_bf16 v[156:159], a[188:191], a[92:95], v[156:159]\n"
    "v_mfma_f32_16x16x32_bf16 v[164:167], a[188:191], a[124:127], v[164:167]\n"
    "ds_read2st64_b32 v[186:187], v249 offset0:2 offset1:3\n"
    "v_mfma_f32_16x16x32_bf16 v[164:167], a[184:187], a[120:123], v[164:167]\n"
    "buffer_load_dwordx4 v248, s[8:11], s40 offen offset:1024 sc1 lds\n"
    "v_mfma_f32_16x16x32_bf16 v[172:175], a[184:187], a[152:155], v[172:175]\n"
    "v_mfma_f32_16x16x32_bf16 v[172:175], a[188:191], a[156:159], v[172:175]\n"
    "v_mfma_f32_16x16x32_bf16 v[176:179], a[160:163], a[128:131], v[236:239]\n"
    "ds_read_b128 a[180:183], v243 offset:18432\n"
    "v_mfma_f32_16x16x32_bf16 v[176:179], a[164:167], a[132:135], v[176:179]\n"
    "buffer_load_dwordx4 v248, s[8:11], s40 offen offset:2048 sc1 lds\n"
    "v_mfma_f32_16x16x32_bf16 v[168:171], a[164:167], a[100:103], v[232:235]\n"
    "ds_read2st64_b32 v[192:193], v249 offset0:4 offset1:5\n"
    "v_mfma_f32_16x16x32_bf16 v[168:171], a[160:163], a[96:99], v[168:171]\n"
    "ds_read_b128 a[184:187], v242 offset:22528\n"
    "v_mfma_f32_16x16x32_bf16 v[160:163], a[160:163], a[64:67], v[228:231]\n"
    "ds_read2st64_b32 v[194:195], v249 offset0:6 offset1:7\n"
    "v_mfma_f32_16x16x32_bf16 v[160:163], a[164:167], a[68:71], v[160:163]\n"
    "buffer_load_dwordx4 v248, s[8:11], s40 offen offset:3072 sc1 lds\n"
    "v_mfma_f32_16x16x32_bf16 v[152:155], a[164:167], a[36:39], v[224:227]\n"
    "s_add_u32 m0, s43, 4096\n"
    "v_exp_f32_e32 v140, v140\n"
    "v_mfma_f32_16x16x32_bf16 v[152:155], a[160:163], a[32:35], v[152:155]\n"
    "s_add_u32 s40, s40, 0x8000\n"
    "v_exp_f32_e32 v141, v141\n"
    "v_mfma_f32_16x16x32_bf16 v[144:147], a[160:163], a[0:3], v[220:223]\n"
    "ds_read2st64_b32 v[200:201], v249 offset0:8 offset1:9\n"
    "v_mfma_f32_16x16x32_bf16 v[144:147], a[164:167], a[4:7], v[144:147]\n"
    "s_waitcnt lgkmcnt(8)\n"
    "buffer_load_dwordx4 v248, s[8:11], s41 offen offset:0 sc1 lds\n"
    "v_mfma_f32_16x16x32_bf16 v[144:147], a[168:171], a[8:11], v[144:147]\n"
    "ds_read_b128 a[188:191], v243 offset:22528\n"
    "v_mfma_f32_16x16x32_bf16 v[144:147], a[172:175], a[12:15], v[144:147]\n"
    "ds_read_b64_tr_b16 a[160:161], v244 offset:8192\n"
    "v_mfma_f32_16x16x32_bf16 v[152:155], a[172:175], a[44:47], v[152:155]\n"
    "ds_read2st64_b32 v[202:203], v249 offset0:10 offset1:11\n"
    "v_mfma_f32_16x16x32_bf16 v[152:155], a[168:171], a[40:43], v[152:155]\n"
    "buffer_load_dwordx4 v248, s[8:11], s41 offen offset:1024 sc1 lds\n"
    "v_mfma_f32_16x16x32_bf16 v[160:163], a[168:171], a[72:75], v[160:163]\n"
    "ds_read_b64_tr_b16 a[162:163], v244 offset:10240\n"
    "v_mfma_f32_16x16x32_bf16 v[160:163], a[172:175], a[76:79], v[160:163]\n"
    "v_exp_f32_e32 v142, v142\n"
    "v_mfma_f32_16x16x32_bf16 v[168:171], a[172:175], a[108:111], v[168:171]\n"
    "ds_read2st64_b32 v[208:209], v249 offset0:12 offset1:13\n"
    "v_mfma_f32_16x16x32_bf16 v[168:171], a[168:171], a[104:107], v[168:171]\n"
    "buffer_load_dwordx4 v248, s[8:11], s41 offen offset:2048 sc1 lds\n"
    "v_mfma_f32_16x16x32_bf16 v[176:179], a[168:171], a[136:139], v[176:179]\n"
    "ds_read_b64_tr_b16 a[164:165], v244 offset:40960\n"
    "v_mfma_f32_16x16x32_bf16 v[176:179], a[172:175], a[140:143], v[176:179]\n"
    "s_waitcnt lgkmcnt(10)\n"
    "ds_read2st64_b32 v[210:211], v249 offset0:14 offset1:15\n"
    "v_mfma_f32_16x16x32_bf16 v[176:179], a[176:179], a[144:147], v[176:179]\n"
    "ds_read_b64_tr_b16 a[168:169], v245 offset:8192\n"
    "v_mfma_f32_16x16x32_bf16 v[176:179], a[180:183], a[148:151], v[176:179]\n"
    "buffer_load_dwordx4 v248, s[8:11], s41 offen offset:3072 sc1 lds\n"
    "v_mfma_f32_16x16x32_bf16 v[168:171], a[180:183], a[116:119], v[168:171]\n"
    "ds_read_b64_tr_b16 a[166:167], v244 offset:43008\n"
    "v_mfma_f32_16x16x32_bf16 v[168:171], a[176:179], a[112:115], v[168:171]\n"
    "s_add_u32 s41, s41, 0x8000\n"
    "v_exp_f32_e32 v143, v143\n"
    "v_mfma_f32_16x16x32_bf16 v[160:163], a[176:179], a[80:83], v[160:163]\n"
    "ds_read_b64_tr_b16 a[170:171], v245 offset:10240\n"
    "v_cvt_pk_bf16_f32 v140, v140, v141\n"
    "v_mfma_f32_16x16x32_bf16 v[160:163], a[180:183], a[84:87], v[160:163]\n"
    "ds_read2st64_b32 v[216:217], v249 offset0:16 offset1:17\n"
    "v_mfma_f32_16x16x32_bf16 v[152:155], a[180:183], a[52:55], v[152:155]\n"
    "v_exp_f32_e32 v148, v148\n"
    "v_mfma_f32_16x16x32_bf16 v[152:155], a[176:179], a[48:51], v[152:155]\n"
    "v_exp_f32_e32 v149, v149\n"
    "v_mfma_f32_16x16x32_bf16 v[144:147], a[176:179], a[16:19], v[144:147]\n"
    "ds_read_b64_tr_b16 a[172:173], v245 offset:40960\n"
    "v_cvt_pk_bf16_f32 v141, v142, v143\n"
    "v_mfma_f32_16x16x32_bf16 v[144:147], a[180:183], a[20:23], v[144:147]\n"
    "s_waitcnt lgkmcnt(11)\n"
    "ds_read2st64_b32 v[218:219], v249 offset0:18 offset1:19\n"
    "v_mfma_f32_16x16x32_bf16 v[144:147], a[184:187], a[24:27], v[144:147]\n"
    "ds_read_b64_tr_b16 a[176:177], v246 offset:8192\n"
    "v_mfma_f32_16x16x32_bf16 v[144:147], a[188:191], a[28:31], v[144:147]\n"
    "ds_read_b64_tr_b16 a[174:175], v245 offset:43008\n"
    "v_mfma_f32_16x16x32_bf16 v[152:155], a[188:191], a[60:63], v[152:155]\n"
    "v_exp_f32_e32 v150, v150\n"
    "v_mfma_f32_16x16x32_bf16 v[152:155], a[184:187], a[56:59], v[152:155]\n"
    "v_exp_f32_e32 v151, v151\n"
    "v_mfma_f32_16x16x32_bf16 v[160:163], a[184:187], a[88:91], v[160:163]\n"
    "s_waitcnt vmcnt(8)\n"
    "v_readfirstlane_b32 s81, v253\n"
    "v_mfma_f32_16x16x32_bf16 v[160:163], a[188:191], a[92:95], v[160:163]\n"
    "ds_read_b64_tr_b16 a[178:179], v246 offset:10240\n"
    "v_mfma_f32_16x16x32_bf16 v[168:171], a[188:191], a[124:127], v[168:171]\n"
    "s_cmp_eq_u32 s81, s39\n"
    "v_cvt_pk_bf16_f32 v148, v148, v149\n"
    "v_mfma_f32_16x16x32_bf16 v[168:171], a[184:187], a[120:123], v[168:171]\n"
    "s_cselect_b32 s88, s83, 2.0\n"
    "v_exp_f32_e32 v156, v156\n"
    "v_mfma_f32_16x16x32_bf16 v[176:179], a[184:187], a[152:155], v[176:179]\n"
    "s_mov_b32 m0, s30\n"
    "ds_read_b64_tr_b16 a[180:181], v246 offset:40960\n"
    "v_mfma_f32_16x16x32_bf16 v[176:179], a[188:191], a[156:159], v[176:179]\n"
    "s_waitcnt lgkmcnt(4)\n"
    "v_exp_f32_e32 v157, v157\n"
    "v_mfma_f32_16x16x32_bf16 a[236:239], a[160:163], v[180:183], a[236:239]\n"
    "buffer_load_dwordx4 v250, s[8:11], s88 offen offset:0 sc1 lds\n"
    "v_mfma_f32_16x16x32_bf16 a[236:239], a[164:167], v[184:187], a[236:239]\n"
    "ds_read_b64_tr_b16 a[182:183], v246 offset:43008\n"
    "v_mfma_f32_16x16x32_bf16 a[240:243], a[164:167], v[192:195], a[240:243]\n"
    "ds_read_b64_tr_b16 a[184:185], v247 offset:8192\n"
    "v_mfma_f32_16x16x32_bf16 a[240:243], a[160:163], v[188:191], a[240:243]\n"
    "v_cvt_pk_bf16_f32 v149, v150, v151\n"
    "v_mfma_f32_16x16x32_bf16 a[244:247], a[160:163], v[196:199], a[244:247]\n"
    "ds_read_b64_tr_b16 a[186:187], v247 offset:10240\n"
    "v_mfma_f32_16x16x32_bf16 a[244:247], a[164:167], v[200:203], a[244:247]\n"
    "v_exp_f32_e32 v158, v158\n"
    "v_mfma_f32_16x16x32_bf16 a[248:251], a[164:167], v[208:211], a[248:251]\n"
    "v_exp_f32_e32 v159, v159\n"
    "v_mfma_f32_16x16x32_bf16 a[248:251], a[160:163], v[204:207], a[248:251]\n"
    "v_cvt_pk_bf16_f32 v156, v156, v157\n"
    "v_mfma_f32_16x16x32_bf16 a[252:255], a[160:163], v[212:215], a[252:255]\n"
    "ds_read_b64_tr_b16 a[188:189], v247 offset:40960\n"
    "v_mfma_f32_16x16x32_bf16 a[252:255], a[164:167], v[216:219], a[252:255]\n"
    "s_waitcnt lgkmcnt(6)\n"
    "v_exp_f32_e32 v164, v164\n"
    "v_mfma_f32_16x16x32_bf16 v[16:19], a[172:175], v[216:219], v[16:19]\n"
    "ds_read_b64_tr_b16 a[160:161], v244 offset:12288\n"
    "v_mfma_f32_16x16x32_bf16 v[16:19], a[168:171], v[212:215], v[16:19]\n"
    "ds_read_b64_tr_b16 a[190:191], v247 offset:43008\n"
    "v_mfma_f32_16x16x32_bf16 v[12:15], a[168:171], v[204:207], v[12:15]\n"
    "v_exp_f32_e32 v165, v165\n"
    "v_mfma_f32_16x16x32_bf16 v[12:15], a[172:175], v[208:211], v[12:15]\n"
    "v_cvt_pk_bf16_f32 v157, v158, v159\n"
    "v_mfma_f32_16x16x32_bf16 v[8:11], a[172:175], v[200:203], v[8:11]\n"
    "ds_read_b64_tr_b16 a[162:163], v244 offset:14336\n"
    "v_mfma_f32_16x16x32_bf16 v[8:11], a[168:171], v[196:199], v[8:11]\n"
    "v_exp_f32_e32 v166, v166\n"
    "v_mfma_f32_16x16x32_bf16 v[4:7], a[168:171], v[188:191], v[4:7]\n"
    "v_exp_f32_e32 v167, v167\n"
    "v_mfma_f32_16x16x32_bf16 v[4:7], a[172:175], v[192:195], v[4:7]\n"
    "v_cvt_pk_bf16_f32 v164, v164, v165\n"
    "v_mfma_f32_16x16x32_bf16 v[0:3], a[172:175], v[184:187], v[0:3]\n"
    "ds_read_b64_tr_b16 a[164:165], v244 offset:45056\n"
    "v_mfma_f32_16x16x32_bf16 v[0:3], a[168:171], v[180:183], v[0:3]\n"
    "s_waitcnt lgkmcnt(7)\n"
    "v_exp_f32_e32 v172, v172\n"
    "v_mfma_f32_16x16x32_bf16 v[20:23], a[176:179], v[180:183], v[20:23]\n"
    "v_mfma_f32_16x16x32_bf16 v[20:23], a[180:183], v[184:187], v[20:23]\n"
    "ds_read_b64_tr_b16 a[166:167], v244 offset:47104\n"
    "v_mfma_f32_16x16x32_bf16 v[24:27], a[180:183], v[192:195], v[24:27]\n"
    "ds_read_b64_tr_b16 a[168:169], v245 offset:12288\n"
    "v_mfma_f32_16x16x32_bf16 v[24:27], a[176:179], v[188:191], v[24:27]\n"
    "v_exp_f32_e32 v173, v173\n"
    "v_mfma_f32_16x16x32_bf16 v[28:31], a[176:179], v[196:199], v[28:31]\n"
    "ds_read_b64_tr_b16 a[170:171], v245 offset:14336\n"
    "v_mfma_f32_16x16x32_bf16 v[28:31], a[180:183], v[200:203], v[28:31]\n"
    "v_cvt_pk_bf16_f32 v165, v166, v167\n"
    "v_mfma_f32_16x16x32_bf16 v[32:35], a[180:183], v[208:211], v[32:35]\n"
    "v_exp_f32_e32 v174, v174\n"
    "v_mfma_f32_16x16x32_bf16 v[32:35], a[176:179], v[204:207], v[32:35]\n"
    "v_exp_f32_e32 v175, v175\n"
    "v_mfma_f32_16x16x32_bf16 v[36:39], a[176:179], v[212:215], v[36:39]\n"
    "ds_read_b64_tr_b16 a[172:173], v245 offset:45056\n"
    "v_mfma_f32_16x16x32_bf16 v[36:39], a[180:183], v[216:219], v[36:39]\n"
    "s_waitcnt lgkmcnt(6)\n"
    "v_cvt_pk_bf16_f32 v172, v172, v173\n"
    "v_mfma_f32_16x16x32_bf16 v[56:59], a[188:191], v[216:219], v[56:59]\n"
    ".long 0xbedf00ff\n"
    ".Lat_w5:\n"
    "s_setpc_b64 s[2:3]\n"
    "ds_read_b64_tr_b16 a[176:177], v246 offset:12288\n"
    "v_mfma_f32_16x16x32_bf16 v[56:59], a[184:187], v[212:215], v[56:59]\n"
    "ds_read_b64_tr_b16 a[174:175], v245 offset:47104\n"
    "v_mfma_f32_16x16x32_bf16 v[52:55], a[184:187], v[204:207], v[52:55]\n"
    "v_cvt_pk_bf16_f32 v173, v174, v175\n"
    "v_mfma_f32_16x16x32_bf16 v[52:55], a[188:191], v[208:211], v[52:55]\n"
    "v_exp_f32_e32 v144, v144\n"
    "v_mfma_f32_16x16x32_bf16 v[48:51], a[188:191], v[200:203], v[48:51]\n"
    "ds_read_b64_tr_b16 a[178:179], v246 offset:14336\n"
    "v_mfma_f32_16x16x32_bf16 v[48:51], a[184:187], v[196:199], v[48:51]\n"
    "v_mfma_f32_16x16x32_bf16 v[44:47], a[184:187], v[188:191], v[44:47]\n"
    "v_exp_f32_e32 v145, v145\n"
    "v_mfma_f32_16x16x32_bf16 v[44:47], a[188:191], v[192:195], v[44:47]\n"
    "v_exp_f32_e32 v146, v146\n"
    "v_mfma_f32_16x16x32_bf16 v[40:43], a[188:191], v[184:187], v[40:43]\n"
    "ds_read_b64_tr_b16 a[180:181], v246 offset:45056\n"
    "v_mfma_f32_16x16x32_bf16 v[40:43], a[184:187], v[180:183], v[40:43]\n"
    "s_waitcnt lgkmcnt(7)\n"
    "v_exp_f32_e32 v147, v147\n"
    "v_mfma_f32_16x16x32_bf16 v[60:63], a[160:163], v[180:183], v[60:63]\n"
    "ds_read_b64_tr_b16 a[184:185], v247 offset:12288\n"
    "v_mfma_f32_16x16x32_bf16 v[60:63], a[164:167], v[184:187], v[60:63]\n"
    "ds_read_b64_tr_b16 a[182:183], v246 offset:47104\n"
    "v_mfma_f32_16x16x32_bf16 v[64:67], a[164:167], v[192:195], v[64:67]\n"
    "ds_read_b64_tr_b16 a[186:187], v247 offset:14336\n"
    "v_mfma_f32_16x16x32_bf16 v[64:67], a[160:163], v[188:191], v[64:67]\n"
    "v_cvt_pk_bf16_f32 v142, v144, v145\n"
    "v_mfma_f32_16x16x32_bf16 v[68:71], a[160:163], v[196:199], v[68:71]\n"
    "v_exp_f32_e32 v152, v152\n"
    "v_mfma_f32_16x16x32_bf16 v[68:71], a[164:167], v[200:203], v[68:71]\n"
    "ds_read_b64_tr_b16 a[188:189], v247 offset:45056\n"
    "v_mfma_f32_16x16x32_bf16 v[72:75], a[164:167], v[208:211], v[72:75]\n"
    "ds_read_b64_tr_b16 a[190:191], v247 offset:47104\n"
    "v_mfma_f32_16x16x32_bf16 v[72:75], a[160:163], v[204:207], v[72:75]\n"
    "s_waitcnt vmcnt(10)\n"
    "v_mfma_f32_16x16x32_bf16 v[76:79], a[160:163], v[212:215], v[76:79]\n"
    "s_barrier\n"
    "v_mfma_f32_16x16x32_bf16 v[76:79], a[164:167], v[216:219], v[76:79]\n"
    "s_waitcnt lgkmcnt(7)\n"
    "s_mov_b32 m0, s42\n"
    "v_mfma_f32_16x16x32_bf16 v[96:99], a[172:175], v[216:219], v[96:99]\n"
    "v_exp_f32_e32 v153, v153\n"
    "v_mfma_f32_16x16x32_bf16 v[96:99], a[168:171], v[212:215], v[96:99]\n"
    "v_cvt_pk_bf16_f32 v143, v146, v147\n"
    "v_mfma_f32_16x16x32_bf16 v[92:95], a[168:171], v[204:207], v[92:95]\n"
    "v_exp_f32_e32 v154, v154\n"
    "v_mfma_f32_16x16x32_bf16 v[92:95], a[172:175], v[208:211], v[92:95]\n"
    "ds_read_b128 a[160:163], v240 offset:24576\n"
    "v_mfma_f32_16x16x32_bf16 v[88:91], a[172:175], v[200:203], v[88:91]\n"
    "ds_write_addtid_b32 v140 offset:0\n"
    "v_mfma_f32_16x16x32_bf16 v[88:91], a[168:171], v[196:199], v[88:91]\n"
    "v_exp_f32_e32 v155, v155\n"
    "v_mfma_f32_16x16x32_bf16 v[84:87], a[168:171], v[188:191], v[84:87]\n"
    "ds_write_addtid_b32 v141 offset:256\n"
    "v_mfma_f32_16x16x32_bf16 v[84:87], a[172:175], v[192:195], v[84:87]\n"
    "ds_write_addtid_b32 v148 offset:1024\n"
    "v_mfma_f32_16x16x32_bf16 v[80:83], a[172:175], v[184:187], v[80:83]\n"
    "v_cvt_pk_bf16_f32 v150, v152, v153\n"
    "v_mfma_f32_16x16x32_bf16 v[80:83], a[168:171], v[180:183], v[80:83]\n"
    "s_waitcnt lgkmcnt(7)\n"
    "v_exp_f32_e32 v160, v160\n"
    "v_mfma_f32_16x16x32_bf16 v[100:103], a[176:179], v[180:183], v[100:103]\n"
    "ds_read_b128 a[164:167], v241 offset:24576\n"
    "v_mfma_f32_16x16x32_bf16 v[100:103], a[180:183], v[184:187], v[100:103]\n"
    "ds_write_addtid_b32 v149 offset:1280\n"
    "v_mfma_f32_16x16x32_bf16 v[104:107], a[180:183], v[192:195], v[104:107]\n"
    "v_exp_f32_e32 v161, v161\n"
    "v_mfma_f32_16x16x32_bf16 v[104:107], a[176:179], v[188:191], v[104:107]\n"
    "ds_read_b128 a[168:171], v240 offset:28672\n"
    "v_mfma_f32_16x16x32_bf16 v[108:111], a[176:179], v[196:199], v[108:111]\n"
    "ds_write_addtid_b32 v156 offset:2048\n"
    "v_mfma_f32_16x16x32_bf16 v[108:111], a[180:183], v[200:203], v[108:111]\n"
    "v_cvt_pk_bf16_f32 v151, v154, v155\n"
    "v_mfma_f32_16x16x32_bf16 v[112:115], a[180:183], v[208:211], v[112:115]\n"
    "ds_write_addtid_b32 v157 offset:2304\n"
    "v_mfma_f32_16x16x32_bf16 v[112:115], a[176:179], v[204:207], v[112:115]\n"
    "v_exp_f32_e32 v162, v162\n"
    "v_mfma_f32_16x16x32_bf16 v[116:119], a[176:179], v[212:215], v[116:119]\n"
    "ds_write_addtid_b32 v164 offset:3072\n"
    "v_mfma_f32_16x16x32_bf16 v[116:119], a[180:183], v[216:219], v[116:119]\n"
    "s_waitcnt lgkmcnt(10)\n"
    "v_exp_f32_e32 v163, v163\n"
    "v_mfma_f32_16x16x32_bf16 v[136:139], a[188:191], v[216:219], v[136:139]\n"
    "ds_read_b128 a[172:175], v241 offset:28672\n"
    "v_mfma_f32_16x16x32_bf16 v[136:139], a[184:187], v[212:215], v[136:139]\n"
    "ds_write_addtid_b32 v165 offset:3328\n"
    "v_cvt_pk_bf16_f32 v158, v160, v161\n"
    "v_mfma_f32_16x16x32_bf16 v[132:135], a[184:187], v[204:207], v[132:135]\n"
    "ds_read_b128 a[176:179], v242 offset:24576\n"
    "v_mfma_f32_16x16x32_bf16 v[132:135], a[188:191], v[208:211], v[132:135]\n"
    "ds_write_addtid_b32 v172 offset:4096\n"
    "v_mfma_f32_16x16x32_bf16 v[128:131], a[188:191], v[200:203], v[128:131]\n"
    "v_exp_f32_e32 v168, v168\n"
    "v_mfma_f32_16x16x32_bf16 v[128:131], a[184:187], v[196:199], v[128:131]\n"
    "s_sub_u32 s44, s44, 1\n"
    "ds_write_addtid_b32 v173 offset:4352\n"
    "v_mfma_f32_16x16x32_bf16 v[124:127], a[184:187], v[188:191], v[124:127]\n"
    "s_cmp_eq_u32 s44, s80\n"
    "v_exp_f32_e32 v169, v169\n"
    "v_mfma_f32_16x16x32_bf16 v[124:127], a[188:191], v[192:195], v[124:127]\n"
    "s_cselect_b32 s88, s39, s81\n"
    "ds_read_b128 a[180:183], v243 offset:24576\n"
    "v_mfma_f32_16x16x32_bf16 v[120:123], a[188:191], v[184:187], v[120:123]\n"
    "s_cmp_lg_u32 s88, s39\n"
    "ds_write_addtid_b32 v142 offset:512\n"
    "v_mfma_f32_16x16x32_bf16 v[120:123], a[184:187], v[180:183], v[120:123]\n"
    "s_waitcnt lgkmcnt(12)\n"
    "v_cvt_pk_bf16_f32 v159, v162, v163\n"
    "s_cbranch_scc1 .Lat_loop\n"
    "s_cmp_eq_u32 s44, 0\n"
    "s_cbranch_scc1 .Lat_tail\n"
    "s_cmp_eq_u32 s85, 0\n"
    "s_cbranch_scc1 .Lat_loop\n"
    "s_cmp_lg_u32 s81, s39\n"
    "s_cbranch_scc1 .Lat_lp0\n"
    ".Lat_g1b:\n"
    "v_lshrrev_b32_e32 v228, 2, v250\n"
    "v_add_u32_e32 v254, s30, v228\n"
    "s_waitcnt vmcnt(0)\n"
    "ds_read_b32 v254, v254 offset:960\n"
    "v_lshrrev_b32_e32 v228, 9, v250\n"
    "v_lshl_add_u32 v251, v228, 2, s30\n"
    "v_and_b32_e32 v252, 0xf0, v250\n"
    "v_lshl_add_u32 v252, v228, 8, v252\n"
    "v_bfe_u32 v229, v250, 8, 1\n"
    "v_lshl_add_u32 v252, v229, 3, v252\n"
    "v_and_b32_e32 v239, 0x1f0, v250\n"
    "v_bfe_u32 v228, v250, 2, 4\n"
    "s_mov_b32 s88, 0xcedc\n"
    "v_bfe_u32 v238, s88, v228, 4\n"
    "v_mul_u32_u24_e32 v238, 0x1111, v238\n"
    "v_mov_b64_e32 v[224:225], 0\n"
    "v_mov_b64_e32 v[226:227], 0\n"
    "s_mov_b32 s85, 0\n"
    "s_mov_b32 s82, 2.0\n"
    "s_mov_b32 s83, 2.0\n"
    "s_mov_b32 s89, 4\n"
    "s_waitcnt lgkmcnt(0)\n"
    "v_readlane_b32 s88, v254, 0\n"
    "v_readlane_b32 s87, v254, 1\n"
    "v_readlane_b32 s84, v254, 2\n"
    "v_readlane_b32 s86, v254, 4\n"
    "s_nop 3\n"
    "s_min_u32 s88, s88, s44\n"
    "s_sub_u32 s80, s44, s88\n"
    "s_cmp_eq_u32 s88, 0\n"
    "s_cbranch_scc1 .Lat_g2\n"
    ".Lat_gloop:\n"
    "v_mfma_f32_16x16x32_bf16 v[212:215], a[160:163], a[128:131], v[220:223]\n"
    "ds_write_addtid_b32 v143 offset:768\n"
    "v_mfma_f32_16x16x32_bf16 v[212:215], a[164:167], a[132:135], v[212:215]\n"
    "v_exp_f32_e32 v170, v170\n"
    "v_mfma_f32_16x16x32_bf16 v[204:207], a[164:167], a[100:103], v[220:223]\n"
    "ds_write_addtid_b32 v150 offset:1536\n"
    "v_mfma_f32_16x16x32_bf16 v[204:207], a[160:163], a[96:99], v[204:207]\n"
    "ds_read_b128 a[184:187], v242 offset:28672\n"
    "v_mfma_f32_16x16x32_bf16 v[196:199], a[160:163], a[64:67], v[220:223]\n"
    "v_exp_f32_e32 v171, v171\n"
    "v_mfma_f32_16x16x32_bf16 v[196:199], a[164:167], a[68:71], v[196:199]\n"
    "ds_write_addtid_b32 v151 offset:1792\n"
    "v_cvt_pk_bf16_f32 v166, v168, v169\n"
    "v_mfma_f32_16x16x32_bf16 v[188:191], a[164:167], a[36:39], v[220:223]\n"
    "v_exp_f32_e32 v176, v176\n"
    "v_mfma_f32_16x16x32_bf16 v[188:191], a[160:163], a[32:35], v[188:191]\n"
    "v_exp_f32_e32 v177, v177\n"
    "v_mfma_f32_16x16x32_bf16 v[180:183], a[160:163], a[0:3], v[220:223]\n"
    "ds_write_addtid_b32 v158 offset:2560\n"
    "v_cvt_pk_bf16_f32 v167, v170, v171\n"
    "v_mfma_f32_16x16x32_bf16 v[180:183], a[164:167], a[4:7], v[180:183]\n"
    "s_waitcnt vmcnt(20)\n"
    "v_exp_f32_e32 v178, v178\n"
    "v_smfmac_f32_16x16x64_bf16 v[224:227], a[232:235], a[192:199], v238\n"
    "s_waitcnt lgkmcnt(10)\n"
    "ds_read_b128 a[188:191], v243 offset:28672\n"
    "v_mfma_f32_16x16x32_bf16 v[180:183], a[168:171], a[8:11], v[180:183]\n"
    "s_bitcmp1_b32 s86, 0\n"
    "ds_read_b128 a[160:163], v240 offset:26624\n"
    "v_mfma_f32_16x16x32_bf16 v[180:183], a[172:175], a[12:15], v[180:183]\n"
    "s_cselect_b64 exec, -1, 0\n"
    "v_cvt_pk_bf16_f32 v236, v224, v225\n"
    "v_cvt_pk_bf16_f32 v237, v226, v227\n"
    "v_mfma_f32_16x16x32_bf16 v[188:191], a[172:175], a[44:47], v[188:191]\n"
    "s_cselect_b32 s88, 512, 0\n"
    "v_mov_b64_e32 v[224:225], 0\n"
    "v_mov_b64_e32 v[226:227], 0\n"
    "v_mfma_f32_16x16x32_bf16 v[188:191], a[168:171], a[40:43], v[188:191]\n"
    "buffer_store_dwordx2 v[236:237], v252, s[8:11], s87 offen sc1\n"
    "v_mfma_f32_16x16x32_bf16 v[196:199], a[168:171], a[72:75], v[196:199]\n"
    "s_mov_b64 exec, -1\n"
    "ds_read2_b32 v[228:229], v251 offset0:0 offset1:2\n"
    "v_mfma_f32_16x16x32_bf16 v[196:199], a[172:175], a[76:79], v[196:199]\n"
    "s_add_u32 s87, s87, s88\n"
    "ds_read2_b32 v[230:231], v251 offset0:4 offset1:6\n"
    "v_mfma_f32_16x16x32_bf16 v[204:207], a[172:175], a[108:111], v[204:207]\n"
    "ds_read_b32 v232, v251 offset:32\n"
    "v_mfma_f32_16x16x32_bf16 v[204:207], a[168:171], a[104:107], v[204:207]\n"
    "v_exp_f32_e32 v179, v179\n"
    "v_mfma_f32_16x16x32_bf16 v[212:215], a[168:171], a[136:139], v[212:215]\n"
    "ds_write_addtid_b32 v159 offset:2816\n"
    "v_cvt_pk_bf16_f32 v174, v176, v177\n"
    "v_mfma_f32_16x16x32_bf16 v[212:215], a[172:175], a[140:143], v[212:215]\n"
    "s_waitcnt lgkmcnt(13)\n"
    "ds_read_b128 a[164:167], v241 offset:26624\n"
    "v_mfma_f32_16x16x32_bf16 v[212:215], a[176:179], a[144:147], v[212:215]\n"
    "ds_write_addtid_b32 v166 offset:3584\n"
    "v_cvt_pk_bf16_f32 v175, v178, v179\n"
    "v_mfma_f32_16x16x32_bf16 v[212:215], a[180:183], a[148:151], v[212:215]\n"
    "ds_read_b128 a[168:171], v240 offset:30720\n"
    "v_mfma_f32_16x16x32_bf16 v[204:207], a[180:183], a[116:119], v[204:207]\n"
    "s_waitcnt lgkmcnt(6)\n"
    "ds_write_addtid_b32 v167 offset:3840\n"
    "v_mad_u32_u16 v234, v228, s90, v239 op_sel:[0,0,0,0]\n"
    "v_mfma_f32_16x16x32_bf16 v[204:207], a[176:179], a[112:115], v[204:207]\n"
    "buffer_load_dwordx4 a[192:195], v234, s[20:23], 0 offen sc1\n"
    "v_mad_u32_u16 v235, v228, s90, v239 op_sel:[1,0,0,0]\n"
    "v_mfma_f32_16x16x32_bf16 v[196:199], a[176:179], a[80:83], v[196:199]\n"
    "ds_write_addtid_b32 v174 offset:4608\n"
    "v_mfma_f32_16x16x32_bf16 v[196:199], a[180:183], a[84:87], v[196:199]\n"
    "ds_write_addtid_b32 v175 offset:4864\n"
    "v_mfma_f32_16x16x32_bf16 v[188:191], a[180:183], a[52:55], v[188:191]\n"
    "buffer_load_dwordx4 a[196:199], v235, s[20:23], 0 offen sc1\n"
    "v_mfma_f32_16x16x32_bf16 v[188:191], a[176:179], a[48:51], v[188:191]\n"
    "v_mfma_f32_16x16x32_bf16 v[180:183], a[176:179], a[16:19], v[180:183]\n"
    "s_waitcnt lgkmcnt(0)\n"
    "v_mfma_f32_16x16x32_bf16 v[180:183], a[180:183], a[20:23], v[180:183]\n"
    "s_barrier\n"
    "v_mfma_f32_16x16x32_bf16 v[180:183], a[184:187], a[24:27], v[180:183]\n"
    "s_add_u32 m0, s43, 8192\n"
    "ds_read_b128 a[172:175], v241 offset:30720\n"
    "v_mfma_f32_16x16x32_bf16 v[180:183], a[188:191], a[28:31], v[180:183]\n"
    "ds_read2st64_b32 v[144:145], v249 offset0:0 offset1:1\n"
    "v_mfma_f32_16x16x32_bf16 v[188:191], a[188:191], a[60:63], v[188:191]\n"
    "buffer_load_dwordx4 v248, s[8:11], s40 offen offset:0 sc1 lds\n"
    "v_mfma_f32_16x16x32_bf16 v[188:191], a[184:187], a[56:59], v[188:191]\n"
    "ds_read_b128 a[176:179], v242 offset:26624\n"
    "v_mfma_f32_16x16x32_bf16 v[196:199], a[184:187], a[88:91], v[196:199]\n"
    "v_mfma_f32_16x16x32_bf16 v[196:199], a[188:191], a[92:95], v[196:199]\n"
    "v_mfma_f32_16x16x32_bf16 v[204:207], a[188:191], a[124:127], v[204:207]\n"
    "buffer_load_dwordx4 v248, s[8:11], s40 offen offset:1024 sc1 lds\n"
    "v_mfma_f32_16x16x32_bf16 v[204:207], a[184:187], a[120:123], v[204:207]\n"
    "ds_read2st64_b32 v[146:147], v249 offset0:2 offset1:3\n"
    "v_mfma_f32_16x16x32_bf16 v[212:215], a[184:187], a[152:155], v[212:215]\n"
    "v_mfma_f32_16x16x32_bf16 v[212:215], a[188:191], a[156:159], v[212:215]\n"
    "ds_read_b128 a[180:183], v243 offset:26624\n"
    "v_mfma_f32_16x16x32_bf16 v[216:219], a[160:163], a[128:131], v[220:223]\n"
    "buffer_load_dwordx4 v248, s[8:11], s40 offen offset:2048 sc1 lds\n"
    "v_mfma_f32_16x16x32_bf16 v[216:219], a[164:167], a[132:135], v[216:219]\n"
    "ds_read2st64_b32 v[152:153], v249 offset0:4 offset1:5\n"
    "v_mfma_f32_16x16x32_bf16 v[208:211], a[164:167], a[100:103], v[220:223]\n"
    "v_exp_f32_e32 v180, v180\n"
    "v_mfma_f32_16x16x32_bf16 v[208:211], a[160:163], a[96:99], v[208:211]\n"
    "ds_read_b128 a[184:187], v242 offset:30720\n"
    "v_mfma_f32_16x16x32_bf16 v[200:203], a[160:163], a[64:67], v[220:223]\n"
    "buffer_load_dwordx4 v248, s[8:11], s40 offen offset:3072 sc1 lds\n"
    "v_mfma_f32_16x16x32_bf16 v[200:203], a[164:167], a[68:71], v[200:203]\n"
    "s_add_u32 m0, s43, 12288\n"
    "ds_read2st64_b32 v[154:155], v249 offset0:6 offset1:7\n"
    "v_mfma_f32_16x16x32_bf16 v[192:195], a[164:167], a[36:39], v[220:223]\n"
    "s_add_u32 s40, s40, 0x8000\n"
    "ds_read2st64_b32 v[160:161], v249 offset0:8 offset1:9\n"
    "v_mfma_f32_16x16x32_bf16 v[192:195], a[160:163], a[32:35], v[192:195]\n"
    "ds_read2st64_b32 v[162:163], v249 offset0:10 offset1:11\n"
    "v_mfma_f32_16x16x32_bf16 v[184:187], a[160:163], a[0:3], v[220:223]\n"
    "buffer_load_dwordx4 v248, s[8:11], s41 offen offset:0 sc1 lds\n"
    "v_mfma_f32_16x16x32_bf16 v[184:187], a[164:167], a[4:7], v[184:187]\n"
    "s_waitcnt vmcnt(17)\n"
    "ds_read_b128 a[188:191], v243 offset:30720\n"
    "v_smfmac_f32_16x16x64_bf16 v[224:227], a[232:235], a[200:207], v238\n"
    "s_waitcnt lgkmcnt(10)\n"
    "v_mad_u32_u16 v234, v229, s90, v239 op_sel:[0,0,0,0]\n"
    "v_mfma_f32_16x16x32_bf16 v[184:187], a[168:171], a[8:11], v[184:187]\n"
    "s_bitcmp1_b32 s86, 1\n"
    "buffer_load_dwordx4 a[200:203], v234, s[20:23], 0 offen sc1\n"
    "v_mad_u32_u16 v235, v229, s90, v239 op_sel:[1,0,0,0]\n"
    "v_mfma_f32_16x16x32_bf16 v[184:187], a[172:175], a[12:15], v[184:187]\n"
    "s_cselect_b64 exec, -1, 0\n"
    "v_cvt_pk_bf16_f32 v236, v224, v225\n"
    "v_cvt_pk_bf16_f32 v237, v226, v227\n"
    "v_mfma_f32_16x16x32_bf16 v[192:195], a[172:175], a[44:47], v[192:195]\n"
    "s_cselect_b32 s88, 512, 0\n"
    "v_mov_b64_e32 v[224:225], 0\n"
    "v_mov_b64_e32 v[226:227], 0\n"
    "v_mfma_f32_16x16x32_bf16 v[192:195], a[168:171], a[40:43], v[192:195]\n"
    "buffer_store_dwordx2 v[236:237], v252, s[8:11], s87 offen sc1\n"
    "v_mfma_f32_16x16x32_bf16 v[200:203], a[168:171], a[72:75], v[200:203]\n"
    "s_mov_b64 exec, -1\n"
    "ds_read_b64_tr_b16 a[160:161], v244 offset:16384\n"
    "v_mfma_f32_16x16x32_bf16 v[200:203], a[172:175], a[76:79], v[200:203]\n"
    "s_add_u32 s87, s87, s88\n"
    "ds_read_b64_tr_b16 a[162:163], v244 offset:18432\n"
    "v_mfma_f32_16x16x32_bf16 v[208:211], a[172:175], a[108:111], v[208:211]\n"
    "buffer_load_dwordx4 v248, s[8:11], s41 offen offset:1024 sc1 lds\n"
    "v_mfma_f32_16x16x32_bf16 v[208:211], a[168:171], a[104:107], v[208:211]\n"
    "ds_read_b64_tr_b16 a[164:165], v244 offset:49152\n"
    "v_mfma_f32_16x16x32_bf16 v[216:219], a[168:171], a[136:139], v[216:219]\n"
    "ds_read2st64_b32 v[168:169], v249 offset0:12 offset1:13\n"
    "v_mfma_f32_16x16x32_bf16 v[216:219], a[172:175], a[140:143], v[216:219]\n"
    "s_waitcnt lgkmcnt(10)\n"
    "ds_read2st64_b32 v[170:171], v249 offset0:14 offset1:15\n"
    "v_mfma_f32_16x16x32_bf16 v[216:219], a[176:179], a[144:147], v[216:219]\n"
    "buffer_load_dwordx4 v248, s[8:11], s41 offen offset:2048 sc1 lds\n"
    "v_mfma_f32_16x16x32_bf16 v[216:219], a[180:183], a[148:151], v[216:219]\n"
    "ds_read_b64_tr_b16 a[166:167], v244 offset:51200\n"
    "v_mfma_f32_16x16x32_bf16 v[208:211], a[180:183], a[116:119], v[208:211]\n"
    "ds_read_b64_tr_b16 a[168:169], v245 offset:16384\n"
    "v_mfma_f32_16x16x32_bf16 v[208:211], a[176:179], a[112:115], v[208:211]\n"
    "v_exp_f32_e32 v181, v181\n"
    "v_mfma_f32_16x16x32_bf16 v[200:203], a[176:179], a[80:83], v[200:203]\n"
    "buffer_load_dwordx4 v248, s[8:11], s41 offen offset:3072 sc1 lds\n"
    "v_mfma_f32_16x16x32_bf16 v[200:203], a[180:183], a[84:87], v[200:203]\n"
    "ds_read_b64_tr_b16 a[170:171], v245 offset:18432\n"
    "v_mfma_f32_16x16x32_bf16 v[192:195], a[180:183], a[52:55], v[192:195]\n"
    "s_add_u32 s41, s41, 0x8000\n"
    "ds_read2st64_b32 v[176:177], v249 offset0:16 offset1:17\n"
    "v_mfma_f32_16x16x32_bf16 v[192:195], a[176:179], a[48:51], v[192:195]\n"
    "buffer_load_dwordx4 a[204:207], v235, s[20:23], 0 offen sc1\n"
    "v_mfma_f32_16x16x32_bf16 v[184:187], a[176:179], a[16:19], v[184:187]\n"
    "ds_read_b64_tr_b16 a[172:173], v245 offset:49152\n"
    "v_mfma_f32_16x16x32_bf16 v[184:187], a[180:183], a[20:23], v[184:187]\n"
    "s_waitcnt lgkmcnt(10)\n"
    "ds_read2st64_b32 v[178:179], v249 offset0:18 offset1:19\n"
    "v_mfma_f32_16x16x32_bf16 v[184:187], a[184:187], a[24:27], v[184:187]\n"
    "ds_read_b64_tr_b16 a[176:177], v246 offset:16384\n"
    "v_mfma_f32_16x16x32_bf16 v[184:187], a[188:191], a[28:31], v[184:187]\n"
    "ds_read_b64_tr_b16 a[174:175], v245 offset:51200\n"
    "v_mfma_f32_16x16x32_bf16 v[192:195], a[188:191], a[60:63], v[192:195]\n"
    "v_exp_f32_e32 v182, v182\n"
    "v_mfma_f32_16x16x32_bf16 v[192:195], a[184:187], a[56:59], v[192:195]\n"
    "v_exp_f32_e32 v183, v183\n"
    "v_mfma_f32_16x16x32_bf16 v[200:203], a[184:187], a[88:91], v[200:203]\n"
    "ds_read_b64_tr_b16 a[178:179], v246 offset:18432\n"
    "v_cvt_pk_bf16_f32 v180, v180, v181\n"
    "v_mfma_f32_16x16x32_bf16 v[200:203], a[188:191], a[92:95], v[200:203]\n"
    "v_exp_f32_e32 v188, v188\n"
    "v_mfma_f32_16x16x32_bf16 v[208:211], a[188:191], a[124:127], v[208:211]\n"
    "v_exp_f32_e32 v189, v189\n"
    "v_mfma_f32_16x16x32_bf16 v[208:211], a[184:187], a[120:123], v[208:211]\n"
    "v_cvt_pk_bf16_f32 v181, v182, v183\n"
    "v_mfma_f32_16x16x32_bf16 v[216:219], a[184:187], a[152:155], v[216:219]\n"
    "ds_read_b64_tr_b16 a[180:181], v246 offset:49152\n"
    "v_mfma_f32_16x16x32_bf16 v[216:219], a[188:191], a[156:159], v[216:219]\n"
    "s_waitcnt vmcnt(20)\n"
    "ds_read_b64_tr_b16 a[182:183], v246 offset:51200\n"
    "v_smfmac_f32_16x16x64_bf16 v[224:227], a[232:235], a[208:215], v238\n"
    "s_waitcnt lgkmcnt(5)\n"
    "v_mad_u32_u16 v234, v230, s90, v239 op_sel:[0,0,0,0]\n"
    "v_mfma_f32_16x16x32_bf16 a[236:239], a[160:163], v[140:143], a[236:239]\n"
    "s_bitcmp1_b32 s86, 2\n"
    "buffer_load_dwordx4 a[208:211], v234, s[20:23], 0 offen sc1\n"
    "v_mad_u32_u16 v235, v230, s90, v239 op_sel:[1,0,0,0]\n"
    "v_mfma_f32_16x16x32_bf16 a[236:239], a[164:167], v[144:147], a[236:239]\n"
    "s_cselect_b64 exec, -1, 0\n"
    "v_cvt_pk_bf16_f32 v236, v224, v225\n"
    "v_cvt_pk_bf16_f32 v237, v226, v227\n"
    "v_mfma_f32_16x16x32_bf16 a[240:243], a[164:167], v[152:155], a[240:243]\n"
    "s_cselect_b32 s88, 512, 0\n"
    "v_mov_b64_e32 v[224:225], 0\n"
    "v_mov_b64_e32 v[226:227], 0\n"
    "v_mfma_f32_16x16x32_bf16 a[240:243], a[160:163], v[148:151], a[240:243]\n"
    "buffer_store_dwordx2 v[236:237], v252, s[8:11], s87 offen sc1\n"
    "v_mfma_f32_16x16x32_bf16 a[244:247], a[160:163], v[156:159], a[244:247]\n"
    "s_mov_b64 exec, -1\n"
    "ds_read_b64_tr_b16 a[184:185], v247 offset:16384\n"
    "v_mfma_f32_16x16x32_bf16 a[244:247], a[164:167], v[160:163], a[244:247]\n"
    "s_add_u32 s87, s87, s88\n"
    "ds_read_b64_tr_b16 a[186:187], v247 offset:18432\n"
    "v_mfma_f32_16x16x32_bf16 a[248:251], a[164:167], v[168:171], a[248:251]\n"
    "buffer_load_dwordx4 a[212:215], v235, s[20:23], 0 offen sc1\n"
    "v_mfma_f32_16x16x32_bf16 a[248:251], a[160:163], v[164:167], a[248:251]\n"
    "ds_read_b64_tr_b16 a[188:189], v247 offset:49152\n"
    "v_mfma_f32_16x16x32_bf16 a[252:255], a[160:163], v[172:175], a[252:255]\n"
    "v_exp_f32_e32 v190, v190\n"
    "v_mfma_f32_16x16x32_bf16 a[252:255], a[164:167], v[176:179], a[252:255]\n"
    "s_waitcnt lgkmcnt(6)\n"
    "v_exp_f32_e32 v191, v191\n"
    "v_mfma_f32_16x16x32_bf16 v[16:19], a[172:175], v[176:179], v[16:19]\n"
    "ds_read_b64_tr_b16 a[190:191], v247 offset:51200\n"
    "v_cvt_pk_bf16_f32 v188, v188, v189\n"
    "v_mfma_f32_16x16x32_bf16 v[16:19], a[168:171], v[172:175], v[16:19]\n"
    "ds_read_b64_tr_b16 a[160:161], v244 offset:20480\n"
    "v_mfma_f32_16x16x32_bf16 v[12:15], a[168:171], v[164:167], v[12:15]\n"
    "v_exp_f32_e32 v196, v196\n"
    "v_mfma_f32_16x16x32_bf16 v[12:15], a[172:175], v[168:171], v[12:15]\n"
    "v_exp_f32_e32 v197, v197\n"
    "v_mfma_f32_16x16x32_bf16 v[8:11], a[172:175], v[160:163], v[8:11]\n"
    "ds_read_b64_tr_b16 a[162:163], v244 offset:22528\n"
    "v_cvt_pk_bf16_f32 v189, v190, v191\n"
    "v_mfma_f32_16x16x32_bf16 v[8:11], a[168:171], v[156:159], v[8:11]\n"
    "v_exp_f32_e32 v198, v198\n"
    "v_mfma_f32_16x16x32_bf16 v[4:7], a[168:171], v[148:151], v[4:7]\n"
    "v_exp_f32_e32 v199, v199\n"
    "v_mfma_f32_16x16x32_bf16 v[4:7], a[172:175], v[152:155], v[4:7]\n"
    "v_cvt_pk_bf16_f32 v196, v196, v197\n"
    "v_mfma_f32_16x16x32_bf16 v[0:3], a[172:175], v[144:147], v[0:3]\n"
    "ds_read_b64_tr_b16 a[164:165], v244 offset:53248\n"
    "v_mfma_f32_16x16x32_bf16 v[0:3], a[168:171], v[140:143], v[0:3]\n"
    "s_waitcnt lgkmcnt(7)\n"
    "v_exp_f32_e32 v204, v204\n"
    "v_mfma_f32_16x16x32_bf16 v[20:23], a[176:179], v[140:143], v[20:23]\n"
    "ds_read_b64_tr_b16 a[168:169], v245 offset:20480\n"
    "v_mfma_f32_16x16x32_bf16 v[20:23], a[180:183], v[144:147], v[20:23]\n"
    "ds_read_b64_tr_b16 a[166:167], v244 offset:55296\n"
    "v_mfma_f32_16x16x32_bf16 v[24:27], a[180:183], v[152:155], v[24:27]\n"
    "v_exp_f32_e32 v205, v205\n"
    "v_mfma_f32_16x16x32_bf16 v[24:27], a[176:179], v[148:151], v[24:27]\n"
    "v_cvt_pk_bf16_f32 v197, v198, v199\n"
    "v_mfma_f32_16x16x32_bf16 v[28:31], a[176:179], v[156:159], v[28:31]\n"
    "ds_read_b64_tr_b16 a[170:171], v245 offset:22528\n"
    "v_mfma_f32_16x16x32_bf16 v[28:31], a[180:183], v[160:163], v[28:31]\n"
    "v_exp_f32_e32 v206, v206\n"
    "v_mfma_f32_16x16x32_bf16 v[32:35], a[180:183], v[168:171], v[32:35]\n"
    "v_exp_f32_e32 v207, v207\n"
    "v_mfma_f32_16x16x32_bf16 v[32:35], a[176:179], v[164:167], v[32:35]\n"
    "v_cvt_pk_bf16_f32 v204, v204, v205\n"
    "v_mfma_f32_16x16x32_bf16 v[36:39], a[176:179], v[172:175], v[36:39]\n"
    "ds_read_b64_tr_b16 a[172:173], v245 offset:53248\n"
    "v_mfma_f32_16x16x32_bf16 v[36:39], a[180:183], v[176:179], v[36:39]\n"
    "s_waitcnt vmcnt(20)\n"
    "ds_read_b64_tr_b16 a[174:175], v245 offset:55296\n"
    "v_smfmac_f32_16x16x64_bf16 v[224:227], a[232:235], a[216:223], v238\n"
    "s_waitcnt lgkmcnt(8)\n"
    "v_mad_u32_u16 v234, v231, s90, v239 op_sel:[0,0,0,0]\n"
    "v_mfma_f32_16x16x32_bf16 v[56:59], a[188:191], v[176:179], v[56:59]\n"
    "s_bitcmp1_b32 s86, 3\n"
    "buffer_load_dwordx4 a[216:219], v234, s[20:23], 0 offen sc1\n"
    "v_mad_u32_u16 v235, v231, s90, v239 op_sel:[1,0,0,0]\n"
    "v_mfma_f32_16x16x32_bf16 v[56:59], a[184:187], v[172:175], v[56:59]\n"
    "s_cselect_b64 exec, -1, 0\n"
    "v_cvt_pk_bf16_f32 v236, v224, v225\n"
    "v_cvt_pk_bf16_f32 v237, v226, v227\n"
    "v_mfma_f32_16x16x32_bf16 v[52:55], a[184:187], v[164:167], v[52:55]\n"
    "s_cselect_b32 s88, 512, 0\n"
    "v_mov_b64_e32 v[224:225], 0\n"
    "v_mov_b64_e32 v[226:227], 0\n"
    "v_mfma_f32_16x16x32_bf16 v[52:55], a[188:191], v[168:171], v[52:55]\n"
    "buffer_store_dwordx2 v[236:237], v252, s[8:11], s87 offen sc1\n"
    "v_mfma_f32_16x16x32_bf16 v[48:51], a[188:191], v[160:163], v[48:51]\n"
    "s_mov_b64 exec, -1\n"
    "ds_read_b64_tr_b16 a[176:177], v246 offset:20480\n"
    "v_mfma_f32_16x16x32_bf16 v[48:51], a[184:187], v[156:159], v[48:51]\n"
    "s_add_u32 s87, s87, s88\n"
    "ds_read_b64_tr_b16 a[178:179], v246 offset:22528\n"
    "v_mfma_f32_16x16x32_bf16 v[44:47], a[184:187], v[148:151], v[44:47]\n"
    "buffer_load_dwordx4 a[220:223], v235, s[20:23], 0 offen sc1\n"
    "v_mfma_f32_16x16x32_bf16 v[44:47], a[188:191], v[152:155], v[44:47]\n"
    "ds_read_b64_tr_b16 a[180:181], v246 offset:53248\n"
    "v_mfma_f32_16x16x32_bf16 v[40:43], a[188:191], v[144:147], v[40:43]\n"
    "v_exp_f32_e32 v212, v212\n"
    "v_mfma_f32_16x16x32_bf16 v[40:43], a[184:187], v[140:143], v[40:43]\n"
    "s_waitcnt lgkmcnt(6)\n"
    "v_exp_f32_e32 v213, v213\n"
    "v_mfma_f32_16x16x32_bf16 v[60:63], a[160:163], v[140:143], v[60:63]\n"
    "ds_read_b64_tr_b16 a[182:183], v246 offset:55296\n"
    "v_cvt_pk_bf16_f32 v205, v206, v207\n"
    "v_mfma_f32_16x16x32_bf16 v[60:63], a[164:167], v[144:147], v[60:63]\n"
    "ds_read_b64_tr_b16 a[184:185], v247 offset:20480\n"
    "v_mfma_f32_16x16x32_bf16 v[64:67], a[164:167], v[152:155], v[64:67]\n"
    "ds_read_b64_tr_b16 a[186:187], v247 offset:22528\n"
    "v_mfma_f32_16x16x32_bf16 v[64:67], a[160:163], v[148:151], v[64:67]\n"
    "v_exp_f32_e32 v214, v214\n"
    "v_mfma_f32_16x16x32_bf16 v[68:71], a[160:163], v[156:159], v[68:71]\n"
    "v_exp_f32_e32 v215, v215\n"
    "v_mfma_f32_16x16x32_bf16 v[68:71], a[164:167], v[160:163], v[68:71]\n"
    "ds_read_b64_tr_b16 a[188:189], v247 offset:53248\n"
    "v_cvt_pk_bf16_f32 v212, v212, v213\n"
    "v_mfma_f32_16x16x32_bf16 v[72:75], a[164:167], v[168:171], v[72:75]\n"
    "ds_read_b64_tr_b16 a[190:191], v247 offset:55296\n"
    "v_cvt_pk_bf16_f32 v213, v214, v215\n"
    "v_mfma_f32_16x16x32_bf16 v[72:75], a[160:163], v[164:167], v[72:75]\n"
    "s_waitcnt vmcnt(30)\n"
    "v_mfma_f32_16x16x32_bf16 v[76:79], a[160:163], v[172:175], v[76:79]\n"
    "s_barrier\n"
    "v_mfma_f32_16x16x32_bf16 v[76:79], a[164:167], v[176:179], v[76:79]\n"
    "s_waitcnt lgkmcnt(8)\n"
    "s_mov_b32 m0, s42\n"
    "v_mfma_f32_16x16x32_bf16 v[96:99], a[172:175], v[176:179], v[96:99]\n"
    "v_exp_f32_e32 v184, v184\n"
    "v_mfma_f32_16x16x32_bf16 v[96:99], a[168:171], v[172:175], v[96:99]\n"
    "v_exp_f32_e32 v185, v185\n"
    "v_mfma_f32_16x16x32_bf16 v[92:95], a[168:171], v[164:167], v[92:95]\n"
    "v_exp_f32_e32 v186, v186\n"
    "v_mfma_f32_16x16x32_bf16 v[92:95], a[172:175], v[168:171], v[92:95]\n"
    "ds_read_b128 a[160:163], v240 offset:0\n"
    "ds_write_addtid_b32 v180 offset:0\n"
    "v_mfma_f32_16x16x32_bf16 v[88:91], a[172:175], v[160:163], v[88:91]\n"
    "v_exp_f32_e32 v187, v187\n"
    "v_mfma_f32_16x16x32_bf16 v[88:91], a[168:171], v[156:159], v[88:91]\n"
    "ds_write_addtid_b32 v181 offset:256\n"
    "v_cvt_pk_bf16_f32 v182, v184, v185\n"
    "v_mfma_f32_16x16x32_bf16 v[84:87], a[168:171], v[148:151], v[84:87]\n"
    "v_exp_f32_e32 v192, v192\n"
    "v_mfma_f32_16x16x32_bf16 v[84:87], a[172:175], v[152:155], v[84:87]\n"
    "v_exp_f32_e32 v193, v193\n"
    "v_mfma_f32_16x16x32_bf16 v[80:83], a[172:175], v[144:147], v[80:83]\n"
    "ds_write_addtid_b32 v188 offset:1024\n"
    "v_cvt_pk_bf16_f32 v183, v186, v187\n"
    "v_mfma_f32_16x16x32_bf16 v[80:83], a[168:171], v[140:143], v[80:83]\n"
    "s_waitcnt vmcnt(20)\n"
    "ds_read_b128 a[164:167], v241 offset:0\n"
    "ds_write_addtid_b32 v189 offset:1280\n"
    "v_smfmac_f32_16x16x64_bf16 v[224:227], a[232:235], a[224:231], v238\n"
    "s_waitcnt lgkmcnt(10)\n"
    "ds_write_addtid_b32 v196 offset:2048\n"
    "v_mad_u32_u16 v234, v232, s90, v239 op_sel:[0,0,0,0]\n"
    "v_mfma_f32_16x16x32_bf16 v[100:103], a[176:179], v[140:143], v[100:103]\n"
    "s_bitcmp1_b32 s86, 4\n"
    "buffer_load_dwordx4 a[224:227], v234, s[20:23], 0 offen sc1\n"
    "v_mad_u32_u16 v235, v232, s90, v239 op_sel:[1,0,0,0]\n"
    "v_mfma_f32_16x16x32_bf16 v[100:103], a[180:183], v[144:147], v[100:103]\n"
    "s_cselect_b64 exec, -1, 0\n"
    "v_cvt_pk_bf16_f32 v236, v224, v225\n"
    "v_cvt_pk_bf16_f32 v237, v226, v227\n"
    "v_mfma_f32_16x16x32_bf16 v[104:107], a[180:183], v[152:155], v[104:107]\n"
    "s_cselect_b32 s88, 512, 0\n"
    "v_mov_b64_e32 v[224:225], 0\n"
    "v_mov_b64_e32 v[226:227], 0\n"
    "v_mfma_f32_16x16x32_bf16 v[104:107], a[176:179], v[148:151], v[104:107]\n"
    "buffer_store_dwordx2 v[236:237], v252, s[8:11], s87 offen sc1\n"
    "v_mfma_f32_16x16x32_bf16 v[108:111], a[176:179], v[156:159], v[108:111]\n"
    "s_mov_b64 exec, -1\n"
    "ds_read_b128 a[168:171], v240 offset:4096\n"
    "ds_write_addtid_b32 v197 offset:2304\n"
    "v_mfma_f32_16x16x32_bf16 v[108:111], a[180:183], v[160:163], v[108:111]\n"
    "s_add_u32 s87, s87, s88\n"
    "v_exp_f32_e32 v194, v194\n"
    "v_mfma_f32_16x16x32_bf16 v[112:115], a[180:183], v[168:171], v[112:115]\n"
    "buffer_load_dwordx4 a[228:231], v235, s[20:23], 0 offen sc1\n"
    "ds_write_addtid_b32 v204 offset:3072\n"
    "v_mfma_f32_16x16x32_bf16 v[112:115], a[176:179], v[164:167], v[112:115]\n"
    "ds_write_addtid_b32 v205 offset:3328\n"
    "v_mfma_f32_16x16x32_bf16 v[116:119], a[176:179], v[172:175], v[116:119]\n"
    "v_exp_f32_e32 v195, v195\n"
    "v_mfma_f32_16x16x32_bf16 v[116:119], a[180:183], v[176:179], v[116:119]\n"
    "s_waitcnt lgkmcnt(11)\n"
    "ds_read_b128 a[172:175], v241 offset:4096\n"
    "v_cvt_pk_bf16_f32 v190, v192, v193\n"
    "v_mfma_f32_16x16x32_bf16 v[136:139], a[188:191], v[176:179], v[136:139]\n"
    "v_exp_f32_e32 v200, v200\n"
    "v_mfma_f32_16x16x32_bf16 v[136:139], a[184:187], v[172:175], v[136:139]\n"
    "v_exp_f32_e32 v201, v201\n"
    "v_mfma_f32_16x16x32_bf16 v[132:135], a[184:187], v[164:167], v[132:135]\n"
    "ds_read_b128 a[176:179], v242 offset:0\n"
    "v_cvt_pk_bf16_f32 v191, v194, v195\n"
    "v_mfma_f32_16x16x32_bf16 v[132:135], a[188:191], v[168:171], v[132:135]\n"
    "v_exp_f32_e32 v202, v202\n"
    "v_mfma_f32_16x16x32_bf16 v[128:131], a[188:191], v[160:163], v[128:131]\n"
    "v_exp_f32_e32 v203, v203\n"
    "v_mfma_f32_16x16x32_bf16 v[128:131], a[184:187], v[156:159], v[128:131]\n"
    "ds_write_addtid_b32 v212 offset:4096\n"
    "v_cvt_pk_bf16_f32 v198, v200, v201\n"
    "v_mfma_f32_16x16x32_bf16 v[124:127], a[184:187], v[148:151], v[124:127]\n"
    "v_exp_f32_e32 v208, v208\n"
    "v_mfma_f32_16x16x32_bf16 v[124:127], a[188:191], v[152:155], v[124:127]\n"
    "ds_read_b128 a[180:183], v243 offset:0\n"
    "ds_write_addtid_b32 v213 offset:4352\n"
    "v_mfma_f32_16x16x32_bf16 v[120:123], a[188:191], v[144:147], v[120:123]\n"
    "v_exp_f32_e32 v209, v209\n"
    "v_mfma_f32_16x16x32_bf16 v[120:123], a[184:187], v[140:143], v[120:123]\n"
    "s_waitcnt lgkmcnt(11)\n"
    "ds_write_addtid_b32 v182 offset:512\n"
    "v_cvt_pk_bf16_f32 v199, v202, v203\n"
    "v_mfma_f32_16x16x32_bf16 v[172:175], a[160:163], a[128:131], v[220:223]\n"
    "ds_write_addtid_b32 v183 offset:768\n"
    "v_mfma_f32_16x16x32_bf16 v[172:175], a[164:167], a[132:135], v[172:175]\n"
    "v_exp_f32_e32 v210, v210\n"
    "v_mfma_f32_16x16x32_bf16 v[164:167], a[164:167], a[100:103], v[220:223]\n"
    "ds_write_addtid_b32 v190 offset:1536\n"
    "v_mfma_f32_16x16x32_bf16 v[164:167], a[160:163], a[96:99], v[164:167]\n"
    "ds_read_b128 a[184:187], v242 offset:4096\n"
    "v_mfma_f32_16x16x32_bf16 v[156:159], a[160:163], a[64:67], v[220:223]\n"
    "v_exp_f32_e32 v211, v211\n"
    "v_mfma_f32_16x16x32_bf16 v[156:159], a[164:167], a[68:71], v[156:159]\n"
    "ds_write_addtid_b32 v191 offset:1792\n"
    "v_cvt_pk_bf16_f32 v206, v208, v209\n"
    "v_mfma_f32_16x16x32_bf16 v[148:151], a[164:167], a[36:39], v[220:223]\n"
    "v_exp_f32_e32 v216, v216\n"
    "v_mfma_f32_16x16x32_bf16 v[148:151], a[160:163], a[32:35], v[148:151]\n"
    "v_exp_f32_e32 v217, v217\n"
    "v_mfma_f32_16x16x32_bf16 v[140:143], a[160:163], a[0:3], v[220:223]\n"
    "ds_write_addtid_b32 v198 offset:2560\n"
    "v_cvt_pk_bf16_f32 v207, v210, v211\n"
    "v_mfma_f32_16x16x32_bf16 v[140:143], a[164:167], a[4:7], v[140:143]\n"
    "s_waitcnt vmcnt(20)\n"
    "v_exp_f32_e32 v218, v218\n"
    "v_smfmac_f32_16x16x64_bf16 v[224:227], a[232:235], a[192:199], v238\n"
    "s_waitcnt lgkmcnt(10)\n"
    "ds_read_b128 a[188:191], v243 offset:4096\n"
    "v_mfma_f32_16x16x32_bf16 v[140:143], a[168:171], a[8:11], v[140:143]\n"
    "s_bitcmp1_b32 s86, 5\n"
    "ds_read_b128 a[160:163], v240 offset:2048\n"
    "v_mfma_f32_16x16x32_bf16 v[140:143], a[172:175], a[12:15], v[140:143]\n"
    "s_cselect_b64 exec, -1, 0\n"
    "v_cvt_pk_bf16_f32 v236, v224, v225\n"
    "v_cvt_pk_bf16_f32 v237, v226, v227\n"
    "v_mfma_f32_16x16x32_bf16 v[148:151], a[172:175], a[44:47], v[148:151]\n"
    ".long 0xbedf00ff\n"
    ".Lat_w6:\n"
    "s_setpc_b64 s[2:3]\n"
    "s_cselect_b32 s88, 512, 0\n"
    "v_mov_b64_e32 v[224:225], 0\n"
    "v_mov_b64_e32 v[226:227], 0\n"
    "v_mfma_f32_16x16x32_bf16 v[148:151], a[168:171], a[40:43], v[148:151]\n"
    "buffer_store_dwordx2 v[236:237], v252, s[8:11], s87 offen sc1\n"
    "v_mfma_f32_16x16x32_bf16 v[156:159], a[168:171], a[72:75], v[156:159]\n"
    "s_mov_b64 exec, -1\n"
    "ds_read2_b32 v[228:229], v251 offset0:10 offset1:12\n"
    "v_mfma_f32_16x16x32_bf16 v[156:159], a[172:175], a[76:79], v[156:159]\n"
    "s_add_u32 s87, s87, s88\n"
    "ds_read2_b32 v[230:231], v251 offset0:14 offset1:16\n"
    "v_mfma_f32_16x16x32_bf16 v[164:167], a[172:175], a[108:111], v[164:167]\n"
    "ds_read_b32 v232, v251 offset:72\n"
    "v_mfma_f32_16x16x32_bf16 v[164:167], a[168:171], a[104:107], v[164:167]\n"
    "v_exp_f32_e32 v219, v219\n"
    "v_mfma_f32_16x16x32_bf16 v[172:175], a[168:171], a[136:139], v[172:175]\n"
    "ds_write_addtid_b32 v199 offset:2816\n"
    "v_cvt_pk_bf16_f32 v214, v216, v217\n"
    "v_mfma_f32_16x16x32_bf16 v[172:175], a[172:175], a[140:143], v[172:175]\n"
    "s_waitcnt lgkmcnt(13)\n"
    "ds_read_b128 a[164:167], v241 offset:2048\n"
    "v_mfma_f32_16x16x32_bf16 v[172:175], a[176:179], a[144:147], v[172:175]\n"
    "ds_write_addtid_b32 v206 offset:3584\n"
    "v_cvt_pk_bf16_f32 v215, v218, v219\n"
    "v_mfma_f32_16x16x32_bf16 v[172:175], a[180:183], a[148:151], v[172:175]\n"
    "ds_read_b128 a[168:171], v240 offset:6144\n"
    "v_mfma_f32_16x16x32_bf16 v[164:167], a[180:183], a[116:119], v[164:167]\n"
    "s_waitcnt lgkmcnt(6)\n"
    "ds_write_addtid_b32 v207 offset:3840\n"
    "v_mad_u32_u16 v234, v228, s90, v239 op_sel:[0,0,0,0]\n"
    "v_mfma_f32_16x16x32_bf16 v[164:167], a[176:179], a[112:115], v[164:167]\n"
    "buffer_load_dwordx4 a[192:195], v234, s[20:23], 0 offen sc1\n"
    "v_mad_u32_u16 v235, v228, s90, v239 op_sel:[1,0,0,0]\n"
    "v_mfma_f32_16x16x32_bf16 v[156:159], a[176:179], a[80:83], v[156:159]\n"
    "ds_write_addtid_b32 v214 offset:4608\n"
    "v_mfma_f32_16x16x32_bf16 v[156:159], a[180:183], a[84:87], v[156:159]\n"
    "ds_write_addtid_b32 v215 offset:4864\n"
    "v_mfma_f32_16x16x32_bf16 v[148:151], a[180:183], a[52:55], v[148:151]\n"
    "buffer_load_dwordx4 a[196:199], v235, s[20:23], 0 offen sc1\n"
    "v_mfma_f32_16x16x32_bf16 v[148:151], a[176:179], a[48:51], v[148:151]\n"
    "v_mfma_f32_16x16x32_bf16 v[140:143], a[176:179], a[16:19], v[140:143]\n"
    "s_waitcnt lgkmcnt(0)\n"
    "v_mfma_f32_16x16x32_bf16 v[140:143], a[180:183], a[20:23], v[140:143]\n"
    "s_barrier\n"
    "v_mfma_f32_16x16x32_bf16 v[140:143], a[184:187], a[24:27], v[140:143]\n"
    "s_add_u32 m0, s43, 16384\n"
    "ds_read_b128 a[172:175], v241 offset:6144\n"
    "v_mfma_f32_16x16x32_bf16 v[140:143], a[188:191], a[28:31], v[140:143]\n"
    "ds_read2st64_b32 v[184:185], v249 offset0:0 offset1:1\n"
    "v_mfma_f32_16x16x32_bf16 v[148:151], a[188:191], a[60:63], v[148:151]\n"
    "buffer_load_dwordx4 v248, s[8:11], s40 offen offset:0 sc1 lds\n"
    "v_mfma_f32_16x16x32_bf16 v[148:151], a[184:187], a[56:59], v[148:151]\n"
    "ds_read_b128 a[176:179], v242 offset:2048\n"
    "v_mfma_f32_16x16x32_bf16 v[156:159], a[184:187], a[88:91], v[156:159]\n"
    "v_mfma_f32_16x16x32_bf16 v[156:159], a[188:191], a[92:95], v[156:159]\n"
    "v_mfma_f32_16x16x32_bf16 v[164:167], a[188:191], a[124:127], v[164:167]\n"
    "buffer_load_dwordx4 v248, s[8:11], s40 offen offset:1024 sc1 lds\n"
    "v_mfma_f32_16x16x32_bf16 v[164:167], a[184:187], a[120:123], v[164:167]\n"
    "ds_read2st64_b32 v[186:187], v249 offset0:2 offset1:3\n"
    "v_mfma_f32_16x16x32_bf16 v[172:175], a[184:187], a[152:155], v[172:175]\n"
    "v_mfma_f32_16x16x32_bf16 v[172:175], a[188:191], a[156:159], v[172:175]\n"
    "ds_read_b128 a[180:183], v243 offset:2048\n"
    "v_mfma_f32_16x16x32_bf16 v[176:179], a[160:163], a[128:131], v[220:223]\n"
    "buffer_load_dwordx4 v248, s[8:11], s40 offen offset:2048 sc1 lds\n"
    "v_mfma_f32_16x16x32_bf16 v[176:179], a[164:167], a[132:135], v[176:179]\n"
    "ds_read2st64_b32 v[192:193], v249 offset0:4 offset1:5\n"
    "v_mfma_f32_16x16x32_bf16 v[168:171], a[164:167], a[100:103], v[220:223]\n"
    "v_exp_f32_e32 v140, v140\n"
    "v_mfma_f32_16x16x32_bf16 v[168:171], a[160:163], a[96:99], v[168:171]\n"
    "ds_read_b128 a[184:187], v242 offset:6144\n"
    "v_mfma_f32_16x16x32_bf16 v[160:163], a[160:163], a[64:67], v[220:223]\n"
    "buffer_load_dwordx4 v248, s[8:11], s40 offen offset:3072 sc1 lds\n"
    "v_mfma_f32_16x16x32_bf16 v[160:163], a[164:167], a[68:71], v[160:163]\n"
    "s_add_u32 m0, s43, 20480\n"
    "ds_read2st64_b32 v[194:195], v249 offset0:6 offset1:7\n"
    "v_mfma_f32_16x16x32_bf16 v[152:155], a[164:167], a[36:39], v[220:223]\n"
    "s_add_u32 s40, s40, 0x8000\n"
    "ds_read2st64_b32 v[200:201], v249 offset0:8 offset1:9\n"
    "v_mfma_f32_16x16x32_bf16 v[152:155], a[160:163], a[32:35], v[152:155]\n"
    "ds_read2st64_b32 v[202:203], v249 offset0:10 offset1:11\n"
    "v_mfma_f32_16x16x32_bf16 v[144:147], a[160:163], a[0:3], v[220:223]\n"
    "buffer_load_dwordx4 v248, s[8:11], s41 offen offset:0 sc1 lds\n"
    "v_mfma_f32_16x16x32_bf16 v[144:147], a[164:167], a[4:7], v[144:147]\n"
    "s_waitcnt vmcnt(17)\n"
    "ds_read_b128 a[188:191], v243 offset:6144\n"
    "v_smfmac_f32_16x16x64_bf16 v[224:227], a[232:235], a[200:207], v238\n"
    "s_waitcnt lgkmcnt(10)\n"
    "v_mad_u32_u16 v234, v229, s90, v239 op_sel:[0,0,0,0]\n"
    "v_mfma_f32_16x16x32_bf16 v[144:147], a[168:171], a[8:11], v[144:147]\n"
    "s_bitcmp1_b32 s86, 6\n"
    "buffer_load_dwordx4 a[200:203], v234, s[20:23], 0 offen sc1\n"
    "v_mad_u32_u16 v235, v229, s90, v239 op_sel:[1,0,0,0]\n"
    "v_mfma_f32_16x16x32_bf16 v[144:147], a[172:175], a[12:15], v[144:147]\n"
    "s_cselect_b64 exec, -1, 0\n"
    "v_cvt_pk_bf16_f32 v236, v224, v225\n"
    "v_cvt_pk_bf16_f32 v237, v226, v227\n"
    "v_mfma_f32_16x16x32_bf16 v[152:155], a[172:175], a[44:47], v[152:155]\n"
    "s_cselect_b32 s88, 512, 0\n"
    "v_mov_b64_e32 v[224:225], 0\n"
    "v_mov_b64_e32 v[226:227], 0\n"
    "v_mfma_f32_16x16x32_bf16 v[152:155], a[168:171], a[40:43], v[152:155]\n"
    "buffer_store_dwordx2 v[236:237], v252, s[8:11], s87 offen sc1\n"
    "v_mfma_f32_16x16x32_bf16 v[160:163], a[168:171], a[72:75], v[160:163]\n"
    "s_mov_b64 exec, -1\n"
    "ds_read_b64_tr_b16 a[160:161], v244 offset:24576\n"
    "v_mfma_f32_16x16x32_bf16 v[160:163], a[172:175], a[76:79], v[160:163]\n"
    "s_add_u32 s87, s87, s88\n"
    "ds_read_b64_tr_b16 a[162:163], v244 offset:26624\n"
    "v_mfma_f32_16x16x32_bf16 v[168:171], a[172:175], a[108:111], v[168:171]\n"
    "buffer_load_dwordx4 v248, s[8:11], s41 offen offset:1024 sc1 lds\n"
    "v_mfma_f32_16x16x32_bf16 v[168:171], a[168:171], a[104:107], v[168:171]\n"
    "ds_read_b64_tr_b16 a[164:165], v244 offset:57344\n"
    "v_mfma_f32_16x16x32_bf16 v[176:179], a[168:171], a[136:139], v[176:179]\n"
    "ds_read2st64_b32 v[208:209], v249 offset0:12 offset1:13\n"
    "v_mfma_f32_16x16x32_bf16 v[176:179], a[172:175], a[140:143], v[176:179]\n"
    "s_waitcnt lgkmcnt(10)\n"
    "ds_read2st64_b32 v[210:211], v249 offset0:14 offset1:15\n"
    "v_mfma_f32_16x16x32_bf16 v[176:179], a[176:179], a[144:147], v[176:179]\n"
    "buffer_load_dwordx4 v248, s[8:11], s41 offen offset:2048 sc1 lds\n"
    "v_mfma_f32_16x16x32_bf16 v[176:179], a[180:183], a[148:151], v[176:179]\n"
    "ds_read_b64_tr_b16 a[166:167], v244 offset:59392\n"
    "v_mfma_f32_16x16x32_bf16 v[168:171], a[180:183], a[116:119], v[168:171]\n"
    "ds_read_b64_tr_b16 a[168:169], v245 offset:24576\n"
    "v_mfma_f32_16x16x32_bf16 v[168:171], a[176:179], a[112:115], v[168:171]\n"
    "v_exp_f32_e32 v141, v141\n"
    "v_mfma_f32_16x16x32_bf16 v[160:163], a[176:179], a[80:83], v[160:163]\n"
    "buffer_load_dwordx4 v248, s[8:11], s41 offen offset:3072 sc1 lds\n"
    "v_mfma_f32_16x16x32_bf16 v[160:163], a[180:183], a[84:87], v[160:163]\n"
    "ds_read_b64_tr_b16 a[170:171], v245 offset:26624\n"
    "v_mfma_f32_16x16x32_bf16 v[152:155], a[180:183], a[52:55], v[152:155]\n"
    "s_add_u32 s41, s41, 0x8000\n"
    "ds_read2st64_b32 v[216:217], v249 offset0:16 offset1:17\n"
    "v_mfma_f32_16x16x32_bf16 v[152:155], a[176:179], a[48:51], v[152:155]\n"
    "buffer_load_dwordx4 a[204:207], v235, s[20:23], 0 offen sc1\n"
    "v_mfma_f32_16x16x32_bf16 v[144:147], a[176:179], a[16:19], v[144:147]\n"
    "ds_read_b64_tr_b16 a[172:173], v245 offset:57344\n"
    "v_mfma_f32_16x16x32_bf16 v[144:147], a[180:183], a[20:23], v[144:147]\n"
    "s_waitcnt lgkmcnt(10)\n"
    "ds_read2st64_b32 v[218:219], v249 offset0:18 offset1:19\n"
    "v_mfma_f32_16x16x32_bf16 v[144:147], a[184:187], a[24:27], v[144:147]\n"
    "ds_read_b64_tr_b16 a[176:177], v246 offset:24576\n"
    "v_mfma_f32_16x16x32_bf16 v[144:147], a[188:191], a[28:31], v[144:147]\n"
    "ds_read_b64_tr_b16 a[174:175], v245 offset:59392\n"
    "v_mfma_f32_16x16x32_bf16 v[152:155], a[188:191], a[60:63], v[152:155]\n"
    "v_exp_f32_e32 v142, v142\n"
    "v_mfma_f32_16x16x32_bf16 v[152:155], a[184:187], a[56:59], v[152:155]\n"
    "v_exp_f32_e32 v143, v143\n"
    "v_mfma_f32_16x16x32_bf16 v[160:163], a[184:187], a[88:91], v[160:163]\n"
    "ds_read_b64_tr_b16 a[178:179], v246 offset:26624\n"
    "v_cvt_pk_bf16_f32 v140, v140, v141\n"
    "v_mfma_f32_16x16x32_bf16 v[160:163], a[188:191], a[92:95], v[160:163]\n"
    "v_exp_f32_e32 v148, v148\n"
    "v_mfma_f32_16x16x32_bf16 v[168:171], a[188:191], a[124:127], v[168:171]\n"
    "v_exp_f32_e32 v149, v149\n"
    "v_mfma_f32_16x16x32_bf16 v[168:171], a[184:187], a[120:123], v[168:171]\n"
    "v_cvt_pk_bf16_f32 v141, v142, v143\n"
    "v_mfma_f32_16x16x32_bf16 v[176:179], a[184:187], a[152:155], v[176:179]\n"
    "ds_read_b64_tr_b16 a[180:181], v246 offset:57344\n"
    "v_mfma_f32_16x16x32_bf16 v[176:179], a[188:191], a[156:159], v[176:179]\n"
    "s_waitcnt vmcnt(20)\n"
    "ds_read_b64_tr_b16 a[182:183], v246 offset:59392\n"
    "v_smfmac_f32_16x16x64_bf16 v[224:227], a[232:235], a[208:215], v238\n"
    "s_waitcnt lgkmcnt(5)\n"
    "v_mad_u32_u16 v234, v230, s90, v239 op_sel:[0,0,0,0]\n"
    "v_mfma_f32_16x16x32_bf16 a[236:239], a[160:163], v[180:183], a[236:239]\n"
    "s_bitcmp1_b32 s86, 7\n"
    "buffer_load_dwordx4 a[208:211], v234, s[20:23], 0 offen sc1\n"
    "v_mad_u32_u16 v235, v230, s90, v239 op_sel:[1,0,0,0]\n"
    "v_mfma_f32_16x16x32_bf16 a[236:239], a[164:167], v[184:187], a[236:239]\n"
    "s_cselect_b64 exec, -1, 0\n"
    "v_cvt_pk_bf16_f32 v236, v224, v225\n"
    "v_cvt_pk_bf16_f32 v237, v226, v227\n"
    "v_mfma_f32_16x16x32_bf16 a[240:243], a[164:167], v[192:195], a[240:243]\n"
    "s_cselect_b32 s88, 512, 0\n"
    "v_mov_b64_e32 v[224:225], 0\n"
    "v_mov_b64_e32 v[226:227], 0\n"
    "v_mfma_f32_16x16x32_bf16 a[240:243], a[160:163], v[188:191], a[240:243]\n"
    "buffer_store_dwordx2 v[236:237], v252, s[8:11], s87 offen sc1\n"
    "v_mfma_f32_16x16x32_bf16 a[244:247], a[160:163], v[196:199], a[244:247]\n"
    "s_mov_b64 exec, -1\n"
    "ds_read_b64_tr_b16 a[184:185], v247 offset:24576\n"
    "v_mfma_f32_16x16x32_bf16 a[244:247], a[164:167], v[200:203], a[244:247]\n"
    "s_add_u32 s87, s87, s88\n"
    "ds_read_b64_tr_b16 a[186:187], v247 offset:26624\n"
    "v_mfma_f32_16x16x32_bf16 a[248:251], a[164:167], v[208:211], a[248:251]\n"
    "buffer_load_dwordx4 a[212:215], v235, s[20:23], 0 offen sc1\n"
    "v_mfma_f32_16x16x32_bf16 a[248:251], a[160:163], v[204:207], a[248:251]\n"
    "ds_read_b64_tr_b16 a[188:189], v247 offset:57344\n"
    "v_mfma_f32_16x16x32_bf16 a[252:255], a[160:163], v[212:215], a[252:255]\n"
    "v_exp_f32_e32 v150, v150\n"
    "v_mfma_f32_16x16x32_bf16 a[252:255], a[164:167], v[216:219], a[252:255]\n"
    "s_waitcnt lgkmcnt(6)\n"
    "v_exp_f32_e32 v151, v151\n"
    "v_mfma_f32_16x16x32_bf16 v[16:19], a[172:175], v[216:219], v[16:19]\n"
    "ds_read_b64_tr_b16 a[190:191], v247 offset:59392\n"
    "v_cvt_pk_bf16_f32 v148, v148, v149\n"
    "v_mfma_f32_16x16x32_bf16 v[16:19], a[168:171], v[212:215], v[16:19]\n"
    "ds_read_b64_tr_b16 a[160:161], v244 offset:28672\n"
    "v_mfma_f32_16x16x32_bf16 v[12:15], a[168:171], v[204:207], v[12:15]\n"
    "v_exp_f32_e32 v156, v156\n"
    "v_mfma_f32_16x16x32_bf16 v[12:15], a[172:175], v[208:211], v[12:15]\n"
    "v_exp_f32_e32 v157, v157\n"
    "v_mfma_f32_16x16x32_bf16 v[8:11], a[172:175], v[200:203], v[8:11]\n"
    "ds_read_b64_tr_b16 a[162:163], v244 offset:30720\n"
    "v_cvt_pk_bf16_f32 v149, v150, v151\n"
    "v_mfma_f32_16x16x32_bf16 v[8:11], a[168:171], v[196:199], v[8:11]\n"
    "v_exp_f32_e32 v158, v158\n"
    "v_mfma_f32_16x16x32_bf16 v[4:7], a[168:171], v[188:191], v[4:7]\n"
    "v_exp_f32_e32 v159, v159\n"
    "v_mfma_f32_16x16x32_bf16 v[4:7], a[172:175], v[192:195], v[4:7]\n"
    "v_cvt_pk_bf16_f32 v156, v156, v157\n"
    "v_mfma_f32_16x16x32_bf16 v[0:3], a[172:175], v[184:187], v[0:3]\n"
    "ds_read_b64_tr_b16 a[164:165], v244 offset:61440\n"
    "v_mfma_f32_16x16x32_bf16 v[0:3], a[168:171], v[180:183], v[0:3]\n"
    "s_waitcnt lgkmcnt(7)\n"
    "v_exp_f32_e32 v164, v164\n"
    "v_mfma_f32_16x16x32_bf16 v[20:23], a[176:179], v[180:183], v[20:23]\n"
    "ds_read_b64_tr_b16 a[168:169], v245 offset:28672\n"
    "v_mfma_f32_16x16x32_bf16 v[20:23], a[180:183], v[184:187], v[20:23]\n"
    "ds_read_b64_tr_b16 a[166:167], v244 offset:63488\n"
    "v_mfma_f32_16x16x32_bf16 v[24:27], a[180:183], v[192:195], v[24:27]\n"
    "v_exp_f32_e32 v165, v165\n"
    "v_mfma_f32_16x16x32_bf16 v[24:27], a[176:179], v[188:191], v[24:27]\n"
    "v_cvt_pk_bf16_f32 v157, v158, v159\n"
    "v_mfma_f32_16x16x32_bf16 v[28:31], a[176:179], v[196:199], v[28:31]\n"
    "ds_read_b64_tr_b16 a[170:171], v245 offset:30720\n"
    "v_mfma_f32_16x16x32_bf16 v[28:31], a[180:183], v[200:203], v[28:31]\n"
    "v_exp_f32_e32 v166, v166\n"
    "v_mfma_f32_16x16x32_bf16 v[32:35], a[180:183], v[208:211], v[32:35]\n"
    "v_exp_f32_e32 v167, v167\n"
    "v_mfma_f32_16x16x32_bf16 v[32:35], a[176:179], v[204:207], v[32:35]\n"
    "v_cvt_pk_bf16_f32 v164, v164, v165\n"
    "v_mfma_f32_16x16x32_bf16 v[36:39], a[176:179], v[212:215], v[36:39]\n"
    "ds_read_b64_tr_b16 a[172:173], v245 offset:61440\n"
    "v_mfma_f32_16x16x32_bf16 v[36:39], a[180:183], v[216:219], v[36:39]\n"
    "s_waitcnt vmcnt(20)\n"
    "ds_read_b64_tr_b16 a[174:175], v245 offset:63488\n"
    "v_smfmac_f32_16x16x64_bf16 v[224:227], a[232:235], a[216:223], v238\n"
    "s_waitcnt lgkmcnt(8)\n"
    "v_mad_u32_u16 v234, v231, s90, v239 op_sel:[0,0,0,0]\n"
    "v_mfma_f32_16x16x32_bf16 v[56:59], a[188:191], v[216:219], v[56:59]\n"
    "s_bitcmp1_b32 s86, 8\n"
    "buffer_load_dwordx4 a[216:219], v234, s[20:23], 0 offen sc1\n"
    "v_mad_u32_u16 v235, v231, s90, v239 op_sel:[1,0,0,0]\n"
    "v_mfma_f32_16x16x32_bf16 v[56:59], a[184:187], v[212:215], v[56:59]\n"
    "s_cselect_b64 exec, -1, 0\n"
    "v_cvt_pk_bf16_f32 v236, v224, v225\n"
    "v_cvt_pk_bf16_f32 v237, v226, v227\n"
    "v_mfma_f32_16x16x32_bf16 v[52:55], a[184:187], v[204:207], v[52:55]\n"
    "s_cselect_b32 s88, 512, 0\n"
    "v_mov_b64_e32 v[224:225], 0\n"
    "v_mov_b64_e32 v[226:227], 0\n"
    "v_mfma_f32_16x16x32_bf16 v[52:55], a[188:191], v[208:211], v[52:55]\n"
    "buffer_store_dwordx2 v[236:237], v252, s[8:11], s87 offen sc1\n"
    "v_mfma_f32_16x16x32_bf16 v[48:51], a[188:191], v[200:203], v[48:51]\n"
    "s_mov_b64 exec, -1\n"
    "ds_read_b64_tr_b16 a[176:177], v246 offset:28672\n"
    "v_mfma_f32_16x16x32_bf16 v[48:51], a[184:187], v[196:199], v[48:51]\n"
    "s_add_u32 s87, s87, s88\n"
    "ds_read_b64_tr_b16 a[178:179], v246 offset:30720\n"
    "v_mfma_f32_16x16x32_bf16 v[44:47], a[184:187], v[188:191], v[44:47]\n"
    "buffer_load_dwordx4 a[220:223], v235, s[20:23], 0 offen sc1\n"
    "v_mfma_f32_16x16x32_bf16 v[44:47], a[188:191], v[192:195], v[44:47]\n"
    "ds_read_b64_tr_b16 a[180:181], v246 offset:61440\n"
    "v_mfma_f32_16x16x32_bf16 v[40:43], a[188:191], v[184:187], v[40:43]\n"
    "v_exp_f32_e32 v172, v172\n"
    "v_mfma_f32_16x16x32_bf16 v[40:43], a[184:187], v[180:183], v[40:43]\n"
    "s_waitcnt lgkmcnt(6)\n"
    "v_exp_f32_e32 v173, v173\n"
    "v_mfma_f32_16x16x32_bf16 v[60:63], a[160:163], v[180:183], v[60:63]\n"
    "ds_read_b64_tr_b16 a[182:183], v246 offset:63488\n"
    "v_cvt_pk_bf16_f32 v165, v166, v167\n"
    "v_mfma_f32_16x16x32_bf16 v[60:63], a[164:167], v[184:187], v[60:63]\n"
    "ds_read_b64_tr_b16 a[184:185], v247 offset:28672\n"
    "v_mfma_f32_16x16x32_bf16 v[64:67], a[164:167], v[192:195], v[64:67]\n"
    "ds_read_b64_tr_b16 a[186:187], v247 offset:30720\n"
    "v_mfma_f32_16x16x32_bf16 v[64:67], a[160:163], v[188:191], v[64:67]\n"
    "v_exp_f32_e32 v174, v174\n"
    "v_mfma_f32_16x16x32_bf16 v[68:71], a[160:163], v[196:199], v[68:71]\n"
    "v_exp_f32_e32 v175, v175\n"
    "v_mfma_f32_16x16x32_bf16 v[68:71], a[164:167], v[200:203], v[68:71]\n"
    "ds_read_b64_tr_b16 a[188:189], v247 offset:61440\n"
    "v_cvt_pk_bf16_f32 v172, v172, v173\n"
    "v_mfma_f32_16x16x32_bf16 v[72:75], a[164:167], v[208:211], v[72:75]\n"
    "ds_read_b64_tr_b16 a[190:191], v247 offset:63488\n"
    "v_cvt_pk_bf16_f32 v173, v174, v175\n"
    "v_mfma_f32_16x16x32_bf16 v[72:75], a[160:163], v[204:207], v[72:75]\n"
    "s_waitcnt vmcnt(30)\n"
    "v_mfma_f32_16x16x32_bf16 v[76:79], a[160:163], v[212:215], v[76:79]\n"
    "s_barrier\n"
    "v_mfma_f32_16x16x32_bf16 v[76:79], a[164:167], v[216:219], v[76:79]\n"
    "s_waitcnt lgkmcnt(8)\n"
    "s_mov_b32 m0, s42\n"
    "v_mfma_f32_16x16x32_bf16 v[96:99], a[172:175], v[216:219], v[96:99]\n"
    "v_exp_f32_e32 v144, v144\n"
    "v_mfma_f32_16x16x32_bf16 v[96:99], a[168:171], v[212:215], v[96:99]\n"
    "v_exp_f32_e32 v145, v145\n"
    "v_mfma_f32_16x16x32_bf16 v[92:95], a[168:171], v[204:207], v[92:95]\n"
    "v_exp_f32_e32 v146, v146\n"
    "v_mfma_f32_16x16x32_bf16 v[92:95], a[172:175], v[208:211], v[92:95]\n"
    "ds_read_b128 a[160:163], v240 offset:8192\n"
    "ds_write_addtid_b32 v140 offset:0\n"
    "v_mfma_f32_16x16x32_bf16 v[88:91], a[172:175], v[200:203], v[88:91]\n"
    "v_exp_f32_e32 v147, v147\n"
    "v_mfma_f32_16x16x32_bf16 v[88:91], a[168:171], v[196:199], v[88:91]\n"
    "ds_write_addtid_b32 v141 offset:256\n"
    "v_cvt_pk_bf16_f32 v142, v144, v145\n"
    "v_mfma_f32_16x16x32_bf16 v[84:87], a[168:171], v[188:191], v[84:87]\n"
    "v_exp_f32_e32 v152, v152\n"
    "v_mfma_f32_16x16x32_bf16 v[84:87], a[172:175], v[192:195], v[84:87]\n"
    "v_exp_f32_e32 v153, v153\n"
    "v_mfma_f32_16x16x32_bf16 v[80:83], a[172:175], v[184:187], v[80:83]\n"
    "ds_write_addtid_b32 v148 offset:1024\n"
    "v_cvt_pk_bf16_f32 v143, v146, v147\n"
    "v_mfma_f32_16x16x32_bf16 v[80:83], a[168:171], v[180:183], v[80:83]\n"
    "s_waitcnt vmcnt(20)\n"
    "ds_read_b128 a[164:167], v241 offset:8192\n"
    "ds_write_addtid_b32 v149 offset:1280\n"
    "v_smfmac_f32_16x16x64_bf16 v[224:227], a[232:235], a[224:231], v238\n"
    "s_waitcnt lgkmcnt(10)\n"
    "ds_write_addtid_b32 v156 offset:2048\n"
    "v_mad_u32_u16 v234, v232, s90, v239 op_sel:[0,0,0,0]\n"
    "v_mfma_f32_16x16x32_bf16 v[100:103], a[176:179], v[180:183], v[100:103]\n"
    "s_bitcmp1_b32 s86, 9\n"
    "buffer_load_dwordx4 a[224:227], v234, s[20:23], 0 offen sc1\n"
    "v_mad_u32_u16 v235, v232, s90, v239 op_sel:[1,0,0,0]\n"
    "v_mfma_f32_16x16x32_bf16 v[100:103], a[180:183], v[184:187], v[100:103]\n"
    "s_cselect_b64 exec, -1, 0\n"
    "v_cvt_pk_bf16_f32 v236, v224, v225\n"
    "v_cvt_pk_bf16_f32 v237, v226, v227\n"
    "v_mfma_f32_16x16x32_bf16 v[104:107], a[180:183], v[192:195], v[104:107]\n"
    "s_cselect_b32 s88, 512, 0\n"
    "v_mov_b64_e32 v[224:225], 0\n"
    "v_mov_b64_e32 v[226:227], 0\n"
    "v_mfma_f32_16x16x32_bf16 v[104:107], a[176:179], v[188:191], v[104:107]\n"
    "buffer_store_dwordx2 v[236:237], v252, s[8:11], s87 offen sc1\n"
    "v_mfma_f32_16x16x32_bf16 v[108:111], a[176:179], v[196:199], v[108:111]\n"
    "s_mov_b64 exec, -1\n"
    "ds_read_b128 a[168:171], v240 offset:12288\n"
    "ds_write_addtid_b32 v157 offset:2304\n"
    "v_mfma_f32_16x16x32_bf16 v[108:111], a[180:183], v[200:203], v[108:111]\n"
    "s_add_u32 s87, s87, s88\n"
    "v_exp_f32_e32 v154, v154\n"
    "v_mfma_f32_16x16x32_bf16 v[112:115], a[180:183], v[208:211], v[112:115]\n"
    "buffer_load_dwordx4 a[228:231], v235, s[20:23], 0 offen sc1\n"
    "ds_write_addtid_b32 v164 offset:3072\n"
    "v_mfma_f32_16x16x32_bf16 v[112:115], a[176:179], v[204:207], v[112:115]\n"
    "ds_write_addtid_b32 v165 offset:3328\n"
    "v_mfma_f32_16x16x32_bf16 v[116:119], a[176:179], v[212:215], v[116:119]\n"
    "v_exp_f32_e32 v155, v155\n"
    "v_mfma_f32_16x16x32_bf16 v[116:119], a[180:183], v[216:219], v[116:119]\n"
    "s_waitcnt lgkmcnt(11)\n"
    "ds_read_b128 a[172:175], v241 offset:12288\n"
    "v_cvt_pk_bf16_f32 v150, v152, v153\n"
    "v_mfma_f32_16x16x32_bf16 v[136:139], a[188:191], v[216:219], v[136:139]\n"
    "v_exp_f32_e32 v160, v160\n"
    "v_mfma_f32_16x16x32_bf16 v[136:139], a[184:187], v[212:215], v[136:139]\n"
    "v_exp_f32_e32 v161, v161\n"
    "v_mfma_f32_16x16x32_bf16 v[132:135], a[184:187], v[204:207], v[132:135]\n"
    "ds_read_b128 a[176:179], v242 offset:8192\n"
    "v_cvt_pk_bf16_f32 v151, v154, v155\n"
    "v_mfma_f32_16x16x32_bf16 v[132:135], a[188:191], v[208:211], v[132:135]\n"
    "v_exp_f32_e32 v162, v162\n"
    "v_mfma_f32_16x16x32_bf16 v[128:131], a[188:191], v[200:203], v[128:131]\n"
    "v_exp_f32_e32 v163, v163\n"
    "v_mfma_f32_16x16x32_bf16 v[128:131], a[184:187], v[196:199], v[128:131]\n"
    "ds_write_addtid_b32 v172 offset:4096\n"
    "v_cvt_pk_bf16_f32 v158, v160, v161\n"
    "v_mfma_f32_16x16x32_bf16 v[124:127], a[184:187], v[188:191], v[124:127]\n"
    "v_exp_f32_e32 v168, v168\n"
    "v_mfma_f32_16x16x32_bf16 v[124:127], a[188:191], v[192:195], v[124:127]\n"
    "ds_read_b128 a[180:183], v243 offset:8192\n"
    "ds_write_addtid_b32 v173 offset:4352\n"
    "v_mfma_f32_16x16x32_bf16 v[120:123], a[188:191], v[184:187], v[120:123]\n"
    "v_exp_f32_e32 v169, v169\n"
    "v_mfma_f32_16x16x32_bf16 v[120:123], a[184:187], v[180:183], v[120:123]\n"
    "s_waitcnt lgkmcnt(11)\n"
    "ds_write_addtid_b32 v142 offset:512\n"
    "v_cvt_pk_bf16_f32 v159, v162, v163\n"
    "v_mfma_f32_16x16x32_bf16 v[212:215], a[160:163], a[128:131], v[220:223]\n"
    "ds_write_addtid_b32 v143 offset:768\n"
    "v_mfma_f32_16x16x32_bf16 v[212:215], a[164:167], a[132:135], v[212:215]\n"
    "v_exp_f32_e32 v170, v170\n"
    "v_mfma_f32_16x16x32_bf16 v[204:207], a[164:167], a[100:103], v[220:223]\n"
    "ds_write_addtid_b32 v150 offset:1536\n"
    "v_mfma_f32_16x16x32_bf16 v[204:207], a[160:163], a[96:99], v[204:207]\n"
    "ds_read_b128 a[184:187], v242 offset:12288\n"
    "v_mfma_f32_16x16x32_bf16 v[196:199], a[160:163], a[64:67], v[220:223]\n"
    "v_exp_f32_e32 v171, v171\n"
    "v_mfma_f32_16x16x32_bf16 v[196:199], a[164:167], a[68:71], v[196:199]\n"
    "ds_write_addtid_b32 v151 offset:1792\n"
    "v_cvt_pk_bf16_f32 v166, v168, v169\n"
    "v_mfma_f32_16x16x32_bf16 v[188:191], a[164:167], a[36:39], v[220:223]\n"
    "v_exp_f32_e32 v176, v176\n"
    "v_mfma_f32_16x16x32_bf16 v[188:191], a[160:163], a[32:35], v[188:191]\n"
    "v_exp_f32_e32 v177, v177\n"
    "v_mfma_f32_16x16x32_bf16 v[180:183], a[160:163], a[0:3], v[220:223]\n"
    "ds_write_addtid_b32 v158 offset:2560\n"
    "v_cvt_pk_bf16_f32 v167, v170, v171\n"
    "v_mfma_f32_16x16x32_bf16 v[180:183], a[164:167], a[4:7], v[180:183]\n"
    "s_waitcnt vmcnt(20)\n"
    "v_exp_f32_e32 v178, v178\n"
    "v_smfmac_f32_16x16x64_bf16 v[224:227], a[232:235], a[192:199], v238\n"
    "s_waitcnt lgkmcnt(10)\n"
    "ds_read_b128 a[188:191], v243 offset:12288\n"
    "v_mfma_f32_16x16x32_bf16 v[180:183], a[168:171], a[8:11], v[180:183]\n"
    "s_bitcmp1_b32 s86, 10\n"
    "ds_read_b128 a[160:163], v240 offset:10240\n"
    "v_mfma_f32_16x16x32_bf16 v[180:183], a[172:175], a[12:15], v[180:183]\n"
    "s_cselect_b64 exec, -1, 0\n"
    "v_cvt_pk_bf16_f32 v236, v224, v225\n"
    "v_cvt_pk_bf16_f32 v237, v226, v227\n"
    "v_mfma_f32_16x16x32_bf16 v[188:191], a[172:175], a[44:47], v[188:191]\n"
    "s_cselect_b32 s88, 512, 0\n"
    "v_mov_b64_e32 v[224:225], 0\n"
    "v_mov_b64_e32 v[226:227], 0\n"
    "v_mfma_f32_16x16x32_bf16 v[188:191], a[168:171], a[40:43], v[188:191]\n"
    "buffer_store_dwordx2 v[236:237], v252, s[8:11], s87 offen sc1\n"
    "v_mfma_f32_16x16x32_bf16 v[196:199], a[168:171], a[72:75], v[196:199]\n"
    "s_mov_b64 exec, -1\n"
    "ds_read2_b32 v[228:229], v251 offset0:20 offset1:22\n"
    "v_mfma_f32_16x16x32_bf16 v[196:199], a[172:175], a[76:79], v[196:199]\n"
    "s_add_u32 s87, s87, s88\n"
    "ds_read2_b32 v[230:231], v251 offset0:24 offset1:26\n"
    "v_mfma_f32_16x16x32_bf16 v[204:207], a[172:175], a[108:111], v[204:207]\n"
    "ds_read_b32 v232, v251 offset:112\n"
    "v_mfma_f32_16x16x32_bf16 v[204:207], a[168:171], a[104:107], v[204:207]\n"
    "v_exp_f32_e32 v179, v179\n"
    "v_mfma_f32_16x16x32_bf16 v[212:215], a[168:171], a[136:139], v[212:215]\n"
    "ds_write_addtid_b32 v159 offset:2816\n"
    "v_cvt_pk_bf16_f32 v174, v176, v177\n"
    "v_mfma_f32_16x16x32_bf16 v[212:215], a[172:175], a[140:143], v[212:215]\n"
    "s_waitcnt lgkmcnt(13)\n"
    "ds_read_b128 a[164:167], v241 offset:10240\n"
    "v_mfma_f32_16x16x32_bf16 v[212:215], a[176:179], a[144:147], v[212:215]\n"
    "ds_write_addtid_b32 v166 offset:3584\n"
    "v_cvt_pk_bf16_f32 v175, v178, v179\n"
    "v_mfma_f32_16x16x32_bf16 v[212:215], a[180:183], a[148:151], v[212:215]\n"
    "ds_read_b128 a[168:171], v240 offset:14336\n"
    "v_mfma_f32_16x16x32_bf16 v[204:207], a[180:183], a[116:119], v[204:207]\n"
    "s_waitcnt lgkmcnt(6)\n"
    "ds_write_addtid_b32 v167 offset:3840\n"
    "v_mad_u32_u16 v234, v228, s90, v239 op_sel:[0,0,0,0]\n"
    "v_mfma_f32_16x16x32_bf16 v[204:207], a[176:179], a[112:115], v[204:207]\n"
    "buffer_load_dwordx4 a[192:195], v234, s[20:23], 0 offen sc1\n"
    "v_mad_u32_u16 v235, v228, s90, v239 op_sel:[1,0,0,0]\n"
    "v_mfma_f32_16x16x32_bf16 v[196:199], a[176:179], a[80:83], v[196:199]\n"
    "ds_write_addtid_b32 v174 offset:4608\n"
    "v_mfma_f32_16x16x32_bf16 v[196:199], a[180:183], a[84:87], v[196:199]\n"
    "ds_write_addtid_b32 v175 offset:4864\n"
    "v_mfma_f32_16x16x32_bf16 v[188:191], a[180:183], a[52:55], v[188:191]\n"
    "buffer_load_dwordx4 a[196:199], v235, s[20:23], 0 offen sc1\n"
    "v_mfma_f32_16x16x32_bf16 v[188:191], a[176:179], a[48:51], v[188:191]\n"
    "v_mfma_f32_16x16x32_bf16 v[180:183], a[176:179], a[16:19], v[180:183]\n"
    "s_waitcnt lgkmcnt(0)\n"
    "v_mfma_f32_16x16x32_bf16 v[180:183], a[180:183], a[20:23], v[180:183]\n"
    "s_barrier\n"
    "v_mfma_f32_16x16x32_bf16 v[180:183], a[184:187], a[24:27], v[180:183]\n"
    "s_add_u32 m0, s43, 24576\n"
    "ds_read_b128 a[172:175], v241 offset:14336\n"
    "v_mfma_f32_16x16x32_bf16 v[180:183], a[188:191], a[28:31], v[180:183]\n"
    "ds_read2st64_b32 v[144:145], v249 offset0:0 offset1:1\n"
    "v_mfma_f32_16x16x32_bf16 v[188:191], a[188:191], a[60:63], v[188:191]\n"
    "buffer_load_dwordx4 v248, s[8:11], s40 offen offset:0 sc1 lds\n"
    "v_mfma_f32_16x16x32_bf16 v[188:191], a[184:187], a[56:59], v[188:191]\n"
    "ds_read_b128 a[176:179], v242 offset:10240\n"
    "v_mfma_f32_16x16x32_bf16 v[196:199], a[184:187], a[88:91], v[196:199]\n"
    "v_mfma_f32_16x16x32_bf16 v[196:199], a[188:191], a[92:95], v[196:199]\n"
    "v_mfma_f32_16x16x32_bf16 v[204:207], a[188:191], a[124:127], v[204:207]\n"
    "buffer_load_dwordx4 v248, s[8:11], s40 offen offset:1024 sc1 lds\n"
    "v_mfma_f32_16x16x32_bf16 v[204:207], a[184:187], a[120:123], v[204:207]\n"
    "ds_read2st64_b32 v[146:147], v249 offset0:2 offset1:3\n"
    "v_mfma_f32_16x16x32_bf16 v[212:215], a[184:187], a[152:155], v[212:215]\n"
    "v_mfma_f32_16x16x32_bf16 v[212:215], a[188:191], a[156:159], v[212:215]\n"
    "ds_read_b128 a[180:183], v243 offset:10240\n"
    "v_mfma_f32_16x16x32_bf16 v[216:219], a[160:163], a[128:131], v[220:223]\n"
    "buffer_load_dwordx4 v248, s[8:11], s40 offen offset:2048 sc1 lds\n"
    "v_mfma_f32_16x16x32_bf16 v[216:219], a[164:167], a[132:135], v[216:219]\n"
    "ds_read2st64_b32 v[152:153], v249 offset0:4 offset1:5\n"
    "v_mfma_f32_16x16x32_bf16 v[208:211], a[164:167], a[100:103], v[220:223]\n"
    "v_exp_f32_e32 v180, v180\n"
    "v_mfma_f32_16x16x32_bf16 v[208:211], a[160:163], a[96:99], v[208:211]\n"
    "ds_read_b128 a[184:187], v242 offset:14336\n"
    "v_mfma_f32_16x16x32_bf16 v[200:203], a[160:163], a[64:67], v[220:223]\n"
    "buffer_load_dwordx4 v248, s[8:11], s40 offen offset:3072 sc1 lds\n"
    "v_mfma_f32_16x16x32_bf16 v[200:203], a[164:167], a[68:71], v[200:203]\n"
    "s_add_u32 m0, s43, 28672\n"
    "ds_read2st64_b32 v[154:155], v249 offset0:6 offset1:7\n"
    "v_mfma_f32_16x16x32_bf16 v[192:195], a[164:167], a[36:39], v[220:223]\n"
    "s_add_u32 s40, s40, 0x8000\n"
    "ds_read2st64_b32 v[160:161], v249 offset0:8 offset1:9\n"
    "v_mfma_f32_16x16x32_bf16 v[192:195], a[160:163], a[32:35], v[192:195]\n"
    "ds_read2st64_b32 v[162:163], v249 offset0:10 offset1:11\n"
    "v_mfma_f32_16x16x32_bf16 v[184:187], a[160:163], a[0:3], v[220:223]\n"
    "buffer_load_dwordx4 v248, s[8:11], s41 offen offset:0 sc1 lds\n"
    "v_mfma_f32_16x16x32_bf16 v[184:187], a[164:167], a[4:7], v[184:187]\n"
    "s_waitcnt vmcnt(17)\n"
    "ds_read_b128 a[188:191], v243 offset:14336\n"
    "v_smfmac_f32_16x16x64_bf16 v[224:227], a[232:235], a[200:207], v238\n"
    "s_waitcnt lgkmcnt(10)\n"
    "v_mad_u32_u16 v234, v229, s90, v239 op_sel:[0,0,0,0]\n"
    "v_mfma_f32_16x16x32_bf16 v[184:187], a[168:171], a[8:11], v[184:187]\n"
    "s_bitcmp1_b32 s86, 11\n"
    "buffer_load_dwordx4 a[200:203], v234, s[20:23], 0 offen sc1\n"
    "v_mad_u32_u16 v235, v229, s90, v239 op_sel:[1,0,0,0]\n"
    "v_mfma_f32_16x16x32_bf16 v[184:187], a[172:175], a[12:15], v[184:187]\n"
    "s_cselect_b64 exec, -1, 0\n"
    "v_cvt_pk_bf16_f32 v236, v224, v225\n"
    "v_cvt_pk_bf16_f32 v237, v226, v227\n"
    "v_mfma_f32_16x16x32_bf16 v[192:195], a[172:175], a[44:47], v[192:195]\n"
    "s_cselect_b32 s88, 512, 0\n"
    "v_mov_b64_e32 v[224:225], 0\n"
    "v_mov_b64_e32 v[226:227], 0\n"
    "v_mfma_f32_16x16x32_bf16 v[192:195], a[168:171], a[40:43], v[192:195]\n"
    "buffer_store_dwordx2 v[236:237], v252, s[8:11], s87 offen sc1\n"
    "v_mfma_f32_16x16x32_bf16 v[200:203], a[168:171], a[72:75], v[200:203]\n"
    "s_mov_b64 exec, -1\n"
    "ds_read_b64_tr_b16 a[160:161], v244 offset:0\n"
    "v_mfma_f32_16x16x32_bf16 v[200:203], a[172:175], a[76:79], v[200:203]\n"
    "s_add_u32 s87, s87, s88\n"
    "ds_read_b64_tr_b16 a[162:163], v244 offset:2048\n"
    "v_mfma_f32_16x16x32_bf16 v[208:211], a[172:175], a[108:111], v[208:211]\n"
    "buffer_load_dwordx4 v248, s[8:11], s41 offen offset:1024 sc1 lds\n"
    "v_mfma_f32_16x16x32_bf16 v[208:211], a[168:171], a[104:107], v[208:211]\n"
    "ds_read_b64_tr_b16 a[164:165], v244 offset:32768\n"
    "v_mfma_f32_16x16x32_bf16 v[216:219], a[168:171], a[136:139], v[216:219]\n"
    "ds_read2st64_b32 v[168:169], v249 offset0:12 offset1:13\n"
    "v_mfma_f32_16x16x32_bf16 v[216:219], a[172:175], a[140:143], v[216:219]\n"
    "s_waitcnt lgkmcnt(10)\n"
    "ds_read2st64_b32 v[170:171], v249 offset0:14 offset1:15\n"
    "v_mfma_f32_16x16x32_bf16 v[216:219], a[176:179], a[144:147], v[216:219]\n"
    "buffer_load_dwordx4 v248, s[8:11], s41 offen offset:2048 sc1 lds\n"
    "v_mfma_f32_16x16x32_bf16 v[216:219], a[180:183], a[148:151], v[216:219]\n"
    "ds_read_b64_tr_b16 a[166:167], v244 offset:34816\n"
    "v_mfma_f32_16x16x32_bf16 v[208:211], a[180:183], a[116:119], v[208:211]\n"
    "ds_read_b64_tr_b16 a[168:169], v245 offset:0\n"
    "v_mfma_f32_16x16x32_bf16 v[208:211], a[176:179], a[112:115], v[208:211]\n"
    "v_exp_f32_e32 v181, v181\n"
    "v_mfma_f32_16x16x32_bf16 v[200:203], a[176:179], a[80:83], v[200:203]\n"
    "buffer_load_dwordx4 v248, s[8:11], s41 offen offset:3072 sc1 lds\n"
    "v_mfma_f32_16x16x32_bf16 v[200:203], a[180:183], a[84:87], v[200:203]\n"
    "ds_read_b64_tr_b16 a[170:171], v245 offset:2048\n"
    "v_mfma_f32_16x16x32_bf16 v[192:195], a[180:183], a[52:55], v[192:195]\n"
    "s_add_u32 s41, s41, 0x8000\n"
    "ds_read2st64_b32 v[176:177], v249 offset0:16 offset1:17\n"
    "v_mfma_f32_16x16x32_bf16 v[192:195], a[176:179], a[48:51], v[192:195]\n"
    "buffer_load_dwordx4 a[204:207], v235, s[20:23], 0 offen sc1\n"
    "v_mfma_f32_16x16x32_bf16 v[184:187], a[176:179], a[16:19], v[184:187]\n"
    "ds_read_b64_tr_b16 a[172:173], v245 offset:32768\n"
    "v_mfma_f32_16x16x32_bf16 v[184:187], a[180:183], a[20:23], v[184:187]\n"
    "s_waitcnt lgkmcnt(10)\n"
    "ds_read2st64_b32 v[178:179], v249 offset0:18 offset1:19\n"
    "v_mfma_f32_16x16x32_bf16 v[184:187], a[184:187], a[24:27], v[184:187]\n"
    "ds_read_b64_tr_b16 a[176:177], v246 offset:0\n"
    "v_mfma_f32_16x16x32_bf16 v[184:187], a[188:191], a[28:31], v[184:187]\n"
    "ds_read_b64_tr_b16 a[174:175], v245 offset:34816\n"
    "v_mfma_f32_16x16x32_bf16 v[192:195], a[188:191], a[60:63], v[192:195]\n"
    "v_exp_f32_e32 v182, v182\n"
    "v_mfma_f32_16x16x32_bf16 v[192:195], a[184:187], a[56:59], v[192:195]\n"
    "v_exp_f32_e32 v183, v183\n"
    "v_mfma_f32_16x16x32_bf16 v[200:203], a[184:187], a[88:91], v[200:203]\n"
    "ds_read_b64_tr_b16 a[178:179], v246 offset:2048\n"
    "v_cvt_pk_bf16_f32 v180, v180, v181\n"
    "v_mfma_f32_16x16x32_bf16 v[200:203], a[188:191], a[92:95], v[200:203]\n"
    "v_exp_f32_e32 v188, v188\n"
    "v_mfma_f32_16x16x32_bf16 v[208:211], a[188:191], a[124:127], v[208:211]\n"
    ".long 0xbedf00ff\n"
    ".Lat_w7:\n"
    "s_setpc_b64 s[2:3]\n"
    "v_exp_f32_e32 v189, v189\n"
    "v_mfma_f32_16x16x32_bf16 v[208:211], a[184:187], a[120:123], v[208:211]\n"
    "v_cvt_pk_bf16_f32 v181, v182, v183\n"
    "v_mfma_f32_16x16x32_bf16 v[216:219], a[184:187], a[152:155], v[216:219]\n"
    "ds_read_b64_tr_b16 a[180:181], v246 offset:32768\n"
    "v_mfma_f32_16x16x32_bf16 v[216:219], a[188:191], a[156:159], v[216:219]\n"
    "s_waitcnt vmcnt(20)\n"
    "ds_read_b64_tr_b16 a[182:183], v246 offset:34816\n"
    "v_smfmac_f32_16x16x64_bf16 v[224:227], a[232:235], a[208:215], v238\n"
    "s_waitcnt lgkmcnt(5)\n"
    "v_mad_u32_u16 v234, v230, s90, v239 op_sel:[0,0,0,0]\n"
    "v_mfma_f32_16x16x32_bf16 a[236:239], a[160:163], v[140:143], a[236:239]\n"
    "s_bitcmp1_b32 s86, 12\n"
    "buffer_load_dwordx4 a[208:211], v234, s[20:23], 0 offen sc1\n"
    "v_mad_u32_u16 v235, v230, s90, v239 op_sel:[1,0,0,0]\n"
    "v_mfma_f32_16x16x32_bf16 a[236:239], a[164:167], v[144:147], a[236:239]\n"
    "s_cselect_b64 exec, -1, 0\n"
    "v_cvt_pk_bf16_f32 v236, v224, v225\n"
    "v_cvt_pk_bf16_f32 v237, v226, v227\n"
    "v_mfma_f32_16x16x32_bf16 a[240:243], a[164:167], v[152:155], a[240:243]\n"
    "s_cselect_b32 s88, 512, 0\n"
    "v_mov_b64_e32 v[224:225], 0\n"
    "v_mov_b64_e32 v[226:227], 0\n"
    "v_mfma_f32_16x16x32_bf16 a[240:243], a[160:163], v[148:151], a[240:243]\n"
    "buffer_store_dwordx2 v[236:237], v252, s[8:11], s87 offen sc1\n"
    "v_mfma_f32_16x16x32_bf16 a[244:247], a[160:163], v[156:159], a[244:247]\n"
    "s_mov_b64 exec, -1\n"
    "ds_read_b64_tr_b16 a[184:185], v247 offset:0\n"
    "v_mfma_f32_16x16x32_bf16 a[244:247], a[164:167], v[160:163], a[244:247]\n"
    "s_add_u32 s87, s87, s88\n"
    "ds_read_b64_tr_b16 a[186:187], v247 offset:2048\n"
    "v_mfma_f32_16x16x32_bf16 a[248:251], a[164:167], v[168:171], a[248:251]\n"
    "buffer_load_dwordx4 a[212:215], v235, s[20:23], 0 offen sc1\n"
    "v_mfma_f32_16x16x32_bf16 a[248:251], a[160:163], v[164:167], a[248:251]\n"
    "ds_read_b64_tr_b16 a[188:189], v247 offset:32768\n"
    "v_mfma_f32_16x16x32_bf16 a[252:255], a[160:163], v[172:175], a[252:255]\n"
    "v_exp_f32_e32 v190, v190\n"
    "v_mfma_f32_16x16x32_bf16 a[252:255], a[164:167], v[176:179], a[252:255]\n"
    "s_waitcnt lgkmcnt(6)\n"
    "v_exp_f32_e32 v191, v191\n"
    "v_mfma_f32_16x16x32_bf16 v[16:19], a[172:175], v[176:179], v[16:19]\n"
    "ds_read_b64_tr_b16 a[190:191], v247 offset:34816\n"
    "v_cvt_pk_bf16_f32 v188, v188, v189\n"
    "v_mfma_f32_16x16x32_bf16 v[16:19], a[168:171], v[172:175], v[16:19]\n"
    "ds_read_b64_tr_b16 a[160:161], v244 offset:4096\n"
    "v_mfma_f32_16x16x32_bf16 v[12:15], a[168:171], v[164:167], v[12:15]\n"
    "v_exp_f32_e32 v196, v196\n"
    "v_mfma_f32_16x16x32_bf16 v[12:15], a[172:175], v[168:171], v[12:15]\n"
    "v_exp_f32_e32 v197, v197\n"
    "v_mfma_f32_16x16x32_bf16 v[8:11], a[172:175], v[160:163], v[8:11]\n"
    "ds_read_b64_tr_b16 a[162:163], v244 offset:6144\n"
    "v_cvt_pk_bf16_f32 v189, v190, v191\n"
    "v_mfma_f32_16x16x32_bf16 v[8:11], a[168:171], v[156:159], v[8:11]\n"
    "v_exp_f32_e32 v198, v198\n"
    "v_mfma_f32_16x16x32_bf16 v[4:7], a[168:171], v[148:151], v[4:7]\n"
    "v_exp_f32_e32 v199, v199\n"
    "v_mfma_f32_16x16x32_bf16 v[4:7], a[172:175], v[152:155], v[4:7]\n"
    "v_cvt_pk_bf16_f32 v196, v196, v197\n"
    "v_mfma_f32_16x16x32_bf16 v[0:3], a[172:175], v[144:147], v[0:3]\n"
    "ds_read_b64_tr_b16 a[164:165], v244 offset:36864\n"
    "v_mfma_f32_16x16x32_bf16 v[0:3], a[168:171], v[140:143], v[0:3]\n"
    "s_waitcnt lgkmcnt(7)\n"
    "v_exp_f32_e32 v204, v204\n"
    "v_mfma_f32_16x16x32_bf16 v[20:23], a[176:179], v[140:143], v[20:23]\n"
    "ds_read_b64_tr_b16 a[168:169], v245 offset:4096\n"
    "v_mfma_f32_16x16x32_bf16 v[20:23], a[180:183], v[144:147], v[20:23]\n"
    "ds_read_b64_tr_b16 a[166:167], v244 offset:38912\n"
    "v_mfma_f32_16x16x32_bf16 v[24:27], a[180:183], v[152:155], v[24:27]\n"
    "v_exp_f32_e32 v205, v205\n"
    "v_mfma_f32_16x16x32_bf16 v[24:27], a[176:179], v[148:151], v[24:27]\n"
    "v_cvt_pk_bf16_f32 v197, v198, v199\n"
    "v_mfma_f32_16x16x32_bf16 v[28:31], a[176:179], v[156:159], v[28:31]\n"
    "ds_read_b64_tr_b16 a[170:171], v245 offset:6144\n"
    "v_mfma_f32_16x16x32_bf16 v[28:31], a[180:183], v[160:163], v[28:31]\n"
    "v_exp_f32_e32 v206, v206\n"
    "v_mfma_f32_16x16x32_bf16 v[32:35], a[180:183], v[168:171], v[32:35]\n"
    "v_exp_f32_e32 v207, v207\n"
    "v_mfma_f32_16x16x32_bf16 v[32:35], a[176:179], v[164:167], v[32:35]\n"
    "v_cvt_pk_bf16_f32 v204, v204, v205\n"
    "v_mfma_f32_16x16x32_bf16 v[36:39], a[176:179], v[172:175], v[36:39]\n"
    "ds_read_b64_tr_b16 a[172:173], v245 offset:36864\n"
    "v_mfma_f32_16x16x32_bf16 v[36:39], a[180:183], v[176:179], v[36:39]\n"
    "s_waitcnt vmcnt(20)\n"
    "ds_read_b64_tr_b16 a[174:175], v245 offset:38912\n"
    "v_smfmac_f32_16x16x64_bf16 v[224:227], a[232:235], a[216:223], v238\n"
    "s_waitcnt lgkmcnt(8)\n"
    "v_mad_u32_u16 v234, v231, s90, v239 op_sel:[0,0,0,0]\n"
    "v_mfma_f32_16x16x32_bf16 v[56:59], a[188:191], v[176:179], v[56:59]\n"
    "s_bitcmp1_b32 s86, 13\n"
    "buffer_load_dwordx4 a[216:219], v234, s[20:23], 0 offen sc1\n"
    "v_mad_u32_u16 v235, v231, s90, v239 op_sel:[1,0,0,0]\n"
    "v_mfma_f32_16x16x32_bf16 v[56:59], a[184:187], v[172:175], v[56:59]\n"
    "s_cselect_b64 exec, -1, 0\n"
    "v_cvt_pk_bf16_f32 v236, v224, v225\n"
    "v_cvt_pk_bf16_f32 v237, v226, v227\n"
    "v_mfma_f32_16x16x32_bf16 v[52:55], a[184:187], v[164:167], v[52:55]\n"
    "s_cselect_b32 s88, 512, 0\n"
    "v_mov_b64_e32 v[224:225], 0\n"
    "v_mov_b64_e32 v[226:227], 0\n"
    "v_mfma_f32_16x16x32_bf16 v[52:55], a[188:191], v[168:171], v[52:55]\n"
    "buffer_store_dwordx2 v[236:237], v252, s[8:11], s87 offen sc1\n"
    "v_mfma_f32_16x16x32_bf16 v[48:51], a[188:191], v[160:163], v[48:51]\n"
    "s_mov_b64 exec, -1\n"
    "ds_read_b64_tr_b16 a[176:177], v246 offset:4096\n"
    "v_mfma_f32_16x16x32_bf16 v[48:51], a[184:187], v[156:159], v[48:51]\n"
    "s_add_u32 s87, s87, s88\n"
    "ds_read_b64_tr_b16 a[178:179], v246 offset:6144\n"
    "v_mfma_f32_16x16x32_bf16 v[44:47], a[184:187], v[148:151], v[44:47]\n"
    "buffer_load_dwordx4 a[220:223], v235, s[20:23], 0 offen sc1\n"
    "v_mfma_f32_16x16x32_bf16 v[44:47], a[188:191], v[152:155], v[44:47]\n"
    "ds_read_b64_tr_b16 a[180:181], v246 offset:36864\n"
    "v_mfma_f32_16x16x32_bf16 v[40:43], a[188:191], v[144:147], v[40:43]\n"
    "v_exp_f32_e32 v212, v212\n"
    "v_mfma_f32_16x16x32_bf16 v[40:43], a[184:187], v[140:143], v[40:43]\n"
    "s_waitcnt lgkmcnt(6)\n"
    "v_exp_f32_e32 v213, v213\n"
    "v_mfma_f32_16x16x32_bf16 v[60:63], a[160:163], v[140:143], v[60:63]\n"
    "ds_read_b64_tr_b16 a[182:183], v246 offset:38912\n"
    "v_cvt_pk_bf16_f32 v205, v206, v207\n"
    "v_mfma_f32_16x16x32_bf16 v[60:63], a[164:167], v[144:147], v[60:63]\n"
    "ds_read_b64_tr_b16 a[184:185], v247 offset:4096\n"
    "v_mfma_f32_16x16x32_bf16 v[64:67], a[164:167], v[152:155], v[64:67]\n"
    "ds_read_b64_tr_b16 a[186:187], v247 offset:6144\n"
    "v_mfma_f32_16x16x32_bf16 v[64:67], a[160:163], v[148:151], v[64:67]\n"
    "v_exp_f32_e32 v214, v214\n"
    "v_mfma_f32_16x16x32_bf16 v[68:71], a[160:163], v[156:159], v[68:71]\n"
    "v_exp_f32_e32 v215, v215\n"
    "v_mfma_f32_16x16x32_bf16 v[68:71], a[164:167], v[160:163], v[68:71]\n"
    "ds_read_b64_tr_b16 a[188:189], v247 offset:36864\n"
    "v_cvt_pk_bf16_f32 v212, v212, v213\n"
    "v_mfma_f32_16x16x32_bf16 v[72:75], a[164:167], v[168:171], v[72:75]\n"
    "ds_read_b64_tr_b16 a[190:191], v247 offset:38912\n"
    "v_cvt_pk_bf16_f32 v213, v214, v215\n"
    "v_mfma_f32_16x16x32_bf16 v[72:75], a[160:163], v[164:167], v[72:75]\n"
    "s_waitcnt vmcnt(30)\n"
    "v_mfma_f32_16x16x32_bf16 v[76:79], a[160:163], v[172:175], v[76:79]\n"
    "s_barrier\n"
    "v_mfma_f32_16x16x32_bf16 v[76:79], a[164:167], v[176:179], v[76:79]\n"
    "s_waitcnt lgkmcnt(8)\n"
    "s_mov_b32 m0, s42\n"
    "v_mfma_f32_16x16x32_bf16 v[96:99], a[172:175], v[176:179], v[96:99]\n"
    "v_exp_f32_e32 v184, v184\n"
    "v_mfma_f32_16x16x32_bf16 v[96:99], a[168:171], v[172:175], v[96:99]\n"
    "v_exp_f32_e32 v185, v185\n"
    "v_mfma_f32_16x16x32_bf16 v[92:95], a[168:171], v[164:167], v[92:95]\n"
    "v_exp_f32_e32 v186, v186\n"
    "v_mfma_f32_16x16x32_bf16 v[92:95], a[172:175], v[168:171], v[92:95]\n"
    "ds_read_b128 a[160:163], v240 offset:16384\n"
    "ds_write_addtid_b32 v180 offset:0\n"
    "v_mfma_f32_16x16x32_bf16 v[88:91], a[172:175], v[160:163], v[88:91]\n"
    "v_exp_f32_e32 v187, v187\n"
    "v_mfma_f32_16x16x32_bf16 v[88:91], a[168:171], v[156:159], v[88:91]\n"
    "ds_write_addtid_b32 v181 offset:256\n"
    "v_cvt_pk_bf16_f32 v182, v184, v185\n"
    "v_mfma_f32_16x16x32_bf16 v[84:87], a[168:171], v[148:151], v[84:87]\n"
    "v_exp_f32_e32 v192, v192\n"
    "v_mfma_f32_16x16x32_bf16 v[84:87], a[172:175], v[152:155], v[84:87]\n"
    "v_exp_f32_e32 v193, v193\n"
    "v_mfma_f32_16x16x32_bf16 v[80:83], a[172:175], v[144:147], v[80:83]\n"
    "ds_write_addtid_b32 v188 offset:1024\n"
    "v_cvt_pk_bf16_f32 v183, v186, v187\n"
    "v_mfma_f32_16x16x32_bf16 v[80:83], a[168:171], v[140:143], v[80:83]\n"
    "s_waitcnt vmcnt(20)\n"
    "ds_read_b128 a[164:167], v241 offset:16384\n"
    "ds_write_addtid_b32 v189 offset:1280\n"
    "v_smfmac_f32_16x16x64_bf16 v[224:227], a[232:235], a[224:231], v238\n"
    "s_waitcnt lgkmcnt(10)\n"
    "ds_write_addtid_b32 v196 offset:2048\n"
    "v_mad_u32_u16 v234, v232, s90, v239 op_sel:[0,0,0,0]\n"
    "v_mfma_f32_16x16x32_bf16 v[100:103], a[176:179], v[140:143], v[100:103]\n"
    "s_bitcmp1_b32 s86, 14\n"
    "buffer_load_dwordx4 a[224:227], v234, s[20:23], 0 offen sc1\n"
    "v_mad_u32_u16 v235, v232, s90, v239 op_sel:[1,0,0,0]\n"
    "v_mfma_f32_16x16x32_bf16 v[100:103], a[180:183], v[144:147], v[100:103]\n"
    "s_cselect_b64 exec, -1, 0\n"
    "v_cvt_pk_bf16_f32 v236, v224, v225\n"
    "v_cvt_pk_bf16_f32 v237, v226, v227\n"
    "v_mfma_f32_16x16x32_bf16 v[104:107], a[180:183], v[152:155], v[104:107]\n"
    "s_cselect_b32 s88, 512, 0\n"
    "v_mov_b64_e32 v[224:225], 0\n"
    "v_mov_b64_e32 v[226:227], 0\n"
    "v_mfma_f32_16x16x32_bf16 v[104:107], a[176:179], v[148:151], v[104:107]\n"
    "buffer_store_dwordx2 v[236:237], v252, s[8:11], s87 offen sc1\n"
    "v_mfma_f32_16x16x32_bf16 v[108:111], a[176:179], v[156:159], v[108:111]\n"
    "s_mov_b64 exec, -1\n"
    "ds_read_b128 a[168:171], v240 offset:20480\n"
    "ds_write_addtid_b32 v197 offset:2304\n"
    "v_mfma_f32_16x16x32_bf16 v[108:111], a[180:183], v[160:163], v[108:111]\n"
    "s_add_u32 s87, s87, s88\n"
    "v_exp_f32_e32 v194, v194\n"
    "v_mfma_f32_16x16x32_bf16 v[112:115], a[180:183], v[168:171], v[112:115]\n"
    "buffer_load_dwordx4 a[228:231], v235, s[20:23], 0 offen sc1\n"
    "ds_write_addtid_b32 v204 offset:3072\n"
    "v_mfma_f32_16x16x32_bf16 v[112:115], a[176:179], v[164:167], v[112:115]\n"
    "ds_write_addtid_b32 v205 offset:3328\n"
    "v_mfma_f32_16x16x32_bf16 v[116:119], a[176:179], v[172:175], v[116:119]\n"
    "v_exp_f32_e32 v195, v195\n"
    "v_mfma_f32_16x16x32_bf16 v[116:119], a[180:183], v[176:179], v[116:119]\n"
    "s_waitcnt lgkmcnt(11)\n"
    "ds_read_b128 a[172:175], v241 offset:20480\n"
    "v_cvt_pk_bf16_f32 v190, v192, v193\n"
    "v_mfma_f32_16x16x32_bf16 v[136:139], a[188:191], v[176:179], v[136:139]\n"
    "v_exp_f32_e32 v200, v200\n"
    "v_mfma_f32_16x16x32_bf16 v[136:139], a[184:187], v[172:175], v[136:139]\n"
    "v_exp_f32_e32 v201, v201\n"
    "v_mfma_f32_16x16x32_bf16 v[132:135], a[184:187], v[164:167], v[132:135]\n"
    "ds_read_b128 a[176:179], v242 offset:16384\n"
    "v_cvt_pk_bf16_f32 v191, v194, v195\n"
    "v_mfma_f32_16x16x32_bf16 v[132:135], a[188:191], v[168:171], v[132:135]\n"
    "v_exp_f32_e32 v202, v202\n"
    "v_mfma_f32_16x16x32_bf16 v[128:131], a[188:191], v[160:163], v[128:131]\n"
    "v_exp_f32_e32 v203, v203\n"
    "v_mfma_f32_16x16x32_bf16 v[128:131], a[184:187], v[156:159], v[128:131]\n"
    "ds_write_addtid_b32 v212 offset:4096\n"
    "v_cvt_pk_bf16_f32 v198, v200, v201\n"
    "v_mfma_f32_16x16x32_bf16 v[124:127], a[184:187], v[148:151], v[124:127]\n"
    "v_exp_f32_e32 v208, v208\n"
    "v_mfma_f32_16x16x32_bf16 v[124:127], a[188:191], v[152:155], v[124:127]\n"
    "ds_read_b128 a[180:183], v243 offset:16384\n"
    "ds_write_addtid_b32 v213 offset:4352\n"
    "v_mfma_f32_16x16x32_bf16 v[120:123], a[188:191], v[144:147], v[120:123]\n"
    "v_exp_f32_e32 v209, v209\n"
    "v_mfma_f32_16x16x32_bf16 v[120:123], a[184:187], v[140:143], v[120:123]\n"
    "s_waitcnt lgkmcnt(11)\n"
    "ds_write_addtid_b32 v182 offset:512\n"
    "v_cvt_pk_bf16_f32 v199, v202, v203\n"
    "v_mfma_f32_16x16x32_bf16 v[172:175], a[160:163], a[128:131], v[220:223]\n"
    "ds_write_addtid_b32 v183 offset:768\n"
    "v_mfma_f32_16x16x32_bf16 v[172:175], a[164:167], a[132:135], v[172:175]\n"
    "v_exp_f32_e32 v210, v210\n"
    "v_mfma_f32_16x16x32_bf16 v[164:167], a[164:167], a[100:103], v[220:223]\n"
    "ds_write_addtid_b32 v190 offset:1536\n"
    "v_mfma_f32_16x16x32_bf16 v[164:167], a[160:163], a[96:99], v[164:167]\n"
    "ds_read_b128 a[184:187], v242 offset:20480\n"
    "v_mfma_f32_16x16x32_bf16 v[156:159], a[160:163], a[64:67], v[220:223]\n"
    "v_exp_f32_e32 v211, v211\n"
    "v_mfma_f32_16x16x32_bf16 v[156:159], a[164:167], a[68:71], v[156:159]\n"
    "ds_write_addtid_b32 v191 offset:1792\n"
    "v_cvt_pk_bf16_f32 v206, v208, v209\n"
    "v_mfma_f32_16x16x32_bf16 v[148:151], a[164:167], a[36:39], v[220:223]\n"
    "v_exp_f32_e32 v216, v216\n"
    "v_mfma_f32_16x16x32_bf16 v[148:151], a[160:163], a[32:35], v[148:151]\n"
    "v_exp_f32_e32 v217, v217\n"
    "v_mfma_f32_16x16x32_bf16 v[140:143], a[160:163], a[0:3], v[220:223]\n"
    "ds_write_addtid_b32 v198 offset:2560\n"
    "v_cvt_pk_bf16_f32 v207, v210, v211\n"
    "v_mfma_f32_16x16x32_bf16 v[140:143], a[164:167], a[4:7], v[140:143]\n"
    "s_waitcnt vmcnt(20)\n"
    "v_exp_f32_e32 v218, v218\n"
    "v_smfmac_f32_16x16x64_bf16 v[224:227], a[232:235], a[192:199], v238\n"
    "s_waitcnt lgkmcnt(10)\n"
    "ds_read_b128 a[188:191], v243 offset:20480\n"
    "v_mfma_f32_16x16x32_bf16 v[140:143], a[168:171], a[8:11], v[140:143]\n"
    "s_bitcmp1_b32 s86, 15\n"
    "ds_read_b128 a[160:163], v240 offset:18432\n"
    "v_mfma_f32_16x16x32_bf16 v[140:143], a[172:175], a[12:15], v[140:143]\n"
    "s_cselect_b64 exec, -1, 0\n"
    "v_cvt_pk_bf16_f32 v236, v224, v225\n"
    "v_cvt_pk_bf16_f32 v237, v226, v227\n"
    "v_mfma_f32_16x16x32_bf16 v[148:151], a[172:175], a[44:47], v[148:151]\n"
    "s_cselect_b32 s88, 512, 0\n"
    "v_mov_b64_e32 v[224:225], 0\n"
    "v_mov_b64_e32 v[226:227], 0\n"
    "v_mfma_f32_16x16x32_bf16 v[148:151], a[168:171], a[40:43], v[148:151]\n"
    "buffer_store_dwordx2 v[236:237], v252, s[8:11], s87 offen sc1\n"
    "v_mfma_f32_16x16x32_bf16 v[156:159], a[168:171], a[72:75], v[156:159]\n"
    "s_mov_b64 exec, -1\n"
    "ds_read2_b32 v[228:229], v251 offset0:30 offset1:32\n"
    "v_mfma_f32_16x16x32_bf16 v[156:159], a[172:175], a[76:79], v[156:159]\n"
    "s_add_u32 s87, s87, s88\n"
    "ds_read2_b32 v[230:231], v251 offset0:34 offset1:36\n"
    "v_mfma_f32_16x16x32_bf16 v[164:167], a[172:175], a[108:111], v[164:167]\n"
    "ds_read_b32 v232, v251 offset:152\n"
    "v_mfma_f32_16x16x32_bf16 v[164:167], a[168:171], a[104:107], v[164:167]\n"
    "v_exp_f32_e32 v219, v219\n"
    "v_mfma_f32_16x16x32_bf16 v[172:175], a[168:171], a[136:139], v[172:175]\n"
    "ds_write_addtid_b32 v199 offset:2816\n"
    "v_cvt_pk_bf16_f32 v214, v216, v217\n"
    "v_mfma_f32_16x16x32_bf16 v[172:175], a[172:175], a[140:143], v[172:175]\n"
    "s_waitcnt lgkmcnt(13)\n"
    "ds_read_b128 a[164:167], v241 offset:18432\n"
    "v_mfma_f32_16x16x32_bf16 v[172:175], a[176:179], a[144:147], v[172:175]\n"
    "ds_write_addtid_b32 v206 offset:3584\n"
    "v_cvt_pk_bf16_f32 v215, v218, v219\n"
    "v_mfma_f32_16x16x32_bf16 v[172:175], a[180:183], a[148:151], v[172:175]\n"
    "ds_read_b128 a[168:171], v240 offset:22528\n"
    "v_mfma_f32_16x16x32_bf16 v[164:167], a[180:183], a[116:119], v[164:167]\n"
    "s_waitcnt lgkmcnt(6)\n"
    "ds_write_addtid_b32 v207 offset:3840\n"
    "v_mad_u32_u16 v234, v228, s90, v239 op_sel:[0,0,0,0]\n"
    "v_mfma_f32_16x16x32_bf16 v[164:167], a[176:179], a[112:115], v[164:167]\n"
    "buffer_load_dwordx4 a[192:195], v234, s[20:23], 0 offen sc1\n"
    "v_mad_u32_u16 v235, v228, s90, v239 op_sel:[1,0,0,0]\n"
    "v_mfma_f32_16x16x32_bf16 v[156:159], a[176:179], a[80:83], v[156:159]\n"
    "ds_write_addtid_b32 v214 offset:4608\n"
    "v_add_u32_e32 v251, 160, v251\n"
    "v_mfma_f32_16x16x32_bf16 v[156:159], a[180:183], a[84:87], v[156:159]\n"
    "ds_write_addtid_b32 v215 offset:4864\n"
    "v_mfma_f32_16x16x32_bf16 v[148:151], a[180:183], a[52:55], v[148:151]\n"
    "buffer_load_dwordx4 a[196:199], v235, s[20:23], 0 offen sc1\n"
    "v_mfma_f32_16x16x32_bf16 v[148:151], a[176:179], a[48:51], v[148:151]\n"
    "v_mfma_f32_16x16x32_bf16 v[140:143], a[176:179], a[16:19], v[140:143]\n"
    "s_waitcnt lgkmcnt(0)\n"
    "v_mfma_f32_16x16x32_bf16 v[140:143], a[180:183], a[20:23], v[140:143]\n"
    "s_barrier\n"
    "v_mfma_f32_16x16x32_bf16 v[140:143], a[184:187], a[24:27], v[140:143]\n"
    "s_add_u32 m0, s43, 0\n"
    "ds_read_b128 a[172:175], v241 offset:22528\n"
    "v_mfma_f32_16x16x32_bf16 v[140:143], a[188:191], a[28:31], v[140:143]\n"
    "ds_read2st64_b32 v[184:185], v249 offset0:0 offset1:1\n"
    "v_mfma_f32_16x16x32_bf16 v[148:151], a[188:191], a[60:63], v[148:151]\n"
    "buffer_load_dwordx4 v248, s[8:11], s40 offen offset:0 sc1 lds\n"
    "v_mfma_f32_16x16x32_bf16 v[148:151], a[184:187], a[56:59], v[148:151]\n"
    "ds_read_b128 a[176:179], v242 offset:18432\n"
    "v_mfma_f32_16x16x32_bf16 v[156:159], a[184:187], a[88:91], v[156:159]\n"
    "v_mfma_f32_16x16x32_bf16 v[156:159], a[188:191], a[92:95], v[156:159]\n"
    "v_mfma_f32_16x16x32_bf16 v[164:167], a[188:191], a[124:127], v[164:167]\n"
    "buffer_load_dwordx4 v248, s[8:11], s40 offen offset:1024 sc1 lds\n"
    "v_mfma_f32_16x16x32_bf16 v[164:167], a[184:187], a[120:123], v[164:167]\n"
    "ds_read2st64_b32 v[186:187], v249 offset0:2 offset1:3\n"
    "v_mfma_f32_16x16x32_bf16 v[172:175], a[184:187], a[152:155], v[172:175]\n"
    "v_mfma_f32_16x16x32_bf16 v[172:175], a[188:191], a[156:159], v[172:175]\n"
    "ds_read_b128 a[180:183], v243 offset:18432\n"
    "v_mfma_f32_16x16x32_bf16 v[176:179], a[160:163], a[128:131], v[220:223]\n"
    "buffer_load_dwordx4 v248, s[8:11], s40 offen offset:2048 sc1 lds\n"
    "v_mfma_f32_16x16x32_bf16 v[176:179], a[164:167], a[132:135], v[176:179]\n"
    "ds_read2st64_b32 v[192:193], v249 offset0:4 offset1:5\n"
    "v_mfma_f32_16x16x32_bf16 v[168:171], a[164:167], a[100:103], v[220:223]\n"
    "v_exp_f32_e32 v140, v140\n"
    "v_mfma_f32_16x16x32_bf16 v[168:171], a[160:163], a[96:99], v[168:171]\n"
    "ds_read_b128 a[184:187], v242 offset:22528\n"
    "v_mfma_f32_16x16x32_bf16 v[160:163], a[160:163], a[64:67], v[220:223]\n"
    "buffer_load_dwordx4 v248, s[8:11], s40 offen offset:3072 sc1 lds\n"
    "v_mfma_f32_16x16x32_bf16 v[160:163], a[164:167], a[68:71], v[160:163]\n"
    "s_add_u32 m0, s43, 4096\n"
    "ds_read2st64_b32 v[194:195], v249 offset0:6 offset1:7\n"
    "v_mfma_f32_16x16x32_bf16 v[152:155], a[164:167], a[36:39], v[220:223]\n"
    "s_add_u32 s40, s40, 0x8000\n"
    "ds_read2st64_b32 v[200:201], v249 offset0:8 offset1:9\n"
    "v_mfma_f32_16x16x32_bf16 v[152:155], a[160:163], a[32:35], v[152:155]\n"
    "ds_read2st64_b32 v[202:203], v249 offset0:10 offset1:11\n"
    "v_mfma_f32_16x16x32_bf16 v[144:147], a[160:163], a[0:3], v[220:223]\n"
    "buffer_load_dwordx4 v248, s[8:11], s41 offen offset:0 sc1 lds\n"
    "v_mfma_f32_16x16x32_bf16 v[144:147], a[164:167], a[4:7], v[144:147]\n"
    "s_waitcnt vmcnt(17)\n"
    "ds_read_b128 a[188:191], v243 offset:22528\n"
    "v_smfmac_f32_16x16x64_bf16 v[224:227], a[232:235], a[200:207], v238\n"
    "s_waitcnt lgkmcnt(10)\n"
    "v_mad_u32_u16 v234, v229, s90, v239 op_sel:[0,0,0,0]\n"
    "v_mfma_f32_16x16x32_bf16 v[144:147], a[168:171], a[8:11], v[144:147]\n"
    "s_bitcmp1_b32 s86, 16\n"
    "buffer_load_dwordx4 a[200:203], v234, s[20:23], 0 offen sc1\n"
    "v_mad_u32_u16 v235, v229, s90, v239 op_sel:[1,0,0,0]\n"
    "v_mfma_f32_16x16x32_bf16 v[144:147], a[172:175], a[12:15], v[144:147]\n"
    "s_cselect_b64 exec, -1, 0\n"
    "v_cvt_pk_bf16_f32 v236, v224, v225\n"
    "v_cvt_pk_bf16_f32 v237, v226, v227\n"
    "v_mfma_f32_16x16x32_bf16 v[152:155], a[172:175], a[44:47], v[152:155]\n"
    "s_cselect_b32 s88, 512, 0\n"
    "v_mov_b64_e32 v[224:225], 0\n"
    "v_mov_b64_e32 v[226:227], 0\n"
    "v_mfma_f32_16x16x32_bf16 v[152:155], a[168:171], a[40:43], v[152:155]\n"
    "buffer_store_dwordx2 v[236:237], v252, s[8:11], s87 offen sc1\n"
    "v_mfma_f32_16x16x32_bf16 v[160:163], a[168:171], a[72:75], v[160:163]\n"
    "s_mov_b64 exec, -1\n"
    "ds_read_b64_tr_b16 a[160:161], v244 offset:8192\n"
    "v_mfma_f32_16x16x32_bf16 v[160:163], a[172:175], a[76:79], v[160:163]\n"
    "s_add_u32 s87, s87, s88\n"
    "ds_read_b64_tr_b16 a[162:163], v244 offset:10240\n"
    "v_mfma_f32_16x16x32_bf16 v[168:171], a[172:175], a[108:111], v[168:171]\n"
    "buffer_load_dwordx4 v248, s[8:11], s41 offen offset:1024 sc1 lds\n"
    "v_mfma_f32_16x16x32_bf16 v[168:171], a[168:171], a[104:107], v[168:171]\n"
    "ds_read_b64_tr_b16 a[164:165], v244 offset:40960\n"
    "v_mfma_f32_16x16x32_bf16 v[176:179], a[168:171], a[136:139], v[176:179]\n"
    "ds_read2st64_b32 v[208:209], v249 offset0:12 offset1:13\n"
    "v_mfma_f32_16x16x32_bf16 v[176:179], a[172:175], a[140:143], v[176:179]\n"
    "s_waitcnt lgkmcnt(10)\n"
    "ds_read2st64_b32 v[210:211], v249 offset0:14 offset1:15\n"
    "v_mfma_f32_16x16x32_bf16 v[176:179], a[176:179], a[144:147], v[176:179]\n"
    "buffer_load_dwordx4 v248, s[8:11], s41 offen offset:2048 sc1 lds\n"
    "v_mfma_f32_16x16x32_bf16 v[176:179], a[180:183], a[148:151], v[176:179]\n"
    "ds_read_b64_tr_b16 a[166:167], v244 offset:43008\n"
    "v_mfma_f32_16x16x32_bf16 v[168:171], a[180:183], a[116:119], v[168:171]\n"
    "ds_read_b64_tr_b16 a[168:169], v245 offset:8192\n"
    "v_mfma_f32_16x16x32_bf16 v[168:171], a[176:179], a[112:115], v[168:171]\n"
    "v_exp_f32_e32 v141, v141\n"
    "v_mfma_f32_16x16x32_bf16 v[160:163], a[176:179], a[80:83], v[160:163]\n"
    "buffer_load_dwordx4 v248, s[8:11], s41 offen offset:3072 sc1 lds\n"
    "v_mfma_f32_16x16x32_bf16 v[160:163], a[180:183], a[84:87], v[160:163]\n"
    "ds_read_b64_tr_b16 a[170:171], v245 offset:10240\n"
    "v_mfma_f32_16x16x32_bf16 v[152:155], a[180:183], a[52:55], v[152:155]\n"
    "s_add_u32 s41, s41, 0x8000\n"
    "ds_read2st64_b32 v[216:217], v249 offset0:16 offset1:17\n"
    "v_mfma_f32_16x16x32_bf16 v[152:155], a[176:179], a[48:51], v[152:155]\n"
    "buffer_load_dwordx4 a[204:207], v235, s[20:23], 0 offen sc1\n"
    "v_mfma_f32_16x16x32_bf16 v[144:147], a[176:179], a[16:19], v[144:147]\n"
    "ds_read_b64_tr_b16 a[172:173], v245 offset:40960\n"
    "v_mfma_f32_16x16x32_bf16 v[144:147], a[180:183], a[20:23], v[144:147]\n"
    "s_waitcnt lgkmcnt(10)\n"
    "ds_read2st64_b32 v[218:219], v249 offset0:18 offset1:19\n"
    "v_mfma_f32_16x16x32_bf16 v[144:147], a[184:187], a[24:27], v[144:147]\n"
    "ds_read_b64_tr_b16 a[176:177], v246 offset:8192\n"
    "v_mfma_f32_16x16x32_bf16 v[144:147], a[188:191], a[28:31], v[144:147]\n"
    "ds_read_b64_tr_b16 a[174:175], v245 offset:43008\n"
    "v_mfma_f32_16x16x32_bf16 v[152:155], a[188:191], a[60:63], v[152:155]\n"
    "v_exp_f32_e32 v142, v142\n"
    "v_mfma_f32_16x16x32_bf16 v[152:155], a[184:187], a[56:59], v[152:155]\n"
    "v_exp_f32_e32 v143, v143\n"
    "v_mfma_f32_16x16x32_bf16 v[160:163], a[184:187], a[88:91], v[160:163]\n"
    "ds_read_b64_tr_b16 a[178:179], v246 offset:10240\n"
    "v_cvt_pk_bf16_f32 v140, v140, v141\n"
    "v_mfma_f32_16x16x32_bf16 v[160:163], a[188:191], a[92:95], v[160:163]\n"
    "v_exp_f32_e32 v148, v148\n"
    "v_mfma_f32_16x16x32_bf16 v[168:171], a[188:191], a[124:127], v[168:171]\n"
    "v_exp_f32_e32 v149, v149\n"
    "v_mfma_f32_16x16x32_bf16 v[168:171], a[184:187], a[120:123], v[168:171]\n"
    "v_cvt_pk_bf16_f32 v141, v142, v143\n"
    "v_mfma_f32_16x16x32_bf16 v[176:179], a[184:187], a[152:155], v[176:179]\n"
    "ds_read_b64_tr_b16 a[180:181], v246 offset:40960\n"
    "v_mfma_f32_16x16x32_bf16 v[176:179], a[188:191], a[156:159], v[176:179]\n"
    "s_waitcnt vmcnt(20)\n"
    "ds_read_b64_tr_b16 a[182:183], v246 offset:43008\n"
    "v_smfmac_f32_16x16x64_bf16 v[224:227], a[232:235], a[208:215], v238\n"
    "s_waitcnt lgkmcnt(5)\n"
    "v_mad_u32_u16 v234, v230, s90, v239 op_sel:[0,0,0,0]\n"
    "v_mfma_f32_16x16x32_bf16 a[236:239], a[160:163], v[180:183], a[236:239]\n"
    "s_bitcmp1_b32 s86, 17\n"
    "buffer_load_dwordx4 a[208:211], v234, s[20:23], 0 offen sc1\n"
    "v_mad_u32_u16 v235, v230, s90, v239 op_sel:[1,0,0,0]\n"
    "v_mfma_f32_16x16x32_bf16 a[236:239], a[164:167], v[184:187], a[236:239]\n"
    "s_cselect_b64 exec, -1, 0\n"
    "v_cvt_pk_bf16_f32 v236, v224, v225\n"
    "v_cvt_pk_bf16_f32 v237, v226, v227\n"
    "v_mfma_f32_16x16x32_bf16 a[240:243], a[164:167], v[192:195], a[240:243]\n"
    "s_cselect_b32 s88, 512, 0\n"
    "v_mov_b64_e32 v[224:225], 0\n"
    "v_mov_b64_e32 v[226:227], 0\n"
    "v_mfma_f32_16x16x32_bf16 a[240:243], a[160:163], v[188:191], a[240:243]\n"
    "buffer_store_dwordx2 v[236:237], v252, s[8:11], s87 offen sc1\n"
    "v_mfma_f32_16x16x32_bf16 a[244:247], a[160:163], v[196:199], a[244:247]\n"
    "s_mov_b64 exec, -1\n"
    "ds_read_b64_tr_b16 a[184:185], v247 offset:8192\n"
    "v_mfma_f32_16x16x32_bf16 a[244:247], a[164:167], v[200:203], a[244:247]\n"
    "s_add_u32 s87, s87, s88\n"
    "ds_read_b64_tr_b16 a[186:187], v247 offset:10240\n"
    "v_mfma_f32_16x16x32_bf16 a[248:251], a[164:167], v[208:211], a[248:251]\n"
    "buffer_load_dwordx4 a[212:215], v235, s[20:23], 0 offen sc1\n"
    "v_mfma_f32_16x16x32_bf16 a[248:251], a[160:163], v[204:207], a[248:251]\n"
    "ds_read_b64_tr_b16 a[188:189], v247 offset:40960\n"
    "v_mfma_f32_16x16x32_bf16 a[252:255], a[160:163], v[212:215], a[252:255]\n"
    "v_exp_f32_e32 v150, v150\n"
    "v_mfma_f32_16x16x32_bf16 a[252:255], a[164:167], v[216:219], a[252:255]\n"
    "s_waitcnt lgkmcnt(6)\n"
    "v_exp_f32_e32 v151, v151\n"
    "v_mfma_f32_16x16x32_bf16 v[16:19], a[172:175], v[216:219], v[16:19]\n"
    "ds_read_b64_tr_b16 a[190:191], v247 offset:43008\n"
    "v_cvt_pk_bf16_f32 v148, v148, v149\n"
    "v_mfma_f32_16x16x32_bf16 v[16:19], a[168:171], v[212:215], v[16:19]\n"
    "ds_read_b64_tr_b16 a[160:161], v244 offset:12288\n"
    "v_mfma_f32_16x16x32_bf16 v[12:15], a[168:171], v[204:207], v[12:15]\n"
    "v_exp_f32_e32 v156, v156\n"
    "v_mfma_f32_16x16x32_bf16 v[12:15], a[172:175], v[208:211], v[12:15]\n"
    "v_exp_f32_e32 v157, v157\n"
    "v_mfma_f32_16x16x32_bf16 v[8:11], a[172:175], v[200:203], v[8:11]\n"
    "ds_read_b64_tr_b16 a[162:163], v244 offset:14336\n"
    "v_cvt_pk_bf16_f32 v149, v150, v151\n"
    "v_mfma_f32_16x16x32_bf16 v[8:11], a[168:171], v[196:199], v[8:11]\n"
    "v_exp_f32_e32 v158, v158\n"
    "v_mfma_f32_16x16x32_bf16 v[4:7], a[168:171], v[188:191], v[4:7]\n"
    "v_exp_f32_e32 v159, v159\n"
    "v_mfma_f32_16x16x32_bf16 v[4:7], a[172:175], v[192:195], v[4:7]\n"
    "v_cvt_pk_bf16_f32 v156, v156, v157\n"
    "v_mfma_f32_16x16x32_bf16 v[0:3], a[172:175], v[184:187], v[0:3]\n"
    "ds_read_b64_tr_b16 a[164:165], v244 offset:45056\n"
    "v_mfma_f32_16x16x32_bf16 v[0:3], a[168:171], v[180:183], v[0:3]\n"
    "s_waitcnt lgkmcnt(7)\n"
    "v_exp_f32_e32 v164, v164\n"
    "v_mfma_f32_16x16x32_bf16 v[20:23], a[176:179], v[180:183], v[20:23]\n"
    "ds_read_b64_tr_b16 a[168:169], v245 offset:12288\n"
    "v_mfma_f32_16x16x32_bf16 v[20:23], a[180:183], v[184:187], v[20:23]\n"
    "ds_read_b64_tr_b16 a[166:167], v244 offset:47104\n"
    "v_mfma_f32_16x16x32_bf16 v[24:27], a[180:183], v[192:195], v[24:27]\n"
    "v_exp_f32_e32 v165, v165\n"
    "v_mfma_f32_16x16x32_bf16 v[24:27], a[176:179], v[188:191], v[24:27]\n"
    "v_cvt_pk_bf16_f32 v157, v158, v159\n"
    "v_mfma_f32_16x16x32_bf16 v[28:31], a[176:179], v[196:199], v[28:31]\n"
    "ds_read_b64_tr_b16 a[170:171], v245 offset:14336\n"
    "v_mfma_f32_16x16x32_bf16 v[28:31], a[180:183], v[200:203], v[28:31]\n"
    "v_exp_f32_e32 v166, v166\n"
    "v_mfma_f32_16x16x32_bf16 v[32:35], a[180:183], v[208:211], v[32:35]\n"
    "v_exp_f32_e32 v167, v167\n"
    "v_mfma_f32_16x16x32_bf16 v[32:35], a[176:179], v[204:207], v[32:35]\n"
    "v_cvt_pk_bf16_f32 v164, v164, v165\n"
    "v_mfma_f32_16x16x32_bf16 v[36:39], a[176:179], v[212:215], v[36:39]\n"
    "ds_read_b64_tr_b16 a[172:173], v245 offset:45056\n"
    "v_mfma_f32_16x16x32_bf16 v[36:39], a[180:183], v[216:219], v[36:39]\n"
    "s_waitcnt vmcnt(20)\n"
    "ds_read_b64_tr_b16 a[174:175], v245 offset:47104\n"
    "v_smfmac_f32_16x16x64_bf16 v[224:227], a[232:235], a[216:223], v238\n"
    "s_waitcnt lgkmcnt(8)\n"
    "v_mad_u32_u16 v234, v231, s90, v239 op_sel:[0,0,0,0]\n"
    "v_mfma_f32_16x16x32_bf16 v[56:59], a[188:191], v[216:219], v[56:59]\n"
    "s_bitcmp1_b32 s86, 18\n"
    "buffer_load_dwordx4 a[216:219], v234, s[20:23], 0 offen sc1\n"
    "v_mad_u32_u16 v235, v231, s90, v239 op_sel:[1,0,0,0]\n"
    "v_mfma_f32_16x16x32_bf16 v[56:59], a[184:187], v[212:215], v[56:59]\n"
    "s_cselect_b64 exec, -1, 0\n"
    "v_cvt_pk_bf16_f32 v236, v224, v225\n"
    "v_cvt_pk_bf16_f32 v237, v226, v227\n"
    "v_mfma_f32_16x16x32_bf16 v[52:55], a[184:187], v[204:207], v[52:55]\n"
    "s_cselect_b32 s88, 512, 0\n"
    "v_mov_b64_e32 v[224:225], 0\n"
    "v_mov_b64_e32 v[226:227], 0\n"
    "v_mfma_f32_16x16x32_bf16 v[52:55], a[188:191], v[208:211], v[52:55]\n"
    "buffer_store_dwordx2 v[236:237], v252, s[8:11], s87 offen sc1\n"
    "v_mfma_f32_16x16x32_bf16 v[48:51], a[188:191], v[200:203], v[48:51]\n"
    "s_mov_b64 exec, -1\n"
    "ds_read_b64_tr_b16 a[176:177], v246 offset:12288\n"
    "v_mfma_f32_16x16x32_bf16 v[48:51], a[184:187], v[196:199], v[48:51]\n"
    "s_add_u32 s87, s87, s88\n"
    "ds_read_b64_tr_b16 a[178:179], v246 offset:14336\n"
    "v_mfma_f32_16x16x32_bf16 v[44:47], a[184:187], v[188:191], v[44:47]\n"
    "buffer_load_dwordx4 a[220:223], v235, s[20:23], 0 offen sc1\n"
    "v_mfma_f32_16x16x32_bf16 v[44:47], a[188:191], v[192:195], v[44:47]\n"
    "ds_read_b64_tr_b16 a[180:181], v246 offset:45056\n"
    "v_mfma_f32_16x16x32_bf16 v[40:43], a[188:191], v[184:187], v[40:43]\n"
    "v_exp_f32_e32 v172, v172\n"
    "v_mfma_f32_16x16x32_bf16 v[40:43], a[184:187], v[180:183], v[40:43]\n"
    "s_waitcnt lgkmcnt(6)\n"
    "v_exp_f32_e32 v173, v173\n"
    "v_mfma_f32_16x16x32_bf16 v[60:63], a[160:163], v[180:183], v[60:63]\n"
    "ds_read_b64_tr_b16 a[182:183], v246 offset:47104\n"
    "v_cvt_pk_bf16_f32 v165, v166, v167\n"
    "v_mfma_f32_16x16x32_bf16 v[60:63], a[164:167], v[184:187], v[60:63]\n"
    "ds_read_b64_tr_b16 a[184:185], v247 offset:12288\n"
    "v_mfma_f32_16x16x32_bf16 v[64:67], a[164:167], v[192:195], v[64:67]\n"
    "ds_read_b64_tr_b16 a[186:187], v247 offset:14336\n"
    "v_mfma_f32_16x16x32_bf16 v[64:67], a[160:163], v[188:191], v[64:67]\n"
    "v_exp_f32_e32 v174, v174\n"
    "v_mfma_f32_16x16x32_bf16 v[68:71], a[160:163], v[196:199], v[68:71]\n"
    "v_exp_f32_e32 v175, v175\n"
    "v_mfma_f32_16x16x32_bf16 v[68:71], a[164:167], v[200:203], v[68:71]\n"
    "ds_read_b64_tr_b16 a[188:189], v247 offset:45056\n"
    "v_cvt_pk_bf16_f32 v172, v172, v173\n"
    "v_mfma_f32_16x16x32_bf16 v[72:75], a[164:167], v[208:211], v[72:75]\n"
    "ds_read_b64_tr_b16 a[190:191], v247 offset:47104\n"
    "v_cvt_pk_bf16_f32 v173, v174, v175\n"
    "v_mfma_f32_16x16x32_bf16 v[72:75], a[160:163], v[204:207], v[72:75]\n"
    "s_waitcnt vmcnt(30)\n"
    "v_mfma_f32_16x16x32_bf16 v[76:79], a[160:163], v[212:215], v[76:79]\n"
    "s_barrier\n"
    "v_mfma_f32_16x16x32_bf16 v[76:79], a[164:167], v[216:219], v[76:79]\n"
    "s_waitcnt lgkmcnt(8)\n"
    "s_mov_b32 m0, s42\n"
    "v_mfma_f32_16x16x32_bf16 v[96:99], a[172:175], v[216:219], v[96:99]\n"
    "v_exp_f32_e32 v144, v144\n"
    "v_mfma_f32_16x16x32_bf16 v[96:99], a[168:171], v[212:215], v[96:99]\n"
    "v_exp_f32_e32 v145, v145\n"
    "v_mfma_f32_16x16x32_bf16 v[92:95], a[168:171], v[204:207], v[92:95]\n"
    "v_exp_f32_e32 v146, v146\n"
    "v_mfma_f32_16x16x32_bf16 v[92:95], a[172:175], v[208:211], v[92:95]\n"
    "ds_read_b128 a[160:163], v240 offset:24576\n"
    "ds_write_addtid_b32 v140 offset:0\n"
    "v_mfma_f32_16x16x32_bf16 v[88:91], a[172:175], v[200:203], v[88:91]\n"
    "v_exp_f32_e32 v147, v147\n"
    "v_mfma_f32_16x16x32_bf16 v[88:91], a[168:171], v[196:199], v[88:91]\n"
    "ds_write_addtid_b32 v141 offset:256\n"
    "v_cvt_pk_bf16_f32 v142, v144, v145\n"
    "v_mfma_f32_16x16x32_bf16 v[84:87], a[168:171], v[188:191], v[84:87]\n"
    "v_exp_f32_e32 v152, v152\n"
    "v_mfma_f32_16x16x32_bf16 v[84:87], a[172:175], v[192:195], v[84:87]\n"
    "v_exp_f32_e32 v153, v153\n"
    "v_mfma_f32_16x16x32_bf16 v[80:83], a[172:175], v[184:187], v[80:83]\n"
    "ds_write_addtid_b32 v148 offset:1024\n"
    "v_cvt_pk_bf16_f32 v143, v146, v147\n"
    "v_mfma_f32_16x16x32_bf16 v[80:83], a[168:171], v[180:183], v[80:83]\n"
    "s_waitcnt vmcnt(20)\n"
    "ds_read_b128 a[164:167], v241 offset:24576\n"
    "ds_write_addtid_b32 v149 offset:1280\n"
    "v_smfmac_f32_16x16x64_bf16 v[224:227], a[232:235], a[224:231], v238\n"
    "s_waitcnt lgkmcnt(10)\n"
    "ds_write_addtid_b32 v156 offset:2048\n"
    "v_mad_u32_u16 v234, v232, s90, v239 op_sel:[0,0,0,0]\n"
    "v_mfma_f32_16x16x32_bf16 v[100:103], a[176:179], v[180:183], v[100:103]\n"
    "s_bitcmp1_b32 s86, 19\n"
    "buffer_load_dwordx4 a[224:227], v234, s[20:23], 0 offen sc1\n"
    "v_mad_u32_u16 v235, v232, s90, v239 op_sel:[1,0,0,0]\n"
    ".long 0xbedf00ff\n"
    ".Lat_w8:\n"
    "s_setpc_b64 s[2:3]\n"
    "v_mfma_f32_16x16x32_bf16 v[100:103], a[180:183], v[184:187], v[100:103]\n"
    "s_cselect_b64 exec, -1, 0\n"
    "v_cvt_pk_bf16_f32 v236, v224, v225\n"
    "v_cvt_pk_bf16_f32 v237, v226, v227\n"
    "v_mfma_f32_16x16x32_bf16 v[104:107], a[180:183], v[192:195], v[104:107]\n"
    "s_cselect_b32 s88, 512, 0\n"
    "v_mov_b64_e32 v[224:225], 0\n"
    "v_mov_b64_e32 v[226:227], 0\n"
    "v_mfma_f32_16x16x32_bf16 v[104:107], a[176:179], v[188:191], v[104:107]\n"
    "buffer_store_dwordx2 v[236:237], v252, s[8:11], s87 offen sc1\n"
    "v_mfma_f32_16x16x32_bf16 v[108:111], a[176:179], v[196:199], v[108:111]\n"
    "s_mov_b64 exec, -1\n"
    "ds_read_b128 a[168:171], v240 offset:28672\n"
    "ds_write_addtid_b32 v157 offset:2304\n"
    "v_mfma_f32_16x16x32_bf16 v[108:111], a[180:183], v[200:203], v[108:111]\n"
    "s_add_u32 s87, s87, s88\n"
    "v_exp_f32_e32 v154, v154\n"
    "v_mfma_f32_16x16x32_bf16 v[112:115], a[180:183], v[208:211], v[112:115]\n"
    "s_add_u32 s89, s89, 1\n"
    "buffer_load_dwordx4 a[228:231], v235, s[20:23], 0 offen sc1\n"
    "ds_write_addtid_b32 v164 offset:3072\n"
    "v_mfma_f32_16x16x32_bf16 v[112:115], a[176:179], v[204:207], v[112:115]\n"
    "ds_write_addtid_b32 v165 offset:3328\n"
    "v_readlane_b32 s86, v254, s89\n"
    "v_mfma_f32_16x16x32_bf16 v[116:119], a[176:179], v[212:215], v[116:119]\n"
    "v_exp_f32_e32 v155, v155\n"
    "v_mfma_f32_16x16x32_bf16 v[116:119], a[180:183], v[216:219], v[116:119]\n"
    "s_waitcnt lgkmcnt(11)\n"
    "ds_read_b128 a[172:175], v241 offset:28672\n"
    "v_cvt_pk_bf16_f32 v150, v152, v153\n"
    "v_mfma_f32_16x16x32_bf16 v[136:139], a[188:191], v[216:219], v[136:139]\n"
    "v_exp_f32_e32 v160, v160\n"
    "v_mfma_f32_16x16x32_bf16 v[136:139], a[184:187], v[212:215], v[136:139]\n"
    "v_exp_f32_e32 v161, v161\n"
    "v_mfma_f32_16x16x32_bf16 v[132:135], a[184:187], v[204:207], v[132:135]\n"
    "ds_read_b128 a[176:179], v242 offset:24576\n"
    "v_cvt_pk_bf16_f32 v151, v154, v155\n"
    "v_mfma_f32_16x16x32_bf16 v[132:135], a[188:191], v[208:211], v[132:135]\n"
    "v_exp_f32_e32 v162, v162\n"
    "v_mfma_f32_16x16x32_bf16 v[128:131], a[188:191], v[200:203], v[128:131]\n"
    "v_exp_f32_e32 v163, v163\n"
    "v_mfma_f32_16x16x32_bf16 v[128:131], a[184:187], v[196:199], v[128:131]\n"
    "ds_write_addtid_b32 v172 offset:4096\n"
    "v_cvt_pk_bf16_f32 v158, v160, v161\n"
    "v_mfma_f32_16x16x32_bf16 v[124:127], a[184:187], v[188:191], v[124:127]\n"
    "v_exp_f32_e32 v168, v168\n"
    "v_mfma_f32_16x16x32_bf16 v[124:127], a[188:191], v[192:195], v[124:127]\n"
    "s_sub_u32 s44, s44, 1\n"
    "ds_read_b128 a[180:183], v243 offset:24576\n"
    "ds_write_addtid_b32 v173 offset:4352\n"
    "v_mfma_f32_16x16x32_bf16 v[120:123], a[188:191], v[184:187], v[120:123]\n"
    "s_cmp_lg_u32 s44, s80\n"
    "v_exp_f32_e32 v169, v169\n"
    "v_mfma_f32_16x16x32_bf16 v[120:123], a[184:187], v[180:183], v[120:123]\n"
    "s_waitcnt lgkmcnt(11)\n"
    "ds_write_addtid_b32 v142 offset:512\n"
    "v_cvt_pk_bf16_f32 v159, v162, v163\n"
    "s_cbranch_scc1 .Lat_gloop\n"
    ".Lat_g2:\n"
    "v_mov_b64_e32 v[224:225], v[220:221]\n"
    "v_mov_b64_e32 v[226:227], v[220:221]\n"
    "v_mov_b64_e32 v[228:229], v[220:221]\n"
    "v_mov_b64_e32 v[230:231], v[220:221]\n"
    "v_mov_b64_e32 v[232:233], v[220:221]\n"
    "v_mov_b64_e32 v[234:235], v[220:221]\n"
    "v_mov_b64_e32 v[236:237], v[220:221]\n"
    "v_mov_b64_e32 v[238:239], v[220:221]\n"
    "s_mov_b32 s80, 0\n"
    "s_waitcnt lgkmcnt(0)\n"
    "s_cmp_lg_u32 s44, 0\n"
    "s_cbranch_scc1 .Lat_loop\n"
    ".Lat_tail:\n"
    "v_mfma_f32_16x16x32_bf16 v[212:215], a[160:163], a[128:131], v[236:239]\n"
    ".long 0xbedf00ff\n"
    ".Lat_w9:\n"
    "s_setpc_b64 s[2:3]\n"
    "v_and_b32_e32 v189, 0x1f0, v250\n"
    "v_lshlrev_b32_e32 v190, 3, v250\n"
    "v_mfma_f32_16x16x32_bf16 v[212:215], a[164:167], a[132:135], v[212:215]\n"
    "ds_read_b128 a[184:187], v242 offset:28672\n"
    "v_lshrrev_b32_e32 v253, 4, v250\n"
    "v_mfma_f32_16x16x32_bf16 v[204:207], a[164:167], a[100:103], v[232:235]\n"
    "s_mov_b32 s52, 6100992\n"
    "ds_read_b128 a[188:191], v243 offset:28672\n"
    "v_bfe_u32 v251, v253, 4, 2\n"
    "v_mfma_f32_16x16x32_bf16 v[204:207], a[160:163], a[96:99], v[204:207]\n"
    "s_lshr_b32 s81, s34, 6\n"
    "v_lshlrev_b32_e32 v251, 1, v251\n"
    "v_xor_b32_e32 v251, v251, v253\n"
    "v_mfma_f32_16x16x32_bf16 v[196:199], a[160:163], a[64:67], v[228:231]\n"
    "s_lshl_b32 s81, s81, 9\n"
    "ds_write_addtid_b32 v143 offset:768\n"
    "v_and_b32_e32 v251, 7, v251\n"
    "v_mfma_f32_16x16x32_bf16 v[196:199], a[164:167], a[68:71], v[196:199]\n"
    "s_waitcnt lgkmcnt(9)\n"
    "v_exp_f32_e32 v170, v170\n"
    "v_mfma_f32_16x16x32_bf16 v[196:199], a[168:171], a[72:75], v[196:199]\n"
    "s_add_u32 s81, s81, 6074368\n"
    "v_exp_f32_e32 v171, v171\n"
    "v_mfma_f32_16x16x32_bf16 v[196:199], a[172:175], a[76:79], v[196:199]\n"
    "s_lshl_b32 s63, s93, 13\n"
    "ds_write_addtid_b32 v150 offset:1536\n"
    "v_cvt_pk_bf16_f32 v166, v168, v169\n"
    "v_mfma_f32_16x16x32_bf16 v[204:207], a[172:175], a[108:111], v[204:207]\n"
    "s_xor_b32 s45, s63, 0x4000\n"
    "ds_read_b128 a[160:163], v240 offset:26624\n"
    "v_lshlrev_b32_e32 v251, 4, v251\n"
    "v_mfma_f32_16x16x32_bf16 v[204:207], a[168:171], a[104:107], v[204:207]\n"
    "s_mul_i32 s58, s93, 33792\n"
    "v_lshrrev_b32_e32 v253, 3, v253\n"
    "v_lshl_add_u32 v251, v253, 9, v251\n"
    "v_mfma_f32_16x16x32_bf16 v[212:215], a[168:171], a[136:139], v[212:215]\n"
    "s_add_u32 s57, s58, 5513216\n"
    "v_bfe_u32 v255, v250, 2, 4\n"
    "v_mov_b32_e32 v254, s39\n"
    "v_mfma_f32_16x16x32_bf16 v[212:215], a[172:175], a[140:143], v[212:215]\n"
    "s_waitcnt lgkmcnt(6)\n"
    "v_exp_f32_e32 v176, v176\n"
    "v_mfma_f32_16x16x32_bf16 v[212:215], a[176:179], a[144:147], v[212:215]\n"
    "s_add_u32 s58, s58, 5378048\n"
    "ds_read_b128 a[168:171], v240 offset:30720\n"
    "v_lshrrev_b32_e32 v252, 2, v250\n"
    "v_mfma_f32_16x16x32_bf16 v[212:215], a[180:183], a[148:151], v[212:215]\n"
    "s_add_u32 s36, s35, 0x8000\n"
    "v_exp_f32_e32 v177, v177\n"
    "v_mfma_f32_16x16x32_bf16 v[204:207], a[180:183], a[116:119], v[204:207]\n"
    "s_add_u32 s37, s35, 0x10000\n"
    "ds_read_b128 a[164:167], v241 offset:26624\n"
    "v_cvt_pk_bf16_f32 v167, v170, v171\n"
    "v_mfma_f32_16x16x32_bf16 v[204:207], a[176:179], a[112:115], v[204:207]\n"
    "s_cmp_lt_u32 s32, 2\n"
    "s_cselect_b32 s37, s37, 2.0\n"
    "ds_write_addtid_b32 v151 offset:1792\n"
    "v_mfma_f32_16x16x32_bf16 v[196:199], a[176:179], a[80:83], v[196:199]\n"
    "buffer_load_dword v253, v255, s[8:11], s52 offen offset:0 sc1\n"
    "v_mfma_f32_16x16x32_bf16 v[196:199], a[180:183], a[84:87], v[196:199]\n"
    "s_waitcnt lgkmcnt(6)\n"
    "ds_write_addtid_b32 v158 offset:2560\n"
    "v_mfma_f32_16x16x32_bf16 v[196:199], a[184:187], a[88:91], v[196:199]\n"
    "ds_write_addtid_b32 v159 offset:2816\n"
    "ds_write_addtid_b32 v166 offset:3584\n"
    "v_mfma_f32_16x16x32_bf16 v[196:199], a[188:191], a[92:95], v[196:199]\n"
    "v_exp_f32_e32 v178, v178\n"
    "v_mfma_f32_16x16x32_bf16 v[204:207], a[188:191], a[124:127], v[204:207]\n"
    "v_exp_f32_e32 v179, v179\n"
    "v_mfma_f32_16x16x32_bf16 v[204:207], a[184:187], a[120:123], v[204:207]\n"
    "v_cvt_pk_bf16_f32 v174, v176, v177\n"
    "v_cvt_pk_bf16_f32 v175, v178, v179\n"
    "v_mfma_f32_16x16x32_bf16 v[212:215], a[184:187], a[152:155], v[212:215]\n"
    "ds_read_b128 a[172:175], v241 offset:30720\n"
    "ds_write_addtid_b32 v167 offset:3840\n"
    "v_mfma_f32_16x16x32_bf16 v[212:215], a[188:191], a[156:159], v[212:215]\n"
    "s_waitcnt lgkmcnt(6)\n"
    "ds_write_addtid_b32 v174 offset:4608\n"
    "ds_write_addtid_b32 v175 offset:4864\n"
    "v_mfma_f32_16x16x32_bf16 v[216:219], a[160:163], a[128:131], v[236:239]\n"
    "ds_read_b128 a[184:187], v242 offset:30720\n"
    "v_mfma_f32_16x16x32_bf16 v[216:219], a[164:167], a[132:135], v[216:219]\n"
    "ds_read_b128 a[176:179], v242 offset:26624\n"
    "v_mfma_f32_16x16x32_bf16 v[208:211], a[164:167], a[100:103], v[232:235]\n"
    "ds_read_b128 a[180:183], v243 offset:26624\n"
    "v_mfma_f32_16x16x32_bf16 v[208:211], a[160:163], a[96:99], v[208:211]\n"
    "s_waitcnt lgkmcnt(3)\n"
    "v_mfma_f32_16x16x32_bf16 v[200:203], a[160:163], a[64:67], v[228:231]\n"
    "s_barrier\n"
    "v_mfma_f32_16x16x32_bf16 v[200:203], a[164:167], a[68:71], v[200:203]\n"
    "ds_read2st64_b32 v[178:179], v249 offset0:18 offset1:19\n"
    "v_mfma_f32_16x16x32_bf16 v[200:203], a[168:171], a[72:75], v[200:203]\n"
    "s_add_u32 m0, s43, 8192\n"
    "ds_read_b128 a[188:191], v243 offset:30720\n"
    "ds_read2st64_b32 v[176:177], v249 offset0:16 offset1:17\n"
    "v_mfma_f32_16x16x32_bf16 v[200:203], a[172:175], a[76:79], v[200:203]\n"
    "buffer_load_dwordx4 v248, s[8:11], s40 offen offset:0 sc1 lds\n"
    "v_mfma_f32_16x16x32_bf16 v[208:211], a[172:175], a[108:111], v[208:211]\n"
    "ds_read_b64_tr_b16 a[160:161], v244 offset:16384\n"
    "ds_read2st64_b32 v[170:171], v249 offset0:14 offset1:15\n"
    "v_mfma_f32_16x16x32_bf16 v[208:211], a[168:171], a[104:107], v[208:211]\n"
    "ds_read2st64_b32 v[168:169], v249 offset0:12 offset1:13\n"
    "v_mfma_f32_16x16x32_bf16 v[216:219], a[168:171], a[136:139], v[216:219]\n"
    "ds_read2st64_b32 v[162:163], v249 offset0:10 offset1:11\n"
    "v_mfma_f32_16x16x32_bf16 v[216:219], a[172:175], a[140:143], v[216:219]\n"
    "s_waitcnt lgkmcnt(7)\n"
    "ds_read_b64_tr_b16 a[162:163], v244 offset:18432\n"
    "v_mfma_f32_16x16x32_bf16 v[216:219], a[176:179], a[144:147], v[216:219]\n"
    "buffer_load_dwordx4 v248, s[8:11], s40 offen offset:1024 sc1 lds\n"
    "v_mfma_f32_16x16x32_bf16 v[216:219], a[180:183], a[148:151], v[216:219]\n"
    "ds_read_b64_tr_b16 a[164:165], v244 offset:49152\n"
    "ds_read_b64_tr_b16 a[166:167], v244 offset:51200\n"
    "v_mfma_f32_16x16x32_bf16 v[208:211], a[180:183], a[116:119], v[208:211]\n"
    "ds_read2st64_b32 v[160:161], v249 offset0:8 offset1:9\n"
    "v_mfma_f32_16x16x32_bf16 v[208:211], a[176:179], a[112:115], v[208:211]\n"
    "ds_read2st64_b32 v[154:155], v249 offset0:6 offset1:7\n"
    "v_mfma_f32_16x16x32_bf16 v[200:203], a[176:179], a[80:83], v[200:203]\n"
    "ds_read2st64_b32 v[152:153], v249 offset0:4 offset1:5\n"
    "v_mfma_f32_16x16x32_bf16 v[200:203], a[180:183], a[84:87], v[200:203]\n"
    "s_waitcnt lgkmcnt(11)\n"
    "ds_read2st64_b32 v[144:145], v249 offset0:0 offset1:1\n"
    "ds_read2st64_b32 v[146:147], v249 offset0:2 offset1:3\n"
    "v_mfma_f32_16x16x32_bf16 v[200:203], a[184:187], a[88:91], v[200:203]\n"
    "buffer_load_dwordx4 v248, s[8:11], s40 offen offset:2048 sc1 lds\n"
    "v_mfma_f32_16x16x32_bf16 v[200:203], a[188:191], a[92:95], v[200:203]\n"
    "ds_read_b64_tr_b16 a[168:169], v245 offset:16384\n"
    "v_mfma_f32_16x16x32_bf16 v[208:211], a[188:191], a[124:127], v[208:211]\n"
    "ds_read_b64_tr_b16 a[170:171], v245 offset:18432\n"
    "v_mfma_f32_16x16x32_bf16 v[208:211], a[184:187], a[120:123], v[208:211]\n"
    "v_mfma_f32_16x16x32_bf16 v[216:219], a[184:187], a[152:155], v[216:219]\n"
    "ds_read_b64_tr_b16 a[176:177], v246 offset:16384\n"
    "v_mfma_f32_16x16x32_bf16 v[216:219], a[188:191], a[156:159], v[216:219]\n"
    "s_waitcnt lgkmcnt(3)\n"
    "v_exp_f32_e32 v196, v196\n"
    "v_mfma_f32_16x16x32_bf16 a[252:255], a[160:163], v[172:175], a[252:255]\n"
    "buffer_load_dwordx4 v248, s[8:11], s40 offen offset:3072 sc1 lds\n"
    "v_mfma_f32_16x16x32_bf16 a[252:255], a[164:167], v[176:179], a[252:255]\n"
    "s_add_u32 s40, s40, 0x8000\n"
    "ds_read_b64_tr_b16 a[172:173], v245 offset:49152\n"
    "v_mfma_f32_16x16x32_bf16 a[248:251], a[164:167], v[168:171], a[248:251]\n"
    "s_add_u32 m0, s43, 12288\n"
    "ds_read_b64_tr_b16 a[174:175], v245 offset:51200\n"
    "v_mfma_f32_16x16x32_bf16 a[248:251], a[160:163], v[164:167], a[248:251]\n"
    "v_exp_f32_e32 v197, v197\n"
    "v_mfma_f32_16x16x32_bf16 a[244:247], a[160:163], v[156:159], a[244:247]\n"
    "ds_read_b64_tr_b16 a[178:179], v246 offset:18432\n"
    "v_mfma_f32_16x16x32_bf16 a[244:247], a[164:167], v[160:163], a[244:247]\n"
    "buffer_load_dwordx4 v248, s[8:11], s41 offen offset:0 sc1 lds\n"
    "v_mfma_f32_16x16x32_bf16 a[240:243], a[164:167], v[152:155], a[240:243]\n"
    "ds_read_b64_tr_b16 a[180:181], v246 offset:49152\n"
    "v_mfma_f32_16x16x32_bf16 a[240:243], a[160:163], v[148:151], a[240:243]\n"
    "ds_read_b64_tr_b16 a[184:185], v247 offset:16384\n"
    "v_mfma_f32_16x16x32_bf16 a[236:239], a[160:163], v[140:143], a[236:239]\n"
    "ds_read_b64_tr_b16 a[186:187], v247 offset:18432\n"
    "v_mfma_f32_16x16x32_bf16 a[236:239], a[164:167], v[144:147], a[236:239]\n"
    "s_waitcnt lgkmcnt(4)\n"
    "v_exp_f32_e32 v198, v198\n"
    "v_mfma_f32_16x16x32_bf16 v[0:3], a[168:171], v[140:143], v[0:3]\n"
    "buffer_load_dwordx4 v248, s[8:11], s41 offen offset:1024 sc1 lds\n"
    "v_mfma_f32_16x16x32_bf16 v[0:3], a[172:175], v[144:147], v[0:3]\n"
    "v_exp_f32_e32 v199, v199\n"
    "v_mfma_f32_16x16x32_bf16 v[4:7], a[172:175], v[152:155], v[4:7]\n"
    "ds_read_b64_tr_b16 a[182:183], v246 offset:51200\n"
    "v_cvt_pk_bf16_f32 v196, v196, v197\n"
    "v_mfma_f32_16x16x32_bf16 v[4:7], a[168:171], v[148:151], v[4:7]\n"
    "ds_read_b64_tr_b16 a[188:189], v247 offset:49152\n"
    "v_mfma_f32_16x16x32_bf16 v[8:11], a[168:171], v[156:159], v[8:11]\n"
    "v_exp_f32_e32 v204, v204\n"
    "v_mfma_f32_16x16x32_bf16 v[8:11], a[172:175], v[160:163], v[8:11]\n"
    "buffer_load_dwordx4 v248, s[8:11], s41 offen offset:2048 sc1 lds\n"
    "v_mfma_f32_16x16x32_bf16 v[12:15], a[172:175], v[168:171], v[12:15]\n"
    "ds_read_b64_tr_b16 a[190:191], v247 offset:51200\n"
    "v_mfma_f32_16x16x32_bf16 v[12:15], a[168:171], v[164:167], v[12:15]\n"
    "s_waitcnt vmcnt(7)\n"
    "v_mfma_f32_16x16x32_bf16 v[16:19], a[168:171], v[172:175], v[16:19]\n"
    "s_barrier\n"
    "v_mfma_f32_16x16x32_bf16 v[16:19], a[172:175], v[176:179], v[16:19]\n"
    "s_waitcnt lgkmcnt(2)\n"
    "v_add_u32_e32 v249, s45, v252\n"
    "v_add_u32_e32 v252, s63, v252\n"
    "v_mfma_f32_16x16x32_bf16 v[36:39], a[176:179], v[172:175], v[36:39]\n"
    "buffer_store_dword v254, off, s[8:11], s84 offset:0 sc1\n"
    "v_mfma_f32_16x16x32_bf16 v[36:39], a[180:183], v[176:179], v[36:39]\n"
    "s_mul_i32 s45, s49, 63\n"
    "ds_read_b64_tr_b16 a[160:161], v244 offset:20480\n"
    "v_mfma_f32_16x16x32_bf16 v[32:35], a[180:183], v[168:171], v[32:35]\n"
    "s_add_u32 s45, s45, s33\n"
    "v_exp_f32_e32 v205, v205\n"
    "v_mfma_f32_16x16x32_bf16 v[32:35], a[176:179], v[164:167], v[32:35]\n"
    "buffer_load_dwordx4 v248, s[8:11], s41 offen offset:3072 sc1 lds\n"
    "v_mfma_f32_16x16x32_bf16 v[28:31], a[176:179], v[156:159], v[28:31]\n"
    "s_add_u32 s41, s41, 0x8000\n"
    "ds_read_b64_tr_b16 a[162:163], v244 offset:22528\n"
    "v_cmp_ne_u32_e32 vcc, s39, v253\n"
    "v_mfma_f32_16x16x32_bf16 v[28:31], a[180:183], v[160:163], v[28:31]\n"
    "s_mov_b32 m0, s63\n"
    "ds_read_b64_tr_b16 a[168:169], v245 offset:20480\n"
    "v_mfma_f32_16x16x32_bf16 v[24:27], a[180:183], v[152:155], v[24:27]\n"
    "s_mul_i32 s45, s45, 405504\n"
    "ds_read_b64_tr_b16 a[164:165], v244 offset:53248\n"
    "v_mfma_f32_16x16x32_bf16 v[24:27], a[176:179], v[148:151], v[24:27]\n"
    "s_cbranch_vccnz .Lat_wpoll\n"
    ".Lat_wpoll_ret:\n"
    "v_mfma_f32_16x16x32_bf16 v[20:23], a[176:179], v[140:143], v[20:23]\n"
    "s_add_u32 s62, s57, 0\n"
    "ds_read_b64_tr_b16 a[166:167], v244 offset:55296\n"
    "v_mfma_f32_16x16x32_bf16 v[20:23], a[180:183], v[144:147], v[20:23]\n"
    "s_waitcnt lgkmcnt(5)\n"
    "ds_read_b64_tr_b16 a[170:171], v245 offset:22528\n"
    "v_cvt_pk_bf16_f32 v197, v198, v199\n"
    "v_mfma_f32_16x16x32_bf16 v[40:43], a[184:187], v[140:143], v[40:43]\n"
    "buffer_load_dwordx4 a[216:219], v250, s[8:11], s62 offen offset:0 sc1\n"
    "v_mfma_f32_16x16x32_bf16 v[40:43], a[188:191], v[144:147], v[40:43]\n"
    "s_add_u32 s45, s45, 16588800\n"
    "ds_read_b64_tr_b16 a[176:177], v246 offset:20480\n"
    "v_mfma_f32_16x16x32_bf16 v[44:47], a[188:191], v[152:155], v[44:47]\n"
    "s_lshl_b32 s46, s51, 12\n"
    "ds_read_b64_tr_b16 a[178:179], v246 offset:22528\n"
    "v_mfma_f32_16x16x32_bf16 v[44:47], a[184:187], v[148:151], v[44:47]\n"
    "s_lshl_b32 s48, s50, 15\n"
    "ds_read_b64_tr_b16 a[180:181], v246 offset:53248\n"
    "v_mfma_f32_16x16x32_bf16 v[48:51], a[184:187], v[156:159], v[48:51]\n"
    "s_add_u32 s46, s46, s48\n"
    "v_exp_f32_e32 v206, v206\n"
    "v_mfma_f32_16x16x32_bf16 v[48:51], a[188:191], v[160:163], v[48:51]\n"
    "buffer_load_dwordx4 a[220:223], v250, s[8:11], s62 offen offset:1024 sc1\n"
    "v_mfma_f32_16x16x32_bf16 v[52:55], a[188:191], v[168:171], v[52:55]\n"
    "s_cmp_eq_u32 s51, 0\n"
    "s_cselect_b32 s47, 2.0, 0\n"
    "ds_read_b64_tr_b16 a[172:173], v245 offset:53248\n"
    "v_mfma_f32_16x16x32_bf16 v[52:55], a[184:187], v[164:167], v[52:55]\n"
    "s_add_u32 s47, s47, 98304\n"
    "ds_read_b64_tr_b16 a[174:175], v245 offset:55296\n"
    "v_mfma_f32_16x16x32_bf16 v[56:59], a[184:187], v[172:175], v[56:59]\n"
    "s_lshl_b32 s61, s50, 9\n"
    "v_exp_f32_e32 v207, v207\n"
    "v_mfma_f32_16x16x32_bf16 v[56:59], a[188:191], v[176:179], v[56:59]\n"
    "s_waitcnt lgkmcnt(6)\n"
    "ds_read_b64_tr_b16 a[184:185], v247 offset:20480\n"
    "v_mfma_f32_16x16x32_bf16 v[76:79], a[160:163], v[172:175], v[76:79]\n"
    "buffer_load_dwordx4 a[224:227], v250, s[8:11], s62 offen offset:2048 sc1\n"
    "v_mfma_f32_16x16x32_bf16 v[76:79], a[164:167], v[176:179], v[76:79]\n"
    "s_add_u32 s61, s61, s47\n"
    "ds_write_addtid_b32 v196 offset:0\n"
    "v_cvt_pk_bf16_f32 v204, v204, v205\n"
    "v_mfma_f32_16x16x32_bf16 v[72:75], a[164:167], v[168:171], v[72:75]\n"
    "s_add_u32 s48, s32, 3\n"
    "v_exp_f32_e32 v212, v212\n"
    "v_mfma_f32_16x16x32_bf16 v[72:75], a[160:163], v[164:167], v[72:75]\n"
    "s_and_b32 s48, s48, 3\n"
    "ds_read_b64_tr_b16 a[182:183], v246 offset:55296\n"
    "v_mfma_f32_16x16x32_bf16 v[68:71], a[160:163], v[156:159], v[68:71]\n"
    "s_mul_i32 s48, s48, 101376\n"
    "ds_read_b64_tr_b16 a[186:187], v247 offset:22528\n"
    "v_mfma_f32_16x16x32_bf16 v[68:71], a[164:167], v[160:163], v[68:71]\n"
    "buffer_load_dwordx4 a[228:231], v250, s[8:11], s62 offen offset:3072 sc1\n"
    "v_mfma_f32_16x16x32_bf16 v[64:67], a[164:167], v[152:155], v[64:67]\n"
    "s_add_u32 s62, s57, 4096\n"
    "ds_write_addtid_b32 v197 offset:256\n"
    "v_mfma_f32_16x16x32_bf16 v[64:67], a[160:163], v[148:151], v[64:67]\n"
    "s_add_u32 s48, s48, s45\n"
    "v_exp_f32_e32 v213, v213\n"
    "v_mfma_f32_16x16x32_bf16 v[60:63], a[160:163], v[140:143], v[60:63]\n"
    "s_add_u32 s29, s48, s46\n"
    "ds_read_b64_tr_b16 a[188:189], v247 offset:53248\n"
    "v_cvt_pk_bf16_f32 v205, v206, v207\n"
    "v_mfma_f32_16x16x32_bf16 v[60:63], a[164:167], v[144:147], v[60:63]\n"
    "s_waitcnt lgkmcnt(6)\n"
    "v_exp_f32_e32 v214, v214\n"
    "v_mfma_f32_16x16x32_bf16 v[80:83], a[168:171], v[140:143], v[80:83]\n"
    "buffer_load_dwordx4 a[232:235], v250, s[8:11], s62 offen offset:0 sc1\n"
    "v_mfma_f32_16x16x32_bf16 v[80:83], a[172:175], v[144:147], v[80:83]\n"
    "s_add_u32 s29, s29, 8192\n"
    "v_exp_f32_e32 v215, v215\n"
    "v_mfma_f32_16x16x32_bf16 v[84:87], a[172:175], v[152:155], v[84:87]\n"
    "s_add_u32 s89, s48, s61\n"
    "ds_read_b64_tr_b16 a[190:191], v247 offset:55296\n"
    "v_mfma_f32_16x16x32_bf16 v[84:87], a[168:171], v[148:151], v[84:87]\n"
    "s_add_u32 s89, s89, 128\n"
    "ds_write_addtid_b32 v204 offset:1024\n"
    "v_cvt_pk_bf16_f32 v212, v212, v213\n"
    "v_mfma_f32_16x16x32_bf16 v[88:91], a[168:171], v[156:159], v[88:91]\n"
    "s_add_u32 s48, s32, 2\n"
    "ds_read_b128 a[160:163], v240 offset:0\n"
    "v_mfma_f32_16x16x32_bf16 v[88:91], a[172:175], v[160:163], v[88:91]\n"
    "v_mfma_f32_16x16x32_bf16 v[92:95], a[172:175], v[168:171], v[92:95]\n"
    "s_barrier\n"
    "v_mfma_f32_16x16x32_bf16 v[92:95], a[168:171], v[164:167], v[92:95]\n"
    "buffer_load_dwordx4 v[192:195], v250, s[8:11], s62 offen offset:1024 sc1\n"
    "v_mfma_f32_16x16x32_bf16 v[96:99], a[168:171], v[172:175], v[96:99]\n"
    "s_add_u32 m0, s43, 16384\n"
    "ds_read_b128 a[164:167], v241 offset:0\n"
    "v_mfma_f32_16x16x32_bf16 v[96:99], a[172:175], v[176:179], v[96:99]\n"
    "s_waitcnt lgkmcnt(7)\n"
    "v_exp_f32_e32 v200, v200\n"
    "v_mfma_f32_16x16x32_bf16 v[116:119], a[176:179], v[172:175], v[116:119]\n"
    "buffer_load_dwordx4 v248, s[8:11], s40 offen offset:0 sc1 lds\n"
    "v_mfma_f32_16x16x32_bf16 v[116:119], a[180:183], v[176:179], v[116:119]\n"
    "s_mov_b32 m0, s63\n"
    "v_mfma_f32_16x16x32_bf16 v[112:115], a[180:183], v[168:171], v[112:115]\n"
    "s_and_b32 s48, s48, 3\n"
    "v_exp_f32_e32 v201, v201\n"
    "v_mfma_f32_16x16x32_bf16 v[112:115], a[176:179], v[164:167], v[112:115]\n"
    "s_mul_i32 s48, s48, 101376\n"
    "ds_read_b128 a[168:171], v240 offset:4096\n"
    "v_cvt_pk_bf16_f32 v213, v214, v215\n"
    "v_mfma_f32_16x16x32_bf16 v[108:111], a[176:179], v[156:159], v[108:111]\n"
    "s_add_u32 m0, s43, 16384\n"
    "v_exp_f32_e32 v202, v202\n"
    "v_mfma_f32_16x16x32_bf16 v[108:111], a[180:183], v[160:163], v[108:111]\n"
    "buffer_load_dwordx4 v248, s[8:11], s40 offen offset:1024 sc1 lds\n"
    "v_mfma_f32_16x16x32_bf16 v[104:107], a[180:183], v[152:155], v[104:107]\n"
    "s_mov_b32 m0, s63\n"
    "v_exp_f32_e32 v203, v203\n"
    "v_mfma_f32_16x16x32_bf16 v[104:107], a[176:179], v[148:151], v[104:107]\n"
    "s_add_u32 s48, s48, s45\n"
    "ds_write_addtid_b32 v205 offset:1280\n"
    "v_cvt_pk_bf16_f32 v198, v200, v201\n"
    "v_mfma_f32_16x16x32_bf16 v[100:103], a[176:179], v[140:143], v[100:103]\n"
    "s_add_u32 s28, s48, s46\n"
    "v_exp_f32_e32 v208, v208\n"
    "v_mfma_f32_16x16x32_bf16 v[100:103], a[180:183], v[144:147], v[100:103]\n"
    "s_waitcnt lgkmcnt(5)\n"
    "ds_write_addtid_b32 v212 offset:2048\n"
    "v_mfma_f32_16x16x32_bf16 v[120:123], a[184:187], v[140:143], v[120:123]\n"
    "s_add_u32 m0, s43, 16384\n"
    "v_exp_f32_e32 v209, v209\n"
    "v_mfma_f32_16x16x32_bf16 v[120:123], a[188:191], v[144:147], v[120:123]\n"
    "buffer_load_dwordx4 v248, s[8:11], s40 offen offset:2048 sc1 lds\n"
    "v_mfma_f32_16x16x32_bf16 v[124:127], a[188:191], v[152:155], v[124:127]\n"
    "s_mov_b32 m0, s63\n"
    "ds_read_b128 a[172:175], v241 offset:4096\n"
    "v_mfma_f32_16x16x32_bf16 v[124:127], a[184:187], v[148:151], v[124:127]\n"
    "s_add_u32 s28, s28, 16384\n"
    "ds_read_b128 a[176:179], v242 offset:0\n"
    "v_cvt_pk_bf16_f32 v199, v202, v203\n"
    "v_mfma_f32_16x16x32_bf16 v[128:131], a[184:187], v[156:159], v[128:131]\n"
    "s_add_u32 s88, s48, s61\n"
    "v_exp_f32_e32 v210, v210\n"
    "v_mfma_f32_16x16x32_bf16 v[128:131], a[188:191], v[160:163], v[128:131]\n"
    "s_add_u32 m0, s43, 16384\n"
    "v_exp_f32_e32 v211, v211\n"
    "v_mfma_f32_16x16x32_bf16 v[132:135], a[188:191], v[168:171], v[132:135]\n"
    "buffer_load_dwordx4 v248, s[8:11], s40 offen offset:3072 sc1 lds\n"
    "v_mfma_f32_16x16x32_bf16 v[132:135], a[184:187], v[164:167], v[132:135]\n"
    "s_mov_b32 m0, s63\n"
    "ds_read_b128 a[180:183], v243 offset:0\n"
    "v_mfma_f32_16x16x32_bf16 v[136:139], a[184:187], v[172:175], v[136:139]\n"
    "s_add_u32 s88, s88, 256\n"
    "ds_write_addtid_b32 v213 offset:2304\n"
    "v_mfma_f32_16x16x32_bf16 v[136:139], a[188:191], v[176:179], v[136:139]\n"
    "s_waitcnt lgkmcnt(7)\n"
    "v_cvt_pk_bf16_f32 v206, v208, v209\n"
    "v_mfma_f32_16x16x32_bf16 v[172:175], a[160:163], a[128:131], v[236:239]\n"
    "s_add_u32 m0, s43, 20480\n"
    "v_exp_f32_e32 v216, v216\n"
    "v_mfma_f32_16x16x32_bf16 v[172:175], a[164:167], a[132:135], v[172:175]\n"
    "buffer_load_dwordx4 v248, s[8:11], s41 offen offset:0 sc1 lds\n"
    "v_mfma_f32_16x16x32_bf16 v[164:167], a[164:167], a[100:103], v[232:235]\n"
    "s_mov_b32 m0, s63\n"
    "v_exp_f32_e32 v217, v217\n"
    "v_mfma_f32_16x16x32_bf16 v[164:167], a[160:163], a[96:99], v[164:167]\n"
    "s_add_u32 s48, s32, 1\n"
    "ds_write_addtid_b32 v198 offset:512\n"
    "v_mfma_f32_16x16x32_bf16 v[156:159], a[160:163], a[64:67], v[228:231]\n"
    "s_and_b32 s48, s48, 3\n"
    "ds_read_b128 a[184:187], v242 offset:4096\n"
    "v_mfma_f32_16x16x32_bf16 v[156:159], a[164:167], a[68:71], v[156:159]\n"
    "s_waitcnt lgkmcnt(5)\n"
    "ds_write_addtid_b32 v199 offset:768\n"
    "v_mfma_f32_16x16x32_bf16 v[156:159], a[168:171], a[72:75], v[156:159]\n"
    "s_add_u32 m0, s43, 20480\n"
    "ds_read_b128 a[188:191], v243 offset:4096\n"
    "v_cvt_pk_bf16_f32 v207, v210, v211\n"
    "v_mfma_f32_16x16x32_bf16 v[156:159], a[172:175], a[76:79], v[156:159]\n"
    "buffer_load_dwordx4 v248, s[8:11], s41 offen offset:1024 sc1 lds\n"
    "v_mfma_f32_16x16x32_bf16 v[164:167], a[172:175], a[108:111], v[164:167]\n"
    "s_mov_b32 m0, s63\n"
    "v_exp_f32_e32 v218, v218\n"
    "v_mfma_f32_16x16x32_bf16 v[164:167], a[168:171], a[104:107], v[164:167]\n"
    "s_mul_i32 s48, s48, 101376\n"
    "v_exp_f32_e32 v219, v219\n"
    "v_mfma_f32_16x16x32_bf16 v[172:175], a[168:171], a[136:139], v[172:175]\n"
    "s_add_u32 s48, s48, s45\n"
    "ds_write_addtid_b32 v206 offset:1536\n"
    "v_cvt_pk_bf16_f32 v214, v216, v217\n"
    "v_mfma_f32_16x16x32_bf16 v[172:175], a[172:175], a[140:143], v[172:175]\n"
    "s_waitcnt lgkmcnt(6)\n"
    "ds_read_b128 a[160:163], v240 offset:2048\n"
    "v_cvt_pk_bf16_f32 v215, v218, v219\n"
    "v_mfma_f32_16x16x32_bf16 v[172:175], a[176:179], a[144:147], v[172:175]\n"
    "s_add_u32 m0, s43, 20480\n"
    "ds_read_b128 a[168:171], v240 offset:6144\n"
    "v_mfma_f32_16x16x32_bf16 v[172:175], a[180:183], a[148:151], v[172:175]\n"
    "buffer_load_dwordx4 v248, s[8:11], s41 offen offset:2048 sc1 lds\n"
    "v_mfma_f32_16x16x32_bf16 v[164:167], a[180:183], a[116:119], v[164:167]\n"
    "s_mov_b32 m0, s63\n"
    "ds_read_b128 a[164:167], v241 offset:2048\n"
    "v_mfma_f32_16x16x32_bf16 v[164:167], a[176:179], a[112:115], v[164:167]\n"
    "s_add_u32 s82, s48, s46\n"
    "ds_write_addtid_b32 v207 offset:1792\n"
    "v_mfma_f32_16x16x32_bf16 v[156:159], a[176:179], a[80:83], v[156:159]\n"
    "s_add_u32 s82, s82, 24576\n"
    "ds_write_addtid_b32 v214 offset:2560\n"
    "v_mfma_f32_16x16x32_bf16 v[156:159], a[180:183], a[84:87], v[156:159]\n"
    "s_waitcnt lgkmcnt(6)\n"
    "ds_write_addtid_b32 v215 offset:2816\n"
    "v_mfma_f32_16x16x32_bf16 v[156:159], a[184:187], a[88:91], v[156:159]\n"
    "s_add_u32 m0, s43, 20480\n"
    "ds_read_b128 a[176:179], v242 offset:2048\n"
    "v_mfma_f32_16x16x32_bf16 v[156:159], a[188:191], a[92:95], v[156:159]\n"
    "buffer_load_dwordx4 v248, s[8:11], s41 offen offset:3072 sc1 lds\n"
    "v_mfma_f32_16x16x32_bf16 v[164:167], a[188:191], a[124:127], v[164:167]\n"
    "s_mov_b32 m0, s63\n"
    "ds_read_b128 a[180:183], v243 offset:2048\n"
    "v_mfma_f32_16x16x32_bf16 v[164:167], a[184:187], a[120:123], v[164:167]\n"
    "s_add_u32 s87, s48, s61\n"
    "v_exp_f32_e32 v156, v156\n"
    "v_mfma_f32_16x16x32_bf16 v[172:175], a[184:187], a[152:155], v[172:175]\n"
    "s_add_u32 s87, s87, 384\n"
    "ds_read_b128 a[172:175], v241 offset:6144\n"
    "v_mfma_f32_16x16x32_bf16 v[172:175], a[188:191], a[156:159], v[172:175]\n"
    "s_waitcnt lgkmcnt(6)\n"
    "ds_read_b128 a[184:187], v242 offset:6144\n"
    "ds_read_b128 a[188:191], v243 offset:6144\n"
    "v_mfma_f32_16x16x32_bf16 v[176:179], a[160:163], a[128:131], v[236:239]\n"
    "s_mul_i32 s48, s32, 101376\n"
    "v_exp_f32_e32 v157, v157\n"
    "v_mfma_f32_16x16x32_bf16 v[176:179], a[164:167], a[132:135], v[176:179]\n"
    "s_add_u32 s48, s48, s45\n"
    "v_exp_f32_e32 v158, v158\n"
    "v_mfma_f32_16x16x32_bf16 v[168:171], a[164:167], a[100:103], v[232:235]\n"
    "s_add_u32 s59, s48, s46\n"
    "v_exp_f32_e32 v159, v159\n"
    "v_mfma_f32_16x16x32_bf16 v[168:171], a[160:163], a[96:99], v[168:171]\n"
    "s_waitcnt lgkmcnt(5)\n"
    "s_waitcnt vmcnt(14)\n"
    "v_mfma_f32_16x16x32_bf16 v[160:163], a[160:163], a[64:67], v[228:231]\n"
    "s_barrier\n"
    "v_mfma_f32_16x16x32_bf16 v[160:163], a[164:167], a[68:71], v[160:163]\n"
    "s_waitcnt lgkmcnt(2)\n"
    "ds_read2st64_b32 a[196:197], v249 offset0:0 offset1:1\n"
    "ds_read2st64_b32 a[198:199], v249 offset0:2 offset1:3\n"
    "v_mfma_f32_16x16x32_bf16 v[160:163], a[168:171], a[72:75], v[160:163]\n"
    "s_add_u32 s60, s48, s61\n"
    "ds_read_b128 a[160:163], v240 offset:8192\n"
    "v_cvt_pk_bf16_f32 v156, v156, v157\n"
    "v_mfma_f32_16x16x32_bf16 v[160:163], a[172:175], a[76:79], v[160:163]\n"
    "s_and_b32 s48, s32, 1\n"
    "ds_read_b128 a[164:167], v241 offset:8192\n"
    "v_mfma_f32_16x16x32_bf16 v[168:171], a[172:175], a[108:111], v[168:171]\n"
    "s_xor_b32 s48, s48, s50\n"
    "v_exp_f32_e32 v164, v164\n"
    "v_mfma_f32_16x16x32_bf16 v[168:171], a[168:171], a[104:107], v[168:171]\n"
    "s_sub_u32 s61, s32, s48\n"
    "v_exp_f32_e32 v165, v165\n"
    "v_mfma_f32_16x16x32_bf16 v[176:179], a[168:171], a[136:139], v[176:179]\n"
    "s_and_b32 s61, s61, 3\n"
    "ds_read2st64_b32 a[192:193], v252 offset0:0 offset1:1\n"
    "v_cvt_pk_bf16_f32 v157, v158, v159\n"
    "v_mfma_f32_16x16x32_bf16 v[176:179], a[172:175], a[140:143], v[176:179]\n"
    "v_exp_f32_e32 v166, v166\n"
    "v_mfma_f32_16x16x32_bf16 v[176:179], a[176:179], a[144:147], v[176:179]\n"
    "s_add_u32 s61, s61, 8\n"
    "ds_read_b128 a[168:171], v240 offset:12288\n"
    "ds_read2st64_b32 a[194:195], v252 offset0:2 offset1:3\n"
    "v_mfma_f32_16x16x32_bf16 v[176:179], a[180:183], a[148:151], v[176:179]\n"
    "s_mul_i32 s48, s48, 101376\n"
    "v_exp_f32_e32 v167, v167\n"
    "v_mfma_f32_16x16x32_bf16 v[168:171], a[180:183], a[116:119], v[168:171]\n"
    "s_add_u32 s48, s48, s45\n"
    "ds_read_b128 a[172:175], v241 offset:12288\n"
    "v_cvt_pk_bf16_f32 v164, v164, v165\n"
    "v_mfma_f32_16x16x32_bf16 v[168:171], a[176:179], a[112:115], v[168:171]\n"
    "s_lshl_b32 s80, s61, 13\n"
    "v_exp_f32_e32 v172, v172\n"
    "v_mfma_f32_16x16x32_bf16 v[160:163], a[176:179], a[80:83], v[160:163]\n"
    "s_add_u32 s80, s80, s48\n"
    "v_exp_f32_e32 v173, v173\n"
    "v_mfma_f32_16x16x32_bf16 v[160:163], a[180:183], a[84:87], v[160:163]\n"
    "s_waitcnt lgkmcnt(8)\n"
    "ds_read_b128 a[176:179], v242 offset:8192\n"
    "v_cvt_pk_bf16_f32 v165, v166, v167\n"
    "v_mfma_f32_16x16x32_bf16 v[160:163], a[184:187], a[88:91], v[160:163]\n"
    "s_lshl_b32 s46, s51, 12\n"
    "ds_read2st64_b32 a[204:205], v249 offset0:4 offset1:5\n"
    "v_mfma_f32_16x16x32_bf16 v[160:163], a[188:191], a[92:95], v[160:163]\n"
    "s_add_u32 s80, s80, s46\n"
    "v_exp_f32_e32 v174, v174\n"
    "v_mfma_f32_16x16x32_bf16 v[168:171], a[188:191], a[124:127], v[168:171]\n"
    "s_lshl_b32 s86, s61, 7\n"
    "ds_read_b128 a[180:183], v243 offset:8192\n"
    "ds_read2st64_b32 a[206:207], v249 offset0:6 offset1:7\n"
    "v_mfma_f32_16x16x32_bf16 v[168:171], a[184:187], a[120:123], v[168:171]\n"
    "s_add_u32 s86, s86, s48\n"
    "v_exp_f32_e32 v175, v175\n"
    "v_mfma_f32_16x16x32_bf16 v[176:179], a[184:187], a[152:155], v[176:179]\n"
    "s_add_u32 s86, s86, s47\n"
    "ds_write_addtid_b32 v156 offset:3072\n"
    "v_cvt_pk_bf16_f32 v172, v172, v173\n"
    "v_mfma_f32_16x16x32_bf16 v[176:179], a[188:191], a[156:159], v[176:179]\n"
    "s_waitcnt lgkmcnt(9)\n"
    "ds_write_addtid_b32 v157 offset:3328\n"
    "ds_write_addtid_b32 v164 offset:4096\n"
    "v_mfma_f32_16x16x32_bf16 v[180:183], a[160:163], a[128:131], v[236:239]\n"
    "s_lshl_b32 s46, s93, 7\n"
    "v_exp_f32_e32 v160, v160\n"
    "v_mfma_f32_16x16x32_bf16 v[180:183], a[164:167], a[132:135], v[180:183]\n"
    "s_lshl_b32 s55, s93, 15\n"
    "ds_read_b128 a[184:187], v242 offset:12288\n"
    "ds_read_b128 a[188:191], v243 offset:12288\n"
    "v_mfma_f32_16x16x32_bf16 v[148:151], a[164:167], a[100:103], v[232:235]\n"
    "s_sub_u32 s54, s55, 24576\n"
    "v_exp_f32_e32 v161, v161\n"
    "v_mfma_f32_16x16x32_bf16 v[148:151], a[160:163], a[96:99], v[148:151]\n"
    "s_add_u32 s55, s55, 32768\n"
    "ds_read2st64_b32 a[200:201], v252 offset0:4 offset1:5\n"
    "v_cvt_pk_bf16_f32 v173, v174, v175\n"
    "v_mfma_f32_16x16x32_bf16 v[140:143], a[160:163], a[64:67], v[228:231]\n"
    "s_lshl2_add_u32 s38, s34, s93\n"
    "ds_write_addtid_b32 v165 offset:4352\n"
    "v_exp_f32_e32 v162, v162\n"
    "v_mfma_f32_16x16x32_bf16 v[140:143], a[164:167], a[68:71], v[140:143]\n"
    "s_waitcnt lgkmcnt(11)\n"
    "v_exp_f32_e32 v163, v163\n"
    "v_mfma_f32_16x16x32_bf16 v[140:143], a[168:171], a[72:75], v[140:143]\n"
    "s_lshl1_add_u32 s38, s38, s49\n"
    "ds_read2st64_b32 a[202:203], v252 offset0:6 offset1:7\n"
    "v_cvt_pk_bf16_f32 v158, v160, v161\n"
    "v_mfma_f32_16x16x32_bf16 v[140:143], a[172:175], a[76:79], v[140:143]\n"
    ".long 0xbedf00ff\n"
    ".Lat_w10:\n"
    "s_setpc_b64 s[2:3]\n"
    "s_lshl_b32 s38, s38, 7\n"
    "v_exp_f32_e32 v168, v168\n"
    "v_mfma_f32_16x16x32_bf16 v[148:151], a[172:175], a[108:111], v[148:151]\n"
    "s_add_u32 s38, s38, 5648384\n"
    "v_exp_f32_e32 v169, v169\n"
    "v_mfma_f32_16x16x32_bf16 v[148:151], a[168:171], a[104:107], v[148:151]\n"
    "s_lshl3_add_u32 s90, s34, s49\n"
    "ds_read_b128 a[160:163], v240 offset:10240\n"
    "v_cvt_pk_bf16_f32 v159, v162, v163\n"
    "v_mfma_f32_16x16x32_bf16 v[180:183], a[168:171], a[136:139], v[180:183]\n"
    "s_lshl2_add_u32 s90, s90, s93\n"
    "ds_read2st64_b32 a[212:213], v249 offset0:8 offset1:9\n"
    "v_exp_f32_e32 v170, v170\n"
    "v_mfma_f32_16x16x32_bf16 v[180:183], a[172:175], a[140:143], v[180:183]\n"
    "s_waitcnt lgkmcnt(11)\n"
    "v_exp_f32_e32 v171, v171\n"
    "v_mfma_f32_16x16x32_bf16 v[180:183], a[176:179], a[144:147], v[180:183]\n"
    "s_lshl2_add_u32 s90, s90, 6041600\n"
    "ds_read_b128 a[164:167], v241 offset:10240\n"
    "ds_read2st64_b32 a[214:215], v249 offset0:10 offset1:11\n"
    "v_mfma_f32_16x16x32_bf16 v[180:183], a[180:183], a[148:151], v[180:183]\n"
    "s_lshl_b32 s45, s51, 15\n"
    "ds_read_b128 a[168:171], v240 offset:14336\n"
    "v_cvt_pk_bf16_f32 v166, v168, v169\n"
    "v_mfma_f32_16x16x32_bf16 v[148:151], a[180:183], a[116:119], v[148:151]\n"
    "s_sub_u32 s45, 24576, s45\n"
    "v_exp_f32_e32 v176, v176\n"
    "v_mfma_f32_16x16x32_bf16 v[148:151], a[176:179], a[112:115], v[148:151]\n"
    "s_add_u32 s47, s46, 6103040\n"
    "v_exp_f32_e32 v177, v177\n"
    "v_mfma_f32_16x16x32_bf16 v[140:143], a[176:179], a[80:83], v[140:143]\n"
    "s_andn2_b32 s48, s38, 0xc00\n"
    "ds_write_addtid_b32 v172 offset:5120\n"
    "v_cvt_pk_bf16_f32 v167, v170, v171\n"
    "v_mfma_f32_16x16x32_bf16 v[140:143], a[180:183], a[84:87], v[140:143]\n"
    "s_waitcnt lgkmcnt(9)\n"
    "ds_read_b128 a[176:179], v242 offset:10240\n"
    "v_exp_f32_e32 v178, v178\n"
    "v_mfma_f32_16x16x32_bf16 v[140:143], a[184:187], a[88:91], v[140:143]\n"
    "s_andn2_b32 s61, s90, 0x18c\n"
    "ds_write_addtid_b32 v173 offset:5376\n"
    "ds_write_addtid_b32 v158 offset:3584\n"
    "v_mfma_f32_16x16x32_bf16 v[140:143], a[188:191], a[92:95], v[140:143]\n"
    "buffer_load_dword v188, v189, s[8:11], s81 offen offset:0 sc1\n"
    "ds_write_addtid_b32 v159 offset:3840\n"
    "v_mfma_f32_16x16x32_bf16 v[148:151], a[188:191], a[124:127], v[148:151]\n"
    "s_add_u32 s31, s46, s35\n"
    "v_exp_f32_e32 v179, v179\n"
    "v_mfma_f32_16x16x32_bf16 v[148:151], a[184:187], a[120:123], v[148:151]\n"
    "s_add_u32 m0, s63, 6144\n"
    "ds_read_b128 a[172:175], v241 offset:14336\n"
    "v_exp_f32_e32 v140, v140\n"
    "v_mfma_f32_16x16x32_bf16 v[180:183], a[184:187], a[152:155], v[180:183]\n"
    "buffer_load_dwordx4 v251, s[12:15], s31 offen offset:0 lds\n"
    "ds_read2st64_b32 a[208:209], v252 offset0:8 offset1:9\n"
    "v_mfma_f32_16x16x32_bf16 v[180:183], a[188:191], a[156:159], v[180:183]\n"
    "s_waitcnt lgkmcnt(9)\n"
    "v_exp_f32_e32 v141, v141\n"
    "v_mfma_f32_16x16x32_bf16 v[184:187], a[160:163], a[128:131], v[236:239]\n"
    "s_mov_b32 m0, s63\n"
    "v_exp_f32_e32 v142, v142\n"
    "v_mfma_f32_16x16x32_bf16 v[184:187], a[164:167], a[132:135], v[184:187]\n"
    "ds_read_b128 a[184:187], v242 offset:14336\n"
    "v_exp_f32_e32 v143, v143\n"
    "v_mfma_f32_16x16x32_bf16 v[152:155], a[164:167], a[100:103], v[232:235]\n"
    "s_add_u32 s31, s31, 0x1000\n"
    "ds_read_b128 a[180:183], v243 offset:10240\n"
    "v_cvt_pk_bf16_f32 v140, v140, v141\n"
    "v_mfma_f32_16x16x32_bf16 v[152:155], a[160:163], a[96:99], v[152:155]\n"
    "s_add_u32 m0, s63, 7168\n"
    "ds_read2st64_b32 a[210:211], v252 offset0:10 offset1:11\n"
    "v_exp_f32_e32 v148, v148\n"
    "v_mfma_f32_16x16x32_bf16 v[144:147], a[160:163], a[64:67], v[228:231]\n"
    "buffer_load_dwordx4 v251, s[12:15], s31 offen offset:0 lds\n"
    "v_mfma_f32_16x16x32_bf16 v[144:147], a[164:167], a[68:71], v[144:147]\n"
    "s_waitcnt lgkmcnt(4)\n"
    "s_waitcnt vmcnt(3)\n"
    "v_mfma_f32_16x16x32_bf16 v[144:147], a[168:171], a[72:75], v[144:147]\n"
    "s_barrier\n"
    "v_mfma_f32_16x16x32_bf16 v[144:147], a[172:175], a[76:79], v[144:147]\n"
    "s_mov_b32 m0, s63\n"
    "v_exp_f32_e32 v149, v149\n"
    "v_mfma_f32_16x16x32_bf16 v[152:155], a[172:175], a[108:111], v[152:155]\n"
    "s_cmp_eq_u32 s93, 1\n"
    "s_cselect_b32 s31, s36, 2.0\n"
    "ds_read_b128 a[188:191], v243 offset:14336\n"
    "v_mfma_f32_16x16x32_bf16 v[152:155], a[168:171], a[104:107], v[152:155]\n"
    "s_cmp_eq_u32 s93, 2\n"
    "s_cselect_b32 s31, s37, s31\n"
    "ds_write_addtid_b32 v166 offset:4608\n"
    "v_mfma_f32_16x16x32_bf16 v[184:187], a[168:171], a[136:139], v[184:187]\n"
    "buffer_load_dword v191, v190, s[12:15], s31 offen offset:0\n"
    "v_mfma_f32_16x16x32_bf16 v[184:187], a[172:175], a[140:143], v[184:187]\n"
    "s_waitcnt lgkmcnt(3)\n"
    "ds_read_b128 a[160:163], v240 offset:16384\n"
    "v_cvt_pk_bf16_f32 v141, v142, v143\n"
    "v_mfma_f32_16x16x32_bf16 v[184:187], a[176:179], a[144:147], v[184:187]\n"
    "s_cmp_eq_u32 s93, 0\n"
    "s_cselect_b32 s31, s35, s31\n"
    "ds_read_b128 a[164:167], v241 offset:16384\n"
    "v_mfma_f32_16x16x32_bf16 v[184:187], a[180:183], a[148:151], v[184:187]\n"
    "s_add_u32 s31, s31, 6103040\n"
    "v_exp_f32_e32 v150, v150\n"
    "v_mfma_f32_16x16x32_bf16 v[152:155], a[180:183], a[116:119], v[152:155]\n"
    "ds_write_addtid_b32 v167 offset:4864\n"
    "v_cvt_pk_bf16_f32 v174, v176, v177\n"
    "v_mfma_f32_16x16x32_bf16 v[152:155], a[176:179], a[112:115], v[152:155]\n"
    "ds_read_b128 a[168:171], v240 offset:20480\n"
    "ds_write_addtid_b32 v140 offset:0\n"
    "v_mfma_f32_16x16x32_bf16 v[144:147], a[176:179], a[80:83], v[144:147]\n"
    "ds_read_b128 a[172:175], v241 offset:20480\n"
    "v_exp_f32_e32 v151, v151\n"
    "v_mfma_f32_16x16x32_bf16 v[144:147], a[180:183], a[84:87], v[144:147]\n"
    "s_waitcnt lgkmcnt(7)\n"
    "ds_write_addtid_b32 v141 offset:256\n"
    "v_cvt_pk_bf16_f32 v148, v148, v149\n"
    "v_mfma_f32_16x16x32_bf16 v[144:147], a[184:187], a[88:91], v[144:147]\n"
    "v_exp_f32_e32 v180, v180\n"
    "v_mfma_f32_16x16x32_bf16 v[144:147], a[188:191], a[92:95], v[144:147]\n"
    "ds_write_addtid_b32 v174 offset:5632\n"
    "v_exp_f32_e32 v181, v181\n"
    "v_mfma_f32_16x16x32_bf16 v[152:155], a[188:191], a[124:127], v[152:155]\n"
    "ds_write_addtid_b32 v148 offset:1024\n"
    "v_cvt_pk_bf16_f32 v149, v150, v151\n"
    "v_mfma_f32_16x16x32_bf16 v[152:155], a[184:187], a[120:123], v[152:155]\n"
    "ds_read_b128 a[176:179], v242 offset:16384\n"
    "ds_write_addtid_b32 v149 offset:1280\n"
    "v_mfma_f32_16x16x32_bf16 v[184:187], a[184:187], a[152:155], v[184:187]\n"
    "v_exp_f32_e32 v182, v182\n"
    "v_mfma_f32_16x16x32_bf16 v[184:187], a[188:191], a[156:159], v[184:187]\n"
    "s_waitcnt lgkmcnt(9)\n"
    "v_cvt_pk_bf16_f32 v180, v180, v181\n"
    "v_exp_f32_e32 v183, v183\n"
    "v_mfma_f32_16x16x32_bf16 v[212:215], a[160:163], a[128:131], v[236:239]\n"
    "ds_read_b128 a[180:183], v243 offset:16384\n"
    "v_exp_f32_e32 v144, v144\n"
    "v_mfma_f32_16x16x32_bf16 v[212:215], a[164:167], a[132:135], v[212:215]\n"
    "ds_read_b128 a[184:187], v242 offset:20480\n"
    "v_cvt_pk_bf16_f32 v175, v178, v179\n"
    "v_mfma_f32_16x16x32_bf16 v[204:207], a[164:167], a[100:103], v[232:235]\n"
    "v_exp_f32_e32 v145, v145\n"
    "v_mfma_f32_16x16x32_bf16 v[204:207], a[160:163], a[96:99], v[204:207]\n"
    "ds_write_addtid_b32 v175 offset:5888\n"
    "v_cvt_pk_bf16_f32 v181, v182, v183\n"
    "v_mfma_f32_16x16x32_bf16 v[196:199], a[160:163], a[64:67], v[228:231]\n"
    "v_exp_f32_e32 v146, v146\n"
    "v_mfma_f32_16x16x32_bf16 v[196:199], a[164:167], a[68:71], v[196:199]\n"
    "s_waitcnt lgkmcnt(8)\n"
    "v_exp_f32_e32 v147, v147\n"
    "v_mfma_f32_16x16x32_bf16 v[196:199], a[168:171], a[72:75], v[196:199]\n"
    "ds_write_addtid_b32 v180 offset:2048\n"
    "v_cvt_pk_bf16_f32 v142, v144, v145\n"
    "v_mfma_f32_16x16x32_bf16 v[196:199], a[172:175], a[76:79], v[196:199]\n"
    "s_waitcnt lgkmcnt(1)\n"
    "v_mfma_f32_16x16x32_bf16 v[204:207], a[172:175], a[108:111], v[204:207]\n"
    "s_barrier\n"
    "v_mfma_f32_16x16x32_bf16 v[204:207], a[168:171], a[104:107], v[204:207]\n"
    "ds_read_b128 a[188:191], v243 offset:20480\n"
    "ds_write_addtid_b32 v181 offset:2304\n"
    "v_mfma_f32_16x16x32_bf16 v[212:215], a[168:171], a[136:139], v[212:215]\n"
    "v_exp_f32_e32 v152, v152\n"
    "v_mfma_f32_16x16x32_bf16 v[212:215], a[172:175], a[140:143], v[212:215]\n"
    "v_exp_f32_e32 v153, v153\n"
    "v_mfma_f32_16x16x32_bf16 v[212:215], a[176:179], a[144:147], v[212:215]\n"
    "ds_read_b128 a[168:171], v240 offset:22528\n"
    "v_cvt_pk_bf16_f32 v143, v146, v147\n"
    "v_mfma_f32_16x16x32_bf16 v[212:215], a[180:183], a[148:151], v[212:215]\n"
    "ds_read_b128 a[160:163], v240 offset:18432\n"
    "ds_read_b128 a[164:167], v241 offset:18432\n"
    "v_mfma_f32_16x16x32_bf16 v[204:207], a[180:183], a[116:119], v[204:207]\n"
    "ds_read_b128 a[172:175], v241 offset:22528\n"
    "v_exp_f32_e32 v154, v154\n"
    "v_mfma_f32_16x16x32_bf16 v[204:207], a[176:179], a[112:115], v[204:207]\n"
    "ds_write_addtid_b32 v142 offset:512\n"
    "v_exp_f32_e32 v155, v155\n"
    "v_mfma_f32_16x16x32_bf16 v[196:199], a[176:179], a[80:83], v[196:199]\n"
    "ds_read2st64_b32 v[160:161], v249 offset0:12 offset1:13\n"
    "v_cvt_pk_bf16_f32 v150, v152, v153\n"
    "v_mfma_f32_16x16x32_bf16 v[196:199], a[180:183], a[84:87], v[196:199]\n"
    "s_waitcnt lgkmcnt(7)\n"
    "ds_read2st64_b32 v[162:163], v249 offset0:14 offset1:15\n"
    "v_exp_f32_e32 v184, v184\n"
    "v_mfma_f32_16x16x32_bf16 v[196:199], a[184:187], a[88:91], v[196:199]\n"
    "ds_read_b128 a[176:179], v242 offset:18432\n"
    "v_exp_f32_e32 v185, v185\n"
    "v_mfma_f32_16x16x32_bf16 v[196:199], a[188:191], a[92:95], v[196:199]\n"
    "ds_read_b128 a[180:183], v243 offset:18432\n"
    "v_cvt_pk_bf16_f32 v151, v154, v155\n"
    "v_mfma_f32_16x16x32_bf16 v[204:207], a[188:191], a[124:127], v[204:207]\n"
    "v_exp_f32_e32 v186, v186\n"
    "v_mfma_f32_16x16x32_bf16 v[204:207], a[184:187], a[120:123], v[204:207]\n"
    "s_waitcnt vmcnt(3)\n"
    "ds_write_addtid_b32 v143 offset:768\n"
    "v_cmp_ne_u32_e32 vcc, s39, v188\n"
    "v_mfma_f32_16x16x32_bf16 v[212:215], a[184:187], a[152:155], v[212:215]\n"
    "ds_read2st64_b32 v[168:169], v249 offset0:16 offset1:17\n"
    "v_exp_f32_e32 v196, v196\n"
    "v_mfma_f32_16x16x32_bf16 v[212:215], a[188:191], a[156:159], v[212:215]\n"
    "s_waitcnt lgkmcnt(8)\n"
    "ds_read2st64_b32 v[170:171], v249 offset0:18 offset1:19\n"
    "v_exp_f32_e32 v197, v197\n"
    "v_mfma_f32_16x16x32_bf16 v[216:219], a[160:163], a[128:131], v[236:239]\n"
    "s_cmp_eq_u64 vcc, 0\n"
    "s_cselect_b32 s31, s31, 2.0\n"
    "s_cselect_b32 s44, 0, 1\n"
    "ds_write_addtid_b32 v150 offset:1536\n"
    "v_mfma_f32_16x16x32_bf16 v[216:219], a[164:167], a[132:135], v[216:219]\n"
    "buffer_load_dword v191, v190, s[8:11], s31 offen offset:0 sc1\n"
    "ds_read_b128 a[184:187], v242 offset:22528\n"
    "v_mfma_f32_16x16x32_bf16 v[208:211], a[164:167], a[100:103], v[232:235]\n"
    "v_exp_f32_e32 v198, v198\n"
    "v_mfma_f32_16x16x32_bf16 v[208:211], a[160:163], a[96:99], v[208:211]\n"
    "ds_write_addtid_b32 v151 offset:1792\n"
    "v_exp_f32_e32 v187, v187\n"
    "v_mfma_f32_16x16x32_bf16 v[200:203], a[160:163], a[64:67], v[228:231]\n"
    "ds_read_b128 a[188:191], v243 offset:22528\n"
    "v_cvt_pk_bf16_f32 v182, v184, v185\n"
    "v_mfma_f32_16x16x32_bf16 v[200:203], a[164:167], a[68:71], v[200:203]\n"
    "s_waitcnt lgkmcnt(12)\n"
    "v_cvt_pk_bf16_f32 v196, v196, v197\n"
    "v_exp_f32_e32 v199, v199\n"
    "v_mfma_f32_16x16x32_bf16 v[200:203], a[168:171], a[72:75], v[200:203]\n"
    "v_exp_f32_e32 v204, v204\n"
    "v_mfma_f32_16x16x32_bf16 v[200:203], a[172:175], a[76:79], v[200:203]\n"
    "ds_read2st64_b32 v[176:177], v249 offset0:20 offset1:21\n"
    "v_exp_f32_e32 v205, v205\n"
    "v_mfma_f32_16x16x32_bf16 v[208:211], a[172:175], a[108:111], v[208:211]\n"
    "ds_read2st64_b32 v[178:179], v249 offset0:22 offset1:23\n"
    "v_and_b32_e32 v248, 0xf0, v250\n"
    "v_lshrrev_b32_e32 v231, 3, v248\n"
    "v_mfma_f32_16x16x32_bf16 v[208:211], a[168:171], a[104:107], v[208:211]\n"
    "ds_read_b64_tr_b16 a[160:161], v244 offset:24576\n"
    "v_cvt_pk_bf16_f32 v197, v198, v199\n"
    "v_mfma_f32_16x16x32_bf16 v[216:219], a[168:171], a[136:139], v[216:219]\n"
    "s_lshr_b32 s31, s35, 8\n"
    "ds_read_b64_tr_b16 a[162:163], v244 offset:26624\n"
    "v_exp_f32_e32 v206, v206\n"
    "v_mfma_f32_16x16x32_bf16 v[216:219], a[172:175], a[140:143], v[216:219]\n"
    "s_waitcnt lgkmcnt(11)\n"
    "ds_read_b64_tr_b16 a[168:169], v245 offset:24576\n"
    "v_cvt_pk_bf16_f32 v183, v186, v187\n"
    "v_mfma_f32_16x16x32_bf16 v[216:219], a[176:179], a[144:147], v[216:219]\n"
    "buffer_load_ushort v228, v231, s[24:27], s31 offen\n"
    "ds_write_addtid_b32 v182 offset:2560\n"
    "v_mfma_f32_16x16x32_bf16 v[216:219], a[180:183], a[148:151], v[216:219]\n"
    "s_lshr_b32 s31, s36, 8\n"
    "ds_read_b64_tr_b16 a[164:165], v244 offset:57344\n"
    "v_exp_f32_e32 v207, v207\n"
    "v_mfma_f32_16x16x32_bf16 v[208:211], a[180:183], a[116:119], v[208:211]\n"
    "ds_read_b64_tr_b16 a[170:171], v245 offset:26624\n"
    "v_cvt_pk_bf16_f32 v204, v204, v205\n"
    "v_mfma_f32_16x16x32_bf16 v[208:211], a[176:179], a[112:115], v[208:211]\n"
    "ds_read_b64_tr_b16 a[172:173], v245 offset:57344\n"
    "v_exp_f32_e32 v212, v212\n"
    "v_mfma_f32_16x16x32_bf16 v[200:203], a[176:179], a[80:83], v[200:203]\n"
    "buffer_load_ushort v229, v231, s[24:27], s31 offen\n"
    "ds_read_b64_tr_b16 a[166:167], v244 offset:59392\n"
    "v_mfma_f32_16x16x32_bf16 v[200:203], a[180:183], a[84:87], v[200:203]\n"
    "s_waitcnt lgkmcnt(10)\n"
    "v_cvt_pk_bf16_f32 v205, v206, v207\n"
    "v_exp_f32_e32 v213, v213\n"
    "v_mfma_f32_16x16x32_bf16 v[200:203], a[184:187], a[88:91], v[200:203]\n"
    "s_lshr_b32 s31, s37, 8\n"
    "ds_write_addtid_b32 v183 offset:2816\n"
    "v_exp_f32_e32 v214, v214\n"
    "v_mfma_f32_16x16x32_bf16 v[200:203], a[188:191], a[92:95], v[200:203]\n"
    "ds_read_b64_tr_b16 a[174:175], v245 offset:59392\n"
    "v_exp_f32_e32 v215, v215\n"
    "v_mfma_f32_16x16x32_bf16 v[208:211], a[188:191], a[124:127], v[208:211]\n"
    "buffer_load_ushort v230, v231, s[24:27], s31 offen\n"
    "ds_read_b64_tr_b16 a[176:177], v246 offset:24576\n"
    "v_mfma_f32_16x16x32_bf16 v[208:211], a[184:187], a[120:123], v[208:211]\n"
    "s_waitcnt lgkmcnt(2)\n"
    "v_mfma_f32_16x16x32_bf16 v[216:219], a[184:187], a[152:155], v[216:219]\n"
    "s_barrier\n"
    "v_mfma_f32_16x16x32_bf16 v[216:219], a[188:191], a[156:159], v[216:219]\n"
    "ds_write_addtid_b32 v196 offset:3072\n"
    "v_cvt_pk_bf16_f32 v212, v212, v213\n"
    "v_mfma_f32_16x16x32_bf16 a[252:255], a[160:163], a[208:211], a[252:255]\n"
    "s_add_u32 s62, s57, 4096\n"
    "ds_write_addtid_b32 v197 offset:3328\n"
    "v_exp_f32_e32 v200, v200\n"
    "v_mfma_f32_16x16x32_bf16 a[252:255], a[164:167], a[212:215], a[252:255]\n"
    "buffer_load_dwordx4 a[64:67], v250, s[8:11], s62 offen offset:2048 sc1\n"
    "ds_read_b64_tr_b16 a[184:185], v246 offset:0\n"
    "v_mfma_f32_16x16x32_bf16 a[248:251], a[164:167], a[204:207], a[248:251]\n"
    "ds_write_addtid_b32 v204 offset:4096\n"
    "v_lshrrev_b32_e32 v231, 2, v250\n"
    "v_mfma_f32_16x16x32_bf16 a[248:251], a[160:163], a[200:203], a[248:251]\n"
    "ds_read_b64_tr_b16 a[178:179], v246 offset:26624\n"
    "ds_read_b64_tr_b16 a[186:187], v246 offset:2048\n"
    "v_mfma_f32_16x16x32_bf16 a[244:247], a[160:163], a[192:195], a[244:247]\n"
    "ds_read_b64_tr_b16 a[180:181], v246 offset:57344\n"
    "ds_read_b64_tr_b16 a[182:183], v246 offset:59392\n"
    "v_mfma_f32_16x16x32_bf16 a[244:247], a[164:167], a[196:199], a[244:247]\n"
    "s_waitcnt lgkmcnt(9)\n"
    "ds_write_addtid_b32 v205 offset:4352\n"
    "v_exp_f32_e32 v201, v201\n"
    "v_mfma_f32_16x16x32_bf16 v[8:11], a[168:171], a[192:195], v[8:11]\n"
    "buffer_load_dwordx4 a[68:71], v250, s[8:11], s62 offen offset:3072 sc1\n"
    "ds_write_addtid_b32 v212 offset:5120\n"
    "v_mfma_f32_16x16x32_bf16 v[8:11], a[172:175], a[196:199], v[8:11]\n"
    "s_add_u32 s62, s57, 8192\n"
    "ds_read_b64_tr_b16 a[160:161], v244 offset:0\n"
    "v_cvt_pk_bf16_f32 v213, v214, v215\n"
    "v_mfma_f32_16x16x32_bf16 v[12:15], a[172:175], a[204:207], v[12:15]\n"
    "ds_read_b64_tr_b16 a[188:189], v246 offset:32768\n"
    "v_exp_f32_e32 v202, v202\n"
    "v_mfma_f32_16x16x32_bf16 v[12:15], a[168:171], a[200:203], v[12:15]\n"
    "ds_write_addtid_b32 v213 offset:5376\n"
    "v_exp_f32_e32 v203, v203\n"
    "v_mfma_f32_16x16x32_bf16 v[16:19], a[168:171], a[208:211], v[16:19]\n"
    "ds_read_b64_tr_b16 a[190:191], v246 offset:34816\n"
    "v_cvt_pk_bf16_f32 v198, v200, v201\n"
    "v_mfma_f32_16x16x32_bf16 v[16:19], a[172:175], a[212:215], v[16:19]\n"
    "s_waitcnt lgkmcnt(6)\n"
    "ds_read_b64_tr_b16 a[168:169], v245 offset:0\n"
    "v_exp_f32_e32 v208, v208\n"
    "v_mfma_f32_16x16x32_bf16 v[36:39], a[176:179], a[208:211], v[36:39]\n"
    "buffer_load_dwordx4 a[72:75], v250, s[8:11], s62 offen offset:0 sc1\n"
    "ds_write_addtid_b32 v198 offset:3584\n"
    "v_mfma_f32_16x16x32_bf16 v[36:39], a[180:183], a[212:215], v[36:39]\n"
    "ds_read2st64_b32 v[144:145], v249 offset0:0 offset1:1\n"
    "v_exp_f32_e32 v209, v209\n"
    "v_mfma_f32_16x16x32_bf16 v[32:35], a[180:183], a[204:207], v[32:35]\n"
    "ds_read2st64_b32 v[146:147], v249 offset0:2 offset1:3\n"
    "ds_read2st64_b32 v[152:153], v249 offset0:4 offset1:5\n"
    "v_cvt_pk_bf16_f32 v199, v202, v203\n"
    "v_mfma_f32_16x16x32_bf16 v[32:35], a[176:179], a[200:203], v[32:35]\n"
    "ds_read_b64_tr_b16 a[162:163], v244 offset:2048\n"
    "ds_read_b64_tr_b16 a[164:165], v244 offset:32768\n"
    "v_mfma_f32_16x16x32_bf16 v[28:31], a[176:179], a[192:195], v[28:31]\n"
    "ds_read_b64_tr_b16 a[166:167], v244 offset:34816\n"
    "v_exp_f32_e32 v210, v210\n"
    "v_mfma_f32_16x16x32_bf16 v[28:31], a[180:183], a[196:199], v[28:31]\n"
    "s_waitcnt lgkmcnt(8)\n"
    "ds_read_b64_tr_b16 a[170:171], v245 offset:2048\n"
    "v_exp_f32_e32 v211, v211\n"
    "v_mfma_f32_16x16x32_bf16 v[28:31], a[184:187], v[156:159], v[28:31]\n"
    "buffer_load_dwordx4 a[76:79], v250, s[8:11], s62 offen offset:1024 sc1\n"
    "ds_read2st64_b32 v[154:155], v249 offset0:6 offset1:7\n"
    "v_mfma_f32_16x16x32_bf16 v[28:31], a[188:191], v[160:163], v[28:31]\n"
    "ds_read_b64_tr_b16 a[172:173], v245 offset:32768\n"
    "ds_read2st64_b32 v[184:185], v249 offset0:8 offset1:9\n"
    "v_mfma_f32_16x16x32_bf16 v[32:35], a[188:191], v[168:171], v[32:35]\n"
    "ds_read2st64_b32 v[186:187], v249 offset0:10 offset1:11\n"
    "v_cvt_pk_bf16_f32 v206, v208, v209\n"
    "v_mfma_f32_16x16x32_bf16 v[32:35], a[184:187], v[164:167], v[32:35]\n"
    "ds_read_b64_tr_b16 a[174:175], v245 offset:34816\n"
    "v_exp_f32_e32 v216, v216\n"
    "v_mfma_f32_16x16x32_bf16 v[36:39], a[184:187], v[172:175], v[36:39]\n"
    "ds_write_addtid_b32 v199 offset:3840\n"
    "v_exp_f32_e32 v217, v217\n"
    "v_mfma_f32_16x16x32_bf16 v[36:39], a[188:191], v[176:179], v[36:39]\n"
    "s_waitcnt lgkmcnt(7)\n"
    "ds_read_b64_tr_b16 a[176:177], v245 offset:8192\n"
    "ds_read_b64_tr_b16 a[178:179], v245 offset:10240\n"
    "v_cvt_pk_bf16_f32 v207, v210, v211\n"
    "v_mfma_f32_16x16x32_bf16 a[252:255], a[160:163], v[172:175], a[252:255]\n"
    "buffer_load_dwordx4 a[80:83], v250, s[8:11], s62 offen offset:2048 sc1\n"
    "ds_write_addtid_b32 v206 offset:4608\n"
    "v_mfma_f32_16x16x32_bf16 a[252:255], a[164:167], v[176:179], a[252:255]\n"
    "ds_read_b64_tr_b16 a[184:185], v244 offset:8192\n"
    "v_exp_f32_e32 v218, v218\n"
    "v_mfma_f32_16x16x32_bf16 a[248:251], a[164:167], v[168:171], a[248:251]\n"
    "ds_read_b64_tr_b16 a[180:181], v245 offset:40960\n"
    "v_exp_f32_e32 v219, v219\n"
    "v_mfma_f32_16x16x32_bf16 a[248:251], a[160:163], v[164:167], a[248:251]\n"
    "ds_write_addtid_b32 v207 offset:4864\n"
    "v_cvt_pk_bf16_f32 v214, v216, v217\n"
    "v_mfma_f32_16x16x32_bf16 a[244:247], a[160:163], v[156:159], a[244:247]\n"
    "ds_read_b64_tr_b16 a[182:183], v245 offset:43008\n"
    "v_cvt_pk_bf16_f32 v215, v218, v219\n"
    "v_mfma_f32_16x16x32_bf16 a[244:247], a[164:167], v[160:163], a[244:247]\n"
    "s_waitcnt lgkmcnt(8)\n"
    "ds_write_addtid_b32 v214 offset:5632\n"
    "v_subrev_u32_e32 v231, 0x80, v231\n"
    "v_mfma_f32_16x16x32_bf16 v[8:11], a[168:171], v[156:159], v[8:11]\n"
    "buffer_load_dwordx4 a[84:87], v250, s[8:11], s62 offen offset:3072 sc1\n"
    "ds_read_b64_tr_b16 a[186:187], v244 offset:10240\n"
    "v_mfma_f32_16x16x32_bf16 v[8:11], a[172:175], v[160:163], v[8:11]\n"
    "s_add_u32 s62, s57, 12288\n"
    "ds_read_b64_tr_b16 a[160:161], v244 offset:16384\n"
    "ds_write_addtid_b32 v215 offset:5888\n"
    "v_mfma_f32_16x16x32_bf16 v[12:15], a[172:175], v[168:171], v[12:15]\n"
    "ds_read_b64_tr_b16 a[162:163], v244 offset:18432\n"
    "ds_read_b64_tr_b16 a[164:165], v244 offset:49152\n"
    "v_mfma_f32_16x16x32_bf16 v[12:15], a[168:171], v[164:167], v[12:15]\n"
    "ds_read_b64_tr_b16 a[188:189], v244 offset:40960\n"
    "ds_read_b64_tr_b16 a[166:167], v244 offset:51200\n"
    "v_mfma_f32_16x16x32_bf16 v[16:19], a[168:171], v[172:175], v[16:19]\n"
    "ds_read_b64_tr_b16 a[190:191], v244 offset:43008\n"
    "v_min_u32_e32 v231, 2.0, v231\n"
    "v_mfma_f32_16x16x32_bf16 v[16:19], a[172:175], v[176:179], v[16:19]\n"
    "s_waitcnt lgkmcnt(9)\n"
    "s_waitcnt lgkmcnt(5)\n"
    "v_mfma_f32_16x16x32_bf16 v[16:19], a[176:179], v[180:183], v[16:19]\n"
    "s_barrier\n"
    "v_mfma_f32_16x16x32_bf16 v[16:19], a[180:183], v[184:187], v[16:19]\n"
    "buffer_load_dwordx4 a[88:91], v250, s[8:11], s62 offen offset:0 sc1\n"
    "ds_read2st64_b32 v[200:201], v249 offset0:12 offset1:13\n"
    "v_mfma_f32_16x16x32_bf16 v[12:15], a[180:183], v[152:155], v[12:15]\n"
    "ds_read2st64_b32 v[202:203], v249 offset0:14 offset1:15\n"
    "v_lshrrev_b32_e32 v252, 4, v250\n"
    "v_mfma_f32_16x16x32_bf16 v[12:15], a[176:179], v[148:151], v[12:15]\n"
    "ds_read2st64_b32 v[208:209], v249 offset0:16 offset1:17\n"
    "v_lshlrev_b32_e32 v239, 5, v248\n"
    "v_mfma_f32_16x16x32_bf16 v[8:11], a[176:179], v[140:143], v[8:11]\n"
    "ds_read_b64_tr_b16 a[168:169], v245 offset:16384\n"
    "ds_read2st64_b32 v[210:211], v249 offset0:18 offset1:19\n"
    "v_mfma_f32_16x16x32_bf16 v[8:11], a[180:183], v[144:147], v[8:11]\n"
    "s_waitcnt lgkmcnt(5)\n"
    "ds_read2st64_b32 v[216:217], v249 offset0:20 offset1:21\n"
    "ds_read2st64_b32 v[218:219], v249 offset0:22 offset1:23\n"
    "v_mfma_f32_16x16x32_bf16 a[244:247], a[184:187], v[140:143], a[244:247]\n"
    "buffer_load_dwordx4 a[92:95], v250, s[8:11], s62 offen offset:1024 sc1\n"
    "v_mfma_f32_16x16x32_bf16 a[244:247], a[188:191], v[144:147], a[244:247]\n"
    "ds_read_b64_tr_b16 a[170:171], v245 offset:18432\n"
    "ds_read_b64_tr_b16 a[172:173], v245 offset:49152\n"
    "v_mfma_f32_16x16x32_bf16 a[248:251], a[188:191], v[152:155], a[248:251]\n"
    "ds_read_b64_tr_b16 a[176:177], v246 offset:8192\n"
    "v_and_or_b32 v239, v252, 48, v239\n"
    "v_mfma_f32_16x16x32_bf16 a[248:251], a[184:187], v[148:151], a[248:251]\n"
    "ds_read_b64_tr_b16 a[174:175], v245 offset:51200\n"
    "v_lshrrev_b32_e32 v248, 2, v248\n"
    "v_mfma_f32_16x16x32_bf16 a[252:255], a[184:187], v[180:183], a[252:255]\n"
    "ds_read_b64_tr_b16 a[178:179], v246 offset:10240\n"
    "v_lshrrev_b32_e32 v253, 4, v252\n"
    "v_mfma_f32_16x16x32_bf16 a[252:255], a[188:191], v[184:187], a[252:255]\n"
    "s_waitcnt lgkmcnt(5)\n"
    "ds_read_b64_tr_b16 a[184:185], v246 offset:16384\n"
    "v_lshl_add_u32 v248, v253, 7, v248\n"
    "v_mfma_f32_16x16x32_bf16 a[252:255], a[160:163], v[212:215], a[252:255]\n"
    "buffer_load_dwordx4 a[96:99], v250, s[8:11], s62 offen offset:2048 sc1\n"
    "v_mfma_f32_16x16x32_bf16 a[252:255], a[164:167], v[216:219], a[252:255]\n"
    "ds_read_b64_tr_b16 a[180:181], v246 offset:40960\n"
    "v_bfe_u32 v255, v252, 2, 2\n"
    "v_mfma_f32_16x16x32_bf16 a[248:251], a[164:167], v[208:211], a[248:251]\n"
    "ds_read_b64_tr_b16 a[186:187], v246 offset:18432\n"
    "v_max_u32_e32 v238, 1, v255\n"
    "v_mfma_f32_16x16x32_bf16 a[248:251], a[160:163], v[204:207], a[248:251]\n"
    "ds_read_b64_tr_b16 a[182:183], v246 offset:43008\n"
    "v_add_u32_e32 v238, s32, v238\n"
    "v_mfma_f32_16x16x32_bf16 a[244:247], a[160:163], v[196:199], a[244:247]\n"
    "v_and_b32_e32 v238, 3, v238\n"
    "v_lshlrev_b32_e32 v238, 10, v238\n"
    "v_mfma_f32_16x16x32_bf16 a[244:247], a[164:167], v[200:203], a[244:247]\n"
    "s_waitcnt lgkmcnt(5)\n"
    "ds_read_b64_tr_b16 a[160:161], v247 offset:24576\n"
    "v_and_b32_e32 v252, 3, v252\n"
    "v_mfma_f32_16x16x32_bf16 v[8:11], a[168:171], v[196:199], v[8:11]\n"
    "buffer_load_dwordx4 a[100:103], v250, s[8:11], s62 offen offset:3072 sc1\n"
    "v_mfma_f32_16x16x32_bf16 v[8:11], a[172:175], v[200:203], v[8:11]\n"
    "s_add_u32 s62, s57, 16384\n"
    "ds_read_b64_tr_b16 a[188:189], v246 offset:49152\n"
    "v_xor_b32_e32 v253, v253, v255\n"
    "v_mfma_f32_16x16x32_bf16 v[12:15], a[172:175], v[208:211], v[12:15]\n"
    "ds_read_b64_tr_b16 a[162:163], v247 offset:26624\n"
    "v_lshl_or_b32 v252, v253, 2, v252\n"
    "v_mfma_f32_16x16x32_bf16 v[12:15], a[168:171], v[204:207], v[12:15]\n"
    "ds_read_b64_tr_b16 a[190:191], v246 offset:51200\n"
    "v_cvt_pk_bf16_f32 v11, v10, v11\n"
    "v_mfma_f32_16x16x32_bf16 v[16:19], a[168:171], v[212:215], v[16:19]\n"
    "v_cvt_pk_bf16_f32 v10, v8, v9\n"
    "v_accvgpr_read_b32 v8, a244\n"
    "v_mfma_f32_16x16x32_bf16 v[16:19], a[172:175], v[216:219], v[16:19]\n"
    "s_waitcnt lgkmcnt(4)\n"
    "v_accvgpr_read_b32 v9, a245\n"
    "v_accvgpr_read_b32 v255, a247\n"
    "v_mfma_f32_16x16x32_bf16 v[36:39], a[176:179], v[180:183], v[36:39]\n"
    "ds_read_b64_tr_b16 a[168:169], v247 offset:0\n"
    "v_cvt_pk_bf16_f32 v8, v8, v9\n"
    "v_mfma_f32_16x16x32_bf16 v[36:39], a[180:183], v[184:187], v[36:39]\n"
    "v_accvgpr_read_b32 v9, a246\n"
    "v_cvt_pk_bf16_f32 v9, v9, v255\n"
    "v_mfma_f32_16x16x32_bf16 v[32:35], a[180:183], v[152:155], v[32:35]\n"
    "buffer_store_dwordx4 v[8:11], v250, s[8:11], s82 offen offset:0 sc1\n"
    "v_mfma_f32_16x16x32_bf16 v[32:35], a[176:179], v[148:151], v[32:35]\n"
    "ds_read_b64_tr_b16 a[164:165], v247 offset:57344\n"
    "ds_read_b64_tr_b16 a[166:167], v247 offset:59392\n"
    "v_mfma_f32_16x16x32_bf16 v[28:31], a[176:179], v[140:143], v[28:31]\n"
    "ds_read_b64_tr_b16 a[170:171], v247 offset:2048\n"
    "v_cvt_pk_bf16_f32 v15, v14, v15\n"
    "v_mfma_f32_16x16x32_bf16 v[28:31], a[180:183], v[144:147], v[28:31]\n"
    "s_waitcnt lgkmcnt(4)\n"
    "ds_read_b64_tr_b16 a[176:177], v247 offset:8192\n"
    "v_mfma_f32_16x16x32_bf16 v[28:31], a[184:187], v[196:199], v[28:31]\n"
    "buffer_load_dwordx4 a[104:107], v250, s[8:11], s62 offen offset:0 sc1\n"
    "v_mfma_f32_16x16x32_bf16 v[28:31], a[188:191], v[200:203], v[28:31]\n"
    "ds_read_b64_tr_b16 a[172:173], v247 offset:32768\n"
    "v_cvt_pk_bf16_f32 v14, v12, v13\n"
    "v_mfma_f32_16x16x32_bf16 v[32:35], a[188:191], v[208:211], v[32:35]\n"
    "ds_read_b64_tr_b16 a[178:179], v247 offset:10240\n"
    "v_accvgpr_read_b32 v12, a248\n"
    "v_mfma_f32_16x16x32_bf16 v[32:35], a[184:187], v[204:207], v[32:35]\n"
    "ds_read_b64_tr_b16 a[174:175], v247 offset:34816\n"
    "v_cvt_pk_bf16_f32 v28, v28, v29\n"
    "v_mfma_f32_16x16x32_bf16 v[36:39], a[184:187], v[212:215], v[36:39]\n"
    "ds_read_b64_tr_b16 a[180:181], v247 offset:40960\n"
    "v_mfma_f32_16x16x32_bf16 v[36:39], a[188:191], v[216:219], v[36:39]\n"
    "s_waitcnt lgkmcnt(6)\n"
    "ds_read_b64_tr_b16 a[182:183], v247 offset:43008\n"
    "v_accvgpr_read_b32 v13, a249\n"
    "v_mfma_f32_16x16x32_bf16 v[56:59], a[160:163], a[208:211], v[56:59]\n"
    "buffer_load_dwordx4 a[108:111], v250, s[8:11], s62 offen offset:1024 sc1\n"
    "v_mfma_f32_16x16x32_bf16 v[56:59], a[164:167], a[212:215], v[56:59]\n"
    "ds_read_b64_tr_b16 a[184:185], v247 offset:16384\n"
    "v_accvgpr_read_b32 v255, a251\n"
    "v_mfma_f32_16x16x32_bf16 v[52:55], a[164:167], a[204:207], v[52:55]\n"
    "v_cvt_pk_bf16_f32 v12, v12, v13\n"
    "v_accvgpr_read_b32 v13, a250\n"
    "v_mfma_f32_16x16x32_bf16 v[52:55], a[160:163], a[200:203], v[52:55]\n"
    "ds_read_b64_tr_b16 a[186:187], v247 offset:18432\n"
    "v_cvt_pk_bf16_f32 v13, v13, v255\n"
    "v_mfma_f32_16x16x32_bf16 v[48:51], a[160:163], a[192:195], v[48:51]\n"
    "buffer_store_dwordx4 v[12:15], v250, s[8:11], s28 offen offset:0 sc1\n"
    "v_mfma_f32_16x16x32_bf16 v[48:51], a[164:167], a[196:199], v[48:51]\n"
    "s_waitcnt lgkmcnt(4)\n"
    "ds_read_b64_tr_b16 a[160:161], v244 offset:28672\n"
    "v_cvt_pk_bf16_f32 v19, v18, v19\n"
    "v_mfma_f32_16x16x32_bf16 v[48:51], a[168:171], v[156:159], v[48:51]\n"
    "s_add_u32 s62, s57, 16384\n"
    "ds_read_b64_tr_b16 a[162:163], v244 offset:30720\n"
    "v_cvt_pk_bf16_f32 v18, v16, v17\n"
    "v_mfma_f32_16x16x32_bf16 v[48:51], a[172:175], v[160:163], v[48:51]\n"
    "buffer_load_dwordx4 a[112:115], v250, s[8:11], s62 offen offset:2048 sc1\n"
    "v_mfma_f32_16x16x32_bf16 v[52:55], a[172:175], v[168:171], v[52:55]\n"
    "ds_read_b64_tr_b16 a[188:189], v247 offset:49152\n"
    "v_accvgpr_read_b32 v16, a252\n"
    "v_mfma_f32_16x16x32_bf16 v[52:55], a[168:171], v[164:167], v[52:55]\n"
    "ds_read_b64_tr_b16 a[164:165], v244 offset:61440\n"
    "v_mfma_f32_16x16x32_bf16 v[56:59], a[168:171], v[172:175], v[56:59]\n"
    "ds_read_b64_tr_b16 a[190:191], v247 offset:51200\n"
    "v_accvgpr_read_b32 v17, a253\n"
    "v_mfma_f32_16x16x32_bf16 v[56:59], a[172:175], v[176:179], v[56:59]\n"
    "s_waitcnt lgkmcnt(7)\n"
    "ds_read_b64_tr_b16 a[166:167], v244 offset:63488\n"
    "v_cvt_pk_bf16_f32 v29, v30, v31\n"
    "v_mfma_f32_16x16x32_bf16 v[56:59], a[176:179], v[180:183], v[56:59]\n"
    "v_mov_b32_e32 v253, 0x3f80\n"
    "v_accvgpr_read_b32 v255, a255\n"
    "v_mfma_f32_16x16x32_bf16 v[56:59], a[180:183], v[184:187], v[56:59]\n"
    "ds_read_b64_tr_b16 a[168:169], v244 offset:4096\n"
    "v_cvt_pk_bf16_f32 v16, v16, v17\n"
    "v_mfma_f32_16x16x32_bf16 v[52:55], a[180:183], v[152:155], v[52:55]\n"
    "ds_read_b64_tr_b16 a[170:171], v244 offset:6144\n"
    "v_cvt_pk_bf16_f32 v32, v32, v33\n"
    "v_mfma_f32_16x16x32_bf16 v[52:55], a[176:179], v[148:151], v[52:55]\n"
    "buffer_load_dwordx4 a[116:119], v250, s[8:11], s62 offen offset:3072 sc1\n"
    "v_mfma_f32_16x16x32_bf16 v[48:51], a[176:179], v[140:143], v[48:51]\n"
    "s_add_u32 s62, s57, 20480\n"
    "v_accvgpr_read_b32 v17, a254\n"
    "v_cvt_pk_bf16_f32 v17, v17, v255\n"
    "v_mfma_f32_16x16x32_bf16 v[48:51], a[180:183], v[144:147], v[48:51]\n"
    "s_waitcnt lgkmcnt(3)\n"
    "ds_read_b64_tr_b16 a[172:173], v244 offset:36864\n"
    "v_mfma_f32_16x16x32_bf16 v[48:51], a[184:187], v[196:199], v[48:51]\n"
    "buffer_store_dwordx4 v[16:19], v250, s[8:11], s29 offen offset:0 sc1\n"
    "v_mfma_f32_16x16x32_bf16 v[48:51], a[188:191], v[200:203], v[48:51]\n"
    "v_mov_b64_e32 v[232:233], 0\n"
    "v_mov_b64_e32 v[236:237], 0\n"
    "v_mfma_f32_16x16x32_bf16 v[52:55], a[188:191], v[208:211], v[52:55]\n"
    "ds_read_b64_tr_b16 a[174:175], v244 offset:38912\n"
    "v_mfma_f32_16x16x32_bf16 v[52:55], a[184:187], v[204:207], v[52:55]\n"
    "ds_read_b64_tr_b16 a[176:177], v244 offset:12288\n"
    "v_mfma_f32_16x16x32_bf16 v[56:59], a[184:187], v[212:215], v[56:59]\n"
    "ds_read_b64_tr_b16 a[178:179], v244 offset:14336\n"
    "v_cvt_pk_bf16_f32 v30, v48, v49\n"
    "v_mfma_f32_16x16x32_bf16 v[56:59], a[188:191], v[216:219], v[56:59]\n"
    "s_waitcnt lgkmcnt(6)\n"
    "ds_read_b64_tr_b16 a[184:185], v244 offset:20480\n"
    "v_mfma_f32_16x16x32_bf16 v[76:79], a[160:163], a[208:211], v[76:79]\n"
    "buffer_load_dwordx4 a[120:123], v250, s[8:11], s62 offen offset:0 sc1\n"
    "v_mfma_f32_16x16x32_bf16 v[76:79], a[164:167], a[212:215], v[76:79]\n"
    ".long 0xbedf00ff\n"
    ".Lat_w11:\n"
    "s_setpc_b64 s[2:3]\n"
    "ds_read_b64_tr_b16 a[186:187], v244 offset:22528\n"
    "v_mfma_f32_16x16x32_bf16 v[72:75], a[164:167], a[204:207], v[72:75]\n"
    "ds_read_b64_tr_b16 a[180:181], v244 offset:45056\n"
    "v_cvt_pk_bf16_f32 v36, v36, v37\n"
    "v_mfma_f32_16x16x32_bf16 v[72:75], a[160:163], a[200:203], v[72:75]\n"
    "ds_read_b64_tr_b16 a[182:183], v244 offset:47104\n"
    "v_mfma_f32_16x16x32_bf16 v[68:71], a[160:163], a[192:195], v[68:71]\n"
    "ds_read_b64_tr_b16 a[188:189], v244 offset:53248\n"
    "v_mfma_f32_16x16x32_bf16 v[68:71], a[164:167], a[196:199], v[68:71]\n"
    "s_waitcnt lgkmcnt(7)\n"
    "ds_read_b64_tr_b16 a[190:191], v244 offset:55296\n"
    "v_cvt_pk_bf16_f32 v37, v38, v39\n"
    "v_mfma_f32_16x16x32_bf16 v[68:71], a[168:171], v[156:159], v[68:71]\n"
    "v_cmp_eq_u32_e32 vcc, 0, v252\n"
    "v_cvt_pk_bf16_f32 v31, v50, v51\n"
    "v_mfma_f32_16x16x32_bf16 v[68:71], a[172:175], v[160:163], v[68:71]\n"
    "ds_read_b64_tr_b16 a[160:161], v245 offset:28672\n"
    "v_mfma_f32_16x16x32_bf16 v[72:75], a[172:175], v[168:171], v[72:75]\n"
    "buffer_store_dwordx4 v[28:31], v250, s[8:11], s82 offen offset:1024 sc1\n"
    "v_mfma_f32_16x16x32_bf16 v[72:75], a[168:171], v[164:167], v[72:75]\n"
    "ds_read_b64_tr_b16 a[162:163], v245 offset:30720\n"
    "v_mfma_f32_16x16x32_bf16 v[76:79], a[168:171], v[172:175], v[76:79]\n"
    "ds_read_b64_tr_b16 a[164:165], v245 offset:61440\n"
    "v_mfma_f32_16x16x32_bf16 v[76:79], a[172:175], v[176:179], v[76:79]\n"
    "s_waitcnt lgkmcnt(5)\n"
    "ds_read_b64_tr_b16 a[168:169], v245 offset:4096\n"
    "v_mfma_f32_16x16x32_bf16 v[76:79], a[176:179], v[180:183], v[76:79]\n"
    "buffer_load_dwordx4 a[124:127], v250, s[8:11], s62 offen offset:1024 sc1\n"
    "v_mfma_f32_16x16x32_bf16 v[76:79], a[180:183], v[184:187], v[76:79]\n"
    "v_cvt_pk_bf16_f32 v33, v34, v35\n"
    "v_mfma_f32_16x16x32_bf16 v[72:75], a[180:183], v[152:155], v[72:75]\n"
    "ds_read_b64_tr_b16 a[170:171], v245 offset:6144\n"
    "v_mfma_f32_16x16x32_bf16 v[72:75], a[176:179], v[148:151], v[72:75]\n"
    "ds_read_b64_tr_b16 a[166:167], v245 offset:63488\n"
    "v_mfma_f32_16x16x32_bf16 v[68:71], a[176:179], v[140:143], v[68:71]\n"
    "ds_read_b64_tr_b16 a[172:173], v245 offset:36864\n"
    "v_cndmask_b32_e32 v234, 0, v253, vcc\n"
    "v_mfma_f32_16x16x32_bf16 v[68:71], a[180:183], v[144:147], v[68:71]\n"
    "s_waitcnt lgkmcnt(7)\n"
    "ds_read_b64_tr_b16 a[176:177], v245 offset:12288\n"
    "v_cmp_eq_u32_e32 vcc, 1, v252\n"
    "v_mfma_f32_16x16x32_bf16 v[68:71], a[184:187], v[196:199], v[68:71]\n"
    "ds_read_b64_tr_b16 a[178:179], v245 offset:14336\n"
    "v_cndmask_b32_e64 v234, v234, 1.0, vcc\n"
    "v_mfma_f32_16x16x32_bf16 v[68:71], a[188:191], v[200:203], v[68:71]\n"
    "v_cvt_pk_bf16_f32 v34, v52, v53\n"
    "v_cvt_pk_bf16_f32 v35, v54, v55\n"
    "v_mfma_f32_16x16x32_bf16 v[72:75], a[188:191], v[208:211], v[72:75]\n"
    "v_mfma_f32_16x16x32_bf16 v[72:75], a[184:187], v[204:207], v[72:75]\n"
    "ds_read_b64_tr_b16 a[174:175], v245 offset:38912\n"
    "v_mfma_f32_16x16x32_bf16 v[76:79], a[184:187], v[212:215], v[76:79]\n"
    "buffer_store_dwordx4 v[32:35], v250, s[8:11], s28 offen offset:1024 sc1\n"
    "v_mfma_f32_16x16x32_bf16 v[76:79], a[188:191], v[216:219], v[76:79]\n"
    "s_waitcnt lgkmcnt(4)\n"
    "ds_read_b64_tr_b16 a[184:185], v245 offset:20480\n"
    "v_cvt_pk_bf16_f32 v68, v68, v69\n"
    "v_mfma_f32_16x16x32_bf16 v[96:99], a[160:163], a[208:211], v[96:99]\n"
    "ds_read_b64_tr_b16 a[186:187], v245 offset:22528\n"
    "v_cvt_pk_bf16_f32 v72, v72, v73\n"
    "v_mfma_f32_16x16x32_bf16 v[96:99], a[164:167], a[212:215], v[96:99]\n"
    "buffer_load_dwordx4 a[128:131], v250, s[8:11], s62 offen offset:2048 sc1\n"
    "v_mfma_f32_16x16x32_bf16 v[92:95], a[164:167], a[204:207], v[92:95]\n"
    "ds_read_b64_tr_b16 a[180:181], v245 offset:45056\n"
    "v_cvt_pk_bf16_f32 v76, v76, v77\n"
    "v_mfma_f32_16x16x32_bf16 v[92:95], a[160:163], a[200:203], v[92:95]\n"
    "ds_read_b64_tr_b16 a[188:189], v245 offset:53248\n"
    "v_cvt_pk_bf16_f32 v38, v56, v57\n"
    "v_mfma_f32_16x16x32_bf16 v[88:91], a[160:163], a[192:195], v[88:91]\n"
    "ds_read_b64_tr_b16 a[182:183], v245 offset:47104\n"
    "v_mfma_f32_16x16x32_bf16 v[88:91], a[164:167], a[196:199], v[88:91]\n"
    "s_waitcnt lgkmcnt(5)\n"
    "ds_read_b64_tr_b16 a[160:161], v246 offset:28672\n"
    "v_cmp_eq_u32_e32 vcc, 2, v252\n"
    "v_mfma_f32_16x16x32_bf16 v[88:91], a[168:171], v[156:159], v[88:91]\n"
    "s_nop 0\n"
    "v_cndmask_b32_e32 v235, 0, v253, vcc\n"
    "v_mfma_f32_16x16x32_bf16 v[88:91], a[172:175], v[160:163], v[88:91]\n"
    "ds_read_b64_tr_b16 a[190:191], v245 offset:55296\n"
    "v_mfma_f32_16x16x32_bf16 v[92:95], a[172:175], v[168:171], v[92:95]\n"
    "v_cvt_pk_bf16_f32 v39, v58, v59\n"
    "v_mfma_f32_16x16x32_bf16 v[92:95], a[168:171], v[164:167], v[92:95]\n"
    "buffer_load_dwordx4 a[132:135], v250, s[8:11], s62 offen offset:3072 sc1\n"
    "v_mfma_f32_16x16x32_bf16 v[96:99], a[168:171], v[172:175], v[96:99]\n"
    "s_add_u32 s62, s57, 24576\n"
    "v_cvt_pk_bf16_f32 v73, v74, v75\n"
    "v_mfma_f32_16x16x32_bf16 v[96:99], a[172:175], v[176:179], v[96:99]\n"
    "s_waitcnt lgkmcnt(2)\n"
    "ds_read_b64_tr_b16 a[162:163], v246 offset:30720\n"
    "v_mfma_f32_16x16x32_bf16 v[96:99], a[176:179], v[180:183], v[96:99]\n"
    "buffer_store_dwordx4 v[36:39], v250, s[8:11], s29 offen offset:1024 sc1\n"
    "v_mfma_f32_16x16x32_bf16 v[96:99], a[180:183], v[184:187], v[96:99]\n"
    "ds_read_b64_tr_b16 a[168:169], v246 offset:4096\n"
    "v_mfma_f32_16x16x32_bf16 v[92:95], a[180:183], v[152:155], v[92:95]\n"
    "ds_read_b64_tr_b16 a[164:165], v246 offset:61440\n"
    "v_mfma_f32_16x16x32_bf16 v[92:95], a[176:179], v[148:151], v[92:95]\n"
    "ds_read_b64_tr_b16 a[166:167], v246 offset:63488\n"
    "v_mfma_f32_16x16x32_bf16 v[88:91], a[176:179], v[140:143], v[88:91]\n"
    "ds_read_b64_tr_b16 a[170:171], v246 offset:6144\n"
    "v_cvt_pk_bf16_f32 v69, v70, v71\n"
    "v_mfma_f32_16x16x32_bf16 v[88:91], a[180:183], v[144:147], v[88:91]\n"
    "s_waitcnt lgkmcnt(5)\n"
    "ds_read_b64_tr_b16 a[176:177], v246 offset:12288\n"
    "v_cvt_pk_bf16_f32 v77, v78, v79\n"
    "v_mfma_f32_16x16x32_bf16 v[88:91], a[184:187], v[196:199], v[88:91]\n"
    "buffer_load_dwordx4 a[136:139], v250, s[8:11], s62 offen offset:0 sc1\n"
    "v_mfma_f32_16x16x32_bf16 v[88:91], a[188:191], v[200:203], v[88:91]\n"
    "ds_read_b64_tr_b16 a[172:173], v246 offset:36864\n"
    "v_mfma_f32_16x16x32_bf16 v[92:95], a[188:191], v[208:211], v[92:95]\n"
    "v_mfma_f32_16x16x32_bf16 v[92:95], a[184:187], v[204:207], v[92:95]\n"
    "ds_read_b64_tr_b16 a[174:175], v246 offset:38912\n"
    "v_cvt_pk_bf16_f32 v70, v88, v89\n"
    "v_mfma_f32_16x16x32_bf16 v[96:99], a[184:187], v[212:215], v[96:99]\n"
    "v_mfma_f32_16x16x32_bf16 v[96:99], a[188:191], v[216:219], v[96:99]\n"
    "s_waitcnt lgkmcnt(4)\n"
    "ds_read_b64_tr_b16 a[178:179], v246 offset:14336\n"
    "v_cmp_eq_u32_e32 vcc, 3, v252\n"
    "v_mfma_f32_16x16x32_bf16 v[116:119], a[160:163], a[208:211], v[116:119]\n"
    "ds_read_b64_tr_b16 a[184:185], v246 offset:20480\n"
    "v_cvt_pk_bf16_f32 v71, v90, v91\n"
    "v_mfma_f32_16x16x32_bf16 v[116:119], a[164:167], a[212:215], v[116:119]\n"
    "v_cvt_pk_bf16_f32 v74, v92, v93\n"
    "v_mfma_f32_16x16x32_bf16 v[112:115], a[164:167], a[204:207], v[112:115]\n"
    "buffer_store_dwordx4 v[68:71], v250, s[8:11], s82 offen offset:2048 sc1\n"
    "v_mfma_f32_16x16x32_bf16 v[112:115], a[160:163], a[200:203], v[112:115]\n"
    "ds_read_b64_tr_b16 a[180:181], v246 offset:45056\n"
    "v_mfma_f32_16x16x32_bf16 v[108:111], a[160:163], a[192:195], v[108:111]\n"
    "ds_read_b64_tr_b16 a[182:183], v246 offset:47104\n"
    "v_mfma_f32_16x16x32_bf16 v[108:111], a[164:167], a[196:199], v[108:111]\n"
    "s_waitcnt lgkmcnt(4)\n"
    "ds_read_b64_tr_b16 a[186:187], v246 offset:22528\n"
    "v_mfma_f32_16x16x32_bf16 v[108:111], a[168:171], v[156:159], v[108:111]\n"
    "buffer_load_dwordx4 a[140:143], v250, s[8:11], s62 offen offset:1024 sc1\n"
    "v_mfma_f32_16x16x32_bf16 v[108:111], a[172:175], v[160:163], v[108:111]\n"
    "ds_read_b64_tr_b16 a[188:189], v246 offset:53248\n"
    "v_cndmask_b32_e64 v235, v235, 1.0, vcc\n"
    "v_mfma_f32_16x16x32_bf16 v[112:115], a[172:175], v[168:171], v[112:115]\n"
    "v_mfma_f32_16x16x32_bf16 v[112:115], a[168:171], v[164:167], v[112:115]\n"
    "ds_read_b64_tr_b16 a[160:161], v247 offset:28672\n"
    "v_mfma_f32_16x16x32_bf16 v[116:119], a[168:171], v[172:175], v[116:119]\n"
    "ds_read_b64_tr_b16 a[190:191], v246 offset:55296\n"
    "v_cvt_pk_bf16_f32 v75, v94, v95\n"
    "v_mfma_f32_16x16x32_bf16 v[116:119], a[172:175], v[176:179], v[116:119]\n"
    "s_waitcnt lgkmcnt(4)\n"
    "ds_read_b64_tr_b16 a[162:163], v247 offset:30720\n"
    "v_cvt_pk_bf16_f32 v78, v96, v97\n"
    "v_mfma_f32_16x16x32_bf16 v[116:119], a[176:179], v[180:183], v[116:119]\n"
    "ds_read_b64_tr_b16 a[164:165], v247 offset:61440\n"
    "v_mfma_f32_16x16x32_bf16 v[116:119], a[180:183], v[184:187], v[116:119]\n"
    "v_mfma_f32_16x16x32_bf16 v[112:115], a[180:183], v[152:155], v[112:115]\n"
    "ds_read_b64_tr_b16 a[168:169], v247 offset:4096\n"
    "v_mfma_f32_16x16x32_bf16 v[112:115], a[176:179], v[148:151], v[112:115]\n"
    "ds_read_b64_tr_b16 a[166:167], v247 offset:63488\n"
    "v_mfma_f32_16x16x32_bf16 v[108:111], a[176:179], v[140:143], v[108:111]\n"
    "buffer_store_dwordx4 v[72:75], v250, s[8:11], s28 offen offset:2048 sc1\n"
    "v_mfma_f32_16x16x32_bf16 v[108:111], a[180:183], v[144:147], v[108:111]\n"
    "s_waitcnt lgkmcnt(4)\n"
    "ds_read_b64_tr_b16 a[170:171], v247 offset:6144\n"
    "v_mfma_f32_16x16x32_bf16 v[108:111], a[184:187], v[196:199], v[108:111]\n"
    "ds_read_b64_tr_b16 a[176:177], v247 offset:12288\n"
    "v_mfma_f32_16x16x32_bf16 v[108:111], a[188:191], v[200:203], v[108:111]\n"
    "buffer_load_dwordx4 a[144:147], v250, s[8:11], s62 offen offset:2048 sc1\n"
    "v_mfma_f32_16x16x32_bf16 v[112:115], a[188:191], v[208:211], v[112:115]\n"
    "ds_read_b64_tr_b16 a[172:173], v247 offset:36864\n"
    "v_mfma_f32_16x16x32_bf16 v[112:115], a[184:187], v[204:207], v[112:115]\n"
    "ds_read_b64_tr_b16 a[178:179], v247 offset:14336\n"
    "v_cvt_pk_bf16_f32 v108, v108, v109\n"
    "v_mfma_f32_16x16x32_bf16 v[116:119], a[184:187], v[212:215], v[116:119]\n"
    "ds_read_b64_tr_b16 a[174:175], v247 offset:38912\n"
    "v_mfma_f32_16x16x32_bf16 v[116:119], a[188:191], v[216:219], v[116:119]\n"
    "s_waitcnt lgkmcnt(5)\n"
    "ds_read_b64_tr_b16 a[180:181], v247 offset:45056\n"
    "v_cvt_pk_bf16_f32 v79, v98, v99\n"
    "v_mfma_f32_16x16x32_bf16 v[136:139], a[160:163], a[208:211], v[136:139]\n"
    "s_cmp_lg_u32 s44, 0\n"
    "s_cbranch_scc1 .Lat_glate\n"
    ".Lat_glate_ret:\n"
    "v_mfma_f32_16x16x32_bf16 v[136:139], a[164:167], a[212:215], v[136:139]\n"
    "ds_read_b64_tr_b16 a[184:185], v247 offset:20480\n"
    "v_mfma_f32_16x16x32_bf16 v[132:135], a[164:167], a[204:207], v[132:135]\n"
    "ds_read_b64_tr_b16 a[186:187], v247 offset:22528\n"
    "v_mfma_f32_16x16x32_bf16 v[132:135], a[160:163], a[200:203], v[132:135]\n"
    "buffer_load_dwordx4 a[148:151], v250, s[8:11], s62 offen offset:3072 sc1\n"
    "v_mfma_f32_16x16x32_bf16 v[128:131], a[160:163], a[192:195], v[128:131]\n"
    "s_add_u32 s62, s57, 28672\n"
    "ds_read_b64_tr_b16 a[182:183], v247 offset:47104\n"
    "v_mfma_f32_16x16x32_bf16 v[128:131], a[164:167], a[196:199], v[128:131]\n"
    "s_waitcnt lgkmcnt(4)\n"
    "v_cvt_pk_bf16_f32 v116, v116, v117\n"
    "v_mfma_f32_16x16x32_bf16 v[128:131], a[168:171], v[156:159], v[128:131]\n"
    "buffer_store_dwordx4 v[76:79], v250, s[8:11], s29 offen offset:2048 sc1\n"
    "v_mfma_f32_16x16x32_bf16 v[128:131], a[172:175], v[160:163], v[128:131]\n"
    "ds_read_b64_tr_b16 a[188:189], v247 offset:53248\n"
    "v_mfma_f32_16x16x32_bf16 v[132:135], a[172:175], v[168:171], v[132:135]\n"
    "ds_read_b64_tr_b16 a[190:191], v247 offset:55296\n"
    "v_mfma_f32_16x16x32_bf16 v[132:135], a[168:171], v[164:167], v[132:135]\n"
    "ds_read_b128 a[160:163], v240 offset:24576\n"
    "v_mfma_f32_16x16x32_bf16 v[136:139], a[168:171], v[172:175], v[136:139]\n"
    "ds_read_b128 a[164:167], v241 offset:24576\n"
    "v_mfma_f32_16x16x32_bf16 v[136:139], a[172:175], v[176:179], v[136:139]\n"
    "s_waitcnt lgkmcnt(4)\n"
    "ds_read_b128 a[168:171], v240 offset:28672\n"
    "v_mfma_f32_16x16x32_bf16 v[136:139], a[176:179], v[180:183], v[136:139]\n"
    "buffer_load_dwordx4 a[152:155], v250, s[8:11], s62 offen offset:0 sc1\n"
    "v_mfma_f32_16x16x32_bf16 v[136:139], a[180:183], v[184:187], v[136:139]\n"
    "v_mfma_f32_16x16x32_bf16 v[132:135], a[180:183], v[152:155], v[132:135]\n"
    "v_mfma_f32_16x16x32_bf16 v[132:135], a[176:179], v[148:151], v[132:135]\n"
    "v_mfma_f32_16x16x32_bf16 v[128:131], a[176:179], v[140:143], v[128:131]\n"
    "ds_read_b128 a[172:175], v241 offset:28672\n"
    "v_cvt_pk_bf16_f32 v109, v110, v111\n"
    "v_mfma_f32_16x16x32_bf16 v[128:131], a[180:183], v[144:147], v[128:131]\n"
    "s_waitcnt lgkmcnt(4)\n"
    "ds_read_b128 a[176:179], v242 offset:24576\n"
    "v_mfma_f32_16x16x32_bf16 v[128:131], a[184:187], v[196:199], v[128:131]\n"
    "v_cvt_pk_bf16_f32 v117, v118, v119\n"
    "v_mfma_f32_16x16x32_bf16 v[128:131], a[188:191], v[200:203], v[128:131]\n"
    "v_mfma_f32_16x16x32_bf16 v[132:135], a[188:191], v[208:211], v[132:135]\n"
    "buffer_load_dwordx4 a[156:159], v250, s[8:11], s62 offen offset:1024 sc1\n"
    "v_mfma_f32_16x16x32_bf16 v[132:135], a[184:187], v[204:207], v[132:135]\n"
    "ds_read_b128 a[180:183], v243 offset:24576\n"
    "v_mfma_f32_16x16x32_bf16 v[136:139], a[184:187], v[212:215], v[136:139]\n"
    "v_cvt_pk_bf16_f32 v110, v128, v129\n"
    "v_cvt_pk_bf16_f32 v111, v130, v131\n"
    "v_mfma_f32_16x16x32_bf16 v[136:139], a[188:191], v[216:219], v[136:139]\n"
    "s_waitcnt lgkmcnt(4)\n"
    "ds_read_b128 a[184:187], v242 offset:28672\n"
    "v_mfma_f32_16x16x32_bf16 v[164:167], a[160:163], a[32:35], v[224:227]\n"
    "buffer_store_dwordx4 v[108:111], v250, s[8:11], s82 offen offset:3072 sc1\n"
    "v_mfma_f32_16x16x32_bf16 v[164:167], a[164:167], a[36:39], v[164:167]\n"
    "ds_read_b128 a[188:191], v243 offset:28672\n"
    "v_mfma_f32_16x16x32_bf16 v[156:159], a[164:167], a[4:7], v[220:223]\n"
    "v_mfma_f32_16x16x32_bf16 v[156:159], a[160:163], a[0:3], v[156:159]\n"
    "s_waitcnt lgkmcnt(4)\n"
    "v_cvt_pk_bf16_f32 v112, v112, v113\n"
    "v_mfma_f32_16x16x32_bf16 v[156:159], a[168:171], a[8:11], v[156:159]\n"
    "buffer_store_dword v131, v231, s[8:11], s87 offen offset:0 sc1\n"
    "v_mfma_f32_16x16x32_bf16 v[156:159], a[172:175], a[12:15], v[156:159]\n"
    "ds_read_b128 a[160:163], v240 offset:26624\n"
    "v_cvt_pk_bf16_f32 v113, v114, v115\n"
    "v_mfma_f32_16x16x32_bf16 v[164:167], a[172:175], a[44:47], v[164:167]\n"
    "ds_read_b128 a[164:167], v241 offset:26624\n"
    "v_mfma_f32_16x16x32_bf16 v[164:167], a[168:171], a[40:43], v[164:167]\n"
    "s_waitcnt lgkmcnt(4)\n"
    "ds_read_b128 a[168:171], v240 offset:30720\n"
    "v_mfma_f32_16x16x32_bf16 v[164:167], a[176:179], a[48:51], v[164:167]\n"
    "v_cvt_pk_bf16_f32 v118, v136, v137\n"
    "v_mfma_f32_16x16x32_bf16 v[164:167], a[180:183], a[52:55], v[164:167]\n"
    "buffer_store_dword v135, v231, s[8:11], s88 offen offset:0 sc1\n"
    "v_mfma_f32_16x16x32_bf16 v[156:159], a[180:183], a[20:23], v[156:159]\n"
    "v_cvt_pk_bf16_f32 v114, v132, v133\n"
    "v_cvt_pk_bf16_f32 v119, v138, v139\n"
    "v_mfma_f32_16x16x32_bf16 v[156:159], a[176:179], a[16:19], v[156:159]\n"
    "s_waitcnt lgkmcnt(3)\n"
    "ds_read_b128 a[172:175], v241 offset:30720\n"
    "v_mfma_f32_16x16x32_bf16 v[156:159], a[184:187], a[24:27], v[156:159]\n"
    "ds_read_b128 a[176:179], v242 offset:26624\n"
    "v_cvt_pk_bf16_f32 v115, v134, v135\n"
    "v_mfma_f32_16x16x32_bf16 v[156:159], a[188:191], a[28:31], v[156:159]\n"
    "ds_read_b128 a[180:183], v243 offset:26624\n"
    "v_mfma_f32_16x16x32_bf16 v[164:167], a[188:191], a[60:63], v[164:167]\n"
    "buffer_store_dwordx4 v[112:115], v250, s[8:11], s28 offen offset:3072 sc1\n"
    "v_mfma_f32_16x16x32_bf16 v[164:167], a[184:187], a[56:59], v[164:167]\n"
    "s_waitcnt lgkmcnt(4)\n"
    "v_exp_f32_e32 v156, v156\n"
    "v_mfma_f32_16x16x32_bf16 v[168:171], a[160:163], a[32:35], v[224:227]\n"
    "v_exp_f32_e32 v157, v157\n"
    "v_mfma_f32_16x16x32_bf16 v[168:171], a[164:167], a[36:39], v[168:171]\n"
    "buffer_store_dword v139, v231, s[8:11], s89 offen offset:0 sc1\n"
    "v_mfma_f32_16x16x32_bf16 v[160:163], a[164:167], a[4:7], v[220:223]\n"
    "s_mov_b32 s56, 0\n"
    "v_exp_f32_e32 v158, v158\n"
    "v_mfma_f32_16x16x32_bf16 v[160:163], a[160:163], a[0:3], v[160:163]\n"
    "s_waitcnt lgkmcnt(2)\n"
    "ds_read_b128 a[184:187], v242 offset:30720\n"
    "ds_read_b128 a[188:191], v243 offset:30720\n"
    "v_mfma_f32_16x16x32_bf16 v[160:163], a[168:171], a[8:11], v[160:163]\n"
    "buffer_load_dwordx4 v[180:183], v250, s[8:11], s62 offen offset:2048 sc1\n"
    "v_mfma_f32_16x16x32_bf16 v[160:163], a[172:175], a[12:15], v[160:163]\n"
    "ds_read_b128 a[160:163], v240 offset:0\n"
    "ds_read_b128 a[164:167], v241 offset:0\n"
    "v_mfma_f32_16x16x32_bf16 v[168:171], a[172:175], a[44:47], v[168:171]\n"
    "v_exp_f32_e32 v159, v159\n"
    "v_mfma_f32_16x16x32_bf16 v[168:171], a[168:171], a[40:43], v[168:171]\n"
    "s_waitcnt lgkmcnt(4)\n"
    "ds_read_b128 a[168:171], v240 offset:4096\n"
    "v_cvt_pk_bf16_f32 v156, v156, v157\n"
    "v_mfma_f32_16x16x32_bf16 v[168:171], a[176:179], a[48:51], v[168:171]\n"
    "ds_write_addtid_b32 v156 offset:0\n"
    "v_add_f32_e32 v128, v131, v135\n"
    "v_mfma_f32_16x16x32_bf16 v[168:171], a[180:183], a[52:55], v[168:171]\n"
    "v_min3_f32 v129, v131, v135, v139\n"
    "v_add_f32_e32 v128, v128, v139\n"
    "v_mfma_f32_16x16x32_bf16 v[160:163], a[180:183], a[20:23], v[160:163]\n"
    "ds_read_b128 a[172:175], v241 offset:4096\n"
    "v_mfma_f32_16x16x32_bf16 v[160:163], a[176:179], a[16:19], v[160:163]\n"
    "s_waitcnt lgkmcnt(5)\n"
    "ds_read_b128 a[176:179], v242 offset:0\n"
    "ds_read_b128 a[180:183], v243 offset:0\n"
    "v_mfma_f32_16x16x32_bf16 v[160:163], a[184:187], a[24:27], v[160:163]\n"
    "buffer_store_dwordx4 v[116:119], v250, s[8:11], s29 offen offset:3072 sc1\n"
    "v_mfma_f32_16x16x32_bf16 v[160:163], a[188:191], a[28:31], v[160:163]\n"
    "v_exp_f32_e32 v164, v164\n"
    "v_mfma_f32_16x16x32_bf16 v[168:171], a[188:191], a[60:63], v[168:171]\n"
    "v_exp_f32_e32 v165, v165\n"
    "v_mfma_f32_16x16x32_bf16 v[168:171], a[184:187], a[56:59], v[168:171]\n"
    "s_waitcnt lgkmcnt(5)\n"
    "ds_read_b128 a[184:187], v242 offset:4096\n"
    "v_cvt_pk_bf16_f32 v157, v158, v159\n"
    "v_mfma_f32_16x16x32_bf16 v[204:207], a[160:163], a[32:35], v[224:227]\n"
    "buffer_load_dwordx4 v[184:187], v250, s[8:11], s62 offen offset:3072 sc1\n"
    "v_mfma_f32_16x16x32_bf16 v[204:207], a[164:167], a[36:39], v[204:207]\n"
    "s_add_u32 s62, s58, 0\n"
    "ds_read_b128 a[188:191], v243 offset:4096\n"
    "ds_write_addtid_b32 v157 offset:256\n"
    "v_mfma_f32_16x16x32_bf16 v[196:199], a[164:167], a[4:7], v[220:223]\n"
    "v_exp_f32_e32 v166, v166\n"
    "v_mfma_f32_16x16x32_bf16 v[196:199], a[160:163], a[0:3], v[196:199]\n"
    "s_waitcnt lgkmcnt(5)\n"
    "ds_read_b128 a[160:163], v240 offset:2048\n"
    "v_mfma_f32_16x16x32_bf16 v[196:199], a[168:171], a[8:11], v[196:199]\n"
    "v_exp_f32_e32 v167, v167\n"
    "v_mfma_f32_16x16x32_bf16 v[196:199], a[172:175], a[12:15], v[196:199]\n"
    "ds_read_b128 a[164:167], v241 offset:2048\n"
    "v_cvt_pk_bf16_f32 v164, v164, v165\n"
    "v_mfma_f32_16x16x32_bf16 v[204:207], a[172:175], a[44:47], v[204:207]\n"
    "v_exp_f32_e32 v160, v160\n"
    "v_mfma_f32_16x16x32_bf16 v[204:207], a[168:171], a[40:43], v[204:207]\n"
    "s_waitcnt lgkmcnt(5)\n"
    "v_exp_f32_e32 v161, v161\n"
    "v_mfma_f32_16x16x32_bf16 v[204:207], a[176:179], a[48:51], v[204:207]\n"
    "buffer_load_dwordx4 v[28:31], v250, s[8:11], s62 offen offset:0 sc1\n"
    "v_mfma_f32_16x16x32_bf16 v[204:207], a[180:183], a[52:55], v[204:207]\n"
    "ds_read_b128 a[168:171], v240 offset:6144\n"
    "v_cvt_pk_bf16_f32 v165, v166, v167\n"
    "v_mfma_f32_16x16x32_bf16 v[196:199], a[180:183], a[20:23], v[196:199]\n"
    "ds_write_addtid_b32 v164 offset:1024\n"
    "v_exp_f32_e32 v162, v162\n"
    "v_mfma_f32_16x16x32_bf16 v[196:199], a[176:179], a[16:19], v[196:199]\n"
    "s_waitcnt lgkmcnt(5)\n"
    "ds_read_b128 a[176:179], v242 offset:2048\n"
    "v_exp_f32_e32 v163, v163\n"
    "v_mfma_f32_16x16x32_bf16 v[196:199], a[184:187], a[24:27], v[196:199]\n"
    "ds_read_b128 a[172:175], v241 offset:6144\n"
    "v_cvt_pk_bf16_f32 v158, v160, v161\n"
    "v_mfma_f32_16x16x32_bf16 v[196:199], a[188:191], a[28:31], v[196:199]\n"
    "v_exp_f32_e32 v168, v168\n"
    "v_mfma_f32_16x16x32_bf16 v[204:207], a[188:191], a[60:63], v[204:207]\n"
    "v_exp_f32_e32 v169, v169\n"
    "v_mfma_f32_16x16x32_bf16 v[204:207], a[184:187], a[56:59], v[204:207]\n"
    "s_waitcnt lgkmcnt(4)\n"
    "ds_read_b128 a[180:183], v243 offset:2048\n"
    "v_cvt_pk_bf16_f32 v159, v162, v163\n"
    "v_mfma_f32_16x16x32_bf16 v[208:211], a[160:163], a[32:35], v[224:227]\n"
    "buffer_load_dwordx4 v[48:51], v250, s[8:11], s62 offen offset:1024 sc1\n"
    "ds_write_addtid_b32 v165 offset:1280\n"
    "v_mfma_f32_16x16x32_bf16 v[208:211], a[164:167], a[36:39], v[208:211]\n"
    "ds_write_addtid_b32 v158 offset:512\n"
    "v_exp_f32_e32 v170, v170\n"
    "v_mfma_f32_16x16x32_bf16 v[200:203], a[164:167], a[4:7], v[220:223]\n"
    "ds_read_b128 a[184:187], v242 offset:6144\n"
    "v_exp_f32_e32 v196, v196\n"
    "v_mfma_f32_16x16x32_bf16 v[200:203], a[160:163], a[0:3], v[200:203]\n"
    "s_waitcnt lgkmcnt(4)\n"
    "v_exp_f32_e32 v197, v197\n"
    "v_mfma_f32_16x16x32_bf16 v[200:203], a[168:171], a[8:11], v[200:203]\n"
    "ds_read_b128 a[188:191], v243 offset:6144\n"
    "v_exp_f32_e32 v198, v198\n"
    "v_mfma_f32_16x16x32_bf16 v[200:203], a[172:175], a[12:15], v[200:203]\n"
    "v_exp_f32_e32 v171, v171\n"
    "v_mfma_f32_16x16x32_bf16 v[208:211], a[172:175], a[44:47], v[208:211]\n"
    "v_exp_f32_e32 v199, v199\n"
    "v_mfma_f32_16x16x32_bf16 v[208:211], a[168:171], a[40:43], v[208:211]\n"
    "s_waitcnt lgkmcnt(4)\n"
    "ds_read_b128 a[160:163], v240 offset:8192\n"
    "ds_read_b128 a[164:167], v241 offset:8192\n"
    "v_mfma_f32_16x16x32_bf16 v[208:211], a[176:179], a[48:51], v[208:211]\n"
    "buffer_load_dwordx4 v[32:35], v250, s[8:11], s62 offen offset:2048 sc1\n"
    "ds_read_b128 a[168:171], v240 offset:12288\n"
    "v_mfma_f32_16x16x32_bf16 v[208:211], a[180:183], a[52:55], v[208:211]\n"
    "ds_read_b128 a[172:175], v241 offset:12288\n"
    "v_cvt_pk_bf16_f32 v196, v196, v197\n"
    "v_mfma_f32_16x16x32_bf16 v[200:203], a[180:183], a[20:23], v[200:203]\n"
    "v_exp_f32_e32 v204, v204\n"
    "v_mfma_f32_16x16x32_bf16 v[200:203], a[176:179], a[16:19], v[200:203]\n"
    "s_waitcnt lgkmcnt(4)\n"
    "v_exp_f32_e32 v205, v205\n"
    "v_mfma_f32_16x16x32_bf16 v[200:203], a[184:187], a[24:27], v[200:203]\n"
    "v_cvt_pk_bf16_f32 v197, v198, v199\n"
    "v_exp_f32_e32 v206, v206\n"
    "v_mfma_f32_16x16x32_bf16 v[200:203], a[188:191], a[28:31], v[200:203]\n"
    "ds_read_b128 a[176:179], v242 offset:8192\n"
    "v_cvt_pk_bf16_f32 v166, v168, v169\n"
    "v_mfma_f32_16x16x32_bf16 v[208:211], a[188:191], a[60:63], v[208:211]\n"
    "v_exp_f32_e32 v207, v207\n"
    "v_mfma_f32_16x16x32_bf16 v[208:211], a[184:187], a[56:59], v[208:211]\n"
    "s_waitcnt lgkmcnt(3)\n"
    "ds_read_b128 a[180:183], v243 offset:8192\n"
    "v_cvt_pk_bf16_f32 v204, v204, v205\n"
    "v_mfma_f32_16x16x32_bf16 v[212:215], a[160:163], a[32:35], v[224:227]\n"
    "buffer_load_dwordx4 v[52:55], v250, s[8:11], s62 offen offset:3072 sc1\n"
    "v_mfma_f32_16x16x32_bf16 v[212:215], a[164:167], a[36:39], v[212:215]\n"
    "s_add_u32 s62, s58, 4096\n"
    "v_exp_f32_e32 v200, v200\n"
    "v_mfma_f32_16x16x32_bf16 v[172:175], a[164:167], a[4:7], v[220:223]\n"
    "v_exp_f32_e32 v201, v201\n"
    "v_mfma_f32_16x16x32_bf16 v[172:175], a[160:163], a[0:3], v[172:175]\n"
    "s_waitcnt lgkmcnt(2)\n"
    "ds_read_b128 a[160:163], v240 offset:10240\n"
    "v_cvt_pk_bf16_f32 v205, v206, v207\n"
    "v_mfma_f32_16x16x32_bf16 v[172:175], a[168:171], a[8:11], v[172:175]\n"
    "ds_read_b128 a[184:187], v242 offset:12288\n"
    "ds_read_b128 a[188:191], v243 offset:12288\n"
    "v_mfma_f32_16x16x32_bf16 v[172:175], a[172:175], a[12:15], v[172:175]\n"
    "ds_read_b128 a[164:167], v241 offset:10240\n"
    "v_exp_f32_e32 v202, v202\n"
    "v_mfma_f32_16x16x32_bf16 v[212:215], a[172:175], a[44:47], v[212:215]\n"
    "ds_write_addtid_b32 v159 offset:768\n"
    "v_cvt_pk_bf16_f32 v167, v170, v171\n"
    "v_mfma_f32_16x16x32_bf16 v[212:215], a[168:171], a[40:43], v[212:215]\n"
    "s_waitcnt lgkmcnt(5)\n"
    "ds_write_addtid_b32 v166 offset:1536\n"
    "v_exp_f32_e32 v203, v203\n"
    "v_mfma_f32_16x16x32_bf16 v[212:215], a[176:179], a[48:51], v[212:215]\n"
    "buffer_load_dwordx4 v[36:39], v250, s[8:11], s62 offen offset:0 sc1\n"
    "v_mfma_f32_16x16x32_bf16 v[212:215], a[180:183], a[52:55], v[212:215]\n"
    "ds_read_b128 a[168:171], v240 offset:14336\n"
    "v_cvt_pk_bf16_f32 v198, v200, v201\n"
    "v_mfma_f32_16x16x32_bf16 v[172:175], a[180:183], a[20:23], v[172:175]\n"
    "ds_read_b128 a[172:175], v241 offset:14336\n"
    "v_exp_f32_e32 v208, v208\n"
    "v_mfma_f32_16x16x32_bf16 v[172:175], a[176:179], a[16:19], v[172:175]\n"
    "s_waitcnt lgkmcnt(5)\n"
    "v_exp_f32_e32 v209, v209\n"
    "v_mfma_f32_16x16x32_bf16 v[172:175], a[184:187], a[24:27], v[172:175]\n"
    "ds_read_b128 a[176:179], v242 offset:10240\n"
    "v_cvt_pk_bf16_f32 v199, v202, v203\n"
    "v_mfma_f32_16x16x32_bf16 v[172:175], a[188:191], a[28:31], v[172:175]\n"
    "ds_read_b128 a[180:183], v243 offset:10240\n"
    "ds_write_addtid_b32 v167 offset:1792\n"
    "v_mfma_f32_16x16x32_bf16 v[212:215], a[188:191], a[60:63], v[212:215]\n"
    "v_exp_f32_e32 v210, v210\n"
    "v_mfma_f32_16x16x32_bf16 v[212:215], a[184:187], a[56:59], v[212:215]\n"
    "s_waitcnt lgkmcnt(7)\n"
    "ds_read_b128 a[184:187], v242 offset:14336\n"
    "v_exp_f32_e32 v172, v172\n"
    "v_mfma_f32_16x16x32_bf16 v[216:219], a[160:163], a[32:35], v[224:227]\n"
    "buffer_load_dwordx4 v[56:59], v250, s[8:11], s62 offen offset:1024 sc1\n"
    "ds_read_b128 a[188:191], v243 offset:14336\n"
    "v_mfma_f32_16x16x32_bf16 v[216:219], a[164:167], a[36:39], v[216:219]\n"
    "s_waitcnt lgkmcnt(2)\n"
    "v_mfma_f32_16x16x32_bf16 v[176:179], a[164:167], a[4:7], v[220:223]\n"
    "s_barrier\n"
    "v_mfma_f32_16x16x32_bf16 v[176:179], a[160:163], a[0:3], v[176:179]\n"
    "ds_read_b64_tr_b16 a[160:161], v244 offset:24576\n"
    "ds_read2st64_b32 v[160:161], v249 offset0:0 offset1:1\n"
    "v_mfma_f32_16x16x32_bf16 v[176:179], a[168:171], a[8:11], v[176:179]\n"
    "ds_read_b64_tr_b16 a[162:163], v244 offset:26624\n"
    "v_exp_f32_e32 v173, v173\n"
    "v_mfma_f32_16x16x32_bf16 v[176:179], a[172:175], a[12:15], v[176:179]\n"
    "ds_read2st64_b32 v[162:163], v249 offset0:2 offset1:3\n"
    "v_exp_f32_e32 v174, v174\n"
    "v_mfma_f32_16x16x32_bf16 v[216:219], a[172:175], a[44:47], v[216:219]\n"
    "ds_read_b64_tr_b16 a[164:165], v244 offset:57344\n"
    "ds_read_b64_tr_b16 a[166:167], v244 offset:59392\n"
    "v_mfma_f32_16x16x32_bf16 v[216:219], a[168:171], a[40:43], v[216:219]\n"
    "ds_read2st64_b32 v[168:169], v249 offset0:4 offset1:5\n"
    "ds_read2st64_b32 v[170:171], v249 offset0:6 offset1:7\n"
    "v_mfma_f32_16x16x32_bf16 v[216:219], a[176:179], a[48:51], v[216:219]\n"
    "s_waitcnt vmcnt(7)\n"
    "buffer_store_dword v254, v231, s[8:11], s38 offen offset:0 sc1\n"
    "v_mfma_f32_16x16x32_bf16 v[216:219], a[180:183], a[52:55], v[216:219]\n"
    "v_exp_f32_e32 v175, v175\n"
    "v_mfma_f32_16x16x32_bf16 v[176:179], a[180:183], a[20:23], v[176:179]\n"
    "ds_read_b64_tr_b16 a[168:169], v245 offset:24576\n"
    "ds_read_b64_tr_b16 a[170:171], v245 offset:26624\n"
    "v_mfma_f32_16x16x32_bf16 v[176:179], a[176:179], a[16:19], v[176:179]\n"
    "s_waitcnt lgkmcnt(10)\n"
    "ds_read_b64_tr_b16 a[172:173], v245 offset:57344\n"
    "v_cvt_pk_bf16_f32 v172, v172, v173\n"
    "v_mfma_f32_16x16x32_bf16 v[176:179], a[184:187], a[24:27], v[176:179]\n"
    "buffer_load_dwordx4 v[68:71], v250, s[8:11], s62 offen offset:2048 sc1\n"
    "ds_read_b64_tr_b16 a[174:175], v245 offset:59392\n"
    "v_mfma_f32_16x16x32_bf16 v[176:179], a[188:191], a[28:31], v[176:179]\n"
    "ds_read_b64_tr_b16 a[176:177], v246 offset:24576\n"
    "v_exp_f32_e32 v211, v211\n"
    "v_mfma_f32_16x16x32_bf16 v[216:219], a[188:191], a[60:63], v[216:219]\n"
    "ds_read_b64_tr_b16 a[178:179], v246 offset:26624\n"
    "ds_read_b64_tr_b16 a[180:181], v246 offset:57344\n"
    "v_mfma_f32_16x16x32_bf16 v[216:219], a[184:187], a[56:59], v[216:219]\n"
    "s_waitcnt lgkmcnt(7)\n"
    "v_exp_f32_e32 v212, v212\n"
    "v_mfma_f32_16x16x32_bf16 a[240:243], a[160:163], v[164:167], a[240:243]\n"
    "ds_read_b64_tr_b16 a[182:183], v246 offset:59392\n"
    "ds_read_b64_tr_b16 a[184:185], v247 offset:24576\n"
    "v_mfma_f32_16x16x32_bf16 a[240:243], a[164:167], v[168:171], a[240:243]\n"
    "ds_write_addtid_b32 v196 offset:2048\n"
    "v_exp_f32_e32 v213, v213\n"
    "v_mfma_f32_16x16x32_bf16 a[236:239], a[164:167], v[160:163], a[236:239]\n"
    "ds_read_b64_tr_b16 a[186:187], v247 offset:26624\n"
    "ds_read_b64_tr_b16 a[188:189], v247 offset:57344\n"
    "v_mfma_f32_16x16x32_bf16 a[236:239], a[160:163], v[156:159], a[236:239]\n"
    "s_waitcnt lgkmcnt(8)\n"
    "ds_read_b64_tr_b16 a[190:191], v247 offset:59392\n"
    "v_cvt_pk_bf16_f32 v173, v174, v175\n"
    "v_mfma_f32_16x16x32_bf16 v[0:3], a[168:171], v[156:159], v[0:3]\n"
    "buffer_load_dwordx4 v[88:91], v250, s[8:11], s62 offen offset:3072 sc1\n"
    "ds_write_addtid_b32 v172 offset:4096\n"
    "v_mfma_f32_16x16x32_bf16 v[0:3], a[172:175], v[160:163], v[0:3]\n"
    "s_add_u32 s62, s58, 8192\n"
    "ds_read_b64_tr_b16 a[160:161], v244 offset:28672\n"
    "ds_read_b64_tr_b16 a[162:163], v244 offset:30720\n"
    "v_mfma_f32_16x16x32_bf16 v[4:7], a[172:175], v[168:171], v[4:7]\n"
    "ds_read_b64_tr_b16 a[164:165], v244 offset:61440\n"
    "v_exp_f32_e32 v214, v214\n"
    "v_mfma_f32_16x16x32_bf16 v[4:7], a[168:171], v[164:167], v[4:7]\n"
    "s_waitcnt lgkmcnt(9)\n"
    "ds_read_b64_tr_b16 a[166:167], v244 offset:63488\n"
    "ds_read_b64_tr_b16 a[168:169], v245 offset:28672\n"
    "v_mfma_f32_16x16x32_bf16 v[24:27], a[176:179], v[164:167], v[24:27]\n"
    "ds_read_b64_tr_b16 a[170:171], v245 offset:30720\n"
    "v_exp_f32_e32 v215, v215\n"
    "v_mfma_f32_16x16x32_bf16 v[24:27], a[180:183], v[168:171], v[24:27]\n"
    "ds_read_b64_tr_b16 a[172:173], v245 offset:61440\n"
    "v_cvt_pk_bf16_f32 v212, v212, v213\n"
    "v_mfma_f32_16x16x32_bf16 v[20:23], a[180:183], v[160:163], v[20:23]\n"
    "v_exp_f32_e32 v176, v176\n"
    "v_mfma_f32_16x16x32_bf16 v[20:23], a[176:179], v[156:159], v[20:23]\n"
    "s_waitcnt lgkmcnt(8)\n"
    "ds_read_b64_tr_b16 a[174:175], v245 offset:63488\n"
    "v_exp_f32_e32 v177, v177\n"
    "v_mfma_f32_16x16x32_bf16 v[40:43], a[184:187], v[156:159], v[40:43]\n"
    "buffer_load_dwordx4 v[72:75], v250, s[8:11], s62 offen offset:0 sc1\n"
    "v_mfma_f32_16x16x32_bf16 v[40:43], a[188:191], v[160:163], v[40:43]\n"
    "ds_read_b64_tr_b16 a[176:177], v246 offset:28672\n"
    "ds_read_b64_tr_b16 a[178:179], v246 offset:30720\n"
    "v_mfma_f32_16x16x32_bf16 v[44:47], a[188:191], v[168:171], v[44:47]\n"
    "ds_write_addtid_b32 v197 offset:2304\n"
    "ds_write_addtid_b32 v204 offset:3072\n"
    "v_mfma_f32_16x16x32_bf16 v[44:47], a[184:187], v[164:167], v[44:47]\n"
    "s_waitcnt lgkmcnt(8)\n"
    "ds_read_b64_tr_b16 a[180:181], v246 offset:61440\n"
    "ds_write_addtid_b32 v173 offset:4352\n"
    "v_mfma_f32_16x16x32_bf16 v[64:67], a[160:163], v[164:167], v[64:67]\n"
    "ds_read_b64_tr_b16 a[182:183], v246 offset:63488\n"
    "ds_read_b64_tr_b16 a[184:185], v247 offset:28672\n"
    "v_mfma_f32_16x16x32_bf16 v[64:67], a[164:167], v[168:171], v[64:67]\n"
    "ds_read_b64_tr_b16 a[186:187], v247 offset:30720\n"
    "v_cvt_pk_bf16_f32 v213, v214, v215\n"
    "v_mfma_f32_16x16x32_bf16 v[60:63], a[164:167], v[160:163], v[60:63]\n"
    "ds_write_addtid_b32 v205 offset:3328\n"
    "ds_write_addtid_b32 v198 offset:2560\n"
    "v_mfma_f32_16x16x32_bf16 v[60:63], a[160:163], v[156:159], v[60:63]\n"
    "s_waitcnt lgkmcnt(11)\n"
    "ds_read_b64_tr_b16 a[188:189], v247 offset:61440\n"
    "ds_read_b64_tr_b16 a[190:191], v247 offset:63488\n"
    "v_mfma_f32_16x16x32_bf16 v[80:83], a[168:171], v[156:159], v[80:83]\n"
    "buffer_load_dwordx4 v[92:95], v250, s[8:11], s62 offen offset:1024 sc1\n"
    "v_mfma_f32_16x16x32_bf16 v[80:83], a[172:175], v[160:163], v[80:83]\n"
    "ds_write_addtid_b32 v212 offset:5120\n"
    "v_cvt_pk_bf16_f32 v206, v208, v209\n"
    "v_mfma_f32_16x16x32_bf16 v[84:87], a[172:175], v[168:171], v[84:87]\n"
    ".long 0xbedf00ff\n"
    ".Lat_w12:\n"
    "s_setpc_b64 s[2:3]\n"
    "ds_read_b128 a[160:163], v240 offset:16384\n"
    "ds_read_b128 a[164:167], v241 offset:16384\n"
    "v_mfma_f32_16x16x32_bf16 v[84:87], a[168:171], v[164:167], v[84:87]\n"
    "s_waitcnt lgkmcnt(9)\n"
    "v_mfma_f32_16x16x32_bf16 v[104:107], a[176:179], v[164:167], v[104:107]\n"
    "s_barrier\n"
    "v_mfma_f32_16x16x32_bf16 v[104:107], a[180:183], v[168:171], v[104:107]\n"
    "ds_write_addtid_b32 v213 offset:5376\n"
    "v_exp_f32_e32 v178, v178\n"
    "v_mfma_f32_16x16x32_bf16 v[100:103], a[180:183], v[160:163], v[100:103]\n"
    "s_add_u32 s31, s47, s35\n"
    "ds_read_b128 a[168:171], v240 offset:20480\n"
    "ds_write_addtid_b32 v199 offset:2816\n"
    "v_mfma_f32_16x16x32_bf16 v[100:103], a[176:179], v[156:159], v[100:103]\n"
    "s_waitcnt lgkmcnt(6)\n"
    "ds_read_b128 a[172:175], v241 offset:20480\n"
    "ds_write_addtid_b32 v206 offset:3584\n"
    "v_mfma_f32_16x16x32_bf16 v[120:123], a[184:187], v[156:159], v[120:123]\n"
    "s_add_u32 m0, s55, 24576\n"
    "v_exp_f32_e32 v179, v179\n"
    "v_mfma_f32_16x16x32_bf16 v[120:123], a[188:191], v[160:163], v[120:123]\n"
    "buffer_load_dwordx4 v251, s[8:11], s31 offen offset:0 sc1 lds\n"
    "v_mfma_f32_16x16x32_bf16 v[124:127], a[188:191], v[168:171], v[124:127]\n"
    "s_mov_b32 m0, s63\n"
    "v_cvt_pk_bf16_f32 v207, v210, v211\n"
    "v_cvt_pk_bf16_f32 v174, v176, v177\n"
    "v_mfma_f32_16x16x32_bf16 v[124:127], a[184:187], v[164:167], v[124:127]\n"
    "s_waitcnt lgkmcnt(5)\n"
    "ds_read_b128 a[176:179], v242 offset:16384\n"
    "ds_read_b128 a[180:183], v243 offset:16384\n"
    "v_mfma_f32_16x16x32_bf16 v[148:151], a[160:163], a[32:35], v[224:227]\n"
    "buffer_load_dwordx4 v[76:79], v250, s[8:11], s62 offen offset:2048 sc1\n"
    "v_mfma_f32_16x16x32_bf16 v[148:151], a[164:167], a[36:39], v[148:151]\n"
    "s_add_u32 s31, s31, 0x1000\n"
    "v_exp_f32_e32 v216, v216\n"
    "v_mfma_f32_16x16x32_bf16 v[140:143], a[164:167], a[4:7], v[220:223]\n"
    "s_add_u32 m0, s55, 25600\n"
    "ds_read_b128 a[184:187], v242 offset:20480\n"
    "ds_read_b128 a[188:191], v243 offset:20480\n"
    "v_mfma_f32_16x16x32_bf16 v[140:143], a[160:163], a[0:3], v[140:143]\n"
    "s_waitcnt lgkmcnt(5)\n"
    "v_exp_f32_e32 v217, v217\n"
    "v_mfma_f32_16x16x32_bf16 v[140:143], a[168:171], a[8:11], v[140:143]\n"
    "buffer_load_dwordx4 v251, s[8:11], s31 offen offset:0 sc1 lds\n"
    "v_mfma_f32_16x16x32_bf16 v[140:143], a[172:175], a[12:15], v[140:143]\n"
    "s_mov_b32 m0, s63\n"
    "ds_read_b128 a[160:163], v240 offset:18432\n"
    "v_cvt_pk_bf16_f32 v175, v178, v179\n"
    "v_mfma_f32_16x16x32_bf16 v[148:151], a[172:175], a[44:47], v[148:151]\n"
    "v_exp_f32_e32 v218, v218\n"
    "v_mfma_f32_16x16x32_bf16 v[148:151], a[168:171], a[40:43], v[148:151]\n"
    "s_waitcnt lgkmcnt(3)\n"
    "ds_write_addtid_b32 v174 offset:4608\n"
    "v_exp_f32_e32 v219, v219\n"
    "v_mfma_f32_16x16x32_bf16 v[148:151], a[176:179], a[48:51], v[148:151]\n"
    "s_add_u32 s31, s47, s36\n"
    "ds_read_b128 a[164:167], v241 offset:18432\n"
    "ds_write_addtid_b32 v175 offset:4864\n"
    "v_mfma_f32_16x16x32_bf16 v[148:151], a[180:183], a[52:55], v[148:151]\n"
    "s_add_u32 m0, s55, 26624\n"
    "ds_read_b128 a[168:171], v240 offset:22528\n"
    "v_cvt_pk_bf16_f32 v214, v216, v217\n"
    "v_mfma_f32_16x16x32_bf16 v[140:143], a[180:183], a[20:23], v[140:143]\n"
    "buffer_load_dwordx4 v251, s[8:11], s31 offen offset:0 sc1 lds\n"
    "v_mfma_f32_16x16x32_bf16 v[140:143], a[176:179], a[16:19], v[140:143]\n"
    "s_waitcnt lgkmcnt(5)\n"
    "ds_read_b128 a[176:179], v242 offset:18432\n"
    "ds_read_b128 a[180:183], v243 offset:18432\n"
    "v_mfma_f32_16x16x32_bf16 v[140:143], a[184:187], a[24:27], v[140:143]\n"
    "s_mov_b32 m0, s63\n"
    "ds_read_b128 a[172:175], v241 offset:22528\n"
    "v_cvt_pk_bf16_f32 v215, v218, v219\n"
    "v_mfma_f32_16x16x32_bf16 v[140:143], a[188:191], a[28:31], v[140:143]\n"
    "buffer_load_dword v253, v238, s[8:11], s48 offen offset:0 sc1\n"
    "ds_write_addtid_b32 v214 offset:5632\n"
    "v_mfma_f32_16x16x32_bf16 v[148:151], a[188:191], a[60:63], v[148:151]\n"
    "s_add_u32 s31, s31, 0x1000\n"
    "ds_write_addtid_b32 v207 offset:3840\n"
    "ds_write_addtid_b32 v215 offset:5888\n"
    "v_mfma_f32_16x16x32_bf16 v[148:151], a[184:187], a[56:59], v[148:151]\n"
    "s_waitcnt lgkmcnt(8)\n"
    "ds_read_b128 a[184:187], v242 offset:22528\n"
    "ds_read_b128 a[188:191], v243 offset:22528\n"
    "v_mfma_f32_16x16x32_bf16 v[152:155], a[160:163], a[32:35], v[224:227]\n"
    "s_add_u32 m0, s55, 27648\n"
    "v_exp_f32_e32 v140, v140\n"
    "v_mfma_f32_16x16x32_bf16 v[152:155], a[164:167], a[36:39], v[152:155]\n"
    "s_waitcnt lgkmcnt(3)\n"
    "s_waitcnt lgkmcnt(2)\n"
    "v_mfma_f32_16x16x32_bf16 v[144:147], a[164:167], a[4:7], v[220:223]\n"
    "s_barrier\n"
    "v_mfma_f32_16x16x32_bf16 v[144:147], a[160:163], a[0:3], v[144:147]\n"
    "ds_read2st64_b32 v[200:201], v249 offset0:8 offset1:9\n"
    "v_exp_f32_e32 v141, v141\n"
    "v_mfma_f32_16x16x32_bf16 v[144:147], a[168:171], a[8:11], v[144:147]\n"
    "buffer_load_dwordx4 v251, s[8:11], s31 offen offset:0 sc1 lds\n"
    "ds_read_b64_tr_b16 a[160:161], v244 offset:0\n"
    "v_mfma_f32_16x16x32_bf16 v[144:147], a[172:175], a[12:15], v[144:147]\n"
    "s_mov_b32 m0, s63\n"
    "ds_read_b64_tr_b16 a[162:163], v244 offset:2048\n"
    "ds_read_b64_tr_b16 a[164:165], v244 offset:32768\n"
    "ds_read2st64_b32 v[202:203], v249 offset0:10 offset1:11\n"
    "v_mfma_f32_16x16x32_bf16 v[152:155], a[172:175], a[44:47], v[152:155]\n"
    "s_add_u32 s87, s59, 8192\n"
    "ds_read_b64_tr_b16 a[166:167], v244 offset:34816\n"
    "v_exp_f32_e32 v142, v142\n"
    "v_mfma_f32_16x16x32_bf16 v[152:155], a[168:171], a[40:43], v[152:155]\n"
    "ds_read_b64_tr_b16 a[168:169], v245 offset:0\n"
    "v_exp_f32_e32 v143, v143\n"
    "v_mfma_f32_16x16x32_bf16 v[152:155], a[176:179], a[48:51], v[152:155]\n"
    "s_add_u32 s31, s47, s37\n"
    "ds_read2st64_b32 v[208:209], v249 offset0:12 offset1:13\n"
    "ds_read2st64_b32 v[210:211], v249 offset0:14 offset1:15\n"
    "v_mfma_f32_16x16x32_bf16 v[152:155], a[180:183], a[52:55], v[152:155]\n"
    "s_add_u32 m0, s55, 28672\n"
    "ds_read_b64_tr_b16 a[170:171], v245 offset:2048\n"
    "v_cvt_pk_bf16_f32 v140, v140, v141\n"
    "v_mfma_f32_16x16x32_bf16 v[144:147], a[180:183], a[20:23], v[144:147]\n"
    "buffer_load_dwordx4 v251, s[8:11], s31 offen offset:0 sc1 lds\n"
    "ds_read_b64_tr_b16 a[172:173], v245 offset:32768\n"
    "v_mfma_f32_16x16x32_bf16 v[144:147], a[176:179], a[16:19], v[144:147]\n"
    "s_waitcnt lgkmcnt(11)\n"
    "ds_read_b64_tr_b16 a[174:175], v245 offset:34816\n"
    "v_exp_f32_e32 v148, v148\n"
    "v_mfma_f32_16x16x32_bf16 v[144:147], a[184:187], a[24:27], v[144:147]\n"
    "s_mov_b32 m0, s63\n"
    "ds_read_b64_tr_b16 a[176:177], v246 offset:0\n"
    "v_exp_f32_e32 v149, v149\n"
    "v_mfma_f32_16x16x32_bf16 v[144:147], a[188:191], a[28:31], v[144:147]\n"
    "s_add_u32 s88, s59, 16384\n"
    "ds_read_b64_tr_b16 a[178:179], v246 offset:2048\n"
    "v_cvt_pk_bf16_f32 v141, v142, v143\n"
    "v_mfma_f32_16x16x32_bf16 v[152:155], a[188:191], a[60:63], v[152:155]\n"
    "s_add_u32 s31, s31, 0x1000\n"
    "ds_read_b64_tr_b16 a[180:181], v246 offset:32768\n"
    "v_exp_f32_e32 v150, v150\n"
    "v_mfma_f32_16x16x32_bf16 v[152:155], a[184:187], a[56:59], v[152:155]\n"
    "s_waitcnt lgkmcnt(6)\n"
    "ds_read_b64_tr_b16 a[182:183], v246 offset:34816\n"
    "ds_write_addtid_b32 v140 offset:0\n"
    "v_mfma_f32_16x16x32_bf16 a[240:243], a[160:163], v[204:207], a[240:243]\n"
    "s_add_u32 m0, s55, 29696\n"
    "ds_read_b64_tr_b16 a[184:185], v247 offset:0\n"
    "v_exp_f32_e32 v151, v151\n"
    "v_mfma_f32_16x16x32_bf16 a[240:243], a[164:167], v[208:211], a[240:243]\n"
    "buffer_load_dwordx4 v251, s[8:11], s31 offen offset:0 sc1 lds\n"
    "ds_read_b64_tr_b16 a[186:187], v247 offset:2048\n"
    "v_mfma_f32_16x16x32_bf16 a[236:239], a[164:167], v[200:203], a[236:239]\n"
    "s_mov_b32 m0, s63\n"
    "ds_read_b64_tr_b16 a[188:189], v247 offset:32768\n"
    "v_cvt_pk_bf16_f32 v148, v148, v149\n"
    "v_mfma_f32_16x16x32_bf16 a[236:239], a[160:163], v[196:199], a[236:239]\n"
    "s_waitcnt lgkmcnt(8)\n"
    "ds_read_b64_tr_b16 a[160:161], v244 offset:4096\n"
    "v_exp_f32_e32 v144, v144\n"
    "v_mfma_f32_16x16x32_bf16 v[0:3], a[168:171], v[196:199], v[0:3]\n"
    "s_add_u32 s89, s59, 24576\n"
    "ds_read_b64_tr_b16 a[190:191], v247 offset:34816\n"
    "ds_write_addtid_b32 v141 offset:256\n"
    "v_mfma_f32_16x16x32_bf16 v[0:3], a[172:175], v[200:203], v[0:3]\n"
    "s_add_u32 s31, s46, s36\n"
    "ds_write_addtid_b32 v148 offset:1024\n"
    "v_exp_f32_e32 v145, v145\n"
    "v_mfma_f32_16x16x32_bf16 v[4:7], a[172:175], v[208:211], v[4:7]\n"
    "s_add_u32 m0, s55, 30720\n"
    "ds_read_b64_tr_b16 a[162:163], v244 offset:6144\n"
    "v_cvt_pk_bf16_f32 v149, v150, v151\n"
    "v_mfma_f32_16x16x32_bf16 v[4:7], a[168:171], v[204:207], v[4:7]\n"
    "s_waitcnt lgkmcnt(9)\n"
    "ds_read_b64_tr_b16 a[164:165], v244 offset:36864\n"
    "ds_read_b64_tr_b16 a[166:167], v244 offset:38912\n"
    "v_mfma_f32_16x16x32_bf16 v[24:27], a[176:179], v[204:207], v[24:27]\n"
    "buffer_load_dwordx4 v251, s[12:15], s31 offen offset:0 lds\n"
    "ds_read_b64_tr_b16 a[168:169], v245 offset:4096\n"
    "v_mfma_f32_16x16x32_bf16 v[24:27], a[180:183], v[208:211], v[24:27]\n"
    "s_mov_b32 m0, s63\n"
    "ds_read_b64_tr_b16 a[170:171], v245 offset:6144\n"
    "v_exp_f32_e32 v146, v146\n"
    "v_mfma_f32_16x16x32_bf16 v[20:23], a[180:183], v[200:203], v[20:23]\n"
    "ds_read_b64_tr_b16 a[172:173], v245 offset:36864\n"
    "ds_read_b64_tr_b16 a[174:175], v245 offset:38912\n"
    "v_mfma_f32_16x16x32_bf16 v[20:23], a[176:179], v[196:199], v[20:23]\n"
    "s_waitcnt lgkmcnt(9)\n"
    "ds_read_b64_tr_b16 a[176:177], v246 offset:4096\n"
    "ds_read2st64_b32 v[176:177], v249 offset0:16 offset1:17\n"
    "v_mfma_f32_16x16x32_bf16 v[40:43], a[184:187], v[196:199], v[40:43]\n"
    "s_add_u32 s31, s31, 0x1000\n"
    "ds_read2st64_b32 v[178:179], v249 offset0:18 offset1:19\n"
    "v_exp_f32_e32 v147, v147\n"
    "v_mfma_f32_16x16x32_bf16 v[40:43], a[188:191], v[200:203], v[40:43]\n"
    "s_add_u32 m0, s55, 31744\n"
    "ds_read_b64_tr_b16 a[178:179], v246 offset:6144\n"
    "ds_read2st64_b32 v[216:217], v249 offset0:20 offset1:21\n"
    "v_mfma_f32_16x16x32_bf16 v[44:47], a[188:191], v[208:211], v[44:47]\n"
    "buffer_load_dwordx4 v251, s[12:15], s31 offen offset:0 lds\n"
    "ds_read_b64_tr_b16 a[180:181], v246 offset:36864\n"
    "v_mfma_f32_16x16x32_bf16 v[44:47], a[184:187], v[204:207], v[44:47]\n"
    "s_waitcnt lgkmcnt(10)\n"
    "ds_read_b64_tr_b16 a[182:183], v246 offset:38912\n"
    "ds_read2st64_b32 v[218:219], v249 offset0:22 offset1:23\n"
    "v_mfma_f32_16x16x32_bf16 v[64:67], a[160:163], v[204:207], v[64:67]\n"
    "s_mov_b32 m0, s63\n"
    "ds_read_b64_tr_b16 a[184:185], v247 offset:4096\n"
    "v_cvt_pk_bf16_f32 v142, v144, v145\n"
    "v_mfma_f32_16x16x32_bf16 v[64:67], a[164:167], v[208:211], v[64:67]\n"
    "s_waitcnt vmcnt(5)\n"
    "ds_read_b64_tr_b16 a[186:187], v247 offset:6144\n"
    "v_subrev_u32_e32 v253, s39, v253\n"
    "v_subrev_u32_e32 v253, 0, v253\n"
    "v_mfma_f32_16x16x32_bf16 v[60:63], a[164:167], v[200:203], v[60:63]\n"
    "ds_read_b64_tr_b16 a[188:189], v247 offset:36864\n"
    "v_cmp_le_u32_e32 vcc, 1, v253\n"
    "v_min_u32_e32 v220, 0x13c, v248\n"
    "v_mfma_f32_16x16x32_bf16 v[60:63], a[160:163], v[196:199], v[60:63]\n"
    "s_waitcnt lgkmcnt(11)\n"
    "ds_read_b64_tr_b16 a[190:191], v247 offset:38912\n"
    "v_exp_f32_e32 v152, v152\n"
    "v_mfma_f32_16x16x32_bf16 v[80:83], a[168:171], v[196:199], v[80:83]\n"
    "ds_read_b64_tr_b16 a[160:161], v244 offset:12288\n"
    "ds_read_b64_tr_b16 a[162:163], v244 offset:14336\n"
    "ds_read_b64_tr_b16 a[164:165], v244 offset:45056\n"
    "v_mfma_f32_16x16x32_bf16 v[80:83], a[172:175], v[200:203], v[80:83]\n"
    "s_cbranch_vccnz .Lat_k\n"
    ".Lat_kr:\n"
    "s_cmp_lg_u32 s56, 0\n"
    "s_cbranch_scc1 .Lat_kx\n"
    ".Lat_kxr:\n"
    "v_mfma_f32_16x16x32_bf16 v[84:87], a[172:175], v[208:211], v[84:87]\n"
    "buffer_load_dwordx4 a[244:247], v250, s[8:11], s87 offen offset:0 sc1\n"
    "ds_read_b64_tr_b16 a[166:167], v244 offset:47104\n"
    "v_mfma_f32_16x16x32_bf16 v[84:87], a[168:171], v[204:207], v[84:87]\n"
    "s_waitcnt lgkmcnt(9)\n"
    "ds_write_addtid_b32 v149 offset:1280\n"
    "v_exp_f32_e32 v153, v153\n"
    "v_mfma_f32_16x16x32_bf16 v[104:107], a[176:179], v[204:207], v[104:107]\n"
    "v_cvt_pk_bf16_f32 v143, v146, v147\n"
    "v_exp_f32_e32 v154, v154\n"
    "v_mfma_f32_16x16x32_bf16 v[104:107], a[180:183], v[208:211], v[104:107]\n"
    "buffer_load_dwordx4 v[96:99], v250, s[8:11], s62 offen offset:3072 sc1\n"
    "ds_read_b64_tr_b16 a[168:169], v245 offset:12288\n"
    "v_mfma_f32_16x16x32_bf16 v[100:103], a[180:183], v[200:203], v[100:103]\n"
    "s_add_u32 s62, s58, 12288\n"
    "ds_read_b64_tr_b16 a[170:171], v245 offset:14336\n"
    "ds_read_b64_tr_b16 a[172:173], v245 offset:45056\n"
    "v_mfma_f32_16x16x32_bf16 v[100:103], a[176:179], v[196:199], v[100:103]\n"
    "s_waitcnt lgkmcnt(8)\n"
    "ds_read_b64_tr_b16 a[176:177], v246 offset:12288\n"
    "ds_write_addtid_b32 v142 offset:512\n"
    "v_mfma_f32_16x16x32_bf16 v[120:123], a[184:187], v[196:199], v[120:123]\n"
    "ds_read_b64_tr_b16 a[174:175], v245 offset:47104\n"
    "ds_read_b64_tr_b16 a[178:179], v246 offset:14336\n"
    "v_mfma_f32_16x16x32_bf16 v[120:123], a[188:191], v[200:203], v[120:123]\n"
    "ds_write_addtid_b32 v143 offset:768\n"
    "v_exp_f32_e32 v155, v155\n"
    "v_mfma_f32_16x16x32_bf16 v[124:127], a[188:191], v[208:211], v[124:127]\n"
    "buffer_load_dwordx4 a[248:251], v250, s[8:11], s88 offen offset:0 sc1\n"
    "ds_read_b64_tr_b16 a[180:181], v246 offset:45056\n"
    "v_mfma_f32_16x16x32_bf16 v[124:127], a[184:187], v[204:207], v[124:127]\n"
    "s_waitcnt lgkmcnt(10)\n"
    "ds_read_b64_tr_b16 a[184:185], v247 offset:12288\n"
    "v_cvt_pk_bf16_f32 v150, v152, v153\n"
    "v_mfma_f32_16x16x32_bf16 v[64:67], a[160:163], v[212:215], v[64:67]\n"
    "ds_read_b64_tr_b16 a[182:183], v246 offset:47104\n"
    "v_cvt_pk_bf16_f32 v151, v154, v155\n"
    "v_mfma_f32_16x16x32_bf16 v[64:67], a[164:167], v[216:219], v[64:67]\n"
    "buffer_load_dwordx4 v[108:111], v250, s[8:11], s62 offen offset:0 sc1\n"
    "ds_read_b64_tr_b16 a[186:187], v247 offset:14336\n"
    "v_mfma_f32_16x16x32_bf16 v[60:63], a[164:167], v[176:179], v[60:63]\n"
    "ds_read_b64_tr_b16 a[188:189], v247 offset:45056\n"
    "ds_read_b64_tr_b16 a[190:191], v247 offset:47104\n"
    "v_mfma_f32_16x16x32_bf16 v[60:63], a[160:163], v[172:175], v[60:63]\n"
    "s_waitcnt lgkmcnt(8)\n"
    "ds_write_addtid_b32 v150 offset:1536\n"
    "ds_write_addtid_b32 v151 offset:1792\n"
    "v_mfma_f32_16x16x32_bf16 v[80:83], a[168:171], v[172:175], v[80:83]\n"
    "ds_read_b64_tr_b16 a[160:161], v244 offset:8192\n"
    "v_add_u32_e32 v240, s45, v240\n"
    "v_mfma_f32_16x16x32_bf16 v[80:83], a[172:175], v[176:179], v[80:83]\n"
    "ds_read_b64_tr_b16 a[162:163], v244 offset:10240\n"
    "ds_read_b64_tr_b16 a[164:165], v244 offset:40960\n"
    "v_mfma_f32_16x16x32_bf16 v[84:87], a[172:175], v[216:219], v[84:87]\n"
    "buffer_load_dwordx4 a[252:255], v250, s[8:11], s89 offen offset:0 sc1\n"
    "ds_read_b64_tr_b16 a[166:167], v244 offset:43008\n"
    "v_mfma_f32_16x16x32_bf16 v[84:87], a[168:171], v[212:215], v[84:87]\n"
    "s_waitcnt lgkmcnt(9)\n"
    "s_waitcnt lgkmcnt(4)\n"
    "s_waitcnt vmcnt(7)\n"
    "v_mfma_f32_16x16x32_bf16 v[104:107], a[176:179], v[212:215], v[104:107]\n"
    "s_barrier\n"
    "v_mfma_f32_16x16x32_bf16 v[104:107], a[180:183], v[216:219], v[104:107]\n"
    "buffer_load_dwordx4 v[112:115], v250, s[8:11], s62 offen offset:1024 sc1\n"
    "ds_read2st64_b32 v[144:145], v249 offset0:0 offset1:1\n"
    "v_mfma_f32_16x16x32_bf16 v[100:103], a[180:183], v[176:179], v[100:103]\n"
    "s_add_u32 s31, s46, s37\n"
    "ds_read2st64_b32 v[146:147], v249 offset0:2 offset1:3\n"
    "ds_read2st64_b32 v[152:153], v249 offset0:4 offset1:5\n"
    "v_mfma_f32_16x16x32_bf16 v[100:103], a[176:179], v[172:175], v[100:103]\n"
    "ds_read_b64_tr_b16 a[168:169], v244 offset:16384\n"
    "ds_read_b64_tr_b16 a[170:171], v244 offset:18432\n"
    "v_mfma_f32_16x16x32_bf16 v[120:123], a[184:187], v[172:175], v[120:123]\n"
    "s_add_u32 m0, s55, 4096\n"
    "ds_read_b64_tr_b16 a[172:173], v244 offset:49152\n"
    "ds_read_b64_tr_b16 a[174:175], v244 offset:51200\n"
    "v_mfma_f32_16x16x32_bf16 v[120:123], a[188:191], v[176:179], v[120:123]\n"
    "buffer_load_dwordx4 v251, s[12:15], s31 offen offset:0 lds\n"
    "ds_read2st64_b32 v[154:155], v249 offset0:6 offset1:7\n"
    "v_mfma_f32_16x16x32_bf16 v[124:127], a[188:191], v[216:219], v[124:127]\n"
    "s_mov_b32 m0, s63\n"
    "ds_read_b64_tr_b16 a[176:177], v245 offset:8192\n"
    "ds_read_b64_tr_b16 a[178:179], v245 offset:10240\n"
    "v_mfma_f32_16x16x32_bf16 v[124:127], a[184:187], v[212:215], v[124:127]\n"
    "s_waitcnt lgkmcnt(10)\n"
    "ds_read_b64_tr_b16 a[180:181], v245 offset:40960\n"
    "ds_read_b64_tr_b16 a[182:183], v245 offset:43008\n"
    "v_mfma_f32_16x16x32_bf16 a[240:243], a[160:163], v[212:215], a[240:243]\n"
    "buffer_load_dwordx4 v[8:11], v250, s[8:11], s87 offen offset:1024 sc1\n"
    "v_mfma_f32_16x16x32_bf16 a[240:243], a[164:167], v[216:219], a[240:243]\n"
    "s_add_u32 s31, s31, 0x1000\n"
    "ds_read_b64_tr_b16 a[184:185], v245 offset:16384\n"
    "v_xor_b32_e32 v241, 64, v240\n"
    "v_mfma_f32_16x16x32_bf16 a[236:239], a[164:167], v[176:179], a[236:239]\n"
    "s_add_u32 m0, s55, 5120\n"
    "ds_read_b64_tr_b16 a[186:187], v245 offset:18432\n"
    "ds_read_b64_tr_b16 a[188:189], v245 offset:49152\n"
    "v_mfma_f32_16x16x32_bf16 a[236:239], a[160:163], v[172:175], a[236:239]\n"
    "s_waitcnt lgkmcnt(7)\n"
    "ds_read_b64_tr_b16 a[190:191], v245 offset:51200\n"
    "v_add_u32_e32 v242, 0x10000, v240\n"
    "v_mfma_f32_16x16x32_bf16 a[236:239], a[168:171], v[140:143], a[236:239]\n"
    "buffer_load_dwordx4 v251, s[12:15], s31 offen offset:0 lds\n"
    "v_mfma_f32_16x16x32_bf16 a[236:239], a[172:175], v[144:147], a[236:239]\n"
    "s_mov_b32 m0, s63\n"
    "ds_read_b64_tr_b16 a[160:161], v246 offset:8192\n"
    "v_add_u32_e32 v243, 0x10000, v241\n"
    "v_mfma_f32_16x16x32_bf16 a[240:243], a[172:175], v[152:155], a[240:243]\n"
    "ds_read_b64_tr_b16 a[162:163], v246 offset:10240\n"
    "ds_read_b64_tr_b16 a[164:165], v246 offset:40960\n"
    "v_mfma_f32_16x16x32_bf16 a[240:243], a[168:171], v[148:151], a[240:243]\n"
    "s_waitcnt lgkmcnt(7)\n"
    "ds_read_b64_tr_b16 a[166:167], v246 offset:43008\n"
    "v_add_u32_e32 v252, s54, v240\n"
    "v_mfma_f32_16x16x32_bf16 v[4:7], a[176:179], v[212:215], v[4:7]\n"
    "buffer_load_dwordx4 v[12:15], v250, s[8:11], s88 offen offset:1024 sc1\n"
    "v_mfma_f32_16x16x32_bf16 v[4:7], a[180:183], v[216:219], v[4:7]\n"
    "ds_read_b64_tr_b16 a[168:169], v246 offset:16384\n"
    "v_mfma_f32_16x16x32_bf16 v[0:3], a[180:183], v[176:179], v[0:3]\n"
    "ds_read_b64_tr_b16 a[170:171], v246 offset:18432\n"
    "v_xor_b32_e32 v255, 64, v252\n"
    "v_mfma_f32_16x16x32_bf16 v[0:3], a[176:179], v[172:175], v[0:3]\n"
    "s_waitcnt lgkmcnt(6)\n"
    "ds_read_b64_tr_b16 a[172:173], v246 offset:49152\n"
    "ds_read_b64_tr_b16 a[174:175], v246 offset:51200\n"
    "v_mfma_f32_16x16x32_bf16 v[0:3], a[184:187], v[140:143], v[0:3]\n"
    "buffer_load_dwordx4 v[132:135], v250, s[8:11], s62 offen offset:2048 sc1\n"
    "v_mfma_f32_16x16x32_bf16 v[0:3], a[188:191], v[144:147], v[0:3]\n"
    "ds_read_b64_tr_b16 a[176:177], v247 offset:8192\n"
    "ds_read_b64_tr_b16 a[178:179], v247 offset:10240\n"
    "v_mfma_f32_16x16x32_bf16 v[4:7], a[188:191], v[152:155], v[4:7]\n"
    "ds_read_b64_tr_b16 a[180:181], v247 offset:40960\n"
    "v_lshlrev_b32_e32 v228, 16, v228\n"
    "v_mfma_f32_16x16x32_bf16 v[4:7], a[184:187], v[148:151], v[4:7]\n"
    "s_waitcnt lgkmcnt(7)\n"
    "ds_read_b64_tr_b16 a[182:183], v247 offset:43008\n"
    "v_lshlrev_b32_e32 v229, 16, v229\n"
    "v_mfma_f32_16x16x32_bf16 v[24:27], a[160:163], v[212:215], v[24:27]\n"
    "buffer_load_dwordx4 v[16:19], v250, s[8:11], s89 offen offset:1024 sc1\n"
    "v_mfma_f32_16x16x32_bf16 v[24:27], a[164:167], v[216:219], v[24:27]\n"
    "ds_read_b64_tr_b16 a[184:185], v247 offset:16384\n"
    "v_cvt_pk_bf16_f32 v7, v6, v7\n"
    "v_mfma_f32_16x16x32_bf16 v[20:23], a[164:167], v[176:179], v[20:23]\n"
    "ds_read_b64_tr_b16 a[186:187], v247 offset:18432\n"
    "ds_read_b64_tr_b16 a[188:189], v247 offset:49152\n"
    "v_mfma_f32_16x16x32_bf16 v[20:23], a[160:163], v[172:175], v[20:23]\n"
    "s_waitcnt lgkmcnt(7)\n"
    "ds_read_b64_tr_b16 a[190:191], v247 offset:51200\n"
    "ds_read_b64_tr_b16 a[160:161], v244 offset:20480\n"
    "v_mfma_f32_16x16x32_bf16 v[20:23], a[168:171], v[140:143], v[20:23]\n"
    "buffer_load_dwordx4 v[116:119], v250, s[8:11], s62 offen offset:3072 sc1\n"
    "v_mfma_f32_16x16x32_bf16 v[20:23], a[172:175], v[144:147], v[20:23]\n"
    "s_add_u32 s62, s58, 16384\n"
    "ds_read_b64_tr_b16 a[162:163], v244 offset:22528\n"
    "ds_read_b64_tr_b16 a[164:165], v244 offset:53248\n"
    "v_mfma_f32_16x16x32_bf16 v[24:27], a[172:175], v[152:155], v[24:27]\n"
    "ds_read_b64_tr_b16 a[166:167], v244 offset:55296\n"
    "v_cvt_pk_bf16_f32 v6, v4, v5\n"
    "v_mfma_f32_16x16x32_bf16 v[24:27], a[168:171], v[148:151], v[24:27]\n"
    "s_waitcnt lgkmcnt(8)\n"
    "ds_read_b64_tr_b16 a[168:169], v245 offset:20480\n"
    "ds_read_b64_tr_b16 a[170:171], v245 offset:22528\n"
    "v_mfma_f32_16x16x32_bf16 v[44:47], a[176:179], v[212:215], v[44:47]\n"
    "buffer_load_dwordx4 a[192:195], v250, s[8:11], s87 offen offset:2048 sc1\n"
    "v_mfma_f32_16x16x32_bf16 v[44:47], a[180:183], v[216:219], v[44:47]\n"
    "ds_read_b64_tr_b16 a[172:173], v245 offset:53248\n"
    "ds_read_b64_tr_b16 a[174:175], v245 offset:55296\n"
    "v_mfma_f32_16x16x32_bf16 v[40:43], a[180:183], v[176:179], v[40:43]\n"
    "v_accvgpr_read_b32 v4, a240\n"
    "v_accvgpr_read_b32 v5, a241\n"
    "v_mfma_f32_16x16x32_bf16 v[40:43], a[176:179], v[172:175], v[40:43]\n"
    "s_waitcnt lgkmcnt(8)\n"
    "ds_read_b64_tr_b16 a[176:177], v246 offset:20480\n"
    "v_lshlrev_b32_e32 v230, 16, v230\n"
    "v_mfma_f32_16x16x32_bf16 v[40:43], a[184:187], v[140:143], v[40:43]\n"
    "buffer_load_dwordx4 v[136:139], v250, s[8:11], s62 offen offset:0 sc1\n"
    "ds_read_b64_tr_b16 a[178:179], v246 offset:22528\n"
    "v_mfma_f32_16x16x32_bf16 v[40:43], a[188:191], v[144:147], v[40:43]\n"
    "ds_read_b64_tr_b16 a[180:181], v246 offset:53248\n"
    "ds_read_b64_tr_b16 a[182:183], v246 offset:55296\n"
    "v_mfma_f32_16x16x32_bf16 v[44:47], a[188:191], v[152:155], v[44:47]\n"
    "v_accvgpr_read_b32 v225, a243\n"
    "v_cvt_pk_bf16_f32 v4, v4, v5\n"
    "v_mfma_f32_16x16x32_bf16 v[44:47], a[184:187], v[148:151], v[44:47]\n"
    "s_waitcnt lgkmcnt(8)\n"
    "ds_read_b64_tr_b16 a[184:185], v247 offset:20480\n"
    "ds_read_b64_tr_b16 a[186:187], v247 offset:22528\n"
    "v_mfma_f32_16x16x32_bf16 v[64:67], a[160:163], v[148:151], v[64:67]\n"
    "buffer_load_dwordx4 a[196:199], v250, s[8:11], s88 offen offset:2048 sc1\n"
    "v_mfma_f32_16x16x32_bf16 v[64:67], a[164:167], v[152:155], v[64:67]\n"
    "s_waitcnt lgkmcnt(6)\n"
    "ds_read_b64_tr_b16 a[188:189], v247 offset:53248\n"
    "ds_read_b64_tr_b16 a[190:191], v247 offset:55296\n"
    "v_mfma_f32_16x16x32_bf16 v[84:87], a[172:175], v[152:155], v[84:87]\n"
    "ds_read_b128 a[48:51], v240 offset:0\n"
    "v_accvgpr_read_b32 v5, a242\n"
    "v_mfma_f32_16x16x32_bf16 v[84:87], a[168:171], v[148:151], v[84:87]\n"
    "s_waitcnt lgkmcnt(5)\n"
    "ds_read_b128 a[52:55], v241 offset:0\n"
    "v_cvt_pk_bf16_f32 v5, v5, v225\n"
    "v_mfma_f32_16x16x32_bf16 v[104:107], a[176:179], v[148:151], v[104:107]\n"
    "buffer_store_dwordx4 v[4:7], v250, s[8:11], s80 offen offset:0 sc1\n"
    "v_mfma_f32_16x16x32_bf16 v[104:107], a[180:183], v[152:155], v[104:107]\n"
    "s_waitcnt lgkmcnt(2)\n"
    "v_cvt_pk_bf16_f32 v24, v24, v25\n"
    "v_cvt_pk_bf16_f32 v25, v26, v27\n"
    "v_mfma_f32_16x16x32_bf16 v[124:127], a[188:191], v[152:155], v[124:127]\n"
    "ds_read_b128 a[56:59], v240 offset:32768\n"
    "v_cvt_pk_bf16_f32 v26, v44, v45\n"
    "v_mfma_f32_16x16x32_bf16 v[124:127], a[184:187], v[148:151], v[124:127]\n"
    "ds_read_b128 a[60:63], v241 offset:32768\n"
    "v_cvt_pk_bf16_f32 v27, v46, v47\n"
    "v_mfma_f32_16x16x32_bf16 v[60:63], a[164:167], v[144:147], v[60:63]\n"
    "buffer_load_dwordx4 a[200:203], v250, s[8:11], s89 offen offset:2048 sc1\n"
    "v_mfma_f32_16x16x32_bf16 v[60:63], a[160:163], v[140:143], v[60:63]\n"
    "buffer_store_dwordx4 v[24:27], v250, s[8:11], s80 offen offset:1024 sc1\n"
    "v_mfma_f32_16x16x32_bf16 v[80:83], a[168:171], v[140:143], v[80:83]\n"
    "v_cvt_pk_bf16_f32 v64, v64, v65\n"
    "v_cvt_pk_bf16_f32 v65, v66, v67\n"
    "v_mfma_f32_16x16x32_bf16 v[80:83], a[172:175], v[144:147], v[80:83]\n"
    "v_cvt_pk_bf16_f32 v66, v84, v85\n"
    "v_cvt_pk_bf16_f32 v67, v86, v87\n"
    "v_mfma_f32_16x16x32_bf16 v[100:103], a[180:183], v[144:147], v[100:103]\n"
    "buffer_store_dwordx4 v[64:67], v250, s[8:11], s80 offen offset:2048 sc1\n"
    "v_mfma_f32_16x16x32_bf16 v[100:103], a[176:179], v[140:143], v[100:103]\n"
    "s_mov_b32 s45, 0x71800000\n"
    "v_cvt_pk_bf16_f32 v104, v104, v105\n"
    "v_cvt_pk_bf16_f32 v105, v106, v107\n"
    "v_mfma_f32_16x16x32_bf16 v[120:123], a[184:187], v[140:143], v[120:123]\n"
    "v_cvt_pk_bf16_f32 v106, v124, v125\n"
    "v_cvt_pk_bf16_f32 v107, v126, v127\n"
    "v_mfma_f32_16x16x32_bf16 v[120:123], a[188:191], v[144:147], v[120:123]\n"
    "s_waitcnt lgkmcnt(2)\n"
    "buffer_store_dwordx4 v[104:107], v250, s[8:11], s80 offen offset:3072 sc1\n"
    "v_mfma_f32_16x16x32_bf16 v[156:159], a[216:219], a[48:51], 0\n"
    "buffer_load_dwordx4 a[204:207], v250, s[8:11], s87 offen offset:3072 sc1\n"
    "ds_read_b128 a[160:163], v242 offset:0\n"
    "v_mfma_f32_16x16x32_bf16 v[160:163], a[220:223], a[48:51], 0\n"
    "ds_read_b128 a[164:167], v243 offset:0\n"
    "buffer_store_dword v127, v231, s[8:11], s86 offen offset:0 sc1\n"
    "v_mfma_f32_16x16x32_bf16 v[164:167], a[224:227], a[48:51], 0\n"
    "ds_read_b128 a[168:171], v242 offset:32768\n"
    "v_mfma_f32_16x16x32_bf16 v[168:171], a[228:231], a[48:51], 0\n"
    "ds_read_b128 a[172:175], v243 offset:32768\n"
    "v_add_f32_e32 v225, v123, v127\n"
    "v_mfma_f32_16x16x32_bf16 v[156:159], a[232:235], a[52:55], v[156:159]\n"
    "ds_read_b128 a[176:179], v240 offset:2048\n"
    "v_min3_f32 v129, v129, v123, v127\n"
    "v_mfma_f32_16x16x32_bf16 v[160:163], v[192:195], a[52:55], v[160:163]\n"
    "ds_read_b128 a[180:183], v241 offset:2048\n"
    "v_add_f32_e32 v225, v225, v128\n"
    "v_mfma_f32_16x16x32_bf16 v[164:167], a[64:67], a[52:55], v[164:167]\n"
    "ds_read_b128 a[184:187], v240 offset:34816\n"
    "v_mov_b32_e32 v227, v123\n"
    "v_mfma_f32_16x16x32_bf16 v[168:171], a[68:71], a[52:55], v[168:171]\n"
    "s_waitcnt lgkmcnt(7)\n"
    "ds_read_b128 a[188:191], v241 offset:34816\n"
    "v_cmp_nlt_f32_e32 vcc, 0x12800000, v129\n"
    "v_mfma_f32_16x16x32_bf16 v[156:159], a[72:75], a[56:59], v[156:159]\n"
    "buffer_load_dwordx4 a[208:211], v250, s[8:11], s88 offen offset:3072 sc1\n"
    "ds_read_b128 a[48:51], v242 offset:2048\n"
    "v_mfma_f32_16x16x32_bf16 v[160:163], a[76:79], a[56:59], v[160:163]\n"
    "s_add_u32 s62, s58, 16384\n"
    "v_cmp_nlt_f32_e64 s[28:29], v225, s45\n"
    "v_mfma_f32_16x16x32_bf16 v[164:167], a[80:83], a[56:59], v[164:167]\n"
    "buffer_load_dwordx4 a[0:3], v250, s[8:11], s62 offen offset:1024 sc1\n"
    "v_mfma_f32_16x16x32_bf16 v[168:171], a[84:87], a[56:59], v[168:171]\n"
    "s_or_b32 s45, s29, vcc_hi\n"
    "v_mfma_f32_16x16x32_bf16 v[156:159], a[88:91], a[60:63], v[156:159]\n"
    "s_lshr_b32 s45, s45, 16\n"
    "s_cselect_b32 s45, s51, 0\n"
    "ds_read_b128 a[52:55], v243 offset:2048\n"
    "v_mfma_f32_16x16x32_bf16 v[160:163], a[92:95], a[60:63], v[160:163]\n"
    "s_andn2_b32 s45, s45, s49\n"
    "v_mfma_f32_16x16x32_bf16 v[164:167], a[96:99], a[60:63], v[164:167]\n"
    "s_add_u32 s45, s45, s39\n"
    "v_mfma_f32_16x16x32_bf16 v[168:171], a[100:103], a[60:63], v[168:171]\n"
    "s_waitcnt lgkmcnt(8)\n"
    "v_mov_b32_e32 v254, s45\n"
    "v_mfma_f32_16x16x32_bf16 v[156:159], a[104:107], a[160:163], v[156:159]\n"
    "buffer_load_dwordx4 a[212:215], v250, s[8:11], s89 offen offset:3072 sc1\n"
    "ds_read_b128 a[56:59], v242 offset:34816\n"
    "v_mfma_f32_16x16x32_bf16 v[160:163], a[108:111], a[160:163], v[160:163]\n"
    "s_lshl1_add_u32 s55, s50, s51\n"
    "v_mfma_f32_16x16x32_bf16 v[164:167], a[112:115], a[160:163], v[164:167]\n"
    "s_lshl_b32 s55, s55, 12\n"
    "v_mfma_f32_16x16x32_bf16 v[168:171], a[116:119], a[160:163], v[168:171]\n"
    "s_xor_b32 s31, s93, 2\n"
    "v_mfma_f32_16x16x32_bf16 v[156:159], a[120:123], a[164:167], v[156:159]\n"
    "s_lshl_b32 s31, s31, 8\n"
    "ds_read_b128 a[60:63], v243 offset:34816\n"
    "v_mfma_f32_16x16x32_bf16 v[160:163], a[124:127], a[164:167], v[160:163]\n"
    "s_add_u32 s31, s31, 0x6000\n"
    "v_mfma_f32_16x16x32_bf16 v[164:167], a[128:131], a[164:167], v[164:167]\n"
    "s_mul_i32 s45, s49, 252\n"
    "v_mfma_f32_16x16x32_bf16 v[168:171], a[132:135], a[164:167], v[168:171]\n"
    "s_waitcnt lgkmcnt(8)\n"
    "v_mfma_f32_16x16x32_bf16 v[156:159], a[136:139], a[168:171], v[156:159]\n"
    "buffer_load_dword v220, v220, s[8:11], s60 offen offset:192 sc1\n"
    "ds_read_b128 a[160:163], v240 offset:4096\n"
    "v_mfma_f32_16x16x32_bf16 v[160:163], a[140:143], a[168:171], v[160:163]\n"
    "s_add_u32 s45, s45, s34\n"
    "v_mfma_f32_16x16x32_bf16 v[164:167], a[144:147], a[168:171], v[164:167]\n"
    "s_mul_i32 s45, s45, 101376\n"
    "v_mfma_f32_16x16x32_bf16 v[168:171], a[148:151], a[168:171], v[168:171]\n"
    "s_add_u32 s45, s45, 16654336\n"
    "v_mfma_f32_16x16x32_bf16 v[156:159], a[152:155], a[172:175], v[156:159]\n"
    "s_lshl_b32 s47, s93, 11\n"
    "ds_read_b128 a[164:167], v241 offset:4096\n"
    "v_mfma_f32_16x16x32_bf16 v[160:163], a[156:159], a[172:175], v[160:163]\n"
    "s_add_u32 s54, s47, s45\n"
    "v_and_b32_e32 v251, 0x7ff, v252\n"
    "v_mfma_f32_16x16x32_bf16 v[164:167], v[180:183], a[172:175], v[164:167]\n"
    "s_add_u32 s45, s45, 32768\n"
    "v_add_u32_e32 v251, s63, v251\n"
    "v_mfma_f32_16x16x32_bf16 v[168:171], v[184:187], a[172:175], v[168:171]\n"
    "s_waitcnt lgkmcnt(8)\n"
    "v_mfma_f32_16x16x32_bf16 v[196:199], a[216:219], a[176:179], 0\n"
    "s_cmp_lt_u32 s32, 2\n"
    "s_cselect_b32 s54, s54, 2.0\n"
    "s_cselect_b32 s45, s45, 2.0\n"
    "ds_read_b128 a[168:171], v240 offset:36864\n"
    "v_mfma_f32_16x16x32_bf16 v[200:203], a[220:223], a[176:179], 0\n"
    "s_add_u32 s80, s35, s46\n"
    "v_mfma_f32_16x16x32_bf16 v[204:207], a[224:227], a[176:179], 0\n"
    "s_add_u32 s82, s36, s46\n"
    "v_mul_f32_e32 v156, v156, v228\n"
    "v_mul_f32_e32 v157, v157, v228\n"
    "v_mfma_f32_16x16x32_bf16 v[208:211], a[228:231], a[176:179], 0\n"
    "s_add_u32 s28, s37, s46\n"
    "v_mul_f32_e32 v158, v158, v228\n"
    "v_mul_f32_e32 v159, v159, v228\n"
    "v_mfma_f32_16x16x32_bf16 v[196:199], a[232:235], a[180:183], v[196:199]\n"
    "ds_read_b128 a[172:175], v241 offset:36864\n"
    "v_mul_f32_e32 v160, v160, v228\n"
    "v_mfma_f32_16x16x32_bf16 v[200:203], v[192:195], a[180:183], v[200:203]\n"
    "v_add_u32_e32 v244, s55, v250\n"
    "v_mul_f32_e32 v161, v161, v228\n"
    "v_mfma_f32_16x16x32_bf16 v[204:207], a[64:67], a[180:183], v[204:207]\n"
    "v_lshrrev_b32_e32 v247, 2, v250\n"
    "v_mfma_f32_16x16x32_bf16 v[208:211], a[68:71], a[180:183], v[208:211]\n"
    "s_waitcnt lgkmcnt(8)\n"
    "v_mul_f32_e32 v162, v162, v228\n"
    "v_mfma_f32_16x16x32_bf16 v[196:199], a[72:75], a[184:187], v[196:199]\n"
    "ds_read_b128 a[176:179], v242 offset:4096\n"
    "v_mfma_f32_16x16x32_bf16 v[200:203], a[76:79], a[184:187], v[200:203]\n"
    "v_xor_b32_e32 v249, 64, v251\n"
    "v_mfma_f32_16x16x32_bf16 v[204:207], a[80:83], a[184:187], v[204:207]\n"
    "s_waitcnt vmcnt(4)\n"
    "buffer_store_dword v254, off, s[8:11], s90 offset:0 sc1\n"
    "v_mfma_f32_16x16x32_bf16 v[208:211], a[84:87], a[184:187], v[208:211]\n"
    ".long 0xbedf00ff\n"
    ".Lat_w13:\n"
    "s_setpc_b64 s[2:3]\n"
    "v_add_u32_e32 v247, s31, v247\n"
    "v_mul_f32_e32 v163, v163, v228\n"
    "v_mfma_f32_16x16x32_bf16 v[196:199], a[88:91], a[188:191], v[196:199]\n"
    "ds_read_b128 a[180:183], v243 offset:4096\n"
    "v_add_u32_e32 v246, s47, v250\n"
    "v_mfma_f32_16x16x32_bf16 v[200:203], a[92:95], a[188:191], v[200:203]\n"
    "v_and_b32_e32 v245, 0x603f, v247\n"
    "v_bfe_u32 v226, v250, 2, 4\n"
    "v_mfma_f32_16x16x32_bf16 v[204:207], a[96:99], a[188:191], v[204:207]\n"
    "v_mul_f32_e32 v164, v164, v228\n"
    "v_mfma_f32_16x16x32_bf16 v[208:211], a[100:103], a[188:191], v[208:211]\n"
    "s_waitcnt lgkmcnt(8)\n"
    "v_bfe_u32 v225, v250, 6, 2\n"
    "v_lshl_or_b32 v226, v225, 7, v226\n"
    "v_mfma_f32_16x16x32_bf16 v[196:199], a[104:107], a[48:51], v[196:199]\n"
    "ds_read_b128 a[184:187], v242 offset:36864\n"
    "v_mfma_f32_16x16x32_bf16 v[200:203], a[108:111], a[48:51], v[200:203]\n"
    "v_and_b32_e32 v225, 0x60ff, v247\n"
    "v_mul_f32_e32 v165, v165, v228\n"
    "v_mfma_f32_16x16x32_bf16 v[204:207], a[112:115], a[48:51], v[204:207]\n"
    "buffer_load_dwordx4 a[4:7], v250, s[8:11], s62 offen offset:2048 sc1\n"
    "v_mfma_f32_16x16x32_bf16 v[208:211], a[116:119], a[48:51], v[208:211]\n"
    "s_waitcnt vmcnt(20)\n"
    "v_mul_f32_e32 v166, v166, v228\n"
    "v_mfma_f32_16x16x32_bf16 v[196:199], a[120:123], a[52:55], v[196:199]\n"
    "ds_read_b128 a[188:191], v243 offset:36864\n"
    "v_mfma_f32_16x16x32_bf16 v[200:203], a[124:127], a[52:55], v[200:203]\n"
    "v_mul_f32_e32 v167, v167, v228\n"
    "v_mfma_f32_16x16x32_bf16 v[204:207], a[128:131], a[52:55], v[204:207]\n"
    "buffer_load_dwordx4 a[8:11], v250, s[8:11], s62 offen offset:3072 sc1\n"
    "v_mfma_f32_16x16x32_bf16 v[208:211], a[132:135], a[52:55], v[208:211]\n"
    "s_waitcnt lgkmcnt(8)\n"
    "v_mul_f32_e32 v168, v168, v228\n"
    "v_mfma_f32_16x16x32_bf16 v[196:199], a[136:139], a[56:59], v[196:199]\n"
    "s_add_u32 s62, s58, 20480\n"
    "v_mfma_f32_16x16x32_bf16 v[200:203], a[140:143], a[56:59], v[200:203]\n"
    "v_mul_f32_e32 v169, v169, v228\n"
    "v_mul_f32_e32 v170, v170, v228\n"
    "v_mfma_f32_16x16x32_bf16 v[204:207], a[144:147], a[56:59], v[204:207]\n"
    "buffer_load_dwordx4 a[12:15], v250, s[8:11], s62 offen offset:0 sc1\n"
    "v_mfma_f32_16x16x32_bf16 v[208:211], a[148:151], a[56:59], v[208:211]\n"
    "v_mul_f32_e32 v171, v171, v228\n"
    "v_mfma_f32_16x16x32_bf16 v[196:199], a[152:155], a[60:63], v[196:199]\n"
    "ds_read_b128 a[48:51], v251 offset:6144\n"
    "v_mfma_f32_16x16x32_bf16 v[200:203], a[156:159], a[60:63], v[200:203]\n"
    "ds_read_b128 a[52:55], v249 offset:6144\n"
    "v_mfma_f32_16x16x32_bf16 v[204:207], v[180:183], a[60:63], v[204:207]\n"
    "s_barrier\n"
    "v_mfma_f32_16x16x32_bf16 v[208:211], v[184:187], a[60:63], v[208:211]\n"
    "s_waitcnt vmcnt(4)\n"
    "buffer_load_dwordx4 a[16:19], v250, s[8:11], s62 offen offset:1024 sc1\n"
    "v_mfma_f32_16x16x32_bf16 a[236:239], v[234:237], a[244:247], a[236:239]\n"
    "v_mfma_f32_16x16x32_bf16 v[0:3], v[232:235], a[244:247], v[0:3]\n"
    "v_cmp_le_u32_e32 vcc, 0x300, v250\n"
    "v_mfma_f32_16x16x32_bf16 a[236:239], v[234:237], a[248:251], a[236:239]\n"
    "v_cndmask_b32_e32 v227, v220, v227, vcc\n"
    "v_mfma_f32_16x16x32_bf16 v[0:3], v[232:235], a[248:251], v[0:3]\n"
    "buffer_load_dwordx4 a[20:23], v250, s[8:11], s62 offen offset:2048 sc1\n"
    "v_mfma_f32_16x16x32_bf16 a[236:239], v[234:237], a[252:255], a[236:239]\n"
    "v_mfma_f32_16x16x32_bf16 v[0:3], v[232:235], a[252:255], v[0:3]\n"
    "ds_write_b32 v247, v227 offset:0\n"
    "v_mfma_f32_16x16x32_bf16 v[20:23], v[234:237], v[8:11], v[20:23]\n"
    "v_mfma_f32_16x16x32_bf16 v[40:43], v[232:235], v[8:11], v[40:43]\n"
    "buffer_load_dwordx4 a[24:27], v250, s[8:11], s62 offen offset:3072 sc1\n"
    "v_mfma_f32_16x16x32_bf16 v[20:23], v[234:237], v[12:15], v[20:23]\n"
    "s_add_u32 s62, s58, 24576\n"
    "v_accvgpr_read_b32 v64, a236\n"
    "v_accvgpr_read_b32 v65, a237\n"
    "v_mfma_f32_16x16x32_bf16 v[40:43], v[232:235], v[12:15], v[40:43]\n"
    "v_accvgpr_read_b32 v66, a238\n"
    "v_accvgpr_read_b32 v67, a239\n"
    "v_mfma_f32_16x16x32_bf16 v[20:23], v[234:237], v[16:19], v[20:23]\n"
    "v_cvt_pk_bf16_f32 v64, v64, v65\n"
    "v_cvt_pk_bf16_f32 v65, v66, v67\n"
    "v_mfma_f32_16x16x32_bf16 v[40:43], v[232:235], v[16:19], v[40:43]\n"
    "buffer_load_dwordx4 a[28:31], v250, s[8:11], s62 offen offset:0 sc1\n"
    "v_mfma_f32_16x16x32_bf16 v[60:63], v[234:237], a[192:195], v[60:63]\n"
    "v_cvt_pk_bf16_f32 v66, v0, v1\n"
    "v_cvt_pk_bf16_f32 v67, v2, v3\n"
    "v_mfma_f32_16x16x32_bf16 v[80:83], v[232:235], a[192:195], v[80:83]\n"
    "ds_write_b128 v244, v[64:67] offset:0\n"
    "v_mfma_f32_16x16x32_bf16 v[60:63], v[234:237], a[196:199], v[60:63]\n"
    "v_mfma_f32_16x16x32_bf16 v[80:83], v[232:235], a[196:199], v[80:83]\n"
    "buffer_load_dwordx4 a[32:35], v250, s[8:11], s62 offen offset:1024 sc1\n"
    "v_mfma_f32_16x16x32_bf16 v[60:63], v[234:237], a[200:203], v[60:63]\n"
    "v_cvt_pk_bf16_f32 v20, v20, v21\n"
    "v_cvt_pk_bf16_f32 v21, v22, v23\n"
    "v_mfma_f32_16x16x32_bf16 v[80:83], v[232:235], a[200:203], v[80:83]\n"
    "v_cvt_pk_bf16_f32 v22, v40, v41\n"
    "v_cvt_pk_bf16_f32 v23, v42, v43\n"
    "v_mfma_f32_16x16x32_bf16 v[100:103], v[234:237], a[204:207], v[100:103]\n"
    "v_mfma_f32_16x16x32_bf16 v[120:123], v[232:235], a[204:207], v[120:123]\n"
    "buffer_load_dwordx4 a[36:39], v250, s[8:11], s62 offen offset:2048 sc1\n"
    "v_mfma_f32_16x16x32_bf16 v[100:103], v[234:237], a[208:211], v[100:103]\n"
    "ds_write_b128 v244, v[20:23] offset:1024\n"
    "v_mfma_f32_16x16x32_bf16 v[120:123], v[232:235], a[208:211], v[120:123]\n"
    "v_cvt_pk_bf16_f32 v60, v60, v61\n"
    "v_cvt_pk_bf16_f32 v61, v62, v63\n"
    "v_mfma_f32_16x16x32_bf16 v[100:103], v[234:237], a[212:215], v[100:103]\n"
    "v_mfma_f32_16x16x32_bf16 v[120:123], v[232:235], a[212:215], v[120:123]\n"
    "s_waitcnt lgkmcnt(11)\n"
    "v_cvt_pk_bf16_f32 v62, v80, v81\n"
    "v_cvt_pk_bf16_f32 v63, v82, v83\n"
    "v_mfma_f32_16x16x32_bf16 v[172:175], a[216:219], a[160:163], 0\n"
    "buffer_load_dword v253, v226, s[8:11], s61 offen offset:0 sc1\n"
    "v_mfma_f32_16x16x32_bf16 v[176:179], a[220:223], a[160:163], 0\n"
    "ds_write_b128 v244, v[60:63] offset:2048\n"
    "v_mfma_f32_16x16x32_bf16 v[212:215], a[224:227], a[160:163], 0\n"
    "buffer_load_dwordx4 a[40:43], v250, s[8:11], s62 offen offset:3072 sc1\n"
    "v_mfma_f32_16x16x32_bf16 v[216:219], a[228:231], a[160:163], 0\n"
    "s_add_u32 s62, s58, 28672\n"
    "v_cvt_pk_bf16_f32 v100, v100, v101\n"
    "v_cvt_pk_bf16_f32 v101, v102, v103\n"
    "v_mfma_f32_16x16x32_bf16 v[172:175], a[232:235], a[164:167], v[172:175]\n"
    "v_cvt_pk_bf16_f32 v102, v120, v121\n"
    "v_cvt_pk_bf16_f32 v103, v122, v123\n"
    "v_mfma_f32_16x16x32_bf16 v[176:179], v[192:195], a[164:167], v[176:179]\n"
    "ds_write_b128 v244, v[100:103] offset:3072\n"
    "v_mfma_f32_16x16x32_bf16 v[212:215], a[64:67], a[164:167], v[212:215]\n"
    "buffer_load_dwordx4 a[44:47], v250, s[8:11], s62 offen offset:0 sc1\n"
    "v_mfma_f32_16x16x32_bf16 v[216:219], a[68:71], a[164:167], v[216:219]\n"
    "s_waitcnt lgkmcnt(12)\n"
    "v_mfma_f32_16x16x32_bf16 v[172:175], a[72:75], a[168:171], v[172:175]\n"
    "v_mfma_f32_16x16x32_bf16 v[176:179], a[76:79], a[168:171], v[176:179]\n"
    "s_waitcnt lgkmcnt(0)\n"
    "v_mfma_f32_16x16x32_bf16 v[212:215], a[80:83], a[168:171], v[212:215]\n"
    "s_barrier\n"
    "v_mfma_f32_16x16x32_bf16 v[216:219], a[84:87], a[168:171], v[216:219]\n"
    "buffer_load_dwordx4 a[240:243], v250, s[8:11], s62 offen offset:1024 sc1\n"
    "v_mfma_f32_16x16x32_bf16 v[172:175], a[88:91], a[172:175], v[172:175]\n"
    "ds_read_b128 a[244:247], v250 offset:0\n"
    "v_mfma_f32_16x16x32_bf16 v[176:179], a[92:95], a[172:175], v[176:179]\n"
    "ds_read_b128 a[248:251], v250 offset:1024\n"
    "v_mfma_f32_16x16x32_bf16 v[212:215], a[96:99], a[172:175], v[212:215]\n"
    "ds_read_b128 a[192:195], v250 offset:2048\n"
    "v_mfma_f32_16x16x32_bf16 v[216:219], a[100:103], a[172:175], v[216:219]\n"
    "ds_read_b128 a[196:199], v250 offset:3072\n"
    "v_mfma_f32_16x16x32_bf16 v[172:175], a[104:107], a[176:179], v[172:175]\n"
    "buffer_load_dwordx4 v[4:7], v250, s[8:11], s62 offen offset:2048 sc1\n"
    "v_mfma_f32_16x16x32_bf16 v[176:179], a[108:111], a[176:179], v[176:179]\n"
    "ds_read_b128 a[200:203], v250 offset:4096\n"
    "v_mfma_f32_16x16x32_bf16 v[212:215], a[112:115], a[176:179], v[212:215]\n"
    "ds_read_b128 a[204:207], v250 offset:5120\n"
    "v_mfma_f32_16x16x32_bf16 v[216:219], a[116:119], a[176:179], v[216:219]\n"
    "ds_read_b128 a[208:211], v250 offset:6144\n"
    "v_mfma_f32_16x16x32_bf16 v[172:175], a[120:123], a[180:183], v[172:175]\n"
    "buffer_load_dwordx4 v[24:27], v250, s[8:11], s62 offen offset:3072 sc1\n"
    "v_mfma_f32_16x16x32_bf16 v[176:179], a[124:127], a[180:183], v[176:179]\n"
    "v_mul_f32_e32 v196, v196, v229\n"
    "v_mul_f32_e32 v197, v197, v229\n"
    "v_mfma_f32_16x16x32_bf16 v[212:215], a[128:131], a[180:183], v[212:215]\n"
    "ds_read_b128 a[212:215], v250 offset:7168\n"
    "v_mfma_f32_16x16x32_bf16 v[216:219], a[132:135], a[180:183], v[216:219]\n"
    "ds_read2_b32 v[220:221], v245 offset0:0 offset1:16\n"
    "v_mfma_f32_16x16x32_bf16 v[172:175], a[136:139], a[184:187], v[172:175]\n"
    "ds_read2_b32 v[222:223], v245 offset0:32 offset1:48\n"
    "v_mfma_f32_16x16x32_bf16 v[176:179], a[140:143], a[184:187], v[176:179]\n"
    "ds_read2_b32 v[80:81], v245 offset0:64 offset1:80\n"
    "v_mfma_f32_16x16x32_bf16 v[212:215], a[144:147], a[184:187], v[212:215]\n"
    "v_mul_f32_e32 v198, v198, v229\n"
    "v_mul_f32_e32 v199, v199, v229\n"
    "v_mfma_f32_16x16x32_bf16 v[216:219], a[148:151], a[184:187], v[216:219]\n"
    "ds_read2_b32 v[82:83], v245 offset0:96 offset1:112\n"
    "v_mfma_f32_16x16x32_bf16 v[172:175], a[152:155], a[188:191], v[172:175]\n"
    "v_mul_f32_e32 v200, v200, v229\n"
    "v_mfma_f32_16x16x32_bf16 v[176:179], a[156:159], a[188:191], v[176:179]\n"
    "v_mfma_f32_16x16x32_bf16 v[212:215], v[180:183], a[188:191], v[212:215]\n"
    "v_mul_f32_e32 v201, v201, v229\n"
    "v_mfma_f32_16x16x32_bf16 v[216:219], v[184:187], a[188:191], v[216:219]\n"
    "v_mul_f32_e32 v202, v202, v229\n"
    "v_mfma_f32_16x16x32_bf16 v[156:159], v[234:237], a[48:51], v[156:159]\n"
    "v_mul_f32_e32 v203, v203, v229\n"
    "v_mfma_f32_16x16x32_bf16 v[160:163], v[232:235], a[48:51], v[160:163]\n"
    "ds_read_b128 a[64:67], v250 offset:8192\n"
    "v_mul_f32_e32 v204, v204, v229\n"
    "v_mfma_f32_16x16x32_bf16 v[164:167], v[234:237], a[52:55], v[164:167]\n"
    "ds_read_b128 a[68:71], v250 offset:9216\n"
    "v_mfma_f32_16x16x32_bf16 v[168:171], v[232:235], a[52:55], v[168:171]\n"
    "s_waitcnt lgkmcnt(12)\n"
    "ds_read_b128 a[72:75], v250 offset:10240\n"
    "v_mfma_f32_16x16x32_bf16 v[140:143], v[28:31], a[244:247], 0\n"
    "ds_read_b128 a[76:79], v250 offset:11264\n"
    "v_mfma_f32_16x16x32_bf16 v[144:147], v[48:51], a[244:247], 0\n"
    "v_mul_f32_e32 v205, v205, v229\n"
    "v_mul_f32_e32 v206, v206, v229\n"
    "v_mfma_f32_16x16x32_bf16 v[148:151], v[32:35], a[244:247], 0\n"
    "v_mul_f32_e32 v207, v207, v229\n"
    "v_mfma_f32_16x16x32_bf16 v[152:155], v[52:55], a[244:247], 0\n"
    "s_waitcnt vmcnt(5)\n"
    "v_subrev_u32_e32 v253, s39, v253\n"
    "v_cmp_lt_u32_e32 vcc, 1, v253\n"
    "v_mfma_f32_16x16x32_bf16 v[140:143], v[36:39], a[248:251], v[140:143]\n"
    "ds_read_b128 a[80:83], v250 offset:12288\n"
    "v_mfma_f32_16x16x32_bf16 v[144:147], v[56:59], a[248:251], v[144:147]\n"
    "ds_read_b128 a[84:87], v250 offset:13312\n"
    "v_mul_f32_e32 v208, v208, v229\n"
    "v_mfma_f32_16x16x32_bf16 v[148:151], v[68:71], a[248:251], v[148:151]\n"
    "s_cbranch_vccnz .Lat_ppoll\n"
    ".Lat_pok:\n"
    "v_cmp_eq_u32_e32 vcc, 1, v253\n"
    "v_mfma_f32_16x16x32_bf16 v[152:155], v[88:91], a[248:251], v[152:155]\n"
    "s_waitcnt lgkmcnt(9)\n"
    "buffer_load_dwordx4 a[128:131], v250, s[8:11], s54 offen offset:0 sc1\n"
    "v_mfma_f32_16x16x32_bf16 v[140:143], v[72:75], a[192:195], v[140:143]\n"
    "v_add_f32_e32 v220, v220, v221\n"
    "v_mfma_f32_16x16x32_bf16 v[144:147], v[92:95], a[192:195], v[144:147]\n"
    "s_cbranch_vccnz .Lat_redo\n"
    "v_mfma_f32_16x16x32_bf16 v[148:151], v[76:79], a[192:195], v[148:151]\n"
    "buffer_load_dwordx4 a[132:135], v250, s[8:11], s54 offen offset:1024 sc1\n"
    "v_mfma_f32_16x16x32_bf16 v[152:155], v[96:99], a[192:195], v[152:155]\n"
    "s_add_u32 s54, s54, 8192\n"
    "ds_read_b128 a[88:91], v250 offset:14336\n"
    "v_mul_f32_e32 v209, v209, v229\n"
    "v_mfma_f32_16x16x32_bf16 v[140:143], v[108:111], a[196:199], v[140:143]\n"
    "buffer_load_dwordx4 a[136:139], v250, s[8:11], s54 offen offset:0 sc1\n"
    "v_mfma_f32_16x16x32_bf16 v[144:147], v[112:115], a[196:199], v[144:147]\n"
    "ds_read_b128 a[92:95], v250 offset:15360\n"
    "v_mfma_f32_16x16x32_bf16 v[148:151], v[132:135], a[196:199], v[148:151]\n"
    "s_waitcnt vmcnt(3)\n"
    "buffer_load_dwordx4 a[140:143], v250, s[8:11], s54 offen offset:1024 sc1\n"
    "v_mfma_f32_16x16x32_bf16 v[152:155], v[116:119], a[196:199], v[152:155]\n"
    "s_add_u32 s54, s54, 8192\n"
    "s_waitcnt lgkmcnt(10)\n"
    "v_mfma_f32_16x16x32_bf16 v[140:143], v[136:139], a[200:203], v[140:143]\n"
    "buffer_load_dwordx4 a[144:147], v250, s[8:11], s54 offen offset:0 sc1\n"
    "v_mfma_f32_16x16x32_bf16 v[144:147], a[0:3], a[200:203], v[144:147]\n"
    "ds_read_b128 a[56:59], v252 offset:30720\n"
    "v_mfma_f32_16x16x32_bf16 v[148:151], a[4:7], a[200:203], v[148:151]\n"
    "buffer_load_dwordx4 a[148:151], v250, s[8:11], s54 offen offset:1024 sc1\n"
    "v_mfma_f32_16x16x32_bf16 v[152:155], a[8:11], a[200:203], v[152:155]\n"
    "s_add_u32 s54, s54, 8192\n"
    "v_mul_f32_e32 v210, v210, v229\n"
    "v_add_f32_e32 v222, v222, v223\n"
    "v_mfma_f32_16x16x32_bf16 v[140:143], a[12:15], a[204:207], v[140:143]\n"
    "buffer_load_dwordx4 a[152:155], v250, s[8:11], s54 offen offset:0 sc1\n"
    "v_mfma_f32_16x16x32_bf16 v[144:147], a[16:19], a[204:207], v[144:147]\n"
    "ds_read_b128 a[60:63], v255 offset:30720\n"
    "v_mfma_f32_16x16x32_bf16 v[148:151], a[20:23], a[204:207], v[148:151]\n"
    "buffer_load_dwordx4 a[156:159], v250, s[8:11], s54 offen offset:1024 sc1\n"
    "v_mfma_f32_16x16x32_bf16 v[152:155], a[24:27], a[204:207], v[152:155]\n"
    "s_waitcnt lgkmcnt(10)\n"
    "v_mul_f32_e32 v211, v211, v229\n"
    "v_mfma_f32_16x16x32_bf16 v[140:143], a[28:31], a[208:211], v[140:143]\n"
    "buffer_load_dword v227, v248, s[8:11], s45 offen offset:1088 sc1\n"
    "v_mfma_f32_16x16x32_bf16 v[144:147], a[32:35], a[208:211], v[144:147]\n"
    "v_add_f32_e32 v220, v220, v222\n"
    "v_mfma_f32_16x16x32_bf16 v[148:151], a[36:39], a[208:211], v[148:151]\n"
    "v_rcp_f32_e32 v220, v220\n"
    "v_mfma_f32_16x16x32_bf16 v[152:155], a[40:43], a[208:211], v[152:155]\n"
    "v_add_f32_e32 v80, v80, v81\n"
    "v_add_f32_e32 v82, v82, v83\n"
    "v_mfma_f32_16x16x32_bf16 v[140:143], a[44:47], a[212:215], v[140:143]\n"
    "v_mul_f32_e32 v172, v172, v230\n"
    "v_add_f32_e32 v80, v80, v82\n"
    "v_mfma_f32_16x16x32_bf16 v[144:147], a[240:243], a[212:215], v[144:147]\n"
    "v_rcp_f32_e32 v80, v80\n"
    "v_mfma_f32_16x16x32_bf16 v[148:151], v[4:7], a[212:215], v[148:151]\n"
    "ds_read_b128 a[160:163], v252 offset:4096\n"
    "v_mfma_f32_16x16x32_bf16 v[152:155], v[24:27], a[212:215], v[152:155]\n"
    "s_waitcnt lgkmcnt(9)\n"
    "ds_read_b128 a[164:167], v255 offset:4096\n"
    "v_mfma_f32_16x16x32_bf16 v[180:183], v[28:31], a[64:67], 0\n"
    "v_mul_f32_e32 v173, v173, v230\n"
    "v_mfma_f32_16x16x32_bf16 v[184:187], v[48:51], a[64:67], 0\n"
    "v_fma_f32 v140, v140, v220, v156\n"
    "v_fma_f32 v141, v141, v220, v157\n"
    "v_mfma_f32_16x16x32_bf16 v[188:191], v[32:35], a[64:67], 0\n"
    "v_mfma_f32_16x16x32_bf16 v[192:195], v[52:55], a[64:67], 0\n"
    "v_fma_f32 v142, v142, v220, v158\n"
    "v_mfma_f32_16x16x32_bf16 v[180:183], v[36:39], a[68:71], v[180:183]\n"
    "v_fma_f32 v143, v143, v220, v159\n"
    "v_mfma_f32_16x16x32_bf16 v[184:187], v[56:59], a[68:71], v[184:187]\n"
    "v_fma_f32 v144, v144, v220, v160\n"
    "v_fma_f32 v145, v145, v220, v161\n"
    "v_mfma_f32_16x16x32_bf16 v[188:191], v[68:71], a[68:71], v[188:191]\n"
    "v_fma_f32 v146, v146, v220, v162\n"
    "v_mfma_f32_16x16x32_bf16 v[192:195], v[88:91], a[68:71], v[192:195]\n"
    "s_waitcnt lgkmcnt(8)\n"
    "v_fma_f32 v147, v147, v220, v163\n"
    "v_mfma_f32_16x16x32_bf16 v[180:183], v[72:75], a[72:75], v[180:183]\n"
    "v_mul_f32_e32 v174, v174, v230\n"
    "v_mfma_f32_16x16x32_bf16 v[184:187], v[92:95], a[72:75], v[184:187]\n"
    "v_cvt_pk_bf16_f32 v140, v140, v141\n"
    "v_cvt_pk_bf16_f32 v141, v142, v143\n"
    "v_mfma_f32_16x16x32_bf16 v[188:191], v[76:79], a[72:75], v[188:191]\n"
    "v_cvt_pk_bf16_f32 v142, v144, v145\n"
    "v_mfma_f32_16x16x32_bf16 v[192:195], v[96:99], a[72:75], v[192:195]\n"
    "v_cvt_pk_bf16_f32 v143, v146, v147\n"
    "v_mfma_f32_16x16x32_bf16 v[180:183], v[108:111], a[76:79], v[180:183]\n"
    "buffer_store_dwordx4 v[140:143], v239, s[16:19], s80 offen offset:0 sc0 sc1\n"
    "v_mfma_f32_16x16x32_bf16 v[184:187], v[112:115], a[76:79], v[184:187]\n"
    "v_mul_f32_e32 v175, v175, v230\n"
    "v_mfma_f32_16x16x32_bf16 v[188:191], v[132:135], a[76:79], v[188:191]\n"
    "v_fma_f32 v148, v148, v220, v164\n"
    "v_mfma_f32_16x16x32_bf16 v[192:195], v[116:119], a[76:79], v[192:195]\n"
    "s_waitcnt lgkmcnt(6)\n"
    "v_fma_f32 v149, v149, v220, v165\n"
    "v_fma_f32 v150, v150, v220, v166\n"
    "v_mfma_f32_16x16x32_bf16 v[180:183], v[136:139], a[80:83], v[180:183]\n"
    "v_fma_f32 v151, v151, v220, v167\n"
    "v_mfma_f32_16x16x32_bf16 v[184:187], a[0:3], a[80:83], v[184:187]\n"
    "v_fma_f32 v152, v152, v220, v168\n"
    "v_mfma_f32_16x16x32_bf16 v[188:191], a[4:7], a[80:83], v[188:191]\n"
    "v_fma_f32 v153, v153, v220, v169\n"
    "v_fma_f32 v154, v154, v220, v170\n"
    "v_mfma_f32_16x16x32_bf16 v[192:195], a[8:11], a[80:83], v[192:195]\n"
    "v_fma_f32 v155, v155, v220, v171\n"
    "v_cvt_pk_bf16_f32 v148, v148, v149\n"
    "v_mfma_f32_16x16x32_bf16 v[180:183], a[12:15], a[84:87], v[180:183]\n"
    "v_cvt_pk_bf16_f32 v149, v150, v151\n"
    "v_cvt_pk_bf16_f32 v150, v152, v153\n"
    "v_mfma_f32_16x16x32_bf16 v[184:187], a[16:19], a[84:87], v[184:187]\n"
    "v_cvt_pk_bf16_f32 v151, v154, v155\n"
    "v_mfma_f32_16x16x32_bf16 v[188:191], a[20:23], a[84:87], v[188:191]\n"
    "buffer_store_dwordx4 v[148:151], v239, s[16:19], s80 offen offset:64 sc0 sc1\n"
    "v_mfma_f32_16x16x32_bf16 v[192:195], a[24:27], a[84:87], v[192:195]\n"
    "s_waitcnt lgkmcnt(4)\n"
    "v_mfma_f32_16x16x32_bf16 v[180:183], a[28:31], a[88:91], v[180:183]\n"
    "v_mul_f32_e32 v176, v176, v230\n"
    "v_mul_f32_e32 v177, v177, v230\n"
    "v_mfma_f32_16x16x32_bf16 v[184:187], a[32:35], a[88:91], v[184:187]\n"
    "v_mul_f32_e32 v178, v178, v230\n"
    "v_mul_f32_e32 v179, v179, v230\n"
    "v_mfma_f32_16x16x32_bf16 v[188:191], a[36:39], a[88:91], v[188:191]\n"
    "v_mul_f32_e32 v212, v212, v230\n"
    "v_mfma_f32_16x16x32_bf16 v[192:195], a[40:43], a[88:91], v[192:195]\n"
    "s_waitcnt vmcnt(9)\n"
    "v_mul_f32_e32 v213, v213, v230\n"
    "v_mfma_f32_16x16x32_bf16 v[64:67], v[234:237], a[128:131], 0\n"
    "v_mfma_f32_16x16x32_bf16 v[84:87], v[232:235], a[128:131], 0\n"
    "v_mfma_f32_16x16x32_bf16 v[104:107], v[234:237], a[132:135], 0\n"
    "v_mul_f32_e32 v214, v214, v230\n"
    "v_mfma_f32_16x16x32_bf16 v[124:127], v[232:235], a[132:135], 0\n"
    "s_waitcnt vmcnt(7)\n"
    "v_mfma_f32_16x16x32_bf16 v[64:67], v[234:237], a[136:139], v[64:67]\n"
    "v_mfma_f32_16x16x32_bf16 v[84:87], v[232:235], a[136:139], v[84:87]\n"
    "v_mul_f32_e32 v215, v215, v230\n"
    "v_mfma_f32_16x16x32_bf16 v[104:107], v[234:237], a[140:143], v[104:107]\n"
    "v_mul_f32_e32 v216, v216, v230\n"
    "v_mfma_f32_16x16x32_bf16 v[124:127], v[232:235], a[140:143], v[124:127]\n"
    "s_waitcnt vmcnt(5)\n"
    "v_mul_f32_e32 v217, v217, v230\n"
    "v_mfma_f32_16x16x32_bf16 v[64:67], v[234:237], a[144:147], v[64:67]\n"
    "v_mul_f32_e32 v218, v218, v230\n"
    "v_mfma_f32_16x16x32_bf16 v[84:87], v[232:235], a[144:147], v[84:87]\n"
    "v_mul_f32_e32 v219, v219, v230\n"
    "v_mfma_f32_16x16x32_bf16 v[104:107], v[234:237], a[148:151], v[104:107]\n"
    "v_mfma_f32_16x16x32_bf16 v[124:127], v[232:235], a[148:151], v[124:127]\n"
    "s_waitcnt vmcnt(3)\n"
    "v_mfma_f32_16x16x32_bf16 v[64:67], v[234:237], a[152:155], v[64:67]\n"
    "s_waitcnt vmcnt(2)\n"
    "ds_write_b32 v225, v227 offset:512\n"
    "v_mfma_f32_16x16x32_bf16 v[84:87], v[232:235], a[152:155], v[84:87]\n"
    "v_mfma_f32_16x16x32_bf16 v[104:107], v[234:237], a[156:159], v[104:107]\n"
    "v_mfma_f32_16x16x32_bf16 v[124:127], v[232:235], a[156:159], v[124:127]\n"
    "v_cvt_pk_bf16_f32 v64, v64, v65\n"
    "v_cvt_pk_bf16_f32 v65, v66, v67\n"
    "v_mfma_f32_16x16x32_bf16 v[180:183], a[44:47], a[92:95], v[180:183]\n"
    "v_cvt_pk_bf16_f32 v66, v84, v85\n"
    "v_cvt_pk_bf16_f32 v67, v86, v87\n"
    "v_mfma_f32_16x16x32_bf16 v[184:187], a[240:243], a[92:95], v[184:187]\n"
    "ds_write_b128 v246, v[64:67] offset:16384\n"
    "v_mfma_f32_16x16x32_bf16 v[188:191], v[4:7], a[92:95], v[188:191]\n"
    "v_cvt_pk_bf16_f32 v104, v104, v105\n"
    "v_cvt_pk_bf16_f32 v105, v106, v107\n"
    "v_mfma_f32_16x16x32_bf16 v[192:195], v[24:27], a[92:95], v[192:195]\n"
    "s_waitcnt lgkmcnt(4)\n"
    "v_cvt_pk_bf16_f32 v106, v124, v125\n"
    "v_cvt_pk_bf16_f32 v107, v126, v127\n"
    "v_mfma_f32_16x16x32_bf16 v[196:199], v[234:237], a[56:59], v[196:199]\n"
    "ds_write_b128 v246, v[104:107] offset:17408\n"
    "v_mfma_f32_16x16x32_bf16 v[200:203], v[232:235], a[56:59], v[200:203]\n"
    "v_mfma_f32_16x16x32_bf16 v[204:207], v[234:237], a[60:63], v[204:207]\n"
    "s_waitcnt lgkmcnt(0)\n"
    "v_mfma_f32_16x16x32_bf16 v[208:211], v[232:235], a[60:63], v[208:211]\n"
    "s_barrier\n"
    "v_mfma_f32_16x16x32_bf16 v[172:175], v[234:237], a[160:163], v[172:175]\n"
    "ds_read_b128 a[96:99], v250 offset:16384\n"
    "v_mfma_f32_16x16x32_bf16 v[176:179], v[232:235], a[160:163], v[176:179]\n"
    "ds_read_b128 a[100:103], v250 offset:17408\n"
    "v_mfma_f32_16x16x32_bf16 v[212:215], v[234:237], a[164:167], v[212:215]\n"
    "ds_read_b128 a[104:107], v250 offset:18432\n"
    "ds_read_b128 a[108:111], v250 offset:19456\n"
    "v_fma_f32 v180, v180, v80, v196\n"
    "v_mfma_f32_16x16x32_bf16 v[216:219], v[232:235], a[164:167], v[216:219]\n"
    "s_waitcnt lgkmcnt(2)\n"
    "ds_read_b128 a[112:115], v250 offset:20480\n"
    "ds_read_b128 a[116:119], v250 offset:21504\n"
    "v_mfma_f32_16x16x32_bf16 v[0:3], v[28:31], a[96:99], 0\n"
    "ds_read_b128 a[120:123], v250 offset:22528\n"
    "ds_read_b128 a[124:127], v250 offset:23552\n"
    "v_mfma_f32_16x16x32_bf16 v[20:23], v[48:51], a[96:99], 0\n"
    "ds_read2_b32 v[220:221], v245 offset0:128 offset1:144\n"
    "ds_read2_b32 v[222:223], v245 offset0:160 offset1:176\n"
    "v_mfma_f32_16x16x32_bf16 v[0:3], v[36:39], a[100:103], v[0:3]\n"
    "v_fma_f32 v181, v181, v80, v197\n"
    "v_fma_f32 v182, v182, v80, v198\n"
    "v_mfma_f32_16x16x32_bf16 v[20:23], v[56:59], a[100:103], v[20:23]\n"
    "s_waitcnt lgkmcnt(6)\n"
    "v_fma_f32 v183, v183, v80, v199\n"
    "v_fma_f32 v184, v184, v80, v200\n"
    "v_mfma_f32_16x16x32_bf16 v[0:3], v[72:75], a[104:107], v[0:3]\n"
    "s_or_b32 s45, s34, s93\n"
    "s_cselect_b32 s45, 2.0, 6101760\n"
    "v_fma_f32 v185, v185, v80, v201\n"
    "v_mfma_f32_16x16x32_bf16 v[20:23], v[92:95], a[104:107], v[20:23]\n"
    "v_fma_f32 v186, v186, v80, v202\n"
    "v_fma_f32 v187, v187, v80, v203\n"
    "v_mfma_f32_16x16x32_bf16 v[0:3], v[108:111], a[108:111], v[0:3]\n"
    "v_cvt_pk_bf16_f32 v180, v180, v181\n"
    "v_cvt_pk_bf16_f32 v181, v182, v183\n"
    "v_mfma_f32_16x16x32_bf16 v[20:23], v[112:115], a[108:111], v[20:23]\n"
    "s_waitcnt lgkmcnt(4)\n"
    "v_cvt_pk_bf16_f32 v182, v184, v185\n"
    "v_cvt_pk_bf16_f32 v183, v186, v187\n"
    "v_mfma_f32_16x16x32_bf16 v[0:3], v[136:139], a[112:115], v[0:3]\n"
    "buffer_store_dwordx4 v[180:183], v239, s[16:19], s82 offen offset:0 sc0 sc1\n"
    "v_mfma_f32_16x16x32_bf16 v[20:23], a[0:3], a[112:115], v[20:23]\n"
    "v_fma_f32 v188, v188, v80, v204\n"
    "v_fma_f32 v189, v189, v80, v205\n"
    "v_mfma_f32_16x16x32_bf16 v[0:3], a[12:15], a[116:119], v[0:3]\n"
    "v_fma_f32 v190, v190, v80, v206\n"
    "v_fma_f32 v191, v191, v80, v207\n"
    "v_mfma_f32_16x16x32_bf16 v[20:23], a[16:19], a[116:119], v[20:23]\n"
    "s_waitcnt lgkmcnt(0)\n"
    "v_fma_f32 v192, v192, v80, v208\n"
    "v_fma_f32 v193, v193, v80, v209\n"
    "v_fma_f32 v194, v194, v80, v210\n"
    "v_mfma_f32_16x16x32_bf16 v[0:3], a[28:31], a[120:123], v[0:3]\n"
    "v_fma_f32 v195, v195, v80, v211\n"
    "v_cvt_pk_bf16_f32 v188, v188, v189\n"
    "v_mfma_f32_16x16x32_bf16 v[20:23], a[32:35], a[120:123], v[20:23]\n"
    "v_cvt_pk_bf16_f32 v189, v190, v191\n"
    "v_cvt_pk_bf16_f32 v190, v192, v193\n"
    "v_cvt_pk_bf16_f32 v191, v194, v195\n"
    "v_mfma_f32_16x16x32_bf16 v[0:3], a[44:47], a[124:127], v[0:3]\n"
    "buffer_store_dwordx4 v[188:191], v239, s[16:19], s82 offen offset:64 sc0 sc1\n"
    "v_mfma_f32_16x16x32_bf16 v[20:23], a[240:243], a[124:127], v[20:23]\n"
    "v_add_f32_e32 v220, v220, v221\n"
    "v_add_f32_e32 v222, v222, v223\n"
    "v_mfma_f32_16x16x32_bf16 v[40:43], v[32:35], a[96:99], 0\n"
    "v_add_f32_e32 v220, v220, v222\n"
    "v_mfma_f32_16x16x32_bf16 v[60:63], v[52:55], a[96:99], 0\n"
    "v_rcp_f32_e32 v220, v220\n"
    "v_mfma_f32_16x16x32_bf16 v[40:43], v[68:71], a[100:103], v[40:43]\n"
    "v_fma_f32 v0, v0, v220, v172\n"
    "v_mfma_f32_16x16x32_bf16 v[60:63], v[88:91], a[100:103], v[60:63]\n"
    "v_fma_f32 v1, v1, v220, v173\n"
    "v_fma_f32 v2, v2, v220, v174\n"
    "v_mfma_f32_16x16x32_bf16 v[40:43], v[76:79], a[104:107], v[40:43]\n"
    "v_fma_f32 v3, v3, v220, v175\n"
    "v_mfma_f32_16x16x32_bf16 v[60:63], v[96:99], a[104:107], v[60:63]\n"
    "v_fma_f32 v20, v20, v220, v176\n"
    "v_fma_f32 v21, v21, v220, v177\n"
    "v_mfma_f32_16x16x32_bf16 v[40:43], v[132:135], a[108:111], v[40:43]\n"
    "v_fma_f32 v22, v22, v220, v178\n"
    "v_fma_f32 v23, v23, v220, v179\n"
    "v_mfma_f32_16x16x32_bf16 v[60:63], v[116:119], a[108:111], v[60:63]\n"
    "v_cvt_pk_bf16_f32 v0, v0, v1\n"
    "v_cvt_pk_bf16_f32 v1, v2, v3\n"
    "v_mfma_f32_16x16x32_bf16 v[40:43], a[4:7], a[112:115], v[40:43]\n"
    "v_cvt_pk_bf16_f32 v2, v20, v21\n"
    "v_cvt_pk_bf16_f32 v3, v22, v23\n"
    "v_mfma_f32_16x16x32_bf16 v[60:63], a[8:11], a[112:115], v[60:63]\n"
    "buffer_store_dwordx4 v[0:3], v239, s[16:19], s28 offen offset:0 sc0 sc1\n"
    "v_mfma_f32_16x16x32_bf16 v[40:43], a[20:23], a[116:119], v[40:43]\n"
    "v_mfma_f32_16x16x32_bf16 v[60:63], a[24:27], a[116:119], v[60:63]\n"
    "v_mfma_f32_16x16x32_bf16 v[40:43], a[36:39], a[120:123], v[40:43]\n"
    "v_mfma_f32_16x16x32_bf16 v[60:63], a[40:43], a[120:123], v[60:63]\n"
    "v_mfma_f32_16x16x32_bf16 v[40:43], v[4:7], a[124:127], v[40:43]\n"
    "v_mfma_f32_16x16x32_bf16 v[60:63], v[24:27], a[124:127], v[60:63]\n"
    "v_mov_b32_e32 v254, s39\n"
    "s_nop 2\n"
    "v_fma_f32 v40, v40, v220, v212\n"
    "v_fma_f32 v41, v41, v220, v213\n"
    "v_fma_f32 v42, v42, v220, v214\n"
    "v_fma_f32 v43, v43, v220, v215\n"
    "v_fma_f32 v60, v60, v220, v216\n"
    "v_fma_f32 v61, v61, v220, v217\n"
    "v_fma_f32 v62, v62, v220, v218\n"
    "v_fma_f32 v63, v63, v220, v219\n"
    "v_cvt_pk_bf16_f32 v40, v40, v41\n"
    "v_cvt_pk_bf16_f32 v41, v42, v43\n"
    "v_cvt_pk_bf16_f32 v42, v60, v61\n"
    "v_cvt_pk_bf16_f32 v43, v62, v63\n"
    "buffer_store_dwordx4 v[40:43], v239, s[16:19], s28 offen offset:64 sc0 sc1\n"
    "buffer_store_dword v254, off, s[8:11], s45 offset:0 sc1\n"
    "s_endpgm\n"
    ".p2align 6\n"
    ".long 0xbedf00ff\n"
    ".Lat_w14:\n"
    "s_setpc_b64 s[2:3]\n"
    ".Lat_k:\n"
    "s_mov_b32 s53, 0x400000\n"
    ".Lat_kl:\n"
    "buffer_load_dword v253, v238, s[8:11], s48 offen offset:0 sc1\n"
    "s_waitcnt vmcnt(0)\n"
    "v_subrev_u32_e32 v253, s39, v253\n"
    "v_subrev_u32_e32 v253, 0, v253\n"
    "v_cmp_le_u32_e32 vcc, 1, v253\n"
    "s_sub_u32 s53, s53, 1\n"
    "s_cbranch_scc1 .Lat_kr\n"
    "s_nop 2\n"
    "s_cbranch_vccnz .Lat_kl\n"
    "s_branch .Lat_kr\n"
    ".Lat_kx:\n"
    "s_branch .Lat_kxr\n"
    ".Lat_ppoll:\n"
    "s_mov_b32 s53, 0x400000\n"
    ".Lat_ppl:\n"
    "buffer_load_dword v253, v226, s[8:11], s61 offen offset:0 sc1\n"
    "s_waitcnt vmcnt(0)\n"
    "v_subrev_u32_e32 v253, s39, v253\n"
    "v_cmp_lt_u32_e32 vcc, 1, v253\n"
    "s_sub_u32 s53, s53, 1\n"
    "s_cbranch_scc1 .Lat_pok\n"
    "s_nop 2\n"
    "s_cbranch_vccnz .Lat_ppl\n"
    "s_branch .Lat_pok\n"
    ".Lat_wpoll:\n"
    "s_mov_b32 s53, 0x400000\n"
    ".Lat_wpoll_l:\n"
    "s_sleep 1\n"
    "buffer_load_dword v253, v255, s[8:11], s52 offen offset:0 sc1\n"
    "s_waitcnt vmcnt(0)\n"
    "v_cmp_ne_u32_e32 vcc, s39, v253\n"
    "s_sub_u32 s53, s53, 1\n"
    "s_cbranch_scc1 .Lat_wpoll_ret\n"
    "s_nop 2\n"
    "s_cbranch_vccnz .Lat_wpoll_l\n"
    "s_branch .Lat_wpoll_ret\n"
    ".Lat_glate:\n"
    "s_mov_b32 s53, 0x400000\n"
    ".Lat_glate_l:\n"
    "s_sleep 2\n"
    "buffer_load_dword v188, v189, s[8:11], s81 offen offset:0 sc1\n"
    "s_waitcnt vmcnt(0)\n"
    "v_cmp_ne_u32_e32 vcc, s39, v188\n"
    "s_sub_u32 s53, s53, 1\n"
    "s_cbranch_scc1 .Lat_glate_ret\n"
    "s_nop 2\n"
    "s_cbranch_vccnz .Lat_glate_l\n"
    "s_branch .Lat_glate_ret\n"
    ".Lat_lp0:\n"
    "s_mov_b32 s53, 0x400000\n"
    ".Lat_lp:\n"
    "s_sleep 1\n"
    "buffer_load_dword v253, off, s[8:11], s82 sc1\n"
    "s_waitcnt vmcnt(0)\n"
    "v_readfirstlane_b32 s81, v253\n"
    "s_sub_u32 s53, s53, 1\n"
    "s_cbranch_scc1 .Lat_lpx\n"
    "s_nop 3\n"
    "s_cmp_lg_u32 s81, s39\n"
    "s_cbranch_scc1 .Lat_lp\n"
    ".Lat_lpx:\n"
    "s_mov_b32 m0, s30\n"
    "s_nop 0\n"
    "buffer_load_dwordx4 v250, s[8:11], s83 offen offset:0 sc1 lds\n"
    "s_mov_b32 m0, s42\n"
    "s_branch .Lat_g1b\n"
    ".Lat_h1poll:\n"
    "s_mov_b32 s53, 0x400000\n"
    ".Lat_h1poll_l:\n"
    "s_sleep 1\n"
    "buffer_load_dword v80, v83, s[8:11], s59 offen offset:0 sc1\n"
    "s_waitcnt vmcnt(0)\n"
    "v_cmp_ne_u32_e32 vcc, s39, v80\n"
    "s_sub_u32 s53, s53, 1\n"
    "s_cbranch_scc1 .Lat_h1poll_ret\n"
    "s_nop 2\n"
    "s_cbranch_vccnz .Lat_h1poll_l\n"
    "s_branch .Lat_h1poll_ret\n"
    ".Lat_hpoll:\n"
    "s_mov_b32 s53, 0x400000\n"
    ".Lat_hpoll_l:\n"
    "s_sleep 1\n"
    "buffer_load_dword v253, v250, s[8:11], s59 offen offset:0 sc1\n"
    "buffer_load_dword v254, v252, s[8:11], s59 offen offset:0 sc1\n"
    "s_waitcnt vmcnt(0)\n"
    "v_xor_b32_e32 v254, s39, v254\n"
    "v_xor_b32_e32 v253, s39, v253\n"
    "v_or_b32_e32 v253, v253, v254\n"
    "v_cmp_ne_u32_e32 vcc, 0, v253\n"
    "s_sub_u32 s53, s53, 1\n"
    "s_cbranch_scc1 .Lat_hpoll_ret\n"
    "s_nop 2\n"
    "s_cbranch_vccnz .Lat_hpoll_l\n"
    "s_branch .Lat_hpoll_ret\n"
    ".Lat_redo:\n"
    "s_waitcnt vmcnt(0) lgkmcnt(0)\n"
    "s_barrier\n"
    "s_mov_b32 s49, 1\n"
    "s_mov_b32 s80, 0\n"
    "s_mov_b32 s82, 2.0\n"
    "v_lshrrev_b32_e32 v224, 4, v250\n"
    "s_and_b32 s50, s93, 1\n"
    "s_lshr_b32 s51, s93, 1\n"
    "v_lshrrev_b32_e32 v225, 3, v224\n"
    "v_and_b32_e32 v226, 6, v225\n"
    "v_and_b32_e32 v227, 7, v224\n"
    "v_xor_b32_e32 v226, v226, v227\n"
    "v_lshlrev_b32_e32 v226, 4, v226\n"
    "v_and_b32_e32 v227, 4, v225\n"
    "v_add_u32_e32 v227, v227, v225\n"
    "v_lshl_add_u32 v251, v227, 9, v226\n"
    "v_add_u32_e32 v252, 0x1c00, v251\n"
    "v_and_b32_e32 v225, 7, v224\n"
    "v_lshlrev_b32_e32 v227, 7, v225\n"
    "v_bfe_u32 v226, v224, 3, 1\n"
    "v_lshl_add_u32 v227, v226, 10, v227\n"
    "v_and_b32_e32 v225, 6, v224\n"
    "v_lshrrev_b32_e32 v226, 4, v224\n"
    "v_xor_b32_e32 v225, v225, v226\n"
    "v_lshl_add_u32 v240, v225, 4, v227\n"
    "v_lshrrev_b32_e32 v8, 4, v250\n"
    "v_and_b32_e32 v10, 0x70, v251\n"
    "v_bfe_u32 v9, v8, 3, 1\n"
    "v_lshl_add_u32 v248, v9, 9, v10\n"
    "v_lshrrev_b32_e32 v9, 4, v8\n"
    "v_lshl_add_u32 v248, v9, 12, v248\n"
    "s_mul_i32 s43, s50, 3\n"
    "s_xor_b32 s43, s43, s51\n"
    "s_lshl_b32 s43, s43, 15\n"
    "s_add_u32 s43, s43, 32768\n"
    "s_mul_i32 s42, s93, 5120\n"
    "s_lshl_b32 s45, s50, 8\n"
    "s_mul_i32 s40, s32, 1277952\n"
    "s_lshl_b32 s41, s51, 14\n"
    "s_add_u32 s40, s40, s41\n"
    "s_add_u32 s40, s40, s45\n"
    "s_add_u32 s41, s40, 128\n"
    "v_bfe_u32 v9, v8, 2, 3\n"
    "v_lshlrev_b32_e32 v244, 7, v9\n"
    "v_lshrrev_b32_e32 v10, 5, v8\n"
    "v_lshl_add_u32 v244, v10, 10, v244\n"
    "v_and_b32_e32 v9, 6, v9\n"
    "v_bfe_u32 v10, v8, 1, 1\n"
    "v_xor_b32_e32 v9, v9, v10\n"
    "v_lshl_add_u32 v244, v9, 4, v244\n"
    "v_and_b32_e32 v10, 1, v8\n"
    "v_lshl_add_u32 v244, v10, 3, v244\n"
    "s_lshl_b32 s45, s51, 16\n"
    "s_add_u32 s45, s45, 32768\n"
    "v_add_u32_e32 v244, s45, v244\n"
    "v_xor_b32_e32 v245, 32, v244\n"
    "v_xor_b32_e32 v246, 64, v244\n"
    "v_xor_b32_e32 v247, 0x60, v244\n"
    "s_lshl_b32 s45, s51, 15\n"
    "s_add_u32 s48, s45, 32768\n"
    "s_sub_u32 s45, 131072, s45\n"
    "v_add_u32_e32 v242, s45, v240\n"
    "v_add_u32_e32 v240, s48, v240\n"
    "v_xor_b32_e32 v241, 64, v240\n"
    "v_xor_b32_e32 v243, 64, v242\n"
    "s_xor_b32 s45, s93, 2\n"
    "s_mul_i32 s45, s45, 5120\n"
    "v_lshl_add_u32 v249, v8, 2, s45\n"
    "s_lshl_b32 s45, s93, 11\n"
    "v_add_u32_e32 v99, s45, v250\n"
    "v_add_u32_e32 v98, 11345920, v99\n"
    "v_and_b32_e32 v252, 0x30, v250\n"
    "v_add_u32_e32 v252, 0x400, v252\n"
    "s_mul_i32 s59, s32, 999\n"
    "s_andn2_b32 s59, s59, 63\n"
    "s_sub_u32 s48, 3968, s59\n"
    "v_cmp_le_u32_e32 vcc, 0x80, v250\n"
    "v_mov_b32_e32 v80, s48\n"
    "s_add_u32 s59, s59, 6076416\n"
    "v_cndmask_b32_e32 v83, v250, v80, vcc\n"
    "v_bfe_u32 v253, v250, 4, 2\n"
    "v_max_u32_e32 v253, 1, v253\n"
    "v_add_u32_e32 v253, s32, v253\n"
    "v_and_b32_e32 v253, 3, v253\n"
    "v_lshlrev_b32_e32 v253, 9, v253\n"
    "v_mov_b32_e32 v84, s39\n"
    "v_lshrrev_b32_e32 v86, 2, v250\n"
    "v_min_u32_e32 v86, 124, v86\n"
    "s_lshl2_add_u32 s58, s34, s93\n"
    "s_lshl_b32 s58, s58, 7\n"
    "s_add_u32 s58, s58, 5910528\n"
    "s_lshl_b32 s45, s50, 13\n"
    "v_add_u32_e32 v85, s45, v250\n"
    "s_lshl3_add_u32 s45, s45, 49152\n"
    "v_add_u32_e32 v255, s45, v250\n"
    "v_lshrrev_b32_e32 v105, 7, v250\n"
    "v_and_b32_e32 v104, 0x70, v251\n"
    "v_lshl_add_u32 v104, v105, 9, v104\n"
    "v_add_u32_e32 v105, 0xc00, v104\n"
    "v_add_u32_e32 v106, 0x1800, v104\n"
    "v_add_u32_e32 v107, 0x2400, v104\n"
    "s_lshl_b32 s57, s93, 7\n"
    "s_lshl4_add_u32 s56, s57, 24576\n"
    "s_add_u32 s57, s57, 5111808\n"
    "s_mul_i32 s45, s51, 32768\n"
    "s_add_u32 s45, s45, 8192\n"
    "v_subrev_u32_e32 v46, s45, v240\n"
    "v_subrev_u32_e32 v47, s45, v241\n"
    "s_mul_i32 s45, s51, 61440\n"
    "s_add_u32 s45, s45, 8192\n"
    "v_subrev_u32_e32 v4, s45, v244\n"
    "v_subrev_u32_e32 v5, s45, v245\n"
    "v_subrev_u32_e32 v6, s45, v246\n"
    "v_subrev_u32_e32 v7, s45, v247\n"
    "v_accvgpr_write_b32 a244, 0\n"
    "v_accvgpr_write_b32 a245, 0\n"
    "v_accvgpr_write_b32 a246, 0\n"
    "v_accvgpr_write_b32 a247, 0\n"
    "v_accvgpr_write_b32 a248, 0\n"
    "v_accvgpr_write_b32 a249, 0\n"
    "v_accvgpr_write_b32 a250, 0\n"
    "v_accvgpr_write_b32 a251, 0\n"
    "v_accvgpr_write_b32 a252, 0\n"
    "v_accvgpr_write_b32 a253, 0\n"
    "v_accvgpr_write_b32 a254, 0\n"
    "v_accvgpr_write_b32 a255, 0\n"
    "v_mov_b64_e32 v[8:9], 0\n"
    "v_mov_b64_e32 v[10:11], 0\n"
    "v_mov_b64_e32 v[12:13], 0\n"
    "v_mov_b64_e32 v[14:15], 0\n"
    "v_mov_b64_e32 v[16:17], 0\n"
    "v_mov_b64_e32 v[18:19], 0\n"
    "v_mov_b64_e32 v[28:29], 0\n"
    "v_mov_b64_e32 v[30:31], 0\n"
    "v_mov_b64_e32 v[32:33], 0\n"
    "v_mov_b64_e32 v[34:35], 0\n"
    "v_mov_b64_e32 v[36:37], 0\n"
    "v_mov_b64_e32 v[38:39], 0\n"
    "v_mov_b64_e32 v[48:49], 0\n"
    "v_mov_b64_e32 v[50:51], 0\n"
    "v_mov_b64_e32 v[52:53], 0\n"
    "v_mov_b64_e32 v[54:55], 0\n"
    "v_mov_b64_e32 v[56:57], 0\n"
    "v_mov_b64_e32 v[58:59], 0\n"
    "v_mov_b64_e32 v[68:69], 0\n"
    "v_mov_b64_e32 v[70:71], 0\n"
    "v_mov_b64_e32 v[72:73], 0\n"
    "v_mov_b64_e32 v[74:75], 0\n"
    "v_mov_b64_e32 v[76:77], 0\n"
    "v_mov_b64_e32 v[78:79], 0\n"
    "v_mov_b64_e32 v[88:89], 0\n"
    "v_mov_b64_e32 v[90:91], 0\n"
    "v_mov_b64_e32 v[92:93], 0\n"
    "v_mov_b64_e32 v[94:95], 0\n"
    "v_mov_b64_e32 v[96:97], 0\n"
    "v_mov_b64_e32 v[98:99], 0\n"
    "v_mov_b64_e32 v[108:109], 0\n"
    "v_mov_b64_e32 v[110:111], 0\n"
    "v_mov_b64_e32 v[112:113], 0\n"
    "v_mov_b64_e32 v[114:115], 0\n"
    "v_mov_b64_e32 v[116:117], 0\n"
    "v_mov_b64_e32 v[118:119], 0\n"
    "v_mov_b64_e32 v[128:129], 0\n"
    "v_mov_b64_e32 v[130:131], 0\n"
    "v_mov_b64_e32 v[132:133], 0\n"
    "v_mov_b64_e32 v[134:135], 0\n"
    "v_mov_b64_e32 v[136:137], 0\n"
    "v_mov_b64_e32 v[138:139], 0\n"
    "s_mov_b32 m0, s56\n"
    "s_nop 0\n"
    "buffer_load_dwordx4 v104, s[8:11], s57 offen offset:0 sc1 lds\n"
    "buffer_load_dwordx4 v105, s[8:11], s57 offen offset:1024 sc1 lds\n"
    "s_add_u32 m0, s43, 0\n"
    "s_nop 0\n"
    "buffer_load_dwordx4 v104, s[8:11], s40 offen offset:0 sc1 lds\n"
    "buffer_load_dwordx4 v105, s[8:11], s40 offen offset:1024 sc1 lds\n"
    "buffer_load_dwordx4 v106, s[8:11], s40 offen offset:2048 sc1 lds\n"
    "buffer_load_dwordx4 v107, s[8:11], s40 offen offset:3072 sc1 lds\n"
    "s_add_u32 s40, s40, 0x8000\n"
    "s_add_u32 m0, s43, 4096\n"
    "s_nop 0\n"
    "buffer_load_dwordx4 v104, s[8:11], s41 offen offset:0 sc1 lds\n"
    "buffer_load_dwordx4 v105, s[8:11], s41 offen offset:1024 sc1 lds\n"
    "buffer_load_dwordx4 v106, s[8:11], s41 offen offset:2048 sc1 lds\n"
    "buffer_load_dwordx4 v107, s[8:11], s41 offen offset:3072 sc1 lds\n"
    "s_add_u32 s41, s41, 0x8000\n"
    "s_add_u32 m0, s43, 8192\n"
    "s_nop 0\n"
    "buffer_load_dwordx4 v104, s[8:11], s40 offen offset:0 sc1 lds\n"
    "buffer_load_dwordx4 v105, s[8:11], s40 offen offset:1024 sc1 lds\n"
    "buffer_load_dwordx4 v106, s[8:11], s40 offen offset:2048 sc1 lds\n"
    "buffer_load_dwordx4 v107, s[8:11], s40 offen offset:3072 sc1 lds\n"
    "s_add_u32 s40, s40, 0x8000\n"
    "s_add_u32 m0, s43, 12288\n"
    "s_nop 0\n"
    "buffer_load_dwordx4 v104, s[8:11], s41 offen offset:0 sc1 lds\n"
    "buffer_load_dwordx4 v105, s[8:11], s41 offen offset:1024 sc1 lds\n"
    "buffer_load_dwordx4 v106, s[8:11], s41 offen offset:2048 sc1 lds\n"
    "buffer_load_dwordx4 v107, s[8:11], s41 offen offset:3072 sc1 lds\n"
    "s_add_u32 s41, s41, 0x8000\n"
    "s_waitcnt vmcnt(0)\n"
    "s_branch .Lat_p2\n"
    ".Lat_maxpass:\n"
    "s_waitcnt lgkmcnt(0)\n"
    "s_mul_i32 s48, s33, 81920\n"
    "s_add_u32 s48, s48, 11345920\n"
    "s_add_u32 s45, s32, 0\n"
    "s_and_b32 s45, s45, 3\n"
    "s_lshl2_add_u32 s45, s50, s45\n"
    "s_lshl_b32 s45, s45, 13\n"
    "s_add_u32 s45, s45, s48\n"
    "s_add_u32 s46, s45, 4096\n"
    "buffer_load_dwordx4 a[0:3], v250, s[8:11], s45 offen offset:0 sc1\n"
    "buffer_load_dwordx4 a[4:7], v250, s[8:11], s45 offen offset:1024 sc1\n"
    "buffer_load_dwordx4 a[8:11], v250, s[8:11], s45 offen offset:2048 sc1\n"
    "buffer_load_dwordx4 a[12:15], v250, s[8:11], s45 offen offset:3072 sc1\n"
    "buffer_load_dwordx4 a[16:19], v250, s[8:11], s46 offen offset:0 sc1\n"
    "buffer_load_dwordx4 a[20:23], v250, s[8:11], s46 offen offset:1024 sc1\n"
    "buffer_load_dwordx4 a[24:27], v250, s[8:11], s46 offen offset:2048 sc1\n"
    "buffer_load_dwordx4 a[28:31], v250, s[8:11], s46 offen offset:3072 sc1\n"
    "s_and_b32 s45, s32, 1\n"
    "s_xor_b32 s45, s45, s50\n"
    "s_add_u32 s45, s45, 8\n"
    "s_lshl_b32 s45, s45, 13\n"
    "s_add_u32 s45, s45, s48\n"
    "s_add_u32 s46, s45, 4096\n"
    "buffer_load_dwordx4 a[32:35], v250, s[8:11], s45 offen offset:0 sc1\n"
    "buffer_load_dwordx4 a[36:39], v250, s[8:11], s45 offen offset:1024 sc1\n"
    "buffer_load_dwordx4 a[40:43], v250, s[8:11], s45 offen offset:2048 sc1\n"
    "buffer_load_dwordx4 a[44:47], v250, s[8:11], s45 offen offset:3072 sc1\n"
    "buffer_load_dwordx4 a[48:51], v250, s[8:11], s46 offen offset:0 sc1\n"
    "buffer_load_dwordx4 a[52:55], v250, s[8:11], s46 offen offset:1024 sc1\n"
    "buffer_load_dwordx4 a[56:59], v250, s[8:11], s46 offen offset:2048 sc1\n"
    "buffer_load_dwordx4 a[60:63], v250, s[8:11], s46 offen offset:3072 sc1\n"
    "s_add_u32 s45, s32, 1\n"
    "s_and_b32 s45, s45, 3\n"
    "s_lshl2_add_u32 s45, s50, s45\n"
    "s_lshl_b32 s45, s45, 13\n"
    "s_add_u32 s45, s45, s48\n"
    "s_add_u32 s46, s45, 4096\n"
    "buffer_load_dwordx4 a[64:67], v250, s[8:11], s45 offen offset:0 sc1\n"
    "buffer_load_dwordx4 a[68:71], v250, s[8:11], s45 offen offset:1024 sc1\n"
    "buffer_load_dwordx4 a[72:75], v250, s[8:11], s45 offen offset:2048 sc1\n"
    "buffer_load_dwordx4 a[76:79], v250, s[8:11], s45 offen offset:3072 sc1\n"
    "buffer_load_dwordx4 a[80:83], v250, s[8:11], s46 offen offset:0 sc1\n"
    "buffer_load_dwordx4 a[84:87], v250, s[8:11], s46 offen offset:1024 sc1\n"
    "buffer_load_dwordx4 a[88:91], v250, s[8:11], s46 offen offset:2048 sc1\n"
    "buffer_load_dwordx4 a[92:95], v250, s[8:11], s46 offen offset:3072 sc1\n"
    "s_add_u32 s45, s32, 2\n"
    "s_and_b32 s45, s45, 3\n"
    "s_lshl2_add_u32 s45, s50, s45\n"
    "s_lshl_b32 s45, s45, 13\n"
    "s_add_u32 s45, s45, s48\n"
    "s_add_u32 s46, s45, 4096\n"
    "buffer_load_dwordx4 a[96:99], v250, s[8:11], s45 offen offset:0 sc1\n"
    "buffer_load_dwordx4 a[100:103], v250, s[8:11], s45 offen offset:1024 sc1\n"
    "buffer_load_dwordx4 a[104:107], v250, s[8:11], s45 offen offset:2048 sc1\n"
    "buffer_load_dwordx4 a[108:111], v250, s[8:11], s45 offen offset:3072 sc1\n"
    "buffer_load_dwordx4 a[112:115], v250, s[8:11], s46 offen offset:0 sc1\n"
    "buffer_load_dwordx4 a[116:119], v250, s[8:11], s46 offen offset:1024 sc1\n"
    "buffer_load_dwordx4 a[120:123], v250, s[8:11], s46 offen offset:2048 sc1\n"
    "buffer_load_dwordx4 a[124:127], v250, s[8:11], s46 offen offset:3072 sc1\n"
    "s_add_u32 s45, s32, 3\n"
    "s_and_b32 s45, s45, 3\n"
    "s_lshl2_add_u32 s45, s50, s45\n"
    "s_lshl_b32 s45, s45, 13\n"
    "s_add_u32 s45, s45, s48\n"
    "s_add_u32 s46, s45, 4096\n"
    "buffer_load_dwordx4 a[128:131], v250, s[8:11], s45 offen offset:0 sc1\n"
    "buffer_load_dwordx4 a[132:135], v250, s[8:11], s45 offen offset:1024 sc1\n"
    "buffer_load_dwordx4 a[136:139], v250, s[8:11], s45 offen offset:2048 sc1\n"
    "buffer_load_dwordx4 a[140:143], v250, s[8:11], s45 offen offset:3072 sc1\n"
    "buffer_load_dwordx4 a[144:147], v250, s[8:11], s46 offen offset:0 sc1\n"
    "buffer_load_dwordx4 a[148:151], v250, s[8:11], s46 offen offset:1024 sc1\n"
    "buffer_load_dwordx4 a[152:155], v250, s[8:11], s46 offen offset:2048 sc1\n"
    "buffer_load_dwordx4 a[156:159], v250, s[8:11], s46 offen offset:3072 sc1\n"
    "s_waitcnt vmcnt(0)\n"
    "v_mov_b32_e32 v200, 0xff800000\n"
    "v_mov_b32_e32 v201, 0xff800000\n"
    "v_mov_b32_e32 v202, 0xff800000\n"
    "v_mov_b32_e32 v203, 0xff800000\n"
    "v_mov_b32_e32 v204, 0xff800000\n"
    "s_lshl_b32 s45, s51, 14\n"
    "s_lshl_b32 s46, s50, 8\n"
    "s_add_u32 s45, s45, s46\n"
    "s_add_u32 s61, s46, 5103616\n"
    "s_movk_i32 s48, 157\n"
    ".Lat_mp_loop:\n"
    "s_min_u32 s45, s45, s61\n"
    "s_add_u32 s46, s45, 128\n"
    "s_add_u32 m0, s43, 16384\n"
    "s_nop 0\n"
    "buffer_load_dwordx4 v248, s[8:11], s45 offen offset:0 sc1 lds\n"
    "buffer_load_dwordx4 v248, s[8:11], s45 offen offset:1024 sc1 lds\n"
    "buffer_load_dwordx4 v248, s[8:11], s45 offen offset:2048 sc1 lds\n"
    "buffer_load_dwordx4 v248, s[8:11], s45 offen offset:3072 sc1 lds\n"
    "s_add_u32 m0, s43, 20480\n"
    "s_nop 0\n"
    "buffer_load_dwordx4 v248, s[8:11], s46 offen offset:0 sc1 lds\n"
    "buffer_load_dwordx4 v248, s[8:11], s46 offen offset:1024 sc1 lds\n"
    "buffer_load_dwordx4 v248, s[8:11], s46 offen offset:2048 sc1 lds\n"
    "buffer_load_dwordx4 v248, s[8:11], s46 offen offset:3072 sc1 lds\n"
    "s_waitcnt vmcnt(0)\n"
    "s_barrier\n"
    "ds_read_b128 a[160:163], v240 offset:16384\n"
    "ds_read_b128 a[164:167], v241 offset:16384\n"
    "ds_read_b128 a[168:171], v240 offset:20480\n"
    "ds_read_b128 a[172:175], v241 offset:20480\n"
    "ds_read_b128 a[176:179], v242 offset:16384\n"
    "ds_read_b128 a[180:183], v243 offset:16384\n"
    "ds_read_b128 a[184:187], v242 offset:20480\n"
    "ds_read_b128 a[188:191], v243 offset:20480\n"
    "s_waitcnt lgkmcnt(0)\n"
    "v_mfma_f32_16x16x32_bf16 v[180:183], a[160:163], a[0:3], 0\n"
    "v_mfma_f32_16x16x32_bf16 v[184:187], a[160:163], a[32:35], 0\n"
    "v_mfma_f32_16x16x32_bf16 v[188:191], a[160:163], a[64:67], 0\n"
    "v_mfma_f32_16x16x32_bf16 v[192:195], a[160:163], a[96:99], 0\n"
    "v_mfma_f32_16x16x32_bf16 v[196:199], a[160:163], a[128:131], 0\n"
    "v_mfma_f32_16x16x32_bf16 v[180:183], a[164:167], a[4:7], v[180:183]\n"
    "v_mfma_f32_16x16x32_bf16 v[184:187], a[164:167], a[36:39], v[184:187]\n"
    "v_mfma_f32_16x16x32_bf16 v[188:191], a[164:167], a[68:71], v[188:191]\n"
    "v_mfma_f32_16x16x32_bf16 v[192:195], a[164:167], a[100:103], v[192:195]\n"
    "v_mfma_f32_16x16x32_bf16 v[196:199], a[164:167], a[132:135], v[196:199]\n"
    "v_mfma_f32_16x16x32_bf16 v[180:183], a[168:171], a[8:11], v[180:183]\n"
    "v_mfma_f32_16x16x32_bf16 v[184:187], a[168:171], a[40:43], v[184:187]\n"
    "v_mfma_f32_16x16x32_bf16 v[188:191], a[168:171], a[72:75], v[188:191]\n"
    "v_mfma_f32_16x16x32_bf16 v[192:195], a[168:171], a[104:107], v[192:195]\n"
    "v_mfma_f32_16x16x32_bf16 v[196:199], a[168:171], a[136:139], v[196:199]\n"
    "v_mfma_f32_16x16x32_bf16 v[180:183], a[172:175], a[12:15], v[180:183]\n"
    "v_mfma_f32_16x16x32_bf16 v[184:187], a[172:175], a[44:47], v[184:187]\n"
    "v_mfma_f32_16x16x32_bf16 v[188:191], a[172:175], a[76:79], v[188:191]\n"
    "v_mfma_f32_16x16x32_bf16 v[192:195], a[172:175], a[108:111], v[192:195]\n"
    "v_mfma_f32_16x16x32_bf16 v[196:199], a[172:175], a[140:143], v[196:199]\n"
    "v_mfma_f32_16x16x32_bf16 v[180:183], a[176:179], a[16:19], v[180:183]\n"
    "v_mfma_f32_16x16x32_bf16 v[184:187], a[176:179], a[48:51], v[184:187]\n"
    "v_mfma_f32_16x16x32_bf16 v[188:191], a[176:179], a[80:83], v[188:191]\n"
    "v_mfma_f32_16x16x32_bf16 v[192:195], a[176:179], a[112:115], v[192:195]\n"
    "v_mfma_f32_16x16x32_bf16 v[196:199], a[176:179], a[144:147], v[196:199]\n"
    "v_mfma_f32_16x16x32_bf16 v[180:183], a[180:183], a[20:23], v[180:183]\n"
    "v_mfma_f32_16x16x32_bf16 v[184:187], a[180:183], a[52:55], v[184:187]\n"
    "v_mfma_f32_16x16x32_bf16 v[188:191], a[180:183], a[84:87], v[188:191]\n"
    "v_mfma_f32_16x16x32_bf16 v[192:195], a[180:183], a[116:119], v[192:195]\n"
    "v_mfma_f32_16x16x32_bf16 v[196:199], a[180:183], a[148:151], v[196:199]\n"
    "v_mfma_f32_16x16x32_bf16 v[180:183], a[184:187], a[24:27], v[180:183]\n"
    "v_mfma_f32_16x16x32_bf16 v[184:187], a[184:187], a[56:59], v[184:187]\n"
    "v_mfma_f32_16x16x32_bf16 v[188:191], a[184:187], a[88:91], v[188:191]\n"
    "v_mfma_f32_16x16x32_bf16 v[192:195], a[184:187], a[120:123], v[192:195]\n"
    "v_mfma_f32_16x16x32_bf16 v[196:199], a[184:187], a[152:155], v[196:199]\n"
    "v_mfma_f32_16x16x32_bf16 v[180:183], a[188:191], a[28:31], v[180:183]\n"
    "v_mfma_f32_16x16x32_bf16 v[184:187], a[188:191], a[60:63], v[184:187]\n"
    "v_mfma_f32_16x16x32_bf16 v[188:191], a[188:191], a[92:95], v[188:191]\n"
    "v_mfma_f32_16x16x32_bf16 v[192:195], a[188:191], a[124:127], v[192:195]\n"
    "v_mfma_f32_16x16x32_bf16 v[196:199], a[188:191], a[156:159], v[196:199]\n"
    "s_nop 15\n"
    "v_max3_f32 v200, v200, v180, v181\n"
    "v_max3_f32 v200, v200, v182, v183\n"
    "v_max3_f32 v201, v201, v184, v185\n"
    "v_max3_f32 v201, v201, v186, v187\n"
    "v_max3_f32 v202, v202, v188, v189\n"
    "v_max3_f32 v202, v202, v190, v191\n"
    "v_max3_f32 v203, v203, v192, v193\n"
    "v_max3_f32 v203, v203, v194, v195\n"
    "v_max3_f32 v204, v204, v196, v197\n"
    "v_max3_f32 v204, v204, v198, v199\n"
    "s_nop 3\n"
    "ds_read_b128 a[160:163], v240 offset:18432\n"
    "ds_read_b128 a[164:167], v241 offset:18432\n"
    "ds_read_b128 a[168:171], v240 offset:22528\n"
    "ds_read_b128 a[172:175], v241 offset:22528\n"
    "ds_read_b128 a[176:179], v242 offset:18432\n"
    "ds_read_b128 a[180:183], v243 offset:18432\n"
    "ds_read_b128 a[184:187], v242 offset:22528\n"
    "ds_read_b128 a[188:191], v243 offset:22528\n"
    "s_waitcnt lgkmcnt(0)\n"
    "v_mfma_f32_16x16x32_bf16 v[180:183], a[160:163], a[0:3], 0\n"
    "v_mfma_f32_16x16x32_bf16 v[184:187], a[160:163], a[32:35], 0\n"
    "v_mfma_f32_16x16x32_bf16 v[188:191], a[160:163], a[64:67], 0\n"
    "v_mfma_f32_16x16x32_bf16 v[192:195], a[160:163], a[96:99], 0\n"
    "v_mfma_f32_16x16x32_bf16 v[196:199], a[160:163], a[128:131], 0\n"
    "v_mfma_f32_16x16x32_bf16 v[180:183], a[164:167], a[4:7], v[180:183]\n"
    "v_mfma_f32_16x16x32_bf16 v[184:187], a[164:167], a[36:39], v[184:187]\n"
    "v_mfma_f32_16x16x32_bf16 v[188:191], a[164:167], a[68:71], v[188:191]\n"
    "v_mfma_f32_16x16x32_bf16 v[192:195], a[164:167], a[100:103], v[192:195]\n"
    "v_mfma_f32_16x16x32_bf16 v[196:199], a[164:167], a[132:135], v[196:199]\n"
    "v_mfma_f32_16x16x32_bf16 v[180:183], a[168:171], a[8:11], v[180:183]\n"
    "v_mfma_f32_16x16x32_bf16 v[184:187], a[168:171], a[40:43], v[184:187]\n"
    "v_mfma_f32_16x16x32_bf16 v[188:191], a[168:171], a[72:75], v[188:191]\n"
    "v_mfma_f32_16x16x32_bf16 v[192:195], a[168:171], a[104:107], v[192:195]\n"
    "v_mfma_f32_16x16x32_bf16 v[196:199], a[168:171], a[136:139], v[196:199]\n"
    "v_mfma_f32_16x16x32_bf16 v[180:183], a[172:175], a[12:15], v[180:183]\n"
    "v_mfma_f32_16x16x32_bf16 v[184:187], a[172:175], a[44:47], v[184:187]\n"
    "v_mfma_f32_16x16x32_bf16 v[188:191], a[172:175], a[76:79], v[188:191]\n"
    "v_mfma_f32_16x16x32_bf16 v[192:195], a[172:175], a[108:111], v[192:195]\n"
    "v_mfma_f32_16x16x32_bf16 v[196:199], a[172:175], a[140:143], v[196:199]\n"
    "v_mfma_f32_16x16x32_bf16 v[180:183], a[176:179], a[16:19], v[180:183]\n"
    "v_mfma_f32_16x16x32_bf16 v[184:187], a[176:179], a[48:51], v[184:187]\n"
    "v_mfma_f32_16x16x32_bf16 v[188:191], a[176:179], a[80:83], v[188:191]\n"
    "v_mfma_f32_16x16x32_bf16 v[192:195], a[176:179], a[112:115], v[192:195]\n"
    "v_mfma_f32_16x16x32_bf16 v[196:199], a[176:179], a[144:147], v[196:199]\n"
    "v_mfma_f32_16x16x32_bf16 v[180:183], a[180:183], a[20:23], v[180:183]\n"
    "v_mfma_f32_16x16x32_bf16 v[184:187], a[180:183], a[52:55], v[184:187]\n"
    "v_mfma_f32_16x16x32_bf16 v[188:191], a[180:183], a[84:87], v[188:191]\n"
    "v_mfma_f32_16x16x32_bf16 v[192:195], a[180:183], a[116:119], v[192:195]\n"
    "v_mfma_f32_16x16x32_bf16 v[196:199], a[180:183], a[148:151], v[196:199]\n"
    "v_mfma_f32_16x16x32_bf16 v[180:183], a[184:187], a[24:27], v[180:183]\n"
    "v_mfma_f32_16x16x32_bf16 v[184:187], a[184:187], a[56:59], v[184:187]\n"
    "v_mfma_f32_16x16x32_bf16 v[188:191], a[184:187], a[88:91], v[188:191]\n"
    "v_mfma_f32_16x16x32_bf16 v[192:195], a[184:187], a[120:123], v[192:195]\n"
    "v_mfma_f32_16x16x32_bf16 v[196:199], a[184:187], a[152:155], v[196:199]\n"
    "v_mfma_f32_16x16x32_bf16 v[180:183], a[188:191], a[28:31], v[180:183]\n"
    "v_mfma_f32_16x16x32_bf16 v[184:187], a[188:191], a[60:63], v[184:187]\n"
    "v_mfma_f32_16x16x32_bf16 v[188:191], a[188:191], a[92:95], v[188:191]\n"
    "v_mfma_f32_16x16x32_bf16 v[192:195], a[188:191], a[124:127], v[192:195]\n"
    "v_mfma_f32_16x16x32_bf16 v[196:199], a[188:191], a[156:159], v[196:199]\n"
    "s_nop 15\n"
    "v_max3_f32 v200, v200, v180, v181\n"
    "v_max3_f32 v200, v200, v182, v183\n"
    "v_max3_f32 v201, v201, v184, v185\n"
    "v_max3_f32 v201, v201, v186, v187\n"
    "v_max3_f32 v202, v202, v188, v189\n"
    "v_max3_f32 v202, v202, v190, v191\n"
    "v_max3_f32 v203, v203, v192, v193\n"
    "v_max3_f32 v203, v203, v194, v195\n"
    "v_max3_f32 v204, v204, v196, v197\n"
    "v_max3_f32 v204, v204, v198, v199\n"
    "s_nop 3\n"
    "s_barrier\n"
    "s_add_u32 s45, s45, 0x8000\n"
    "s_sub_u32 s48, s48, 1\n"
    "s_cmp_lg_u32 s48, 0\n"
    "s_cbranch_scc1 .Lat_mp_loop\n"
    "s_mov_b32 m0, s42\n"
    "s_nop 1\n"
    "ds_write_addtid_b32 v200 offset:0\n"
    "ds_write_addtid_b32 v201 offset:256\n"
    "ds_write_addtid_b32 v202 offset:512\n"
    "ds_write_addtid_b32 v203 offset:768\n"
    "ds_write_addtid_b32 v204 offset:1024\n"
    "s_waitcnt lgkmcnt(0)\n"
    "s_barrier\n"
    "ds_read_b32 v180, v249 offset:0\n"
    "ds_read_b32 v184, v249 offset:256\n"
    "ds_read_b32 v188, v249 offset:512\n"
    "ds_read_b32 v192, v249 offset:768\n"
    "ds_read_b32 v196, v249 offset:1024\n"
    "s_waitcnt lgkmcnt(0)\n"
    "v_max_f32_e32 v205, v200, v180\n"
    "v_mov_b32_e32 v206, v205\n"
    "s_nop 1\n"
    "v_permlane16_swap_b32_e32 v205, v206\n"
    "s_nop 0\n"
    "v_max_f32_e32 v205, v205, v206\n"
    "v_mov_b32_e32 v206, v205\n"
    "s_nop 1\n"
    "v_permlane32_swap_b32_e32 v205, v206\n"
    "s_nop 0\n"
    "v_max_f32_e32 v205, v205, v206\n"
    "v_add_f32_e32 v205, 0x42540000, v205\n"
    "v_xor_b32_e32 v220, 0x80000000, v205\n"
    "v_xor_b32_e32 v221, 0x80000000, v205\n"
    "v_xor_b32_e32 v222, 0x80000000, v205\n"
    "v_xor_b32_e32 v223, 0x80000000, v205\n"
    "v_max_f32_e32 v205, v201, v184\n"
    "v_mov_b32_e32 v206, v205\n"
    "s_nop 1\n"
    "v_permlane16_swap_b32_e32 v205, v206\n"
    "s_nop 0\n"
    "v_max_f32_e32 v205, v205, v206\n"
    "v_mov_b32_e32 v206, v205\n"
    "s_nop 1\n"
    "v_permlane32_swap_b32_e32 v205, v206\n"
    "s_nop 0\n"
    "v_max_f32_e32 v205, v205, v206\n"
    "v_add_f32_e32 v205, 0x42540000, v205\n"
    "v_xor_b32_e32 v224, 0x80000000, v205\n"
    "v_xor_b32_e32 v225, 0x80000000, v205\n"
    "v_xor_b32_e32 v226, 0x80000000, v205\n"
    "v_xor_b32_e32 v227, 0x80000000, v205\n"
    "v_max_f32_e32 v205, v202, v188\n"
    "v_mov_b32_e32 v206, v205\n"
    "s_nop 1\n"
    "v_permlane16_swap_b32_e32 v205, v206\n"
    "s_nop 0\n"
    "v_max_f32_e32 v205, v205, v206\n"
    "v_mov_b32_e32 v206, v205\n"
    "s_nop 1\n"
    "v_permlane32_swap_b32_e32 v205, v206\n"
    "s_nop 0\n"
    "v_max_f32_e32 v205, v205, v206\n"
    "v_add_f32_e32 v205, 0x42540000, v205\n"
    "v_xor_b32_e32 v228, 0x80000000, v205\n"
    "v_xor_b32_e32 v229, 0x80000000, v205\n"
    "v_xor_b32_e32 v230, 0x80000000, v205\n"
    "v_xor_b32_e32 v231, 0x80000000, v205\n"
    "v_max_f32_e32 v205, v203, v192\n"
    "v_mov_b32_e32 v206, v205\n"
    "s_nop 1\n"
    "v_permlane16_swap_b32_e32 v205, v206\n"
    "s_nop 0\n"
    "v_max_f32_e32 v205, v205, v206\n"
    "v_mov_b32_e32 v206, v205\n"
    "s_nop 1\n"
    "v_permlane32_swap_b32_e32 v205, v206\n"
    "s_nop 0\n"
    "v_max_f32_e32 v205, v205, v206\n"
    "v_add_f32_e32 v205, 0x42540000, v205\n"
    "v_xor_b32_e32 v232, 0x80000000, v205\n"
    "v_xor_b32_e32 v233, 0x80000000, v205\n"
    "v_xor_b32_e32 v234, 0x80000000, v205\n"
    "v_xor_b32_e32 v235, 0x80000000, v205\n"
    "v_max_f32_e32 v205, v204, v196\n"
    "v_mov_b32_e32 v206, v205\n"
    "s_nop 1\n"
    "v_permlane16_swap_b32_e32 v205, v206\n"
    "s_nop 0\n"
    "v_max_f32_e32 v205, v205, v206\n"
    "v_mov_b32_e32 v206, v205\n"
    "s_nop 1\n"
    "v_permlane32_swap_b32_e32 v205, v206\n"
    "s_nop 0\n"
    "v_max_f32_e32 v205, v205, v206\n"
    "v_add_f32_e32 v205, 0x42540000, v205\n"
    "v_xor_b32_e32 v236, 0x80000000, v205\n"
    "v_xor_b32_e32 v237, 0x80000000, v205\n"
    "v_xor_b32_e32 v238, 0x80000000, v205\n"
    "v_xor_b32_e32 v239, 0x80000000, v205\n"
    "s_nop 1\n"
    "s_barrier\n"
    "s_mul_i32 s48, s33, 81920\n"
    "s_add_u32 s48, s48, 11345920\n"
    "s_lshl_b32 s46, s93, 11\n"
    "s_add_u32 s48, s48, s46\n"
    "s_add_u32 s46, s46, 49152\n"
    "s_and_b32 s45, s32, 1\n"
    "s_xor_b32 s45, s45, 8\n"
    "s_lshl_b32 s45, s45, 13\n"
    "s_add_u32 s45, s45, s48\n"
    "s_add_u32 m0, s46, 0\n"
    "s_nop 0\n"
    "buffer_load_dwordx4 v250, s[8:11], s45 offen offset:0 sc1 lds\n"
    "buffer_load_dwordx4 v250, s[8:11], s45 offen offset:1024 sc1 lds\n"
    "s_branch .Lat_maxpass_ret\n"
    ".p2align 12\n"
    ".Lat_out:\n"
    "s_waitcnt lgkmcnt(0)\n"
    "s_mov_b64 s[2:3], s[0:1]\n"
    "s_mov_b64 %0, s[2:3]\n"
    "s_mov_b32 %1, s92\n"
    "s_mov_b32 %2, s93\n"
    : "=s"(ka), "=s"(b), "=s"(wid) : "0"(__builtin_amdgcn_kernarg_segment_ptr()), "1"(blockIdx.x)
    : "v0", "v1", "v2", "v3", "v4", "v5", "v6", "v7", "v8", "v9", "v10", "v11", "v12", "v13", "v14", "v15", "v16", "v17", "v18", "v19", "v20", "v21", "v22", "v23", "v24", "v25", "v26", "v27", "v28", "v29", "v30", "v31", "v32", "v33", "v34", "v35", "v36", "v37", "v38", "v39", "v40", "v41", "v42", "v43", "v44", "v45", "v46", "v47", "v48", "v49", "v50", "v51", "v52", "v53", "v54", "v55", "v56", "v57", "v58", "v59", "v60", "v61", "v62", "v63", "v64", "v65", "v66", "v67", "v68", "v69", "v70", "v71", "v72", "v73", "v74", "v75", "v76", "v77", "v78", "v79", "v80", "v81", "v82", "v83", "v84", "v85", "v86", "v87", "v88", "v89", "v90", "v91", "v92", "v93", "v94", "v95", "v96", "v97", "v98", "v99", "v100", "v101", "v102", "v103", "v104", "v105", "v106", "v107", "v108", "v109", "v110", "v111", "v112", "v113", "v114", "v115", "v116", "v117", "v118", "v119", "v120", "v121", "v122", "v123", "v124", "v125", "v126", "v127", "v128", "v129", "v130", "v131", "v132", "v133", "v134", "v135", "v136", "v137", "v138", "v139", "v140", "v141", "v142", "v143", "v144", "v145", "v146", "v147", "v148", "v149", "v150", "v151", "v152", "v153", "v154", "v155", "v156", "v157", "v158", "v159", "v160", "v161", "v162", "v163", "v164", "v165", "v166", "v167", "v168", "v169", "v170", "v171", "v172", "v173", "v174", "v175", "v176", "v177", "v178", "v179", "v180", "v181", "v182", "v183", "v184", "v185", "v186", "v187", "v188", "v189", "v190", "v191", "v192", "v193", "v194", "v195", "v196", "v197", "v198", "v199", "v200", "v201", "v202", "v203", "v204", "v205", "v206", "v207", "v208", "v209", "v210", "v211", "v212", "v213", "v214", "v215", "v216", "v217", "v218", "v219", "v220", "v221", "v222", "v223", "v224", "v225", "v226", "v227", "v228", "v229", "v230", "v231", "v232", "v233", "v234", "v235", "v236", "v237", "v238", "v239", "v240", "v241", "v242", "v243", "v244", "v245", "v246", "v247", "v248", "v249", "v250", "v251", "v252", "v253", "v254", "v255", "a0", "a1", "a2", "a3", "a4", "a5", "a6", "a7", "a8", "a9", "a10", "a11", "a12", "a13", "a14", "a15", "a16", "a17", "a18", "a19", "a20", "a21", "a22", "a23", "a24", "a25", "a26", "a27", "a28", "a29", "a30", "a31", "a32", "a33", "a34", "a35", "a36", "a37", "a38", "a39", "a40", "a41", "a42", "a43", "a44", "a45", "a46", "a47", "a48", "a49", "a50", "a51", "a52", "a53", "a54", "a55", "a56", "a57", "a58", "a59", "a60", "a61", "a62", "a63", "a64", "a65", "a66", "a67", "a68", "a69", "a70", "a71", "a72", "a73", "a74", "a75", "a76", "a77", "a78", "a79", "a80", "a81", "a82", "a83", "a84", "a85", "a86", "a87", "a88", "a89", "a90", "a91", "a92", "a93", "a94", "a95", "a96", "a97", "a98", "a99", "a100", "a101", "a102", "a103", "a104", "a105", "a106", "a107", "a108", "a109", "a110", "a111", "a112", "a113", "a114", "a115", "a116", "a117", "a118", "a119", "a120", "a121", "a122", "a123", "a124", "a125", "a126", "a127", "a128", "a129", "a130", "a131", "a132", "a133", "a134", "a135", "a136", "a137", "a138", "a139", "a140", "a141", "a142", "a143", "a144", "a145", "a146", "a147", "a148", "a149", "a150", "a151", "a152", "a153", "a154", "a155", "a156", "a157", "a158", "a159", "a160", "a161", "a162", "a163", "a164", "a165", "a166", "a167", "a168", "a169", "a170", "a171", "a172", "a173", "a174", "a175", "a176", "a177", "a178", "a179", "a180", "a181", "a182", "a183", "a184", "a185", "a186", "a187", "a188", "a189", "a190", "a191", "a192", "a193", "a194", "a195", "a196", "a197", "a198", "a199", "a200", "a201", "a202", "a203", "a204", "a205", "a206", "a207", "a208", "a209", "a210", "a211", "a212", "a213", "a214", "a215", "a216", "a217", "a218", "a219", "a220", "a221", "a222", "a223", "a224", "a225", "a226", "a227", "a228", "a229", "a230", "a231", "a232", "a233", "a234", "a235", "a236", "a237", "a238", "a239", "a240", "a241", "a242", "a243", "a244", "a245", "a246", "a247", "a248", "a249", "a250", "a251", "a252", "a253", "a254", "a255", "s2", "s3", "s4", "s5", "s6", "s7", "s8", "s9", "s10", "s11", "s12", "s13", "s14", "s15", "s16", "s17", "s18", "s19", "s20", "s21", "s22", "s23", "s24", "s25", "s26", "s27", "s28", "s29", "s30", "s31", "s32", "s33", "s34", "s35", "s36", "s37", "s38", "s39", "s40", "s41", "s42", "s43", "s44", "s45", "s46", "s47", "s48", "s49", "s50", "s51", "s52", "s53", "s54", "s55", "s56", "s57", "s58", "s59", "s60", "s61", "s62", "s63", "s80", "s81", "s82", "s83", "s84", "s85", "s86", "s87", "s88", "s89", "s90", "s91", "s92", "s93", "s94", "s95", "vcc", "m0", "scc", "memory");
  typedef __attribute__((address_space(1))) void* gp;
  helper(lds, b >> 1 & 3, wid, (const int*)(gp)ka[2], (const int*)(gp)ka[4], (const u16*)(gp)ka[8], (const u16*)(gp)ka[9], (const u16*)(gp)ka[10], (char*)(gp)ka[3]);
}

extern "C" void kernel_launch(void* const* d_in, const int* in_sizes, int n_in, void* d_out, int out_size, void* d_ws,
                              size_t ws_size, hipStream_t stream) {
  const u16* x = (const u16*)d_in[0];
  const int* src = (const int*)d_in[1];
  const int* dst = (const int*)d_in[2];
  const u16* deg_inv = (const u16*)d_in[3];
  const u16* wmsg = (const u16*)d_in[4];
  const u16* wq = (const u16*)d_in[5];
  const u16* wk = (const u16*)d_in[6];
  const u16* wv = (const u16*)d_in[7];
  const u16* wout = (const u16*)d_in[8];
  char* ws = (char*)d_ws;
  hipFuncSetAttribute((const void*)gps_kernel, hipFuncAttributeMaxDynamicSharedMemorySize, 163840);
  gps_kernel<<<256, 256, 163840, stream>>>(wq, wk, src, ws, dst, x, (u16*)d_out, deg_inv, wv, wmsg, wout);
}
```

```cpp
#include <hip/hip_runtime.h>
#include <hip/hip_bf16.h>
#include <stdint.h>

typedef unsigned short u16;
typedef __attribute__((ext_vector_type(8))) short bf16x8;
typedef __attribute__((ext_vector_type(4))) short bf16x4;
typedef __attribute__((ext_vector_type(4))) float f32x4;
typedef __attribute__((ext_vector_type(2))) float f32x2;
typedef __attribute__((ext_vector_type(4))) unsigned u32x4;
typedef __attribute__((ext_vector_type(2))) unsigned u32x2;
#define DI __device__ __forceinline__

constexpr int NN = 10000, DD = 256, EE = 160000;
constexpr int NATT = 252, NSP = 4, SPN = 2560;
constexpr int GU = 5, GNB = 5; static_assert(GU * GNB <= 30, "list size");

constexpr unsigned WS_H = 0;
constexpr unsigned WS_AF = WS_H + 10240 * 512;
constexpr unsigned WS_WV = WS_AF + 135168;
constexpr unsigned WS_WM = WS_WV + 135168;
constexpr unsigned WS_XF = WS_WM + 135168;
constexpr unsigned WS_GF = WS_XF + 3328 * 128;
constexpr unsigned WS_HF = WS_GF + 2048;
constexpr unsigned WS_PF = WS_HF + 4096;
constexpr unsigned WS_NF = WS_PF + 4096;
constexpr unsigned WS_WF = WS_NF + 16384;
constexpr unsigned WS_LF = WS_WF + 256;
constexpr unsigned WS_EP = WS_LF + 512;
constexpr unsigned WS_G = WS_EP + 256 + 1024;
constexpr unsigned WS_QP = WS_G + 10240 * 512;
constexpr unsigned WS_XB = WS_QP + 10240 * 512;
constexpr unsigned WS_LS = WS_XB + 2 * NATT * 101376;
constexpr unsigned WS_BL = WS_LS + 524288;
constexpr unsigned WS_DBG = WS_BL + 64 * 260 * 64;
constexpr unsigned WS_END = WS_DBG + 1024 * 128 + 8192;

DI float bf2f(unsigned x) { return __uint_as_float(x << 16); }
DI float bfhi(unsigned x) { return __uint_as_float(x & 0xffff0000u); }
DI unsigned pk2(float lo, float hi) {
  typedef __attribute__((ext_vector_type(2))) __bf16 bf2_t;
  const bf2_t r = __builtin_convertvector((f32x2){lo, hi}, bf2_t);
  return __builtin_bit_cast(unsigned, r);
}
typedef __attribute__((address_space(3))) bf16x4* lds_bf16x4_p;
DI bf16x4 tr_read(unsigned p) { return __builtin_amdgcn_ds_read_tr16_b64_v4i16((lds_bf16x4_p)p); }
DI bf16x8 as_bf(u32x4 v) { return __builtin_bit_cast(bf16x8, v); }
#define SB() __builtin_amdgcn_sched_barrier(0)
#define MFMA16(a, b, c) __builtin_amdgcn_mfma_f32_16x16x32_bf16(a, b, c, 0, 0, 0)

typedef __amdgpu_buffer_rsrc_t srd_t;
DI void drain() { asm volatile("s_waitcnt vmcnt(0)" ::: "memory"); }
DI void st_flag(srd_t w, unsigned off, unsigned tag) { __builtin_amdgcn_raw_buffer_store_b32(tag, w, off, 0, 16); }
DI void wait_flag(srd_t w, unsigned off, unsigned tag) {
#pragma unroll 1
  for (int it = 0; it < (1 << 22); ++it) {
    unsigned v;
    asm volatile("buffer_load_dword %0, %1, %2, 0 offen sc1\n s_waitcnt vmcnt(0)" : "=v"(v) : "v"(off), "s"(w) : "memory");
    if (__ballot(v != tag) == 0) break;
    __builtin_amdgcn_s_sleep(2);
    asm volatile("" ::: "memory");
  }
  asm volatile("" ::: "memory");
}

DI void prod_store(const f32x4 (&acc)[2][2], int mat, int ks, int J, int lane, srd_t wsrd) {
#pragma unroll
  for (int s = 0; s < 2; ++s) {
    u32x4 o = {pk2(acc[0][s][0], acc[0][s][1]), pk2(acc[0][s][2], acc[0][s][3]), pk2(acc[1][s][0], acc[1][s][1]), pk2(acc[1][s][2], acc[1][s][3])};
    if (ks == 7 && lane >= 48) o[3] &= 0xffffu;
    const int T = 2 * J + s;
    const int at = mat * 135168 + ks * 4096 + (T & 3) * 1024;
    __builtin_amdgcn_raw_buffer_store_b128(o, wsrd, WS_WV + (T >> 2) * 33792 + at + lane * 16, 0, 16);
  }
}
DI void corr(unsigned* dl, const unsigned (&lr)[16]) {
  float a0 = 0.f, a1 = 0.f;
#pragma unroll
  for (int r = 0; r < 16; ++r) { a0 += bf2f(lr[r] & 0xffffu); a1 += bfhi(lr[r]); }
  dl[0] = pk2(a0 * -0.0625f, a1 * -0.0625f);
  dl[128] = pk2((bf2f(lr[15] & 0xffffu) - 0.25f * a0) * (1.f / 3.f), (bfhi(lr[15]) - 0.25f * a1) * (1.f / 3.f));
}
DI void product_load(char* lds, int wid, int lane, int job, const u16* __restrict__ wv, const u16* __restrict__ wm, const u16* __restrict__ wo) {
  const unsigned ldsb = (unsigned)(size_t)lds;
  const int r = lane >> 3, sw = ((lane & 7) ^ (r & 6)) * 16 + wid * 128;
  const unsigned vp = (8 * (r >> 2) + (r & 3)) * 512 + sw, vn = r * 512 + sw;
  auto image = [&](int n, const u16* w, int row0, bool nat) {
#pragma unroll
    for (int i = 0; i < 8; ++i) {
      const int a = i & 3;
      asm volatile("s_mov_b32 m0, %2\n s_nop 0\n global_load_lds_dwordx4 %0, %1"
                   :: "v"(nat ? vn : vp), "s"(w + (row0 + (i >> 2) * 32 + (nat ? 8 * a : 16 * (a & 1) + 4 * (a >> 1))) * DD),
                      "s"(ldsb + n * 32768 + (i >> 2) * 16384 + wid * 4096 + a * 1024) : "m0", "memory");
    }
  };
  image(2, wv, (job >> 2) * 64, true);
  image(3, wm, (job >> 2) * 64, false);
#pragma unroll
  for (int k = 0; k < 8; ++k)
    asm volatile("s_mov_b32 m0, %2\n s_nop 0\n global_load_lds_dwordx4 %0, %1"
                 :: "v"((lane >> 3) * 512 + (lane & 7) * 16), "s"(wo + (k * 32 + wid * 8) * DD + (job & 3) * 64), "s"(ldsb + k * 4096 + wid * 1024) : "m0", "memory");
}
DI void product_w(char* lds, int wid, int lane, int job, srd_t wsrd) {
  const int m = lane & 15, g = lane >> 4;
  const int ks = 2 * (job >> 2) + (wid >> 1), J = 2 * (job & 3) + (wid & 1);
  const unsigned ldsb = (unsigned)(size_t)lds;
  unsigned* dl = reinterpret_cast<unsigned*>(lds + 0x20000);
  const bool hi = (m >> 3) != 0;
  const unsigned ra = (m >> 3) * 1024 + (m & 7) * 128 + (g ^ (m & 6)) * 16;
  auto frag = [&](int n, int tl, int t, int k) {
    return *reinterpret_cast<const bf16x8*>(lds + n * 32768 + tl * 16384 + (k >> 1) * 4096 + t * 2048 + (ra ^ ((k & 1) * 64)));
  };
  f32x4 av[2][2] = {}, am[2][2] = {}, ad[2] = {};
  const unsigned tb = ldsb + (8 * g + ((lane >> 2) & 3)) * 128 + (wid & 1) * 64 + (lane & 3) * 16;
  bf16x8 d0[8], d1[8], d2[8], rr[8][2], vf[8][2], mf[8][2];
#pragma unroll
  for (int kk = 0; kk < 9; ++kk) {
    if (kk < 8) {
      d0[kk] = *reinterpret_cast<const bf16x8*>(dl + 512 + 16 * kk + 4 * g); d1[kk] = *reinterpret_cast<const bf16x8*>(dl + 512 + (ks == 7 ? 128 : 0) + 16 * kk + 4 * g);
      d2[kk] = *reinterpret_cast<const bf16x8*>(dl + 768 + (ks == 7 && hi ? 128 : 0) + 16 * kk + 4 * g);
#pragma unroll
      for (int s = 0; s < 2; ++s) {
        rr[kk][s] = __builtin_shufflevector(tr_read(tb + kk * 4096 + s * 8), tr_read(tb + kk * 4096 + s * 8 + 512), 0, 1, 2, 3, 4, 5, 6, 7);
        vf[kk][s] = frag(2, wid >> 1, s, kk); mf[kk][s] = frag(3, wid >> 1, s, kk);
      }
    }
    SB();
    if (kk) {
      const int k = kk - 1;
#pragma unroll
      for (int s = 0; s < 2; ++s) {
        av[0][s] = MFMA16(d0[k], rr[k][s], av[0][s]); av[1][s] = MFMA16(d1[k], rr[k][s], av[1][s]); ad[s] = MFMA16(d2[k], rr[k][s], ad[s]);
#pragma unroll
        for (int t = 0; t < 2; ++t) { av[t][s] = MFMA16(vf[k][t], rr[k][s], av[t][s]); am[t][s] = MFMA16(mf[k][t], rr[k][s], am[t][s]); }
      }
    }
    SB();
  }
#pragma unroll
  for (int t = 0; t < 2; ++t)
#pragma unroll
    for (int s = 0; s < 2; ++s) am[t][s] += ad[s];
  prod_store(av, 0, ks, J, lane, wsrd);
  prod_store(am, 1, ks, J, lane, wsrd);
}

DI void helper(char* lds, int j, int wid, const int* __restrict__ src, const int* __restrict__ dst,
               const u16* __restrict__ wv, const u16* __restrict__ wm, const u16* __restrict__ wo, char* ws) {
  unsigned* img = reinterpret_cast<unsigned*>(lds);
  char* cntb = lds + 0x20000;
  int* cnt = reinterpret_cast<int*>(cntb);
  unsigned* ub = reinterpret_cast<unsigned*>(lds + 0x22c00);
  int* red = reinterpret_cast<int*>(ub + 256);
  u16* dg = reinterpret_cast<u16*>(lds + 0x23400);
  u16* tmp = reinterpret_cast<u16*>(lds + 0x25000) + wid * 512;
  int lane; asm volatile("v_mbcnt_lo_u32_b32 %0, -1, 0\n\tv_mbcnt_hi_u32_b32 %0, -1, %0" : "=v"(lane));
  const int tid = wid * 64 + lane, lo = j * SPN, nn_ = min(SPN, NN - lo);
  const srd_t wsrd = __builtin_amdgcn_make_buffer_rsrc(ws, 0, WS_END, 0x00020000);
  const unsigned tag = __builtin_amdgcn_raw_buffer_load_b32(wsrd, WS_EP, 0, 0) + 4u;
  auto touch = [&](unsigned at) { asm volatile("s_mov_b32 m0, 0x27c00\n s_nop 0\n buffer_load_dword %0, %1, 0 offen sc1 lds" :: "v"(at), "s"(wsrd) : "m0", "memory"); };
  touch(lane < 16 ? min(WS_GF + (lane * 16 + wid * 4 + j) * 128, WS_EP) : 0x40000000u);
  for (int i = tid; i < 8192; i += 256) { const unsigned f = (i & 63) >= 60 ? 0u : ~0u; reinterpret_cast<u32x4*>(img)[i] = (u32x4){f, f, f, f}; }
  for (int i = tid; i < SPN + 64; i += 256) cnt[i] = 0;
  ub[tid] = 0u;
  if (tid < 8) red[tid] = 0;
  touch(WS_XF + 262144 + (j * 256 + tid) * 128);
  wait_flag(wsrd, WS_NF + j * 4096 + min(tid, 249) * 16, tag);
  __syncthreads();
  const unsigned dum = (SPN + lane) << 18;
  auto fetch = [&](int k) {
    return __builtin_amdgcn_raw_buffer_load_b128(wsrd, WS_BL + (k < 48 ? tid * 16 + (k & 7) * 4096 : (tid >> 2) * 128 + (tid & 3) * 16 + (k & 3) * 8192 + (k & 4) * 16), (j * 8 + (k >> 3)) * 33280, 16);
  };
  auto clean = [&](u32x4& e, int k) {
#pragma unroll
    for (int r = 0; r < 4; ++r) e[r] = (k < 48 ? (k & 7) == 7 && tid >= 208 : (k & 3) == 3 && tid >= 232) ? dum : min(e[r], dum);
  };
  auto slot = [&](unsigned e) { return reinterpret_cast<int*>(cntb + (e >> 16)); };
  constexpr int NL = 52;
  u32x4 ent[NL];
#pragma unroll
  for (int k = 0; k < NL; ++k) ent[k] = fetch(k);
  SB();
#pragma unroll
  for (int k = 0; k < NL; ++k) {
    clean(ent[k], k);
    int* p[4];
#pragma unroll
    for (int r = 0; r < 4; ++r) p[r] = slot(ent[k][r]);
    SB();
#pragma unroll
    for (int r = 0; r < 4; ++r) atomicAdd(p[r], 2);
    SB();
  }
  unsigned use = 0;
  const unsigned dum3 = (tid & 3) == 3 ? dum : 0;
#pragma unroll
  for (int k = 0; k < 4; ++k) use |= __ballot(ent[NL - 4 + k][3] < dum3) ? 1u << k : 0u;
  if (use)
#pragma unroll 1
    for (int k = NL; k < 64; ++k)
      if (use >> (k - NL) & 1) {
        u32x4 e = fetch(k); clean(e, k);
#pragma unroll
        for (int r = 0; r < 4; ++r) atomicAdd(slot(e[r]), 2);
        if (__ballot(e[3] < dum3)) { use |= 16u << (k - NL); if (k >= 60) red[7] = 1; }
      }
  __syncthreads();
  int inc = 0, nu[10], cn[10], cm = 0;
  auto units = [&](int c) { return max(1, (c + 6) >> 3); };
#pragma unroll
  for (int i = 0; i < 10; i += 2) { const u32x2 v = *reinterpret_cast<u32x2*>(cnt + 10 * tid + i); cn[i] = v[0]; cn[i + 1] = v[1]; }
  const int nup = units(cnt[max(10 * tid - 1, 0)]);
  const bool act = 10 * tid < nn_;
#pragma unroll
  for (int i = 0; i < 10; ++i) { cm = max(cm, cn[i]); nu[i] = act ? units(cn[i]) : 0; inc += nu[i]; }
  const int own = inc;
  inc += __builtin_amdgcn_update_dpp(0, inc, 0x111, 15, 15, true); inc += __builtin_amdgcn_update_dpp(0, inc, 0x112, 15, 15, true);
  inc += __builtin_amdgcn_update_dpp(0, inc, 0x114, 15, 15, true); inc += __builtin_amdgcn_update_dpp(0, inc, 0x118, 15, 15, true);
  inc += __builtin_amdgcn_update_dpp(0, inc, 0x142, 10, 15, false); inc += __builtin_amdgcn_update_dpp(0, inc, 0x143, 12, 15, false);
  if (lane == 63) red[wid] = inc;
  const bool srt = __ballot(cm > 64);
  __syncthreads();
  const u32x4 rd = *reinterpret_cast<u32x4*>(red);
  const float rs_ = 64.f / (float)(rd[0] + rd[1] + rd[2] + rd[3]);
  inc += (wid > 0 ? rd[0] : 0) + (wid > 1 ? rd[1] : 0) + (wid > 2 ? rd[2] : 0) - own;
  int wv_[10];
  {
    int o = inc, wp = tid ? min(127, (int)((float)(2 * o - nup) * rs_)) : 128;
#pragma unroll
    for (int i = 0; i < 10; ++i) {
      wv_[i] = min(127, (int)((float)(2 * o + nu[i]) * rs_));
      ub[act && wv_[i] != wp ? wv_[i] : 128 + lane] = (unsigned)o << 12 | (unsigned)(10 * tid + i);
      wp = wv_[i]; o += nu[i];
    }
  }
  __syncthreads();
  {
    int u0[10], top = 0;
#pragma unroll
    for (int i = 0; i < 10; ++i) u0[i] = ub[wv_[i]] >> 12;
#pragma unroll
    for (int i = 0; i < 10; ++i) {
      const int lu = inc - u0[i], e = lu + nu[i] - 1 + GU, q = min(e / (4 * GU), GNB);
      u0[i] = wv_[i] * 1024 + lu * 8; top = max(top, q);
      atomicOr(img + wv_[i] * 256 + 244 + q, act ? 1u << (e - q * 4 * GU) : 0u);
      inc += nu[i];
    }
#pragma unroll
    for (int i = 0; i < 10; i += 2) *reinterpret_cast<u32x2*>(cnt + 10 * tid + i) = (u32x2){(unsigned)u0[i], (unsigned)u0[i + 1]};
    if (tid < 64) cnt[SPN + tid] = 0x30000;
    if (act && top == GNB) red[7] = 1;
  }
  __syncthreads();
  const bool big_ = red[7];
  if (tid < 128) {
    unsigned nb = 0;
#pragma unroll
    for (int q = 0; q < GNB; ++q) nb = img[tid * 256 + 244 + q] ? q + 1 : nb;
    *reinterpret_cast<u32x4*>(img + tid * 256 + 240) = (u32x4){big_ ? 0u : nb, WS_G + (lo + (ub[tid] & 4095u)) * 512, big_ || (tid & 3) ? 0x40000000u : WS_GF + j * 512 + tid * 4, 0u};
  }
  if (!big_) {
    const unsigned dmp = 0x27000 + lane * 4;
    typedef __attribute__((address_space(3))) u16* lds_u16_p;
    unsigned p[2][8];
#pragma unroll
    for (int k = 0; k < NL + 2; k += 2) {
      if (k < NL) {
        int* q[8];
#pragma unroll
        for (int r = 0; r < 8; ++r) q[r] = slot(ent[k + (r >> 2)][r & 3]);
        SB();
#pragma unroll
        for (int r = 0; r < 8; ++r) p[k >> 1 & 1][r] = atomicAdd(q[r], 2);
        SB();
      }
      if (k) {
#pragma unroll
        for (int r = 0; r < 8; ++r) p[~k >> 1 & 1][r] = min(p[~k >> 1 & 1][r], dmp);
        SB();
#pragma unroll
        for (int r = 0; r < 8; ++r) *(lds_u16_p)p[~k >> 1 & 1][r] = ent[k - 2 + (r >> 2)][r & 3];
        SB();
      }
    }
    if (use)
#pragma unroll 1
      for (int k = NL; k < 64; ++k)
        if (use >> (k - NL) & 1) {
          u32x4 e = fetch(k); clean(e, k);
#pragma unroll
          for (int r = 0; r < 4; ++r) *(lds_u16_p)min((unsigned)atomicAdd(slot(e[r]), 2), dmp) = e[r];
        }
    __syncthreads();
    if (srt) {
#pragma unroll
      for (int i = 0; i < 10; ++i) dg[10 * tid + i] = cn[i] >> 1;
      for (unsigned long long big = __ballot(cm > 64); big; big &= big - 1)
      for (int n = 10 * (wid * 64 + __builtin_ctzll(big)), n1 = n + 10; n < n1; ++n) {
        const int d = __builtin_amdgcn_readfirstlane(dg[n]);
        if (d <= 32) continue;
        u16* f = reinterpret_cast<u16*>(lds + cnt[n]) - d;
        for (int i = lane; i < d + 8; i += 64) tmp[i] = i < d ? f[i] : 0xffff;
        __builtin_amdgcn_wave_barrier();
        for (int i = lane; i < d; i += 64) {
          const unsigned key = tmp[i] << 9 | i; int p_ = 0;
          for (int q = 0; q < d; q += 8) {
            const u32x4 v = *reinterpret_cast<u32x4*>(tmp + q);
#pragma unroll
            for (int e = 0; e < 4; ++e) p_ += (((v[e] & 0xffffu) << 9 | (q + 2 * e)) < key) + (((v[e] >> 16) << 9 | (q + 2 * e + 1)) < key);
          }
          f[p_] = key >> 9;
        }
        __builtin_amdgcn_wave_barrier();
      }
    }
  }
  __syncthreads();
#pragma unroll 8
  for (int k = 0; k < 32; ++k)
    __builtin_amdgcn_raw_buffer_store_b128(reinterpret_cast<u32x4*>(img)[k * 256 + tid], wsrd, WS_LS + j * 131072 + (k * 256 + tid) * 16, 0, 16);
  unsigned lr[16];
#pragma unroll
  for (int r = 0; r < 16; ++r) lr[r] = reinterpret_cast<const unsigned*>((wid >= 2 ? wm : wv) + (240 + r) * DD)[tid & 127];
  wait_flag(wsrd, WS_HF + min(tid, NATT - 1) * 16, tag);
  drain();
  __syncthreads();
  if (tid == 0) st_flag(wsrd, WS_LF + j * 128, tag);
  touch(WS_XF + (j * 256 + tid) * 256);
  touch(wid ? 0x40000000u : WS_GF - 32768 + (j * 64 + lane) * 128);
  corr(reinterpret_cast<unsigned*>(lds + 0x20000) + 512 + (wid >> 1) * 256 + (tid & 127), lr);
#pragma unroll 1
  for (int job = 4 * j; job < 4 * j + 4; ++job) {
    product_load(lds, wid, lane, job, wv, wm, wo);
    drain();
    __syncthreads();
    product_w(lds, wid, lane, job, wsrd);
    __syncthreads();
  }
  drain();
  __syncthreads();
  if (tid == 0) st_flag(wsrd, WS_WF + j * 4, tag);
  if (big_) {
    const srd_t msrd = __builtin_amdgcn_make_buffer_rsrc(ws + WS_H, 0, 10240 * 512, 0x00020000);
    bf16x8 b1;
#pragma unroll
    for (int e = 0; e < 8; ++e) b1[e] = (lane & 15) == e + ((lane >> 1) & 8) ? 0x3f80 : 0;
    for (int n = wid; n < nn_; n += 4) {
      f32x4 acc = {};
      for (int e0 = 0; e0 < EE; e0 += 64) {
        unsigned long long m = __ballot(dst[e0 + lane] == lo + n);
        while (m) {
          const int e = e0 + __builtin_ctzll(m); m &= m - 1;
          const unsigned vo = lane < 32 ? (unsigned)src[e] * 512 + lane * 16 : 0x7fffff00u;
          acc = MFMA16(b1, as_bf(__builtin_amdgcn_raw_buffer_load_b128(msrd, vo, 0, 16)), acc);
        }
      }
      __builtin_amdgcn_raw_buffer_store_b64((u32x2){pk2(acc[0], acc[1]), pk2(acc[2], acc[3])}, wsrd,
                                            WS_G + (lo + n) * 512 + (lane >> 5) * 256 + (lane & 15) * 16 + (lane & 16) / 2, 0, 16);
    }
    drain();
    __syncthreads();
    if (tid < 32) st_flag(wsrd, WS_GF + j * 512 + tid * 16, tag);
  }
}

constexpr unsigned CODE_BYTES = 77000;
extern "C" __global__ __launch_bounds__(256, 1) void gps_kernel(const u16* wq, const u16* wk, const int* src, char* ws, const int* dst, const u16* x, u16* out,
                                                                const u16* deg_inv, const u16* wv, const u16* wm, const u16* wo) {
  extern __shared__ __attribute__((aligned(16))) char lds[];
  static_assert(WS_END == 69410816 && WS_EP == 6101760, "workspace layout of the generated code");
  typedef void* const __attribute__((address_space(4)))* arg_p;
  arg_p ka; int b, wid;
  asm volatile(
    ".Lat_0:\n"
    "s_mov_b32 s92, s2\n"
    "s_load_dwordx2 s[2:3], s[0:1], 0x0\n"
    "s_load_dwordx4 s[4:7], s[0:1], 0x8\n"
    "s_load_dwordx8 s[8:15], s[0:1], 0x18\n"
    "s_load_dwordx2 s[28:29], s[0:1], 0x38\n"
    "s_getpc_b64 s[30:31]\n"
    "v_readfirstlane_b32 s93, v0\n"
    "s_bfe_u32 s45, s92, 0x40003\n"
    "v_lshlrev_b32_e32 v1, 5, v0\n"
    "s_lshl_b32 s45, s45, 13\n"
    "s_mov_b32 m0, 0x27f00\n"
    "v_add_u32_e32 v1, s45, v1\n"
    "v_min_u32_e32 v1, 76488, v1\n"
    "global_load_lds_dword v1, s[30:31]\n"
    "s_and_b32 s45, s92, 249\n"
    "s_lshr_b32 s93, s93, 6\n"
    "s_cmp_eq_u32 s45, 249\n"
    "s_cbranch_scc1 .Lat_out\n"
    "v_mbcnt_lo_u32_b32 v24, -1, 0\n"
    "v_mbcnt_hi_u32_b32 v24, -1, v24\n"
    "s_and_b32 s50, s93, 1\n"
    "s_lshr_b32 s51, s93, 1\n"
    "v_lshrrev_b32_e32 v25, 3, v24\n"
    "v_and_b32_e32 v26, 6, v25\n"
    "v_and_b32_e32 v27, 7, v24\n"
    "v_xor_b32_e32 v26, v26, v27\n"
    "v_lshlrev_b32_e32 v26, 4, v26\n"
    "v_and_b32_e32 v27, 4, v25\n"
    "v_add_u32_e32 v27, v27, v25\n"
    "v_lshl_add_u32 v251, v27, 9, v26\n"
    "v_add_u32_e32 v252, 0x1c00, v251\n"
    "s_mov_b32 s22, 0x20000\n"
    "s_mov_b32 s23, 0x20000\n"
    "s_mov_b32 s26, 0x20000\n"
    "s_mov_b32 s27, 0x20000\n"
    "s_lshr_b32 s36, s92, 7\n"
    "s_mul_i32 s36, s36, 3\n"
    "s_add_u32 s36, s36, s92\n"
    "s_and_b32 s36, s36, 127\n"
    "s_lshl_b32 s45, s93, 7\n"
    "s_lshl_b32 s46, s93, 12\n"
    "s_and_b32 s47, s36, 7\n"
    "s_lshl_b32 s47, s47, 14\n"
    "s_mov_b32 m0, s46\n"
    "s_add_u32 s47, s47, s45\n"
    "s_and_b32 s34, s92, 7\n"
    "s_lshl_b32 s34, s34, 5\n"
    "s_lshr_b32 s32, s92, 3\n"
    "s_add_u32 s34, s34, s32\n"
    "s_lshl_b32 s32, s34, 6\n"
    "s_or_b32 s32, s32, 63\n"
    "s_and_b32 s32, s32, 255\n"
    "s_cmp_ge_u32 s34, 252\n"
    "s_cselect_b32 s34, s32, s34\n"
    "s_and_b32 s32, s34, 3\n"
    "s_lshr_b32 s33, s34, 2\n"
    "s_or_b32 s38, s33, 16\n"
    "s_cmp_eq_u32 s38, 62\n"
    "s_cselect_b32 s38, 16, 0\n"
    "s_xor_b32 s33, s33, s38\n"
    "s_lshl2_add_u32 s34, s33, s32\n"
    "s_mul_i32 s38, s33, 81920\n"
    "s_lshl_b32 s35, s32, 13\n"
    "s_and_b32 s55, s35, 0x2000\n"
    "s_add_u32 s35, s35, s38\n"
    "s_add_u32 s55, s55, s38\n"
    "s_mov_b32 s39, 6101760\n"
    "s_waitcnt lgkmcnt(0)\n"
    "s_mov_b64 s[20:21], s[2:3]\n"
    "s_mov_b64 s[24:25], s[4:5]\n"
    "buffer_load_dwordx4 v251, s[20:23], s47 offen offset:0 lds\n"
    "s_add_u32 s48, s47, 1024\n"
    "buffer_load_dwordx4 v252, s[20:23], s47 offen offset:3072 lds\n"
    "s_add_u32 s47, s47, 0x1800\n"
    "buffer_load_dwordx4 v251, s[20:23], s48 offen offset:1024 lds\n"
    "buffer_load_dwordx4 v251, s[20:23], s47 offen offset:2048 lds\n"
    "s_lshr_b32 s47, s36, 4\n"
    "s_lshl_b32 s47, s47, 14\n"
    "s_and_b32 s48, s36, 8\n"
    "s_lshl_b32 s48, s48, 8\n"
    "s_add_u32 s47, s47, s48\n"
    "s_add_u32 m0, s46, 0x4000\n"
    "s_add_u32 s47, s47, s45\n"
    "buffer_load_dwordx4 v251, s[24:27], s47 offen offset:0 lds\n"
    "buffer_load_dwordx4 v252, s[24:27], s47 offen offset:1024 lds\n"
    "s_add_u32 s47, s45, 120832\n"
    "buffer_load_dwordx4 v251, s[24:27], s47 offen offset:2048 lds\n"
    "s_add_u32 s47, s47, 1024\n"
    "buffer_load_dwordx4 v251, s[24:27], s47 offen offset:3072 lds\n"
    "s_mov_b64 s[16:17], s[14:15]\n"
    "s_mov_b64 s[86:87], s[10:11]\n"
    "s_mov_b32 s14, 5120000\n"
    "s_mov_b32 s15, 0x20000\n"
    "v_mov_b32_e32 v102, s39\n"
    "global_load_dword v102, v102, s[8:9]\n"
    "s_lshl_b32 s45, s93, 2\n"
    "s_add_u32 s54, s35, 0x8000\n"
    "s_add_u32 s55, s55, 0x10000\n"
    "v_lshlrev_b32_e32 v250, 4, v24\n"
    "v_lshrrev_b32_e32 v28, 4, v24\n"
    "v_add_u32_e32 v28, s45, v28\n"
    "v_and_b32_e32 v104, 0xf0, v250\n"
    "v_lshl_add_u32 v104, v28, 9, v104\n"
    "buffer_load_dwordx4 v[0:3], v104, s[12:15], s35 offen offset:0\n"
    "buffer_load_dwordx4 v[4:7], v104, s[12:15], s35 offen offset:256\n"
    "s_cmp_lt_u32 s32, 2\n"
    "s_cselect_b32 s60, s55, 2.0\n"
    "buffer_load_dwordx4 v[8:11], v104, s[12:15], s54 offen offset:0\n"
    "buffer_load_dwordx4 v[12:15], v104, s[12:15], s54 offen offset:256\n"
    "buffer_load_dwordx4 v[16:19], v104, s[12:15], s55 offen offset:0\n"
    "buffer_load_dwordx4 v[20:23], v104, s[12:15], s55 offen offset:256\n"
    "s_min_u32 s46, s34, 249\n"
    "s_mul_i32 s46, s46, 2560\n"
    "s_lshl_b32 s47, s50, 10\n"
    "s_add_u32 s47, s47, s46\n"
    "v_add_u32_e32 v158, s47, v250\n"
    "s_lshl_b32 s47, s50, 8\n"
    "s_add_u32 s47, s47, s46\n"
    "s_add_u32 s47, s47, 2048\n"
    "v_lshl_add_u32 v159, v24, 2, s47\n"
    "s_cmp_lg_u32 s51, 0\n"
    "s_cselect_b64 exec, -1, 1\n"
    "global_load_dwordx4 v[144:147], v158, s[86:87]\n"
    "global_load_dword v148, v159, s[86:87]\n"
    "global_load_dwordx4 v[152:155], v158, s[6:7]\n"
    "global_load_dword v156, v159, s[6:7]\n"
    "s_mov_b64 exec, -1\n"
    "s_mov_b32 s10, 69410816\n"
    "s_mov_b32 s11, 0x20000\n"
    "s_mov_b32 s49, 0\n"
    "v_and_b32_e32 v25, 7, v24\n"
    "v_lshlrev_b32_e32 v27, 7, v25\n"
    "v_bfe_u32 v26, v24, 3, 1\n"
    "v_lshl_add_u32 v27, v26, 10, v27\n"
    "v_and_b32_e32 v25, 6, v24\n"
    "v_lshrrev_b32_e32 v26, 4, v24\n"
    "v_xor_b32_e32 v25, v25, v26\n"
    "v_lshl_add_u32 v240, v25, 4, v27\n"
    "s_lshl_b32 s48, s50, 11\n"
    "v_xor_b32_e32 v241, 64, v240\n"
    "v_add_u32_e32 v72, s48, v240\n"
    "v_add_u32_e32 v73, s48, v241\n"
    "s_lshl_b32 s47, s93, 10\n"
    "v_add_u32_e32 v108, s47, v250\n"
    "v_lshrrev_b32_e32 v109, 7, v108\n"
    "v_mul_u32_u24_e32 v109, 0x8200, v109\n"
    "v_and_b32_e32 v32, 0x70, v250\n"
    "v_or_b32_e32 v109, v109, v32\n"
    "v_add_u32_e32 v108, 81920, v108\n"
    "v_mov_b64_e32 v[32:33], -1\n"
    "v_mov_b64_e32 v[34:35], -1\n"
    "ds_write_b128 v108, v[32:35] offset:0\n"
    "ds_write_b128 v108, v[32:35] offset:16\n"
    "v_and_b32_e32 v29, 6, v28\n"
    "v_xor_b32_e32 v29, v29, v24\n"
    "v_and_b32_e32 v29, 7, v29\n"
    "v_lshlrev_b32_e32 v105, 7, v28\n"
    "v_lshl_add_u32 v105, v29, 4, v105\n"
    "v_bfe_u32 v29, v24, 3, 1\n"
    "v_subrev_u32_e32 v29, s32, v29\n"
    "v_and_b32_e32 v29, 3, v29\n"
    "v_lshl_add_u32 v105, v29, 11, v105\n"
    "v_add_u32_e32 v105, 32768, v105\n"
    "v_xor_b32_e32 v106, 0x1000, v105\n"
    "v_and_b32_e32 v29, 15, v24\n"
    "v_cmp_eq_u32_e32 vcc, 15, v29\n"
    "v_mov_b32_e32 v107, 0xffff\n"
    "s_mov_b32 s58, 0x3f803f80\n"
    "s_mov_b32 s57, 0x43c00000\n"
    "v_cndmask_b32_e32 v107, -1, v107, vcc\n"
    "s_mov_b32 s56, 0xc3c00000\n"
    "s_mov_b32 s62, 0xc000c000\n"
    "s_mov_b32 s63, 0xc000c000\n"
    "s_lshl_b32 s47, s93, 10\n"
    "s_add_u32 s47, s47, 6080512\n"
    "v_add_u32_e32 v100, s47, v250\n"
    "v_and_b32_e32 v29, 15, v24\n"
    "s_cmp_ge_u32 s36, 112\n"
    "s_cselect_b32 s47, 8, 16\n"
    "v_cmp_le_u32_e32 vcc, s47, v29\n"
    "v_mov_b32_e32 v64, 0xbd80bd80\n"
    "v_mov_b32_e32 v65, 0xbdabbdab\n"
    "v_mov_b32_e32 v66, 0x392b392b\n"
    "v_cndmask_b32_e32 v64, v64, v65, vcc\n"
    "v_cndmask_b32_e32 v66, 0, v66, vcc\n"
    "s_cselect_b32 s47, 56, 64\n"
    "v_cmp_le_u32_e32 vcc, s47, v24\n"
    "v_mov_b32_e32 v65, 0x3e80bdab\n"
    "v_mov_b32_e32 v67, 0x392b\n"
    "v_cndmask_b32_e32 v65, v64, v65, vcc\n"
    "v_cndmask_b32_e32 v67, v66, v67, vcc\n"
    "s_cmp_ge_u32 s36, 120\n"
    "s_cselect_b32 s47, 15, 16\n"
    "v_cmp_ne_u32_e32 vcc, s47, v29\n"
    "v_mov_b32_e32 v68, 0x3db8aa3b\n"
    "s_lshr_b32 s47, s36, 5\n"
    "v_cndmask_b32_e32 v68, 0, v68, vcc\n"
    "s_waitcnt vmcnt(11)\n"
    "s_waitcnt lgkmcnt(0)\n"
    "s_barrier\n"
    "s_cmp_lg_u32 s51, 0\n"
    "s_cbranch_scc1 .Lat_noj2\n"
    "ds_read_b128 v[112:115], v72 offset:0\n"
    "ds_read_b128 v[208:211], v240 offset:18432\n"
    "ds_read_b128 v[176:179], v240 offset:16384\n"
    "ds_read_b128 v[116:119], v73 offset:0\n"
    "ds_read_b128 v[212:215], v241 offset:18432\n"
    "ds_read_b128 v[180:183], v241 offset:16384\n"
    "ds_read_b128 v[120:123], v72 offset:4096\n"
    "ds_read_b128 v[216:219], v240 offset:22528\n"
    "ds_read_b128 v[184:187], v240 offset:20480\n"
    "ds_read_b128 v[124:127], v73 offset:4096\n"
    "ds_read_b128 v[220:223], v241 offset:22528\n"
    "ds_read_b128 v[188:191], v241 offset:20480\n"
    "ds_read_b128 v[128:131], v72 offset:8192\n"
    "ds_read_b128 v[224:227], v240 offset:26624\n"
    "ds_read_b128 v[192:195], v240 offset:24576\n"
    "s_waitcnt lgkmcnt(12)\n"
    "v_mfma_f32_16x16x32_bf16 v[56:59], v[208:211], v[112:115], 0\n"
    "ds_read_b128 v[132:135], v73 offset:8192\n"
    "ds_read_b128 v[228:231], v241 offset:26624\n"
    "v_mfma_f32_16x16x32_bf16 v[48:51], v[112:115], v[176:179], 0\n"
    "ds_read_b128 v[196:199], v241 offset:24576\n"
    "s_waitcnt lgkmcnt(12)\n"
    "v_mfma_f32_16x16x32_bf16 v[56:59], v[212:215], v[116:119], v[56:59]\n"
    "ds_read_b128 v[136:139], v72 offset:12288\n"
    "ds_read_b128 v[232:235], v240 offset:30720\n"
    "v_mfma_f32_16x16x32_bf16 v[48:51], v[116:119], v[180:183], v[48:51]\n"
    "ds_read_b128 v[200:203], v240 offset:28672\n"
    "s_waitcnt lgkmcnt(12)\n"
    "v_mfma_f32_16x16x32_bf16 v[56:59], v[216:219], v[120:123], v[56:59]\n"
    "ds_read_b128 v[140:143], v73 offset:12288\n"
    "ds_read_b128 v[236:239], v241 offset:30720\n"
    "v_mfma_f32_16x16x32_bf16 v[48:51], v[120:123], v[184:187], v[48:51]\n"
    "ds_read_b128 v[204:207], v241 offset:28672\n"
    "s_waitcnt lgkmcnt(12)\n"
    "v_mfma_f32_16x16x32_bf16 v[56:59], v[220:223], v[124:127], v[56:59]\n"
    "s_mul_i32 s47, s47, 33792\n"
    "s_and_b32 s48, s36, 7\n"
    "s_lshl_b32 s48, s48, 11\n"
    "v_mfma_f32_16x16x32_bf16 v[48:51], v[124:127], v[188:191], v[48:51]\n"
    "s_add_u32 s47, s47, s48\n"
    "s_and_b32 s48, s36, 8\n"
    "s_lshl_b32 s48, s48, 7\n"
    "s_waitcnt lgkmcnt(9)\n"
    "v_mfma_f32_16x16x32_bf16 v[56:59], v[224:227], v[128:131], v[56:59]\n"
    "s_add_u32 s47, s47, s48\n"
    "s_and_b32 s48, s36, 16\n"
    "s_lshl_b32 s48, s48, 10\n"
    "v_mfma_f32_16x16x32_bf16 v[48:51], v[128:131], v[192:195], v[48:51]\n"
    "s_add_u32 s47, s47, s48\n"
    "s_add_u32 s52, s47, 5242880\n"
    "s_lshl_b32 s48, s50, 9\n"
    "s_waitcnt lgkmcnt(6)\n"
    "v_mfma_f32_16x16x32_bf16 v[56:59], v[228:231], v[132:135], v[56:59]\n"
    "s_add_u32 s52, s52, s48\n"
    "v_and_b32_e32 v74, 0x1f0, v250\n"
    "v_lshrrev_b32_e32 v29, 5, v24\n"
    "v_mfma_f32_16x16x32_bf16 v[48:51], v[132:135], v[196:199], v[48:51]\n"
    "v_lshl_add_u32 v74, v29, 3, v74\n"
    "s_lshl1_add_u32 s53, s36, s50\n"
    "s_lshl4_add_u32 s53, s53, 6080512\n"
    "s_waitcnt lgkmcnt(3)\n"
    "v_mfma_f32_16x16x32_bf16 v[56:59], v[232:235], v[136:139], v[56:59]\n"
    "s_cmp_lg_u32 s51, 0\n"
    "v_mfma_f32_16x16x32_bf16 v[48:51], v[136:139], v[200:203], v[48:51]\n"
    "s_waitcnt lgkmcnt(0)\n"
    "v_mfma_f32_16x16x32_bf16 v[56:59], v[236:239], v[140:143], v[56:59]\n"
    "v_mfma_f32_16x16x32_bf16 v[48:51], v[140:143], v[204:207], v[48:51]\n"
    "s_nop 3\n"
    "v_cvt_pk_bf16_f32 v56, v56, v57\n"
    "v_cvt_pk_bf16_f32 v57, v58, v59\n"
    "s_nop 1\n"
    "v_mfma_f32_16x16x16_bf16 v[48:51], v[56:57], v[64:65], v[48:51]\n"
    "v_mfma_f32_16x16x16_bf16 v[48:51], v[56:57], v[66:67], v[48:51]\n"
    "s_nop 7\n"
    "v_mul_f32_e32 v48, v48, v68\n"
    "v_mul_f32_e32 v49, v49, v68\n"
    "v_mul_f32_e32 v50, v50, v68\n"
    "v_mul_f32_e32 v51, v51, v68\n"
    "v_cvt_pk_bf16_f32 v48, v48, v49\n"
    "v_cvt_pk_bf16_f32 v49, v50, v51\n"
    "buffer_store_dwordx2 v[48:49], v74, s[8:11], s52 offen offset:0 sc1\n"
    "s_waitcnt vmcnt(5)\n"
    "s_branch .Lat_ln\n"
    ".Lat_noj2:\n"
    "s_cmp_lg_u32 s93, 3\n"
    "s_cbranch_scc1 .Lat_wn10\n"
    "s_call_b64 s[2:3], .Lat_w0\n"
    "s_call_b64 s[2:3], .Lat_w2\n"
    "s_call_b64 s[2:3], .Lat_w4\n"
    ".Lat_wn10:\n"
    "s_cmp_lg_u32 s93, 2\n"
    "s_cbranch_scc1 .Lat_wn11\n"
    "s_call_b64 s[2:3], .Lat_w1\n"
    "s_call_b64 s[2:3], .Lat_w3\n"
    "s_call_b64 s[2:3], .Lat_w5\n"
    ".Lat_wn11:\n"
    "s_cmp_lg_u32 s51, 0\n"
    "s_waitcnt vmcnt(4)\n"
    ".Lat_ln:\n"
    "v_add_u32_e32 v102, 4, v102\n"
    "s_nop 1\n"
    "v_readfirstlane_b32 s39, v102\n"
    "v_dot2_f32_bf16 v24, v0, s58, 0\n"
    "v_dot2_f32_bf16 v32, v8, s58, 0\n"
    "v_dot2_f32_bf16 v40, v16, s58, 0\n"
    "v_dot2_f32_bf16 v25, v4, s58, 0\n"
    "v_dot2_f32_bf16 v33, v12, s58, 0\n"
    "v_dot2_f32_bf16 v41, v20, s58, 0\n"
    "v_dot2_f32_bf16 v26, v0, v0, 0\n"
    "v_dot2_f32_bf16 v34, v8, v8, 0\n"
    "v_dot2_f32_bf16 v42, v16, v16, 0\n"
    "v_dot2_f32_bf16 v27, v4, v4, 0\n"
    "v_dot2_f32_bf16 v35, v12, v12, 0\n"
    "v_dot2_f32_bf16 v43, v20, v20, 0\n"
    "v_dot2_f32_bf16 v24, v1, s58, v24\n"
    "v_dot2_f32_bf16 v32, v9, s58, v32\n"
    "v_dot2_f32_bf16 v40, v17, s58, v40\n"
    "v_dot2_f32_bf16 v25, v5, s58, v25\n"
    "v_dot2_f32_bf16 v33, v13, s58, v33\n"
    "v_dot2_f32_bf16 v41, v21, s58, v41\n"
    "v_dot2_f32_bf16 v26, v1, v1, v26\n"
    "v_dot2_f32_bf16 v34, v9, v9, v34\n"
    "v_dot2_f32_bf16 v42, v17, v17, v42\n"
    "v_dot2_f32_bf16 v27, v5, v5, v27\n"
    "v_dot2_f32_bf16 v35, v13, v13, v35\n"
    "v_dot2_f32_bf16 v43, v21, v21, v43\n"
    "v_dot2_f32_bf16 v24, v2, s58, v24\n"
    "v_dot2_f32_bf16 v32, v10, s58, v32\n"
    "v_dot2_f32_bf16 v40, v18, s58, v40\n"
    "v_dot2_f32_bf16 v25, v6, s58, v25\n"
    "v_dot2_f32_bf16 v33, v14, s58, v33\n"
    "v_dot2_f32_bf16 v41, v22, s58, v41\n"
    "v_dot2_f32_bf16 v26, v2, v2, v26\n"
    "v_dot2_f32_bf16 v34, v10, v10, v34\n"
    "v_dot2_f32_bf16 v42, v18, v18, v42\n"
    "v_dot2_f32_bf16 v27, v6, v6, v27\n"
    "v_dot2_f32_bf16 v35, v14, v14, v35\n"
    "v_dot2_f32_bf16 v43, v22, v22, v43\n"
    "v_dot2_f32_bf16 v24, v3, s58, v24\n"
    "v_dot2_f32_bf16 v32, v11, s58, v32\n"
    "v_dot2_f32_bf16 v40, v19, s58, v40\n"
    "v_dot2_f32_bf16 v25, v7, s58, v25\n"
    "v_dot2_f32_bf16 v33, v15, s58, v33\n"
    "v_dot2_f32_bf16 v41, v23, s58, v41\n"
    "v_dot2_f32_bf16 v26, v3, v3, v26\n"
    "v_dot2_f32_bf16 v34, v11, v11, v34\n"
    "v_dot2_f32_bf16 v42, v19, v19, v42\n"
    "v_dot2_f32_bf16 v27, v7, v7, v27\n"
    "v_dot2_f32_bf16 v35, v15, v15, v35\n"
    "v_dot2_f32_bf16 v43, v23, v23, v43\n"
    "v_and_b32_e32 v30, 0xffff0000, v7\n"
    "v_and_b32_e32 v38, 0xffff0000, v15\n"
    "v_and_b32_e32 v46, 0xffff0000, v23\n"
    "v_add_f32_e32 v24, v24, v25\n"
    "v_add_f32_e32 v32, v32, v33\n"
    "v_add_f32_e32 v40, v40, v41\n"
    "v_add_f32_e32 v26, v26, v27\n"
    "v_add_f32_e32 v34, v34, v35\n"
    "v_add_f32_e32 v42, v42, v43\n"
    "s_cbranch_scc1 .Lat_nojf\n"
    "s_waitcnt vmcnt(0)\n"
    "buffer_store_dword v102, off, s[8:11], s53 offset:0 sc1\n"
    ".Lat_nojf:\n"
    "v_add_f32_dpp v25, v25, v25 quad_perm:[1,0,3,2] row_mask:0xf bank_mask:0xf bound_ctrl:1\n"
    "v_lshlrev_b32_e32 v48, 16, v0\n"
    "v_add_f32_dpp v33, v33, v33 quad_perm:[1,0,3,2] row_mask:0xf bank_mask:0xf bound_ctrl:1\n"
    "v_and_b32_e32 v49, 0xffff0000, v0\n"
    "v_add_f32_dpp v41, v41, v41 quad_perm:[1,0,3,2] row_mask:0xf bank_mask:0xf bound_ctrl:1\n"
    "v_lshlrev_b32_e32 v50, 16, v1\n"
    "v_mov_b32_dpp v30, v30 quad_perm:[3,3,3,3] row_mask:0xf bank_mask:0xf bound_ctrl:1\n"
    "v_and_b32_e32 v51, 0xffff0000, v1\n"
    "v_mov_b32_dpp v38, v38 quad_perm:[3,3,3,3] row_mask:0xf bank_mask:0xf bound_ctrl:1\n"
    "v_lshlrev_b32_e32 v52, 16, v2\n"
    "v_mov_b32_dpp v46, v46 quad_perm:[3,3,3,3] row_mask:0xf bank_mask:0xf bound_ctrl:1\n"
    "v_and_b32_e32 v53, 0xffff0000, v2\n"
    "v_add_f32_dpp v24, v24, v24 quad_perm:[1,0,3,2] row_mask:0xf bank_mask:0xf bound_ctrl:1\n"
    "v_lshlrev_b32_e32 v54, 16, v3\n"
    "v_add_f32_dpp v32, v32, v32 quad_perm:[1,0,3,2] row_mask:0xf bank_mask:0xf bound_ctrl:1\n"
    "v_and_b32_e32 v55, 0xffff0000, v3\n"
    "v_add_f32_dpp v40, v40, v40 quad_perm:[1,0,3,2] row_mask:0xf bank_mask:0xf bound_ctrl:1\n"
    "v_lshlrev_b32_e32 v56, 16, v4\n"
    "v_add_f32_dpp v26, v26, v26 quad_perm:[1,0,3,2] row_mask:0xf bank_mask:0xf bound_ctrl:1\n"
    "v_and_b32_e32 v57, 0xffff0000, v4\n"
    "v_add_f32_dpp v34, v34, v34 quad_perm:[1,0,3,2] row_mask:0xf bank_mask:0xf bound_ctrl:1\n"
    "v_lshlrev_b32_e32 v58, 16, v5\n"
    "v_add_f32_dpp v42, v42, v42 quad_perm:[1,0,3,2] row_mask:0xf bank_mask:0xf bound_ctrl:1\n"
    "v_and_b32_e32 v59, 0xffff0000, v5\n"
    "v_add_f32_dpp v24, v24, v24 quad_perm:[2,3,0,1] row_mask:0xf bank_mask:0xf bound_ctrl:1\n"
    "v_lshlrev_b32_e32 v60, 16, v6\n"
    "v_add_f32_dpp v32, v32, v32 quad_perm:[2,3,0,1] row_mask:0xf bank_mask:0xf bound_ctrl:1\n"
    "v_and_b32_e32 v61, 0xffff0000, v6\n"
    "v_add_f32_dpp v40, v40, v40 quad_perm:[2,3,0,1] row_mask:0xf bank_mask:0xf bound_ctrl:1\n"
    "v_lshlrev_b32_e32 v62, 16, v7\n"
    "v_add_f32_dpp v26, v26, v26 quad_perm:[2,3,0,1] row_mask:0xf bank_mask:0xf bound_ctrl:1\n"
    "v_and_b32_e32 v63, 0xffff0000, v7\n"
    "v_add_f32_dpp v34, v34, v34 quad_perm:[2,3,0,1] row_mask:0xf bank_mask:0xf bound_ctrl:1\n"
    "v_lshlrev_b32_e32 v64, 16, v8\n"
    "v_add_f32_dpp v42, v42, v42 quad_perm:[2,3,0,1] row_mask:0xf bank_mask:0xf bound_ctrl:1\n"
    "v_and_b32_e32 v65, 0xffff0000, v8\n"
    "v_add_f32_dpp v24, v24, v24 row_half_mirror row_mask:0xf bank_mask:0xf bound_ctrl:1\n"
    "v_lshlrev_b32_e32 v66, 16, v9\n"
    "v_add_f32_dpp v32, v32, v32 row_half_mirror row_mask:0xf bank_mask:0xf bound_ctrl:1\n"
    "v_and_b32_e32 v67, 0xffff0000, v9\n"
    "v_add_f32_dpp v40, v40, v40 row_half_mirror row_mask:0xf bank_mask:0xf bound_ctrl:1\n"
    "v_lshlrev_b32_e32 v68, 16, v10\n"
    "v_add_f32_dpp v26, v26, v26 row_half_mirror row_mask:0xf bank_mask:0xf bound_ctrl:1\n"
    "v_and_b32_e32 v69, 0xffff0000, v10\n"
    "v_add_f32_dpp v34, v34, v34 row_half_mirror row_mask:0xf bank_mask:0xf bound_ctrl:1\n"
    "v_lshlrev_b32_e32 v70, 16, v11\n"
    "v_add_f32_dpp v42, v42, v42 row_half_mirror row_mask:0xf bank_mask:0xf bound_ctrl:1\n"
    "v_and_b32_e32 v71, 0xffff0000, v11\n"
    "v_add_f32_dpp v24, v24, v24 row_mirror row_mask:0xf bank_mask:0xf bound_ctrl:1\n"
    "v_lshlrev_b32_e32 v72, 16, v12\n"
    "v_add_f32_dpp v32, v32, v32 row_mirror row_mask:0xf bank_mask:0xf bound_ctrl:1\n"
    "v_and_b32_e32 v73, 0xffff0000, v12\n"
    "v_add_f32_dpp v40, v40, v40 row_mirror row_mask:0xf bank_mask:0xf bound_ctrl:1\n"
    "v_lshlrev_b32_e32 v74, 16, v13\n"
    "v_add_f32_dpp v26, v26, v26 row_mirror row_mask:0xf bank_mask:0xf bound_ctrl:1\n"
    "v_and_b32_e32 v75, 0xffff0000, v13\n"
    "v_add_f32_dpp v34, v34, v34 row_mirror row_mask:0xf bank_mask:0xf bound_ctrl:1\n"
    "v_lshlrev_b32_e32 v76, 16, v14\n"
    "v_add_f32_dpp v42, v42, v42 row_mirror row_mask:0xf bank_mask:0xf bound_ctrl:1\n"
    "v_and_b32_e32 v77, 0xffff0000, v14\n"
    "v_mul_f32_e32 v24, 0x3b800000, v24\n"
    "v_lshlrev_b32_e32 v78, 16, v15\n"
    "v_mul_f32_e32 v32, 0x3b800000, v32\n"
    "v_and_b32_e32 v79, 0xffff0000, v15\n"
    "v_mul_f32_e32 v40, 0x3b800000, v40\n"
    "v_lshlrev_b32_e32 v80, 16, v16\n"
    "v_mul_f32_e32 v25, 0.25, v25\n"
    "v_and_b32_e32 v81, 0xffff0000, v16\n"
    "v_mul_f32_e32 v33, 0.25, v33\n"
    "v_lshlrev_b32_e32 v82, 16, v17\n"
    "v_mul_f32_e32 v41, 0.25, v41\n"
    "v_and_b32_e32 v83, 0xffff0000, v17\n"
    "v_mul_f32_e64 v27, v24, -v24\n"
    "v_lshlrev_b32_e32 v84, 16, v18\n"
    "v_mul_f32_e64 v35, v32, -v32\n"
    "v_and_b32_e32 v85, 0xffff0000, v18\n"
    "v_mul_f32_e64 v43, v40, -v40\n"
    "v_lshlrev_b32_e32 v86, 16, v19\n"
    "v_sub_f32_e32 v25, v25, v30\n"
    "v_and_b32_e32 v87, 0xffff0000, v19\n"
    "v_sub_f32_e32 v33, v33, v38\n"
    "v_lshlrev_b32_e32 v88, 16, v20\n"
    "v_sub_f32_e32 v41, v41, v46\n"
    "v_and_b32_e32 v89, 0xffff0000, v20\n"
    "v_fmac_f32_e32 v27, 0x3b800000, v26\n"
    "v_lshlrev_b32_e32 v90, 16, v21\n"
    "v_fmac_f32_e32 v35, 0x3b800000, v34\n"
    "v_and_b32_e32 v91, 0xffff0000, v21\n"
    "v_fmac_f32_e32 v43, 0x3b800000, v42\n"
    "v_lshlrev_b32_e32 v92, 16, v22\n"
    "v_mul_f32_e32 v25, 0x3eaaaaab, v25\n"
    "v_and_b32_e32 v93, 0xffff0000, v22\n"
    "v_mul_f32_e32 v33, 0x3eaaaaab, v33\n"
    "v_lshlrev_b32_e32 v94, 16, v23\n"
    "v_mul_f32_e32 v41, 0x3eaaaaab, v41\n"
    "v_and_b32_e32 v95, 0xffff0000, v23\n"
    "v_max_f32_e32 v27, 0, v27\n"
    "v_max_f32_e32 v35, 0, v35\n"
    "v_max_f32_e32 v43, 0, v43\n"
    "s_cbranch_scc0 .Lat_bin\n"
    "s_movk_i32 s45, -2560\n"
    "s_mov_b32 s48, 81920\n"
    "v_mov_b32_e32 v172, 1\n"
    "s_waitcnt vmcnt(0)\n"
    "v_mul_u32_u24_e32 v160, 0x199a, v144\n"
    "v_mul_u32_u24_e32 v161, 0x199a, v145\n"
    "v_mul_u32_u24_e32 v162, 0x199a, v146\n"
    "v_mul_u32_u24_e32 v163, 0x199a, v147\n"
    "v_mul_u32_u24_e32 v164, 0x199a, v148\n"
    "v_bfe_u32 v160, v160, 24, 2\n"
    "v_bfe_u32 v161, v161, 24, 2\n"
    "v_bfe_u32 v162, v162, 24, 2\n"
    "v_bfe_u32 v163, v163, 24, 2\n"
    "v_bfe_u32 v164, v164, 24, 2\n"
    "v_lshl_add_u32 v166, v160, 2, s48\n"
    "v_lshl_add_u32 v167, v161, 2, s48\n"
    "v_lshl_add_u32 v168, v162, 2, s48\n"
    "v_lshl_add_u32 v169, v163, 2, s48\n"
    "v_lshl_add_u32 v170, v164, 2, s48\n"
    "ds_add_rtn_u32 v166, v166, v172 offset:4096\n"
    "ds_add_rtn_u32 v167, v167, v172 offset:4096\n"
    "ds_add_rtn_u32 v168, v168, v172 offset:4096\n"
    "ds_add_rtn_u32 v169, v169, v172 offset:4096\n"
    "ds_add_rtn_u32 v170, v170, v172 offset:4096\n"
    "v_mad_i32_i24 v144, v160, s45, v144\n"
    "v_mad_i32_i24 v145, v161, s45, v145\n"
    "v_mad_i32_i24 v146, v162, s45, v146\n"
    "v_mad_i32_i24 v147, v163, s45, v147\n"
    "v_mad_i32_i24 v148, v164, s45, v148\n"
    "v_lshl_or_b32 v144, v144, 18, v152\n"
    "v_lshl_or_b32 v145, v145, 18, v153\n"
    "v_lshl_or_b32 v146, v146, 18, v154\n"
    "v_lshl_or_b32 v147, v147, 18, v155\n"
    "v_lshl_or_b32 v148, v148, 18, v156\n"
    "v_lshl_add_u32 v160, v160, 10, s48\n"
    "v_lshl_add_u32 v161, v161, 10, s48\n"
    "v_lshl_add_u32 v162, v162, 10, s48\n"
    "v_lshl_add_u32 v163, v163, 10, s48\n"
    "v_lshl_add_u32 v164, v164, 10, s48\n"
    ".Lat_bin:\n"
    "v_cndmask_b32_e64 v25, v24, v25, s[62:63]\n"
    "v_cndmask_b32_e64 v33, v32, v33, s[62:63]\n"
    "v_cndmask_b32_e64 v41, v40, v41, s[62:63]\n"
    "v_add_f32_e32 v27, 0x3727c5ac, v27\n"
    "v_add_f32_e32 v35, 0x3727c5ac, v35\n"
    "v_add_f32_e32 v43, 0x3727c5ac, v43\n"
    "v_rsq_f32_e32 v28, v27\n"
    "v_rsq_f32_e32 v36, v35\n"
    "v_rsq_f32_e32 v44, v43\n"
    "v_fma_f32 v29, -v24, v28, s57\n"
    "v_fma_f32 v37, -v32, v36, s57\n"
    "v_fma_f32 v45, -v40, v44, s57\n"
    "v_fma_f32 v31, -v25, v28, s57\n"
    "v_fma_f32 v39, -v33, v36, s57\n"
    "v_fma_f32 v47, -v41, v44, s57\n"
    "v_sub_f32_e32 v24, v29, v31\n"
    "v_sub_f32_e32 v32, v37, v39\n"
    "v_sub_f32_e32 v40, v45, v47\n"
    "v_pk_fma_f32 v[48:49], v[48:49], v[28:29], v[28:29] op_sel:[0,0,1] op_sel_hi:[1,0,1]\n"
    "v_pk_fma_f32 v[50:51], v[50:51], v[28:29], v[28:29] op_sel:[0,0,1] op_sel_hi:[1,0,1]\n"
    "v_pk_fma_f32 v[52:53], v[52:53], v[28:29], v[28:29] op_sel:[0,0,1] op_sel_hi:[1,0,1]\n"
    "v_pk_fma_f32 v[54:55], v[54:55], v[28:29], v[28:29] op_sel:[0,0,1] op_sel_hi:[1,0,1]\n"
    "v_pk_fma_f32 v[56:57], v[56:57], v[28:29], v[30:31] op_sel:[0,0,1] op_sel_hi:[1,0,1]\n"
    "v_pk_fma_f32 v[58:59], v[58:59], v[28:29], v[30:31] op_sel:[0,0,1] op_sel_hi:[1,0,1]\n"
    "v_pk_fma_f32 v[60:61], v[60:61], v[28:29], v[30:31] op_sel:[0,0,1] op_sel_hi:[1,0,1]\n"
    "v_pk_fma_f32 v[62:63], v[62:63], v[28:29], v[30:31] op_sel:[0,0,1] op_sel_hi:[1,0,1]\n"
    "v_pk_add_f32 v[48:49], v[48:49], s[56:57] op_sel_hi:[1,0]\n"
    "v_pk_add_f32 v[50:51], v[50:51], s[56:57] op_sel_hi:[1,0]\n"
    "v_pk_add_f32 v[52:53], v[52:53], s[56:57] op_sel_hi:[1,0]\n"
    "v_pk_add_f32 v[54:55], v[54:55], s[56:57] op_sel_hi:[1,0]\n"
    "v_pk_add_f32 v[56:57], v[56:57], s[56:57] op_sel_hi:[1,0]\n"
    "v_pk_add_f32 v[58:59], v[58:59], s[56:57] op_sel_hi:[1,0]\n"
    "v_pk_add_f32 v[60:61], v[60:61], s[56:57] op_sel_hi:[1,0]\n"
    "v_pk_add_f32 v[62:63], v[62:63], s[56:57] op_sel_hi:[1,0]\n"
    "v_cvt_pk_bf16_f32 v0, v48, v49\n"
    "v_cvt_pk_bf16_f32 v1, v50, v51\n"
    "v_cvt_pk_bf16_f32 v2, v52, v53\n"
    "v_cvt_pk_bf16_f32 v3, v54, v55\n"
    "v_cvt_pk_bf16_f32 v4, v56, v57\n"
    "v_cvt_pk_bf16_f32 v5, v58, v59\n"
    "v_cvt_pk_bf16_f32 v6, v60, v61\n"
    "v_cvt_pk_bf16_f32 v7, v62, v63\n"
    "buffer_store_dwordx4 v[0:3], v104, s[8:11], s35 offen offset:0 sc1\n"
    "v_bfi_b32 v7, v107, v7, 1.0\n"
    "ds_write_b128 v105, v[0:3] offset:0\n"
    "buffer_store_dwordx4 v[4:7], v104, s[8:11], s35 offen offset:256 sc1\n"
    "v_pk_fma_f32 v[64:65], v[64:65], v[36:37], v[36:37] op_sel:[0,0,1] op_sel_hi:[1,0,1]\n"
    "v_pk_fma_f32 v[66:67], v[66:67], v[36:37], v[36:37] op_sel:[0,0,1] op_sel_hi:[1,0,1]\n"
    "v_pk_fma_f32 v[68:69], v[68:69], v[36:37], v[36:37] op_sel:[0,0,1] op_sel_hi:[1,0,1]\n"
    "v_pk_fma_f32 v[70:71], v[70:71], v[36:37], v[36:37] op_sel:[0,0,1] op_sel_hi:[1,0,1]\n"
    "v_pk_fma_f32 v[72:73], v[72:73], v[36:37], v[38:39] op_sel:[0,0,1] op_sel_hi:[1,0,1]\n"
    "v_pk_fma_f32 v[74:75], v[74:75], v[36:37], v[38:39] op_sel:[0,0,1] op_sel_hi:[1,0,1]\n"
    "v_pk_fma_f32 v[76:77], v[76:77], v[36:37], v[38:39] op_sel:[0,0,1] op_sel_hi:[1,0,1]\n"
    "v_pk_fma_f32 v[78:79], v[78:79], v[36:37], v[38:39] op_sel:[0,0,1] op_sel_hi:[1,0,1]\n"
    "v_pk_add_f32 v[64:65], v[64:65], s[56:57] op_sel_hi:[1,0]\n"
    "v_pk_add_f32 v[66:67], v[66:67], s[56:57] op_sel_hi:[1,0]\n"
    "v_pk_add_f32 v[68:69], v[68:69], s[56:57] op_sel_hi:[1,0]\n"
    "v_pk_add_f32 v[70:71], v[70:71], s[56:57] op_sel_hi:[1,0]\n"
    "v_pk_add_f32 v[72:73], v[72:73], s[56:57] op_sel_hi:[1,0]\n"
    "v_pk_add_f32 v[74:75], v[74:75], s[56:57] op_sel_hi:[1,0]\n"
    "v_pk_add_f32 v[76:77], v[76:77], s[56:57] op_sel_hi:[1,0]\n"
    "v_pk_add_f32 v[78:79], v[78:79], s[56:57] op_sel_hi:[1,0]\n"
    "v_cvt_pk_bf16_f32 v8, v64, v65\n"
    "v_cvt_pk_bf16_f32 v9, v66, v67\n"
    "v_cvt_pk_bf16_f32 v10, v68, v69\n"
    "v_cvt_pk_bf16_f32 v11, v70, v71\n"
    "v_cvt_pk_bf16_f32 v12, v72, v73\n"
    "v_cvt_pk_bf16_f32 v13, v74, v75\n"
    "v_cvt_pk_bf16_f32 v14, v76, v77\n"
    "v_cvt_pk_bf16_f32 v15, v78, v79\n"
    "buffer_store_dwordx4 v[8:11], v104, s[8:11], s54 offen offset:0 sc1\n"
    "v_bfi_b32 v15, v107, v15, 1.0\n"
    "ds_write_b128 v105, v[8:11] offset:8192\n"
    "buffer_store_dwordx4 v[12:15], v104, s[8:11], s54 offen offset:256 sc1\n"
    "v_pk_fma_f32 v[80:81], v[80:81], v[44:45], v[44:45] op_sel:[0,0,1] op_sel_hi:[1,0,1]\n"
    "v_pk_fma_f32 v[82:83], v[82:83], v[44:45], v[44:45] op_sel:[0,0,1] op_sel_hi:[1,0,1]\n"
    "v_pk_fma_f32 v[84:85], v[84:85], v[44:45], v[44:45] op_sel:[0,0,1] op_sel_hi:[1,0,1]\n"
    "v_pk_fma_f32 v[86:87], v[86:87], v[44:45], v[44:45] op_sel:[0,0,1] op_sel_hi:[1,0,1]\n"
    "v_pk_fma_f32 v[88:89], v[88:89], v[44:45], v[46:47] op_sel:[0,0,1] op_sel_hi:[1,0,1]\n"
    "v_pk_fma_f32 v[90:91], v[90:91], v[44:45], v[46:47] op_sel:[0,0,1] op_sel_hi:[1,0,1]\n"
    "v_pk_fma_f32 v[92:93], v[92:93], v[44:45], v[46:47] op_sel:[0,0,1] op_sel_hi:[1,0,1]\n"
    "v_pk_fma_f32 v[94:95], v[94:95], v[44:45], v[46:47] op_sel:[0,0,1] op_sel_hi:[1,0,1]\n"
    "v_pk_add_f32 v[80:81], v[80:81], s[56:57] op_sel_hi:[1,0]\n"
    "v_pk_add_f32 v[82:83], v[82:83], s[56:57] op_sel_hi:[1,0]\n"
    "v_pk_add_f32 v[84:85], v[84:85], s[56:57] op_sel_hi:[1,0]\n"
    "v_pk_add_f32 v[86:87], v[86:87], s[56:57] op_sel_hi:[1,0]\n"
    "v_pk_add_f32 v[88:89], v[88:89], s[56:57] op_sel_hi:[1,0]\n"
    "v_pk_add_f32 v[90:91], v[90:91], s[56:57] op_sel_hi:[1,0]\n"
    "v_pk_add_f32 v[92:93], v[92:93], s[56:57] op_sel_hi:[1,0]\n"
    "v_pk_add_f32 v[94:95], v[94:95], s[56:57] op_sel_hi:[1,0]\n"
    "v_cvt_pk_bf16_f32 v16, v80, v81\n"
    "v_cvt_pk_bf16_f32 v17, v82, v83\n"
    "v_cvt_pk_bf16_f32 v18, v84, v85\n"
    "v_cvt_pk_bf16_f32 v19, v86, v87\n"
    "v_cvt_pk_bf16_f32 v20, v88, v89\n"
    "v_cvt_pk_bf16_f32 v21, v90, v91\n"
    "v_cvt_pk_bf16_f32 v22, v92, v93\n"
    "v_cvt_pk_bf16_f32 v23, v94, v95\n"
    "buffer_store_dwordx4 v[16:19], v104, s[8:11], s60 offen offset:0 sc1\n"
    "v_bfi_b32 v23, v107, v23, 1.0\n"
    "ds_write_b128 v105, v[16:19] offset:16384\n"
    "buffer_store_dwordx4 v[20:23], v104, s[8:11], s60 offen offset:256 sc1\n"
    "v_pk_add_f32 v[56:57], v[56:57], v[24:25] op_sel_hi:[1,0]\n"
    "v_pk_add_f32 v[58:59], v[58:59], v[24:25] op_sel_hi:[1,0]\n"
    "v_pk_add_f32 v[60:61], v[60:61], v[24:25] op_sel_hi:[1,0]\n"
    "v_pk_add_f32 v[62:63], v[62:63], v[24:25] op_sel_hi:[1,0]\n"
    "v_cvt_pk_bf16_f32 v56, v56, v57\n"
    "v_cvt_pk_bf16_f32 v57, v58, v59\n"
    "v_cvt_pk_bf16_f32 v58, v60, v61\n"
    "v_cvt_pk_bf16_f32 v59, v62, v63\n"
    "ds_write_b128 v106, v[56:59] offset:0\n"
    "v_pk_add_f32 v[72:73], v[72:73], v[32:33] op_sel_hi:[1,0]\n"
    "v_pk_add_f32 v[74:75], v[74:75], v[32:33] op_sel_hi:[1,0]\n"
    "v_pk_add_f32 v[76:77], v[76:77], v[32:33] op_sel_hi:[1,0]\n"
    "v_pk_add_f32 v[78:79], v[78:79], v[32:33] op_sel_hi:[1,0]\n"
    "v_cvt_pk_bf16_f32 v72, v72, v73\n"
    "v_cvt_pk_bf16_f32 v73, v74, v75\n"
    ".long 0xbedf00ff\n"
    ".Lat_w0:\n"
    "s_setpc_b64 s[2:3]\n"
    "v_cvt_pk_bf16_f32 v74, v76, v77\n"
    "v_cvt_pk_bf16_f32 v75, v78, v79\n"
    "ds_write_b128 v106, v[72:75] offset:8192\n"
    "v_pk_add_f32 v[88:89], v[88:89], v[40:41] op_sel_hi:[1,0]\n"
    "v_pk_add_f32 v[90:91], v[90:91], v[40:41] op_sel_hi:[1,0]\n"
    "v_pk_add_f32 v[92:93], v[92:93], v[40:41] op_sel_hi:[1,0]\n"
    "v_pk_add_f32 v[94:95], v[94:95], v[40:41] op_sel_hi:[1,0]\n"
    "v_cvt_pk_bf16_f32 v88, v88, v89\n"
    "v_cvt_pk_bf16_f32 v89, v90, v91\n"
    "v_cvt_pk_bf16_f32 v90, v92, v93\n"
    "v_cvt_pk_bf16_f32 v91, v94, v95\n"
    "ds_write_b128 v106, v[88:91] offset:16384\n"
    "s_cbranch_scc0 .Lat_bin2\n"
    "s_waitcnt lgkmcnt(0)\n"
    "v_min_i32_e32 v166, 254, v166\n"
    "v_min_i32_e32 v167, 254, v167\n"
    "v_min_i32_e32 v168, 254, v168\n"
    "v_min_i32_e32 v169, 254, v169\n"
    "v_min_i32_e32 v170, 254, v170\n"
    "v_lshl_add_u32 v160, v166, 2, v160\n"
    "v_lshl_add_u32 v161, v167, 2, v161\n"
    "v_lshl_add_u32 v162, v168, 2, v162\n"
    "v_lshl_add_u32 v163, v169, 2, v163\n"
    "v_lshl_add_u32 v164, v170, 2, v164\n"
    "ds_write_b32 v160, v144 offset:4\n"
    "ds_write_b32 v161, v145 offset:4\n"
    "ds_write_b32 v162, v146 offset:4\n"
    "ds_write_b32 v163, v147 offset:4\n"
    "ds_write_b32 v164, v148 offset:4\n"
    ".Lat_bin2:\n"
    "s_waitcnt lgkmcnt(0)\n"
    "s_barrier\n"
    "ds_read_b128 a[0:3], v240 offset:32768\n"
    "ds_read_b128 a[32:35], v240 offset:40960\n"
    "ds_read_b128 a[64:67], v240 offset:49152\n"
    "ds_read_b128 a[4:7], v241 offset:32768\n"
    "ds_read_b128 a[36:39], v241 offset:40960\n"
    "ds_read_b128 a[68:71], v241 offset:49152\n"
    "ds_read_b128 a[8:11], v240 offset:34816\n"
    "ds_read_b128 a[40:43], v240 offset:43008\n"
    "ds_read_b128 a[72:75], v240 offset:51200\n"
    "ds_read_b128 a[12:15], v241 offset:34816\n"
    "ds_read_b128 a[44:47], v241 offset:43008\n"
    "ds_read_b128 a[76:79], v241 offset:51200\n"
    "buffer_load_dword v101, v100, s[8:11], 0 offen offset:0 sc1\n"
    "ds_read_b128 a[16:19], v240 offset:36864\n"
    "ds_read_b128 a[48:51], v240 offset:45056\n"
    "ds_read_b128 a[80:83], v240 offset:53248\n"
    "ds_read_b128 a[20:23], v241 offset:36864\n"
    "ds_read_b128 a[52:55], v241 offset:45056\n"
    "ds_read_b128 a[84:87], v241 offset:53248\n"
    "ds_read_b128 a[24:27], v240 offset:38912\n"
    "ds_read_b128 a[56:59], v240 offset:47104\n"
    "ds_read_b128 a[88:91], v240 offset:55296\n"
    "ds_read_b128 a[28:31], v241 offset:38912\n"
    "ds_read_b128 a[60:63], v241 offset:47104\n"
    "ds_read_b128 a[92:95], v241 offset:55296\n"
    "ds_read_b128 v[40:43], v108 offset:0\n"
    "s_mul_i32 s52, s93, 33792\n"
    "s_add_u32 s52, s52, 5242880\n"
    "s_lshl_b32 s61, s32, 12\n"
    "s_lshl4_add_u32 s46, s34, 6076416\n"
    "s_cmp_eq_u32 s93, 0\n"
    "s_cselect_b32 s46, s46, 2.0\n"
    "s_lshl_b32 s48, s34, 7\n"
    "s_add_u32 s48, s48, 68206592\n"
    "s_lshl_b32 s47, s93, 12\n"
    "s_waitcnt vmcnt(1)\n"
    "s_waitcnt lgkmcnt(0)\n"
    "s_barrier\n"
    "buffer_store_dword v102, off, s[8:11], s46 offset:0 sc1\n"
    "buffer_store_dwordx4 v[40:43], v109, s[8:11], s48 offen offset:0 sc1\n"
    "s_lshl4_add_u32 s46, s34, s47\n"
    "s_add_u32 s46, s46, 6084608\n"
    "s_waitcnt vmcnt(2)\n"
    "v_cmp_ne_u32_e32 vcc, s39, v101\n"
    "s_nop 4\n"
    "s_cbranch_vccz .Lat_apoll_ret\n"
    ".Lat_apoll:\n"
    "s_mov_b32 s53, 0x400000\n"
    ".Lat_apoll_l:\n"
    "s_sleep 1\n"
    "buffer_load_dword v101, v100, s[8:11], 0 offen offset:0 sc1\n"
    "s_waitcnt vmcnt(0)\n"
    "v_cmp_ne_u32_e32 vcc, s39, v101\n"
    "s_sub_u32 s53, s53, 1\n"
    "s_cbranch_scc1 .Lat_apoll_ret\n"
    "s_nop 2\n"
    "s_cbranch_vccnz .Lat_apoll_l\n"
    ".Lat_apoll_ret:\n"
    "s_add_u32 s47, s52, s61\n"
    "buffer_load_dwordx4 v[112:115], v250, s[8:11], s47 offen offset:0 sc1\n"
    "buffer_load_dwordx4 v[116:119], v250, s[8:11], s47 offen offset:1024 sc1\n"
    "buffer_load_dwordx4 v[128:131], v250, s[8:11], s47 offen offset:2048 sc1\n"
    "s_add_u32 s61, s61, 0x1000\n"
    "buffer_load_dwordx4 v[132:135], v250, s[8:11], s47 offen offset:3072 sc1\n"
    "s_bitset0_b32 s61, 14\n"
    "s_add_u32 s47, s52, s61\n"
    "buffer_load_dwordx4 v[144:147], v250, s[8:11], s47 offen offset:0 sc1\n"
    "buffer_load_dwordx4 v[148:151], v250, s[8:11], s47 offen offset:1024 sc1\n"
    "buffer_load_dwordx4 v[160:163], v250, s[8:11], s47 offen offset:2048 sc1\n"
    "s_add_u32 s61, s61, 0x1000\n"
    "buffer_load_dwordx4 v[164:167], v250, s[8:11], s47 offen offset:3072 sc1\n"
    "s_bitset0_b32 s61, 14\n"
    "s_add_u32 s47, s52, s61\n"
    "buffer_load_dwordx4 v[176:179], v250, s[8:11], s47 offen offset:0 sc1\n"
    "buffer_load_dwordx4 v[180:183], v250, s[8:11], s47 offen offset:1024 sc1\n"
    "buffer_load_dwordx4 v[192:195], v250, s[8:11], s47 offen offset:2048 sc1\n"
    "s_add_u32 s61, s61, 0x1000\n"
    "v_lshrrev_b32_e32 v8, 4, v250\n"
    "v_and_b32_e32 v10, 0x70, v251\n"
    "v_bfe_u32 v9, v8, 3, 1\n"
    "v_lshl_add_u32 v248, v9, 9, v10\n"
    "v_lshrrev_b32_e32 v9, 4, v8\n"
    "v_lshl_add_u32 v248, v9, 12, v248\n"
    "s_mul_i32 s43, s50, 3\n"
    "s_xor_b32 s43, s43, s51\n"
    "s_lshl_b32 s43, s43, 15\n"
    "s_add_u32 s43, s43, 32768\n"
    "s_mul_i32 s42, s93, 5120\n"
    "s_lshl_b32 s45, s50, 8\n"
    "s_mul_i32 s40, s32, 1277952\n"
    "s_lshl_b32 s41, s51, 14\n"
    "s_add_u32 s40, s40, s41\n"
    "s_add_u32 s40, s40, s45\n"
    "s_add_u32 s41, s40, 128\n"
    "v_bfe_u32 v9, v8, 2, 3\n"
    "v_lshlrev_b32_e32 v244, 7, v9\n"
    "v_lshrrev_b32_e32 v10, 5, v8\n"
    "v_lshl_add_u32 v244, v10, 10, v244\n"
    "v_and_b32_e32 v9, 6, v9\n"
    "v_bfe_u32 v10, v8, 1, 1\n"
    "v_xor_b32_e32 v9, v9, v10\n"
    "v_lshl_add_u32 v244, v9, 4, v244\n"
    "v_and_b32_e32 v10, 1, v8\n"
    "v_lshl_add_u32 v244, v10, 3, v244\n"
    "s_lshl_b32 s45, s51, 16\n"
    "s_add_u32 s45, s45, 32768\n"
    "v_add_u32_e32 v244, s45, v244\n"
    "v_xor_b32_e32 v245, 32, v244\n"
    "v_xor_b32_e32 v246, 64, v244\n"
    "v_xor_b32_e32 v247, 0x60, v244\n"
    "s_lshl_b32 s45, s51, 15\n"
    "s_add_u32 s48, s45, 32768\n"
    "s_sub_u32 s45, 131072, s45\n"
    "v_add_u32_e32 v242, s45, v240\n"
    "v_add_u32_e32 v240, s48, v240\n"
    "v_xor_b32_e32 v241, 64, v240\n"
    "v_xor_b32_e32 v243, 64, v242\n"
    "s_xor_b32 s45, s93, 2\n"
    "s_mul_i32 s45, s45, 5120\n"
    "v_lshl_add_u32 v249, v8, 2, s45\n"
    "s_lshl_b32 s45, s93, 11\n"
    "v_add_u32_e32 v99, s45, v250\n"
    "v_add_u32_e32 v98, 11345920, v99\n"
    "v_and_b32_e32 v252, 0x30, v250\n"
    "v_add_u32_e32 v252, 0x400, v252\n"
    "s_mul_i32 s59, s32, 999\n"
    "s_andn2_b32 s59, s59, 63\n"
    "s_sub_u32 s48, 3968, s59\n"
    "v_cmp_le_u32_e32 vcc, 0x80, v250\n"
    "v_mov_b32_e32 v80, s48\n"
    "s_add_u32 s59, s59, 6076416\n"
    "v_cndmask_b32_e32 v83, v250, v80, vcc\n"
    "v_bfe_u32 v253, v250, 4, 2\n"
    "v_max_u32_e32 v253, 1, v253\n"
    "v_add_u32_e32 v253, s32, v253\n"
    "v_and_b32_e32 v253, 3, v253\n"
    "v_lshlrev_b32_e32 v253, 9, v253\n"
    "v_mov_b32_e32 v84, s39\n"
    "v_lshrrev_b32_e32 v86, 2, v250\n"
    "v_min_u32_e32 v86, 124, v86\n"
    "s_lshl2_add_u32 s58, s34, s93\n"
    "s_lshl_b32 s58, s58, 7\n"
    "s_add_u32 s58, s58, 5910528\n"
    "s_lshl_b32 s45, s50, 13\n"
    "v_add_u32_e32 v85, s45, v250\n"
    "s_lshl3_add_u32 s45, s45, 49152\n"
    "v_add_u32_e32 v255, s45, v250\n"
    "v_lshrrev_b32_e32 v105, 7, v250\n"
    "v_and_b32_e32 v104, 0x70, v251\n"
    "v_lshl_add_u32 v104, v105, 9, v104\n"
    "v_add_u32_e32 v105, 0xc00, v104\n"
    "v_add_u32_e32 v106, 0x1800, v104\n"
    "v_add_u32_e32 v107, 0x2400, v104\n"
    "s_lshl_b32 s57, s93, 7\n"
    "s_lshl4_add_u32 s56, s57, 24576\n"
    "s_add_u32 s57, s57, 5111808\n"
    "s_mul_i32 s45, s51, 32768\n"
    "s_add_u32 s45, s45, 8192\n"
    "v_subrev_u32_e32 v46, s45, v240\n"
    "v_subrev_u32_e32 v47, s45, v241\n"
    "s_mul_i32 s45, s51, 61440\n"
    "s_add_u32 s45, s45, 8192\n"
    "v_subrev_u32_e32 v4, s45, v244\n"
    "v_subrev_u32_e32 v5, s45, v245\n"
    "v_subrev_u32_e32 v6, s45, v246\n"
    "v_subrev_u32_e32 v7, s45, v247\n"
    "s_waitcnt vmcnt(9)\n"
    "v_mfma_f32_16x16x32_bf16 v[24:27], v[112:115], a[0:3], 0\n"
    "buffer_load_dwordx4 v[196:199], v250, s[8:11], s47 offen offset:3072 sc1\n"
    "s_bitset0_b32 s61, 14\n"
    "v_mfma_f32_16x16x32_bf16 v[28:31], v[116:119], a[0:3], 0\n"
    "v_accvgpr_write_b32 a244, 0\n"
    "v_mfma_f32_16x16x32_bf16 v[48:51], v[112:115], a[32:35], 0\n"
    "v_accvgpr_write_b32 a245, 0\n"
    "v_mfma_f32_16x16x32_bf16 v[52:55], v[116:119], a[32:35], 0\n"
    "s_add_u32 s47, s52, s61\n"
    "buffer_load_dwordx4 v[208:211], v250, s[8:11], s47 offen offset:0 sc1\n"
    "v_mfma_f32_16x16x32_bf16 v[64:67], v[112:115], a[64:67], 0\n"
    "v_bfe_u32 v0, v250, 4, 2\n"
    "v_mfma_f32_16x16x32_bf16 v[68:71], v[116:119], a[64:67], 0\n"
    "s_mov_b32 s18, 5120000\n"
    "v_accvgpr_write_b32 a246, 0\n"
    "s_waitcnt vmcnt(9)\n"
    "v_mfma_f32_16x16x32_bf16 v[24:27], v[128:131], a[4:7], v[24:27]\n"
    "buffer_load_dwordx4 v[212:215], v250, s[8:11], s47 offen offset:1024 sc1\n"
    "s_add_u32 s52, s52, 0x4000\n"
    "v_mfma_f32_16x16x32_bf16 v[28:31], v[132:135], a[4:7], v[28:31]\n"
    "buffer_store_dword v102, off, s[8:11], s46 offset:0 sc1\n"
    "v_accvgpr_write_b32 a247, 0\n"
    "v_mfma_f32_16x16x32_bf16 v[48:51], v[128:131], a[36:39], v[48:51]\n"
    "v_bfe_u32 v1, v250, 6, 2\n"
    "v_accvgpr_write_b32 a248, 0\n"
    "v_mfma_f32_16x16x32_bf16 v[52:55], v[132:135], a[36:39], v[52:55]\n"
    "buffer_load_dwordx4 v[224:227], v250, s[8:11], s47 offen offset:2048 sc1\n"
    "s_add_u32 s61, s61, 0x1000\n"
    "v_mfma_f32_16x16x32_bf16 v[64:67], v[128:131], a[68:71], v[64:67]\n"
    "v_mov_b32_e32 v2, 0x3f80\n"
    "v_accvgpr_write_b32 a249, 0\n"
    "v_mfma_f32_16x16x32_bf16 v[68:71], v[132:135], a[68:71], v[68:71]\n"
    "s_mov_b32 s19, 0x20000\n"
    "s_waitcnt vmcnt(10)\n"
    "v_mfma_f32_16x16x32_bf16 v[24:27], v[144:147], a[8:11], v[24:27]\n"
    "buffer_load_dwordx4 v[228:231], v250, s[8:11], s47 offen offset:3072 sc1\n"
    "s_bitset0_b32 s61, 14\n"
    "v_mfma_f32_16x16x32_bf16 v[28:31], v[148:151], a[8:11], v[28:31]\n"
    "v_accvgpr_write_b32 a250, 0\n"
    "v_mfma_f32_16x16x32_bf16 v[48:51], v[144:147], a[40:43], v[48:51]\n"
    "s_mov_b64 s[20:21], s[8:9]\n"
    "v_accvgpr_write_b32 a251, 0\n"
    "v_mfma_f32_16x16x32_bf16 v[52:55], v[148:151], a[40:43], v[52:55]\n"
    "s_add_u32 s47, s52, s61\n"
    "buffer_load_dwordx4 v[120:123], v250, s[8:11], s47 offen offset:0 sc1\n"
    "v_mfma_f32_16x16x32_bf16 v[64:67], v[144:147], a[72:75], v[64:67]\n"
    "v_cmp_eq_u32_e32 vcc, 3, v0\n"
    "s_nop 1\n"
    "v_cndmask_b32_e64 v2, v2, 1.0, vcc\n"
    "v_mfma_f32_16x16x32_bf16 v[68:71], v[148:151], a[72:75], v[68:71]\n"
    "v_accvgpr_write_b32 a252, 0\n"
    "s_waitcnt vmcnt(10)\n"
    "v_mfma_f32_16x16x32_bf16 v[24:27], v[160:163], a[12:15], v[24:27]\n"
    "buffer_load_dwordx4 v[124:127], v250, s[8:11], s47 offen offset:1024 sc1\n"
    "v_accvgpr_write_b32 a253, 0\n"
    "v_mfma_f32_16x16x32_bf16 v[28:31], v[164:167], a[12:15], v[28:31]\n"
    "s_mov_b32 s22, 5242880\n"
    "v_accvgpr_write_b32 a254, 0\n"
    "v_mfma_f32_16x16x32_bf16 v[48:51], v[160:163], a[44:47], v[48:51]\n"
    "v_cmp_eq_u32_e32 vcc, 0, v1\n"
    "s_nop 1\n"
    "v_cndmask_b32_e32 v3, 0, v2, vcc\n"
    "v_mfma_f32_16x16x32_bf16 v[52:55], v[164:167], a[44:47], v[52:55]\n"
    "buffer_load_dwordx4 v[136:139], v250, s[8:11], s47 offen offset:2048 sc1\n"
    "s_add_u32 s61, s61, 0x1000\n"
    "v_mfma_f32_16x16x32_bf16 v[64:67], v[160:163], a[76:79], v[64:67]\n"
    "v_accvgpr_write_b32 a232, v3\n"
    "v_accvgpr_write_b32 a255, 0\n"
    "v_mfma_f32_16x16x32_bf16 v[68:71], v[164:167], a[76:79], v[68:71]\n"
    "s_mov_b64 s[24:25], s[28:29]\n"
    "v_accvgpr_write_b32 a192, 0\n"
    "s_waitcnt vmcnt(10)\n"
    "v_mfma_f32_16x16x32_bf16 v[24:27], v[176:179], a[16:19], v[24:27]\n"
    "buffer_load_dwordx4 v[140:143], v250, s[8:11], s47 offen offset:3072 sc1\n"
    "s_bitset0_b32 s61, 14\n"
    "v_mfma_f32_16x16x32_bf16 v[28:31], v[180:183], a[16:19], v[28:31]\n"
    "v_accvgpr_write_b32 a193, 0\n"
    "v_mfma_f32_16x16x32_bf16 v[48:51], v[176:179], a[48:51], v[48:51]\n"
    "v_cmp_eq_u32_e32 vcc, 1, v1\n"
    "s_nop 1\n"
    "v_cndmask_b32_e32 v3, 0, v2, vcc\n"
    "v_mfma_f32_16x16x32_bf16 v[52:55], v[180:183], a[48:51], v[52:55]\n"
    "s_add_u32 s47, s52, s61\n"
    "buffer_load_dwordx4 v[152:155], v250, s[8:11], s47 offen offset:0 sc1\n"
    "v_mfma_f32_16x16x32_bf16 v[64:67], v[176:179], a[80:83], v[64:67]\n"
    "buffer_load_dword v80, v83, s[8:11], s59 offen offset:0 sc1\n"
    "v_accvgpr_write_b32 a194, 0\n"
    "v_mfma_f32_16x16x32_bf16 v[68:71], v[180:183], a[80:83], v[68:71]\n"
    "s_movk_i32 s26, 20000\n"
    "v_accvgpr_write_b32 a195, 0\n"
    "s_waitcnt vmcnt(11)\n"
    "v_mfma_f32_16x16x32_bf16 v[24:27], v[192:195], a[20:23], v[24:27]\n"
    "buffer_load_dwordx4 v[156:159], v250, s[8:11], s47 offen offset:1024 sc1\n"
    "v_accvgpr_write_b32 a196, 0\n"
    "v_mfma_f32_16x16x32_bf16 v[28:31], v[196:199], a[20:23], v[28:31]\n"
    "v_accvgpr_write_b32 a233, v3\n"
    "v_mfma_f32_16x16x32_bf16 v[48:51], v[192:195], a[52:55], v[48:51]\n"
    "s_bfe_u32 s88, s92, 0x20001\n"
    "v_accvgpr_write_b32 a197, 0\n"
    "v_mfma_f32_16x16x32_bf16 v[52:55], v[196:199], a[52:55], v[52:55]\n"
    "buffer_load_dwordx4 v[168:171], v250, s[8:11], s47 offen offset:2048 sc1\n"
    "s_add_u32 s61, s61, 0x1000\n"
    "v_mfma_f32_16x16x32_bf16 v[64:67], v[192:195], a[84:87], v[64:67]\n"
    "v_cmp_eq_u32_e32 vcc, 2, v1\n"
    "s_nop 1\n"
    "v_cndmask_b32_e32 v3, 0, v2, vcc\n"
    "v_mfma_f32_16x16x32_bf16 v[68:71], v[196:199], a[84:87], v[68:71]\n"
    "v_accvgpr_write_b32 a198, 0\n"
    "s_waitcnt vmcnt(11)\n"
    "v_mfma_f32_16x16x32_bf16 v[24:27], v[208:211], a[24:27], v[24:27]\n"
    "buffer_load_dwordx4 v[172:175], v250, s[8:11], s47 offen offset:3072 sc1\n"
    "s_bitset0_b32 s61, 14\n"
    "v_mfma_f32_16x16x32_bf16 v[28:31], v[212:215], a[24:27], v[28:31]\n"
    "s_lshl_b32 s82, s88, 7\n"
    "v_accvgpr_write_b32 a199, 0\n"
    "v_mfma_f32_16x16x32_bf16 v[48:51], v[208:211], a[56:59], v[48:51]\n"
    "v_accvgpr_write_b32 a234, v3\n"
    "v_accvgpr_write_b32 a200, 0\n"
    "v_mfma_f32_16x16x32_bf16 v[52:55], v[212:215], a[56:59], v[52:55]\n"
    "s_add_u32 s47, s52, s61\n"
    "buffer_load_dwordx4 v[184:187], v250, s[8:11], s47 offen offset:0 sc1\n"
    "v_mfma_f32_16x16x32_bf16 v[64:67], v[208:211], a[88:91], v[64:67]\n"
    "v_cmp_eq_u32_e32 vcc, 3, v1\n"
    "s_nop 1\n"
    "v_cndmask_b32_e32 v3, 0, v2, vcc\n"
    "v_mfma_f32_16x16x32_bf16 v[68:71], v[212:215], a[88:91], v[68:71]\n"
    "s_add_u32 s82, s82, 6101248\n"
    "v_accvgpr_write_b32 a201, 0\n"
    "s_waitcnt vmcnt(10)\n"
    "v_mfma_f32_16x16x32_bf16 v[24:27], v[224:227], a[28:31], v[24:27]\n"
    "buffer_load_dwordx4 v[188:191], v250, s[8:11], s47 offen offset:1024 sc1\n"
    "v_accvgpr_write_b32 a202, 0\n"
    "v_mfma_f32_16x16x32_bf16 v[28:31], v[228:231], a[28:31], v[28:31]\n"
    "v_accvgpr_write_b32 a235, v3\n"
    "v_accvgpr_write_b32 a203, 0\n"
    "v_mfma_f32_16x16x32_bf16 v[48:51], v[224:227], a[60:63], v[48:51]\n"
    "s_lshl_b32 s83, s88, 17\n"
    "v_accvgpr_write_b32 a204, 0\n"
    "v_mfma_f32_16x16x32_bf16 v[52:55], v[228:231], a[60:63], v[52:55]\n"
    "buffer_load_dwordx4 v[200:203], v250, s[8:11], s47 offen offset:2048 sc1\n"
    "s_add_u32 s61, s61, 0x1000\n"
    "v_mfma_f32_16x16x32_bf16 v[64:67], v[224:227], a[92:95], v[64:67]\n"
    "s_and_b32 s89, s34, 31\n"
    "v_accvgpr_write_b32 a205, 0\n"
    "v_mfma_f32_16x16x32_bf16 v[68:71], v[228:231], a[92:95], v[68:71]\n"
    "s_lshl2_add_u32 s89, s89, s93\n"
    "v_accvgpr_write_b32 a206, 0\n"
    "s_waitcnt vmcnt(10)\n"
    "v_mfma_f32_16x16x32_bf16 v[32:35], v[120:123], a[0:3], 0\n"
    "buffer_load_dwordx4 v[204:207], v250, s[8:11], s47 offen offset:3072 sc1\n"
    "s_bitset0_b32 s61, 14\n"
    "v_mfma_f32_16x16x32_bf16 v[36:39], v[124:127], a[0:3], 0\n"
    "v_cvt_pk_bf16_f32 v24, v24, v25\n"
    "v_accvgpr_write_b32 a207, 0\n"
    "v_mfma_f32_16x16x32_bf16 v[56:59], v[120:123], a[32:35], 0\n"
    "v_cvt_pk_bf16_f32 v25, v26, v27\n"
    "v_accvgpr_write_b32 a208, 0\n"
    "v_mfma_f32_16x16x32_bf16 v[60:63], v[124:127], a[32:35], 0\n"
    "s_add_u32 s47, s52, s61\n"
    "buffer_load_dwordx4 v[216:219], v250, s[8:11], s47 offen offset:0 sc1\n"
    "v_mfma_f32_16x16x32_bf16 v[72:75], v[120:123], a[64:67], 0\n"
    "v_cvt_pk_bf16_f32 v26, v28, v29\n"
    "v_cvt_pk_bf16_f32 v27, v30, v31\n"
    "v_mfma_f32_16x16x32_bf16 v[76:79], v[124:127], a[64:67], 0\n"
    "buffer_store_dwordx4 v[24:27], v98, s[8:11], s35 offen offset:0 sc1\n"
    "v_accvgpr_write_b32 a209, 0\n"
    "s_waitcnt vmcnt(11)\n"
    "v_mfma_f32_16x16x32_bf16 v[32:35], v[136:139], a[4:7], v[32:35]\n"
    "buffer_load_dwordx4 v[220:223], v250, s[8:11], s47 offen offset:1024 sc1\n"
    "s_add_u32 s52, s52, 0x4000\n"
    "v_mfma_f32_16x16x32_bf16 v[36:39], v[140:143], a[4:7], v[36:39]\n"
    "v_cvt_pk_bf16_f32 v48, v48, v49\n"
    "v_accvgpr_write_b32 a210, 0\n"
    "v_mfma_f32_16x16x32_bf16 v[56:59], v[136:139], a[36:39], v[56:59]\n"
    "v_cvt_pk_bf16_f32 v49, v50, v51\n"
    "v_cvt_pk_bf16_f32 v50, v52, v53\n"
    "v_mfma_f32_16x16x32_bf16 v[60:63], v[140:143], a[36:39], v[60:63]\n"
    "buffer_load_dwordx4 v[232:235], v250, s[8:11], s47 offen offset:2048 sc1\n"
    "s_add_u32 s61, s61, 0x1000\n"
    "s_waitcnt vmcnt(11)\n"
    "v_cmp_ne_u32_e32 vcc, s39, v80\n"
    "v_mfma_f32_16x16x32_bf16 v[72:75], v[136:139], a[68:71], v[72:75]\n"
    "v_cvt_pk_bf16_f32 v51, v54, v55\n"
    "buffer_store_dwordx4 v[48:51], v98, s[8:11], s54 offen offset:0 sc1\n"
    "v_mfma_f32_16x16x32_bf16 v[76:79], v[140:143], a[68:71], v[76:79]\n"
    "v_cvt_pk_bf16_f32 v64, v64, v65\n"
    "v_cvt_pk_bf16_f32 v65, v66, v67\n"
    "s_waitcnt vmcnt(11)\n"
    "v_mfma_f32_16x16x32_bf16 v[32:35], v[152:155], a[8:11], v[32:35]\n"
    "buffer_load_dwordx4 v[236:239], v250, s[8:11], s47 offen offset:3072 sc1\n"
    "s_bitset0_b32 s61, 14\n"
    "v_mfma_f32_16x16x32_bf16 v[36:39], v[156:159], a[8:11], v[36:39]\n"
    "s_cbranch_vccnz .Lat_h1poll\n"
    ".Lat_h1poll_ret:\n"
    "s_mov_b32 m0, s56\n"
    "v_mfma_f32_16x16x32_bf16 v[56:59], v[152:155], a[40:43], v[56:59]\n"
    "buffer_load_dwordx4 v104, s[8:11], s57 offen offset:0 sc1 lds\n"
    "v_cvt_pk_bf16_f32 v66, v68, v69\n"
    "v_mfma_f32_16x16x32_bf16 v[60:63], v[156:159], a[40:43], v[60:63]\n"
    "v_cvt_pk_bf16_f32 v67, v70, v71\n"
    "buffer_store_dwordx4 v[64:67], v98, s[8:11], s60 offen offset:0 sc1\n"
    "v_mfma_f32_16x16x32_bf16 v[72:75], v[152:155], a[72:75], v[72:75]\n"
    "s_lshl_b32 s89, s89, 10\n"
    "v_accvgpr_write_b32 a211, 0\n"
    "v_mfma_f32_16x16x32_bf16 v[76:79], v[156:159], a[72:75], v[76:79]\n"
    "s_add_u32 s83, s83, s89\n"
    "v_accvgpr_write_b32 a212, 0\n"
    "s_waitcnt vmcnt(12)\n"
    "v_mfma_f32_16x16x32_bf16 v[32:35], v[168:171], a[12:15], v[32:35]\n"
    "buffer_load_dwordx4 v105, s[8:11], s57 offen offset:1024 sc1 lds\n"
    "v_accvgpr_write_b32 a213, 0\n"
    "v_mfma_f32_16x16x32_bf16 v[36:39], v[172:175], a[12:15], v[36:39]\n"
    "s_add_u32 s83, s83, 67682304\n"
    "v_accvgpr_write_b32 a214, 0\n"
    "v_mfma_f32_16x16x32_bf16 v[56:59], v[168:171], a[44:47], v[56:59]\n"
    "ds_write_b128 v99, v[24:27] offset:0\n"
    "v_accvgpr_write_b32 a215, 0\n"
    "v_mfma_f32_16x16x32_bf16 v[60:63], v[172:175], a[44:47], v[60:63]\n"
    "s_add_u32 m0, s43, 0\n"
    "v_accvgpr_write_b32 a216, 0\n"
    "v_mfma_f32_16x16x32_bf16 v[72:75], v[168:171], a[76:79], v[72:75]\n"
    "buffer_load_dwordx4 v104, s[8:11], s40 offen offset:0 sc1 lds\n"
    "v_accvgpr_write_b32 a217, 0\n"
    "v_mfma_f32_16x16x32_bf16 v[76:79], v[172:175], a[76:79], v[76:79]\n"
    "v_accvgpr_write_b32 a218, 0\n"
    "s_waitcnt vmcnt(12)\n"
    "v_mfma_f32_16x16x32_bf16 v[32:35], v[184:187], a[16:19], v[32:35]\n"
    "s_lshl_b32 s30, s93, 10\n"
    "v_mfma_f32_16x16x32_bf16 v[36:39], v[188:191], a[16:19], v[36:39]\n"
    "v_accvgpr_write_b32 a219, 0\n"
    "v_mfma_f32_16x16x32_bf16 v[56:59], v[184:187], a[48:51], v[56:59]\n"
    "buffer_load_dwordx4 v105, s[8:11], s40 offen offset:1024 sc1 lds\n"
    "v_mfma_f32_16x16x32_bf16 v[60:63], v[188:191], a[48:51], v[60:63]\n"
    "v_accvgpr_write_b32 a220, 0\n"
    "v_mfma_f32_16x16x32_bf16 v[72:75], v[184:187], a[80:83], v[72:75]\n"
    "s_add_u32 s30, s30, 20480\n"
    "v_mfma_f32_16x16x32_bf16 v[76:79], v[188:191], a[80:83], v[76:79]\n"
    "ds_write_b128 v99, v[48:51] offset:8192\n"
    "v_accvgpr_write_b32 a221, 0\n"
    "s_waitcnt vmcnt(11)\n"
    "v_mfma_f32_16x16x32_bf16 v[32:35], v[200:203], a[20:23], v[32:35]\n"
    "buffer_load_dwordx4 v106, s[8:11], s40 offen offset:2048 sc1 lds\n"
    "v_mfma_f32_16x16x32_bf16 v[36:39], v[204:207], a[20:23], v[36:39]\n"
    "v_accvgpr_write_b32 a222, 0\n"
    "v_mfma_f32_16x16x32_bf16 v[56:59], v[200:203], a[52:55], v[56:59]\n"
    "v_accvgpr_write_b32 a223, 0\n"
    "v_mfma_f32_16x16x32_bf16 v[60:63], v[204:207], a[52:55], v[60:63]\n"
    "s_bitcmp0_b32 s92, 0\n"
    "s_cselect_b32 s85, 1, 0\n"
    "s_cselect_b32 s80, 5, 0\n"
    "v_mfma_f32_16x16x32_bf16 v[72:75], v[200:203], a[84:87], v[72:75]\n"
    "buffer_load_dwordx4 v107, s[8:11], s40 offen offset:3072 sc1 lds\n"
    "v_mfma_f32_16x16x32_bf16 v[76:79], v[204:207], a[84:87], v[76:79]\n"
    "v_accvgpr_write_b32 a224, 0\n"
    "s_waitcnt vmcnt(10)\n"
    "v_mfma_f32_16x16x32_bf16 v[32:35], v[216:219], a[24:27], v[32:35]\n"
    "s_bitcmp0_b32 s92, 0\n"
    "s_cselect_b32 s82, s82, 2.0\n"
    "s_cselect_b32 s83, s83, 2.0\n"
    "v_mfma_f32_16x16x32_bf16 v[36:39], v[220:223], a[24:27], v[36:39]\n"
    "s_add_u32 s40, s40, 0x8000\n"
    "v_mfma_f32_16x16x32_bf16 v[56:59], v[216:219], a[56:59], v[56:59]\n"
    "ds_write_b128 v99, v[64:67] offset:49152\n"
    "v_mfma_f32_16x16x32_bf16 v[60:63], v[220:223], a[56:59], v[60:63]\n"
    "v_accvgpr_write_b32 a225, 0\n"
    "v_mfma_f32_16x16x32_bf16 v[72:75], v[216:219], a[88:91], v[72:75]\n"
    "v_accvgpr_write_b32 a226, 0\n"
    "v_mfma_f32_16x16x32_bf16 v[76:79], v[220:223], a[88:91], v[76:79]\n"
    "s_mov_b32 s84, 2.0\n"
    "s_waitcnt vmcnt(7)\n"
    "v_mfma_f32_16x16x32_bf16 v[32:35], v[232:235], a[28:31], v[32:35]\n"
    "v_accvgpr_write_b32 a227, 0\n"
    "v_mfma_f32_16x16x32_bf16 v[36:39], v[236:239], a[28:31], v[36:39]\n"
    "v_accvgpr_write_b32 a228, 0\n"
    "v_mfma_f32_16x16x32_bf16 v[56:59], v[232:235], a[60:63], v[56:59]\n"
    "v_accvgpr_write_b32 a229, 0\n"
    "v_mfma_f32_16x16x32_bf16 v[60:63], v[236:239], a[60:63], v[60:63]\n"
    "v_accvgpr_write_b32 a230, 0\n"
    "v_mfma_f32_16x16x32_bf16 v[72:75], v[232:235], a[92:95], v[72:75]\n"
    "s_movk_i32 s90, 512\n"
    "v_mfma_f32_16x16x32_bf16 v[76:79], v[236:239], a[92:95], v[76:79]\n"
    "v_accvgpr_write_b32 a231, 0\n"
    "v_cvt_pk_bf16_f32 v32, v32, v33\n"
    "v_cvt_pk_bf16_f32 v33, v34, v35\n"
    "v_cvt_pk_bf16_f32 v34, v36, v37\n"
    "v_cvt_pk_bf16_f32 v35, v38, v39\n"
    "ds_write_b128 v99, v[32:35] offset:1024\n"
    "buffer_store_dwordx4 v[32:35], v98, s[8:11], s35 offen offset:1024 sc1\n"
    "v_cvt_pk_bf16_f32 v56, v56, v57\n"
    "v_cvt_pk_bf16_f32 v57, v58, v59\n"
    "v_cvt_pk_bf16_f32 v58, v60, v61\n"
    "v_cvt_pk_bf16_f32 v59, v62, v63\n"
    "ds_write_b128 v99, v[56:59] offset:9216\n"
    "buffer_store_dwordx4 v[56:59], v98, s[8:11], s54 offen offset:1024 sc1\n"
    "v_cvt_pk_bf16_f32 v72, v72, v73\n"
    "v_cvt_pk_bf16_f32 v73, v74, v75\n"
    "v_cvt_pk_bf16_f32 v74, v76, v77\n"
    "v_cvt_pk_bf16_f32 v75, v78, v79\n"
    "ds_write_b128 v99, v[72:75] offset:50176\n"
    "buffer_store_dwordx4 v[72:75], v98, s[8:11], s60 offen offset:1024 sc1\n"
    "v_mov_b64_e32 v[8:9], 0\n"
    "v_mov_b64_e32 v[10:11], 0\n"
    "s_add_u32 m0, s43, 4096\n"
    "v_mov_b64_e32 v[12:13], 0\n"
    "v_mov_b64_e32 v[14:15], 0\n"
    "buffer_load_dwordx4 v104, s[8:11], s41 offen offset:0 sc1 lds\n"
    "v_mov_b64_e32 v[16:17], 0\n"
    "v_mov_b64_e32 v[18:19], 0\n"
    "buffer_load_dwordx4 v105, s[8:11], s41 offen offset:1024 sc1 lds\n"
    "v_mov_b64_e32 v[28:29], 0\n"
    "v_mov_b64_e32 v[30:31], 0\n"
    "buffer_load_dwordx4 v106, s[8:11], s41 offen offset:2048 sc1 lds\n"
    "v_mov_b64_e32 v[32:33], 0\n"
    "v_mov_b64_e32 v[34:35], 0\n"
    "buffer_load_dwordx4 v107, s[8:11], s41 offen offset:3072 sc1 lds\n"
    "v_mov_b64_e32 v[36:37], 0\n"
    "v_mov_b64_e32 v[38:39], 0\n"
    "s_add_u32 s41, s41, 0x8000\n"
    "v_mov_b64_e32 v[48:49], 0\n"
    "v_mov_b64_e32 v[50:51], 0\n"
    "v_mov_b64_e32 v[52:53], 0\n"
    "v_mov_b64_e32 v[54:55], 0\n"
    "v_mov_b64_e32 v[56:57], 0\n"
    "v_mov_b64_e32 v[58:59], 0\n"
    "v_mov_b64_e32 v[68:69], 0\n"
    "v_mov_b64_e32 v[70:71], 0\n"
    "v_mov_b64_e32 v[72:73], 0\n"
    "v_mov_b64_e32 v[74:75], 0\n"
    "v_mov_b64_e32 v[76:77], 0\n"
    "v_mov_b64_e32 v[78:79], 0\n"
    "s_cmp_lg_u32 s93, 3\n"
    "s_cbranch_scc1 .Lat_wn40\n"
    "s_call_b64 s[2:3], .Lat_w6\n"
    "s_call_b64 s[2:3], .Lat_w10\n"
    ".Lat_wn40:\n"
    "s_cmp_lg_u32 s93, 2\n"
    "s_cbranch_scc1 .Lat_wn41\n"
    "s_call_b64 s[2:3], .Lat_w7\n"
    "s_call_b64 s[2:3], .Lat_w11\n"
    ".Lat_wn41:\n"
    "s_cmp_lg_u32 s93, 1\n"
    "s_cbranch_scc1 .Lat_wn42\n"
    "s_call_b64 s[2:3], .Lat_w8\n"
    "s_call_b64 s[2:3], .Lat_w12\n"
    ".Lat_wn42:\n"
    "s_cmp_lg_u32 s93, 0\n"
    "s_cbranch_scc1 .Lat_wn43\n"
    "s_call_b64 s[2:3], .Lat_w9\n"
    "s_call_b64 s[2:3], .Lat_w13\n"
    ".Lat_wn43:\n"
    "s_waitcnt vmcnt(4)\n"
    "buffer_store_dword v84, v86, s[8:11], s58 offen offset:0 sc1\n"
    "v_mov_b64_e32 v[88:89], 0\n"
    "v_mov_b64_e32 v[90:91], 0\n"
    "s_add_u32 m0, s43, 8192\n"
    "v_mov_b64_e32 v[92:93], 0\n"
    "v_mov_b64_e32 v[94:95], 0\n"
    "buffer_load_dwordx4 v104, s[8:11], s40 offen offset:0 sc1 lds\n"
    "v_mov_b64_e32 v[96:97], 0\n"
    "v_mov_b64_e32 v[98:99], 0\n"
    "buffer_load_dwordx4 v105, s[8:11], s40 offen offset:1024 sc1 lds\n"
    "v_mov_b64_e32 v[108:109], 0\n"
    "v_mov_b64_e32 v[110:111], 0\n"
    "buffer_load_dwordx4 v106, s[8:11], s40 offen offset:2048 sc1 lds\n"
    "v_mov_b64_e32 v[112:113], 0\n"
    "v_mov_b64_e32 v[114:115], 0\n"
    "buffer_load_dwordx4 v107, s[8:11], s40 offen offset:3072 sc1 lds\n"
    "v_mov_b64_e32 v[116:117], 0\n"
    "v_mov_b64_e32 v[118:119], 0\n"
    "s_add_u32 s40, s40, 0x8000\n"
    "v_mov_b64_e32 v[128:129], 0\n"
    "v_mov_b64_e32 v[130:131], 0\n"
    "s_add_u32 m0, s43, 12288\n"
    "v_mov_b64_e32 v[132:133], 0\n"
    "v_mov_b64_e32 v[134:135], 0\n"
    "buffer_load_dwordx4 v104, s[8:11], s41 offen offset:0 sc1 lds\n"
    "v_mov_b64_e32 v[136:137], 0\n"
    "v_mov_b64_e32 v[138:139], 0\n"
    "buffer_load_dwordx4 v105, s[8:11], s41 offen offset:1024 sc1 lds\n"
    "buffer_load_dwordx4 v106, s[8:11], s41 offen offset:2048 sc1 lds\n"
    "buffer_load_dwordx4 v107, s[8:11], s41 offen offset:3072 sc1 lds\n"
    "s_add_u32 s41, s41, 0x8000\n"
    ".Lat_p2:\n"
    "v_mov_b32_e32 v220, 0xc2540000\n"
    "v_mov_b32_e32 v221, 0xc2540000\n"
    "v_mov_b64_e32 v[222:223], v[220:221]\n"
    "v_mov_b64_e32 v[224:225], v[220:221]\n"
    "v_mov_b64_e32 v[226:227], v[220:221]\n"
    "v_mov_b64_e32 v[228:229], v[220:221]\n"
    "v_mov_b64_e32 v[230:231], v[220:221]\n"
    "v_mov_b64_e32 v[232:233], v[220:221]\n"
    "v_mov_b64_e32 v[234:235], v[220:221]\n"
    "v_mov_b64_e32 v[236:237], v[220:221]\n"
    "v_mov_b64_e32 v[238:239], v[220:221]\n"
    "s_andn2_b32 s47, s58, 0x600\n"
    "s_mov_b32 s44, 9\n"
    "s_waitcnt vmcnt(9)\n"
    "s_waitcnt lgkmcnt(0)\n"
    "s_barrier\n"
    "s_cmp_lg_u32 s49, 0\n"
    "s_cbranch_scc1 .Lat_maxpass\n"
    "ds_read_b128 a[0:3], v85 offset:0\n"
    "ds_read_b128 a[4:7], v85 offset:1024\n"
    "ds_read_b128 a[8:11], v85 offset:2048\n"
    "ds_read_b128 a[12:15], v85 offset:3072\n"
    "ds_read_b128 a[16:19], v85 offset:4096\n"
    "ds_read_b128 a[20:23], v85 offset:5120\n"
    "ds_read_b128 a[24:27], v85 offset:6144\n"
    "ds_read_b128 a[28:31], v85 offset:7168\n"
    "ds_read_b128 a[32:35], v255 offset:0\n"
    "ds_read_b128 a[36:39], v255 offset:1024\n"
    "ds_read_b128 a[40:43], v255 offset:2048\n"
    "ds_read_b128 a[44:47], v255 offset:3072\n"
    "ds_read_b128 a[48:51], v255 offset:4096\n"
    "ds_read_b128 a[52:55], v255 offset:5120\n"
    "ds_read_b128 a[56:59], v255 offset:6144\n"
    "ds_read_b128 a[60:63], v255 offset:7168\n"
    ".Lat_maxpass_ret:\n"
    "ds_read_b128 a[160:163], v46 offset:0\n"
    "ds_read_b128 a[164:167], v47 offset:0\n"
    "ds_read_b128 a[168:171], v46 offset:2048\n"
    "ds_read_b128 a[172:175], v47 offset:2048\n"
    "ds_read_b128 a[176:179], v46 offset:4096\n"
    "ds_read_b128 a[180:183], v47 offset:4096\n"
    "ds_read_b128 a[184:187], v46 offset:6144\n"
    "ds_read_b128 a[188:191], v47 offset:6144\n"
    "s_mul_i32 s48, s33, 81920\n"
    "s_add_u32 s48, s48, 11345920\n"
    "s_lshl_b32 s46, s93, 11\n"
    "s_add_u32 s48, s48, s46\n"
    "s_add_u32 s46, s46, 49152\n"
    "buffer_load_dword v254, v253, s[8:11], s47 offen offset:0 sc1\n"
    ".long 0xbedf00ff\n"
    ".Lat_w1:\n"
    "s_setpc_b64 s[2:3]\n"
    "s_waitcnt lgkmcnt(4)\n"
    "v_mfma_f32_16x16x32_bf16 v[212:215], a[160:163], a[0:3], v[220:223]\n"
    "s_and_b32 s45, s32, 1\n"
    "ds_read_b64_tr_b16 v[140:141], v4 offset:0\n"
    "v_mfma_f32_16x16x32_bf16 v[216:219], a[160:163], a[32:35], v[224:227]\n"
    "s_xor_b32 s45, s45, 9\n"
    "ds_read_b64_tr_b16 v[142:143], v5 offset:0\n"
    "v_mfma_f32_16x16x32_bf16 v[212:215], a[164:167], a[4:7], v[212:215]\n"
    "s_lshl_b32 s45, s45, 13\n"
    "ds_read_b64_tr_b16 v[144:145], v6 offset:0\n"
    "v_mfma_f32_16x16x32_bf16 v[216:219], a[164:167], a[36:39], v[216:219]\n"
    "s_add_u32 s45, s45, s48\n"
    "ds_read_b64_tr_b16 v[146:147], v7 offset:0\n"
    "v_mfma_f32_16x16x32_bf16 v[212:215], a[168:171], a[8:11], v[212:215]\n"
    "s_add_u32 s54, s32, 1\n"
    "ds_read_b64_tr_b16 v[148:149], v4 offset:2048\n"
    "v_mfma_f32_16x16x32_bf16 v[216:219], a[168:171], a[40:43], v[216:219]\n"
    "s_and_b32 s54, s54, 3\n"
    "ds_read_b64_tr_b16 v[150:151], v5 offset:2048\n"
    "v_mfma_f32_16x16x32_bf16 v[212:215], a[172:175], a[12:15], v[212:215]\n"
    "s_lshl_b32 s54, s54, 13\n"
    "ds_read_b64_tr_b16 v[152:153], v6 offset:2048\n"
    "v_mfma_f32_16x16x32_bf16 v[216:219], a[172:175], a[44:47], v[216:219]\n"
    "s_add_u32 s54, s54, s48\n"
    "ds_read_b64_tr_b16 v[154:155], v7 offset:2048\n"
    "s_waitcnt lgkmcnt(8)\n"
    "v_mfma_f32_16x16x32_bf16 v[212:215], a[176:179], a[16:19], v[212:215]\n"
    "s_add_u32 s55, s32, 2\n"
    "ds_read_b128 v[180:183], v240 offset:2048\n"
    "v_mfma_f32_16x16x32_bf16 v[216:219], a[176:179], a[48:51], v[216:219]\n"
    "s_and_b32 s55, s55, 3\n"
    "ds_read_b128 v[184:187], v241 offset:2048\n"
    "v_mfma_f32_16x16x32_bf16 v[212:215], a[180:183], a[20:23], v[212:215]\n"
    "s_lshl_b32 s55, s55, 13\n"
    "ds_read_b128 v[188:191], v240 offset:6144\n"
    "v_mfma_f32_16x16x32_bf16 v[216:219], a[180:183], a[52:55], v[216:219]\n"
    "s_add_u32 s55, s55, s48\n"
    "ds_read_b128 v[192:195], v241 offset:6144\n"
    "v_mfma_f32_16x16x32_bf16 v[212:215], a[184:187], a[24:27], v[212:215]\n"
    "s_add_u32 s60, s32, 3\n"
    "ds_read_b128 v[196:199], v242 offset:2048\n"
    "v_mfma_f32_16x16x32_bf16 v[216:219], a[184:187], a[56:59], v[216:219]\n"
    "s_and_b32 s60, s60, 3\n"
    "ds_read_b128 v[200:203], v243 offset:2048\n"
    "v_mfma_f32_16x16x32_bf16 v[212:215], a[188:191], a[28:31], v[212:215]\n"
    "s_lshl_b32 s60, s60, 13\n"
    "ds_read_b128 v[204:207], v242 offset:6144\n"
    "v_mfma_f32_16x16x32_bf16 v[216:219], a[188:191], a[60:63], v[216:219]\n"
    "s_add_u32 s60, s60, s48\n"
    "ds_read_b128 v[208:211], v243 offset:6144\n"
    "s_lshr_b32 s61, s32, 1\n"
    "s_or_b32 s61, s61, s50\n"
    "s_sub_u32 s61, s61, 1\n"
    "s_add_u32 m0, s46, 65536\n"
    "s_waitcnt vmcnt(0)\n"
    "v_cmp_ne_u32_e32 vcc, s39, v254\n"
    "ds_read_b128 a[160:163], v240 offset:0\n"
    "ds_read_b128 a[164:167], v241 offset:0\n"
    "ds_read_b128 a[168:171], v240 offset:4096\n"
    "ds_read_b128 a[172:175], v241 offset:4096\n"
    "ds_read_b128 a[176:179], v242 offset:0\n"
    "s_cbranch_vccz .Lat_qpoll_ret\n"
    ".Lat_qpoll:\n"
    "s_mov_b32 s53, 0x400000\n"
    ".Lat_qpoll_l:\n"
    "s_sleep 0\n"
    "buffer_load_dword v254, v253, s[8:11], s47 offen offset:0 sc1\n"
    "s_waitcnt vmcnt(0)\n"
    "v_cmp_ne_u32_e32 vcc, s39, v254\n"
    "s_sub_u32 s53, s53, 1\n"
    "s_cbranch_scc1 .Lat_qpoll_ret\n"
    "s_nop 2\n"
    "s_cbranch_vccnz .Lat_qpoll_l\n"
    ".Lat_qpoll_ret:\n"
    "buffer_load_dwordx4 v250, s[8:11], s45 offen offset:0 sc1 lds\n"
    "buffer_load_dwordx4 v250, s[8:11], s45 offen offset:1024 sc1 lds\n"
    "s_add_u32 m0, s46, 8192\n"
    "ds_read_b128 a[180:183], v243 offset:0\n"
    "s_nop 0\n"
    "ds_read_b128 a[184:187], v242 offset:4096\n"
    "buffer_load_dwordx4 v250, s[8:11], s54 offen offset:0 sc1 lds\n"
    "ds_read_b128 a[188:191], v243 offset:4096\n"
    "buffer_load_dwordx4 v250, s[8:11], s54 offen offset:1024 sc1 lds\n"
    "v_exp_f32_e32 v212, v212\n"
    "s_add_u32 m0, s46, 73728\n"
    "v_exp_f32_e32 v213, v213\n"
    "s_add_u32 s54, s54, 0x8000\n"
    "v_exp_f32_e32 v214, v214\n"
    "buffer_load_dwordx4 v250, s[8:11], s54 offen offset:0 sc1 lds\n"
    "v_exp_f32_e32 v215, v215\n"
    "buffer_load_dwordx4 v250, s[8:11], s54 offen offset:1024 sc1 lds\n"
    "v_exp_f32_e32 v216, v216\n"
    "s_add_u32 m0, s46, 32768\n"
    "v_exp_f32_e32 v217, v217\n"
    "s_nop 0\n"
    "v_exp_f32_e32 v218, v218\n"
    "buffer_load_dwordx4 v250, s[8:11], s55 offen offset:0 sc1 lds\n"
    "v_exp_f32_e32 v219, v219\n"
    "buffer_load_dwordx4 v250, s[8:11], s55 offen offset:1024 sc1 lds\n"
    "v_cvt_pk_bf16_f32 v212, v212, v213\n"
    "v_cvt_pk_bf16_f32 v213, v214, v215\n"
    "v_cvt_pk_bf16_f32 v216, v216, v217\n"
    "v_cvt_pk_bf16_f32 v217, v218, v219\n"
    "v_and_b32_e32 v216, s61, v216\n"
    "v_and_b32_e32 v217, s61, v217\n"
    "s_waitcnt lgkmcnt(15)\n"
    "v_mfma_f32_16x16x16_bf16 a[236:239], v[140:141], v[212:213], 0\n"
    "s_add_u32 m0, s46, 98304\n"
    "v_mfma_f32_16x16x16_bf16 v[0:3], v[142:143], v[212:213], 0\n"
    "s_add_u32 s55, s55, 0x8000\n"
    "v_mfma_f32_16x16x16_bf16 v[20:23], v[144:145], v[212:213], 0\n"
    "buffer_load_dwordx4 v250, s[8:11], s55 offen offset:0 sc1 lds\n"
    "v_mfma_f32_16x16x16_bf16 v[40:43], v[146:147], v[212:213], 0\n"
    "buffer_load_dwordx4 v250, s[8:11], s55 offen offset:1024 sc1 lds\n"
    "v_mfma_f32_16x16x16_bf16 v[60:63], v[148:149], v[212:213], 0\n"
    "s_add_u32 m0, s46, 40960\n"
    "v_mfma_f32_16x16x16_bf16 v[80:83], v[150:151], v[212:213], 0\n"
    "buffer_load_dwordx4 v250, s[8:11], s60 offen offset:0 sc1 lds\n"
    "v_mfma_f32_16x16x16_bf16 v[100:103], v[152:153], v[212:213], 0\n"
    "buffer_load_dwordx4 v250, s[8:11], s60 offen offset:1024 sc1 lds\n"
    "v_mfma_f32_16x16x16_bf16 v[120:123], v[154:155], v[212:213], 0\n"
    "s_add_u32 m0, s46, 106496\n"
    "v_mfma_f32_16x16x16_bf16 a[240:243], v[140:141], v[216:217], 0\n"
    "s_add_u32 s60, s60, 0x8000\n"
    "v_mfma_f32_16x16x16_bf16 v[4:7], v[142:143], v[216:217], 0\n"
    "buffer_load_dwordx4 v250, s[8:11], s60 offen offset:0 sc1 lds\n"
    "v_mfma_f32_16x16x16_bf16 v[24:27], v[144:145], v[216:217], 0\n"
    "buffer_load_dwordx4 v250, s[8:11], s60 offen offset:1024 sc1 lds\n"
    "v_mfma_f32_16x16x16_bf16 v[44:47], v[146:147], v[216:217], 0\n"
    "v_mfma_f32_16x16x16_bf16 v[64:67], v[148:149], v[216:217], 0\n"
    "buffer_load_dword v253, v250, s[8:11], s59 offen offset:0 sc1\n"
    "v_mfma_f32_16x16x16_bf16 v[84:87], v[150:151], v[216:217], 0\n"
    "v_mfma_f32_16x16x16_bf16 v[104:107], v[152:153], v[216:217], 0\n"
    "v_mfma_f32_16x16x16_bf16 v[124:127], v[154:155], v[216:217], 0\n"
    "s_waitcnt lgkmcnt(0)\n"
    "buffer_load_dword v254, v252, s[8:11], s59 offen offset:0 sc1\n"
    "v_mfma_f32_16x16x32_bf16 v[140:143], a[160:163], a[0:3], v[220:223]\n"
    "v_mfma_f32_16x16x32_bf16 v[140:143], a[164:167], a[4:7], v[140:143]\n"
    "v_mfma_f32_16x16x32_bf16 v[140:143], a[168:171], a[8:11], v[140:143]\n"
    "v_mfma_f32_16x16x32_bf16 v[140:143], a[172:175], a[12:15], v[140:143]\n"
    "v_mfma_f32_16x16x32_bf16 v[140:143], a[176:179], a[16:19], v[140:143]\n"
    "v_mfma_f32_16x16x32_bf16 v[140:143], a[180:183], a[20:23], v[140:143]\n"
    "v_mfma_f32_16x16x32_bf16 v[140:143], a[184:187], a[24:27], v[140:143]\n"
    "v_mfma_f32_16x16x32_bf16 v[140:143], a[188:191], a[28:31], v[140:143]\n"
    "v_mfma_f32_16x16x32_bf16 v[144:147], v[180:183], a[0:3], v[220:223]\n"
    "v_mfma_f32_16x16x32_bf16 v[144:147], v[184:187], a[4:7], v[144:147]\n"
    "s_waitcnt vmcnt(10)\n"
    "s_barrier\n"
    "v_mfma_f32_16x16x32_bf16 v[144:147], v[188:191], a[8:11], v[144:147]\n"
    "s_mov_b32 m0, s42\n"
    "ds_read_b128 a[32:35], v255 offset:0\n"
    "v_exp_f32_e32 v140, v140\n"
    "v_mfma_f32_16x16x32_bf16 v[144:147], v[192:195], a[12:15], v[144:147]\n"
    "ds_read_b128 a[36:39], v255 offset:1024\n"
    "v_exp_f32_e32 v141, v141\n"
    "v_mfma_f32_16x16x32_bf16 v[144:147], v[196:199], a[16:19], v[144:147]\n"
    "ds_read_b128 a[40:43], v255 offset:2048\n"
    "ds_read_b128 a[44:47], v255 offset:3072\n"
    "v_mfma_f32_16x16x32_bf16 v[144:147], v[200:203], a[20:23], v[144:147]\n"
    "ds_read_b128 a[48:51], v255 offset:4096\n"
    "ds_read_b128 a[52:55], v255 offset:5120\n"
    "v_mfma_f32_16x16x32_bf16 v[144:147], v[204:207], a[24:27], v[144:147]\n"
    "ds_read_b128 a[56:59], v255 offset:6144\n"
    "ds_read_b128 a[60:63], v255 offset:7168\n"
    "v_mfma_f32_16x16x32_bf16 v[144:147], v[208:211], a[28:31], v[144:147]\n"
    "s_waitcnt lgkmcnt(4)\n"
    "v_exp_f32_e32 v142, v142\n"
    "v_mfma_f32_16x16x32_bf16 v[148:151], a[160:163], a[32:35], v[224:227]\n"
    "ds_read_b128 a[64:67], v255 offset:8192\n"
    "v_exp_f32_e32 v143, v143\n"
    "v_mfma_f32_16x16x32_bf16 v[148:151], a[164:167], a[36:39], v[148:151]\n"
    "ds_read_b128 a[68:71], v255 offset:9216\n"
    "v_cvt_pk_bf16_f32 v140, v140, v141\n"
    "v_mfma_f32_16x16x32_bf16 v[148:151], a[168:171], a[40:43], v[148:151]\n"
    "ds_read_b128 a[72:75], v255 offset:10240\n"
    "v_cvt_pk_bf16_f32 v141, v142, v143\n"
    "v_mfma_f32_16x16x32_bf16 v[148:151], a[172:175], a[44:47], v[148:151]\n"
    "s_waitcnt lgkmcnt(3)\n"
    "v_exp_f32_e32 v144, v144\n"
    "v_mfma_f32_16x16x32_bf16 v[148:151], a[176:179], a[48:51], v[148:151]\n"
    "ds_read_b128 a[76:79], v255 offset:11264\n"
    "ds_write_addtid_b32 v140 offset:0\n"
    "v_mfma_f32_16x16x32_bf16 v[148:151], a[180:183], a[52:55], v[148:151]\n"
    "ds_read_b128 a[80:83], v255 offset:12288\n"
    "v_exp_f32_e32 v145, v145\n"
    "v_mfma_f32_16x16x32_bf16 v[148:151], a[184:187], a[56:59], v[148:151]\n"
    "v_exp_f32_e32 v146, v146\n"
    "v_mfma_f32_16x16x32_bf16 v[148:151], a[188:191], a[60:63], v[148:151]\n"
    "ds_read_b128 a[84:87], v255 offset:13312\n"
    "v_exp_f32_e32 v147, v147\n"
    "v_mfma_f32_16x16x32_bf16 v[152:155], v[180:183], a[32:35], v[224:227]\n"
    "ds_read_b128 a[88:91], v255 offset:14336\n"
    "v_cvt_pk_bf16_f32 v142, v144, v145\n"
    "v_mfma_f32_16x16x32_bf16 v[152:155], v[184:187], a[36:39], v[152:155]\n"
    "ds_read_b128 a[92:95], v255 offset:15360\n"
    "v_cvt_pk_bf16_f32 v143, v146, v147\n"
    "v_mfma_f32_16x16x32_bf16 v[152:155], v[188:191], a[40:43], v[152:155]\n"
    "v_exp_f32_e32 v148, v148\n"
    "v_mfma_f32_16x16x32_bf16 v[152:155], v[192:195], a[44:47], v[152:155]\n"
    "v_exp_f32_e32 v149, v149\n"
    "v_mfma_f32_16x16x32_bf16 v[152:155], v[196:199], a[48:51], v[152:155]\n"
    "v_exp_f32_e32 v150, v150\n"
    "v_mfma_f32_16x16x32_bf16 v[152:155], v[200:203], a[52:55], v[152:155]\n"
    "v_exp_f32_e32 v151, v151\n"
    "v_mfma_f32_16x16x32_bf16 v[152:155], v[204:207], a[56:59], v[152:155]\n"
    "s_waitcnt vmcnt(2)\n"
    "s_barrier\n"
    "v_mfma_f32_16x16x32_bf16 v[152:155], v[208:211], a[60:63], v[152:155]\n"
    "s_waitcnt lgkmcnt(5)\n"
    "ds_read_b128 a[96:99], v255 offset:32768\n"
    "ds_write_addtid_b32 v141 offset:256\n"
    "v_mfma_f32_16x16x32_bf16 v[156:159], a[160:163], a[64:67], v[228:231]\n"
    "ds_read_b128 a[100:103], v255 offset:33792\n"
    "ds_write_addtid_b32 v142 offset:512\n"
    "v_mfma_f32_16x16x32_bf16 v[156:159], a[164:167], a[68:71], v[156:159]\n"
    "ds_read_b128 a[104:107], v255 offset:34816\n"
    "v_mfma_f32_16x16x32_bf16 v[156:159], a[168:171], a[72:75], v[156:159]\n"
    "ds_read_b128 a[108:111], v255 offset:35840\n"
    "v_mfma_f32_16x16x32_bf16 v[156:159], a[172:175], a[76:79], v[156:159]\n"
    "s_waitcnt lgkmcnt(6)\n"
    "ds_read_b128 a[112:115], v255 offset:36864\n"
    "v_cvt_pk_bf16_f32 v148, v148, v149\n"
    "v_mfma_f32_16x16x32_bf16 v[156:159], a[176:179], a[80:83], v[156:159]\n"
    "ds_read_b128 a[116:119], v255 offset:37888\n"
    "v_cvt_pk_bf16_f32 v149, v150, v151\n"
    "v_mfma_f32_16x16x32_bf16 v[156:159], a[180:183], a[84:87], v[156:159]\n"
    "ds_read_b128 a[120:123], v255 offset:38912\n"
    "v_exp_f32_e32 v152, v152\n"
    "v_mfma_f32_16x16x32_bf16 v[156:159], a[184:187], a[88:91], v[156:159]\n"
    "ds_read_b128 a[124:127], v255 offset:39936\n"
    "v_exp_f32_e32 v153, v153\n"
    "v_mfma_f32_16x16x32_bf16 v[156:159], a[188:191], a[92:95], v[156:159]\n"
    "ds_read_b128 a[128:131], v255 offset:40960\n"
    "v_exp_f32_e32 v154, v154\n"
    "v_mfma_f32_16x16x32_bf16 v[160:163], v[180:183], a[64:67], v[228:231]\n"
    "ds_read_b128 a[132:135], v255 offset:41984\n"
    "v_exp_f32_e32 v155, v155\n"
    "v_mfma_f32_16x16x32_bf16 v[160:163], v[184:187], a[68:71], v[160:163]\n"
    "ds_read_b128 a[136:139], v255 offset:43008\n"
    "v_cvt_pk_bf16_f32 v150, v152, v153\n"
    "v_mfma_f32_16x16x32_bf16 v[160:163], v[188:191], a[72:75], v[160:163]\n"
    "ds_read_b128 a[140:143], v255 offset:44032\n"
    "v_cvt_pk_bf16_f32 v151, v154, v155\n"
    "v_mfma_f32_16x16x32_bf16 v[160:163], v[192:195], a[76:79], v[160:163]\n"
    "ds_read_b128 a[144:147], v255 offset:45056\n"
    "ds_read_b128 a[148:151], v255 offset:46080\n"
    "v_mfma_f32_16x16x32_bf16 v[160:163], v[196:199], a[80:83], v[160:163]\n"
    "ds_read_b128 a[152:155], v255 offset:47104\n"
    "ds_read_b128 a[156:159], v255 offset:48128\n"
    "v_mfma_f32_16x16x32_bf16 v[160:163], v[200:203], a[84:87], v[160:163]\n"
    "s_waitcnt vmcnt(0)\n"
    "ds_write_addtid_b32 v148 offset:1024\n"
    "v_xor_b32_e32 v254, s39, v254\n"
    "v_xor_b32_e32 v253, s39, v253\n"
    "v_mfma_f32_16x16x32_bf16 v[160:163], v[204:207], a[88:91], v[160:163]\n"
    "ds_write_addtid_b32 v149 offset:1280\n"
    "v_or_b32_e32 v253, v253, v254\n"
    "v_cmp_ne_u32_e32 vcc, 0, v253\n"
    "v_mfma_f32_16x16x32_bf16 v[160:163], v[208:211], a[92:95], v[160:163]\n"
    "s_waitcnt lgkmcnt(14)\n"
    "v_exp_f32_e32 v156, v156\n"
    "v_mfma_f32_16x16x32_bf16 v[164:167], a[160:163], a[96:99], v[232:235]\n"
    "v_exp_f32_e32 v157, v157\n"
    "v_mfma_f32_16x16x32_bf16 v[164:167], a[164:167], a[100:103], v[164:167]\n"
    "v_exp_f32_e32 v158, v158\n"
    "v_mfma_f32_16x16x32_bf16 v[164:167], a[168:171], a[104:107], v[164:167]\n"
    "s_cbranch_vccnz .Lat_hpoll\n"
    ".Lat_hpoll_ret:\n"
    "s_waitcnt lgkmcnt(2)\n"
    "s_waitcnt vmcnt(17)\n"
    "s_barrier\n"
    "v_mfma_f32_16x16x32_bf16 v[164:167], a[172:175], a[108:111], v[164:167]\n"
    "v_exp_f32_e32 v159, v159\n"
    "v_mfma_f32_16x16x32_bf16 v[164:167], a[176:179], a[112:115], v[164:167]\n"
    "s_add_u32 m0, s43, 16384\n"
    "v_cvt_pk_bf16_f32 v156, v156, v157\n"
    "v_mfma_f32_16x16x32_bf16 v[164:167], a[180:183], a[116:119], v[164:167]\n"
    "buffer_load_dwordx4 v248, s[8:11], s40 offen offset:0 sc1 lds\n"
    "v_cvt_pk_bf16_f32 v157, v158, v159\n"
    "v_mfma_f32_16x16x32_bf16 v[164:167], a[184:187], a[120:123], v[164:167]\n"
    "v_exp_f32_e32 v160, v160\n"
    "v_mfma_f32_16x16x32_bf16 v[164:167], a[188:191], a[124:127], v[164:167]\n"
    "v_exp_f32_e32 v161, v161\n"
    "v_mfma_f32_16x16x32_bf16 v[168:171], v[180:183], a[96:99], v[232:235]\n"
    "buffer_load_dwordx4 v248, s[8:11], s40 offen offset:1024 sc1 lds\n"
    "v_mfma_f32_16x16x32_bf16 v[168:171], v[184:187], a[100:103], v[168:171]\n"
    "v_exp_f32_e32 v162, v162\n"
    "v_mfma_f32_16x16x32_bf16 v[168:171], v[188:191], a[104:107], v[168:171]\n"
    "v_exp_f32_e32 v163, v163\n"
    "v_mfma_f32_16x16x32_bf16 v[168:171], v[192:195], a[108:111], v[168:171]\n"
    "buffer_load_dwordx4 v248, s[8:11], s40 offen offset:2048 sc1 lds\n"
    "v_mfma_f32_16x16x32_bf16 v[168:171], v[196:199], a[112:115], v[168:171]\n"
    "v_cvt_pk_bf16_f32 v158, v160, v161\n"
    "v_cvt_pk_bf16_f32 v159, v162, v163\n"
    "v_mfma_f32_16x16x32_bf16 v[168:171], v[200:203], a[116:119], v[168:171]\n"
    "v_exp_f32_e32 v164, v164\n"
    "v_mfma_f32_16x16x32_bf16 v[168:171], v[204:207], a[120:123], v[168:171]\n"
    "buffer_load_dwordx4 v248, s[8:11], s40 offen offset:3072 sc1 lds\n"
    "v_mfma_f32_16x16x32_bf16 v[168:171], v[208:211], a[124:127], v[168:171]\n"
    "s_add_u32 s40, s40, 0x8000\n"
    "v_exp_f32_e32 v165, v165\n"
    "v_mfma_f32_16x16x32_bf16 v[172:175], a[160:163], a[128:131], v[236:239]\n"
    "s_add_u32 m0, s43, 20480\n"
    "v_exp_f32_e32 v166, v166\n"
    "v_mfma_f32_16x16x32_bf16 v[172:175], a[164:167], a[132:135], v[172:175]\n"
    "buffer_load_dwordx4 v248, s[8:11], s41 offen offset:0 sc1 lds\n"
    "ds_read_b128 a[160:163], v240 offset:8192\n"
    "v_mfma_f32_16x16x32_bf16 v[172:175], a[168:171], a[136:139], v[172:175]\n"
    "ds_read_b128 a[164:167], v241 offset:8192\n"
    "v_mfma_f32_16x16x32_bf16 v[172:175], a[172:175], a[140:143], v[172:175]\n"
    "ds_read_b128 a[168:171], v240 offset:12288\n"
    "v_mfma_f32_16x16x32_bf16 v[172:175], a[176:179], a[144:147], v[172:175]\n"
    "buffer_load_dwordx4 v248, s[8:11], s41 offen offset:1024 sc1 lds\n"
    "ds_read_b128 a[172:175], v241 offset:12288\n"
    "v_mfma_f32_16x16x32_bf16 v[172:175], a[180:183], a[148:151], v[172:175]\n"
    "ds_read_b128 a[176:179], v242 offset:8192\n"
    "v_exp_f32_e32 v167, v167\n"
    "v_mfma_f32_16x16x32_bf16 v[172:175], a[184:187], a[152:155], v[172:175]\n"
    "ds_read_b128 a[180:183], v243 offset:8192\n"
    "v_cvt_pk_bf16_f32 v164, v164, v165\n"
    "v_mfma_f32_16x16x32_bf16 v[172:175], a[188:191], a[156:159], v[172:175]\n"
    "buffer_load_dwordx4 v248, s[8:11], s41 offen offset:2048 sc1 lds\n"
    "v_cvt_pk_bf16_f32 v165, v166, v167\n"
    "v_mfma_f32_16x16x32_bf16 v[176:179], v[180:183], a[128:131], v[236:239]\n"
    "v_exp_f32_e32 v168, v168\n"
    "v_mfma_f32_16x16x32_bf16 v[176:179], v[184:187], a[132:135], v[176:179]\n"
    "v_exp_f32_e32 v169, v169\n"
    "v_mfma_f32_16x16x32_bf16 v[176:179], v[188:191], a[136:139], v[176:179]\n"
    "buffer_load_dwordx4 v248, s[8:11], s41 offen offset:3072 sc1 lds\n"
    "v_mfma_f32_16x16x32_bf16 v[176:179], v[192:195], a[140:143], v[176:179]\n"
    "s_mov_b32 m0, s42\n"
    "s_add_u32 s41, s41, 0x8000\n"
    "v_exp_f32_e32 v172, v172\n"
    "v_mfma_f32_16x16x32_bf16 v[176:179], v[196:199], a[144:147], v[176:179]\n"
    "ds_write_addtid_b32 v156 offset:2048\n"
    "v_exp_f32_e32 v173, v173\n"
    "v_mfma_f32_16x16x32_bf16 v[176:179], v[200:203], a[148:151], v[176:179]\n"
    "ds_write_addtid_b32 v157 offset:2304\n"
    "v_exp_f32_e32 v174, v174\n"
    "v_mfma_f32_16x16x32_bf16 v[176:179], v[204:207], a[152:155], v[176:179]\n"
    "ds_write_addtid_b32 v164 offset:3072\n"
    "v_exp_f32_e32 v175, v175\n"
    "v_mfma_f32_16x16x32_bf16 v[176:179], v[208:211], a[156:159], v[176:179]\n"
    "ds_write_addtid_b32 v165 offset:3328\n"
    "v_cvt_pk_bf16_f32 v172, v172, v173\n"
    "v_cvt_pk_bf16_f32 v173, v174, v175\n"
    "ds_write_addtid_b32 v172 offset:4096\n"
    "ds_write_addtid_b32 v173 offset:4352\n"
    "s_waitcnt lgkmcnt(10)\n"
    "s_branch .Lat_in\n"
    ".fill 250, 4, 0xbf800000\n"
    ".long 0xbedf00ff\n"
    ".Lat_w2:\n"
    "s_setpc_b64 s[2:3]\n"
    ".Lat_loop:\n"
    "v_mfma_f32_16x16x32_bf16 v[212:215], a[160:163], a[128:131], v[236:239]\n"
    "v_mfma_f32_16x16x32_bf16 v[212:215], a[164:167], a[132:135], v[212:215]\n"
    "v_exp_f32_e32 v170, v170\n"
    "v_mfma_f32_16x16x32_bf16 v[204:207], a[164:167], a[100:103], v[232:235]\n"
    "ds_write_addtid_b32 v143 offset:768\n"
    "v_mfma_f32_16x16x32_bf16 v[204:207], a[160:163], a[96:99], v[204:207]\n"
    "ds_read_b128 a[184:187], v242 offset:28672\n"
    "v_mfma_f32_16x16x32_bf16 v[196:199], a[160:163], a[64:67], v[228:231]\n"
    "v_exp_f32_e32 v171, v171\n"
    "v_mfma_f32_16x16x32_bf16 v[196:199], a[164:167], a[68:71], v[196:199]\n"
    "ds_write_addtid_b32 v150 offset:1536\n"
    "v_mfma_f32_16x16x32_bf16 v[188:191], a[164:167], a[36:39], v[224:227]\n"
    "v_cvt_pk_bf16_f32 v166, v168, v169\n"
    "v_mfma_f32_16x16x32_bf16 v[188:191], a[160:163], a[32:35], v[188:191]\n"
    "v_mfma_f32_16x16x32_bf16 v[180:183], a[160:163], a[0:3], v[220:223]\n"
    "ds_write_addtid_b32 v151 offset:1792\n"
    "v_mfma_f32_16x16x32_bf16 v[180:183], a[164:167], a[4:7], v[180:183]\n"
    "s_waitcnt lgkmcnt(10)\n"
    "v_exp_f32_e32 v176, v176\n"
    "v_mfma_f32_16x16x32_bf16 v[180:183], a[168:171], a[8:11], v[180:183]\n"
    "ds_read_b128 a[188:191], v243 offset:28672\n"
    "v_mfma_f32_16x16x32_bf16 v[180:183], a[172:175], a[12:15], v[180:183]\n"
    "v_exp_f32_e32 v177, v177\n"
    "v_mfma_f32_16x16x32_bf16 v[188:191], a[172:175], a[44:47], v[188:191]\n"
    "ds_write_addtid_b32 v158 offset:2560\n"
    "v_mfma_f32_16x16x32_bf16 v[188:191], a[168:171], a[40:43], v[188:191]\n"
    "ds_read_b128 a[160:163], v240 offset:26624\n"
    "v_mfma_f32_16x16x32_bf16 v[196:199], a[168:171], a[72:75], v[196:199]\n"
    "v_cvt_pk_bf16_f32 v167, v170, v171\n"
    "v_mfma_f32_16x16x32_bf16 v[196:199], a[172:175], a[76:79], v[196:199]\n"
    "ds_write_addtid_b32 v159 offset:2816\n"
    "v_mfma_f32_16x16x32_bf16 v[204:207], a[172:175], a[108:111], v[204:207]\n"
    "v_exp_f32_e32 v178, v178\n"
    "v_mfma_f32_16x16x32_bf16 v[204:207], a[168:171], a[104:107], v[204:207]\n"
    "ds_write_addtid_b32 v166 offset:3584\n"
    "v_mfma_f32_16x16x32_bf16 v[212:215], a[168:171], a[136:139], v[212:215]\n"
    "v_mfma_f32_16x16x32_bf16 v[212:215], a[172:175], a[140:143], v[212:215]\n"
    "s_waitcnt lgkmcnt(10)\n"
    "v_exp_f32_e32 v179, v179\n"
    "v_mfma_f32_16x16x32_bf16 v[212:215], a[176:179], a[144:147], v[212:215]\n"
    "ds_read_b128 a[164:167], v241 offset:26624\n"
    "v_mfma_f32_16x16x32_bf16 v[212:215], a[180:183], a[148:151], v[212:215]\n"
    "v_cvt_pk_bf16_f32 v174, v176, v177\n"
    "v_mfma_f32_16x16x32_bf16 v[204:207], a[180:183], a[116:119], v[204:207]\n"
    "ds_write_addtid_b32 v167 offset:3840\n"
    "v_mfma_f32_16x16x32_bf16 v[204:207], a[176:179], a[112:115], v[204:207]\n"
    "ds_read_b128 a[168:171], v240 offset:30720\n"
    "v_mfma_f32_16x16x32_bf16 v[196:199], a[176:179], a[80:83], v[196:199]\n"
    "v_cvt_pk_bf16_f32 v175, v178, v179\n"
    "v_mfma_f32_16x16x32_bf16 v[196:199], a[180:183], a[84:87], v[196:199]\n"
    "ds_write_addtid_b32 v174 offset:4608\n"
    "v_mfma_f32_16x16x32_bf16 v[188:191], a[180:183], a[52:55], v[188:191]\n"
    "ds_write_addtid_b32 v175 offset:4864\n"
    "v_mfma_f32_16x16x32_bf16 v[188:191], a[176:179], a[48:51], v[188:191]\n"
    "v_mfma_f32_16x16x32_bf16 v[180:183], a[176:179], a[16:19], v[180:183]\n"
    "ds_read_b128 a[172:175], v241 offset:30720\n"
    "v_mfma_f32_16x16x32_bf16 v[180:183], a[180:183], a[20:23], v[180:183]\n"
    "s_waitcnt lgkmcnt(10)\n"
    "s_waitcnt lgkmcnt(1)\n"
    "v_mfma_f32_16x16x32_bf16 v[180:183], a[184:187], a[24:27], v[180:183]\n"
    "s_barrier\n"
    "v_mfma_f32_16x16x32_bf16 v[180:183], a[188:191], a[28:31], v[180:183]\n"
    "s_add_u32 m0, s43, 8192\n"
    "v_mfma_f32_16x16x32_bf16 v[188:191], a[188:191], a[60:63], v[188:191]\n"
    "ds_read_b128 a[176:179], v242 offset:26624\n"
    "v_mfma_f32_16x16x32_bf16 v[188:191], a[184:187], a[56:59], v[188:191]\n"
    "buffer_load_dwordx4 v248, s[8:11], s40 offen offset:0 sc1 lds\n"
    "v_mfma_f32_16x16x32_bf16 v[196:199], a[184:187], a[88:91], v[196:199]\n"
    "ds_read2st64_b32 v[144:145], v249 offset0:0 offset1:1\n"
    "v_mfma_f32_16x16x32_bf16 v[196:199], a[188:191], a[92:95], v[196:199]\n"
    "v_mfma_f32_16x16x32_bf16 v[204:207], a[188:191], a[124:127], v[204:207]\n"
    "ds_read2st64_b32 v[146:147], v249 offset0:2 offset1:3\n"
    "v_mfma_f32_16x16x32_bf16 v[204:207], a[184:187], a[120:123], v[204:207]\n"
    "buffer_load_dwordx4 v248, s[8:11], s40 offen offset:1024 sc1 lds\n"
    "v_mfma_f32_16x16x32_bf16 v[212:215], a[184:187], a[152:155], v[212:215]\n"
    "v_mfma_f32_16x16x32_bf16 v[212:215], a[188:191], a[156:159], v[212:215]\n"
    "v_mfma_f32_16x16x32_bf16 v[216:219], a[160:163], a[128:131], v[236:239]\n"
    "ds_read_b128 a[180:183], v243 offset:26624\n"
    "v_mfma_f32_16x16x32_bf16 v[216:219], a[164:167], a[132:135], v[216:219]\n"
    "buffer_load_dwordx4 v248, s[8:11], s40 offen offset:2048 sc1 lds\n"
    "v_mfma_f32_16x16x32_bf16 v[208:211], a[164:167], a[100:103], v[232:235]\n"
    "ds_read2st64_b32 v[152:153], v249 offset0:4 offset1:5\n"
    "v_mfma_f32_16x16x32_bf16 v[208:211], a[160:163], a[96:99], v[208:211]\n"
    "ds_read_b128 a[184:187], v242 offset:30720\n"
    "v_mfma_f32_16x16x32_bf16 v[200:203], a[160:163], a[64:67], v[228:231]\n"
    "ds_read2st64_b32 v[154:155], v249 offset0:6 offset1:7\n"
    "v_mfma_f32_16x16x32_bf16 v[200:203], a[164:167], a[68:71], v[200:203]\n"
    "buffer_load_dwordx4 v248, s[8:11], s40 offen offset:3072 sc1 lds\n"
    "v_mfma_f32_16x16x32_bf16 v[192:195], a[164:167], a[36:39], v[224:227]\n"
    "s_add_u32 m0, s43, 12288\n"
    "v_exp_f32_e32 v180, v180\n"
    "v_mfma_f32_16x16x32_bf16 v[192:195], a[160:163], a[32:35], v[192:195]\n"
    "s_add_u32 s40, s40, 0x8000\n"
    "v_exp_f32_e32 v181, v181\n"
    "v_mfma_f32_16x16x32_bf16 v[184:187], a[160:163], a[0:3], v[220:223]\n"
    "ds_read2st64_b32 v[160:161], v249 offset0:8 offset1:9\n"
    "v_mfma_f32_16x16x32_bf16 v[184:187], a[164:167], a[4:7], v[184:187]\n"
    "s_waitcnt lgkmcnt(8)\n"
    "buffer_load_dwordx4 v248, s[8:11], s41 offen offset:0 sc1 lds\n"
    "v_mfma_f32_16x16x32_bf16 v[184:187], a[168:171], a[8:11], v[184:187]\n"
    ".long 0xbedf00ff\n"
    ".Lat_w3:\n"
    "s_setpc_b64 s[2:3]\n"
    "ds_read_b128 a[188:191], v243 offset:30720\n"
    "v_mfma_f32_16x16x32_bf16 v[184:187], a[172:175], a[12:15], v[184:187]\n"
    "ds_read_b64_tr_b16 a[160:161], v244 offset:16384\n"
    "v_mfma_f32_16x16x32_bf16 v[192:195], a[172:175], a[44:47], v[192:195]\n"
    "ds_read2st64_b32 v[162:163], v249 offset0:10 offset1:11\n"
    "v_mfma_f32_16x16x32_bf16 v[192:195], a[168:171], a[40:43], v[192:195]\n"
    "buffer_load_dwordx4 v248, s[8:11], s41 offen offset:1024 sc1 lds\n"
    "v_mfma_f32_16x16x32_bf16 v[200:203], a[168:171], a[72:75], v[200:203]\n"
    "ds_read_b64_tr_b16 a[162:163], v244 offset:18432\n"
    "v_mfma_f32_16x16x32_bf16 v[200:203], a[172:175], a[76:79], v[200:203]\n"
    "v_exp_f32_e32 v182, v182\n"
    "v_mfma_f32_16x16x32_bf16 v[208:211], a[172:175], a[108:111], v[208:211]\n"
    "ds_read2st64_b32 v[168:169], v249 offset0:12 offset1:13\n"
    "v_mfma_f32_16x16x32_bf16 v[208:211], a[168:171], a[104:107], v[208:211]\n"
    "buffer_load_dwordx4 v248, s[8:11], s41 offen offset:2048 sc1 lds\n"
    "v_mfma_f32_16x16x32_bf16 v[216:219], a[168:171], a[136:139], v[216:219]\n"
    "ds_read_b64_tr_b16 a[164:165], v244 offset:49152\n"
    "v_mfma_f32_16x16x32_bf16 v[216:219], a[172:175], a[140:143], v[216:219]\n"
    "s_waitcnt lgkmcnt(10)\n"
    "ds_read2st64_b32 v[170:171], v249 offset0:14 offset1:15\n"
    "v_mfma_f32_16x16x32_bf16 v[216:219], a[176:179], a[144:147], v[216:219]\n"
    "ds_read_b64_tr_b16 a[168:169], v245 offset:16384\n"
    "v_mfma_f32_16x16x32_bf16 v[216:219], a[180:183], a[148:151], v[216:219]\n"
    "buffer_load_dwordx4 v248, s[8:11], s41 offen offset:3072 sc1 lds\n"
    "v_mfma_f32_16x16x32_bf16 v[208:211], a[180:183], a[116:119], v[208:211]\n"
    "ds_read_b64_tr_b16 a[166:167], v244 offset:51200\n"
    "v_mfma_f32_16x16x32_bf16 v[208:211], a[176:179], a[112:115], v[208:211]\n"
    "s_add_u32 s41, s41, 0x8000\n"
    "v_exp_f32_e32 v183, v183\n"
    "v_mfma_f32_16x16x32_bf16 v[200:203], a[176:179], a[80:83], v[200:203]\n"
    "ds_read_b64_tr_b16 a[170:171], v245 offset:18432\n"
    "v_cvt_pk_bf16_f32 v180, v180, v181\n"
    "v_mfma_f32_16x16x32_bf16 v[200:203], a[180:183], a[84:87], v[200:203]\n"
    "ds_read2st64_b32 v[176:177], v249 offset0:16 offset1:17\n"
    "v_mfma_f32_16x16x32_bf16 v[192:195], a[180:183], a[52:55], v[192:195]\n"
    "v_exp_f32_e32 v188, v188\n"
    "v_mfma_f32_16x16x32_bf16 v[192:195], a[176:179], a[48:51], v[192:195]\n"
    "v_exp_f32_e32 v189, v189\n"
    "v_mfma_f32_16x16x32_bf16 v[184:187], a[176:179], a[16:19], v[184:187]\n"
    "ds_read_b64_tr_b16 a[172:173], v245 offset:49152\n"
    "v_cvt_pk_bf16_f32 v181, v182, v183\n"
    "v_mfma_f32_16x16x32_bf16 v[184:187], a[180:183], a[20:23], v[184:187]\n"
    "s_waitcnt lgkmcnt(11)\n"
    "ds_read2st64_b32 v[178:179], v249 offset0:18 offset1:19\n"
    "v_mfma_f32_16x16x32_bf16 v[184:187], a[184:187], a[24:27], v[184:187]\n"
    "ds_read_b64_tr_b16 a[176:177], v246 offset:16384\n"
    "v_mfma_f32_16x16x32_bf16 v[184:187], a[188:191], a[28:31], v[184:187]\n"
    "ds_read_b64_tr_b16 a[174:175], v245 offset:51200\n"
    "v_mfma_f32_16x16x32_bf16 v[192:195], a[188:191], a[60:63], v[192:195]\n"
    "v_exp_f32_e32 v190, v190\n"
    "v_mfma_f32_16x16x32_bf16 v[192:195], a[184:187], a[56:59], v[192:195]\n"
    "v_exp_f32_e32 v191, v191\n"
    "v_mfma_f32_16x16x32_bf16 v[200:203], a[184:187], a[88:91], v[200:203]\n"
    "v_mfma_f32_16x16x32_bf16 v[200:203], a[188:191], a[92:95], v[200:203]\n"
    "ds_read_b64_tr_b16 a[178:179], v246 offset:18432\n"
    "v_mfma_f32_16x16x32_bf16 v[208:211], a[188:191], a[124:127], v[208:211]\n"
    "v_cvt_pk_bf16_f32 v188, v188, v189\n"
    "v_mfma_f32_16x16x32_bf16 v[208:211], a[184:187], a[120:123], v[208:211]\n"
    "v_exp_f32_e32 v196, v196\n"
    "v_mfma_f32_16x16x32_bf16 v[216:219], a[184:187], a[152:155], v[216:219]\n"
    "ds_read_b64_tr_b16 a[180:181], v246 offset:49152\n"
    "v_mfma_f32_16x16x32_bf16 v[216:219], a[188:191], a[156:159], v[216:219]\n"
    "s_waitcnt lgkmcnt(4)\n"
    "v_exp_f32_e32 v197, v197\n"
    "v_mfma_f32_16x16x32_bf16 a[236:239], a[160:163], v[140:143], a[236:239]\n"
    "v_mfma_f32_16x16x32_bf16 a[236:239], a[164:167], v[144:147], a[236:239]\n"
    "ds_read_b64_tr_b16 a[182:183], v246 offset:51200\n"
    "v_mfma_f32_16x16x32_bf16 a[240:243], a[164:167], v[152:155], a[240:243]\n"
    "ds_read_b64_tr_b16 a[184:185], v247 offset:16384\n"
    "v_mfma_f32_16x16x32_bf16 a[240:243], a[160:163], v[148:151], a[240:243]\n"
    "v_cvt_pk_bf16_f32 v189, v190, v191\n"
    "v_mfma_f32_16x16x32_bf16 a[244:247], a[160:163], v[156:159], a[244:247]\n"
    "ds_read_b64_tr_b16 a[186:187], v247 offset:18432\n"
    "v_mfma_f32_16x16x32_bf16 a[244:247], a[164:167], v[160:163], a[244:247]\n"
    "v_exp_f32_e32 v198, v198\n"
    "v_mfma_f32_16x16x32_bf16 a[248:251], a[164:167], v[168:171], a[248:251]\n"
    "v_exp_f32_e32 v199, v199\n"
    "v_mfma_f32_16x16x32_bf16 a[248:251], a[160:163], v[164:167], a[248:251]\n"
    "v_cvt_pk_bf16_f32 v196, v196, v197\n"
    "v_mfma_f32_16x16x32_bf16 a[252:255], a[160:163], v[172:175], a[252:255]\n"
    "ds_read_b64_tr_b16 a[188:189], v247 offset:49152\n"
    "v_mfma_f32_16x16x32_bf16 a[252:255], a[164:167], v[176:179], a[252:255]\n"
    "s_waitcnt lgkmcnt(6)\n"
    "v_exp_f32_e32 v204, v204\n"
    "v_mfma_f32_16x16x32_bf16 v[16:19], a[172:175], v[176:179], v[16:19]\n"
    "ds_read_b64_tr_b16 a[160:161], v244 offset:20480\n"
    "v_mfma_f32_16x16x32_bf16 v[16:19], a[168:171], v[172:175], v[16:19]\n"
    "ds_read_b64_tr_b16 a[190:191], v247 offset:51200\n"
    "v_mfma_f32_16x16x32_bf16 v[12:15], a[168:171], v[164:167], v[12:15]\n"
    "v_exp_f32_e32 v205, v205\n"
    "v_mfma_f32_16x16x32_bf16 v[12:15], a[172:175], v[168:171], v[12:15]\n"
    "v_cvt_pk_bf16_f32 v197, v198, v199\n"
    "v_mfma_f32_16x16x32_bf16 v[8:11], a[172:175], v[160:163], v[8:11]\n"
    "ds_read_b64_tr_b16 a[162:163], v244 offset:22528\n"
    "v_mfma_f32_16x16x32_bf16 v[8:11], a[168:171], v[156:159], v[8:11]\n"
    "v_exp_f32_e32 v206, v206\n"
    "v_mfma_f32_16x16x32_bf16 v[4:7], a[168:171], v[148:151], v[4:7]\n"
    "v_exp_f32_e32 v207, v207\n"
    "v_mfma_f32_16x16x32_bf16 v[4:7], a[172:175], v[152:155], v[4:7]\n"
    "v_cvt_pk_bf16_f32 v204, v204, v205\n"
    "v_mfma_f32_16x16x32_bf16 v[0:3], a[172:175], v[144:147], v[0:3]\n"
    "ds_read_b64_tr_b16 a[164:165], v244 offset:53248\n"
    "v_mfma_f32_16x16x32_bf16 v[0:3], a[168:171], v[140:143], v[0:3]\n"
    "s_waitcnt lgkmcnt(7)\n"
    "v_exp_f32_e32 v212, v212\n"
    "v_mfma_f32_16x16x32_bf16 v[20:23], a[176:179], v[140:143], v[20:23]\n"
    "v_mfma_f32_16x16x32_bf16 v[20:23], a[180:183], v[144:147], v[20:23]\n"
    "ds_read_b64_tr_b16 a[166:167], v244 offset:55296\n"
    "v_mfma_f32_16x16x32_bf16 v[24:27], a[180:183], v[152:155], v[24:27]\n"
    "ds_read_b64_tr_b16 a[168:169], v245 offset:20480\n"
    "v_mfma_f32_16x16x32_bf16 v[24:27], a[176:179], v[148:151], v[24:27]\n"
    "v_exp_f32_e32 v213, v213\n"
    "v_mfma_f32_16x16x32_bf16 v[28:31], a[176:179], v[156:159], v[28:31]\n"
    "ds_read_b64_tr_b16 a[170:171], v245 offset:22528\n"
    "v_mfma_f32_16x16x32_bf16 v[28:31], a[180:183], v[160:163], v[28:31]\n"
    "v_cvt_pk_bf16_f32 v205, v206, v207\n"
    "v_mfma_f32_16x16x32_bf16 v[32:35], a[180:183], v[168:171], v[32:35]\n"
    "v_exp_f32_e32 v214, v214\n"
    "v_mfma_f32_16x16x32_bf16 v[32:35], a[176:179], v[164:167], v[32:35]\n"
    "v_exp_f32_e32 v215, v215\n"
    "v_mfma_f32_16x16x32_bf16 v[36:39], a[176:179], v[172:175], v[36:39]\n"
    "ds_read_b64_tr_b16 a[172:173], v245 offset:53248\n"
    "v_mfma_f32_16x16x32_bf16 v[36:39], a[180:183], v[176:179], v[36:39]\n"
    "s_waitcnt lgkmcnt(6)\n"
    "v_cvt_pk_bf16_f32 v212, v212, v213\n"
    "v_mfma_f32_16x16x32_bf16 v[56:59], a[188:191], v[176:179], v[56:59]\n"
    "ds_read_b64_tr_b16 a[176:177], v246 offset:20480\n"
    "v_mfma_f32_16x16x32_bf16 v[56:59], a[184:187], v[172:175], v[56:59]\n"
    "ds_read_b64_tr_b16 a[174:175], v245 offset:55296\n"
    "v_mfma_f32_16x16x32_bf16 v[52:55], a[184:187], v[164:167], v[52:55]\n"
    "v_cvt_pk_bf16_f32 v213, v214, v215\n"
    "v_mfma_f32_16x16x32_bf16 v[52:55], a[188:191], v[168:171], v[52:55]\n"
    "v_exp_f32_e32 v184, v184\n"
    "v_mfma_f32_16x16x32_bf16 v[48:51], a[188:191], v[160:163], v[48:51]\n"
    "ds_read_b64_tr_b16 a[178:179], v246 offset:22528\n"
    "v_mfma_f32_16x16x32_bf16 v[48:51], a[184:187], v[156:159], v[48:51]\n"
    "v_mfma_f32_16x16x32_bf16 v[44:47], a[184:187], v[148:151], v[44:47]\n"
    "v_exp_f32_e32 v185, v185\n"
    "v_mfma_f32_16x16x32_bf16 v[44:47], a[188:191], v[152:155], v[44:47]\n"
    "v_exp_f32_e32 v186, v186\n"
    "v_mfma_f32_16x16x32_bf16 v[40:43], a[188:191], v[144:147], v[40:43]\n"
    "ds_read_b64_tr_b16 a[180:181], v246 offset:53248\n"
    "v_mfma_f32_16x16x32_bf16 v[40:43], a[184:187], v[140:143], v[40:43]\n"
    "s_waitcnt lgkmcnt(7)\n"
    "v_exp_f32_e32 v187, v187\n"
    "v_mfma_f32_16x16x32_bf16 v[60:63], a[160:163], v[140:143], v[60:63]\n"
    "ds_read_b64_tr_b16 a[184:185], v247 offset:20480\n"
    "v_mfma_f32_16x16x32_bf16 v[60:63], a[164:167], v[144:147], v[60:63]\n"
    "ds_read_b64_tr_b16 a[182:183], v246 offset:55296\n"
    "v_mfma_f32_16x16x32_bf16 v[64:67], a[164:167], v[152:155], v[64:67]\n"
    "ds_read_b64_tr_b16 a[186:187], v247 offset:22528\n"
    "v_mfma_f32_16x16x32_bf16 v[64:67], a[160:163], v[148:151], v[64:67]\n"
    "v_cvt_pk_bf16_f32 v182, v184, v185\n"
    "v_mfma_f32_16x16x32_bf16 v[68:71], a[160:163], v[156:159], v[68:71]\n"
    "v_exp_f32_e32 v192, v192\n"
    "v_mfma_f32_16x16x32_bf16 v[68:71], a[164:167], v[160:163], v[68:71]\n"
    "ds_read_b64_tr_b16 a[188:189], v247 offset:53248\n"
    "v_mfma_f32_16x16x32_bf16 v[72:75], a[164:167], v[168:171], v[72:75]\n"
    "ds_read_b64_tr_b16 a[190:191], v247 offset:55296\n"
    "v_mfma_f32_16x16x32_bf16 v[72:75], a[160:163], v[164:167], v[72:75]\n"
    "s_waitcnt vmcnt(9)\n"
    "v_mfma_f32_16x16x32_bf16 v[76:79], a[160:163], v[172:175], v[76:79]\n"
    "s_barrier\n"
    "v_mfma_f32_16x16x32_bf16 v[76:79], a[164:167], v[176:179], v[76:79]\n"
    "s_waitcnt lgkmcnt(7)\n"
    "s_mov_b32 m0, s42\n"
    "v_mfma_f32_16x16x32_bf16 v[96:99], a[172:175], v[176:179], v[96:99]\n"
    "v_exp_f32_e32 v193, v193\n"
    "v_mfma_f32_16x16x32_bf16 v[96:99], a[168:171], v[172:175], v[96:99]\n"
    "v_cvt_pk_bf16_f32 v183, v186, v187\n"
    "v_mfma_f32_16x16x32_bf16 v[92:95], a[168:171], v[164:167], v[92:95]\n"
    "v_exp_f32_e32 v194, v194\n"
    "v_mfma_f32_16x16x32_bf16 v[92:95], a[172:175], v[168:171], v[92:95]\n"
    "ds_read_b128 a[160:163], v240 offset:0\n"
    "v_mfma_f32_16x16x32_bf16 v[88:91], a[172:175], v[160:163], v[88:91]\n"
    "ds_write_addtid_b32 v180 offset:0\n"
    "v_mfma_f32_16x16x32_bf16 v[88:91], a[168:171], v[156:159], v[88:91]\n"
    "v_exp_f32_e32 v195, v195\n"
    "v_mfma_f32_16x16x32_bf16 v[84:87], a[168:171], v[148:151], v[84:87]\n"
    "ds_write_addtid_b32 v181 offset:256\n"
    "v_mfma_f32_16x16x32_bf16 v[84:87], a[172:175], v[152:155], v[84:87]\n"
    "ds_write_addtid_b32 v188 offset:1024\n"
    "v_mfma_f32_16x16x32_bf16 v[80:83], a[172:175], v[144:147], v[80:83]\n"
    "v_cvt_pk_bf16_f32 v190, v192, v193\n"
    "v_mfma_f32_16x16x32_bf16 v[80:83], a[168:171], v[140:143], v[80:83]\n"
    "s_waitcnt lgkmcnt(7)\n"
    "v_exp_f32_e32 v200, v200\n"
    "v_mfma_f32_16x16x32_bf16 v[100:103], a[176:179], v[140:143], v[100:103]\n"
    "ds_read_b128 a[164:167], v241 offset:0\n"
    "v_mfma_f32_16x16x32_bf16 v[100:103], a[180:183], v[144:147], v[100:103]\n"
    "ds_write_addtid_b32 v189 offset:1280\n"
    "v_mfma_f32_16x16x32_bf16 v[104:107], a[180:183], v[152:155], v[104:107]\n"
    "v_exp_f32_e32 v201, v201\n"
    "v_mfma_f32_16x16x32_bf16 v[104:107], a[176:179], v[148:151], v[104:107]\n"
    "ds_read_b128 a[168:171], v240 offset:4096\n"
    "v_mfma_f32_16x16x32_bf16 v[108:111], a[176:179], v[156:159], v[108:111]\n"
    "ds_write_addtid_b32 v196 offset:2048\n"
    "v_mfma_f32_16x16x32_bf16 v[108:111], a[180:183], v[160:163], v[108:111]\n"
    "v_cvt_pk_bf16_f32 v191, v194, v195\n"
    "v_mfma_f32_16x16x32_bf16 v[112:115], a[180:183], v[168:171], v[112:115]\n"
    "ds_write_addtid_b32 v197 offset:2304\n"
    "v_mfma_f32_16x16x32_bf16 v[112:115], a[176:179], v[164:167], v[112:115]\n"
    "v_exp_f32_e32 v202, v202\n"
    "v_mfma_f32_16x16x32_bf16 v[116:119], a[176:179], v[172:175], v[116:119]\n"
    "ds_write_addtid_b32 v204 offset:3072\n"
    "v_mfma_f32_16x16x32_bf16 v[116:119], a[180:183], v[176:179], v[116:119]\n"
    "s_waitcnt lgkmcnt(10)\n"
    "v_exp_f32_e32 v203, v203\n"
    "v_mfma_f32_16x16x32_bf16 v[136:139], a[188:191], v[176:179], v[136:139]\n"
    "ds_read_b128 a[172:175], v241 offset:4096\n"
    "v_mfma_f32_16x16x32_bf16 v[136:139], a[184:187], v[172:175], v[136:139]\n"
    "ds_write_addtid_b32 v205 offset:3328\n"
    "v_cvt_pk_bf16_f32 v198, v200, v201\n"
    "v_mfma_f32_16x16x32_bf16 v[132:135], a[184:187], v[164:167], v[132:135]\n"
    "ds_read_b128 a[176:179], v242 offset:0\n"
    "v_mfma_f32_16x16x32_bf16 v[132:135], a[188:191], v[168:171], v[132:135]\n"
    "ds_write_addtid_b32 v212 offset:4096\n"
    "v_mfma_f32_16x16x32_bf16 v[128:131], a[188:191], v[160:163], v[128:131]\n"
    "v_exp_f32_e32 v208, v208\n"
    "v_mfma_f32_16x16x32_bf16 v[128:131], a[184:187], v[156:159], v[128:131]\n"
    "ds_write_addtid_b32 v213 offset:4352\n"
    "v_mfma_f32_16x16x32_bf16 v[124:127], a[184:187], v[148:151], v[124:127]\n"
    "v_exp_f32_e32 v209, v209\n"
    "v_mfma_f32_16x16x32_bf16 v[124:127], a[188:191], v[152:155], v[124:127]\n"
    "ds_read_b128 a[180:183], v243 offset:0\n"
    "v_mfma_f32_16x16x32_bf16 v[120:123], a[188:191], v[144:147], v[120:123]\n"
    "ds_write_addtid_b32 v182 offset:512\n"
    "v_mfma_f32_16x16x32_bf16 v[120:123], a[184:187], v[140:143], v[120:123]\n"
    "s_waitcnt lgkmcnt(12)\n"
    "v_cvt_pk_bf16_f32 v199, v202, v203\n"
    "v_mfma_f32_16x16x32_bf16 v[172:175], a[160:163], a[128:131], v[236:239]\n"
    "v_mfma_f32_16x16x32_bf16 v[172:175], a[164:167], a[132:135], v[172:175]\n"
    "v_exp_f32_e32 v210, v210\n"
    "v_mfma_f32_16x16x32_bf16 v[164:167], a[164:167], a[100:103], v[232:235]\n"
    "ds_write_addtid_b32 v183 offset:768\n"
    "v_mfma_f32_16x16x32_bf16 v[164:167], a[160:163], a[96:99], v[164:167]\n"
    "ds_read_b128 a[184:187], v242 offset:4096\n"
    "v_mfma_f32_16x16x32_bf16 v[156:159], a[160:163], a[64:67], v[228:231]\n"
    "v_exp_f32_e32 v211, v211\n"
    "v_mfma_f32_16x16x32_bf16 v[156:159], a[164:167], a[68:71], v[156:159]\n"
    "ds_write_addtid_b32 v190 offset:1536\n"
    "v_mfma_f32_16x16x32_bf16 v[148:151], a[164:167], a[36:39], v[224:227]\n"
    "v_cvt_pk_bf16_f32 v206, v208, v209\n"
    "v_mfma_f32_16x16x32_bf16 v[148:151], a[160:163], a[32:35], v[148:151]\n"
    "v_mfma_f32_16x16x32_bf16 v[140:143], a[160:163], a[0:3], v[220:223]\n"
    "ds_write_addtid_b32 v191 offset:1792\n"
    "v_mfma_f32_16x16x32_bf16 v[140:143], a[164:167], a[4:7], v[140:143]\n"
    "s_waitcnt lgkmcnt(10)\n"
    "v_exp_f32_e32 v216, v216\n"
    "v_mfma_f32_16x16x32_bf16 v[140:143], a[168:171], a[8:11], v[140:143]\n"
    "ds_read_b128 a[188:191], v243 offset:4096\n"
    "v_mfma_f32_16x16x32_bf16 v[140:143], a[172:175], a[12:15], v[140:143]\n"
    "v_exp_f32_e32 v217, v217\n"
    "v_mfma_f32_16x16x32_bf16 v[148:151], a[172:175], a[44:47], v[148:151]\n"
    "ds_write_addtid_b32 v198 offset:2560\n"
    "v_mfma_f32_16x16x32_bf16 v[148:151], a[168:171], a[40:43], v[148:151]\n"
    "ds_read_b128 a[160:163], v240 offset:2048\n"
    "v_mfma_f32_16x16x32_bf16 v[156:159], a[168:171], a[72:75], v[156:159]\n"
    "v_cvt_pk_bf16_f32 v207, v210, v211\n"
    "v_mfma_f32_16x16x32_bf16 v[156:159], a[172:175], a[76:79], v[156:159]\n"
    "ds_write_addtid_b32 v199 offset:2816\n"
    "v_mfma_f32_16x16x32_bf16 v[164:167], a[172:175], a[108:111], v[164:167]\n"
    "v_exp_f32_e32 v218, v218\n"
    "v_mfma_f32_16x16x32_bf16 v[164:167], a[168:171], a[104:107], v[164:167]\n"
    "ds_write_addtid_b32 v206 offset:3584\n"
    "v_mfma_f32_16x16x32_bf16 v[172:175], a[168:171], a[136:139], v[172:175]\n"
    "v_mfma_f32_16x16x32_bf16 v[172:175], a[172:175], a[140:143], v[172:175]\n"
    "s_waitcnt lgkmcnt(10)\n"
    "v_exp_f32_e32 v219, v219\n"
    "v_mfma_f32_16x16x32_bf16 v[172:175], a[176:179], a[144:147], v[172:175]\n"
    "ds_read_b128 a[164:167], v241 offset:2048\n"
    "v_mfma_f32_16x16x32_bf16 v[172:175], a[180:183], a[148:151], v[172:175]\n"
    "v_cvt_pk_bf16_f32 v214, v216, v217\n"
    "v_mfma_f32_16x16x32_bf16 v[164:167], a[180:183], a[116:119], v[164:167]\n"
    "ds_write_addtid_b32 v207 offset:3840\n"
    "v_mfma_f32_16x16x32_bf16 v[164:167], a[176:179], a[112:115], v[164:167]\n"
    "ds_read_b128 a[168:171], v240 offset:6144\n"
    "v_mfma_f32_16x16x32_bf16 v[156:159], a[176:179], a[80:83], v[156:159]\n"
    "v_cvt_pk_bf16_f32 v215, v218, v219\n"
    "v_mfma_f32_16x16x32_bf16 v[156:159], a[180:183], a[84:87], v[156:159]\n"
    "ds_write_addtid_b32 v214 offset:4608\n"
    "v_mfma_f32_16x16x32_bf16 v[148:151], a[180:183], a[52:55], v[148:151]\n"
    "ds_write_addtid_b32 v215 offset:4864\n"
    "v_mfma_f32_16x16x32_bf16 v[148:151], a[176:179], a[48:51], v[148:151]\n"
    "v_mfma_f32_16x16x32_bf16 v[140:143], a[176:179], a[16:19], v[140:143]\n"
    "ds_read_b128 a[172:175], v241 offset:6144\n"
    "v_mfma_f32_16x16x32_bf16 v[140:143], a[180:183], a[20:23], v[140:143]\n"
    "s_waitcnt lgkmcnt(10)\n"
    "s_waitcnt lgkmcnt(1)\n"
    "v_mfma_f32_16x16x32_bf16 v[140:143], a[184:187], a[24:27], v[140:143]\n"
    "s_barrier\n"
    "v_mfma_f32_16x16x32_bf16 v[140:143], a[188:191], a[28:31], v[140:143]\n"
    "s_add_u32 m0, s43, 16384\n"
    "v_mfma_f32_16x16x32_bf16 v[148:151], a[188:191], a[60:63], v[148:151]\n"
    "ds_read_b128 a[176:179], v242 offset:2048\n"
    "v_mfma_f32_16x16x32_bf16 v[148:151], a[184:187], a[56:59], v[148:151]\n"
    "buffer_load_dwordx4 v248, s[8:11], s40 offen offset:0 sc1 lds\n"
    "v_mfma_f32_16x16x32_bf16 v[156:159], a[184:187], a[88:91], v[156:159]\n"
    "ds_read2st64_b32 v[184:185], v249 offset0:0 offset1:1\n"
    "v_mfma_f32_16x16x32_bf16 v[156:159], a[188:191], a[92:95], v[156:159]\n"
    "v_mfma_f32_16x16x32_bf16 v[164:167], a[188:191], a[124:127], v[164:167]\n"
    "ds_read2st64_b32 v[186:187], v249 offset0:2 offset1:3\n"
    "v_mfma_f32_16x16x32_bf16 v[164:167], a[184:187], a[120:123], v[164:167]\n"
    "buffer_load_dwordx4 v248, s[8:11], s40 offen offset:1024 sc1 lds\n"
    "v_mfma_f32_16x16x32_bf16 v[172:175], a[184:187], a[152:155], v[172:175]\n"
    "v_mfma_f32_16x16x32_bf16 v[172:175], a[188:191], a[156:159], v[172:175]\n"
    "v_mfma_f32_16x16x32_bf16 v[176:179], a[160:163], a[128:131], v[236:239]\n"
    "ds_read_b128 a[180:183], v243 offset:2048\n"
    "v_mfma_f32_16x16x32_bf16 v[176:179], a[164:167], a[132:135], v[176:179]\n"
    "buffer_load_dwordx4 v248, s[8:11], s40 offen offset:2048 sc1 lds\n"
    "v_mfma_f32_16x16x32_bf16 v[168:171], a[164:167], a[100:103], v[232:235]\n"
    "ds_read2st64_b32 v[192:193], v249 offset0:4 offset1:5\n"
    "v_mfma_f32_16x16x32_bf16 v[168:171], a[160:163], a[96:99], v[168:171]\n"
    "ds_read_b128 a[184:187], v242 offset:6144\n"
    "v_mfma_f32_16x16x32_bf16 v[160:163], a[160:163], a[64:67], v[228:231]\n"
    "ds_read2st64_b32 v[194:195], v249 offset0:6 offset1:7\n"
    "v_mfma_f32_16x16x32_bf16 v[160:163], a[164:167], a[68:71], v[160:163]\n"
    "buffer_load_dwordx4 v248, s[8:11], s40 offen offset:3072 sc1 lds\n"
    "v_mfma_f32_16x16x32_bf16 v[152:155], a[164:167], a[36:39], v[224:227]\n"
    "s_add_u32 m0, s43, 20480\n"
    "v_exp_f32_e32 v140, v140\n"
    "v_mfma_f32_16x16x32_bf16 v[152:155], a[160:163], a[32:35], v[152:155]\n"
    "s_add_u32 s40, s40, 0x8000\n"
    "v_exp_f32_e32 v141, v141\n"
    "v_mfma_f32_16x16x32_bf16 v[144:147], a[160:163], a[0:3], v[220:223]\n"
    "ds_read2st64_b32 v[200:201], v249 offset0:8 offset1:9\n"
    "v_mfma_f32_16x16x32_bf16 v[144:147], a[164:167], a[4:7], v[144:147]\n"
    "s_waitcnt lgkmcnt(8)\n"
    "buffer_load_dwordx4 v248, s[8:11], s41 offen offset:0 sc1 lds\n"
    "v_mfma_f32_16x16x32_bf16 v[144:147], a[168:171], a[8:11], v[144:147]\n"
    "ds_read_b128 a[188:191], v243 offset:6144\n"
    "v_mfma_f32_16x16x32_bf16 v[144:147], a[172:175], a[12:15], v[144:147]\n"
    "ds_read_b64_tr_b16 a[160:161], v244 offset:24576\n"
    "v_mfma_f32_16x16x32_bf16 v[152:155], a[172:175], a[44:47], v[152:155]\n"
    "ds_read2st64_b32 v[202:203], v249 offset0:10 offset1:11\n"
    "v_mfma_f32_16x16x32_bf16 v[152:155], a[168:171], a[40:43], v[152:155]\n"
    "buffer_load_dwordx4 v248, s[8:11], s41 offen offset:1024 sc1 lds\n"
    "v_mfma_f32_16x16x32_bf16 v[160:163], a[168:171], a[72:75], v[160:163]\n"
    "ds_read_b64_tr_b16 a[162:163], v244 offset:26624\n"
    "v_mfma_f32_16x16x32_bf16 v[160:163], a[172:175], a[76:79], v[160:163]\n"
    "v_exp_f32_e32 v142, v142\n"
    "v_mfma_f32_16x16x32_bf16 v[168:171], a[172:175], a[108:111], v[168:171]\n"
    "ds_read2st64_b32 v[208:209], v249 offset0:12 offset1:13\n"
    "v_mfma_f32_16x16x32_bf16 v[168:171], a[168:171], a[104:107], v[168:171]\n"
    "buffer_load_dwordx4 v248, s[8:11], s41 offen offset:2048 sc1 lds\n"
    "v_mfma_f32_16x16x32_bf16 v[176:179], a[168:171], a[136:139], v[176:179]\n"
    "ds_read_b64_tr_b16 a[164:165], v244 offset:57344\n"
    "v_mfma_f32_16x16x32_bf16 v[176:179], a[172:175], a[140:143], v[176:179]\n"
    "s_waitcnt lgkmcnt(10)\n"
    "ds_read2st64_b32 v[210:211], v249 offset0:14 offset1:15\n"
    "v_mfma_f32_16x16x32_bf16 v[176:179], a[176:179], a[144:147], v[176:179]\n"
    "ds_read_b64_tr_b16 a[168:169], v245 offset:24576\n"
    "v_mfma_f32_16x16x32_bf16 v[176:179], a[180:183], a[148:151], v[176:179]\n"
    "buffer_load_dwordx4 v248, s[8:11], s41 offen offset:3072 sc1 lds\n"
    "v_mfma_f32_16x16x32_bf16 v[168:171], a[180:183], a[116:119], v[168:171]\n"
    "ds_read_b64_tr_b16 a[166:167], v244 offset:59392\n"
    "v_mfma_f32_16x16x32_bf16 v[168:171], a[176:179], a[112:115], v[168:171]\n"
    "s_add_u32 s41, s41, 0x8000\n"
    "v_exp_f32_e32 v143, v143\n"
    "v_mfma_f32_16x16x32_bf16 v[160:163], a[176:179], a[80:83], v[160:163]\n"
    "ds_read_b64_tr_b16 a[170:171], v245 offset:26624\n"
    "v_cvt_pk_bf16_f32 v140, v140, v141\n"
    "v_mfma_f32_16x16x32_bf16 v[160:163], a[180:183], a[84:87], v[160:163]\n"
    "ds_read2st64_b32 v[216:217], v249 offset0:16 offset1:17\n"
    "v_mfma_f32_16x16x32_bf16 v[152:155], a[180:183], a[52:55], v[152:155]\n"
    "v_exp_f32_e32 v148, v148\n"
    "v_mfma_f32_16x16x32_bf16 v[152:155], a[176:179], a[48:51], v[152:155]\n"
    "v_exp_f32_e32 v149, v149\n"
    "v_mfma_f32_16x16x32_bf16 v[144:147], a[176:179], a[16:19], v[144:147]\n"
    "ds_read_b64_tr_b16 a[172:173], v245 offset:57344\n"
    "v_cvt_pk_bf16_f32 v141, v142, v143\n"
    "v_mfma_f32_16x16x32_bf16 v[144:147], a[180:183], a[20:23], v[144:147]\n"
    "s_waitcnt lgkmcnt(11)\n"
    "ds_read2st64_b32 v[218:219], v249 offset0:18 offset1:19\n"
    "v_mfma_f32_16x16x32_bf16 v[144:147], a[184:187], a[24:27], v[144:147]\n"
    "ds_read_b64_tr_b16 a[176:177], v246 offset:24576\n"
    "v_mfma_f32_16x16x32_bf16 v[144:147], a[188:191], a[28:31], v[144:147]\n"
    "ds_read_b64_tr_b16 a[174:175], v245 offset:59392\n"
    "v_mfma_f32_16x16x32_bf16 v[152:155], a[188:191], a[60:63], v[152:155]\n"
    "v_exp_f32_e32 v150, v150\n"
    "v_mfma_f32_16x16x32_bf16 v[152:155], a[184:187], a[56:59], v[152:155]\n"
    "v_exp_f32_e32 v151, v151\n"
    "v_mfma_f32_16x16x32_bf16 v[160:163], a[184:187], a[88:91], v[160:163]\n"
    "v_mfma_f32_16x16x32_bf16 v[160:163], a[188:191], a[92:95], v[160:163]\n"
    "ds_read_b64_tr_b16 a[178:179], v246 offset:26624\n"
    "v_mfma_f32_16x16x32_bf16 v[168:171], a[188:191], a[124:127], v[168:171]\n"
    "v_cvt_pk_bf16_f32 v148, v148, v149\n"
    "v_mfma_f32_16x16x32_bf16 v[168:171], a[184:187], a[120:123], v[168:171]\n"
    "v_exp_f32_e32 v156, v156\n"
    "v_mfma_f32_16x16x32_bf16 v[176:179], a[184:187], a[152:155], v[176:179]\n"
    "ds_read_b64_tr_b16 a[180:181], v246 offset:57344\n"
    "v_mfma_f32_16x16x32_bf16 v[176:179], a[188:191], a[156:159], v[176:179]\n"
    "s_waitcnt lgkmcnt(4)\n"
    "v_exp_f32_e32 v157, v157\n"
    "v_mfma_f32_16x16x32_bf16 a[236:239], a[160:163], v[180:183], a[236:239]\n"
    "v_mfma_f32_16x16x32_bf16 a[236:239], a[164:167], v[184:187], a[236:239]\n"
    "ds_read_b64_tr_b16 a[182:183], v246 offset:59392\n"
    "v_mfma_f32_16x16x32_bf16 a[240:243], a[164:167], v[192:195], a[240:243]\n"
    "ds_read_b64_tr_b16 a[184:185], v247 offset:24576\n"
    "v_mfma_f32_16x16x32_bf16 a[240:243], a[160:163], v[188:191], a[240:243]\n"
    "v_cvt_pk_bf16_f32 v149, v150, v151\n"
    "v_mfma_f32_16x16x32_bf16 a[244:247], a[160:163], v[196:199], a[244:247]\n"
    "ds_read_b64_tr_b16 a[186:187], v247 offset:26624\n"
    "v_mfma_f32_16x16x32_bf16 a[244:247], a[164:167], v[200:203], a[244:247]\n"
    "v_exp_f32_e32 v158, v158\n"
    "v_mfma_f32_16x16x32_bf16 a[248:251], a[164:167], v[208:211], a[248:251]\n"
    "v_exp_f32_e32 v159, v159\n"
    "v_mfma_f32_16x16x32_bf16 a[248:251], a[160:163], v[204:207], a[248:251]\n"
    "v_cvt_pk_bf16_f32 v156, v156, v157\n"
    "v_mfma_f32_16x16x32_bf16 a[252:255], a[160:163], v[212:215], a[252:255]\n"
    "ds_read_b64_tr_b16 a[188:189], v247 offset:57344\n"
    "v_mfma_f32_16x16x32_bf16 a[252:255], a[164:167], v[216:219], a[252:255]\n"
    "s_waitcnt lgkmcnt(6)\n"
    "v_exp_f32_e32 v164, v164\n"
    "v_mfma_f32_16x16x32_bf16 v[16:19], a[172:175], v[216:219], v[16:19]\n"
    "ds_read_b64_tr_b16 a[160:161], v244 offset:28672\n"
    "v_mfma_f32_16x16x32_bf16 v[16:19], a[168:171], v[212:215], v[16:19]\n"
    "ds_read_b64_tr_b16 a[190:191], v247 offset:59392\n"
    "v_mfma_f32_16x16x32_bf16 v[12:15], a[168:171], v[204:207], v[12:15]\n"
    "v_exp_f32_e32 v165, v165\n"
    "v_mfma_f32_16x16x32_bf16 v[12:15], a[172:175], v[208:211], v[12:15]\n"
    "v_cvt_pk_bf16_f32 v157, v158, v159\n"
    "v_mfma_f32_16x16x32_bf16 v[8:11], a[172:175], v[200:203], v[8:11]\n"
    "ds_read_b64_tr_b16 a[162:163], v244 offset:30720\n"
    "v_mfma_f32_16x16x32_bf16 v[8:11], a[168:171], v[196:199], v[8:11]\n"
    "v_exp_f32_e32 v166, v166\n"
    "v_mfma_f32_16x16x32_bf16 v[4:7], a[168:171], v[188:191], v[4:7]\n"
    "v_exp_f32_e32 v167, v167\n"
    "v_mfma_f32_16x16x32_bf16 v[4:7], a[172:175], v[192:195], v[4:7]\n"
    "v_cvt_pk_bf16_f32 v164, v164, v165\n"
    "v_mfma_f32_16x16x32_bf16 v[0:3], a[172:175], v[184:187], v[0:3]\n"
    "ds_read_b64_tr_b16 a[164:165], v244 offset:61440\n"
    "v_mfma_f32_16x16x32_bf16 v[0:3], a[168:171], v[180:183], v[0:3]\n"
    "s_waitcnt lgkmcnt(7)\n"
    "v_exp_f32_e32 v172, v172\n"
    "v_mfma_f32_16x16x32_bf16 v[20:23], a[176:179], v[180:183], v[20:23]\n"
    "v_mfma_f32_16x16x32_bf16 v[20:23], a[180:183], v[184:187], v[20:23]\n"
    "ds_read_b64_tr_b16 a[166:167], v244 offset:63488\n"
    "v_mfma_f32_16x16x32_bf16 v[24:27], a[180:183], v[192:195], v[24:27]\n"
    "ds_read_b64_tr_b16 a[168:169], v245 offset:28672\n"
    "v_mfma_f32_16x16x32_bf16 v[24:27], a[176:179], v[188:191], v[24:27]\n"
    "v_exp_f32_e32 v173, v173\n"
    "v_mfma_f32_16x16x32_bf16 v[28:31], a[176:179], v[196:199], v[28:31]\n"
    "ds_read_b64_tr_b16 a[170:171], v245 offset:30720\n"
    "v_mfma_f32_16x16x32_bf16 v[28:31], a[180:183], v[200:203], v[28:31]\n"
    "v_cvt_pk_bf16_f32 v165, v166, v167\n"
    "v_mfma_f32_16x16x32_bf16 v[32:35], a[180:183], v[208:211], v[32:35]\n"
    "v_exp_f32_e32 v174, v174\n"
    "v_mfma_f32_16x16x32_bf16 v[32:35], a[176:179], v[204:207], v[32:35]\n"
    "v_exp_f32_e32 v175, v175\n"
    "v_mfma_f32_16x16x32_bf16 v[36:39], a[176:179], v[212:215], v[36:39]\n"
    "ds_read_b64_tr_b16 a[172:173], v245 offset:61440\n"
    "v_mfma_f32_16x16x32_bf16 v[36:39], a[180:183], v[216:219], v[36:39]\n"
    "s_waitcnt lgkmcnt(6)\n"
    "v_cvt_pk_bf16_f32 v172, v172, v173\n"
    "v_mfma_f32_16x16x32_bf16 v[56:59], a[188:191], v[216:219], v[56:59]\n"
    "ds_read_b64_tr_b16 a[176:177], v246 offset:28672\n"
    "v_mfma_f32_16x16x32_bf16 v[56:59], a[184:187], v[212:215], v[56:59]\n"
    "ds_read_b64_tr_b16 a[174:175], v245 offset:63488\n"
    "v_mfma_f32_16x16x32_bf16 v[52:55], a[184:187], v[204:207], v[52:55]\n"
    "v_cvt_pk_bf16_f32 v173, v174, v175\n"
    "v_mfma_f32_16x16x32_bf16 v[52:55], a[188:191], v[208:211], v[52:55]\n"
    "v_exp_f32_e32 v144, v144\n"
    "v_mfma_f32_16x16x32_bf16 v[48:51], a[188:191], v[200:203], v[48:51]\n"
    "ds_read_b64_tr_b16 a[178:179], v246 offset:30720\n"
    "v_mfma_f32_16x16x32_bf16 v[48:51], a[184:187], v[196:199], v[48:51]\n"
    "v_mfma_f32_16x16x32_bf16 v[44:47], a[184:187], v[188:191], v[44:47]\n"
    "v_exp_f32_e32 v145, v145\n"
    "v_mfma_f32_16x16x32_bf16 v[44:47], a[188:191], v[192:195], v[44:47]\n"
    "v_exp_f32_e32 v146, v146\n"
    "v_mfma_f32_16x16x32_bf16 v[40:43], a[188:191], v[184:187], v[40:43]\n"
    "ds_read_b64_tr_b16 a[180:181], v246 offset:61440\n"
    "v_mfma_f32_16x16x32_bf16 v[40:43], a[184:187], v[180:183], v[40:43]\n"
    "s_waitcnt lgkmcnt(7)\n"
    "v_exp_f32_e32 v147, v147\n"
    "v_mfma_f32_16x16x32_bf16 v[60:63], a[160:163], v[180:183], v[60:63]\n"
    "ds_read_b64_tr_b16 a[184:185], v247 offset:28672\n"
    "v_mfma_f32_16x16x32_bf16 v[60:63], a[164:167], v[184:187], v[60:63]\n"
    "ds_read_b64_tr_b16 a[182:183], v246 offset:63488\n"
    "v_mfma_f32_16x16x32_bf16 v[64:67], a[164:167], v[192:195], v[64:67]\n"
    "ds_read_b64_tr_b16 a[186:187], v247 offset:30720\n"
    "v_mfma_f32_16x16x32_bf16 v[64:67], a[160:163], v[188:191], v[64:67]\n"
    "v_cvt_pk_bf16_f32 v142, v144, v145\n"
    "v_mfma_f32_16x16x32_bf16 v[68:71], a[160:163], v[196:199], v[68:71]\n"
    "v_exp_f32_e32 v152, v152\n"
    "v_mfma_f32_16x16x32_bf16 v[68:71], a[164:167], v[200:203], v[68:71]\n"
    "ds_read_b64_tr_b16 a[188:189], v247 offset:61440\n"
    "v_mfma_f32_16x16x32_bf16 v[72:75], a[164:167], v[208:211], v[72:75]\n"
    "ds_read_b64_tr_b16 a[190:191], v247 offset:63488\n"
    "v_mfma_f32_16x16x32_bf16 v[72:75], a[160:163], v[204:207], v[72:75]\n"
    "s_waitcnt vmcnt(8)\n"
    "v_mfma_f32_16x16x32_bf16 v[76:79], a[160:163], v[212:215], v[76:79]\n"
    "s_barrier\n"
    "v_mfma_f32_16x16x32_bf16 v[76:79], a[164:167], v[216:219], v[76:79]\n"
    "s_waitcnt lgkmcnt(7)\n"
    "s_mov_b32 m0, s42\n"
    "v_mfma_f32_16x16x32_bf16 v[96:99], a[172:175], v[216:219], v[96:99]\n"
    "v_exp_f32_e32 v153, v153\n"
    "v_mfma_f32_16x16x32_bf16 v[96:99], a[168:171], v[212:215], v[96:99]\n"
    "v_cvt_pk_bf16_f32 v143, v146, v147\n"
    "v_mfma_f32_16x16x32_bf16 v[92:95], a[168:171], v[204:207], v[92:95]\n"
    "v_exp_f32_e32 v154, v154\n"
    "v_mfma_f32_16x16x32_bf16 v[92:95], a[172:175], v[208:211], v[92:95]\n"
    "ds_read_b128 a[160:163], v240 offset:8192\n"
    "v_mfma_f32_16x16x32_bf16 v[88:91], a[172:175], v[200:203], v[88:91]\n"
    "ds_write_addtid_b32 v140 offset:0\n"
    "v_mfma_f32_16x16x32_bf16 v[88:91], a[168:171], v[196:199], v[88:91]\n"
    "v_exp_f32_e32 v155, v155\n"
    "v_mfma_f32_16x16x32_bf16 v[84:87], a[168:171], v[188:191], v[84:87]\n"
    "ds_write_addtid_b32 v141 offset:256\n"
    "v_mfma_f32_16x16x32_bf16 v[84:87], a[172:175], v[192:195], v[84:87]\n"
    "ds_write_addtid_b32 v148 offset:1024\n"
    "v_mfma_f32_16x16x32_bf16 v[80:83], a[172:175], v[184:187], v[80:83]\n"
    "v_cvt_pk_bf16_f32 v150, v152, v153\n"
    "v_mfma_f32_16x16x32_bf16 v[80:83], a[168:171], v[180:183], v[80:83]\n"
    "s_waitcnt lgkmcnt(7)\n"
    "v_exp_f32_e32 v160, v160\n"
    "v_mfma_f32_16x16x32_bf16 v[100:103], a[176:179], v[180:183], v[100:103]\n"
    "ds_read_b128 a[164:167], v241 offset:8192\n"
    "v_mfma_f32_16x16x32_bf16 v[100:103], a[180:183], v[184:187], v[100:103]\n"
    "ds_write_addtid_b32 v149 offset:1280\n"
    "v_mfma_f32_16x16x32_bf16 v[104:107], a[180:183], v[192:195], v[104:107]\n"
    "v_exp_f32_e32 v161, v161\n"
    "v_mfma_f32_16x16x32_bf16 v[104:107], a[176:179], v[188:191], v[104:107]\n"
    "ds_read_b128 a[168:171], v240 offset:12288\n"
    "v_mfma_f32_16x16x32_bf16 v[108:111], a[176:179], v[196:199], v[108:111]\n"
    "ds_write_addtid_b32 v156 offset:2048\n"
    "v_mfma_f32_16x16x32_bf16 v[108:111], a[180:183], v[200:203], v[108:111]\n"
    "v_cvt_pk_bf16_f32 v151, v154, v155\n"
    "v_mfma_f32_16x16x32_bf16 v[112:115], a[180:183], v[208:211], v[112:115]\n"
    "ds_write_addtid_b32 v157 offset:2304\n"
    "v_mfma_f32_16x16x32_bf16 v[112:115], a[176:179], v[204:207], v[112:115]\n"
    "v_exp_f32_e32 v162, v162\n"
    "v_mfma_f32_16x16x32_bf16 v[116:119], a[176:179], v[212:215], v[116:119]\n"
    "ds_write_addtid_b32 v164 offset:3072\n"
    "v_mfma_f32_16x16x32_bf16 v[116:119], a[180:183], v[216:219], v[116:119]\n"
    "s_waitcnt lgkmcnt(10)\n"
    "v_exp_f32_e32 v163, v163\n"
    "v_mfma_f32_16x16x32_bf16 v[136:139], a[188:191], v[216:219], v[136:139]\n"
    "ds_read_b128 a[172:175], v241 offset:12288\n"
    "v_mfma_f32_16x16x32_bf16 v[136:139], a[184:187], v[212:215], v[136:139]\n"
    "ds_write_addtid_b32 v165 offset:3328\n"
    "v_cvt_pk_bf16_f32 v158, v160, v161\n"
    "v_mfma_f32_16x16x32_bf16 v[132:135], a[184:187], v[204:207], v[132:135]\n"
    "ds_read_b128 a[176:179], v242 offset:8192\n"
    "v_mfma_f32_16x16x32_bf16 v[132:135], a[188:191], v[208:211], v[132:135]\n"
    "ds_write_addtid_b32 v172 offset:4096\n"
    "v_mfma_f32_16x16x32_bf16 v[128:131], a[188:191], v[200:203], v[128:131]\n"
    "v_exp_f32_e32 v168, v168\n"
    "v_mfma_f32_16x16x32_bf16 v[128:131], a[184:187], v[196:199], v[128:131]\n"
    "ds_write_addtid_b32 v173 offset:4352\n"
    "v_mfma_f32_16x16x32_bf16 v[124:127], a[184:187], v[188:191], v[124:127]\n"
    "v_exp_f32_e32 v169, v169\n"
    "v_mfma_f32_16x16x32_bf16 v[124:127], a[188:191], v[192:195], v[124:127]\n"
    "ds_read_b128 a[180:183], v243 offset:8192\n"
    "v_mfma_f32_16x16x32_bf16 v[120:123], a[188:191], v[184:187], v[120:123]\n"
    "ds_write_addtid_b32 v142 offset:512\n"
    "v_mfma_f32_16x16x32_bf16 v[120:123], a[184:187], v[180:183], v[120:123]\n"
    "s_waitcnt lgkmcnt(12)\n"
    "v_cvt_pk_bf16_f32 v159, v162, v163\n"
    ".Lat_in:\n"
    "v_mfma_f32_16x16x32_bf16 v[212:215], a[160:163], a[128:131], v[236:239]\n"
    ".long 0xbedf00ff\n"
    ".Lat_w4:\n"
    "s_setpc_b64 s[2:3]\n"
    "v_mfma_f32_16x16x32_bf16 v[212:215], a[164:167], a[132:135], v[212:215]\n"
    "v_exp_f32_e32 v170, v170\n"
    "v_mfma_f32_16x16x32_bf16 v[204:207], a[164:167], a[100:103], v[232:235]\n"
    "ds_write_addtid_b32 v143 offset:768\n"
    "v_mfma_f32_16x16x32_bf16 v[204:207], a[160:163], a[96:99], v[204:207]\n"
    "ds_read_b128 a[184:187], v242 offset:12288\n"
    "v_mfma_f32_16x16x32_bf16 v[196:199], a[160:163], a[64:67], v[228:231]\n"
    "v_exp_f32_e32 v171, v171\n"
    "v_mfma_f32_16x16x32_bf16 v[196:199], a[164:167], a[68:71], v[196:199]\n"
    "ds_write_addtid_b32 v150 offset:1536\n"
    "v_mfma_f32_16x16x32_bf16 v[188:191], a[164:167], a[36:39], v[224:227]\n"
    "v_cvt_pk_bf16_f32 v166, v168, v169\n"
    "v_mfma_f32_16x16x32_bf16 v[188:191], a[160:163], a[32:35], v[188:191]\n"
    "v_mfma_f32_16x16x32_bf16 v[180:183], a[160:163], a[0:3], v[220:223]\n"
    "ds_write_addtid_b32 v151 offset:1792\n"
    "v_mfma_f32_16x16x32_bf16 v[180:183], a[164:167], a[4:7], v[180:183]\n"
    "s_waitcnt lgkmcnt(10)\n"
    "v_exp_f32_e32 v176, v176\n"
    "v_mfma_f32_16x16x32_bf16 v[180:183], a[168:171], a[8:11], v[180:183]\n"
    "ds_read_b128 a[188:191], v243 offset:12288\n"
    "v_mfma_f32_16x16x32_bf16 v[180:183], a[172:175], a[12:15], v[180:183]\n"
    "v_exp_f32_e32 v177, v177\n"
    "v_mfma_f32_16x16x32_bf16 v[188:191], a[172:175], a[44:47], v[188:191]\n"
    "ds_write_addtid_b32 v158 offset:2560\n"
    "v_mfma_f32_16x16x32_bf16 v[188:191], a[168:171], a[40:43], v[188:191]\n"
    "ds_read_b128 a[160:163], v240 offset:10240\n"
    "v_mfma_f32_16x16x32_bf16 v[196:199], a[168:171], a[72:75], v[196:199]\n"
    "v_cvt_pk_bf16_f32 v167, v170, v171\n"
    "v_mfma_f32_16x16x32_bf16 v[196:199], a[172:175], a[76:79], v[196:199]\n"
    "ds_write_addtid_b32 v159 offset:2816\n"
    "v_mfma_f32_16x16x32_bf16 v[204:207], a[172:175], a[108:111], v[204:207]\n"
    "v_exp_f32_e32 v178, v178\n"
    "v_mfma_f32_16x16x32_bf16 v[204:207], a[168:171], a[104:107], v[204:207]\n"
    "ds_write_addtid_b32 v166 offset:3584\n"
    "v_mfma_f32_16x16x32_bf16 v[212:215], a[168:171], a[136:139], v[212:215]\n"
    "v_mfma_f32_16x16x32_bf16 v[212:215], a[172:175], a[140:143], v[212:215]\n"
    "s_waitcnt lgkmcnt(10)\n"
    "v_exp_f32_e32 v179, v179\n"
    "v_mfma_f32_16x16x32_bf16 v[212:215], a[176:179], a[144:147], v[212:215]\n"
    "ds_read_b128 a[164:167], v241 offset:10240\n"
    "v_mfma_f32_16x16x32_bf16 v[212:215], a[180:183], a[148:151], v[212:215]\n"
    "v_cvt_pk_bf16_f32 v174, v176, v177\n"
    "v_mfma_f32_16x16x32_bf16 v[204:207], a[180:183], a[116:119], v[204:207]\n"
    "ds_write_addtid_b32 v167 offset:3840\n"
    "v_mfma_f32_16x16x32_bf16 v[204:207], a[176:179], a[112:115], v[204:207]\n"
    "ds_read_b128 a[168:171], v240 offset:14336\n"
    "v_mfma_f32_16x16x32_bf16 v[196:199], a[176:179], a[80:83], v[196:199]\n"
    "v_cvt_pk_bf16_f32 v175, v178, v179\n"
    "v_mfma_f32_16x16x32_bf16 v[196:199], a[180:183], a[84:87], v[196:199]\n"
    "ds_write_addtid_b32 v174 offset:4608\n"
    "v_mfma_f32_16x16x32_bf16 v[188:191], a[180:183], a[52:55], v[188:191]\n"
    "ds_write_addtid_b32 v175 offset:4864\n"
    "v_mfma_f32_16x16x32_bf16 v[188:191], a[176:179], a[48:51], v[188:191]\n"
    "v_mfma_f32_16x16x32_bf16 v[180:183], a[176:179], a[16:19], v[180:183]\n"
    "ds_read_b128 a[172:175], v241 offset:14336\n"
    "v_mfma_f32_16x16x32_bf16 v[180:183], a[180:183], a[20:23], v[180:183]\n"
    "s_waitcnt lgkmcnt(10)\n"
    "s_waitcnt lgkmcnt(1)\n"
    "v_mfma_f32_16x16x32_bf16 v[180:183], a[184:187], a[24:27], v[180:183]\n"
    "s_barrier\n"
    "v_mfma_f32_16x16x32_bf16 v[180:183], a[188:191], a[28:31], v[180:183]\n"
    "s_add_u32 m0, s43, 24576\n"
    "v_mfma_f32_16x16x32_bf16 v[188:191], a[188:191], a[60:63], v[188:191]\n"
    "ds_read_b128 a[176:179], v242 offset:10240\n"
    "v_mfma_f32_16x16x32_bf16 v[188:191], a[184:187], a[56:59], v[188:191]\n"
    "buffer_load_dwordx4 v248, s[8:11], s40 offen offset:0 sc1 lds\n"
    "v_mfma_f32_16x16x32_bf16 v[196:199], a[184:187], a[88:91], v[196:199]\n"
    "ds_read2st64_b32 v[144:145], v249 offset0:0 offset1:1\n"
    "v_mfma_f32_16x16x32_bf16 v[196:199], a[188:191], a[92:95], v[196:199]\n"
    "v_mfma_f32_16x16x32_bf16 v[204:207], a[188:191], a[124:127], v[204:207]\n"
    "ds_read2st64_b32 v[146:147], v249 offset0:2 offset1:3\n"
    "v_mfma_f32_16x16x32_bf16 v[204:207], a[184:187], a[120:123], v[204:207]\n"
    "buffer_load_dwordx4 v248, s[8:11], s40 offen offset:1024 sc1 lds\n"
    "v_mfma_f32_16x16x32_bf16 v[212:215], a[184:187], a[152:155], v[212:215]\n"
    "v_mfma_f32_16x16x32_bf16 v[212:215], a[188:191], a[156:159], v[212:215]\n"
    "v_mfma_f32_16x16x32_bf16 v[216:219], a[160:163], a[128:131], v[236:239]\n"
    "ds_read_b128 a[180:183], v243 offset:10240\n"
    "v_mfma_f32_16x16x32_bf16 v[216:219], a[164:167], a[132:135], v[216:219]\n"
    "buffer_load_dwordx4 v248, s[8:11], s40 offen offset:2048 sc1 lds\n"
    "v_mfma_f32_16x16x32_bf16 v[208:211], a[164:167], a[100:103], v[232:235]\n"
    "ds_read2st64_b32 v[152:153], v249 offset0:4 offset1:5\n"
    "v_mfma_f32_16x16x32_bf16 v[208:211], a[160:163], a[96:99], v[208:211]\n"
    "ds_read_b128 a[184:187], v242 offset:14336\n"
    "v_mfma_f32_16x16x32_bf16 v[200:203], a[160:163], a[64:67], v[228:231]\n"
    "ds_read2st64_b32 v[154:155], v249 offset0:6 offset1:7\n"
    "v_mfma_f32_16x16x32_bf16 v[200:203], a[164:167], a[68:71], v[200:203]\n"
    "buffer_load_dwordx4 v248, s[8:11], s40 offen offset:3072 sc1 lds\n"
    "v_mfma_f32_16x16x32_bf16 v[192:195], a[164:167], a[36:39], v[224:227]\n"
    "s_add_u32 m0, s43, 28672\n"
    "v_exp_f32_e32 v180, v180\n"
    "v_mfma_f32_16x16x32_bf16 v[192:195], a[160:163], a[32:35], v[192:195]\n"
    "s_add_u32 s40, s40, 0x8000\n"
    "v_exp_f32_e32 v181, v181\n"
    "v_mfma_f32_16x16x32_bf16 v[184:187], a[160:163], a[0:3], v[220:223]\n"
    "ds_read2st64_b32 v[160:161], v249 offset0:8 offset1:9\n"
    "v_mfma_f32_16x16x32_bf16 v[184:187], a[164:167], a[4:7], v[184:187]\n"
    "s_waitcnt lgkmcnt(8)\n"
    "buffer_load_dwordx4 v248, s[8:11], s41 offen offset:0 sc1 lds\n"
    "v_mfma_f32_16x16x32_bf16 v[184:187], a[168:171], a[8:11], v[184:187]\n"
    "ds_read_b128 a[188:191], v243 offset:14336\n"
    "v_mfma_f32_16x16x32_bf16 v[184:187], a[172:175], a[12:15], v[184:187]\n"
    "ds_read_b64_tr_b16 a[160:161], v244 offset:0\n"
    "v_mfma_f32_16x16x32_bf16 v[192:195], a[172:175], a[44:47], v[192:195]\n"
    "ds_read2st64_b32 v[162:163], v249 offset0:10 offset1:11\n"
    "v_mfma_f32_16x16x32_bf16 v[192:195], a[168:171], a[40:43], v[192:195]\n"
    "buffer_load_dwordx4 v248, s[8:11], s41 offen offset:1024 sc1 lds\n"
    "v_mfma_f32_16x16x32_bf16 v[200:203], a[168:171], a[72:75], v[200:203]\n"
    "ds_read_b64_tr_b16 a[162:163], v244 offset:2048\n"
    "v_mfma_f32_16x16x32_bf16 v[200:203], a[172:175], a[76:79], v[200:203]\n"
    "v_exp_f32_e32 v182, v182\n"
    "v_mfma_f32_16x16x32_bf16 v[208:211], a[172:175], a[108:111], v[208:211]\n"
    "ds_read2st64_b32 v[168:169], v249 offset0:12 offset1:13\n"
    "v_mfma_f32_16x16x32_bf16 v[208:211], a[168:171], a[104:107], v[208:211]\n"
    "buffer_load_dwordx4 v248, s[8:11], s41 offen offset:2048 sc1 lds\n"
    "v_mfma_f32_16x16x32_bf16 v[216:219], a[168:171], a[136:139], v[216:219]\n"
    "ds_read_b64_tr_b16 a[164:165], v244 offset:32768\n"
    "v_mfma_f32_16x16x32_bf16 v[216:219], a[172:175], a[140:143], v[216:219]\n"
    "s_waitcnt lgkmcnt(10)\n"
    "ds_read2st64_b32 v[170:171], v249 offset0:14 offset1:15\n"
    "v_mfma_f32_16x16x32_bf16 v[216:219], a[176:179], a[144:147], v[216:219]\n"
    "ds_read_b64_tr_b16 a[168:169], v245 offset:0\n"
    "v_mfma_f32_16x16x32_bf16 v[216:219], a[180:183], a[148:151], v[216:219]\n"
    "buffer_load_dwordx4 v248, s[8:11], s41 offen offset:3072 sc1 lds\n"
    "v_mfma_f32_16x16x32_bf16 v[208:211], a[180:183], a[116:119], v[208:211]\n"
    "ds_read_b64_tr_b16 a[166:167], v244 offset:34816\n"
    "v_mfma_f32_16x16x32_bf16 v[208:211], a[176:179], a[112:115], v[208:211]\n"
    "s_add_u32 s41, s41, 0x8000\n"
    "v_exp_f32_e32 v183, v183\n"
    "v_mfma_f32_16x16x32_bf16 v[200:203], a[176:179], a[80:83], v[200:203]\n"
    "ds_read_b64_tr_b16 a[170:171], v245 offset:2048\n"
    "v_cvt_pk_bf16_f32 v180, v180, v181\n"
    "v_mfma_f32_16x16x32_bf16 v[200:203], a[180:183], a[84:87], v[200:203]\n"
    "ds_read2st64_b32 v[176:177], v249 offset0:16 offset1:17\n"
    "v_mfma_f32_16x16x32_bf16 v[192:195], a[180:183], a[52:55], v[192:195]\n"
    "v_exp_f32_e32 v188, v188\n"
    "v_mfma_f32_16x16x32_bf16 v[192:195], a[176:179], a[48:51], v[192:195]\n"
    "v_exp_f32_e32 v189, v189\n"
    "v_mfma_f32_16x16x32_bf16 v[184:187], a[176:179], a[16:19], v[184:187]\n"
    "ds_read_b64_tr_b16 a[172:173], v245 offset:32768\n"
    "v_cvt_pk_bf16_f32 v181, v182, v183\n"
    "v_mfma_f32_16x16x32_bf16 v[184:187], a[180:183], a[20:23], v[184:187]\n"
    "s_waitcnt lgkmcnt(11)\n"
    "ds_read2st64_b32 v[178:179], v249 offset0:18 offset1:19\n"
    "v_mfma_f32_16x16x32_bf16 v[184:187], a[184:187], a[24:27], v[184:187]\n"
    "ds_read_b64_tr_b16 a[176:177], v246 offset:0\n"
    "v_mfma_f32_16x16x32_bf16 v[184:187], a[188:191], a[28:31], v[184:187]\n"
    "ds_read_b64_tr_b16 a[174:175], v245 offset:34816\n"
    "v_mfma_f32_16x16x32_bf16 v[192:195], a[188:191], a[60:63], v[192:195]\n"
    "v_exp_f32_e32 v190, v190\n"
    "v_mfma_f32_16x16x32_bf16 v[192:195], a[184:187], a[56:59], v[192:195]\n"
    "v_exp_f32_e32 v191, v191\n"
    "v_mfma_f32_16x16x32_bf16 v[200:203], a[184:187], a[88:91], v[200:203]\n"
    "v_mfma_f32_16x16x32_bf16 v[200:203], a[188:191], a[92:95], v[200:203]\n"
    "ds_read_b64_tr_b16 a[178:179], v246 offset:2048\n"
    "v_mfma_f32_16x16x32_bf16 v[208:211], a[188:191], a[124:127], v[208:211]\n"
    "v_cvt_pk_bf16_f32 v188, v188, v189\n"
    "v_mfma_f32_16x16x32_bf16 v[208:211], a[184:187], a[120:123], v[208:211]\n"
    "v_exp_f32_e32 v196, v196\n"
    "v_mfma_f32_16x16x32_bf16 v[216:219], a[184:187], a[152:155], v[216:219]\n"
    "ds_read_b64_tr_b16 a[180:181], v246 offset:32768\n"
    "v_mfma_f32_16x16x32_bf16 v[216:219], a[188:191], a[156:159], v[216:219]\n"
    "s_waitcnt lgkmcnt(4)\n"
    "v_exp_f32_e32 v197, v197\n"
    "v_mfma_f32_16x16x32_bf16 a[236:239], a[160:163], v[140:143], a[236:239]\n"
    "v_mfma_f32_16x16x32_bf16 a[236:239], a[164:167], v[144:147], a[236:239]\n"
    "ds_read_b64_tr_b16 a[182:183], v246 offset:34816\n"
    "v_mfma_f32_16x16x32_bf16 a[240:243], a[164:167], v[152:155], a[240:243]\n"
    "ds_read_b64_tr_b16 a[184:185], v247 offset:0\n"
    "v_mfma_f32_16x16x32_bf16 a[240:243], a[160:163], v[148:151], a[240:243]\n"
    "v_cvt_pk_bf16_f32 v189, v190, v191\n"
    "v_mfma_f32_16x16x32_bf16 a[244:247], a[160:163], v[156:159], a[244:247]\n"
    "ds_read_b64_tr_b16 a[186:187], v247 offset:2048\n"
    "v_mfma_f32_16x16x32_bf16 a[244:247], a[164:167], v[160:163], a[244:247]\n"
    "v_exp_f32_e32 v198, v198\n"
    "v_mfma_f32_16x16x32_bf16 a[248:251], a[164:167], v[168:171], a[248:251]\n"
    "v_exp_f32_e32 v199, v199\n"
    "v_mfma_f32_16x16x32_bf16 a[248:251], a[160:163], v[164:167], a[248:251]\n"
    "v_cvt_pk_bf16_f32 v196, v196, v197\n"
    "v_mfma_f32_16x16x32_bf16 a[252:255], a[160:163], v[172:175], a[252:255]\n"
    "ds_read_b64_tr_b16 a[188:189], v247 offset:32768\n"
    "v_mfma_f32_16x16x32_bf16 a[252:255], a[164:167], v[176:179], a[252:255]\n"
    "s_waitcnt lgkmcnt(6)\n"
    "v_exp_f32_e32 v204, v204\n"
    "v_mfma_f32_16x16x32_bf16 v[16:19], a[172:175], v[176:179], v[16:19]\n"
    "ds_read_b64_tr_b16 a[160:161], v244 offset:4096\n"
    "v_mfma_f32_16x16x32_bf16 v[16:19], a[168:171], v[172:175], v[16:19]\n"
    "ds_read_b64_tr_b16 a[190:191], v247 offset:34816\n"
    "v_mfma_f32_16x16x32_bf16 v[12:15], a[168:171], v[164:167], v[12:15]\n"
    "v_exp_f32_e32 v205, v205\n"
    "v_mfma_f32_16x16x32_bf16 v[12:15], a[172:175], v[168:171], v[12:15]\n"
    "v_cvt_pk_bf16_f32 v197, v198, v199\n"
    "v_mfma_f32_16x16x32_bf16 v[8:11], a[172:175], v[160:163], v[8:11]\n"
    "ds_read_b64_tr_b16 a[162:163], v244 offset:6144\n"
    "v_mfma_f32_16x16x32_bf16 v[8:11], a[168:171], v[156:159], v[8:11]\n"
    "v_exp_f32_e32 v206, v206\n"
    "v_mfma_f32_16x16x32_bf16 v[4:7], a[168:171], v[148:151], v[4:7]\n"
    "v_exp_f32_e32 v207, v207\n"
    "v_mfma_f32_16x16x32_bf16 v[4:7], a[172:175], v[152:155], v[4:7]\n"
    "v_cvt_pk_bf16_f32 v204, v204, v205\n"
    "v_mfma_f32_16x16x32_bf16 v[0:3], a[172:175], v[144:147], v[0:3]\n"
    "ds_read_b64_tr_b16 a[164:165], v244 offset:36864\n"
    "v_mfma_f32_16x16x32_bf16 v[0:3], a[168:171], v[140:143], v[0:3]\n"
    "s_waitcnt lgkmcnt(7)\n"
    "v_exp_f32_e32 v212, v212\n"
    "v_mfma_f32_16x16x32_bf16 v[20:23], a[176:179], v[140:143], v[20:23]\n"
    "buffer_load_dword v253, off, s[8:11], s82 sc1\n"
    "v_mfma_f32_16x16x32_bf16 v[20:23], a[180:183], v[144:147], v[20:23]\n"
    "ds_read_b64_tr_b16 a[166:167], v244 offset:38912\n"
    "v_mfma_f32_16x16x32_bf16 v[24:27], a[180:183], v[152:155], v[24:27]\n"
    "ds_read_b64_tr_b16 a[168:169], v245 offset:4096\n"
    "v_mfma_f32_16x16x32_bf16 v[24:27], a[176:179], v[148:151], v[24:27]\n"
    "v_exp_f32_e32 v213, v213\n"
    "v_mfma_f32_16x16x32_bf16 v[28:31], a[176:179], v[156:159], v[28:31]\n"
    "ds_read_b64_tr_b16 a[170:171], v245 offset:6144\n"
    "v_mfma_f32_16x16x32_bf16 v[28:31], a[180:183], v[160:163], v[28:31]\n"
    "v_cvt_pk_bf16_f32 v205, v206, v207\n"
    "v_mfma_f32_16x16x32_bf16 v[32:35], a[180:183], v[168:171], v[32:35]\n"
    "v_exp_f32_e32 v214, v214\n"
    "v_mfma_f32_16x16x32_bf16 v[32:35], a[176:179], v[164:167], v[32:35]\n"
    "v_exp_f32_e32 v215, v215\n"
    "v_mfma_f32_16x16x32_bf16 v[36:39], a[176:179], v[172:175], v[36:39]\n"
    "ds_read_b64_tr_b16 a[172:173], v245 offset:36864\n"
    "v_mfma_f32_16x16x32_bf16 v[36:39], a[180:183], v[176:179], v[36:39]\n"
    "s_waitcnt lgkmcnt(6)\n"
    "v_cvt_pk_bf16_f32 v212, v212, v213\n"
    "v_mfma_f32_16x16x32_bf16 v[56:59], a[188:191], v[176:179], v[56:59]\n"
    "ds_read_b64_tr_b16 a[176:177], v246 offset:4096\n"
    "v_mfma_f32_16x16x32_bf16 v[56:59], a[184:187], v[172:175], v[56:59]\n"
    "ds_read_b64_tr_b16 a[174:175], v245 offset:38912\n"
    "v_mfma_f32_16x16x32_bf16 v[52:55], a[184:187], v[164:167], v[52:55]\n"
    "v_cvt_pk_bf16_f32 v213, v214, v215\n"
    "v_mfma_f32_16x16x32_bf16 v[52:55], a[188:191], v[168:171], v[52:55]\n"
    "v_exp_f32_e32 v184, v184\n"
    "v_mfma_f32_16x16x32_bf16 v[48:51], a[188:191], v[160:163], v[48:51]\n"
    "ds_read_b64_tr_b16 a[178:179], v246 offset:6144\n"
    "v_mfma_f32_16x16x32_bf16 v[48:51], a[184:187], v[156:159], v[48:51]\n"
    "v_mfma_f32_16x16x32_bf16 v[44:47], a[184:187], v[148:151], v[44:47]\n"
    "v_exp_f32_e32 v185, v185\n"
    "v_mfma_f32_16x16x32_bf16 v[44:47], a[188:191], v[152:155], v[44:47]\n"
    "v_exp_f32_e32 v186, v186\n"
    "v_mfma_f32_16x16x32_bf16 v[40:43], a[188:191], v[144:147], v[40:43]\n"
    "ds_read_b64_tr_b16 a[180:181], v246 offset:36864\n"
    "v_mfma_f32_16x16x32_bf16 v[40:43], a[184:187], v[140:143], v[40:43]\n"
    "s_waitcnt lgkmcnt(7)\n"
    "v_exp_f32_e32 v187, v187\n"
    "v_mfma_f32_16x16x32_bf16 v[60:63], a[160:163], v[140:143], v[60:63]\n"
    "ds_read_b64_tr_b16 a[184:185], v247 offset:4096\n"
    "v_mfma_f32_16x16x32_bf16 v[60:63], a[164:167], v[144:147], v[60:63]\n"
    "ds_read_b64_tr_b16 a[182:183], v246 offset:38912\n"
    "v_mfma_f32_16x16x32_bf16 v[64:67], a[164:167], v[152:155], v[64:67]\n"
    "ds_read_b64_tr_b16 a[186:187], v247 offset:6144\n"
    "v_mfma_f32_16x16x32_bf16 v[64:67], a[160:163], v[148:151], v[64:67]\n"
    "v_cvt_pk_bf16_f32 v182, v184, v185\n"
    "v_mfma_f32_16x16x32_bf16 v[68:71], a[160:163], v[156:159], v[68:71]\n"
    "v_exp_f32_e32 v192, v192\n"
    "v_mfma_f32_16x16x32_bf16 v[68:71], a[164:167], v[160:163], v[68:71]\n"
    "ds_read_b64_tr_b16 a[188:189], v247 offset:36864\n"
    "v_mfma_f32_16x16x32_bf16 v[72:75], a[164:167], v[168:171], v[72:75]\n"
    "ds_read_b64_tr_b16 a[190:191], v247 offset:38912\n"
    "v_mfma_f32_16x16x32_bf16 v[72:75], a[160:163], v[164:167], v[72:75]\n"
    "s_waitcnt vmcnt(9)\n"
    "v_mfma_f32_16x16x32_bf16 v[76:79], a[160:163], v[172:175], v[76:79]\n"
    "s_barrier\n"
    "v_mfma_f32_16x16x32_bf16 v[76:79], a[164:167], v[176:179], v[76:79]\n"
    "s_waitcnt lgkmcnt(7)\n"
    "s_mov_b32 m0, s42\n"
    "v_mfma_f32_16x16x32_bf16 v[96:99], a[172:175], v[176:179], v[96:99]\n"
    "v_exp_f32_e32 v193, v193\n"
    "v_mfma_f32_16x16x32_bf16 v[96:99], a[168:171], v[172:175], v[96:99]\n"
    "v_cvt_pk_bf16_f32 v183, v186, v187\n"
    "v_mfma_f32_16x16x32_bf16 v[92:95], a[168:171], v[164:167], v[92:95]\n"
    "v_exp_f32_e32 v194, v194\n"
    "v_mfma_f32_16x16x32_bf16 v[92:95], a[172:175], v[168:171], v[92:95]\n"
    "ds_read_b128 a[160:163], v240 offset:16384\n"
    "v_mfma_f32_16x16x32_bf16 v[88:91], a[172:175], v[160:163], v[88:91]\n"
    "ds_write_addtid_b32 v180 offset:0\n"
    "v_mfma_f32_16x16x32_bf16 v[88:91], a[168:171], v[156:159], v[88:91]\n"
    "v_exp_f32_e32 v195, v195\n"
    "v_mfma_f32_16x16x32_bf16 v[84:87], a[168:171], v[148:151], v[84:87]\n"
    "ds_write_addtid_b32 v181 offset:256\n"
    "v_mfma_f32_16x16x32_bf16 v[84:87], a[172:175], v[152:155], v[84:87]\n"
    "ds_write_addtid_b32 v188 offset:1024\n"
    "v_mfma_f32_16x16x32_bf16 v[80:83], a[172:175], v[144:147], v[80:83]\n"
    "v_cvt_pk_bf16_f32 v190, v192, v193\n"
    "v_mfma_f32_16x16x32_bf16 v[80:83], a[168:171], v[140:143], v[80:83]\n"
    "s_waitcnt lgkmcnt(7)\n"
    "v_exp_f32_e32 v200, v200\n"
    "v_mfma_f32_16x16x32_bf16 v[100:103], a[176:179], v[140:143], v[100:103]\n"
    "ds_read_b128 a[164:167], v241 offset:16384\n"
    "v_mfma_f32_16x16x32_bf16 v[100:103], a[180:183], v[144:147], v[100:103]\n"
    "ds_write_addtid_b32 v189 offset:1280\n"
    "v_mfma_f32_16x16x32_bf16 v[104:107], a[180:183], v[152:155], v[104:107]\n"
    "v_exp_f32_e32 v201, v201\n"
    "v_mfma_f32_16x16x32_bf16 v[104:107], a[176:179], v[148:151], v[104:107]\n"
    "ds_read_b128 a[168:171], v240 offset:20480\n"
    "v_mfma_f32_16x16x32_bf16 v[108:111], a[176:179], v[156:159], v[108:111]\n"
    "ds_write_addtid_b32 v196 offset:2048\n"
    "v_mfma_f32_16x16x32_bf16 v[108:111], a[180:183], v[160:163], v[108:111]\n"
    "v_cvt_pk_bf16_f32 v191, v194, v195\n"
    "v_mfma_f32_16x16x32_bf16 v[112:115], a[180:183], v[168:171], v[112:115]\n"
    "ds_write_addtid_b32 v197 offset:2304\n"
    "v_mfma_f32_16x16x32_bf16 v[112:115], a[176:179], v[164:167], v[112:115]\n"
    "v_exp_f32_e32 v202, v202\n"
    "v_mfma_f32_16x16x32_bf16 v[116:119], a[176:179], v[172:175], v[116:119]\n"
    "ds_write_addtid_b32 v204 offset:3072\n"
    "v_mfma_f32_16x16x32_bf16 v[116:119], a[180:183], v[176:179], v[116:119]\n"
    "s_waitcnt lgkmcnt(10)\n"
    "v_exp_f32_e32 v203, v203\n"
    "v_mfma_f32_16x16x32_bf16 v[136:139], a[188:191], v[176:179], v[136:139]\n"
    "ds_read_b128 a[172:175], v241 offset:20480\n"
    "v_mfma_f32_16x16x32_bf16 v[136:139], a[184:187], v[172:175], v[136:139]\n"
    "ds_write_addtid_b32 v205 offset:3328\n"
    "v_cvt_pk_bf16_f32 v198, v200, v201\n"
    "v_mfma_f32_16x16x32_bf16 v[132:135], a[184:187], v[164:167], v[132:135]\n"
    "ds_read_b128 a[176:179], v242 offset:16384\n"
    "v_mfma_f32_16x16x32_bf16 v[132:135], a[188:191], v[168:171], v[132:135]\n"
    "ds_write_addtid_b32 v212 offset:4096\n"
    "v_mfma_f32_16x16x32_bf16 v[128:131], a[188:191], v[160:163], v[128:131]\n"
    "v_exp_f32_e32 v208, v208\n"
    "v_mfma_f32_16x16x32_bf16 v[128:131], a[184:187], v[156:159], v[128:131]\n"
    "ds_write_addtid_b32 v213 offset:4352\n"
    "v_mfma_f32_16x16x32_bf16 v[124:127], a[184:187], v[148:151], v[124:127]\n"
    "v_exp_f32_e32 v209, v209\n"
    "v_mfma_f32_16x16x32_bf16 v[124:127], a[188:191], v[152:155], v[124:127]\n"
    "ds_read_b128 a[180:183], v243 offset:16384\n"
    "v_mfma_f32_16x16x32_bf16 v[120:123], a[188:191], v[144:147], v[120:123]\n"
    "ds_write_addtid_b32 v182 offset:512\n"
    "v_mfma_f32_16x16x32_bf16 v[120:123], a[184:187], v[140:143], v[120:123]\n"
    "s_waitcnt lgkmcnt(12)\n"
    "v_cvt_pk_bf16_f32 v199, v202, v203\n"
    "v_mfma_f32_16x16x32_bf16 v[172:175], a[160:163], a[128:131], v[236:239]\n"
    "v_mfma_f32_16x16x32_bf16 v[172:175], a[164:167], a[132:135], v[172:175]\n"
    "v_exp_f32_e32 v210, v210\n"
    "v_mfma_f32_16x16x32_bf16 v[164:167], a[164:167], a[100:103], v[232:235]\n"
    "ds_write_addtid_b32 v183 offset:768\n"
    "v_mfma_f32_16x16x32_bf16 v[164:167], a[160:163], a[96:99], v[164:167]\n"
    "ds_read_b128 a[184:187], v242 offset:20480\n"
    "v_mfma_f32_16x16x32_bf16 v[156:159], a[160:163], a[64:67], v[228:231]\n"
    "v_exp_f32_e32 v211, v211\n"
    "v_mfma_f32_16x16x32_bf16 v[156:159], a[164:167], a[68:71], v[156:159]\n"
    "ds_write_addtid_b32 v190 offset:1536\n"
    "v_mfma_f32_16x16x32_bf16 v[148:151], a[164:167], a[36:39], v[224:227]\n"
    "v_cvt_pk_bf16_f32 v206, v208, v209\n"
    "v_mfma_f32_16x16x32_bf16 v[148:151], a[160:163], a[32:35], v[148:151]\n"
    "v_mfma_f32_16x16x32_bf16 v[140:143], a[160:163], a[0:3], v[220:223]\n"
    "ds_write_addtid_b32 v191 offset:1792\n"
    "v_mfma_f32_16x16x32_bf16 v[140:143], a[164:167], a[4:7], v[140:143]\n"
    "s_waitcnt lgkmcnt(10)\n"
    "v_exp_f32_e32 v216, v216\n"
    "v_mfma_f32_16x16x32_bf16 v[140:143], a[168:171], a[8:11], v[140:143]\n"
    "ds_read_b128 a[188:191], v243 offset:20480\n"
    "v_mfma_f32_16x16x32_bf16 v[140:143], a[172:175], a[12:15], v[140:143]\n"
    "v_exp_f32_e32 v217, v217\n"
    "v_mfma_f32_16x16x32_bf16 v[148:151], a[172:175], a[44:47], v[148:151]\n"
    "ds_write_addtid_b32 v198 offset:2560\n"
    "v_mfma_f32_16x16x32_bf16 v[148:151], a[168:171], a[40:43], v[148:151]\n"
    "ds_read_b128 a[160:163], v240 offset:18432\n"
    "v_mfma_f32_16x16x32_bf16 v[156:159], a[168:171], a[72:75], v[156:159]\n"
    "v_cvt_pk_bf16_f32 v207, v210, v211\n"
    "v_mfma_f32_16x16x32_bf16 v[156:159], a[172:175], a[76:79], v[156:159]\n"
    "ds_write_addtid_b32 v199 offset:2816\n"
    "v_mfma_f32_16x16x32_bf16 v[164:167], a[172:175], a[108:111], v[164:167]\n"
    "v_exp_f32_e32 v218, v218\n"
    "v_mfma_f32_16x16x32_bf16 v[164:167], a[168:171], a[104:107], v[164:167]\n"
    "ds_write_addtid_b32 v206 offset:3584\n"
    "v_mfma_f32_16x16x32_bf16 v[172:175], a[168:171], a[136:139], v[172:175]\n"
    "v_mfma_f32_16x16x32_bf16 v[172:175], a[172:175], a[140:143], v[172:175]\n"
    "s_waitcnt lgkmcnt(10)\n"
    "v_exp_f32_e32 v219, v219\n"
    "v_mfma_f32_16x16x32_bf16 v[172:175], a[176:179], a[144:147], v[172:175]\n"
    "ds_read_b128 a[164:167], v241 offset:18432\n"
    "v_mfma_f32_16x16x32_bf16 v[172:175], a[180:183], a[148:151], v[172:175]\n"
    "v_cvt_pk_bf16_f32 v214, v216, v217\n"
    "v_mfma_f32_16x16x32_bf16 v[164:167], a[180:183], a[116:119], v[164:167]\n"
    "ds_write_addtid_b32 v207 offset:3840\n"
    "v_mfma_f32_16x16x32_bf16 v[164:167], a[176:179], a[112:115], v[164:167]\n"
    "ds_read_b128 a[168:171], v240 offset:22528\n"
    "v_mfma_f32_16x16x32_bf16 v[156:159], a[176:179], a[80:83], v[156:159]\n"
    "v_cvt_pk_bf16_f32 v215, v218, v219\n"
    "v_mfma_f32_16x16x32_bf16 v[156:159], a[180:183], a[84:87], v[156:159]\n"
    "ds_write_addtid_b32 v214 offset:4608\n"
    "v_mfma_f32_16x16x32_bf16 v[148:151], a[180:183], a[52:55], v[148:151]\n"
    "ds_write_addtid_b32 v215 offset:4864\n"
    "v_mfma_f32_16x16x32_bf16 v[148:151], a[176:179], a[48:51], v[148:151]\n"
    "v_mfma_f32_16x16x32_bf16 v[140:143], a[176:179], a[16:19], v[140:143]\n"
    "ds_read_b128 a[172:175], v241 offset:22528\n"
    "v_mfma_f32_16x16x32_bf16 v[140:143], a[180:183], a[20:23], v[140:143]\n"
    "s_waitcnt lgkmcnt(10)\n"
    "s_waitcnt lgkmcnt(1)\n"
    "v_mfma_f32_16x16x32_bf16 v[140:143], a[184:187], a[24:27], v[140:143]\n"
    "s_barrier\n"
    "v_mfma_f32_16x16x32_bf16 v[140:143], a[188:191], a[28:31], v[140:143]\n"
    "s_add_u32 m0, s43, 0\n"
    "v_mfma_f32_16x16x32_bf16 v[148:151], a[188:191], a[60:63], v[148:151]\n"
    "ds_read_b128 a[176:179], v242 offset:18432\n"
    "v_mfma_f32_16x16x32_bf16 v[148:151], a[184:187], a[56:59], v[148:151]\n"
    "buffer_load_dwordx4 v248, s[8:11], s40 offen offset:0 sc1 lds\n"
    "v_mfma_f32_16x16x32_bf16 v[156:159], a[184:187], a[88:91], v[156:159]\n"
    "ds_read2st64_b32 v[184:185], v249 offset0:0 offset1:1\n"
    "v_mfma_f32_16x16x32_bf16 v[156:159], a[188:191], a[92:95], v[156:159]\n"
    "v_mfma_f32_16x16x32_bf16 v[164:167], a[188:191], a[124:127], v[164:167]\n"
    "ds_read2st64_b32 v[186:187], v249 offset0:2 offset1:3\n"
    "v_mfma_f32_16x16x32_bf16 v[164:167], a[184:187], a[120:123], v[164:167]\n"
    "buffer_load_dwordx4 v248, s[8:11], s40 offen offset:1024 sc1 lds\n"
    "v_mfma_f32_16x16x32_bf16 v[172:175], a[184:187], a[152:155], v[172:175]\n"
    "v_mfma_f32_16x16x32_bf16 v[172:175], a[188:191], a[156:159], v[172:175]\n"
    "v_mfma_f32_16x16x32_bf16 v[176:179], a[160:163], a[128:131], v[236:239]\n"
    "ds_read_b128 a[180:183], v243 offset:18432\n"
    "v_mfma_f32_16x16x32_bf16 v[176:179], a[164:167], a[132:135], v[176:179]\n"
    "buffer_load_dwordx4 v248, s[8:11], s40 offen offset:2048 sc1 lds\n"
    "v_mfma_f32_16x16x32_bf16 v[168:171], a[164:167], a[100:103], v[232:235]\n"
    "ds_read2st64_b32 v[192:193], v249 offset0:4 offset1:5\n"
    "v_mfma_f32_16x16x32_bf16 v[168:171], a[160:163], a[96:99], v[168:171]\n"
    "ds_read_b128 a[184:187], v242 offset:22528\n"
    "v_mfma_f32_16x16x32_bf16 v[160:163], a[160:163], a[64:67], v[228:231]\n"
    "ds_read2st64_b32 v[194:195], v249 offset0:6 offset1:7\n"
    "v_mfma_f32_16x16x32_bf16 v[160:163], a[164:167], a[68:71], v[160:163]\n"
    "buffer_load_dwordx4 v248, s[8:11], s40 offen offset:3072 sc1 lds\n"
    "v_mfma_f32_16x16x32_bf16 v[152:155], a[164:167], a[36:39], v[224:227]\n"
    "s_add_u32 m0, s43, 4096\n"
    "v_exp_f32_e32 v140, v140\n"
    "v_mfma_f32_16x16x32_bf16 v[152:155], a[160:163], a[32:35], v[152:155]\n"
    "s_add_u32 s40, s40, 0x8000\n"
    "v_exp_f32_e32 v141, v141\n"
    "v_mfma_f32_16x16x32_bf16 v[144:147], a[160:163], a[0:3], v[220:223]\n"
    "ds_read2st64_b32 v[200:201], v249 offset0:8 offset1:9\n"
    "v_mfma_f32_16x16x32_bf16 v[144:147], a[164:167], a[4:7], v[144:147]\n"
    "s_waitcnt lgkmcnt(8)\n"
    "buffer_load_dwordx4 v248, s[8:11], s41 offen offset:0 sc1 lds\n"
    "v_mfma_f32_16x16x32_bf16 v[144:147], a[168:171], a[8:11], v[144:147]\n"
    "ds_read_b128 a[188:191], v243 offset:22528\n"
    "v_mfma_f32_16x16x32_bf16 v[144:147], a[172:175], a[12:15], v[144:147]\n"
    "ds_read_b64_tr_b16 a[160:161], v244 offset:8192\n"
    "v_mfma_f32_16x16x32_bf16 v[152:155], a[172:175], a[44:47], v[152:155]\n"
    "ds_read2st64_b32 v[202:203], v249 offset0:10 offset1:11\n"
    "v_mfma_f32_16x16x32_bf16 v[152:155], a[168:171], a[40:43], v[152:155]\n"
    "buffer_load_dwordx4 v248, s[8:11], s41 offen offset:1024 sc1 lds\n"
    "v_mfma_f32_16x16x32_bf16 v[160:163], a[168:171], a[72:75], v[160:163]\n"
    "ds_read_b64_tr_b16 a[162:163], v244 offset:10240\n"
    "v_mfma_f32_16x16x32_bf16 v[160:163], a[172:175], a[76:79], v[160:163]\n"
    "v_exp_f32_e32 v142, v142\n"
    "v_mfma_f32_16x16x32_bf16 v[168:171], a[172:175], a[108:111], v[168:171]\n"
    "ds_read2st64_b32 v[208:209], v249 offset0:12 offset1:13\n"
    "v_mfma_f32_16x16x32_bf16 v[168:171], a[168:171], a[104:107], v[168:171]\n"
    "buffer_load_dwordx4 v248, s[8:11], s41 offen offset:2048 sc1 lds\n"
    "v_mfma_f32_16x16x32_bf16 v[176:179], a[168:171], a[136:139], v[176:179]\n"
    "ds_read_b64_tr_b16 a[164:165], v244 offset:40960\n"
    "v_mfma_f32_16x16x32_bf16 v[176:179], a[172:175], a[140:143], v[176:179]\n"
    "s_waitcnt lgkmcnt(10)\n"
    "ds_read2st64_b32 v[210:211], v249 offset0:14 offset1:15\n"
    "v_mfma_f32_16x16x32_bf16 v[176:179], a[176:179], a[144:147], v[176:179]\n"
    "ds_read_b64_tr_b16 a[168:169], v245 offset:8192\n"
    "v_mfma_f32_16x16x32_bf16 v[176:179], a[180:183], a[148:151], v[176:179]\n"
    "buffer_load_dwordx4 v248, s[8:11], s41 offen offset:3072 sc1 lds\n"
    "v_mfma_f32_16x16x32_bf16 v[168:171], a[180:183], a[116:119], v[168:171]\n"
    "ds_read_b64_tr_b16 a[166:167], v244 offset:43008\n"
    "v_mfma_f32_16x16x32_bf16 v[168:171], a[176:179], a[112:115], v[168:171]\n"
    "s_add_u32 s41, s41, 0x8000\n"
    "v_exp_f32_e32 v143, v143\n"
    "v_mfma_f32_16x16x32_bf16 v[160:163], a[176:179], a[80:83], v[160:163]\n"
    "ds_read_b64_tr_b16 a[170:171], v245 offset:10240\n"
    "v_cvt_pk_bf16_f32 v140, v140, v141\n"
    "v_mfma_f32_16x16x32_bf16 v[160:163], a[180:183], a[84:87], v[160:163]\n"
    "ds_read2st64_b32 v[216:217], v249 offset0:16 offset1:17\n"
    "v_mfma_f32_16x16x32_bf16 v[152:155], a[180:183], a[52:55], v[152:155]\n"
    "v_exp_f32_e32 v148, v148\n"
    "v_mfma_f32_16x16x32_bf16 v[152:155], a[176:179], a[48:51], v[152:155]\n"
    "v_exp_f32_e32 v149, v149\n"
    "v_mfma_f32_16x16x32_bf16 v[144:147], a[176:179], a[16:19], v[144:147]\n"
    "ds_read_b64_tr_b16 a[172:173], v245 offset:40960\n"
    "v_cvt_pk_bf16_f32 v141, v142, v143\n"
    "v_mfma_f32_16x16x32_bf16 v[144:147], a[180:183], a[20:23], v[144:147]\n"
    "s_waitcnt lgkmcnt(11)\n"
    "ds_read2st64_b32 v[218:219], v249 offset0:18 offset1:19\n"
    "v_mfma_f32_16x16x32_bf16 v[144:147], a[184:187], a[24:27], v[144:147]\n"
    "ds_read_b64_tr_b16 a[176:177], v246 offset:8192\n"
    "v_mfma_f32_16x16x32_bf16 v[144:147], a[188:191], a[28:31], v[144:147]\n"
    "ds_read_b64_tr_b16 a[174:175], v245 offset:43008\n"
    "v_mfma_f32_16x16x32_bf16 v[152:155], a[188:191], a[60:63], v[152:155]\n"
    "v_exp_f32_e32 v150, v150\n"
    "v_mfma_f32_16x16x32_bf16 v[152:155], a[184:187], a[56:59], v[152:155]\n"
    "v_exp_f32_e32 v151, v151\n"
    "v_mfma_f32_16x16x32_bf16 v[160:163], a[184:187], a[88:91], v[160:163]\n"
    "s_waitcnt vmcnt(8)\n"
    "v_readfirstlane_b32 s81, v253\n"
    "v_mfma_f32_16x16x32_bf16 v[160:163], a[188:191], a[92:95], v[160:163]\n"
    "ds_read_b64_tr_b16 a[178:179], v246 offset:10240\n"
    "v_mfma_f32_16x16x32_bf16 v[168:171], a[188:191], a[124:127], v[168:171]\n"
    "s_cmp_eq_u32 s81, s39\n"
    "v_cvt_pk_bf16_f32 v148, v148, v149\n"
    "v_mfma_f32_16x16x32_bf16 v[168:171], a[184:187], a[120:123], v[168:171]\n"
    "s_cselect_b32 s88, s83, 2.0\n"
    "v_exp_f32_e32 v156, v156\n"
    "v_mfma_f32_16x16x32_bf16 v[176:179], a[184:187], a[152:155], v[176:179]\n"
    "s_mov_b32 m0, s30\n"
    "ds_read_b64_tr_b16 a[180:181], v246 offset:40960\n"
    "v_mfma_f32_16x16x32_bf16 v[176:179], a[188:191], a[156:159], v[176:179]\n"
    "s_waitcnt lgkmcnt(4)\n"
    "v_exp_f32_e32 v157, v157\n"
    "v_mfma_f32_16x16x32_bf16 a[236:239], a[160:163], v[180:183], a[236:239]\n"
    "buffer_load_dwordx4 v250, s[8:11], s88 offen offset:0 sc1 lds\n"
    "v_mfma_f32_16x16x32_bf16 a[236:239], a[164:167], v[184:187], a[236:239]\n"
    "ds_read_b64_tr_b16 a[182:183], v246 offset:43008\n"
    "v_mfma_f32_16x16x32_bf16 a[240:243], a[164:167], v[192:195], a[240:243]\n"
    "ds_read_b64_tr_b16 a[184:185], v247 offset:8192\n"
    "v_mfma_f32_16x16x32_bf16 a[240:243], a[160:163], v[188:191], a[240:243]\n"
    "v_cvt_pk_bf16_f32 v149, v150, v151\n"
    "v_mfma_f32_16x16x32_bf16 a[244:247], a[160:163], v[196:199], a[244:247]\n"
    "ds_read_b64_tr_b16 a[186:187], v247 offset:10240\n"
    "v_mfma_f32_16x16x32_bf16 a[244:247], a[164:167], v[200:203], a[244:247]\n"
    "v_exp_f32_e32 v158, v158\n"
    "v_mfma_f32_16x16x32_bf16 a[248:251], a[164:167], v[208:211], a[248:251]\n"
    "v_exp_f32_e32 v159, v159\n"
    "v_mfma_f32_16x16x32_bf16 a[248:251], a[160:163], v[204:207], a[248:251]\n"
    "v_cvt_pk_bf16_f32 v156, v156, v157\n"
    "v_mfma_f32_16x16x32_bf16 a[252:255], a[160:163], v[212:215], a[252:255]\n"
    "ds_read_b64_tr_b16 a[188:189], v247 offset:40960\n"
    "v_mfma_f32_16x16x32_bf16 a[252:255], a[164:167], v[216:219], a[252:255]\n"
    "s_waitcnt lgkmcnt(6)\n"
    "v_exp_f32_e32 v164, v164\n"
    "v_mfma_f32_16x16x32_bf16 v[16:19], a[172:175], v[216:219], v[16:19]\n"
    "ds_read_b64_tr_b16 a[160:161], v244 offset:12288\n"
    "v_mfma_f32_16x16x32_bf16 v[16:19], a[168:171], v[212:215], v[16:19]\n"
    "ds_read_b64_tr_b16 a[190:191], v247 offset:43008\n"
    "v_mfma_f32_16x16x32_bf16 v[12:15], a[168:171], v[204:207], v[12:15]\n"
    "v_exp_f32_e32 v165, v165\n"
    "v_mfma_f32_16x16x32_bf16 v[12:15], a[172:175], v[208:211], v[12:15]\n"
    "v_cvt_pk_bf16_f32 v157, v158, v159\n"
    "v_mfma_f32_16x16x32_bf16 v[8:11], a[172:175], v[200:203], v[8:11]\n"
    "ds_read_b64_tr_b16 a[162:163], v244 offset:14336\n"
    "v_mfma_f32_16x16x32_bf16 v[8:11], a[168:171], v[196:199], v[8:11]\n"
    "v_exp_f32_e32 v166, v166\n"
    "v_mfma_f32_16x16x32_bf16 v[4:7], a[168:171], v[188:191], v[4:7]\n"
    "v_exp_f32_e32 v167, v167\n"
    "v_mfma_f32_16x16x32_bf16 v[4:7], a[172:175], v[192:195], v[4:7]\n"
    "v_cvt_pk_bf16_f32 v164, v164, v165\n"
    "v_mfma_f32_16x16x32_bf16 v[0:3], a[172:175], v[184:187], v[0:3]\n"
    "ds_read_b64_tr_b16 a[164:165], v244 offset:45056\n"
    "v_mfma_f32_16x16x32_bf16 v[0:3], a[168:171], v[180:183], v[0:3]\n"
    "s_waitcnt lgkmcnt(7)\n"
    "v_exp_f32_e32 v172, v172\n"
    "v_mfma_f32_16x16x32_bf16 v[20:23], a[176:179], v[180:183], v[20:23]\n"
    "v_mfma_f32_16x16x32_bf16 v[20:23], a[180:183], v[184:187], v[20:23]\n"
    "ds_read_b64_tr_b16 a[166:167], v244 offset:47104\n"
    "v_mfma_f32_16x16x32_bf16 v[24:27], a[180:183], v[192:195], v[24:27]\n"
    "ds_read_b64_tr_b16 a[168:169], v245 offset:12288\n"
    "v_mfma_f32_16x16x32_bf16 v[24:27], a[176:179], v[188:191], v[24:27]\n"
    "v_exp_f32_e32 v173, v173\n"
    "v_mfma_f32_16x16x32_bf16 v[28:31], a[176:179], v[196:199], v[28:31]\n"
    "ds_read_b64_tr_b16 a[170:171], v245 offset:14336\n"
    "v_mfma_f32_16x16x32_bf16 v[28:31], a[180:183], v[200:203], v[28:31]\n"
    "v_cvt_pk_bf16_f32 v165, v166, v167\n"
    "v_mfma_f32_16x16x32_bf16 v[32:35], a[180:183], v[208:211], v[32:35]\n"
    "v_exp_f32_e32 v174, v174\n"
    "v_mfma_f32_16x16x32_bf16 v[32:35], a[176:179], v[204:207], v[32:35]\n"
    "v_exp_f32_e32 v175, v175\n"
    "v_mfma_f32_16x16x32_bf16 v[36:39], a[176:179], v[212:215], v[36:39]\n"
    "ds_read_b64_tr_b16 a[172:173], v245 offset:45056\n"
    "v_mfma_f32_16x16x32_bf16 v[36:39], a[180:183], v[216:219], v[36:39]\n"
    "s_waitcnt lgkmcnt(6)\n"
    "v_cvt_pk_bf16_f32 v172, v172, v173\n"
    "v_mfma_f32_16x16x32_bf16 v[56:59], a[188:191], v[216:219], v[56:59]\n"
    ".long 0xbedf00ff\n"
    ".Lat_w5:\n"
    "s_setpc_b64 s[2:3]\n"
    "ds_read_b64_tr_b16 a[176:177], v246 offset:12288\n"
    "v_mfma_f32_16x16x32_bf16 v[56:59], a[184:187], v[212:215], v[56:59]\n"
    "ds_read_b64_tr_b16 a[174:175], v245 offset:47104\n"
    "v_mfma_f32_16x16x32_bf16 v[52:55], a[184:187], v[204:207], v[52:55]\n"
    "v_cvt_pk_bf16_f32 v173, v174, v175\n"
    "v_mfma_f32_16x16x32_bf16 v[52:55], a[188:191], v[208:211], v[52:55]\n"
    "v_exp_f32_e32 v144, v144\n"
    "v_mfma_f32_16x16x32_bf16 v[48:51], a[188:191], v[200:203], v[48:51]\n"
    "ds_read_b64_tr_b16 a[178:179], v246 offset:14336\n"
    "v_mfma_f32_16x16x32_bf16 v[48:51], a[184:187], v[196:199], v[48:51]\n"
    "v_mfma_f32_16x16x32_bf16 v[44:47], a[184:187], v[188:191], v[44:47]\n"
    "v_exp_f32_e32 v145, v145\n"
    "v_mfma_f32_16x16x32_bf16 v[44:47], a[188:191], v[192:195], v[44:47]\n"
    "v_exp_f32_e32 v146, v146\n"
    "v_mfma_f32_16x16x32_bf16 v[40:43], a[188:191], v[184:187], v[40:43]\n"
    "ds_read_b64_tr_b16 a[180:181], v246 offset:45056\n"
    "v_mfma_f32_16x16x32_bf16 v[40:43], a[184:187], v[180:183], v[40:43]\n"
    "s_waitcnt lgkmcnt(7)\n"
    "v_exp_f32_e32 v147, v147\n"
    "v_mfma_f32_16x16x32_bf16 v[60:63], a[160:163], v[180:183], v[60:63]\n"
    "ds_read_b64_tr_b16 a[184:185], v247 offset:12288\n"
    "v_mfma_f32_16x16x32_bf16 v[60:63], a[164:167], v[184:187], v[60:63]\n"
    "ds_read_b64_tr_b16 a[182:183], v246 offset:47104\n"
    "v_mfma_f32_16x16x32_bf16 v[64:67], a[164:167], v[192:195], v[64:67]\n"
    "ds_read_b64_tr_b16 a[186:187], v247 offset:14336\n"
    "v_mfma_f32_16x16x32_bf16 v[64:67], a[160:163], v[188:191], v[64:67]\n"
    "v_cvt_pk_bf16_f32 v142, v144, v145\n"
    "v_mfma_f32_16x16x32_bf16 v[68:71], a[160:163], v[196:199], v[68:71]\n"
    "v_exp_f32_e32 v152, v152\n"
    "v_mfma_f32_16x16x32_bf16 v[68:71], a[164:167], v[200:203], v[68:71]\n"
    "ds_read_b64_tr_b16 a[188:189], v247 offset:45056\n"
    "v_mfma_f32_16x16x32_bf16 v[72:75], a[164:167], v[208:211], v[72:75]\n"
    "ds_read_b64_tr_b16 a[190:191], v247 offset:47104\n"
    "v_mfma_f32_16x16x32_bf16 v[72:75], a[160:163], v[204:207], v[72:75]\n"
    "s_waitcnt vmcnt(10)\n"
    "v_mfma_f32_16x16x32_bf16 v[76:79], a[160:163], v[212:215], v[76:79]\n"
    "s_barrier\n"
    "v_mfma_f32_16x16x32_bf16 v[76:79], a[164:167], v[216:219], v[76:79]\n"
    "s_waitcnt lgkmcnt(7)\n"
    "s_mov_b32 m0, s42\n"
    "v_mfma_f32_16x16x32_bf16 v[96:99], a[172:175], v[216:219], v[96:99]\n"
    "v_exp_f32_e32 v153, v153\n"
    "v_mfma_f32_16x16x32_bf16 v[96:99], a[168:171], v[212:215], v[96:99]\n"
    "v_cvt_pk_bf16_f32 v143, v146, v147\n"
    "v_mfma_f32_16x16x32_bf16 v[92:95], a[168:171], v[204:207], v[92:95]\n"
    "v_exp_f32_e32 v154, v154\n"
    "v_mfma_f32_16x16x32_bf16 v[92:95], a[172:175], v[208:211], v[92:95]\n"
    "ds_read_b128 a[160:163], v240 offset:24576\n"
    "v_mfma_f32_16x16x32_bf16 v[88:91], a[172:175], v[200:203], v[88:91]\n"
    "ds_write_addtid_b32 v140 offset:0\n"
    "v_mfma_f32_16x16x32_bf16 v[88:91], a[168:171], v[196:199], v[88:91]\n"
    "v_exp_f32_e32 v155, v155\n"
    "v_mfma_f32_16x16x32_bf16 v[84:87], a[168:171], v[188:191], v[84:87]\n"
    "ds_write_addtid_b32 v141 offset:256\n"
    "v_mfma_f32_16x16x32_bf16 v[84:87], a[172:175], v[192:195], v[84:87]\n"
    "ds_write_addtid_b32 v148 offset:1024\n"
    "v_mfma_f32_16x16x32_bf16 v[80:83], a[172:175], v[184:187], v[80:83]\n"
    "v_cvt_pk_bf16_f32 v150, v152, v153\n"
    "v_mfma_f32_16x16x32_bf16 v[80:83], a[168:171], v[180:183], v[80:83]\n"
    "s_waitcnt lgkmcnt(7)\n"
    "v_exp_f32_e32 v160, v160\n"
    "v_mfma_f32_16x16x32_bf16 v[100:103], a[176:179], v[180:183], v[100:103]\n"
    "ds_read_b128 a[164:167], v241 offset:24576\n"
    "v_mfma_f32_16x16x32_bf16 v[100:103], a[180:183], v[184:187], v[100:103]\n"
    "ds_write_addtid_b32 v149 offset:1280\n"
    "v_mfma_f32_16x16x32_bf16 v[104:107], a[180:183], v[192:195], v[104:107]\n"
    "v_exp_f32_e32 v161, v161\n"
    "v_mfma_f32_16x16x32_bf16 v[104:107], a[176:179], v[188:191], v[104:107]\n"
    "ds_read_b128 a[168:171], v240 offset:28672\n"
    "v_mfma_f32_16x16x32_bf16 v[108:111], a[176:179], v[196:199], v[108:111]\n"
    "ds_write_addtid_b32 v156 offset:2048\n"
    "v_mfma_f32_16x16x32_bf16 v[108:111], a[180:183], v[200:203], v[108:111]\n"
    "v_cvt_pk_bf16_f32 v151, v154, v155\n"
    "v_mfma_f32_16x16x32_bf16 v[112:115], a[180:183], v[208:211], v[112:115]\n"
    "ds_write_addtid_b32 v157 offset:2304\n"
    "v_mfma_f32_16x16x32_bf16 v[112:115], a[176:179], v[204:207], v[112:115]\n"
    "v_exp_f32_e32 v162, v162\n"
    "v_mfma_f32_16x16x32_bf16 v[116:119], a[176:179], v[212:215], v[116:119]\n"
    "ds_write_addtid_b32 v164 offset:3072\n"
    "v_mfma_f32_16x16x32_bf16 v[116:119], a[180:183], v[216:219], v[116:119]\n"
    "s_waitcnt lgkmcnt(10)\n"
    "v_exp_f32_e32 v163, v163\n"
    "v_mfma_f32_16x16x32_bf16 v[136:139], a[188:191], v[216:219], v[136:139]\n"
    "ds_read_b128 a[172:175], v241 offset:28672\n"
    "v_mfma_f32_16x16x32_bf16 v[136:139], a[184:187], v[212:215], v[136:139]\n"
    "ds_write_addtid_b32 v165 offset:3328\n"
    "v_cvt_pk_bf16_f32 v158, v160, v161\n"
    "v_mfma_f32_16x16x32_bf16 v[132:135], a[184:187], v[204:207], v[132:135]\n"
    "ds_read_b128 a[176:179], v242 offset:24576\n"
    "v_mfma_f32_16x16x32_bf16 v[132:135], a[188:191], v[208:211], v[132:135]\n"
    "ds_write_addtid_b32 v172 offset:4096\n"
    "v_mfma_f32_16x16x32_bf16 v[128:131], a[188:191], v[200:203], v[128:131]\n"
    "v_exp_f32_e32 v168, v168\n"
    "v_mfma_f32_16x16x32_bf16 v[128:131], a[184:187], v[196:199], v[128:131]\n"
    "s_sub_u32 s44, s44, 1\n"
    "ds_write_addtid_b32 v173 offset:4352\n"
    "v_mfma_f32_16x16x32_bf16 v[124:127], a[184:187], v[188:191], v[124:127]\n"
    "s_cmp_eq_u32 s44, s80\n"
    "v_exp_f32_e32 v169, v169\n"
    "v_mfma_f32_16x16x32_bf16 v[124:127], a[188:191], v[192:195], v[124:127]\n"
    "s_cselect_b32 s88, s39, s81\n"
    "ds_read_b128 a[180:183], v243 offset:24576\n"
    "v_mfma_f32_16x16x32_bf16 v[120:123], a[188:191], v[184:187], v[120:123]\n"
    "s_cmp_lg_u32 s88, s39\n"
    "ds_write_addtid_b32 v142 offset:512\n"
    "v_mfma_f32_16x16x32_bf16 v[120:123], a[184:187], v[180:183], v[120:123]\n"
    "s_waitcnt lgkmcnt(12)\n"
    "v_cvt_pk_bf16_f32 v159, v162, v163\n"
    "s_cbranch_scc1 .Lat_loop\n"
    "s_cmp_eq_u32 s44, 0\n"
    "s_cbranch_scc1 .Lat_tail\n"
    "s_cmp_eq_u32 s85, 0\n"
    "s_cbranch_scc1 .Lat_loop\n"
    "s_cmp_lg_u32 s81, s39\n"
    "s_cbranch_scc1 .Lat_lp0\n"
    ".Lat_g1b:\n"
    "v_lshrrev_b32_e32 v228, 2, v250\n"
    "v_add_u32_e32 v254, s30, v228\n"
    "s_waitcnt vmcnt(0)\n"
    "ds_read_b32 v254, v254 offset:960\n"
    "v_lshrrev_b32_e32 v228, 9, v250\n"
    "v_lshl_add_u32 v251, v228, 2, s30\n"
    "v_and_b32_e32 v252, 0xf0, v250\n"
    "v_lshl_add_u32 v252, v228, 8, v252\n"
    "v_bfe_u32 v229, v250, 8, 1\n"
    "v_lshl_add_u32 v252, v229, 3, v252\n"
    "v_and_b32_e32 v239, 0x1f0, v250\n"
    "v_bfe_u32 v228, v250, 2, 4\n"
    "s_mov_b32 s88, 0xcedc\n"
    "v_bfe_u32 v238, s88, v228, 4\n"
    "v_mul_u32_u24_e32 v238, 0x1111, v238\n"
    "v_mov_b64_e32 v[224:225], 0\n"
    "v_mov_b64_e32 v[226:227], 0\n"
    "s_mov_b32 s85, 0\n"
    "s_mov_b32 s82, 2.0\n"
    "s_mov_b32 s83, 2.0\n"
    "s_mov_b32 s89, 4\n"
    "s_waitcnt lgkmcnt(0)\n"
    "v_readlane_b32 s88, v254, 0\n"
    "v_readlane_b32 s87, v254, 1\n"
    "v_readlane_b32 s84, v254, 2\n"
    "v_readlane_b32 s86, v254, 4\n"
    "s_nop 3\n"
    "s_min_u32 s88, s88, s44\n"
    "s_sub_u32 s80, s44, s88\n"
    "s_cmp_eq_u32 s88, 0\n"
    "s_cbranch_scc1 .Lat_g2\n"
    ".Lat_gloop:\n"
    "v_mfma_f32_16x16x32_bf16 v[212:215], a[160:163], a[128:131], v[220:223]\n"
    "ds_write_addtid_b32 v143 offset:768\n"
    "v_mfma_f32_16x16x32_bf16 v[212:215], a[164:167], a[132:135], v[212:215]\n"
    "v_exp_f32_e32 v170, v170\n"
    "v_mfma_f32_16x16x32_bf16 v[204:207], a[164:167], a[100:103], v[220:223]\n"
    "ds_write_addtid_b32 v150 offset:1536\n"
    "v_mfma_f32_16x16x32_bf16 v[204:207], a[160:163], a[96:99], v[204:207]\n"
    "ds_read_b128 a[184:187], v242 offset:28672\n"
    "v_mfma_f32_16x16x32_bf16 v[196:199], a[160:163], a[64:67], v[220:223]\n"
    "v_exp_f32_e32 v171, v171\n"
    "v_mfma_f32_16x16x32_bf16 v[196:199], a[164:167], a[68:71], v[196:199]\n"
    "ds_write_addtid_b32 v151 offset:1792\n"
    "v_cvt_pk_bf16_f32 v166, v168, v169\n"
    "v_mfma_f32_16x16x32_bf16 v[188:191], a[164:167], a[36:39], v[220:223]\n"
    "v_exp_f32_e32 v176, v176\n"
    "v_mfma_f32_16x16x32_bf16 v[188:191], a[160:163], a[32:35], v[188:191]\n"
    "v_exp_f32_e32 v177, v177\n"
    "v_mfma_f32_16x16x32_bf16 v[180:183], a[160:163], a[0:3], v[220:223]\n"
    "ds_write_addtid_b32 v158 offset:2560\n"
    "v_cvt_pk_bf16_f32 v167, v170, v171\n"
    "v_mfma_f32_16x16x32_bf16 v[180:183], a[164:167], a[4:7], v[180:183]\n"
    "s_waitcnt vmcnt(20)\n"
    "v_exp_f32_e32 v178, v178\n"
    "v_smfmac_f32_16x16x64_bf16 v[224:227], a[232:235], a[192:199], v238\n"
    "s_waitcnt lgkmcnt(10)\n"
    "ds_read_b128 a[188:191], v243 offset:28672\n"
    "v_mfma_f32_16x16x32_bf16 v[180:183], a[168:171], a[8:11], v[180:183]\n"
    "s_bitcmp1_b32 s86, 0\n"
    "ds_read_b128 a[160:163], v240 offset:26624\n"
    "v_mfma_f32_16x16x32_bf16 v[180:183], a[172:175], a[12:15], v[180:183]\n"
    "s_cselect_b64 exec, -1, 0\n"
    "v_cvt_pk_bf16_f32 v236, v224, v225\n"
    "v_cvt_pk_bf16_f32 v237, v226, v227\n"
    "v_mfma_f32_16x16x32_bf16 v[188:191], a[172:175], a[44:47], v[188:191]\n"
    "s_cselect_b32 s88, 512, 0\n"
    "v_mov_b64_e32 v[224:225], 0\n"
    "v_mov_b64_e32 v[226:227], 0\n"
    "v_mfma_f32_16x16x32_bf16 v[188:191], a[168:171], a[40:43], v[188:191]\n"
    "buffer_store_dwordx2 v[236:237], v252, s[8:11], s87 offen sc1\n"
    "v_mfma_f32_16x16x32_bf16 v[196:199], a[168:171], a[72:75], v[196:199]\n"
    "s_mov_b64 exec, -1\n"
    "ds_read2_b32 v[228:229], v251 offset0:0 offset1:2\n"
    "v_mfma_f32_16x16x32_bf16 v[196:199], a[172:175], a[76:79], v[196:199]\n"
    "s_add_u32 s87, s87, s88\n"
    "ds_read2_b32 v[230:231], v251 offset0:4 offset1:6\n"
    "v_mfma_f32_16x16x32_bf16 v[204:207], a[172:175], a[108:111], v[204:207]\n"
    "ds_read_b32 v232, v251 offset:32\n"
    "v_mfma_f32_16x16x32_bf16 v[204:207], a[168:171], a[104:107], v[204:207]\n"
    "v_exp_f32_e32 v179, v179\n"
    "v_mfma_f32_16x16x32_bf16 v[212:215], a[168:171], a[136:139], v[212:215]\n"
    "ds_write_addtid_b32 v159 offset:2816\n"
    "v_cvt_pk_bf16_f32 v174, v176, v177\n"
    "v_mfma_f32_16x16x32_bf16 v[212:215], a[172:175], a[140:143], v[212:215]\n"
    "s_waitcnt lgkmcnt(13)\n"
    "ds_read_b128 a[164:167], v241 offset:26624\n"
    "v_mfma_f32_16x16x32_bf16 v[212:215], a[176:179], a[144:147], v[212:215]\n"
    "ds_write_addtid_b32 v166 offset:3584\n"
    "v_cvt_pk_bf16_f32 v175, v178, v179\n"
    "v_mfma_f32_16x16x32_bf16 v[212:215], a[180:183], a[148:151], v[212:215]\n"
    "ds_read_b128 a[168:171], v240 offset:30720\n"
    "v_mfma_f32_16x16x32_bf16 v[204:207], a[180:183], a[116:119], v[204:207]\n"
    "s_waitcnt lgkmcnt(6)\n"
    "ds_write_addtid_b32 v167 offset:3840\n"
    "v_mad_u32_u16 v234, v228, s90, v239 op_sel:[0,0,0,0]\n"
    "v_mfma_f32_16x16x32_bf16 v[204:207], a[176:179], a[112:115], v[204:207]\n"
    "buffer_load_dwordx4 a[192:195], v234, s[20:23], 0 offen sc1\n"
    "v_mad_u32_u16 v235, v228, s90, v239 op_sel:[1,0,0,0]\n"
    "v_mfma_f32_16x16x32_bf16 v[196:199], a[176:179], a[80:83], v[196:199]\n"
    "ds_write_addtid_b32 v174 offset:4608\n"
    "v_mfma_f32_16x16x32_bf16 v[196:199], a[180:183], a[84:87], v[196:199]\n"
    "ds_write_addtid_b32 v175 offset:4864\n"
    "v_mfma_f32_16x16x32_bf16 v[188:191], a[180:183], a[52:55], v[188:191]\n"
    "buffer_load_dwordx4 a[196:199], v235, s[20:23], 0 offen sc1\n"
    "v_mfma_f32_16x16x32_bf16 v[188:191], a[176:179], a[48:51], v[188:191]\n"
    "v_mfma_f32_16x16x32_bf16 v[180:183], a[176:179], a[16:19], v[180:183]\n"
    "s_waitcnt lgkmcnt(0)\n"
    "v_mfma_f32_16x16x32_bf16 v[180:183], a[180:183], a[20:23], v[180:183]\n"
    "s_barrier\n"
    "v_mfma_f32_16x16x32_bf16 v[180:183], a[184:187], a[24:27], v[180:183]\n"
    "s_add_u32 m0, s43, 8192\n"
    "ds_read_b128 a[172:175], v241 offset:30720\n"
    "v_mfma_f32_16x16x32_bf16 v[180:183], a[188:191], a[28:31], v[180:183]\n"
    "ds_read2st64_b32 v[144:145], v249 offset0:0 offset1:1\n"
    "v_mfma_f32_16x16x32_bf16 v[188:191], a[188:191], a[60:63], v[188:191]\n"
    "buffer_load_dwordx4 v248, s[8:11], s40 offen offset:0 sc1 lds\n"
    "v_mfma_f32_16x16x32_bf16 v[188:191], a[184:187], a[56:59], v[188:191]\n"
    "ds_read_b128 a[176:179], v242 offset:26624\n"
    "v_mfma_f32_16x16x32_bf16 v[196:199], a[184:187], a[88:91], v[196:199]\n"
    "v_mfma_f32_16x16x32_bf16 v[196:199], a[188:191], a[92:95], v[196:199]\n"
    "v_mfma_f32_16x16x32_bf16 v[204:207], a[188:191], a[124:127], v[204:207]\n"
    "buffer_load_dwordx4 v248, s[8:11], s40 offen offset:1024 sc1 lds\n"
    "v_mfma_f32_16x16x32_bf16 v[204:207], a[184:187], a[120:123], v[204:207]\n"
    "ds_read2st64_b32 v[146:147], v249 offset0:2 offset1:3\n"
    "v_mfma_f32_16x16x32_bf16 v[212:215], a[184:187], a[152:155], v[212:215]\n"
    "v_mfma_f32_16x16x32_bf16 v[212:215], a[188:191], a[156:159], v[212:215]\n"
    "ds_read_b128 a[180:183], v243 offset:26624\n"
    "v_mfma_f32_16x16x32_bf16 v[216:219], a[160:163], a[128:131], v[220:223]\n"
    "buffer_load_dwordx4 v248, s[8:11], s40 offen offset:2048 sc1 lds\n"
    "v_mfma_f32_16x16x32_bf16 v[216:219], a[164:167], a[132:135], v[216:219]\n"
    "ds_read2st64_b32 v[152:153], v249 offset0:4 offset1:5\n"
    "v_mfma_f32_16x16x32_bf16 v[208:211], a[164:167], a[100:103], v[220:223]\n"
    "v_exp_f32_e32 v180, v180\n"
    "v_mfma_f32_16x16x32_bf16 v[208:211], a[160:163], a[96:99], v[208:211]\n"
    "ds_read_b128 a[184:187], v242 offset:30720\n"
    "v_mfma_f32_16x16x32_bf16 v[200:203], a[160:163], a[64:67], v[220:223]\n"
    "buffer_load_dwordx4 v248, s[8:11], s40 offen offset:3072 sc1 lds\n"
    "v_mfma_f32_16x16x32_bf16 v[200:203], a[164:167], a[68:71], v[200:203]\n"
    "s_add_u32 m0, s43, 12288\n"
    "ds_read2st64_b32 v[154:155], v249 offset0:6 offset1:7\n"
    "v_mfma_f32_16x16x32_bf16 v[192:195], a[164:167], a[36:39], v[220:223]\n"
    "s_add_u32 s40, s40, 0x8000\n"
    "ds_read2st64_b32 v[160:161], v249 offset0:8 offset1:9\n"
    "v_mfma_f32_16x16x32_bf16 v[192:195], a[160:163], a[32:35], v[192:195]\n"
    "ds_read2st64_b32 v[162:163], v249 offset0:10 offset1:11\n"
    "v_mfma_f32_16x16x32_bf16 v[184:187], a[160:163], a[0:3], v[220:223]\n"
    "buffer_load_dwordx4 v248, s[8:11], s41 offen offset:0 sc1 lds\n"
    "v_mfma_f32_16x16x32_bf16 v[184:187], a[164:167], a[4:7], v[184:187]\n"
    "s_waitcnt vmcnt(17)\n"
    "ds_read_b128 a[188:191], v243 offset:30720\n"
    "v_smfmac_f32_16x16x64_bf16 v[224:227], a[232:235], a[200:207], v238\n"
    "s_waitcnt lgkmcnt(10)\n"
    "v_mad_u32_u16 v234, v229, s90, v239 op_sel:[0,0,0,0]\n"
    "v_mfma_f32_16x16x32_bf16 v[184:187], a[168:171], a[8:11], v[184:187]\n"
    "s_bitcmp1_b32 s86, 1\n"
    "buffer_load_dwordx4 a[200:203], v234, s[20:23], 0 offen sc1\n"
    "v_mad_u32_u16 v235, v229, s90, v239 op_sel:[1,0,0,0]\n"
    "v_mfma_f32_16x16x32_bf16 v[184:187], a[172:175], a[12:15], v[184:187]\n"
    "s_cselect_b64 exec, -1, 0\n"
    "v_cvt_pk_bf16_f32 v236, v224, v225\n"
    "v_cvt_pk_bf16_f32 v237, v226, v227\n"
    "v_mfma_f32_16x16x32_bf16 v[192:195], a[172:175], a[44:47], v[192:195]\n"
    "s_cselect_b32 s88, 512, 0\n"
    "v_mov_b64_e32 v[224:225], 0\n"
    "v_mov_b64_e32 v[226:227], 0\n"
    "v_mfma_f32_16x16x32_bf16 v[192:195], a[168:171], a[40:43], v[192:195]\n"
    "buffer_store_dwordx2 v[236:237], v252, s[8:11], s87 offen sc1\n"
    "v_mfma_f32_16x16x32_bf16 v[200:203], a[168:171], a[72:75], v[200:203]\n"
    "s_mov_b64 exec, -1\n"
    "ds_read_b64_tr_b16 a[160:161], v244 offset:16384\n"
    "v_mfma_f32_16x16x32_bf16 v[200:203], a[172:175], a[76:79], v[200:203]\n"
    "s_add_u32 s87, s87, s88\n"
    "ds_read_b64_tr_b16 a[162:163], v244 offset:18432\n"
    "v_mfma_f32_16x16x32_bf16 v[208:211], a[172:175], a[108:111], v[208:211]\n"
    "buffer_load_dwordx4 v248, s[8:11], s41 offen offset:1024 sc1 lds\n"
    "v_mfma_f32_16x16x32_bf16 v[208:211], a[168:171], a[104:107], v[208:211]\n"
    "ds_read_b64_tr_b16 a[164:165], v244 offset:49152\n"
    "v_mfma_f32_16x16x32_bf16 v[216:219], a[168:171], a[136:139], v[216:219]\n"
    "ds_read2st64_b32 v[168:169], v249 offset0:12 offset1:13\n"
    "v_mfma_f32_16x16x32_bf16 v[216:219], a[172:175], a[140:143], v[216:219]\n"
    "s_waitcnt lgkmcnt(10)\n"
    "ds_read2st64_b32 v[170:171], v249 offset0:14 offset1:15\n"
    "v_mfma_f32_16x16x32_bf16 v[216:219], a[176:179], a[144:147], v[216:219]\n"
    "buffer_load_dwordx4 v248, s[8:11], s41 offen offset:2048 sc1 lds\n"
    "v_mfma_f32_16x16x32_bf16 v[216:219], a[180:183], a[148:151], v[216:219]\n"
    "ds_read_b64_tr_b16 a[166:167], v244 offset:51200\n"
    "v_mfma_f32_16x16x32_bf16 v[208:211], a[180:183], a[116:119], v[208:211]\n"
    "ds_read_b64_tr_b16 a[168:169], v245 offset:16384\n"
    "v_mfma_f32_16x16x32_bf16 v[208:211], a[176:179], a[112:115], v[208:211]\n"
    "v_exp_f32_e32 v181, v181\n"
    "v_mfma_f32_16x16x32_bf16 v[200:203], a[176:179], a[80:83], v[200:203]\n"
    "buffer_load_dwordx4 v248, s[8:11], s41 offen offset:3072 sc1 lds\n"
    "v_mfma_f32_16x16x32_bf16 v[200:203], a[180:183], a[84:87], v[200:203]\n"
    "ds_read_b64_tr_b16 a[170:171], v245 offset:18432\n"
    "v_mfma_f32_16x16x32_bf16 v[192:195], a[180:183], a[52:55], v[192:195]\n"
    "s_add_u32 s41, s41, 0x8000\n"
    "ds_read2st64_b32 v[176:177], v249 offset0:16 offset1:17\n"
    "v_mfma_f32_16x16x32_bf16 v[192:195], a[176:179], a[48:51], v[192:195]\n"
    "buffer_load_dwordx4 a[204:207], v235, s[20:23], 0 offen sc1\n"
    "v_mfma_f32_16x16x32_bf16 v[184:187], a[176:179], a[16:19], v[184:187]\n"
    "ds_read_b64_tr_b16 a[172:173], v245 offset:49152\n"
    "v_mfma_f32_16x16x32_bf16 v[184:187], a[180:183], a[20:23], v[184:187]\n"
    "s_waitcnt lgkmcnt(10)\n"
    "ds_read2st64_b32 v[178:179], v249 offset0:18 offset1:19\n"
    "v_mfma_f32_16x16x32_bf16 v[184:187], a[184:187], a[24:27], v[184:187]\n"
    "ds_read_b64_tr_b16 a[176:177], v246 offset:16384\n"
    "v_mfma_f32_16x16x32_bf16 v[184:187], a[188:191], a[28:31], v[184:187]\n"
    "ds_read_b64_tr_b16 a[174:175], v245 offset:51200\n"
    "v_mfma_f32_16x16x32_bf16 v[192:195], a[188:191], a[60:63], v[192:195]\n"
    "v_exp_f32_e32 v182, v182\n"
    "v_mfma_f32_16x16x32_bf16 v[192:195], a[184:187], a[56:59], v[192:195]\n"
    "v_exp_f32_e32 v183, v183\n"
    "v_mfma_f32_16x16x32_bf16 v[200:203], a[184:187], a[88:91], v[200:203]\n"
    "ds_read_b64_tr_b16 a[178:179], v246 offset:18432\n"
    "v_cvt_pk_bf16_f32 v180, v180, v181\n"
    "v_mfma_f32_16x16x32_bf16 v[200:203], a[188:191], a[92:95], v[200:203]\n"
    "v_exp_f32_e32 v188, v188\n"
    "v_mfma_f32_16x16x32_bf16 v[208:211], a[188:191], a[124:127], v[208:211]\n"
    "v_exp_f32_e32 v189, v189\n"
    "v_mfma_f32_16x16x32_bf16 v[208:211], a[184:187], a[120:123], v[208:211]\n"
    "v_cvt_pk_bf16_f32 v181, v182, v183\n"
    "v_mfma_f32_16x16x32_bf16 v[216:219], a[184:187], a[152:155], v[216:219]\n"
    "ds_read_b64_tr_b16 a[180:181], v246 offset:49152\n"
    "v_mfma_f32_16x16x32_bf16 v[216:219], a[188:191], a[156:159], v[216:219]\n"
    "s_waitcnt vmcnt(20)\n"
    "ds_read_b64_tr_b16 a[182:183], v246 offset:51200\n"
    "v_smfmac_f32_16x16x64_bf16 v[224:227], a[232:235], a[208:215], v238\n"
    "s_waitcnt lgkmcnt(5)\n"
    "v_mad_u32_u16 v234, v230, s90, v239 op_sel:[0,0,0,0]\n"
    "v_mfma_f32_16x16x32_bf16 a[236:239], a[160:163], v[140:143], a[236:239]\n"
    "s_bitcmp1_b32 s86, 2\n"
    "buffer_load_dwordx4 a[208:211], v234, s[20:23], 0 offen sc1\n"
    "v_mad_u32_u16 v235, v230, s90, v239 op_sel:[1,0,0,0]\n"
    "v_mfma_f32_16x16x32_bf16 a[236:239], a[164:167], v[144:147], a[236:239]\n"
    "s_cselect_b64 exec, -1, 0\n"
    "v_cvt_pk_bf16_f32 v236, v224, v225\n"
    "v_cvt_pk_bf16_f32 v237, v226, v227\n"
    "v_mfma_f32_16x16x32_bf16 a[240:243], a[164:167], v[152:155], a[240:243]\n"
    "s_cselect_b32 s88, 512, 0\n"
    "v_mov_b64_e32 v[224:225], 0\n"
    "v_mov_b64_e32 v[226:227], 0\n"
    "v_mfma_f32_16x16x32_bf16 a[240:243], a[160:163], v[148:151], a[240:243]\n"
    "buffer_store_dwordx2 v[236:237], v252, s[8:11], s87 offen sc1\n"
    "v_mfma_f32_16x16x32_bf16 a[244:247], a[160:163], v[156:159], a[244:247]\n"
    "s_mov_b64 exec, -1\n"
    "ds_read_b64_tr_b16 a[184:185], v247 offset:16384\n"
    "v_mfma_f32_16x16x32_bf16 a[244:247], a[164:167], v[160:163], a[244:247]\n"
    "s_add_u32 s87, s87, s88\n"
    "ds_read_b64_tr_b16 a[186:187], v247 offset:18432\n"
    "v_mfma_f32_16x16x32_bf16 a[248:251], a[164:167], v[168:171], a[248:251]\n"
    "buffer_load_dwordx4 a[212:215], v235, s[20:23], 0 offen sc1\n"
    "v_mfma_f32_16x16x32_bf16 a[248:251], a[160:163], v[164:167], a[248:251]\n"
    "ds_read_b64_tr_b16 a[188:189], v247 offset:49152\n"
    "v_mfma_f32_16x16x32_bf16 a[252:255], a[160:163], v[172:175], a[252:255]\n"
    "v_exp_f32_e32 v190, v190\n"
    "v_mfma_f32_16x16x32_bf16 a[252:255], a[164:167], v[176:179], a[252:255]\n"
    "s_waitcnt lgkmcnt(6)\n"
    "v_exp_f32_e32 v191, v191\n"
    "v_mfma_f32_16x16x32_bf16 v[16:19], a[172:175], v[176:179], v[16:19]\n"
    "ds_read_b64_tr_b16 a[190:191], v247 offset:51200\n"
    "v_cvt_pk_bf16_f32 v188, v188, v189\n"
    "v_mfma_f32_16x16x32_bf16 v[16:19], a[168:171], v[172:175], v[16:19]\n"
    "ds_read_b64_tr_b16 a[160:161], v244 offset:20480\n"
    "v_mfma_f32_16x16x32_bf16 v[12:15], a[168:171], v[164:167], v[12:15]\n"
    "v_exp_f32_e32 v196, v196\n"
    "v_mfma_f32_16x16x32_bf16 v[12:15], a[172:175], v[168:171], v[12:15]\n"
    "v_exp_f32_e32 v197, v197\n"
    "v_mfma_f32_16x16x32_bf16 v[8:11], a[172:175], v[160:163], v[8:11]\n"
    "ds_read_b64_tr_b16 a[162:163], v244 offset:22528\n"
    "v_cvt_pk_bf16_f32 v189, v190, v191\n"
    "v_mfma_f32_16x16x32_bf16 v[8:11], a[168:171], v[156:159], v[8:11]\n"
    "v_exp_f32_e32 v198, v198\n"
    "v_mfma_f32_16x16x32_bf16 v[4:7], a[168:171], v[148:151], v[4:7]\n"
    "v_exp_f32_e32 v199, v199\n"
    "v_mfma_f32_16x16x32_bf16 v[4:7], a[172:175], v[152:155], v[4:7]\n"
    "v_cvt_pk_bf16_f32 v196, v196, v197\n"
    "v_mfma_f32_16x16x32_bf16 v[0:3], a[172:175], v[144:147], v[0:3]\n"
    "ds_read_b64_tr_b16 a[164:165], v244 offset:53248\n"
    "v_mfma_f32_16x16x32_bf16 v[0:3], a[168:171], v[140:143], v[0:3]\n"
    "s_waitcnt lgkmcnt(7)\n"
    "v_exp_f32_e32 v204, v204\n"
    "v_mfma_f32_16x16x32_bf16 v[20:23], a[176:179], v[140:143], v[20:23]\n"
    "ds_read_b64_tr_b16 a[168:169], v245 offset:20480\n"
    "v_mfma_f32_16x16x32_bf16 v[20:23], a[180:183], v[144:147], v[20:23]\n"
    "ds_read_b64_tr_b16 a[166:167], v244 offset:55296\n"
    "v_mfma_f32_16x16x32_bf16 v[24:27], a[180:183], v[152:155], v[24:27]\n"
    "v_exp_f32_e32 v205, v205\n"
    "v_mfma_f32_16x16x32_bf16 v[24:27], a[176:179], v[148:151], v[24:27]\n"
    "v_cvt_pk_bf16_f32 v197, v198, v199\n"
    "v_mfma_f32_16x16x32_bf16 v[28:31], a[176:179], v[156:159], v[28:31]\n"
    "ds_read_b64_tr_b16 a[170:171], v245 offset:22528\n"
    "v_mfma_f32_16x16x32_bf16 v[28:31], a[180:183], v[160:163], v[28:31]\n"
    "v_exp_f32_e32 v206, v206\n"
    "v_mfma_f32_16x16x32_bf16 v[32:35], a[180:183], v[168:171], v[32:35]\n"
    "v_exp_f32_e32 v207, v207\n"
    "v_mfma_f32_16x16x32_bf16 v[32:35], a[176:179], v[164:167], v[32:35]\n"
    "v_cvt_pk_bf16_f32 v204, v204, v205\n"
    "v_mfma_f32_16x16x32_bf16 v[36:39], a[176:179], v[172:175], v[36:39]\n"
    "ds_read_b64_tr_b16 a[172:173], v245 offset:53248\n"
    "v_mfma_f32_16x16x32_bf16 v[36:39], a[180:183], v[176:179], v[36:39]\n"
    "s_waitcnt vmcnt(20)\n"
    "ds_read_b64_tr_b16 a[174:175], v245 offset:55296\n"
    "v_smfmac_f32_16x16x64_bf16 v[224:227], a[232:235], a[216:223], v238\n"
    "s_waitcnt lgkmcnt(8)\n"
    "v_mad_u32_u16 v234, v231, s90, v239 op_sel:[0,0,0,0]\n"
    "v_mfma_f32_16x16x32_bf16 v[56:59], a[188:191], v[176:179], v[56:59]\n"
    "s_bitcmp1_b32 s86, 3\n"
    "buffer_load_dwordx4 a[216:219], v234, s[20:23], 0 offen sc1\n"
    "v_mad_u32_u16 v235, v231, s90, v239 op_sel:[1,0,0,0]\n"
    "v_mfma_f32_16x16x32_bf16 v[56:59], a[184:187], v[172:175], v[56:59]\n"
    "s_cselect_b64 exec, -1, 0\n"
    "v_cvt_pk_bf16_f32 v236, v224, v225\n"
    "v_cvt_pk_bf16_f32 v237, v226, v227\n"
    "v_mfma_f32_16x16x32_bf16 v[52:55], a[184:187], v[164:167], v[52:55]\n"
    "s_cselect_b32 s88, 512, 0\n"
    "v_mov_b64_e32 v[224:225], 0\n"
    "v_mov_b64_e32 v[226:227], 0\n"
    "v_mfma_f32_16x16x32_bf16 v[52:55], a[188:191], v[168:171], v[52:55]\n"
    "buffer_store_dwordx2 v[236:237], v252, s[8:11], s87 offen sc1\n"
    "v_mfma_f32_16x16x32_bf16 v[48:51], a[188:191], v[160:163], v[48:51]\n"
    "s_mov_b64 exec, -1\n"
    "ds_read_b64_tr_b16 a[176:177], v246 offset:20480\n"
    "v_mfma_f32_16x16x32_bf16 v[48:51], a[184:187], v[156:159], v[48:51]\n"
    "s_add_u32 s87, s87, s88\n"
    "ds_read_b64_tr_b16 a[178:179], v246 offset:22528\n"
    "v_mfma_f32_16x16x32_bf16 v[44:47], a[184:187], v[148:151], v[44:47]\n"
    "buffer_load_dwordx4 a[220:223], v235, s[20:23], 0 offen sc1\n"
    "v_mfma_f32_16x16x32_bf16 v[44:47], a[188:191], v[152:155], v[44:47]\n"
    "ds_read_b64_tr_b16 a[180:181], v246 offset:53248\n"
    "v_mfma_f32_16x16x32_bf16 v[40:43], a[188:191], v[144:147], v[40:43]\n"
    "v_exp_f32_e32 v212, v212\n"
    "v_mfma_f32_16x16x32_bf16 v[40:43], a[184:187], v[140:143], v[40:43]\n"
    "s_waitcnt lgkmcnt(6)\n"
    "v_exp_f32_e32 v213, v213\n"
    "v_mfma_f32_16x16x32_bf16 v[60:63], a[160:163], v[140:143], v[60:63]\n"
    "ds_read_b64_tr_b16 a[182:183], v246 offset:55296\n"
    "v_cvt_pk_bf16_f32 v205, v206, v207\n"
    "v_mfma_f32_16x16x32_bf16 v[60:63], a[164:167], v[144:147], v[60:63]\n"
    "ds_read_b64_tr_b16 a[184:185], v247 offset:20480\n"
    "v_mfma_f32_16x16x32_bf16 v[64:67], a[164:167], v[152:155], v[64:67]\n"
    "ds_read_b64_tr_b16 a[186:187], v247 offset:22528\n"
    "v_mfma_f32_16x16x32_bf16 v[64:67], a[160:163], v[148:151], v[64:67]\n"
    "v_exp_f32_e32 v214, v214\n"
    "v_mfma_f32_16x16x32_bf16 v[68:71], a[160:163], v[156:159], v[68:71]\n"
    "v_exp_f32_e32 v215, v215\n"
    "v_mfma_f32_16x16x32_bf16 v[68:71], a[164:167], v[160:163], v[68:71]\n"
    "ds_read_b64_tr_b16 a[188:189], v247 offset:53248\n"
    "v_cvt_pk_bf16_f32 v212, v212, v213\n"
    "v_mfma_f32_16x16x32_bf16 v[72:75], a[164:167], v[168:171], v[72:75]\n"
    "ds_read_b64_tr_b16 a[190:191], v247 offset:55296\n"
    "v_cvt_pk_bf16_f32 v213, v214, v215\n"
    "v_mfma_f32_16x16x32_bf16 v[72:75], a[160:163], v[164:167], v[72:75]\n"
    "s_waitcnt vmcnt(30)\n"
    "v_mfma_f32_16x16x32_bf16 v[76:79], a[160:163], v[172:175], v[76:79]\n"
    "s_barrier\n"
    "v_mfma_f32_16x16x32_bf16 v[76:79], a[164:167], v[176:179], v[76:79]\n"
    "s_waitcnt lgkmcnt(8)\n"
    "s_mov_b32 m0, s42\n"
    "v_mfma_f32_16x16x32_bf16 v[96:99], a[172:175], v[176:179], v[96:99]\n"
    "v_exp_f32_e32 v184, v184\n"
    "v_mfma_f32_16x16x32_bf16 v[96:99], a[168:171], v[172:175], v[96:99]\n"
    "v_exp_f32_e32 v185, v185\n"
    "v_mfma_f32_16x16x32_bf16 v[92:95], a[168:171], v[164:167], v[92:95]\n"
    "v_exp_f32_e32 v186, v186\n"
    "v_mfma_f32_16x16x32_bf16 v[92:95], a[172:175], v[168:171], v[92:95]\n"
    "ds_read_b128 a[160:163], v240 offset:0\n"
    "ds_write_addtid_b32 v180 offset:0\n"
    "v_mfma_f32_16x16x32_bf16 v[88:91], a[172:175], v[160:163], v[88:91]\n"
    "v_exp_f32_e32 v187, v187\n"
    "v_mfma_f32_16x16x32_bf16 v[88:91], a[168:171], v[156:159], v[88:91]\n"
    "ds_write_addtid_b32 v181 offset:256\n"
    "v_cvt_pk_bf16_f32 v182, v184, v185\n"
    "v_mfma_f32_16x16x32_bf16 v[84:87], a[168:171], v[148:151], v[84:87]\n"
    "v_exp_f32_e32 v192, v192\n"
    "v_mfma_f32_16x16x32_bf16 v[84:87], a[172:175], v[152:155], v[84:87]\n"
    "v_exp_f32_e32 v193, v193\n"
    "v_mfma_f32_16x16x32_bf16 v[80:83], a[172:175], v[144:147], v[80:83]\n"
    "ds_write_addtid_b32 v188 offset:1024\n"
    "v_cvt_pk_bf16_f32 v183, v186, v187\n"
    "v_mfma_f32_16x16x32_bf16 v[80:83], a[168:171], v[140:143], v[80:83]\n"
    "s_waitcnt vmcnt(20)\n"
    "ds_read_b128 a[164:167], v241 offset:0\n"
    "ds_write_addtid_b32 v189 offset:1280\n"
    "v_smfmac_f32_16x16x64_bf16 v[224:227], a[232:235], a[224:231], v238\n"
    "s_waitcnt lgkmcnt(10)\n"
    "ds_write_addtid_b32 v196 offset:2048\n"
    "v_mad_u32_u16 v234, v232, s90, v239 op_sel:[0,0,0,0]\n"
    "v_mfma_f32_16x16x32_bf16 v[100:103], a[176:179], v[140:143], v[100:103]\n"
    "s_bitcmp1_b32 s86, 4\n"
    "buffer_load_dwordx4 a[224:227], v234, s[20:23], 0 offen sc1\n"
    "v_mad_u32_u16 v235, v232, s90, v239 op_sel:[1,0,0,0]\n"
    "v_mfma_f32_16x16x32_bf16 v[100:103], a[180:183], v[144:147], v[100:103]\n"
    "s_cselect_b64 exec, -1, 0\n"
    "v_cvt_pk_bf16_f32 v236, v224, v225\n"
    "v_cvt_pk_bf16_f32 v237, v226, v227\n"
    "v_mfma_f32_16x16x32_bf16 v[104:107], a[180:183], v[152:155], v[104:107]\n"
    "s_cselect_b32 s88, 512, 0\n"
    "v_mov_b64_e32 v[224:225], 0\n"
    "v_mov_b64_e32 v[226:227], 0\n"
    "v_mfma_f32_16x16x32_bf16 v[104:107], a[176:179], v[148:151], v[104:107]\n"
    "buffer_store_dwordx2 v[236:237], v252, s[8:11], s87 offen sc1\n"
    "v_mfma_f32_16x16x32_bf16 v[108:111], a[176:179], v[156:159], v[108:111]\n"
    "s_mov_b64 exec, -1\n"
    "ds_read_b128 a[168:171], v240 offset:4096\n"
    "ds_write_addtid_b32 v197 offset:2304\n"
    "v_mfma_f32_16x16x32_bf16 v[108:111], a[180:183], v[160:163], v[108:111]\n"
    "s_add_u32 s87, s87, s88\n"
    "v_exp_f32_e32 v194, v194\n"
    "v_mfma_f32_16x16x32_bf16 v[112:115], a[180:183], v[168:171], v[112:115]\n"
    "buffer_load_dwordx4 a[228:231], v235, s[20:23], 0 offen sc1\n"
    "ds_write_addtid_b32 v204 offset:3072\n"
    "v_mfma_f32_16x16x32_bf16 v[112:115], a[176:179], v[164:167], v[112:115]\n"
    "ds_write_addtid_b32 v205 offset:3328\n"
    "v_mfma_f32_16x16x32_bf16 v[116:119], a[176:179], v[172:175], v[116:119]\n"
    "v_exp_f32_e32 v195, v195\n"
    "v_mfma_f32_16x16x32_bf16 v[116:119], a[180:183], v[176:179], v[116:119]\n"
    "s_waitcnt lgkmcnt(11)\n"
    "ds_read_b128 a[172:175], v241 offset:4096\n"
    "v_cvt_pk_bf16_f32 v190, v192, v193\n"
    "v_mfma_f32_16x16x32_bf16 v[136:139], a[188:191], v[176:179], v[136:139]\n"
    "v_exp_f32_e32 v200, v200\n"
    "v_mfma_f32_16x16x32_bf16 v[136:139], a[184:187], v[172:175], v[136:139]\n"
    "v_exp_f32_e32 v201, v201\n"
    "v_mfma_f32_16x16x32_bf16 v[132:135], a[184:187], v[164:167], v[132:135]\n"
    "ds_read_b128 a[176:179], v242 offset:0\n"
    "v_cvt_pk_bf16_f32 v191, v194, v195\n"
    "v_mfma_f32_16x16x32_bf16 v[132:135], a[188:191], v[168:171], v[132:135]\n"
    "v_exp_f32_e32 v202, v202\n"
    "v_mfma_f32_16x16x32_bf16 v[128:131], a[188:191], v[160:163], v[128:131]\n"
    "v_exp_f32_e32 v203, v203\n"
    "v_mfma_f32_16x16x32_bf16 v[128:131], a[184:187], v[156:159], v[128:131]\n"
    "ds_write_addtid_b32 v212 offset:4096\n"
    "v_cvt_pk_bf16_f32 v198, v200, v201\n"
    "v_mfma_f32_16x16x32_bf16 v[124:127], a[184:187], v[148:151], v[124:127]\n"
    "v_exp_f32_e32 v208, v208\n"
    "v_mfma_f32_16x16x32_bf16 v[124:127], a[188:191], v[152:155], v[124:127]\n"
    "ds_read_b128 a[180:183], v243 offset:0\n"
    "ds_write_addtid_b32 v213 offset:4352\n"
    "v_mfma_f32_16x16x32_bf16 v[120:123], a[188:191], v[144:147], v[120:123]\n"
    "v_exp_f32_e32 v209, v209\n"
    "v_mfma_f32_16x16x32_bf16 v[120:123], a[184:187], v[140:143], v[120:123]\n"
    "s_waitcnt lgkmcnt(11)\n"
    "ds_write_addtid_b32 v182 offset:512\n"
    "v_cvt_pk_bf16_f32 v199, v202, v203\n"
    "v_mfma_f32_16x16x32_bf16 v[172:175], a[160:163], a[128:131], v[220:223]\n"
    "ds_write_addtid_b32 v183 offset:768\n"
    "v_mfma_f32_16x16x32_bf16 v[172:175], a[164:167], a[132:135], v[172:175]\n"
    "v_exp_f32_e32 v210, v210\n"
    "v_mfma_f32_16x16x32_bf16 v[164:167], a[164:167], a[100:103], v[220:223]\n"
    "ds_write_addtid_b32 v190 offset:1536\n"
    "v_mfma_f32_16x16x32_bf16 v[164:167], a[160:163], a[96:99], v[164:167]\n"
    "ds_read_b128 a[184:187], v242 offset:4096\n"
    "v_mfma_f32_16x16x32_bf16 v[156:159], a[160:163], a[64:67], v[220:223]\n"
    "v_exp_f32_e32 v211, v211\n"
    "v_mfma_f32_16x16x32_bf16 v[156:159], a[164:167], a[68:71], v[156:159]\n"
    "ds_write_addtid_b32 v191 offset:1792\n"
    "v_cvt_pk_bf16_f32 v206, v208, v209\n"
    "v_mfma_f32_16x16x32_bf16 v[148:151], a[164:167], a[36:39], v[220:223]\n"
    "v_exp_f32_e32 v216, v216\n"
    "v_mfma_f32_16x16x32_bf16 v[148:151], a[160:163], a[32:35], v[148:151]\n"
    "v_exp_f32_e32 v217, v217\n"
    "v_mfma_f32_16x16x32_bf16 v[140:143], a[160:163], a[0:3], v[220:223]\n"
    "ds_write_addtid_b32 v198 offset:2560\n"
    "v_cvt_pk_bf16_f32 v207, v210, v211\n"
    "v_mfma_f32_16x16x32_bf16 v[140:143], a[164:167], a[4:7], v[140:143]\n"
    "s_waitcnt vmcnt(20)\n"
    "v_exp_f32_e32 v218, v218\n"
    "v_smfmac_f32_16x16x64_bf16 v[224:227], a[232:235], a[192:199], v238\n"
    "s_waitcnt lgkmcnt(10)\n"
    "ds_read_b128 a[188:191], v243 offset:4096\n"
    "v_mfma_f32_16x16x32_bf16 v[140:143], a[168:171], a[8:11], v[140:143]\n"
    "s_bitcmp1_b32 s86, 5\n"
    "ds_read_b128 a[160:163], v240 offset:2048\n"
    "v_mfma_f32_16x16x32_bf16 v[140:143], a[172:175], a[12:15], v[140:143]\n"
    "s_cselect_b64 exec, -1, 0\n"
    "v_cvt_pk_bf16_f32 v236, v224, v225\n"
    "v_cvt_pk_bf16_f32 v237, v226, v227\n"
    "v_mfma_f32_16x16x32_bf16 v[148:151], a[172:175], a[44:47], v[148:151]\n"
    ".long 0xbedf00ff\n"
    ".Lat_w6:\n"
    "s_setpc_b64 s[2:3]\n"
    "s_cselect_b32 s88, 512, 0\n"
    "v_mov_b64_e32 v[224:225], 0\n"
    "v_mov_b64_e32 v[226:227], 0\n"
    "v_mfma_f32_16x16x32_bf16 v[148:151], a[168:171], a[40:43], v[148:151]\n"
    "buffer_store_dwordx2 v[236:237], v252, s[8:11], s87 offen sc1\n"
    "v_mfma_f32_16x16x32_bf16 v[156:159], a[168:171], a[72:75], v[156:159]\n"
    "s_mov_b64 exec, -1\n"
    "ds_read2_b32 v[228:229], v251 offset0:10 offset1:12\n"
    "v_mfma_f32_16x16x32_bf16 v[156:159], a[172:175], a[76:79], v[156:159]\n"
    "s_add_u32 s87, s87, s88\n"
    "ds_read2_b32 v[230:231], v251 offset0:14 offset1:16\n"
    "v_mfma_f32_16x16x32_bf16 v[164:167], a[172:175], a[108:111], v[164:167]\n"
    "ds_read_b32 v232, v251 offset:72\n"
    "v_mfma_f32_16x16x32_bf16 v[164:167], a[168:171], a[104:107], v[164:167]\n"
    "v_exp_f32_e32 v219, v219\n"
    "v_mfma_f32_16x16x32_bf16 v[172:175], a[168:171], a[136:139], v[172:175]\n"
    "ds_write_addtid_b32 v199 offset:2816\n"
    "v_cvt_pk_bf16_f32 v214, v216, v217\n"
    "v_mfma_f32_16x16x32_bf16 v[172:175], a[172:175], a[140:143], v[172:175]\n"
    "s_waitcnt lgkmcnt(13)\n"
    "ds_read_b128 a[164:167], v241 offset:2048\n"
    "v_mfma_f32_16x16x32_bf16 v[172:175], a[176:179], a[144:147], v[172:175]\n"
    "ds_write_addtid_b32 v206 offset:3584\n"
    "v_cvt_pk_bf16_f32 v215, v218, v219\n"
    "v_mfma_f32_16x16x32_bf16 v[172:175], a[180:183], a[148:151], v[172:175]\n"
    "ds_read_b128 a[168:171], v240 offset:6144\n"
    "v_mfma_f32_16x16x32_bf16 v[164:167], a[180:183], a[116:119], v[164:167]\n"
    "s_waitcnt lgkmcnt(6)\n"
    "ds_write_addtid_b32 v207 offset:3840\n"
    "v_mad_u32_u16 v234, v228, s90, v239 op_sel:[0,0,0,0]\n"
    "v_mfma_f32_16x16x32_bf16 v[164:167], a[176:179], a[112:115], v[164:167]\n"
    "buffer_load_dwordx4 a[192:195], v234, s[20:23], 0 offen sc1\n"
    "v_mad_u32_u16 v235, v228, s90, v239 op_sel:[1,0,0,0]\n"
    "v_mfma_f32_16x16x32_bf16 v[156:159], a[176:179], a[80:83], v[156:159]\n"
    "ds_write_addtid_b32 v214 offset:4608\n"
    "v_mfma_f32_16x16x32_bf16 v[156:159], a[180:183], a[84:87], v[156:159]\n"
    "ds_write_addtid_b32 v215 offset:4864\n"
    "v_mfma_f32_16x16x32_bf16 v[148:151], a[180:183], a[52:55], v[148:151]\n"
    "buffer_load_dwordx4 a[196:199], v235, s[20:23], 0 offen sc1\n"
    "v_mfma_f32_16x16x32_bf16 v[148:151], a[176:179], a[48:51], v[148:151]\n"
    "v_mfma_f32_16x16x32_bf16 v[140:143], a[176:179], a[16:19], v[140:143]\n"
    "s_waitcnt lgkmcnt(0)\n"
    "v_mfma_f32_16x16x32_bf16 v[140:143], a[180:183], a[20:23], v[140:143]\n"
    "s_barrier\n"
    "v_mfma_f32_16x16x32_bf16 v[140:143], a[184:187], a[24:27], v[140:143]\n"
    "s_add_u32 m0, s43, 16384\n"
    "ds_read_b128 a[172:175], v241 offset:6144\n"
    "v_mfma_f32_16x16x32_bf16 v[140:143], a[188:191], a[28:31], v[140:143]\n"
    "ds_read2st64_b32 v[184:185], v249 offset0:0 offset1:1\n"
    "v_mfma_f32_16x16x32_bf16 v[148:151], a[188:191], a[60:63], v[148:151]\n"
    "buffer_load_dwordx4 v248, s[8:11], s40 offen offset:0 sc1 lds\n"
    "v_mfma_f32_16x16x32_bf16 v[148:151], a[184:187], a[56:59], v[148:151]\n"
    "ds_read_b128 a[176:179], v242 offset:2048\n"
    "v_mfma_f32_16x16x32_bf16 v[156:159], a[184:187], a[88:91], v[156:159]\n"
    "v_mfma_f32_16x16x32_bf16 v[156:159], a[188:191], a[92:95], v[156:159]\n"
    "v_mfma_f32_16x16x32_bf16 v[164:167], a[188:191], a[124:127], v[164:167]\n"
    "buffer_load_dwordx4 v248, s[8:11], s40 offen offset:1024 sc1 lds\n"
    "v_mfma_f32_16x16x32_bf16 v[164:167], a[184:187], a[120:123], v[164:167]\n"
    "ds_read2st64_b32 v[186:187], v249 offset0:2 offset1:3\n"
    "v_mfma_f32_16x16x32_bf16 v[172:175], a[184:187], a[152:155], v[172:175]\n"
    "v_mfma_f32_16x16x32_bf16 v[172:175], a[188:191], a[156:159], v[172:175]\n"
    "ds_read_b128 a[180:183], v243 offset:2048\n"
    "v_mfma_f32_16x16x32_bf16 v[176:179], a[160:163], a[128:131], v[220:223]\n"
    "buffer_load_dwordx4 v248, s[8:11], s40 offen offset:2048 sc1 lds\n"
    "v_mfma_f32_16x16x32_bf16 v[176:179], a[164:167], a[132:135], v[176:179]\n"
    "ds_read2st64_b32 v[192:193], v249 offset0:4 offset1:5\n"
    "v_mfma_f32_16x16x32_bf16 v[168:171], a[164:167], a[100:103], v[220:223]\n"
    "v_exp_f32_e32 v140, v140\n"
    "v_mfma_f32_16x16x32_bf16 v[168:171], a[160:163], a[96:99], v[168:171]\n"
    "ds_read_b128 a[184:187], v242 offset:6144\n"
    "v_mfma_f32_16x16x32_bf16 v[160:163], a[160:163], a[64:67], v[220:223]\n"
    "buffer_load_dwordx4 v248, s[8:11], s40 offen offset:3072 sc1 lds\n"
    "v_mfma_f32_16x16x32_bf16 v[160:163], a[164:167], a[68:71], v[160:163]\n"
    "s_add_u32 m0, s43, 20480\n"
    "ds_read2st64_b32 v[194:195], v249 offset0:6 offset1:7\n"
    "v_mfma_f32_16x16x32_bf16 v[152:155], a[164:167], a[36:39], v[220:223]\n"
    "s_add_u32 s40, s40, 0x8000\n"
    "ds_read2st64_b32 v[200:201], v249 offset0:8 offset1:9\n"
    "v_mfma_f32_16x16x32_bf16 v[152:155], a[160:163], a[32:35], v[152:155]\n"
    "ds_read2st64_b32 v[202:203], v249 offset0:10 offset1:11\n"
    "v_mfma_f32_16x16x32_bf16 v[144:147], a[160:163], a[0:3], v[220:223]\n"
    "buffer_load_dwordx4 v248, s[8:11], s41 offen offset:0 sc1 lds\n"
    "v_mfma_f32_16x16x32_bf16 v[144:147], a[164:167], a[4:7], v[144:147]\n"
    "s_waitcnt vmcnt(17)\n"
    "ds_read_b128 a[188:191], v243 offset:6144\n"
    "v_smfmac_f32_16x16x64_bf16 v[224:227], a[232:235], a[200:207], v238\n"
    "s_waitcnt lgkmcnt(10)\n"
    "v_mad_u32_u16 v234, v229, s90, v239 op_sel:[0,0,0,0]\n"
    "v_mfma_f32_16x16x32_bf16 v[144:147], a[168:171], a[8:11], v[144:147]\n"
    "s_bitcmp1_b32 s86, 6\n"
    "buffer_load_dwordx4 a[200:203], v234, s[20:23], 0 offen sc1\n"
    "v_mad_u32_u16 v235, v229, s90, v239 op_sel:[1,0,0,0]\n"
    "v_mfma_f32_16x16x32_bf16 v[144:147], a[172:175], a[12:15], v[144:147]\n"
    "s_cselect_b64 exec, -1, 0\n"
    "v_cvt_pk_bf16_f32 v236, v224, v225\n"
    "v_cvt_pk_bf16_f32 v237, v226, v227\n"
    "v_mfma_f32_16x16x32_bf16 v[152:155], a[172:175], a[44:47], v[152:155]\n"
    "s_cselect_b32 s88, 512, 0\n"
    "v_mov_b64_e32 v[224:225], 0\n"
    "v_mov_b64_e32 v[226:227], 0\n"
    "v_mfma_f32_16x16x32_bf16 v[152:155], a[168:171], a[40:43], v[152:155]\n"
    "buffer_store_dwordx2 v[236:237], v252, s[8:11], s87 offen sc1\n"
    "v_mfma_f32_16x16x32_bf16 v[160:163], a[168:171], a[72:75], v[160:163]\n"
    "s_mov_b64 exec, -1\n"
    "ds_read_b64_tr_b16 a[160:161], v244 offset:24576\n"
    "v_mfma_f32_16x16x32_bf16 v[160:163], a[172:175], a[76:79], v[160:163]\n"
    "s_add_u32 s87, s87, s88\n"
    "ds_read_b64_tr_b16 a[162:163], v244 offset:26624\n"
    "v_mfma_f32_16x16x32_bf16 v[168:171], a[172:175], a[108:111], v[168:171]\n"
    "buffer_load_dwordx4 v248, s[8:11], s41 offen offset:1024 sc1 lds\n"
    "v_mfma_f32_16x16x32_bf16 v[168:171], a[168:171], a[104:107], v[168:171]\n"
    "ds_read_b64_tr_b16 a[164:165], v244 offset:57344\n"
    "v_mfma_f32_16x16x32_bf16 v[176:179], a[168:171], a[136:139], v[176:179]\n"
    "ds_read2st64_b32 v[208:209], v249 offset0:12 offset1:13\n"
    "v_mfma_f32_16x16x32_bf16 v[176:179], a[172:175], a[140:143], v[176:179]\n"
    "s_waitcnt lgkmcnt(10)\n"
    "ds_read2st64_b32 v[210:211], v249 offset0:14 offset1:15\n"
    "v_mfma_f32_16x16x32_bf16 v[176:179], a[176:179], a[144:147], v[176:179]\n"
    "buffer_load_dwordx4 v248, s[8:11], s41 offen offset:2048 sc1 lds\n"
    "v_mfma_f32_16x16x32_bf16 v[176:179], a[180:183], a[148:151], v[176:179]\n"
    "ds_read_b64_tr_b16 a[166:167], v244 offset:59392\n"
    "v_mfma_f32_16x16x32_bf16 v[168:171], a[180:183], a[116:119], v[168:171]\n"
    "ds_read_b64_tr_b16 a[168:169], v245 offset:24576\n"
    "v_mfma_f32_16x16x32_bf16 v[168:171], a[176:179], a[112:115], v[168:171]\n"
    "v_exp_f32_e32 v141, v141\n"
    "v_mfma_f32_16x16x32_bf16 v[160:163], a[176:179], a[80:83], v[160:163]\n"
    "buffer_load_dwordx4 v248, s[8:11], s41 offen offset:3072 sc1 lds\n"
    "v_mfma_f32_16x16x32_bf16 v[160:163], a[180:183], a[84:87], v[160:163]\n"
    "ds_read_b64_tr_b16 a[170:171], v245 offset:26624\n"
    "v_mfma_f32_16x16x32_bf16 v[152:155], a[180:183], a[52:55], v[152:155]\n"
    "s_add_u32 s41, s41, 0x8000\n"
    "ds_read2st64_b32 v[216:217], v249 offset0:16 offset1:17\n"
    "v_mfma_f32_16x16x32_bf16 v[152:155], a[176:179], a[48:51], v[152:155]\n"
    "buffer_load_dwordx4 a[204:207], v235, s[20:23], 0 offen sc1\n"
    "v_mfma_f32_16x16x32_bf16 v[144:147], a[176:179], a[16:19], v[144:147]\n"
    "ds_read_b64_tr_b16 a[172:173], v245 offset:57344\n"
    "v_mfma_f32_16x16x32_bf16 v[144:147], a[180:183], a[20:23], v[144:147]\n"
    "s_waitcnt lgkmcnt(10)\n"
    "ds_read2st64_b32 v[218:219], v249 offset0:18 offset1:19\n"
    "v_mfma_f32_16x16x32_bf16 v[144:147], a[184:187], a[24:27], v[144:147]\n"
    "ds_read_b64_tr_b16 a[176:177], v246 offset:24576\n"
    "v_mfma_f32_16x16x32_bf16 v[144:147], a[188:191], a[28:31], v[144:147]\n"
    "ds_read_b64_tr_b16 a[174:175], v245 offset:59392\n"
    "v_mfma_f32_16x16x32_bf16 v[152:155], a[188:191], a[60:63], v[152:155]\n"
    "v_exp_f32_e32 v142, v142\n"
    "v_mfma_f32_16x16x32_bf16 v[152:155], a[184:187], a[56:59], v[152:155]\n"
    "v_exp_f32_e32 v143, v143\n"
    "v_mfma_f32_16x16x32_bf16 v[160:163], a[184:187], a[88:91], v[160:163]\n"
    "ds_read_b64_tr_b16 a[178:179], v246 offset:26624\n"
    "v_cvt_pk_bf16_f32 v140, v140, v141\n"
    "v_mfma_f32_16x16x32_bf16 v[160:163], a[188:191], a[92:95], v[160:163]\n"
    "v_exp_f32_e32 v148, v148\n"
    "v_mfma_f32_16x16x32_bf16 v[168:171], a[188:191], a[124:127], v[168:171]\n"
    "v_exp_f32_e32 v149, v149\n"
    "v_mfma_f32_16x16x32_bf16 v[168:171], a[184:187], a[120:123], v[168:171]\n"
    "v_cvt_pk_bf16_f32 v141, v142, v143\n"
    "v_mfma_f32_16x16x32_bf16 v[176:179], a[184:187], a[152:155], v[176:179]\n"
    "ds_read_b64_tr_b16 a[180:181], v246 offset:57344\n"
    "v_mfma_f32_16x16x32_bf16 v[176:179], a[188:191], a[156:159], v[176:179]\n"
    "s_waitcnt vmcnt(20)\n"
    "ds_read_b64_tr_b16 a[182:183], v246 offset:59392\n"
    "v_smfmac_f32_16x16x64_bf16 v[224:227], a[232:235], a[208:215], v238\n"
    "s_waitcnt lgkmcnt(5)\n"
    "v_mad_u32_u16 v234, v230, s90, v239 op_sel:[0,0,0,0]\n"
    "v_mfma_f32_16x16x32_bf16 a[236:239], a[160:163], v[180:183], a[236:239]\n"
    "s_bitcmp1_b32 s86, 7\n"
    "buffer_load_dwordx4 a[208:211], v234, s[20:23], 0 offen sc1\n"
    "v_mad_u32_u16 v235, v230, s90, v239 op_sel:[1,0,0,0]\n"
    "v_mfma_f32_16x16x32_bf16 a[236:239], a[164:167], v[184:187], a[236:239]\n"
    "s_cselect_b64 exec, -1, 0\n"
    "v_cvt_pk_bf16_f32 v236, v224, v225\n"
    "v_cvt_pk_bf16_f32 v237, v226, v227\n"
    "v_mfma_f32_16x16x32_bf16 a[240:243], a[164:167], v[192:195], a[240:243]\n"
    "s_cselect_b32 s88, 512, 0\n"
    "v_mov_b64_e32 v[224:225], 0\n"
    "v_mov_b64_e32 v[226:227], 0\n"
    "v_mfma_f32_16x16x32_bf16 a[240:243], a[160:163], v[188:191], a[240:243]\n"
    "buffer_store_dwordx2 v[236:237], v252, s[8:11], s87 offen sc1\n"
    "v_mfma_f32_16x16x32_bf16 a[244:247], a[160:163], v[196:199], a[244:247]\n"
    "s_mov_b64 exec, -1\n"
    "ds_read_b64_tr_b16 a[184:185], v247 offset:24576\n"
    "v_mfma_f32_16x16x32_bf16 a[244:247], a[164:167], v[200:203], a[244:247]\n"
    "s_add_u32 s87, s87, s88\n"
    "ds_read_b64_tr_b16 a[186:187], v247 offset:26624\n"
    "v_mfma_f32_16x16x32_bf16 a[248:251], a[164:167], v[208:211], a[248:251]\n"
    "buffer_load_dwordx4 a[212:215], v235, s[20:23], 0 offen sc1\n"
    "v_mfma_f32_16x16x32_bf16 a[248:251], a[160:163], v[204:207], a[248:251]\n"
    "ds_read_b64_tr_b16 a[188:189], v247 offset:57344\n"
    "v_mfma_f32_16x16x32_bf16 a[252:255], a[160:163], v[212:215], a[252:255]\n"
    "v_exp_f32_e32 v150, v150\n"
    "v_mfma_f32_16x16x32_bf16 a[252:255], a[164:167], v[216:219], a[252:255]\n"
    "s_waitcnt lgkmcnt(6)\n"
    "v_exp_f32_e32 v151, v151\n"
    "v_mfma_f32_16x16x32_bf16 v[16:19], a[172:175], v[216:219], v[16:19]\n"
    "ds_read_b64_tr_b16 a[190:191], v247 offset:59392\n"
    "v_cvt_pk_bf16_f32 v148, v148, v149\n"
    "v_mfma_f32_16x16x32_bf16 v[16:19], a[168:171], v[212:215], v[16:19]\n"
    "ds_read_b64_tr_b16 a[160:161], v244 offset:28672\n"
    "v_mfma_f32_16x16x32_bf16 v[12:15], a[168:171], v[204:207], v[12:15]\n"
    "v_exp_f32_e32 v156, v156\n"
    "v_mfma_f32_16x16x32_bf16 v[12:15], a[172:175], v[208:211], v[12:15]\n"
    "v_exp_f32_e32 v157, v157\n"
    "v_mfma_f32_16x16x32_bf16 v[8:11], a[172:175], v[200:203], v[8:11]\n"
    "ds_read_b64_tr_b16 a[162:163], v244 offset:30720\n"
    "v_cvt_pk_bf16_f32 v149, v150, v151\n"
    "v_mfma_f32_16x16x32_bf16 v[8:11], a[168:171], v[196:199], v[8:11]\n"
    "v_exp_f32_e32 v158, v158\n"
    "v_mfma_f32_16x16x32_bf16 v[4:7], a[168:171], v[188:191], v[4:7]\n"
    "v_exp_f32_e32 v159, v159\n"
    "v_mfma_f32_16x16x32_bf16 v[4:7], a[172:175], v[192:195], v[4:7]\n"
    "v_cvt_pk_bf16_f32 v156, v156, v157\n"
    "v_mfma_f32_16x16x32_bf16 v[0:3], a[172:175], v[184:187], v[0:3]\n"
    "ds_read_b64_tr_b16 a[164:165], v244 offset:61440\n"
    "v_mfma_f32_16x16x32_bf16 v[0:3], a[168:171], v[180:183], v[0:3]\n"
    "s_waitcnt lgkmcnt(7)\n"
    "v_exp_f32_e32 v164, v164\n"
    "v_mfma_f32_16x16x32_bf16 v[20:23], a[176:179], v[180:183], v[20:23]\n"
    "ds_read_b64_tr_b16 a[168:169], v245 offset:28672\n"
    "v_mfma_f32_16x16x32_bf16 v[20:23], a[180:183], v[184:187], v[20:23]\n"
    "ds_read_b64_tr_b16 a[166:167], v244 offset:63488\n"
    "v_mfma_f32_16x16x32_bf16 v[24:27], a[180:183], v[192:195], v[24:27]\n"
    "v_exp_f32_e32 v165, v165\n"
    "v_mfma_f32_16x16x32_bf16 v[24:27], a[176:179], v[188:191], v[24:27]\n"
    "v_cvt_pk_bf16_f32 v157, v158, v159\n"
    "v_mfma_f32_16x16x32_bf16 v[28:31], a[176:179], v[196:199], v[28:31]\n"
    "ds_read_b64_tr_b16 a[170:171], v245 offset:30720\n"
    "v_mfma_f32_16x16x32_bf16 v[28:31], a[180:183], v[200:203], v[28:31]\n"
    "v_exp_f32_e32 v166, v166\n"
    "v_mfma_f32_16x16x32_bf16 v[32:35], a[180:183], v[208:211], v[32:35]\n"
    "v_exp_f32_e32 v167, v167\n"
    "v_mfma_f32_16x16x32_bf16 v[32:35], a[176:179], v[204:207], v[32:35]\n"
    "v_cvt_pk_bf16_f32 v164, v164, v165\n"
    "v_mfma_f32_16x16x32_bf16 v[36:39], a[176:179], v[212:215], v[36:39]\n"
    "ds_read_b64_tr_b16 a[172:173], v245 offset:61440\n"
    "v_mfma_f32_16x16x32_bf16 v[36:39], a[180:183], v[216:219], v[36:39]\n"
    "s_waitcnt vmcnt(20)\n"
    "ds_read_b64_tr_b16 a[174:175], v245 offset:63488\n"
    "v_smfmac_f32_16x16x64_bf16 v[224:227], a[232:235], a[216:223], v238\n"
    "s_waitcnt lgkmcnt(8)\n"
    "v_mad_u32_u16 v234, v231, s90, v239 op_sel:[0,0,0,0]\n"
    "v_mfma_f32_16x16x32_bf16 v[56:59], a[188:191], v[216:219], v[56:59]\n"
    "s_bitcmp1_b32 s86, 8\n"
    "buffer_load_dwordx4 a[216:219], v234, s[20:23], 0 offen sc1\n"
    "v_mad_u32_u16 v235, v231, s90, v239 op_sel:[1,0,0,0]\n"
    "v_mfma_f32_16x16x32_bf16 v[56:59], a[184:187], v[212:215], v[56:59]\n"
    "s_cselect_b64 exec, -1, 0\n"
    "v_cvt_pk_bf16_f32 v236, v224, v225\n"
    "v_cvt_pk_bf16_f32 v237, v226, v227\n"
    "v_mfma_f32_16x16x32_bf16 v[52:55], a[184:187], v[204:207], v[52:55]\n"
    "s_cselect_b32 s88, 512, 0\n"
    "v_mov_b64_e32 v[224:225], 0\n"
    "v_mov_b64_e32 v[226:227], 0\n"
    "v_mfma_f32_16x16x32_bf16 v[52:55], a[188:191], v[208:211], v[52:55]\n"
    "buffer_store_dwordx2 v[236:237], v252, s[8:11], s87 offen sc1\n"
    "v_mfma_f32_16x16x32_bf16 v[48:51], a[188:191], v[200:203], v[48:51]\n"
    "s_mov_b64 exec, -1\n"
    "ds_read_b64_tr_b16 a[176:177], v246 offset:28672\n"
    "v_mfma_f32_16x16x32_bf16 v[48:51], a[184:187], v[196:199], v[48:51]\n"
    "s_add_u32 s87, s87, s88\n"
    "ds_read_b64_tr_b16 a[178:179], v246 offset:30720\n"
    "v_mfma_f32_16x16x32_bf16 v[44:47], a[184:187], v[188:191], v[44:47]\n"
    "buffer_load_dwordx4 a[220:223], v235, s[20:23], 0 offen sc1\n"
    "v_mfma_f32_16x16x32_bf16 v[44:47], a[188:191], v[192:195], v[44:47]\n"
    "ds_read_b64_tr_b16 a[180:181], v246 offset:61440\n"
    "v_mfma_f32_16x16x32_bf16 v[40:43], a[188:191], v[184:187], v[40:43]\n"
    "v_exp_f32_e32 v172, v172\n"
    "v_mfma_f32_16x16x32_bf16 v[40:43], a[184:187], v[180:183], v[40:43]\n"
    "s_waitcnt lgkmcnt(6)\n"
    "v_exp_f32_e32 v173, v173\n"
    "v_mfma_f32_16x16x32_bf16 v[60:63], a[160:163], v[180:183], v[60:63]\n"
    "ds_read_b64_tr_b16 a[182:183], v246 offset:63488\n"
    "v_cvt_pk_bf16_f32 v165, v166, v167\n"
    "v_mfma_f32_16x16x32_bf16 v[60:63], a[164:167], v[184:187], v[60:63]\n"
    "ds_read_b64_tr_b16 a[184:185], v247 offset:28672\n"
    "v_mfma_f32_16x16x32_bf16 v[64:67], a[164:167], v[192:195], v[64:67]\n"
    "ds_read_b64_tr_b16 a[186:187], v247 offset:30720\n"
    "v_mfma_f32_16x16x32_bf16 v[64:67], a[160:163], v[188:191], v[64:67]\n"
    "v_exp_f32_e32 v174, v174\n"
    "v_mfma_f32_16x16x32_bf16 v[68:71], a[160:163], v[196:199], v[68:71]\n"
    "v_exp_f32_e32 v175, v175\n"
    "v_mfma_f32_16x16x32_bf16 v[68:71], a[164:167], v[200:203], v[68:71]\n"
    "ds_read_b64_tr_b16 a[188:189], v247 offset:61440\n"
    "v_cvt_pk_bf16_f32 v172, v172, v173\n"
    "v_mfma_f32_16x16x32_bf16 v[72:75], a[164:167], v[208:211], v[72:75]\n"
    "ds_read_b64_tr_b16 a[190:191], v247 offset:63488\n"
    "v_cvt_pk_bf16_f32 v173, v174, v175\n"
    "v_mfma_f32_16x16x32_bf16 v[72:75], a[160:163], v[204:207], v[72:75]\n"
    "s_waitcnt vmcnt(30)\n"
    "v_mfma_f32_16x16x32_bf16 v[76:79], a[160:163], v[212:215], v[76:79]\n"
    "s_barrier\n"
    "v_mfma_f32_16x16x32_bf16 v[76:79], a[164:167], v[216:219], v[76:79]\n"
    "s_waitcnt lgkmcnt(8)\n"
    "s_mov_b32 m0, s42\n"
    "v_mfma_f32_16x16x32_bf16 v[96:99], a[172:175], v[216:219], v[96:99]\n"
    "v_exp_f32_e32 v144, v144\n"
    "v_mfma_f32_16x16x32_bf16 v[96:99], a[168:171], v[212:215], v[96:99]\n"
    "v_exp_f32_e32 v145, v145\n"
    "v_mfma_f32_16x16x32_bf16 v[92:95], a[168:171], v[204:207], v[92:95]\n"
    "v_exp_f32_e32 v146, v146\n"
    "v_mfma_f32_16x16x32_bf16 v[92:95], a[172:175], v[208:211], v[92:95]\n"
    "ds_read_b128 a[160:163], v240 offset:8192\n"
    "ds_write_addtid_b32 v140 offset:0\n"
    "v_mfma_f32_16x16x32_bf16 v[88:91], a[172:175], v[200:203], v[88:91]\n"
    "v_exp_f32_e32 v147, v147\n"
    "v_mfma_f32_16x16x32_bf16 v[88:91], a[168:171], v[196:199], v[88:91]\n"
    "ds_write_addtid_b32 v141 offset:256\n"
    "v_cvt_pk_bf16_f32 v142, v144, v145\n"
    "v_mfma_f32_16x16x32_bf16 v[84:87], a[168:171], v[188:191], v[84:87]\n"
    "v_exp_f32_e32 v152, v152\n"
    "v_mfma_f32_16x16x32_bf16 v[84:87], a[172:175], v[192:195], v[84:87]\n"
    "v_exp_f32_e32 v153, v153\n"
    "v_mfma_f32_16x16x32_bf16 v[80:83], a[172:175], v[184:187], v[80:83]\n"
    "ds_write_addtid_b32 v148 offset:1024\n"
    "v_cvt_pk_bf16_f32 v143, v146, v147\n"
    "v_mfma_f32_16x16x32_bf16 v[80:83], a[168:171], v[180:183], v[80:83]\n"
    "s_waitcnt vmcnt(20)\n"
    "ds_read_b128 a[164:167], v241 offset:8192\n"
    "ds_write_addtid_b32 v149 offset:1280\n"
    "v_smfmac_f32_16x16x64_bf16 v[224:227], a[232:235], a[224:231], v238\n"
    "s_waitcnt lgkmcnt(10)\n"
    "ds_write_addtid_b32 v156 offset:2048\n"
    "v_mad_u32_u16 v234, v232, s90, v239 op_sel:[0,0,0,0]\n"
    "v_mfma_f32_16x16x32_bf16 v[100:103], a[176:179], v[180:183], v[100:103]\n"
    "s_bitcmp1_b32 s86, 9\n"
    "buffer_load_dwordx4 a[224:227], v234, s[20:23], 0 offen sc1\n"
    "v_mad_u32_u16 v235, v232, s90, v239 op_sel:[1,0,0,0]\n"
    "v_mfma_f32_16x16x32_bf16 v[100:103], a[180:183], v[184:187], v[100:103]\n"
    "s_cselect_b64 exec, -1, 0\n"
    "v_cvt_pk_bf16_f32 v236, v224, v225\n"
    "v_cvt_pk_bf16_f32 v237, v226, v227\n"
    "v_mfma_f32_16x16x32_bf16 v[104:107], a[180:183], v[192:195], v[104:107]\n"
    "s_cselect_b32 s88, 512, 0\n"
    "v_mov_b64_e32 v[224:225], 0\n"
    "v_mov_b64_e32 v[226:227], 0\n"
    "v_mfma_f32_16x16x32_bf16 v[104:107], a[176:179], v[188:191], v[104:107]\n"
    "buffer_store_dwordx2 v[236:237], v252, s[8:11], s87 offen sc1\n"
    "v_mfma_f32_16x16x32_bf16 v[108:111], a[176:179], v[196:199], v[108:111]\n"
    "s_mov_b64 exec, -1\n"
    "ds_read_b128 a[168:171], v240 offset:12288\n"
    "ds_write_addtid_b32 v157 offset:2304\n"
    "v_mfma_f32_16x16x32_bf16 v[108:111], a[180:183], v[200:203], v[108:111]\n"
    "s_add_u32 s87, s87, s88\n"
    "v_exp_f32_e32 v154, v154\n"
    "v_mfma_f32_16x16x32_bf16 v[112:115], a[180:183], v[208:211], v[112:115]\n"
    "buffer_load_dwordx4 a[228:231], v235, s[20:23], 0 offen sc1\n"
    "ds_write_addtid_b32 v164 offset:3072\n"
    "v_mfma_f32_16x16x32_bf16 v[112:115], a[176:179], v[204:207], v[112:115]\n"
    "ds_write_addtid_b32 v165 offset:3328\n"
    "v_mfma_f32_16x16x32_bf16 v[116:119], a[176:179], v[212:215], v[116:119]\n"
    "v_exp_f32_e32 v155, v155\n"
    "v_mfma_f32_16x16x32_bf16 v[116:119], a[180:183], v[216:219], v[116:119]\n"
    "s_waitcnt lgkmcnt(11)\n"
    "ds_read_b128 a[172:175], v241 offset:12288\n"
    "v_cvt_pk_bf16_f32 v150, v152, v153\n"
    "v_mfma_f32_16x16x32_bf16 v[136:139], a[188:191], v[216:219], v[136:139]\n"
    "v_exp_f32_e32 v160, v160\n"
    "v_mfma_f32_16x16x32_bf16 v[136:139], a[184:187], v[212:215], v[136:139]\n"
    "v_exp_f32_e32 v161, v161\n"
    "v_mfma_f32_16x16x32_bf16 v[132:135], a[184:187], v[204:207], v[132:135]\n"
    "ds_read_b128 a[176:179], v242 offset:8192\n"
    "v_cvt_pk_bf16_f32 v151, v154, v155\n"
    "v_mfma_f32_16x16x32_bf16 v[132:135], a[188:191], v[208:211], v[132:135]\n"
    "v_exp_f32_e32 v162, v162\n"
    "v_mfma_f32_16x16x32_bf16 v[128:131], a[188:191], v[200:203], v[128:131]\n"
    "v_exp_f32_e32 v163, v163\n"
    "v_mfma_f32_16x16x32_bf16 v[128:131], a[184:187], v[196:199], v[128:131]\n"
    "ds_write_addtid_b32 v172 offset:4096\n"
    "v_cvt_pk_bf16_f32 v158, v160, v161\n"
    "v_mfma_f32_16x16x32_bf16 v[124:127], a[184:187], v[188:191], v[124:127]\n"
    "v_exp_f32_e32 v168, v168\n"
    "v_mfma_f32_16x16x32_bf16 v[124:127], a[188:191], v[192:195], v[124:127]\n"
    "ds_read_b128 a[180:183], v243 offset:8192\n"
    "ds_write_addtid_b32 v173 offset:4352\n"
    "v_mfma_f32_16x16x32_bf16 v[120:123], a[188:191], v[184:187], v[120:123]\n"
    "v_exp_f32_e32 v169, v169\n"
    "v_mfma_f32_16x16x32_bf16 v[120:123], a[184:187], v[180:183], v[120:123]\n"
    "s_waitcnt lgkmcnt(11)\n"
    "ds_write_addtid_b32 v142 offset:512\n"
    "v_cvt_pk_bf16_f32 v159, v162, v163\n"
    "v_mfma_f32_16x16x32_bf16 v[212:215], a[160:163], a[128:131], v[220:223]\n"
    "ds_write_addtid_b32 v143 offset:768\n"
    "v_mfma_f32_16x16x32_bf16 v[212:215], a[164:167], a[132:135], v[212:215]\n"
    "v_exp_f32_e32 v170, v170\n"
    "v_mfma_f32_16x16x32_bf16 v[204:207], a[164:167], a[100:103], v[220:223]\n"
    "ds_write_addtid_b32 v150 offset:1536\n"
    "v_mfma_f32_16x16x32_bf16 v[204:207], a[160:163], a[96:99], v[204:207]\n"
    "ds_read_b128 a[184:187], v242 offset:12288\n"
    "v_mfma_f32_16x16x32_bf16 v[196:199], a[160:163], a[64:67], v[220:223]\n"
    "v_exp_f32_e32 v171, v171\n"
    "v_mfma_f32_16x16x32_bf16 v[196:199], a[164:167], a[68:71], v[196:199]\n"
    "ds_write_addtid_b32 v151 offset:1792\n"
    "v_cvt_pk_bf16_f32 v166, v168, v169\n"
    "v_mfma_f32_16x16x32_bf16 v[188:191], a[164:167], a[36:39], v[220:223]\n"
    "v_exp_f32_e32 v176, v176\n"
    "v_mfma_f32_16x16x32_bf16 v[188:191], a[160:163], a[32:35], v[188:191]\n"
    "v_exp_f32_e32 v177, v177\n"
    "v_mfma_f32_16x16x32_bf16 v[180:183], a[160:163], a[0:3], v[220:223]\n"
    "ds_write_addtid_b32 v158 offset:2560\n"
    "v_cvt_pk_bf16_f32 v167, v170, v171\n"
    "v_mfma_f32_16x16x32_bf16 v[180:183], a[164:167], a[4:7], v[180:183]\n"
    "s_waitcnt vmcnt(20)\n"
    "v_exp_f32_e32 v178, v178\n"
    "v_smfmac_f32_16x16x64_bf16 v[224:227], a[232:235], a[192:199], v238\n"
    "s_waitcnt lgkmcnt(10)\n"
    "ds_read_b128 a[188:191], v243 offset:12288\n"
    "v_mfma_f32_16x16x32_bf16 v[180:183], a[168:171], a[8:11], v[180:183]\n"
    "s_bitcmp1_b32 s86, 10\n"
    "ds_read_b128 a[160:163], v240 offset:10240\n"
    "v_mfma_f32_16x16x32_bf16 v[180:183], a[172:175], a[12:15], v[180:183]\n"
    "s_cselect_b64 exec, -1, 0\n"
    "v_cvt_pk_bf16_f32 v236, v224, v225\n"
    "v_cvt_pk_bf16_f32 v237, v226, v227\n"
    "v_mfma_f32_16x16x32_bf16 v[188:191], a[172:175], a[44:47], v[188:191]\n"
    "s_cselect_b32 s88, 512, 0\n"
    "v_mov_b64_e32 v[224:225], 0\n"
    "v_mov_b64_e32 v[226:227], 0\n"
    "v_mfma_f32_16x16x32_bf16 v[188:191], a[168:171], a[40:43], v[188:191]\n"
    "buffer_store_dwordx2 v[236:237], v252, s[8:11], s87 offen sc1\n"
    "v_mfma_f32_16x16x32_bf16 v[196:199], a[168:171], a[72:75], v[196:199]\n"
    "s_mov_b64 exec, -1\n"
    "ds_read2_b32 v[228:229], v251 offset0:20 offset1:22\n"
    "v_mfma_f32_16x16x32_bf16 v[196:199], a[172:175], a[76:79], v[196:199]\n"
    "s_add_u32 s87, s87, s88\n"
    "ds_read2_b32 v[230:231], v251 offset0:24 offset1:26\n"
    "v_mfma_f32_16x16x32_bf16 v[204:207], a[172:175], a[108:111], v[204:207]\n"
    "ds_read_b32 v232, v251 offset:112\n"
    "v_mfma_f32_16x16x32_bf16 v[204:207], a[168:171], a[104:107], v[204:207]\n"
    "v_exp_f32_e32 v179, v179\n"
    "v_mfma_f32_16x16x32_bf16 v[212:215], a[168:171], a[136:139], v[212:215]\n"
    "ds_write_addtid_b32 v159 offset:2816\n"
    "v_cvt_pk_bf16_f32 v174, v176, v177\n"
    "v_mfma_f32_16x16x32_bf16 v[212:215], a[172:175], a[140:143], v[212:215]\n"
    "s_waitcnt lgkmcnt(13)\n"
    "ds_read_b128 a[164:167], v241 offset:10240\n"
    "v_mfma_f32_16x16x32_bf16 v[212:215], a[176:179], a[144:147], v[212:215]\n"
    "ds_write_addtid_b32 v166 offset:3584\n"
    "v_cvt_pk_bf16_f32 v175, v178, v179\n"
    "v_mfma_f32_16x16x32_bf16 v[212:215], a[180:183], a[148:151], v[212:215]\n"
    "ds_read_b128 a[168:171], v240 offset:14336\n"
    "v_mfma_f32_16x16x32_bf16 v[204:207], a[180:183], a[116:119], v[204:207]\n"
    "s_waitcnt lgkmcnt(6)\n"
    "ds_write_addtid_b32 v167 offset:3840\n"
    "v_mad_u32_u16 v234, v228, s90, v239 op_sel:[0,0,0,0]\n"
    "v_mfma_f32_16x16x32_bf16 v[204:207], a[176:179], a[112:115], v[204:207]\n"
    "buffer_load_dwordx4 a[192:195], v234, s[20:23], 0 offen sc1\n"
    "v_mad_u32_u16 v235, v228, s90, v239 op_sel:[1,0,0,0]\n"
    "v_mfma_f32_16x16x32_bf16 v[196:199], a[176:179], a[80:83], v[196:199]\n"
    "ds_write_addtid_b32 v174 offset:4608\n"
    "v_mfma_f32_16x16x32_bf16 v[196:199], a[180:183], a[84:87], v[196:199]\n"
    "ds_write_addtid_b32 v175 offset:4864\n"
    "v_mfma_f32_16x16x32_bf16 v[188:191], a[180:183], a[52:55], v[188:191]\n"
    "buffer_load_dwordx4 a[196:199], v235, s[20:23], 0 offen sc1\n"
    "v_mfma_f32_16x16x32_bf16 v[188:191], a[176:179], a[48:51], v[188:191]\n"
    "v_mfma_f32_16x16x32_bf16 v[180:183], a[176:179], a[16:19], v[180:183]\n"
    "s_waitcnt lgkmcnt(0)\n"
    "v_mfma_f32_16x16x32_bf16 v[180:183], a[180:183], a[20:23], v[180:183]\n"
    "s_barrier\n"
    "v_mfma_f32_16x16x32_bf16 v[180:183], a[184:187], a[24:27], v[180:183]\n"
    "s_add_u32 m0, s43, 24576\n"
    "ds_read_b128 a[172:175], v241 offset:14336\n"
    "v_mfma_f32_16x16x32_bf16 v[180:183], a[188:191], a[28:31], v[180:183]\n"
    "ds_read2st64_b32 v[144:145], v249 offset0:0 offset1:1\n"
    "v_mfma_f32_16x16x32_bf16 v[188:191], a[188:191], a[60:63], v[188:191]\n"
    "buffer_load_dwordx4 v248, s[8:11], s40 offen offset:0 sc1 lds\n"
    "v_mfma_f32_16x16x32_bf16 v[188:191], a[184:187], a[56:59], v[188:191]\n"
    "ds_read_b128 a[176:179], v242 offset:10240\n"
    "v_mfma_f32_16x16x32_bf16 v[196:199], a[184:187], a[88:91], v[196:199]\n"
    "v_mfma_f32_16x16x32_bf16 v[196:199], a[188:191], a[92:95], v[196:199]\n"
    "v_mfma_f32_16x16x32_bf16 v[204:207], a[188:191], a[124:127], v[204:207]\n"
    "buffer_load_dwordx4 v248, s[8:11], s40 offen offset:1024 sc1 lds\n"
    "v_mfma_f32_16x16x32_bf16 v[204:207], a[184:187], a[120:123], v[204:207]\n"
    "ds_read2st64_b32 v[146:147], v249 offset0:2 offset1:3\n"
    "v_mfma_f32_16x16x32_bf16 v[212:215], a[184:187], a[152:155], v[212:215]\n"
    "v_mfma_f32_16x16x32_bf16 v[212:215], a[188:191], a[156:159], v[212:215]\n"
    "ds_read_b128 a[180:183], v243 offset:10240\n"
    "v_mfma_f32_16x16x32_bf16 v[216:219], a[160:163], a[128:131], v[220:223]\n"
    "buffer_load_dwordx4 v248, s[8:11], s40 offen offset:2048 sc1 lds\n"
    "v_mfma_f32_16x16x32_bf16 v[216:219], a[164:167], a[132:135], v[216:219]\n"
    "ds_read2st64_b32 v[152:153], v249 offset0:4 offset1:5\n"
    "v_mfma_f32_16x16x32_bf16 v[208:211], a[164:167], a[100:103], v[220:223]\n"
    "v_exp_f32_e32 v180, v180\n"
    "v_mfma_f32_16x16x32_bf16 v[208:211], a[160:163], a[96:99], v[208:211]\n"
    "ds_read_b128 a[184:187], v242 offset:14336\n"
    "v_mfma_f32_16x16x32_bf16 v[200:203], a[160:163], a[64:67], v[220:223]\n"
    "buffer_load_dwordx4 v248, s[8:11], s40 offen offset:3072 sc1 lds\n"
    "v_mfma_f32_16x16x32_bf16 v[200:203], a[164:167], a[68:71], v[200:203]\n"
    "s_add_u32 m0, s43, 28672\n"
    "ds_read2st64_b32 v[154:155], v249 offset0:6 offset1:7\n"
    "v_mfma_f32_16x16x32_bf16 v[192:195], a[164:167], a[36:39], v[220:223]\n"
    "s_add_u32 s40, s40, 0x8000\n"
    "ds_read2st64_b32 v[160:161], v249 offset0:8 offset1:9\n"
    "v_mfma_f32_16x16x32_bf16 v[192:195], a[160:163], a[32:35], v[192:195]\n"
    "ds_read2st64_b32 v[162:163], v249 offset0:10 offset1:11\n"
    "v_mfma_f32_16x16x32_bf16 v[184:187], a[160:163], a[0:3], v[220:223]\n"
    "buffer_load_dwordx4 v248, s[8:11], s41 offen offset:0 sc1 lds\n"
    "v_mfma_f32_16x16x32_bf16 v[184:187], a[164:167], a[4:7], v[184:187]\n"
    "s_waitcnt vmcnt(17)\n"
    "ds_read_b128 a[188:191], v243 offset:14336\n"
    "v_smfmac_f32_16x16x64_bf16 v[224:227], a[232:235], a[200:207], v238\n"
    "s_waitcnt lgkmcnt(10)\n"
    "v_mad_u32_u16 v234, v229, s90, v239 op_sel:[0,0,0,0]\n"
    "v_mfma_f32_16x16x32_bf16 v[184:187], a[168:171], a[8:11], v[184:187]\n"
    "s_bitcmp1_b32 s86, 11\n"
    "buffer_load_dwordx4 a[200:203], v234, s[20:23], 0 offen sc1\n"
    "v_mad_u32_u16 v235, v229, s90, v239 op_sel:[1,0,0,0]\n"
    "v_mfma_f32_16x16x32_bf16 v[184:187], a[172:175], a[12:15], v[184:187]\n"
    "s_cselect_b64 exec, -1, 0\n"
    "v_cvt_pk_bf16_f32 v236, v224, v225\n"
    "v_cvt_pk_bf16_f32 v237, v226, v227\n"
    "v_mfma_f32_16x16x32_bf16 v[192:195], a[172:175], a[44:47], v[192:195]\n"
    "s_cselect_b32 s88, 512, 0\n"
    "v_mov_b64_e32 v[224:225], 0\n"
    "v_mov_b64_e32 v[226:227], 0\n"
    "v_mfma_f32_16x16x32_bf16 v[192:195], a[168:171], a[40:43], v[192:195]\n"
    "buffer_store_dwordx2 v[236:237], v252, s[8:11], s87 offen sc1\n"
    "v_mfma_f32_16x16x32_bf16 v[200:203], a[168:171], a[72:75], v[200:203]\n"
    "s_mov_b64 exec, -1\n"
    "ds_read_b64_tr_b16 a[160:161], v244 offset:0\n"
    "v_mfma_f32_16x16x32_bf16 v[200:203], a[172:175], a[76:79], v[200:203]\n"
    "s_add_u32 s87, s87, s88\n"
    "ds_read_b64_tr_b16 a[162:163], v244 offset:2048\n"
    "v_mfma_f32_16x16x32_bf16 v[208:211], a[172:175], a[108:111], v[208:211]\n"
    "buffer_load_dwordx4 v248, s[8:11], s41 offen offset:1024 sc1 lds\n"
    "v_mfma_f32_16x16x32_bf16 v[208:211], a[168:171], a[104:107], v[208:211]\n"
    "ds_read_b64_tr_b16 a[164:165], v244 offset:32768\n"
    "v_mfma_f32_16x16x32_bf16 v[216:219], a[168:171], a[136:139], v[216:219]\n"
    "ds_read2st64_b32 v[168:169], v249 offset0:12 offset1:13\n"
    "v_mfma_f32_16x16x32_bf16 v[216:219], a[172:175], a[140:143], v[216:219]\n"
    "s_waitcnt lgkmcnt(10)\n"
    "ds_read2st64_b32 v[170:171], v249 offset0:14 offset1:15\n"
    "v_mfma_f32_16x16x32_bf16 v[216:219], a[176:179], a[144:147], v[216:219]\n"
    "buffer_load_dwordx4 v248, s[8:11], s41 offen offset:2048 sc1 lds\n"
    "v_mfma_f32_16x16x32_bf16 v[216:219], a[180:183], a[148:151], v[216:219]\n"
    "ds_read_b64_tr_b16 a[166:167], v244 offset:34816\n"
    "v_mfma_f32_16x16x32_bf16 v[208:211], a[180:183], a[116:119], v[208:211]\n"
    "ds_read_b64_tr_b16 a[168:169], v245 offset:0\n"
    "v_mfma_f32_16x16x32_bf16 v[208:211], a[176:179], a[112:115], v[208:211]\n"
    "v_exp_f32_e32 v181, v181\n"
    "v_mfma_f32_16x16x32_bf16 v[200:203], a[176:179], a[80:83], v[200:203]\n"
    "buffer_load_dwordx4 v248, s[8:11], s41 offen offset:3072 sc1 lds\n"
    "v_mfma_f32_16x16x32_bf16 v[200:203], a[180:183], a[84:87], v[200:203]\n"
    "ds_read_b64_tr_b16 a[170:171], v245 offset:2048\n"
    "v_mfma_f32_16x16x32_bf16 v[192:195], a[180:183], a[52:55], v[192:195]\n"
    "s_add_u32 s41, s41, 0x8000\n"
    "ds_read2st64_b32 v[176:177], v249 offset0:16 offset1:17\n"
    "v_mfma_f32_16x16x32_bf16 v[192:195], a[176:179], a[48:51], v[192:195]\n"
    "buffer_load_dwordx4 a[204:207], v235, s[20:23], 0 offen sc1\n"
    "v_mfma_f32_16x16x32_bf16 v[184:187], a[176:179], a[16:19], v[184:187]\n"
    "ds_read_b64_tr_b16 a[172:173], v245 offset:32768\n"
    "v_mfma_f32_16x16x32_bf16 v[184:187], a[180:183], a[20:23], v[184:187]\n"
    "s_waitcnt lgkmcnt(10)\n"
    "ds_read2st64_b32 v[178:179], v249 offset0:18 offset1:19\n"
    "v_mfma_f32_16x16x32_bf16 v[184:187], a[184:187], a[24:27], v[184:187]\n"
    "ds_read_b64_tr_b16 a[176:177], v246 offset:0\n"
    "v_mfma_f32_16x16x32_bf16 v[184:187], a[188:191], a[28:31], v[184:187]\n"
    "ds_read_b64_tr_b16 a[174:175], v245 offset:34816\n"
    "v_mfma_f32_16x16x32_bf16 v[192:195], a[188:191], a[60:63], v[192:195]\n"
    "v_exp_f32_e32 v182, v182\n"
    "v_mfma_f32_16x16x32_bf16 v[192:195], a[184:187], a[56:59], v[192:195]\n"
    "v_exp_f32_e32 v183, v183\n"
    "v_mfma_f32_16x16x32_bf16 v[200:203], a[184:187], a[88:91], v[200:203]\n"
    "ds_read_b64_tr_b16 a[178:179], v246 offset:2048\n"
    "v_cvt_pk_bf16_f32 v180, v180, v181\n"
    "v_mfma_f32_16x16x32_bf16 v[200:203], a[188:191], a[92:95], v[200:203]\n"
    "v_exp_f32_e32 v188, v188\n"
    "v_mfma_f32_16x16x32_bf16 v[208:211], a[188:191], a[124:127], v[208:211]\n"
    ".long 0xbedf00ff\n"
    ".Lat_w7:\n"
    "s_setpc_b64 s[2:3]\n"
    "v_exp_f32_e32 v189, v189\n"
    "v_mfma_f32_16x16x32_bf16 v[208:211], a[184:187], a[120:123], v[208:211]\n"
    "v_cvt_pk_bf16_f32 v181, v182, v183\n"
    "v_mfma_f32_16x16x32_bf16 v[216:219], a[184:187], a[152:155], v[216:219]\n"
    "ds_read_b64_tr_b16 a[180:181], v246 offset:32768\n"
    "v_mfma_f32_16x16x32_bf16 v[216:219], a[188:191], a[156:159], v[216:219]\n"
    "s_waitcnt vmcnt(20)\n"
    "ds_read_b64_tr_b16 a[182:183], v246 offset:34816\n"
    "v_smfmac_f32_16x16x64_bf16 v[224:227], a[232:235], a[208:215], v238\n"
    "s_waitcnt lgkmcnt(5)\n"
    "v_mad_u32_u16 v234, v230, s90, v239 op_sel:[0,0,0,0]\n"
    "v_mfma_f32_16x16x32_bf16 a[236:239], a[160:163], v[140:143], a[236:239]\n"
    "s_bitcmp1_b32 s86, 12\n"
    "buffer_load_dwordx4 a[208:211], v234, s[20:23], 0 offen sc1\n"
    "v_mad_u32_u16 v235, v230, s90, v239 op_sel:[1,0,0,0]\n"
    "v_mfma_f32_16x16x32_bf16 a[236:239], a[164:167], v[144:147], a[236:239]\n"
    "s_cselect_b64 exec, -1, 0\n"
    "v_cvt_pk_bf16_f32 v236, v224, v225\n"
    "v_cvt_pk_bf16_f32 v237, v226, v227\n"
    "v_mfma_f32_16x16x32_bf16 a[240:243], a[164:167], v[152:155], a[240:243]\n"
    "s_cselect_b32 s88, 512, 0\n"
    "v_mov_b64_e32 v[224:225], 0\n"
    "v_mov_b64_e32 v[226:227], 0\n"
    "v_mfma_f32_16x16x32_bf16 a[240:243], a[160:163], v[148:151], a[240:243]\n"
    "buffer_store_dwordx2 v[236:237], v252, s[8:11], s87 offen sc1\n"
    "v_mfma_f32_16x16x32_bf16 a[244:247], a[160:163], v[156:159], a[244:247]\n"
    "s_mov_b64 exec, -1\n"
    "ds_read_b64_tr_b16 a[184:185], v247 offset:0\n"
    "v_mfma_f32_16x16x32_bf16 a[244:247], a[164:167], v[160:163], a[244:247]\n"
    "s_add_u32 s87, s87, s88\n"
    "ds_read_b64_tr_b16 a[186:187], v247 offset:2048\n"
    "v_mfma_f32_16x16x32_bf16 a[248:251], a[164:167], v[168:171], a[248:251]\n"
    "buffer_load_dwordx4 a[212:215], v235, s[20:23], 0 offen sc1\n"
    "v_mfma_f32_16x16x32_bf16 a[248:251], a[160:163], v[164:167], a[248:251]\n"
    "ds_read_b64_tr_b16 a[188:189], v247 offset:32768\n"
    "v_mfma_f32_16x16x32_bf16 a[252:255], a[160:163], v[172:175], a[252:255]\n"
    "v_exp_f32_e32 v190, v190\n"
    "v_mfma_f32_16x16x32_bf16 a[252:255], a[164:167], v[176:179], a[252:255]\n"
    "s_waitcnt lgkmcnt(6)\n"
    "v_exp_f32_e32 v191, v191\n"
    "v_mfma_f32_16x16x32_bf16 v[16:19], a[172:175], v[176:179], v[16:19]\n"
    "ds_read_b64_tr_b16 a[190:191], v247 offset:34816\n"
    "v_cvt_pk_bf16_f32 v188, v188, v189\n"
    "v_mfma_f32_16x16x32_bf16 v[16:19], a[168:171], v[172:175], v[16:19]\n"
    "ds_read_b64_tr_b16 a[160:161], v244 offset:4096\n"
    "v_mfma_f32_16x16x32_bf16 v[12:15], a[168:171], v[164:167], v[12:15]\n"
    "v_exp_f32_e32 v196, v196\n"
    "v_mfma_f32_16x16x32_bf16 v[12:15], a[172:175], v[168:171], v[12:15]\n"
    "v_exp_f32_e32 v197, v197\n"
    "v_mfma_f32_16x16x32_bf16 v[8:11], a[172:175], v[160:163], v[8:11]\n"
    "ds_read_b64_tr_b16 a[162:163], v244 offset:6144\n"
    "v_cvt_pk_bf16_f32 v189, v190, v191\n"
    "v_mfma_f32_16x16x32_bf16 v[8:11], a[168:171], v[156:159], v[8:11]\n"
    "v_exp_f32_e32 v198, v198\n"
    "v_mfma_f32_16x16x32_bf16 v[4:7], a[168:171], v[148:151], v[4:7]\n"
    "v_exp_f32_e32 v199, v199\n"
    "v_mfma_f32_16x16x32_bf16 v[4:7], a[172:175], v[152:155], v[4:7]\n"
    "v_cvt_pk_bf16_f32 v196, v196, v197\n"
    "v_mfma_f32_16x16x32_bf16 v[0:3], a[172:175], v[144:147], v[0:3]\n"
    "ds_read_b64_tr_b16 a[164:165], v244 offset:36864\n"
    "v_mfma_f32_16x16x32_bf16 v[0:3], a[168:171], v[140:143], v[0:3]\n"
    "s_waitcnt lgkmcnt(7)\n"
    "v_exp_f32_e32 v204, v204\n"
    "v_mfma_f32_16x16x32_bf16 v[20:23], a[176:179], v[140:143], v[20:23]\n"
    "ds_read_b64_tr_b16 a[168:169], v245 offset:4096\n"
    "v_mfma_f32_16x16x32_bf16 v[20:23], a[180:183], v[144:147], v[20:23]\n"
    "ds_read_b64_tr_b16 a[166:167], v244 offset:38912\n"
    "v_mfma_f32_16x16x32_bf16 v[24:27], a[180:183], v[152:155], v[24:27]\n"
    "v_exp_f32_e32 v205, v205\n"
    "v_mfma_f32_16x16x32_bf16 v[24:27], a[176:179], v[148:151], v[24:27]\n"
    "v_cvt_pk_bf16_f32 v197, v198, v199\n"
    "v_mfma_f32_16x16x32_bf16 v[28:31], a[176:179], v[156:159], v[28:31]\n"
    "ds_read_b64_tr_b16 a[170:171], v245 offset:6144\n"
    "v_mfma_f32_16x16x32_bf16 v[28:31], a[180:183], v[160:163], v[28:31]\n"
    "v_exp_f32_e32 v206, v206\n"
    "v_mfma_f32_16x16x32_bf16 v[32:35], a[180:183], v[168:171], v[32:35]\n"
    "v_exp_f32_e32 v207, v207\n"
    "v_mfma_f32_16x16x32_bf16 v[32:35], a[176:179], v[164:167], v[32:35]\n"
    "v_cvt_pk_bf16_f32 v204, v204, v205\n"
    "v_mfma_f32_16x16x32_bf16 v[36:39], a[176:179], v[172:175], v[36:39]\n"
    "ds_read_b64_tr_b16 a[172:173], v245 offset:36864\n"
    "v_mfma_f32_16x16x32_bf16 v[36:39], a[180:183], v[176:179], v[36:39]\n"
    "s_waitcnt vmcnt(20)\n"
    "ds_read_b64_tr_b16 a[174:175], v245 offset:38912\n"
    "v_smfmac_f32_16x16x64_bf16 v[224:227], a[232:235], a[216:223], v238\n"
    "s_waitcnt lgkmcnt(8)\n"
    "v_mad_u32_u16 v234, v231, s90, v239 op_sel:[0,0,0,0]\n"
    "v_mfma_f32_16x16x32_bf16 v[56:59], a[188:191], v[176:179], v[56:59]\n"
    "s_bitcmp1_b32 s86, 13\n"
    "buffer_load_dwordx4 a[216:219], v234, s[20:23], 0 offen sc1\n"
    "v_mad_u32_u16 v235, v231, s90, v239 op_sel:[1,0,0,0]\n"
    "v_mfma_f32_16x16x32_bf16 v[56:59], a[184:187], v[172:175], v[56:59]\n"
    "s_cselect_b64 exec, -1, 0\n"
    "v_cvt_pk_bf16_f32 v236, v224, v225\n"
    "v_cvt_pk_bf16_f32 v237, v226, v227\n"
    "v_mfma_f32_16x16x32_bf16 v[52:55], a[184:187], v[164:167], v[52:55]\n"
    "s_cselect_b32 s88, 512, 0\n"
    "v_mov_b64_e32 v[224:225], 0\n"
    "v_mov_b64_e32 v[226:227], 0\n"
    "v_mfma_f32_16x16x32_bf16 v[52:55], a[188:191], v[168:171], v[52:55]\n"
    "buffer_store_dwordx2 v[236:237], v252, s[8:11], s87 offen sc1\n"
    "v_mfma_f32_16x16x32_bf16 v[48:51], a[188:191], v[160:163], v[48:51]\n"
    "s_mov_b64 exec, -1\n"
    "ds_read_b64_tr_b16 a[176:177], v246 offset:4096\n"
    "v_mfma_f32_16x16x32_bf16 v[48:51], a[184:187], v[156:159], v[48:51]\n"
    "s_add_u32 s87, s87, s88\n"
    "ds_read_b64_tr_b16 a[178:179], v246 offset:6144\n"
    "v_mfma_f32_16x16x32_bf16 v[44:47], a[184:187], v[148:151], v[44:47]\n"
    "buffer_load_dwordx4 a[220:223], v235, s[20:23], 0 offen sc1\n"
    "v_mfma_f32_16x16x32_bf16 v[44:47], a[188:191], v[152:155], v[44:47]\n"
    "ds_read_b64_tr_b16 a[180:181], v246 offset:36864\n"
    "v_mfma_f32_16x16x32_bf16 v[40:43], a[188:191], v[144:147], v[40:43]\n"
    "v_exp_f32_e32 v212, v212\n"
    "v_mfma_f32_16x16x32_bf16 v[40:43], a[184:187], v[140:143], v[40:43]\n"
    "s_waitcnt lgkmcnt(6)\n"
    "v_exp_f32_e32 v213, v213\n"
    "v_mfma_f32_16x16x32_bf16 v[60:63], a[160:163], v[140:143], v[60:63]\n"
    "ds_read_b64_tr_b16 a[182:183], v246 offset:38912\n"
    "v_cvt_pk_bf16_f32 v205, v206, v207\n"
    "v_mfma_f32_16x16x32_bf16 v[60:63], a[164:167], v[144:147], v[60:63]\n"
    "ds_read_b64_tr_b16 a[184:185], v247 offset:4096\n"
    "v_mfma_f32_16x16x32_bf16 v[64:67], a[164:167], v[152:155], v[64:67]\n"
    "ds_read_b64_tr_b16 a[186:187], v247 offset:6144\n"
    "v_mfma_f32_16x16x32_bf16 v[64:67], a[160:163], v[148:151], v[64:67]\n"
    "v_exp_f32_e32 v214, v214\n"
    "v_mfma_f32_16x16x32_bf16 v[68:71], a[160:163], v[156:159], v[68:71]\n"
    "v_exp_f32_e32 v215, v215\n"
    "v_mfma_f32_16x16x32_bf16 v[68:71], a[164:167], v[160:163], v[68:71]\n"
    "ds_read_b64_tr_b16 a[188:189], v247 offset:36864\n"
    "v_cvt_pk_bf16_f32 v212, v212, v213\n"
    "v_mfma_f32_16x16x32_bf16 v[72:75], a[164:167], v[168:171], v[72:75]\n"
    "ds_read_b64_tr_b16 a[190:191], v247 offset:38912\n"
    "v_cvt_pk_bf16_f32 v213, v214, v215\n"
    "v_mfma_f32_16x16x32_bf16 v[72:75], a[160:163], v[164:167], v[72:75]\n"
    "s_waitcnt vmcnt(30)\n"
    "v_mfma_f32_16x16x32_bf16 v[76:79], a[160:163], v[172:175], v[76:79]\n"
    "s_barrier\n"
    "v_mfma_f32_16x16x32_bf16 v[76:79], a[164:167], v[176:179], v[76:79]\n"
    "s_waitcnt lgkmcnt(8)\n"
    "s_mov_b32 m0, s42\n"
    "v_mfma_f32_16x16x32_bf16 v[96:99], a[172:175], v[176:179], v[96:99]\n"
    "v_exp_f32_e32 v184, v184\n"
    "v_mfma_f32_16x16x32_bf16 v[96:99], a[168:171], v[172:175], v[96:99]\n"
    "v_exp_f32_e32 v185, v185\n"
    "v_mfma_f32_16x16x32_bf16 v[92:95], a[168:171], v[164:167], v[92:95]\n"
    "v_exp_f32_e32 v186, v186\n"
    "v_mfma_f32_16x16x32_bf16 v[92:95], a[172:175], v[168:171], v[92:95]\n"
    "ds_read_b128 a[160:163], v240 offset:16384\n"
    "ds_write_addtid_b32 v180 offset:0\n"
    "v_mfma_f32_16x16x32_bf16 v[88:91], a[172:175], v[160:163], v[88:91]\n"
    "v_exp_f32_e32 v187, v187\n"
    "v_mfma_f32_16x16x32_bf16 v[88:91], a[168:171], v[156:159], v[88:91]\n"
    "ds_write_addtid_b32 v181 offset:256\n"
    "v_cvt_pk_bf16_f32 v182, v184, v185\n"
    "v_mfma_f32_16x16x32_bf16 v[84:87], a[168:171], v[148:151], v[84:87]\n"
    "v_exp_f32_e32 v192, v192\n"
    "v_mfma_f32_16x16x32_bf16 v[84:87], a[172:175], v[152:155], v[84:87]\n"
    "v_exp_f32_e32 v193, v193\n"
    "v_mfma_f32_16x16x32_bf16 v[80:83], a[172:175], v[144:147], v[80:83]\n"
    "ds_write_addtid_b32 v188 offset:1024\n"
    "v_cvt_pk_bf16_f32 v183, v186, v187\n"
    "v_mfma_f32_16x16x32_bf16 v[80:83], a[168:171], v[140:143], v[80:83]\n"
    "s_waitcnt vmcnt(20)\n"
    "ds_read_b128 a[164:167], v241 offset:16384\n"
    "ds_write_addtid_b32 v189 offset:1280\n"
    "v_smfmac_f32_16x16x64_bf16 v[224:227], a[232:235], a[224:231], v238\n"
    "s_waitcnt lgkmcnt(10)\n"
    "ds_write_addtid_b32 v196 offset:2048\n"
    "v_mad_u32_u16 v234, v232, s90, v239 op_sel:[0,0,0,0]\n"
    "v_mfma_f32_16x16x32_bf16 v[100:103], a[176:179], v[140:143], v[100:103]\n"
    "s_bitcmp1_b32 s86, 14\n"
    "buffer_load_dwordx4 a[224:227], v234, s[20:23], 0 offen sc1\n"
    "v_mad_u32_u16 v235, v232, s90, v239 op_sel:[1,0,0,0]\n"
    "v_mfma_f32_16x16x32_bf16 v[100:103], a[180:183], v[144:147], v[100:103]\n"
    "s_cselect_b64 exec, -1, 0\n"
    "v_cvt_pk_bf16_f32 v236, v224, v225\n"
    "v_cvt_pk_bf16_f32 v237, v226, v227\n"
    "v_mfma_f32_16x16x32_bf16 v[104:107], a[180:183], v[152:155], v[104:107]\n"
    "s_cselect_b32 s88, 512, 0\n"
    "v_mov_b64_e32 v[224:225], 0\n"
    "v_mov_b64_e32 v[226:227], 0\n"
    "v_mfma_f32_16x16x32_bf16 v[104:107], a[176:179], v[148:151], v[104:107]\n"
    "buffer_store_dwordx2 v[236:237], v252, s[8:11], s87 offen sc1\n"
    "v_mfma_f32_16x16x32_bf16 v[108:111], a[176:179], v[156:159], v[108:111]\n"
    "s_mov_b64 exec, -1\n"
    "ds_read_b128 a[168:171], v240 offset:20480\n"
    "ds_write_addtid_b32 v197 offset:2304\n"
    "v_mfma_f32_16x16x32_bf16 v[108:111], a[180:183], v[160:163], v[108:111]\n"
    "s_add_u32 s87, s87, s88\n"
    "v_exp_f32_e32 v194, v194\n"
    "v_mfma_f32_16x16x32_bf16 v[112:115], a[180:183], v[168:171], v[112:115]\n"
    "buffer_load_dwordx4 a[228:231], v235, s[20:23], 0 offen sc1\n"
    "ds_write_addtid_b32 v204 offset:3072\n"
    "v_mfma_f32_16x16x32_bf16 v[112:115], a[176:179], v[164:167], v[112:115]\n"
    "ds_write_addtid_b32 v205 offset:3328\n"
    "v_mfma_f32_16x16x32_bf16 v[116:119], a[176:179], v[172:175], v[116:119]\n"
    "v_exp_f32_e32 v195, v195\n"
    "v_mfma_f32_16x16x32_bf16 v[116:119], a[180:183], v[176:179], v[116:119]\n"
    "s_waitcnt lgkmcnt(11)\n"
    "ds_read_b128 a[172:175], v241 offset:20480\n"
    "v_cvt_pk_bf16_f32 v190, v192, v193\n"
    "v_mfma_f32_16x16x32_bf16 v[136:139], a[188:191], v[176:179], v[136:139]\n"
    "v_exp_f32_e32 v200, v200\n"
    "v_mfma_f32_16x16x32_bf16 v[136:139], a[184:187], v[172:175], v[136:139]\n"
    "v_exp_f32_e32 v201, v201\n"
    "v_mfma_f32_16x16x32_bf16 v[132:135], a[184:187], v[164:167], v[132:135]\n"
    "ds_read_b128 a[176:179], v242 offset:16384\n"
    "v_cvt_pk_bf16_f32 v191, v194, v195\n"
    "v_mfma_f32_16x16x32_bf16 v[132:135], a[188:191], v[168:171], v[132:135]\n"
    "v_exp_f32_e32 v202, v202\n"
    "v_mfma_f32_16x16x32_bf16 v[128:131], a[188:191], v[160:163], v[128:131]\n"
    "v_exp_f32_e32 v203, v203\n"
    "v_mfma_f32_16x16x32_bf16 v[128:131], a[184:187], v[156:159], v[128:131]\n"
    "ds_write_addtid_b32 v212 offset:4096\n"
    "v_cvt_pk_bf16_f32 v198, v200, v201\n"
    "v_mfma_f32_16x16x32_bf16 v[124:127], a[184:187], v[148:151], v[124:127]\n"
    "v_exp_f32_e32 v208, v208\n"
    "v_mfma_f32_16x16x32_bf16 v[124:127], a[188:191], v[152:155], v[124:127]\n"
    "ds_read_b128 a[180:183], v243 offset:16384\n"
    "ds_write_addtid_b32 v213 offset:4352\n"
    "v_mfma_f32_16x16x32_bf16 v[120:123], a[188:191], v[144:147], v[120:123]\n"
    "v_exp_f32_e32 v209, v209\n"
    "v_mfma_f32_16x16x32_bf16 v[120:123], a[184:187], v[140:143], v[120:123]\n"
    "s_waitcnt lgkmcnt(11)\n"
    "ds_write_addtid_b32 v182 offset:512\n"
    "v_cvt_pk_bf16_f32 v199, v202, v203\n"
    "v_mfma_f32_16x16x32_bf16 v[172:175], a[160:163], a[128:131], v[220:223]\n"
    "ds_write_addtid_b32 v183 offset:768\n"
    "v_mfma_f32_16x16x32_bf16 v[172:175], a[164:167], a[132:135], v[172:175]\n"
    "v_exp_f32_e32 v210, v210\n"
    "v_mfma_f32_16x16x32_bf16 v[164:167], a[164:167], a[100:103], v[220:223]\n"
    "ds_write_addtid_b32 v190 offset:1536\n"
    "v_mfma_f32_16x16x32_bf16 v[164:167], a[160:163], a[96:99], v[164:167]\n"
    "ds_read_b128 a[184:187], v242 offset:20480\n"
    "v_mfma_f32_16x16x32_bf16 v[156:159], a[160:163], a[64:67], v[220:223]\n"
    "v_exp_f32_e32 v211, v211\n"
    "v_mfma_f32_16x16x32_bf16 v[156:159], a[164:167], a[68:71], v[156:159]\n"
    "ds_write_addtid_b32 v191 offset:1792\n"
    "v_cvt_pk_bf16_f32 v206, v208, v209\n"
    "v_mfma_f32_16x16x32_bf16 v[148:151], a[164:167], a[36:39], v[220:223]\n"
    "v_exp_f32_e32 v216, v216\n"
    "v_mfma_f32_16x16x32_bf16 v[148:151], a[160:163], a[32:35], v[148:151]\n"
    "v_exp_f32_e32 v217, v217\n"
    "v_mfma_f32_16x16x32_bf16 v[140:143], a[160:163], a[0:3], v[220:223]\n"
    "ds_write_addtid_b32 v198 offset:2560\n"
    "v_cvt_pk_bf16_f32 v207, v210, v211\n"
    "v_mfma_f32_16x16x32_bf16 v[140:143], a[164:167], a[4:7], v[140:143]\n"
    "s_waitcnt vmcnt(20)\n"
    "v_exp_f32_e32 v218, v218\n"
    "v_smfmac_f32_16x16x64_bf16 v[224:227], a[232:235], a[192:199], v238\n"
    "s_waitcnt lgkmcnt(10)\n"
    "ds_read_b128 a[188:191], v243 offset:20480\n"
    "v_mfma_f32_16x16x32_bf16 v[140:143], a[168:171], a[8:11], v[140:143]\n"
    "s_bitcmp1_b32 s86, 15\n"
    "ds_read_b128 a[160:163], v240 offset:18432\n"
    "v_mfma_f32_16x16x32_bf16 v[140:143], a[172:175], a[12:15], v[140:143]\n"
    "s_cselect_b64 exec, -1, 0\n"
    "v_cvt_pk_bf16_f32 v236, v224, v225\n"
    "v_cvt_pk_bf16_f32 v237, v226, v227\n"
    "v_mfma_f32_16x16x32_bf16 v[148:151], a[172:175], a[44:47], v[148:151]\n"
    "s_cselect_b32 s88, 512, 0\n"
    "v_mov_b64_e32 v[224:225], 0\n"
    "v_mov_b64_e32 v[226:227], 0\n"
    "v_mfma_f32_16x16x32_bf16 v[148:151], a[168:171], a[40:43], v[148:151]\n"
    "buffer_store_dwordx2 v[236:237], v252, s[8:11], s87 offen sc1\n"
    "v_mfma_f32_16x16x32_bf16 v[156:159], a[168:171], a[72:75], v[156:159]\n"
    "s_mov_b64 exec, -1\n"
    "ds_read2_b32 v[228:229], v251 offset0:30 offset1:32\n"
    "v_mfma_f32_16x16x32_bf16 v[156:159], a[172:175], a[76:79], v[156:159]\n"
    "s_add_u32 s87, s87, s88\n"
    "ds_read2_b32 v[230:231], v251 offset0:34 offset1:36\n"
    "v_mfma_f32_16x16x32_bf16 v[164:167], a[172:175], a[108:111], v[164:167]\n"
    "ds_read_b32 v232, v251 offset:152\n"
    "v_mfma_f32_16x16x32_bf16 v[164:167], a[168:171], a[104:107], v[164:167]\n"
    "v_exp_f32_e32 v219, v219\n"
    "v_mfma_f32_16x16x32_bf16 v[172:175], a[168:171], a[136:139], v[172:175]\n"
    "ds_write_addtid_b32 v199 offset:2816\n"
    "v_cvt_pk_bf16_f32 v214, v216, v217\n"
    "v_mfma_f32_16x16x32_bf16 v[172:175], a[172:175], a[140:143], v[172:175]\n"
    "s_waitcnt lgkmcnt(13)\n"
    "ds_read_b128 a[164:167], v241 offset:18432\n"
    "v_mfma_f32_16x16x32_bf16 v[172:175], a[176:179], a[144:147], v[172:175]\n"
    "ds_write_addtid_b32 v206 offset:3584\n"
    "v_cvt_pk_bf16_f32 v215, v218, v219\n"
    "v_mfma_f32_16x16x32_bf16 v[172:175], a[180:183], a[148:151], v[172:175]\n"
    "ds_read_b128 a[168:171], v240 offset:22528\n"
    "v_mfma_f32_16x16x32_bf16 v[164:167], a[180:183], a[116:119], v[164:167]\n"
    "s_waitcnt lgkmcnt(6)\n"
    "ds_write_addtid_b32 v207 offset:3840\n"
    "v_mad_u32_u16 v234, v228, s90, v239 op_sel:[0,0,0,0]\n"
    "v_mfma_f32_16x16x32_bf16 v[164:167], a[176:179], a[112:115], v[164:167]\n"
    "buffer_load_dwordx4 a[192:195], v234, s[20:23], 0 offen sc1\n"
    "v_mad_u32_u16 v235, v228, s90, v239 op_sel:[1,0,0,0]\n"
    "v_mfma_f32_16x16x32_bf16 v[156:159], a[176:179], a[80:83], v[156:159]\n"
    "ds_write_addtid_b32 v214 offset:4608\n"
    "v_add_u32_e32 v251, 160, v251\n"
    "v_mfma_f32_16x16x32_bf16 v[156:159], a[180:183], a[84:87], v[156:159]\n"
    "ds_write_addtid_b32 v215 offset:4864\n"
    "v_mfma_f32_16x16x32_bf16 v[148:151], a[180:183], a[52:55], v[148:151]\n"
    "buffer_load_dwordx4 a[196:199], v235, s[20:23], 0 offen sc1\n"
    "v_mfma_f32_16x16x32_bf16 v[148:151], a[176:179], a[48:51], v[148:151]\n"
    "v_mfma_f32_16x16x32_bf16 v[140:143], a[176:179], a[16:19], v[140:143]\n"
    "s_waitcnt lgkmcnt(0)\n"
    "v_mfma_f32_16x16x32_bf16 v[140:143], a[180:183], a[20:23], v[140:143]\n"
    "s_barrier\n"
    "v_mfma_f32_16x16x32_bf16 v[140:143], a[184:187], a[24:27], v[140:143]\n"
    "s_add_u32 m0, s43, 0\n"
    "ds_read_b128 a[172:175], v241 offset:22528\n"
    "v_mfma_f32_16x16x32_bf16 v[140:143], a[188:191], a[28:31], v[140:143]\n"
    "ds_read2st64_b32 v[184:185], v249 offset0:0 offset1:1\n"
    "v_mfma_f32_16x16x32_bf16 v[148:151], a[188:191], a[60:63], v[148:151]\n"
    "buffer_load_dwordx4 v248, s[8:11], s40 offen offset:0 sc1 lds\n"
    "v_mfma_f32_16x16x32_bf16 v[148:151], a[184:187], a[56:59], v[148:151]\n"
    "ds_read_b128 a[176:179], v242 offset:18432\n"
    "v_mfma_f32_16x16x32_bf16 v[156:159], a[184:187], a[88:91], v[156:159]\n"
    "v_mfma_f32_16x16x32_bf16 v[156:159], a[188:191], a[92:95], v[156:159]\n"
    "v_mfma_f32_16x16x32_bf16 v[164:167], a[188:191], a[124:127], v[164:167]\n"
    "buffer_load_dwordx4 v248, s[8:11], s40 offen offset:1024 sc1 lds\n"
    "v_mfma_f32_16x16x32_bf16 v[164:167], a[184:187], a[120:123], v[164:167]\n"
    "ds_read2st64_b32 v[186:187], v249 offset0:2 offset1:3\n"
    "v_mfma_f32_16x16x32_bf16 v[172:175], a[184:187], a[152:155], v[172:175]\n"
    "v_mfma_f32_16x16x32_bf16 v[172:175], a[188:191], a[156:159], v[172:175]\n"
    "ds_read_b128 a[180:183], v243 offset:18432\n"
    "v_mfma_f32_16x16x32_bf16 v[176:179], a[160:163], a[128:131], v[220:223]\n"
    "buffer_load_dwordx4 v248, s[8:11], s40 offen offset:2048 sc1 lds\n"
    "v_mfma_f32_16x16x32_bf16 v[176:179], a[164:167], a[132:135], v[176:179]\n"
    "ds_read2st64_b32 v[192:193], v249 offset0:4 offset1:5\n"
    "v_mfma_f32_16x16x32_bf16 v[168:171], a[164:167], a[100:103], v[220:223]\n"
    "v_exp_f32_e32 v140, v140\n"
    "v_mfma_f32_16x16x32_bf16 v[168:171], a[160:163], a[96:99], v[168:171]\n"
    "ds_read_b128 a[184:187], v242 offset:22528\n"
    "v_mfma_f32_16x16x32_bf16 v[160:163], a[160:163], a[64:67], v[220:223]\n"
    "buffer_load_dwordx4 v248, s[8:11], s40 offen offset:3072 sc1 lds\n"
    "v_mfma_f32_16x16x32_bf16 v[160:163], a[164:167], a[68:71], v[160:163]\n"
    "s_add_u32 m0, s43, 4096\n"
    "ds_read2st64_b32 v[194:195], v249 offset0:6 offset1:7\n"
    "v_mfma_f32_16x16x32_bf16 v[152:155], a[164:167], a[36:39], v[220:223]\n"
    "s_add_u32 s40, s40, 0x8000\n"
    "ds_read2st64_b32 v[200:201], v249 offset0:8 offset1:9\n"
    "v_mfma_f32_16x16x32_bf16 v[152:155], a[160:163], a[32:35], v[152:155]\n"
    "ds_read2st64_b32 v[202:203], v249 offset0:10 offset1:11\n"
    "v_mfma_f32_16x16x32_bf16 v[144:147], a[160:163], a[0:3], v[220:223]\n"
    "buffer_load_dwordx4 v248, s[8:11], s41 offen offset:0 sc1 lds\n"
    "v_mfma_f32_16x16x32_bf16 v[144:147], a[164:167], a[4:7], v[144:147]\n"
    "s_waitcnt vmcnt(17)\n"
    "ds_read_b128 a[188:191], v243 offset:22528\n"
    "v_smfmac_f32_16x16x64_bf16 v[224:227], a[232:235], a[200:207], v238\n"
    "s_waitcnt lgkmcnt(10)\n"
    "v_mad_u32_u16 v234, v229, s90, v239 op_sel:[0,0,0,0]\n"
    "v_mfma_f32_16x16x32_bf16 v[144:147], a[168:171], a[8:11], v[144:147]\n"
    "s_bitcmp1_b32 s86, 16\n"
    "buffer_load_dwordx4 a[200:203], v234, s[20:23], 0 offen sc1\n"
    "v_mad_u32_u16 v235, v229, s90, v239 op_sel:[1,0,0,0]\n"
    "v_mfma_f32_16x16x32_bf16 v[144:147], a[172:175], a[12:15], v[144:147]\n"
    "s_cselect_b64 exec, -1, 0\n"
    "v_cvt_pk_bf16_f32 v236, v224, v225\n"
    "v_cvt_pk_bf16_f32 v237, v226, v227\n"
    "v_mfma_f32_16x16x32_bf16 v[152:155], a[172:175], a[44:47], v[152:155]\n"
    "s_cselect_b32 s88, 512, 0\n"
    "v_mov_b64_e32 v[224:225], 0\n"
    "v_mov_b64_e32 v[226:227], 0\n"
    "v_mfma_f32_16x16x32_bf16 v[152:155], a[168:171], a[40:43], v[152:155]\n"
    "buffer_store_dwordx2 v[236:237], v252, s[8:11], s87 offen sc1\n"
    "v_mfma_f32_16x16x32_bf16 v[160:163], a[168:171], a[72:75], v[160:163]\n"
    "s_mov_b64 exec, -1\n"
    "ds_read_b64_tr_b16 a[160:161], v244 offset:8192\n"
    "v_mfma_f32_16x16x32_bf16 v[160:163], a[172:175], a[76:79], v[160:163]\n"
    "s_add_u32 s87, s87, s88\n"
    "ds_read_b64_tr_b16 a[162:163], v244 offset:10240\n"
    "v_mfma_f32_16x16x32_bf16 v[168:171], a[172:175], a[108:111], v[168:171]\n"
    "buffer_load_dwordx4 v248, s[8:11], s41 offen offset:1024 sc1 lds\n"
    "v_mfma_f32_16x16x32_bf16 v[168:171], a[168:171], a[104:107], v[168:171]\n"
    "ds_read_b64_tr_b16 a[164:165], v244 offset:40960\n"
    "v_mfma_f32_16x16x32_bf16 v[176:179], a[168:171], a[136:139], v[176:179]\n"
    "ds_read2st64_b32 v[208:209], v249 offset0:12 offset1:13\n"
    "v_mfma_f32_16x16x32_bf16 v[176:179], a[172:175], a[140:143], v[176:179]\n"
    "s_waitcnt lgkmcnt(10)\n"
    "ds_read2st64_b32 v[210:211], v249 offset0:14 offset1:15\n"
    "v_mfma_f32_16x16x32_bf16 v[176:179], a[176:179], a[144:147], v[176:179]\n"
    "buffer_load_dwordx4 v248, s[8:11], s41 offen offset:2048 sc1 lds\n"
    "v_mfma_f32_16x16x32_bf16 v[176:179], a[180:183], a[148:151], v[176:179]\n"
    "ds_read_b64_tr_b16 a[166:167], v244 offset:43008\n"
    "v_mfma_f32_16x16x32_bf16 v[168:171], a[180:183], a[116:119], v[168:171]\n"
    "ds_read_b64_tr_b16 a[168:169], v245 offset:8192\n"
    "v_mfma_f32_16x16x32_bf16 v[168:171], a[176:179], a[112:115], v[168:171]\n"
    "v_exp_f32_e32 v141, v141\n"
    "v_mfma_f32_16x16x32_bf16 v[160:163], a[176:179], a[80:83], v[160:163]\n"
    "buffer_load_dwordx4 v248, s[8:11], s41 offen offset:3072 sc1 lds\n"
    "v_mfma_f32_16x16x32_bf16 v[160:163], a[180:183], a[84:87], v[160:163]\n"
    "ds_read_b64_tr_b16 a[170:171], v245 offset:10240\n"
    "v_mfma_f32_16x16x32_bf16 v[152:155], a[180:183], a[52:55], v[152:155]\n"
    "s_add_u32 s41, s41, 0x8000\n"
    "ds_read2st64_b32 v[216:217], v249 offset0:16 offset1:17\n"
    "v_mfma_f32_16x16x32_bf16 v[152:155], a[176:179], a[48:51], v[152:155]\n"
    "buffer_load_dwordx4 a[204:207], v235, s[20:23], 0 offen sc1\n"
    "v_mfma_f32_16x16x32_bf16 v[144:147], a[176:179], a[16:19], v[144:147]\n"
    "ds_read_b64_tr_b16 a[172:173], v245 offset:40960\n"
    "v_mfma_f32_16x16x32_bf16 v[144:147], a[180:183], a[20:23], v[144:147]\n"
    "s_waitcnt lgkmcnt(10)\n"
    "ds_read2st64_b32 v[218:219], v249 offset0:18 offset1:19\n"
    "v_mfma_f32_16x16x32_bf16 v[144:147], a[184:187], a[24:27], v[144:147]\n"
    "ds_read_b64_tr_b16 a[176:177], v246 offset:8192\n"
    "v_mfma_f32_16x16x32_bf16 v[144:147], a[188:191], a[28:31], v[144:147]\n"
    "ds_read_b64_tr_b16 a[174:175], v245 offset:43008\n"
    "v_mfma_f32_16x16x32_bf16 v[152:155], a[188:191], a[60:63], v[152:155]\n"
    "v_exp_f32_e32 v142, v142\n"
    "v_mfma_f32_16x16x32_bf16 v[152:155], a[184:187], a[56:59], v[152:155]\n"
    "v_exp_f32_e32 v143, v143\n"
    "v_mfma_f32_16x16x32_bf16 v[160:163], a[184:187], a[88:91], v[160:163]\n"
    "ds_read_b64_tr_b16 a[178:179], v246 offset:10240\n"
    "v_cvt_pk_bf16_f32 v140, v140, v141\n"
    "v_mfma_f32_16x16x32_bf16 v[160:163], a[188:191], a[92:95], v[160:163]\n"
    "v_exp_f32_e32 v148, v148\n"
    "v_mfma_f32_16x16x32_bf16 v[168:171], a[188:191], a[124:127], v[168:171]\n"
    "v_exp_f32_e32 v149, v149\n"
    "v_mfma_f32_16x16x32_bf16 v[168:171], a[184:187], a[120:123], v[168:171]\n"
    "v_cvt_pk_bf16_f32 v141, v142, v143\n"
    "v_mfma_f32_16x16x32_bf16 v[176:179], a[184:187], a[152:155], v[176:179]\n"
    "ds_read_b64_tr_b16 a[180:181], v246 offset:40960\n"
    "v_mfma_f32_16x16x32_bf16 v[176:179], a[188:191], a[156:159], v[176:179]\n"
    "s_waitcnt vmcnt(20)\n"
    "ds_read_b64_tr_b16 a[182:183], v246 offset:43008\n"
    "v_smfmac_f32_16x16x64_bf16 v[224:227], a[232:235], a[208:215], v238\n"
    "s_waitcnt lgkmcnt(5)\n"
    "v_mad_u32_u16 v234, v230, s90, v239 op_sel:[0,0,0,0]\n"
    "v_mfma_f32_16x16x32_bf16 a[236:239], a[160:163], v[180:183], a[236:239]\n"
    "s_bitcmp1_b32 s86, 17\n"
    "buffer_load_dwordx4 a[208:211], v234, s[20:23], 0 offen sc1\n"
    "v_mad_u32_u16 v235, v230, s90, v239 op_sel:[1,0,0,0]\n"
    "v_mfma_f32_16x16x32_bf16 a[236:239], a[164:167], v[184:187], a[236:239]\n"
    "s_cselect_b64 exec, -1, 0\n"
    "v_cvt_pk_bf16_f32 v236, v224, v225\n"
    "v_cvt_pk_bf16_f32 v237, v226, v227\n"
    "v_mfma_f32_16x16x32_bf16 a[240:243], a[164:167], v[192:195], a[240:243]\n"
    "s_cselect_b32 s88, 512, 0\n"
    "v_mov_b64_e32 v[224:225], 0\n"
    "v_mov_b64_e32 v[226:227], 0\n"
    "v_mfma_f32_16x16x32_bf16 a[240:243], a[160:163], v[188:191], a[240:243]\n"
    "buffer_store_dwordx2 v[236:237], v252, s[8:11], s87 offen sc1\n"
    "v_mfma_f32_16x16x32_bf16 a[244:247], a[160:163], v[196:199], a[244:247]\n"
    "s_mov_b64 exec, -1\n"
    "ds_read_b64_tr_b16 a[184:185], v247 offset:8192\n"
    "v_mfma_f32_16x16x32_bf16 a[244:247], a[164:167], v[200:203], a[244:247]\n"
    "s_add_u32 s87, s87, s88\n"
    "ds_read_b64_tr_b16 a[186:187], v247 offset:10240\n"
    "v_mfma_f32_16x16x32_bf16 a[248:251], a[164:167], v[208:211], a[248:251]\n"
    "buffer_load_dwordx4 a[212:215], v235, s[20:23], 0 offen sc1\n"
    "v_mfma_f32_16x16x32_bf16 a[248:251], a[160:163], v[204:207], a[248:251]\n"
    "ds_read_b64_tr_b16 a[188:189], v247 offset:40960\n"
    "v_mfma_f32_16x16x32_bf16 a[252:255], a[160:163], v[212:215], a[252:255]\n"
    "v_exp_f32_e32 v150, v150\n"
    "v_mfma_f32_16x16x32_bf16 a[252:255], a[164:167], v[216:219], a[252:255]\n"
    "s_waitcnt lgkmcnt(6)\n"
    "v_exp_f32_e32 v151, v151\n"
    "v_mfma_f32_16x16x32_bf16 v[16:19], a[172:175], v[216:219], v[16:19]\n"
    "ds_read_b64_tr_b16 a[190:191], v247 offset:43008\n"
    "v_cvt_pk_bf16_f32 v148, v148, v149\n"
    "v_mfma_f32_16x16x32_bf16 v[16:19], a[168:171], v[212:215], v[16:19]\n"
    "ds_read_b64_tr_b16 a[160:161], v244 offset:12288\n"
    "v_mfma_f32_16x16x32_bf16 v[12:15], a[168:171], v[204:207], v[12:15]\n"
    "v_exp_f32_e32 v156, v156\n"
    "v_mfma_f32_16x16x32_bf16 v[12:15], a[172:175], v[208:211], v[12:15]\n"
    "v_exp_f32_e32 v157, v157\n"
    "v_mfma_f32_16x16x32_bf16 v[8:11], a[172:175], v[200:203], v[8:11]\n"
    "ds_read_b64_tr_b16 a[162:163], v244 offset:14336\n"
    "v_cvt_pk_bf16_f32 v149, v150, v151\n"
    "v_mfma_f32_16x16x32_bf16 v[8:11], a[168:171], v[196:199], v[8:11]\n"
    "v_exp_f32_e32 v158, v158\n"
    "v_mfma_f32_16x16x32_bf16 v[4:7], a[168:171], v[188:191], v[4:7]\n"
    "v_exp_f32_e32 v159, v159\n"
    "v_mfma_f32_16x16x32_bf16 v[4:7], a[172:175], v[192:195], v[4:7]\n"
    "v_cvt_pk_bf16_f32 v156, v156, v157\n"
    "v_mfma_f32_16x16x32_bf16 v[0:3], a[172:175], v[184:187], v[0:3]\n"
    "ds_read_b64_tr_b16 a[164:165], v244 offset:45056\n"
    "v_mfma_f32_16x16x32_bf16 v[0:3], a[168:171], v[180:183], v[0:3]\n"
    "s_waitcnt lgkmcnt(7)\n"
    "v_exp_f32_e32 v164, v164\n"
    "v_mfma_f32_16x16x32_bf16 v[20:23], a[176:179], v[180:183], v[20:23]\n"
    "ds_read_b64_tr_b16 a[168:169], v245 offset:12288\n"
    "v_mfma_f32_16x16x32_bf16 v[20:23], a[180:183], v[184:187], v[20:23]\n"
    "ds_read_b64_tr_b16 a[166:167], v244 offset:47104\n"
    "v_mfma_f32_16x16x32_bf16 v[24:27], a[180:183], v[192:195], v[24:27]\n"
    "v_exp_f32_e32 v165, v165\n"
    "v_mfma_f32_16x16x32_bf16 v[24:27], a[176:179], v[188:191], v[24:27]\n"
    "v_cvt_pk_bf16_f32 v157, v158, v159\n"
    "v_mfma_f32_16x16x32_bf16 v[28:31], a[176:179], v[196:199], v[28:31]\n"
    "ds_read_b64_tr_b16 a[170:171], v245 offset:14336\n"
    "v_mfma_f32_16x16x32_bf16 v[28:31], a[180:183], v[200:203], v[28:31]\n"
    "v_exp_f32_e32 v166, v166\n"
    "v_mfma_f32_16x16x32_bf16 v[32:35], a[180:183], v[208:211], v[32:35]\n"
    "v_exp_f32_e32 v167, v167\n"
    "v_mfma_f32_16x16x32_bf16 v[32:35], a[176:179], v[204:207], v[32:35]\n"
    "v_cvt_pk_bf16_f32 v164, v164, v165\n"
    "v_mfma_f32_16x16x32_bf16 v[36:39], a[176:179], v[212:215], v[36:39]\n"
    "ds_read_b64_tr_b16 a[172:173], v245 offset:45056\n"
    "v_mfma_f32_16x16x32_bf16 v[36:39], a[180:183], v[216:219], v[36:39]\n"
    "s_waitcnt vmcnt(20)\n"
    "ds_read_b64_tr_b16 a[174:175], v245 offset:47104\n"
    "v_smfmac_f32_16x16x64_bf16 v[224:227], a[232:235], a[216:223], v238\n"
    "s_waitcnt lgkmcnt(8)\n"
    "v_mad_u32_u16 v234, v231, s90, v239 op_sel:[0,0,0,0]\n"
    "v_mfma_f32_16x16x32_bf16 v[56:59], a[188:191], v[216:219], v[56:59]\n"
    "s_bitcmp1_b32 s86, 18\n"
    "buffer_load_dwordx4 a[216:219], v234, s[20:23], 0 offen sc1\n"
    "v_mad_u32_u16 v235, v231, s90, v239 op_sel:[1,0,0,0]\n"
    "v_mfma_f32_16x16x32_bf16 v[56:59], a[184:187], v[212:215], v[56:59]\n"
    "s_cselect_b64 exec, -1, 0\n"
    "v_cvt_pk_bf16_f32 v236, v224, v225\n"
    "v_cvt_pk_bf16_f32 v237, v226, v227\n"
    "v_mfma_f32_16x16x32_bf16 v[52:55], a[184:187], v[204:207], v[52:55]\n"
    "s_cselect_b32 s88, 512, 0\n"
    "v_mov_b64_e32 v[224:225], 0\n"
    "v_mov_b64_e32 v[226:227], 0\n"
    "v_mfma_f32_16x16x32_bf16 v[52:55], a[188:191], v[208:211], v[52:55]\n"
    "buffer_store_dwordx2 v[236:237], v252, s[8:11], s87 offen sc1\n"
    "v_mfma_f32_16x16x32_bf16 v[48:51], a[188:191], v[200:203], v[48:51]\n"
    "s_mov_b64 exec, -1\n"
    "ds_read_b64_tr_b16 a[176:177], v246 offset:12288\n"
    "v_mfma_f32_16x16x32_bf16 v[48:51], a[184:187], v[196:199], v[48:51]\n"
    "s_add_u32 s87, s87, s88\n"
    "ds_read_b64_tr_b16 a[178:179], v246 offset:14336\n"
    "v_mfma_f32_16x16x32_bf16 v[44:47], a[184:187], v[188:191], v[44:47]\n"
    "buffer_load_dwordx4 a[220:223], v235, s[20:23], 0 offen sc1\n"
    "v_mfma_f32_16x16x32_bf16 v[44:47], a[188:191], v[192:195], v[44:47]\n"
    "ds_read_b64_tr_b16 a[180:181], v246 offset:45056\n"
    "v_mfma_f32_16x16x32_bf16 v[40:43], a[188:191], v[184:187], v[40:43]\n"
    "v_exp_f32_e32 v172, v172\n"
    "v_mfma_f32_16x16x32_bf16 v[40:43], a[184:187], v[180:183], v[40:43]\n"
    "s_waitcnt lgkmcnt(6)\n"
    "v_exp_f32_e32 v173, v173\n"
    "v_mfma_f32_16x16x32_bf16 v[60:63], a[160:163], v[180:183], v[60:63]\n"
    "ds_read_b64_tr_b16 a[182:183], v246 offset:47104\n"
    "v_cvt_pk_bf16_f32 v165, v166, v167\n"
    "v_mfma_f32_16x16x32_bf16 v[60:63], a[164:167], v[184:187], v[60:63]\n"
    "ds_read_b64_tr_b16 a[184:185], v247 offset:12288\n"
    "v_mfma_f32_16x16x32_bf16 v[64:67], a[164:167], v[192:195], v[64:67]\n"
    "ds_read_b64_tr_b16 a[186:187], v247 offset:14336\n"
    "v_mfma_f32_16x16x32_bf16 v[64:67], a[160:163], v[188:191], v[64:67]\n"
    "v_exp_f32_e32 v174, v174\n"
    "v_mfma_f32_16x16x32_bf16 v[68:71], a[160:163], v[196:199], v[68:71]\n"
    "v_exp_f32_e32 v175, v175\n"
    "v_mfma_f32_16x16x32_bf16 v[68:71], a[164:167], v[200:203], v[68:71]\n"
    "ds_read_b64_tr_b16 a[188:189], v247 offset:45056\n"
    "v_cvt_pk_bf16_f32 v172, v172, v173\n"
    "v_mfma_f32_16x16x32_bf16 v[72:75], a[164:167], v[208:211], v[72:75]\n"
    "ds_read_b64_tr_b16 a[190:191], v247 offset:47104\n"
    "v_cvt_pk_bf16_f32 v173, v174, v175\n"
    "v_mfma_f32_16x16x32_bf16 v[72:75], a[160:163], v[204:207], v[72:75]\n"
    "s_waitcnt vmcnt(30)\n"
    "v_mfma_f32_16x16x32_bf16 v[76:79], a[160:163], v[212:215], v[76:79]\n"
    "s_barrier\n"
    "v_mfma_f32_16x16x32_bf16 v[76:79], a[164:167], v[216:219], v[76:79]\n"
    "s_waitcnt lgkmcnt(8)\n"
    "s_mov_b32 m0, s42\n"
    "v_mfma_f32_16x16x32_bf16 v[96:99], a[172:175], v[216:219], v[96:99]\n"
    "v_exp_f32_e32 v144, v144\n"
    "v_mfma_f32_16x16x32_bf16 v[96:99], a[168:171], v[212:215], v[96:99]\n"
    "v_exp_f32_e32 v145, v145\n"
    "v_mfma_f32_16x16x32_bf16 v[92:95], a[168:171], v[204:207], v[92:95]\n"
    "v_exp_f32_e32 v146, v146\n"
    "v_mfma_f32_16x16x32_bf16 v[92:95], a[172:175], v[208:211], v[92:95]\n"
    "ds_read_b128 a[160:163], v240 offset:24576\n"
    "ds_write_addtid_b32 v140 offset:0\n"
    "v_mfma_f32_16x16x32_bf16 v[88:91], a[172:175], v[200:203], v[88:91]\n"
    "v_exp_f32_e32 v147, v147\n"
    "v_mfma_f32_16x16x32_bf16 v[88:91], a[168:171], v[196:199], v[88:91]\n"
    "ds_write_addtid_b32 v141 offset:256\n"
    "v_cvt_pk_bf16_f32 v142, v144, v145\n"
    "v_mfma_f32_16x16x32_bf16 v[84:87], a[168:171], v[188:191], v[84:87]\n"
    "v_exp_f32_e32 v152, v152\n"
    "v_mfma_f32_16x16x32_bf16 v[84:87], a[172:175], v[192:195], v[84:87]\n"
    "v_exp_f32_e32 v153, v153\n"
    "v_mfma_f32_16x16x32_bf16 v[80:83], a[172:175], v[184:187], v[80:83]\n"
    "ds_write_addtid_b32 v148 offset:1024\n"
    "v_cvt_pk_bf16_f32 v143, v146, v147\n"
    "v_mfma_f32_16x16x32_bf16 v[80:83], a[168:171], v[180:183], v[80:83]\n"
    "s_waitcnt vmcnt(20)\n"
    "ds_read_b128 a[164:167], v241 offset:24576\n"
    "ds_write_addtid_b32 v149 offset:1280\n"
    "v_smfmac_f32_16x16x64_bf16 v[224:227], a[232:235], a[224:231], v238\n"
    "s_waitcnt lgkmcnt(10)\n"
    "ds_write_addtid_b32 v156 offset:2048\n"
    "v_mad_u32_u16 v234, v232, s90, v239 op_sel:[0,0,0,0]\n"
    "v_mfma_f32_16x16x32_bf16 v[100:103], a[176:179], v[180:183], v[100:103]\n"
    "s_bitcmp1_b32 s86, 19\n"
    "buffer_load_dwordx4 a[224:227], v234, s[20:23], 0 offen sc1\n"
    "v_mad_u32_u16 v235, v232, s90, v239 op_sel:[1,0,0,0]\n"
    ".long 0xbedf00ff\n"
    ".Lat_w8:\n"
    "s_setpc_b64 s[2:3]\n"
    "v_mfma_f32_16x16x32_bf16 v[100:103], a[180:183], v[184:187], v[100:103]\n"
    "s_cselect_b64 exec, -1, 0\n"
    "v_cvt_pk_bf16_f32 v236, v224, v225\n"
    "v_cvt_pk_bf16_f32 v237, v226, v227\n"
    "v_mfma_f32_16x16x32_bf16 v[104:107], a[180:183], v[192:195], v[104:107]\n"
    "s_cselect_b32 s88, 512, 0\n"
    "v_mov_b64_e32 v[224:225], 0\n"
    "v_mov_b64_e32 v[226:227], 0\n"
    "v_mfma_f32_16x16x32_bf16 v[104:107], a[176:179], v[188:191], v[104:107]\n"
    "buffer_store_dwordx2 v[236:237], v252, s[8:11], s87 offen sc1\n"
    "v_mfma_f32_16x16x32_bf16 v[108:111], a[176:179], v[196:199], v[108:111]\n"
    "s_mov_b64 exec, -1\n"
    "ds_read_b128 a[168:171], v240 offset:28672\n"
    "ds_write_addtid_b32 v157 offset:2304\n"
    "v_mfma_f32_16x16x32_bf16 v[108:111], a[180:183], v[200:203], v[108:111]\n"
    "s_add_u32 s87, s87, s88\n"
    "v_exp_f32_e32 v154, v154\n"
    "v_mfma_f32_16x16x32_bf16 v[112:115], a[180:183], v[208:211], v[112:115]\n"
    "s_add_u32 s89, s89, 1\n"
    "buffer_load_dwordx4 a[228:231], v235, s[20:23], 0 offen sc1\n"
    "ds_write_addtid_b32 v164 offset:3072\n"
    "v_mfma_f32_16x16x32_bf16 v[112:115], a[176:179], v[204:207], v[112:115]\n"
    "ds_write_addtid_b32 v165 offset:3328\n"
    "v_readlane_b32 s86, v254, s89\n"
    "v_mfma_f32_16x16x32_bf16 v[116:119], a[176:179], v[212:215], v[116:119]\n"
    "v_exp_f32_e32 v155, v155\n"
    "v_mfma_f32_16x16x32_bf16 v[116:119], a[180:183], v[216:219], v[116:119]\n"
    "s_waitcnt lgkmcnt(11)\n"
    "ds_read_b128 a[172:175], v241 offset:28672\n"
    "v_cvt_pk_bf16_f32 v150, v152, v153\n"
    "v_mfma_f32_16x16x32_bf16 v[136:139], a[188:191], v[216:219], v[136:139]\n"
    "v_exp_f32_e32 v160, v160\n"
    "v_mfma_f32_16x16x32_bf16 v[136:139], a[184:187], v[212:215], v[136:139]\n"
    "v_exp_f32_e32 v161, v161\n"
    "v_mfma_f32_16x16x32_bf16 v[132:135], a[184:187], v[204:207], v[132:135]\n"
    "ds_read_b128 a[176:179], v242 offset:24576\n"
    "v_cvt_pk_bf16_f32 v151, v154, v155\n"
    "v_mfma_f32_16x16x32_bf16 v[132:135], a[188:191], v[208:211], v[132:135]\n"
    "v_exp_f32_e32 v162, v162\n"
    "v_mfma_f32_16x16x32_bf16 v[128:131], a[188:191], v[200:203], v[128:131]\n"
    "v_exp_f32_e32 v163, v163\n"
    "v_mfma_f32_16x16x32_bf16 v[128:131], a[184:187], v[196:199], v[128:131]\n"
    "ds_write_addtid_b32 v172 offset:4096\n"
    "v_cvt_pk_bf16_f32 v158, v160, v161\n"
    "v_mfma_f32_16x16x32_bf16 v[124:127], a[184:187], v[188:191], v[124:127]\n"
    "v_exp_f32_e32 v168, v168\n"
    "v_mfma_f32_16x16x32_bf16 v[124:127], a[188:191], v[192:195], v[124:127]\n"
    "s_sub_u32 s44, s44, 1\n"
    "ds_read_b128 a[180:183], v243 offset:24576\n"
    "ds_write_addtid_b32 v173 offset:4352\n"
    "v_mfma_f32_16x16x32_bf16 v[120:123], a[188:191], v[184:187], v[120:123]\n"
    "s_cmp_lg_u32 s44, s80\n"
    "v_exp_f32_e32 v169, v169\n"
    "v_mfma_f32_16x16x32_bf16 v[120:123], a[184:187], v[180:183], v[120:123]\n"
    "s_waitcnt lgkmcnt(11)\n"
    "ds_write_addtid_b32 v142 offset:512\n"
    "v_cvt_pk_bf16_f32 v159, v162, v163\n"
    "s_cbranch_scc1 .Lat_gloop\n"
    ".Lat_g2:\n"
    "v_mov_b64_e32 v[224:225], v[220:221]\n"
    "v_mov_b64_e32 v[226:227], v[220:221]\n"
    "v_mov_b64_e32 v[228:229], v[220:221]\n"
    "v_mov_b64_e32 v[230:231], v[220:221]\n"
    "v_mov_b64_e32 v[232:233], v[220:221]\n"
    "v_mov_b64_e32 v[234:235], v[220:221]\n"
    "v_mov_b64_e32 v[236:237], v[220:221]\n"
    "v_mov_b64_e32 v[238:239], v[220:221]\n"
    "s_mov_b32 s80, 0\n"
    "s_waitcnt lgkmcnt(0)\n"
    "s_cmp_lg_u32 s44, 0\n"
    "s_cbranch_scc1 .Lat_loop\n"
    ".Lat_tail:\n"
    "v_mfma_f32_16x16x32_bf16 v[212:215], a[160:163], a[128:131], v[236:239]\n"
    ".long 0xbedf00ff\n"
    ".Lat_w9:\n"
    "s_setpc_b64 s[2:3]\n"
    "v_and_b32_e32 v189, 0x1f0, v250\n"
    "v_lshlrev_b32_e32 v190, 3, v250\n"
    "v_mfma_f32_16x16x32_bf16 v[212:215], a[164:167], a[132:135], v[212:215]\n"
    "ds_read_b128 a[184:187], v242 offset:28672\n"
    "v_lshrrev_b32_e32 v253, 4, v250\n"
    "v_mfma_f32_16x16x32_bf16 v[204:207], a[164:167], a[100:103], v[232:235]\n"
    "s_mov_b32 s52, 6100992\n"
    "ds_read_b128 a[188:191], v243 offset:28672\n"
    "v_bfe_u32 v251, v253, 4, 2\n"
    "v_mfma_f32_16x16x32_bf16 v[204:207], a[160:163], a[96:99], v[204:207]\n"
    "s_lshr_b32 s81, s34, 6\n"
    "v_lshlrev_b32_e32 v251, 1, v251\n"
    "v_xor_b32_e32 v251, v251, v253\n"
    "v_mfma_f32_16x16x32_bf16 v[196:199], a[160:163], a[64:67], v[228:231]\n"
    "s_lshl_b32 s81, s81, 9\n"
    "ds_write_addtid_b32 v143 offset:768\n"
    "v_and_b32_e32 v251, 7, v251\n"
    "v_mfma_f32_16x16x32_bf16 v[196:199], a[164:167], a[68:71], v[196:199]\n"
    "s_waitcnt lgkmcnt(9)\n"
    "v_exp_f32_e32 v170, v170\n"
    "v_mfma_f32_16x16x32_bf16 v[196:199], a[168:171], a[72:75], v[196:199]\n"
    "s_add_u32 s81, s81, 6074368\n"
    "v_exp_f32_e32 v171, v171\n"
    "v_mfma_f32_16x16x32_bf16 v[196:199], a[172:175], a[76:79], v[196:199]\n"
    "s_lshl_b32 s63, s93, 13\n"
    "ds_write_addtid_b32 v150 offset:1536\n"
    "v_cvt_pk_bf16_f32 v166, v168, v169\n"
    "v_mfma_f32_16x16x32_bf16 v[204:207], a[172:175], a[108:111], v[204:207]\n"
    "s_xor_b32 s45, s63, 0x4000\n"
    "ds_read_b128 a[160:163], v240 offset:26624\n"
    "v_lshlrev_b32_e32 v251, 4, v251\n"
    "v_mfma_f32_16x16x32_bf16 v[204:207], a[168:171], a[104:107], v[204:207]\n"
    "s_mul_i32 s58, s93, 33792\n"
    "v_lshrrev_b32_e32 v253, 3, v253\n"
    "v_lshl_add_u32 v251, v253, 9, v251\n"
    "v_mfma_f32_16x16x32_bf16 v[212:215], a[168:171], a[136:139], v[212:215]\n"
    "s_add_u32 s57, s58, 5513216\n"
    "v_bfe_u32 v255, v250, 2, 4\n"
    "v_mov_b32_e32 v254, s39\n"
    "v_mfma_f32_16x16x32_bf16 v[212:215], a[172:175], a[140:143], v[212:215]\n"
    "s_waitcnt lgkmcnt(6)\n"
    "v_exp_f32_e32 v176, v176\n"
    "v_mfma_f32_16x16x32_bf16 v[212:215], a[176:179], a[144:147], v[212:215]\n"
    "s_add_u32 s58, s58, 5378048\n"
    "ds_read_b128 a[168:171], v240 offset:30720\n"
    "v_lshrrev_b32_e32 v252, 2, v250\n"
    "v_mfma_f32_16x16x32_bf16 v[212:215], a[180:183], a[148:151], v[212:215]\n"
    "s_add_u32 s36, s35, 0x8000\n"
    "v_exp_f32_e32 v177, v177\n"
    "v_mfma_f32_16x16x32_bf16 v[204:207], a[180:183], a[116:119], v[204:207]\n"
    "s_add_u32 s37, s35, 0x10000\n"
    "ds_read_b128 a[164:167], v241 offset:26624\n"
    "v_cvt_pk_bf16_f32 v167, v170, v171\n"
    "v_mfma_f32_16x16x32_bf16 v[204:207], a[176:179], a[112:115], v[204:207]\n"
    "s_cmp_lt_u32 s32, 2\n"
    "s_cselect_b32 s37, s37, 2.0\n"
    "ds_write_addtid_b32 v151 offset:1792\n"
    "v_mfma_f32_16x16x32_bf16 v[196:199], a[176:179], a[80:83], v[196:199]\n"
    "buffer_load_dword v253, v255, s[8:11], s52 offen offset:0 sc1\n"
    "v_mfma_f32_16x16x32_bf16 v[196:199], a[180:183], a[84:87], v[196:199]\n"
    "s_waitcnt lgkmcnt(6)\n"
    "ds_write_addtid_b32 v158 offset:2560\n"
    "v_mfma_f32_16x16x32_bf16 v[196:199], a[184:187], a[88:91], v[196:199]\n"
    "ds_write_addtid_b32 v159 offset:2816\n"
    "ds_write_addtid_b32 v166 offset:3584\n"
    "v_mfma_f32_16x16x32_bf16 v[196:199], a[188:191], a[92:95], v[196:199]\n"
    "v_exp_f32_e32 v178, v178\n"
    "v_mfma_f32_16x16x32_bf16 v[204:207], a[188:191], a[124:127], v[204:207]\n"
    "v_exp_f32_e32 v179, v179\n"
    "v_mfma_f32_16x16x32_bf16 v[204:207], a[184:187], a[120:123], v[204:207]\n"
    "v_cvt_pk_bf16_f32 v174, v176, v177\n"
    "v_cvt_pk_bf16_f32 v175, v178, v179\n"
    "v_mfma_f32_16x16x32_bf16 v[212:215], a[184:187], a[152:155], v[212:215]\n"
    "ds_read_b128 a[172:175], v241 offset:30720\n"
    "ds_write_addtid_b32 v167 offset:3840\n"
    "v_mfma_f32_16x16x32_bf16 v[212:215], a[188:191], a[156:159], v[212:215]\n"
    "s_waitcnt lgkmcnt(6)\n"
    "ds_write_addtid_b32 v174 offset:4608\n"
    "ds_write_addtid_b32 v175 offset:4864\n"
    "v_mfma_f32_16x16x32_bf16 v[216:219], a[160:163], a[128:131], v[236:239]\n"
    "ds_read_b128 a[184:187], v242 offset:30720\n"
    "v_mfma_f32_16x16x32_bf16 v[216:219], a[164:167], a[132:135], v[216:219]\n"
    "ds_read_b128 a[176:179], v242 offset:26624\n"
    "v_mfma_f32_16x16x32_bf16 v[208:211], a[164:167], a[100:103], v[232:235]\n"
    "ds_read_b128 a[180:183], v243 offset:26624\n"
    "v_mfma_f32_16x16x32_bf16 v[208:211], a[160:163], a[96:99], v[208:211]\n"
    "s_waitcnt lgkmcnt(3)\n"
    "v_mfma_f32_16x16x32_bf16 v[200:203], a[160:163], a[64:67], v[228:231]\n"
    "s_barrier\n"
    "v_mfma_f32_16x16x32_bf16 v[200:203], a[164:167], a[68:71], v[200:203]\n"
    "ds_read2st64_b32 v[178:179], v249 offset0:18 offset1:19\n"
    "v_mfma_f32_16x16x32_bf16 v[200:203], a[168:171], a[72:75], v[200:203]\n"
    "s_add_u32 m0, s43, 8192\n"
    "ds_read_b128 a[188:191], v243 offset:30720\n"
    "ds_read2st64_b32 v[176:177], v249 offset0:16 offset1:17\n"
    "v_mfma_f32_16x16x32_bf16 v[200:203], a[172:175], a[76:79], v[200:203]\n"
    "buffer_load_dwordx4 v248, s[8:11], s40 offen offset:0 sc1 lds\n"
    "v_mfma_f32_16x16x32_bf16 v[208:211], a[172:175], a[108:111], v[208:211]\n"
    "ds_read_b64_tr_b16 a[160:161], v244 offset:16384\n"
    "ds_read2st64_b32 v[170:171], v249 offset0:14 offset1:15\n"
    "v_mfma_f32_16x16x32_bf16 v[208:211], a[168:171], a[104:107], v[208:211]\n"
    "ds_read2st64_b32 v[168:169], v249 offset0:12 offset1:13\n"
    "v_mfma_f32_16x16x32_bf16 v[216:219], a[168:171], a[136:139], v[216:219]\n"
    "ds_read2st64_b32 v[162:163], v249 offset0:10 offset1:11\n"
    "v_mfma_f32_16x16x32_bf16 v[216:219], a[172:175], a[140:143], v[216:219]\n"
    "s_waitcnt lgkmcnt(7)\n"
    "ds_read_b64_tr_b16 a[162:163], v244 offset:18432\n"
    "v_mfma_f32_16x16x32_bf16 v[216:219], a[176:179], a[144:147], v[216:219]\n"
    "buffer_load_dwordx4 v248, s[8:11], s40 offen offset:1024 sc1 lds\n"
    "v_mfma_f32_16x16x32_bf16 v[216:219], a[180:183], a[148:151], v[216:219]\n"
    "ds_read_b64_tr_b16 a[164:165], v244 offset:49152\n"
    "ds_read_b64_tr_b16 a[166:167], v244 offset:51200\n"
    "v_mfma_f32_16x16x32_bf16 v[208:211], a[180:183], a[116:119], v[208:211]\n"
    "ds_read2st64_b32 v[160:161], v249 offset0:8 offset1:9\n"
    "v_mfma_f32_16x16x32_bf16 v[208:211], a[176:179], a[112:115], v[208:211]\n"
    "ds_read2st64_b32 v[154:155], v249 offset0:6 offset1:7\n"
    "v_mfma_f32_16x16x32_bf16 v[200:203], a[176:179], a[80:83], v[200:203]\n"
    "ds_read2st64_b32 v[152:153], v249 offset0:4 offset1:5\n"
    "v_mfma_f32_16x16x32_bf16 v[200:203], a[180:183], a[84:87], v[200:203]\n"
    "s_waitcnt lgkmcnt(11)\n"
    "ds_read2st64_b32 v[144:145], v249 offset0:0 offset1:1\n"
    "ds_read2st64_b32 v[146:147], v249 offset0:2 offset1:3\n"
    "v_mfma_f32_16x16x32_bf16 v[200:203], a[184:187], a[88:91], v[200:203]\n"
    "buffer_load_dwordx4 v248, s[8:11], s40 offen offset:2048 sc1 lds\n"
    "v_mfma_f32_16x16x32_bf16 v[200:203], a[188:191], a[92:95], v[200:203]\n"
    "ds_read_b64_tr_b16 a[168:169], v245 offset:16384\n"
    "v_mfma_f32_16x16x32_bf16 v[208:211], a[188:191], a[124:127], v[208:211]\n"
    "ds_read_b64_tr_b16 a[170:171], v245 offset:18432\n"
    "v_mfma_f32_16x16x32_bf16 v[208:211], a[184:187], a[120:123], v[208:211]\n"
    "v_mfma_f32_16x16x32_bf16 v[216:219], a[184:187], a[152:155], v[216:219]\n"
    "ds_read_b64_tr_b16 a[176:177], v246 offset:16384\n"
    "v_mfma_f32_16x16x32_bf16 v[216:219], a[188:191], a[156:159], v[216:219]\n"
    "s_waitcnt lgkmcnt(3)\n"
    "v_exp_f32_e32 v196, v196\n"
    "v_mfma_f32_16x16x32_bf16 a[252:255], a[160:163], v[172:175], a[252:255]\n"
    "buffer_load_dwordx4 v248, s[8:11], s40 offen offset:3072 sc1 lds\n"
    "v_mfma_f32_16x16x32_bf16 a[252:255], a[164:167], v[176:179], a[252:255]\n"
    "s_add_u32 s40, s40, 0x8000\n"
    "ds_read_b64_tr_b16 a[172:173], v245 offset:49152\n"
    "v_mfma_f32_16x16x32_bf16 a[248:251], a[164:167], v[168:171], a[248:251]\n"
    "s_add_u32 m0, s43, 12288\n"
    "ds_read_b64_tr_b16 a[174:175], v245 offset:51200\n"
    "v_mfma_f32_16x16x32_bf16 a[248:251], a[160:163], v[164:167], a[248:251]\n"
    "v_exp_f32_e32 v197, v197\n"
    "v_mfma_f32_16x16x32_bf16 a[244:247], a[160:163], v[156:159], a[244:247]\n"
    "ds_read_b64_tr_b16 a[178:179], v246 offset:18432\n"
    "v_mfma_f32_16x16x32_bf16 a[244:247], a[164:167], v[160:163], a[244:247]\n"
    "buffer_load_dwordx4 v248, s[8:11], s41 offen offset:0 sc1 lds\n"
    "v_mfma_f32_16x16x32_bf16 a[240:243], a[164:167], v[152:155], a[240:243]\n"
    "ds_read_b64_tr_b16 a[180:181], v246 offset:49152\n"
    "v_mfma_f32_16x16x32_bf16 a[240:243], a[160:163], v[148:151], a[240:243]\n"
    "ds_read_b64_tr_b16 a[184:185], v247 offset:16384\n"
    "v_mfma_f32_16x16x32_bf16 a[236:239], a[160:163], v[140:143], a[236:239]\n"
    "ds_read_b64_tr_b16 a[186:187], v247 offset:18432\n"
    "v_mfma_f32_16x16x32_bf16 a[236:239], a[164:167], v[144:147], a[236:239]\n"
    "s_waitcnt lgkmcnt(4)\n"
    "v_exp_f32_e32 v198, v198\n"
    "v_mfma_f32_16x16x32_bf16 v[0:3], a[168:171], v[140:143], v[0:3]\n"
    "buffer_load_dwordx4 v248, s[8:11], s41 offen offset:1024 sc1 lds\n"
    "v_mfma_f32_16x16x32_bf16 v[0:3], a[172:175], v[144:147], v[0:3]\n"
    "v_exp_f32_e32 v199, v199\n"
    "v_mfma_f32_16x16x32_bf16 v[4:7], a[172:175], v[152:155], v[4:7]\n"
    "ds_read_b64_tr_b16 a[182:183], v246 offset:51200\n"
    "v_cvt_pk_bf16_f32 v196, v196, v197\n"
    "v_mfma_f32_16x16x32_bf16 v[4:7], a[168:171], v[148:151], v[4:7]\n"
    "ds_read_b64_tr_b16 a[188:189], v247 offset:49152\n"
    "v_mfma_f32_16x16x32_bf16 v[8:11], a[168:171], v[156:159], v[8:11]\n"
    "v_exp_f32_e32 v204, v204\n"
    "v_mfma_f32_16x16x32_bf16 v[8:11], a[172:175], v[160:163], v[8:11]\n"
    "buffer_load_dwordx4 v248, s[8:11], s41 offen offset:2048 sc1 lds\n"
    "v_mfma_f32_16x16x32_bf16 v[12:15], a[172:175], v[168:171], v[12:15]\n"
    "ds_read_b64_tr_b16 a[190:191], v247 offset:51200\n"
    "v_mfma_f32_16x16x32_bf16 v[12:15], a[168:171], v[164:167], v[12:15]\n"
    "s_waitcnt vmcnt(7)\n"
    "v_mfma_f32_16x16x32_bf16 v[16:19], a[168:171], v[172:175], v[16:19]\n"
    "s_barrier\n"
    "v_mfma_f32_16x16x32_bf16 v[16:19], a[172:175], v[176:179], v[16:19]\n"
    "s_waitcnt lgkmcnt(2)\n"
    "v_add_u32_e32 v249, s45, v252\n"
    "v_add_u32_e32 v252, s63, v252\n"
    "v_mfma_f32_16x16x32_bf16 v[36:39], a[176:179], v[172:175], v[36:39]\n"
    "buffer_store_dword v254, off, s[8:11], s84 offset:0 sc1\n"
    "v_mfma_f32_16x16x32_bf16 v[36:39], a[180:183], v[176:179], v[36:39]\n"
    "s_mul_i32 s45, s49, 63\n"
    "ds_read_b64_tr_b16 a[160:161], v244 offset:20480\n"
    "v_mfma_f32_16x16x32_bf16 v[32:35], a[180:183], v[168:171], v[32:35]\n"
    "s_add_u32 s45, s45, s33\n"
    "v_exp_f32_e32 v205, v205\n"
    "v_mfma_f32_16x16x32_bf16 v[32:35], a[176:179], v[164:167], v[32:35]\n"
    "buffer_load_dwordx4 v248, s[8:11], s41 offen offset:3072 sc1 lds\n"
    "v_mfma_f32_16x16x32_bf16 v[28:31], a[176:179], v[156:159], v[28:31]\n"
    "s_add_u32 s41, s41, 0x8000\n"
    "ds_read_b64_tr_b16 a[162:163], v244 offset:22528\n"
    "v_cmp_ne_u32_e32 vcc, s39, v253\n"
    "v_mfma_f32_16x16x32_bf16 v[28:31], a[180:183], v[160:163], v[28:31]\n"
    "s_mov_b32 m0, s63\n"
    "ds_read_b64_tr_b16 a[168:169], v245 offset:20480\n"
    "v_mfma_f32_16x16x32_bf16 v[24:27], a[180:183], v[152:155], v[24:27]\n"
    "s_mul_i32 s45, s45, 405504\n"
    "ds_read_b64_tr_b16 a[164:165], v244 offset:53248\n"
    "v_mfma_f32_16x16x32_bf16 v[24:27], a[176:179], v[148:151], v[24:27]\n"
    "s_cbranch_vccnz .Lat_wpoll\n"
    ".Lat_wpoll_ret:\n"
    "v_mfma_f32_16x16x32_bf16 v[20:23], a[176:179], v[140:143], v[20:23]\n"
    "s_add_u32 s62, s57, 0\n"
    "ds_read_b64_tr_b16 a[166:167], v244 offset:55296\n"
    "v_mfma_f32_16x16x32_bf16 v[20:23], a[180:183], v[144:147], v[20:23]\n"
    "s_waitcnt lgkmcnt(5)\n"
    "ds_read_b64_tr_b16 a[170:171], v245 offset:22528\n"
    "v_cvt_pk_bf16_f32 v197, v198, v199\n"
    "v_mfma_f32_16x16x32_bf16 v[40:43], a[184:187], v[140:143], v[40:43]\n"
    "buffer_load_dwordx4 a[216:219], v250, s[8:11], s62 offen offset:0 sc1\n"
    "v_mfma_f32_16x16x32_bf16 v[40:43], a[188:191], v[144:147], v[40:43]\n"
    "s_add_u32 s45, s45, 16588800\n"
    "ds_read_b64_tr_b16 a[176:177], v246 offset:20480\n"
    "v_mfma_f32_16x16x32_bf16 v[44:47], a[188:191], v[152:155], v[44:47]\n"
    "s_lshl_b32 s46, s51, 12\n"
    "ds_read_b64_tr_b16 a[178:179], v246 offset:22528\n"
    "v_mfma_f32_16x16x32_bf16 v[44:47], a[184:187], v[148:151], v[44:47]\n"
    "s_lshl_b32 s48, s50, 15\n"
    "ds_read_b64_tr_b16 a[180:181], v246 offset:53248\n"
    "v_mfma_f32_16x16x32_bf16 v[48:51], a[184:187], v[156:159], v[48:51]\n"
    "s_add_u32 s46, s46, s48\n"
    "v_exp_f32_e32 v206, v206\n"
    "v_mfma_f32_16x16x32_bf16 v[48:51], a[188:191], v[160:163], v[48:51]\n"
    "buffer_load_dwordx4 a[220:223], v250, s[8:11], s62 offen offset:1024 sc1\n"
    "v_mfma_f32_16x16x32_bf16 v[52:55], a[188:191], v[168:171], v[52:55]\n"
    "s_cmp_eq_u32 s51, 0\n"
    "s_cselect_b32 s47, 2.0, 0\n"
    "ds_read_b64_tr_b16 a[172:173], v245 offset:53248\n"
    "v_mfma_f32_16x16x32_bf16 v[52:55], a[184:187], v[164:167], v[52:55]\n"
    "s_add_u32 s47, s47, 98304\n"
    "ds_read_b64_tr_b16 a[174:175], v245 offset:55296\n"
    "v_mfma_f32_16x16x32_bf16 v[56:59], a[184:187], v[172:175], v[56:59]\n"
    "s_lshl_b32 s61, s50, 9\n"
    "v_exp_f32_e32 v207, v207\n"
    "v_mfma_f32_16x16x32_bf16 v[56:59], a[188:191], v[176:179], v[56:59]\n"
    "s_waitcnt lgkmcnt(6)\n"
    "ds_read_b64_tr_b16 a[184:185], v247 offset:20480\n"
    "v_mfma_f32_16x16x32_bf16 v[76:79], a[160:163], v[172:175], v[76:79]\n"
    "buffer_load_dwordx4 a[224:227], v250, s[8:11], s62 offen offset:2048 sc1\n"
    "v_mfma_f32_16x16x32_bf16 v[76:79], a[164:167], v[176:179], v[76:79]\n"
    "s_add_u32 s61, s61, s47\n"
    "ds_write_addtid_b32 v196 offset:0\n"
    "v_cvt_pk_bf16_f32 v204, v204, v205\n"
    "v_mfma_f32_16x16x32_bf16 v[72:75], a[164:167], v[168:171], v[72:75]\n"
    "s_add_u32 s48, s32, 3\n"
    "v_exp_f32_e32 v212, v212\n"
    "v_mfma_f32_16x16x32_bf16 v[72:75], a[160:163], v[164:167], v[72:75]\n"
    "s_and_b32 s48, s48, 3\n"
    "ds_read_b64_tr_b16 a[182:183], v246 offset:55296\n"
    "v_mfma_f32_16x16x32_bf16 v[68:71], a[160:163], v[156:159], v[68:71]\n"
    "s_mul_i32 s48, s48, 101376\n"
    "ds_read_b64_tr_b16 a[186:187], v247 offset:22528\n"
    "v_mfma_f32_16x16x32_bf16 v[68:71], a[164:167], v[160:163], v[68:71]\n"
    "buffer_load_dwordx4 a[228:231], v250, s[8:11], s62 offen offset:3072 sc1\n"
    "v_mfma_f32_16x16x32_bf16 v[64:67], a[164:167], v[152:155], v[64:67]\n"
    "s_add_u32 s62, s57, 4096\n"
    "ds_write_addtid_b32 v197 offset:256\n"
    "v_mfma_f32_16x16x32_bf16 v[64:67], a[160:163], v[148:151], v[64:67]\n"
    "s_add_u32 s48, s48, s45\n"
    "v_exp_f32_e32 v213, v213\n"
    "v_mfma_f32_16x16x32_bf16 v[60:63], a[160:163], v[140:143], v[60:63]\n"
    "s_add_u32 s29, s48, s46\n"
    "ds_read_b64_tr_b16 a[188:189], v247 offset:53248\n"
    "v_cvt_pk_bf16_f32 v205, v206, v207\n"
    "v_mfma_f32_16x16x32_bf16 v[60:63], a[164:167], v[144:147], v[60:63]\n"
    "s_waitcnt lgkmcnt(6)\n"
    "v_exp_f32_e32 v214, v214\n"
    "v_mfma_f32_16x16x32_bf16 v[80:83], a[168:171], v[140:143], v[80:83]\n"
    "buffer_load_dwordx4 a[232:235], v250, s[8:11], s62 offen offset:0 sc1\n"
    "v_mfma_f32_16x16x32_bf16 v[80:83], a[172:175], v[144:147], v[80:83]\n"
    "s_add_u32 s29, s29, 8192\n"
    "v_exp_f32_e32 v215, v215\n"
    "v_mfma_f32_16x16x32_bf16 v[84:87], a[172:175], v[152:155], v[84:87]\n"
    "s_add_u32 s89, s48, s61\n"
    "ds_read_b64_tr_b16 a[190:191], v247 offset:55296\n"
    "v_mfma_f32_16x16x32_bf16 v[84:87], a[168:171], v[148:151], v[84:87]\n"
    "s_add_u32 s89, s89, 128\n"
    "ds_write_addtid_b32 v204 offset:1024\n"
    "v_cvt_pk_bf16_f32 v212, v212, v213\n"
    "v_mfma_f32_16x16x32_bf16 v[88:91], a[168:171], v[156:159], v[88:91]\n"
    "s_add_u32 s48, s32, 2\n"
    "ds_read_b128 a[160:163], v240 offset:0\n"
    "v_mfma_f32_16x16x32_bf16 v[88:91], a[172:175], v[160:163], v[88:91]\n"
    "v_mfma_f32_16x16x32_bf16 v[92:95], a[172:175], v[168:171], v[92:95]\n"
    "s_barrier\n"
    "v_mfma_f32_16x16x32_bf16 v[92:95], a[168:171], v[164:167], v[92:95]\n"
    "buffer_load_dwordx4 v[192:195], v250, s[8:11], s62 offen offset:1024 sc1\n"
    "v_mfma_f32_16x16x32_bf16 v[96:99], a[168:171], v[172:175], v[96:99]\n"
    "s_add_u32 m0, s43, 16384\n"
    "ds_read_b128 a[164:167], v241 offset:0\n"
    "v_mfma_f32_16x16x32_bf16 v[96:99], a[172:175], v[176:179], v[96:99]\n"
    "s_waitcnt lgkmcnt(7)\n"
    "v_exp_f32_e32 v200, v200\n"
    "v_mfma_f32_16x16x32_bf16 v[116:119], a[176:179], v[172:175], v[116:119]\n"
    "buffer_load_dwordx4 v248, s[8:11], s40 offen offset:0 sc1 lds\n"
    "v_mfma_f32_16x16x32_bf16 v[116:119], a[180:183], v[176:179], v[116:119]\n"
    "s_mov_b32 m0, s63\n"
    "v_mfma_f32_16x16x32_bf16 v[112:115], a[180:183], v[168:171], v[112:115]\n"
    "s_and_b32 s48, s48, 3\n"
    "v_exp_f32_e32 v201, v201\n"
    "v_mfma_f32_16x16x32_bf16 v[112:115], a[176:179], v[164:167], v[112:115]\n"
    "s_mul_i32 s48, s48, 101376\n"
    "ds_read_b128 a[168:171], v240 offset:4096\n"
    "v_cvt_pk_bf16_f32 v213, v214, v215\n"
    "v_mfma_f32_16x16x32_bf16 v[108:111], a[176:179], v[156:159], v[108:111]\n"
    "s_add_u32 m0, s43, 16384\n"
    "v_exp_f32_e32 v202, v202\n"
    "v_mfma_f32_16x16x32_bf16 v[108:111], a[180:183], v[160:163], v[108:111]\n"
    "buffer_load_dwordx4 v248, s[8:11], s40 offen offset:1024 sc1 lds\n"
    "v_mfma_f32_16x16x32_bf16 v[104:107], a[180:183], v[152:155], v[104:107]\n"
    "s_mov_b32 m0, s63\n"
    "v_exp_f32_e32 v203, v203\n"
    "v_mfma_f32_16x16x32_bf16 v[104:107], a[176:179], v[148:151], v[104:107]\n"
    "s_add_u32 s48, s48, s45\n"
    "ds_write_addtid_b32 v205 offset:1280\n"
    "v_cvt_pk_bf16_f32 v198, v200, v201\n"
    "v_mfma_f32_16x16x32_bf16 v[100:103], a[176:179], v[140:143], v[100:103]\n"
    "s_add_u32 s28, s48, s46\n"
    "v_exp_f32_e32 v208, v208\n"
    "v_mfma_f32_16x16x32_bf16 v[100:103], a[180:183], v[144:147], v[100:103]\n"
    "s_waitcnt lgkmcnt(5)\n"
    "ds_write_addtid_b32 v212 offset:2048\n"
    "v_mfma_f32_16x16x32_bf16 v[120:123], a[184:187], v[140:143], v[120:123]\n"
    "s_add_u32 m0, s43, 16384\n"
    "v_exp_f32_e32 v209, v209\n"
    "v_mfma_f32_16x16x32_bf16 v[120:123], a[188:191], v[144:147], v[120:123]\n"
    "buffer_load_dwordx4 v248, s[8:11], s40 offen offset:2048 sc1 lds\n"
    "v_mfma_f32_16x16x32_bf16 v[124:127], a[188:191], v[152:155], v[124:127]\n"
    "s_mov_b32 m0, s63\n"
    "ds_read_b128 a[172:175], v241 offset:4096\n"
    "v_mfma_f32_16x16x32_bf16 v[124:127], a[184:187], v[148:151], v[124:127]\n"
    "s_add_u32 s28, s28, 16384\n"
    "ds_read_b128 a[176:179], v242 offset:0\n"
    "v_cvt_pk_bf16_f32 v199, v202, v203\n"
    "v_mfma_f32_16x16x32_bf16 v[128:131], a[184:187], v[156:159], v[128:131]\n"
    "s_add_u32 s88, s48, s61\n"
    "v_exp_f32_e32 v210, v210\n"
    "v_mfma_f32_16x16x32_bf16 v[128:131], a[188:191], v[160:163], v[128:131]\n"
    "s_add_u32 m0, s43, 16384\n"
    "v_exp_f32_e32 v211, v211\n"
    "v_mfma_f32_16x16x32_bf16 v[132:135], a[188:191], v[168:171], v[132:135]\n"
    "buffer_load_dwordx4 v248, s[8:11], s40 offen offset:3072 sc1 lds\n"
    "v_mfma_f32_16x16x32_bf16 v[132:135], a[184:187], v[164:167], v[132:135]\n"
    "s_mov_b32 m0, s63\n"
    "ds_read_b128 a[180:183], v243 offset:0\n"
    "v_mfma_f32_16x16x32_bf16 v[136:139], a[184:187], v[172:175], v[136:139]\n"
    "s_add_u32 s88, s88, 256\n"
    "ds_write_addtid_b32 v213 offset:2304\n"
    "v_mfma_f32_16x16x32_bf16 v[136:139], a[188:191], v[176:179], v[136:139]\n"
    "s_waitcnt lgkmcnt(7)\n"
    "v_cvt_pk_bf16_f32 v206, v208, v209\n"
    "v_mfma_f32_16x16x32_bf16 v[172:175], a[160:163], a[128:131], v[236:239]\n"
    "s_add_u32 m0, s43, 20480\n"
    "v_exp_f32_e32 v216, v216\n"
    "v_mfma_f32_16x16x32_bf16 v[172:175], a[164:167], a[132:135], v[172:175]\n"
    "buffer_load_dwordx4 v248, s[8:11], s41 offen offset:0 sc1 lds\n"
    "v_mfma_f32_16x16x32_bf16 v[164:167], a[164:167], a[100:103], v[232:235]\n"
    "s_mov_b32 m0, s63\n"
    "v_exp_f32_e32 v217, v217\n"
    "v_mfma_f32_16x16x32_bf16 v[164:167], a[160:163], a[96:99], v[164:167]\n"
    "s_add_u32 s48, s32, 1\n"
    "ds_write_addtid_b32 v198 offset:512\n"
    "v_mfma_f32_16x16x32_bf16 v[156:159], a[160:163], a[64:67], v[228:231]\n"
    "s_and_b32 s48, s48, 3\n"
    "ds_read_b128 a[184:187], v242 offset:4096\n"
    "v_mfma_f32_16x16x32_bf16 v[156:159], a[164:167], a[68:71], v[156:159]\n"
    "s_waitcnt lgkmcnt(5)\n"
    "ds_write_addtid_b32 v199 offset:768\n"
    "v_mfma_f32_16x16x32_bf16 v[156:159], a[168:171], a[72:75], v[156:159]\n"
    "s_add_u32 m0, s43, 20480\n"
    "ds_read_b128 a[188:191], v243 offset:4096\n"
    "v_cvt_pk_bf16_f32 v207, v210, v211\n"
    "v_mfma_f32_16x16x32_bf16 v[156:159], a[172:175], a[76:79], v[156:159]\n"
    "buffer_load_dwordx4 v248, s[8:11], s41 offen offset:1024 sc1 lds\n"
    "v_mfma_f32_16x16x32_bf16 v[164:167], a[172:175], a[108:111], v[164:167]\n"
    "s_mov_b32 m0, s63\n"
    "v_exp_f32_e32 v218, v218\n"
    "v_mfma_f32_16x16x32_bf16 v[164:167], a[168:171], a[104:107], v[164:167]\n"
    "s_mul_i32 s48, s48, 101376\n"
    "v_exp_f32_e32 v219, v219\n"
    "v_mfma_f32_16x16x32_bf16 v[172:175], a[168:171], a[136:139], v[172:175]\n"
    "s_add_u32 s48, s48, s45\n"
    "ds_write_addtid_b32 v206 offset:1536\n"
    "v_cvt_pk_bf16_f32 v214, v216, v217\n"
    "v_mfma_f32_16x16x32_bf16 v[172:175], a[172:175], a[140:143], v[172:175]\n"
    "s_waitcnt lgkmcnt(6)\n"
    "ds_read_b128 a[160:163], v240 offset:2048\n"
    "v_cvt_pk_bf16_f32 v215, v218, v219\n"
    "v_mfma_f32_16x16x32_bf16 v[172:175], a[176:179], a[144:147], v[172:175]\n"
    "s_add_u32 m0, s43, 20480\n"
    "ds_read_b128 a[168:171], v240 offset:6144\n"
    "v_mfma_f32_16x16x32_bf16 v[172:175], a[180:183], a[148:151], v[172:175]\n"
    "buffer_load_dwordx4 v248, s[8:11], s41 offen offset:2048 sc1 lds\n"
    "v_mfma_f32_16x16x32_bf16 v[164:167], a[180:183], a[116:119], v[164:167]\n"
    "s_mov_b32 m0, s63\n"
    "ds_read_b128 a[164:167], v241 offset:2048\n"
    "v_mfma_f32_16x16x32_bf16 v[164:167], a[176:179], a[112:115], v[164:167]\n"
    "s_add_u32 s82, s48, s46\n"
    "ds_write_addtid_b32 v207 offset:1792\n"
    "v_mfma_f32_16x16x32_bf16 v[156:159], a[176:179], a[80:83], v[156:159]\n"
    "s_add_u32 s82, s82, 24576\n"
    "ds_write_addtid_b32 v214 offset:2560\n"
    "v_mfma_f32_16x16x32_bf16 v[156:159], a[180:183], a[84:87], v[156:159]\n"
    "s_waitcnt lgkmcnt(6)\n"
    "ds_write_addtid_b32 v215 offset:2816\n"
    "v_mfma_f32_16x16x32_bf16 v[156:159], a[184:187], a[88:91], v[156:159]\n"
    "s_add_u32 m0, s43, 20480\n"
    "ds_read_b128 a[176:179], v242 offset:2048\n"
    "v_mfma_f32_16x16x32_bf16 v[156:159], a[188:191], a[92:95], v[156:159]\n"
    "buffer_load_dwordx4 v248, s[8:11], s41 offen offset:3072 sc1 lds\n"
    "v_mfma_f32_16x16x32_bf16 v[164:167], a[188:191], a[124:127], v[164:167]\n"
    "s_mov_b32 m0, s63\n"
    "ds_read_b128 a[180:183], v243 offset:2048\n"
    "v_mfma_f32_16x16x32_bf16 v[164:167], a[184:187], a[120:123], v[164:167]\n"
    "s_add_u32 s87, s48, s61\n"
    "v_exp_f32_e32 v156, v156\n"
    "v_mfma_f32_16x16x32_bf16 v[172:175], a[184:187], a[152:155], v[172:175]\n"
    "s_add_u32 s87, s87, 384\n"
    "ds_read_b128 a[172:175], v241 offset:6144\n"
    "v_mfma_f32_16x16x32_bf16 v[172:175], a[188:191], a[156:159], v[172:175]\n"
    "s_waitcnt lgkmcnt(6)\n"
    "ds_read_b128 a[184:187], v242 offset:6144\n"
    "ds_read_b128 a[188:191], v243 offset:6144\n"
    "v_mfma_f32_16x16x32_bf16 v[176:179], a[160:163], a[128:131], v[236:239]\n"
    "s_mul_i32 s48, s32, 101376\n"
    "v_exp_f32_e32 v157, v157\n"
    "v_mfma_f32_16x16x32_bf16 v[176:179], a[164:167], a[132:135], v[176:179]\n"
    "s_add_u32 s48, s48, s45\n"
    "v_exp_f32_e32 v158, v158\n"
    "v_mfma_f32_16x16x32_bf16 v[168:171], a[164:167], a[100:103], v[232:235]\n"
    "s_add_u32 s59, s48, s46\n"
    "v_exp_f32_e32 v159, v159\n"
    "v_mfma_f32_16x16x32_bf16 v[168:171], a[160:163], a[96:99], v[168:171]\n"
    "s_waitcnt lgkmcnt(5)\n"
    "s_waitcnt vmcnt(14)\n"
    "v_mfma_f32_16x16x32_bf16 v[160:163], a[160:163], a[64:67], v[228:231]\n"
    "s_barrier\n"
    "v_mfma_f32_16x16x32_bf16 v[160:163], a[164:167], a[68:71], v[160:163]\n"
    "s_waitcnt lgkmcnt(2)\n"
    "ds_read2st64_b32 a[196:197], v249 offset0:0 offset1:1\n"
    "ds_read2st64_b32 a[198:199], v249 offset0:2 offset1:3\n"
    "v_mfma_f32_16x16x32_bf16 v[160:163], a[168:171], a[72:75], v[160:163]\n"
    "s_add_u32 s60, s48, s61\n"
    "ds_read_b128 a[160:163], v240 offset:8192\n"
    "v_cvt_pk_bf16_f32 v156, v156, v157\n"
    "v_mfma_f32_16x16x32_bf16 v[160:163], a[172:175], a[76:79], v[160:163]\n"
    "s_and_b32 s48, s32, 1\n"
    "ds_read_b128 a[164:167], v241 offset:8192\n"
    "v_mfma_f32_16x16x32_bf16 v[168:171], a[172:175], a[108:111], v[168:171]\n"
    "s_xor_b32 s48, s48, s50\n"
    "v_exp_f32_e32 v164, v164\n"
    "v_mfma_f32_16x16x32_bf16 v[168:171], a[168:171], a[104:107], v[168:171]\n"
    "s_sub_u32 s61, s32, s48\n"
    "v_exp_f32_e32 v165, v165\n"
    "v_mfma_f32_16x16x32_bf16 v[176:179], a[168:171], a[136:139], v[176:179]\n"
    "s_and_b32 s61, s61, 3\n"
    "ds_read2st64_b32 a[192:193], v252 offset0:0 offset1:1\n"
    "v_cvt_pk_bf16_f32 v157, v158, v159\n"
    "v_mfma_f32_16x16x32_bf16 v[176:179], a[172:175], a[140:143], v[176:179]\n"
    "v_exp_f32_e32 v166, v166\n"
    "v_mfma_f32_16x16x32_bf16 v[176:179], a[176:179], a[144:147], v[176:179]\n"
    "s_add_u32 s61, s61, 8\n"
    "ds_read_b128 a[168:171], v240 offset:12288\n"
    "ds_read2st64_b32 a[194:195], v252 offset0:2 offset1:3\n"
    "v_mfma_f32_16x16x32_bf16 v[176:179], a[180:183], a[148:151], v[176:179]\n"
    "s_mul_i32 s48, s48, 101376\n"
    "v_exp_f32_e32 v167, v167\n"
    "v_mfma_f32_16x16x32_bf16 v[168:171], a[180:183], a[116:119], v[168:171]\n"
    "s_add_u32 s48, s48, s45\n"
    "ds_read_b128 a[172:175], v241 offset:12288\n"
    "v_cvt_pk_bf16_f32 v164, v164, v165\n"
    "v_mfma_f32_16x16x32_bf16 v[168:171], a[176:179], a[112:115], v[168:171]\n"
    "s_lshl_b32 s80, s61, 13\n"
    "v_exp_f32_e32 v172, v172\n"
    "v_mfma_f32_16x16x32_bf16 v[160:163], a[176:179], a[80:83], v[160:163]\n"
    "s_add_u32 s80, s80, s48\n"
    "v_exp_f32_e32 v173, v173\n"
    "v_mfma_f32_16x16x32_bf16 v[160:163], a[180:183], a[84:87], v[160:163]\n"
    "s_waitcnt lgkmcnt(8)\n"
    "ds_read_b128 a[176:179], v242 offset:8192\n"
    "v_cvt_pk_bf16_f32 v165, v166, v167\n"
    "v_mfma_f32_16x16x32_bf16 v[160:163], a[184:187], a[88:91], v[160:163]\n"
    "s_lshl_b32 s46, s51, 12\n"
    "ds_read2st64_b32 a[204:205], v249 offset0:4 offset1:5\n"
    "v_mfma_f32_16x16x32_bf16 v[160:163], a[188:191], a[92:95], v[160:163]\n"
    "s_add_u32 s80, s80, s46\n"
    "v_exp_f32_e32 v174, v174\n"
    "v_mfma_f32_16x16x32_bf16 v[168:171], a[188:191], a[124:127], v[168:171]\n"
    "s_lshl_b32 s86, s61, 7\n"
    "ds_read_b128 a[180:183], v243 offset:8192\n"
    "ds_read2st64_b32 a[206:207], v249 offset0:6 offset1:7\n"
    "v_mfma_f32_16x16x32_bf16 v[168:171], a[184:187], a[120:123], v[168:171]\n"
    "s_add_u32 s86, s86, s48\n"
    "v_exp_f32_e32 v175, v175\n"
    "v_mfma_f32_16x16x32_bf16 v[176:179], a[184:187], a[152:155], v[176:179]\n"
    "s_add_u32 s86, s86, s47\n"
    "ds_write_addtid_b32 v156 offset:3072\n"
    "v_cvt_pk_bf16_f32 v172, v172, v173\n"
    "v_mfma_f32_16x16x32_bf16 v[176:179], a[188:191], a[156:159], v[176:179]\n"
    "s_waitcnt lgkmcnt(9)\n"
    "ds_write_addtid_b32 v157 offset:3328\n"
    "ds_write_addtid_b32 v164 offset:4096\n"
    "v_mfma_f32_16x16x32_bf16 v[180:183], a[160:163], a[128:131], v[236:239]\n"
    "s_lshl_b32 s46, s93, 7\n"
    "v_exp_f32_e32 v160, v160\n"
    "v_mfma_f32_16x16x32_bf16 v[180:183], a[164:167], a[132:135], v[180:183]\n"
    "s_lshl_b32 s55, s93, 15\n"
    "ds_read_b128 a[184:187], v242 offset:12288\n"
    "ds_read_b128 a[188:191], v243 offset:12288\n"
    "v_mfma_f32_16x16x32_bf16 v[148:151], a[164:167], a[100:103], v[232:235]\n"
    "s_sub_u32 s54, s55, 24576\n"
    "v_exp_f32_e32 v161, v161\n"
    "v_mfma_f32_16x16x32_bf16 v[148:151], a[160:163], a[96:99], v[148:151]\n"
    "s_add_u32 s55, s55, 32768\n"
    "ds_read2st64_b32 a[200:201], v252 offset0:4 offset1:5\n"
    "v_cvt_pk_bf16_f32 v173, v174, v175\n"
    "v_mfma_f32_16x16x32_bf16 v[140:143], a[160:163], a[64:67], v[228:231]\n"
    "s_lshl2_add_u32 s38, s34, s93\n"
    "ds_write_addtid_b32 v165 offset:4352\n"
    "v_exp_f32_e32 v162, v162\n"
    "v_mfma_f32_16x16x32_bf16 v[140:143], a[164:167], a[68:71], v[140:143]\n"
    "s_waitcnt lgkmcnt(11)\n"
    "v_exp_f32_e32 v163, v163\n"
    "v_mfma_f32_16x16x32_bf16 v[140:143], a[168:171], a[72:75], v[140:143]\n"
    "s_lshl1_add_u32 s38, s38, s49\n"
    "ds_read2st64_b32 a[202:203], v252 offset0:6 offset1:7\n"
    "v_cvt_pk_bf16_f32 v158, v160, v161\n"
    "v_mfma_f32_16x16x32_bf16 v[140:143], a[172:175], a[76:79], v[140:143]\n"
    ".long 0xbedf00ff\n"
    ".Lat_w10:\n"
    "s_setpc_b64 s[2:3]\n"
    "s_lshl_b32 s38, s38, 7\n"
    "v_exp_f32_e32 v168, v168\n"
    "v_mfma_f32_16x16x32_bf16 v[148:151], a[172:175], a[108:111], v[148:151]\n"
    "s_add_u32 s38, s38, 5648384\n"
    "v_exp_f32_e32 v169, v169\n"
    "v_mfma_f32_16x16x32_bf16 v[148:151], a[168:171], a[104:107], v[148:151]\n"
    "s_lshl3_add_u32 s90, s34, s49\n"
    "ds_read_b128 a[160:163], v240 offset:10240\n"
    "v_cvt_pk_bf16_f32 v159, v162, v163\n"
    "v_mfma_f32_16x16x32_bf16 v[180:183], a[168:171], a[136:139], v[180:183]\n"
    "s_lshl2_add_u32 s90, s90, s93\n"
    "ds_read2st64_b32 a[212:213], v249 offset0:8 offset1:9\n"
    "v_exp_f32_e32 v170, v170\n"
    "v_mfma_f32_16x16x32_bf16 v[180:183], a[172:175], a[140:143], v[180:183]\n"
    "s_waitcnt lgkmcnt(11)\n"
    "v_exp_f32_e32 v171, v171\n"
    "v_mfma_f32_16x16x32_bf16 v[180:183], a[176:179], a[144:147], v[180:183]\n"
    "s_lshl2_add_u32 s90, s90, 6041600\n"
    "ds_read_b128 a[164:167], v241 offset:10240\n"
    "ds_read2st64_b32 a[214:215], v249 offset0:10 offset1:11\n"
    "v_mfma_f32_16x16x32_bf16 v[180:183], a[180:183], a[148:151], v[180:183]\n"
    "s_lshl_b32 s45, s51, 15\n"
    "ds_read_b128 a[168:171], v240 offset:14336\n"
    "v_cvt_pk_bf16_f32 v166, v168, v169\n"
    "v_mfma_f32_16x16x32_bf16 v[148:151], a[180:183], a[116:119], v[148:151]\n"
    "s_sub_u32 s45, 24576, s45\n"
    "v_exp_f32_e32 v176, v176\n"
    "v_mfma_f32_16x16x32_bf16 v[148:151], a[176:179], a[112:115], v[148:151]\n"
    "s_add_u32 s47, s46, 6103040\n"
    "v_exp_f32_e32 v177, v177\n"
    "v_mfma_f32_16x16x32_bf16 v[140:143], a[176:179], a[80:83], v[140:143]\n"
    "s_andn2_b32 s48, s38, 0xc00\n"
    "ds_write_addtid_b32 v172 offset:5120\n"
    "v_cvt_pk_bf16_f32 v167, v170, v171\n"
    "v_mfma_f32_16x16x32_bf16 v[140:143], a[180:183], a[84:87], v[140:143]\n"
    "s_waitcnt lgkmcnt(9)\n"
    "ds_read_b128 a[176:179], v242 offset:10240\n"
    "v_exp_f32_e32 v178, v178\n"
    "v_mfma_f32_16x16x32_bf16 v[140:143], a[184:187], a[88:91], v[140:143]\n"
    "s_andn2_b32 s61, s90, 0x18c\n"
    "ds_write_addtid_b32 v173 offset:5376\n"
    "ds_write_addtid_b32 v158 offset:3584\n"
    "v_mfma_f32_16x16x32_bf16 v[140:143], a[188:191], a[92:95], v[140:143]\n"
    "buffer_load_dword v188, v189, s[8:11], s81 offen offset:0 sc1\n"
    "ds_write_addtid_b32 v159 offset:3840\n"
    "v_mfma_f32_16x16x32_bf16 v[148:151], a[188:191], a[124:127], v[148:151]\n"
    "s_add_u32 s31, s46, s35\n"
    "v_exp_f32_e32 v179, v179\n"
    "v_mfma_f32_16x16x32_bf16 v[148:151], a[184:187], a[120:123], v[148:151]\n"
    "s_add_u32 m0, s63, 6144\n"
    "ds_read_b128 a[172:175], v241 offset:14336\n"
    "v_exp_f32_e32 v140, v140\n"
    "v_mfma_f32_16x16x32_bf16 v[180:183], a[184:187], a[152:155], v[180:183]\n"
    "buffer_load_dwordx4 v251, s[12:15], s31 offen offset:0 lds\n"
    "ds_read2st64_b32 a[208:209], v252 offset0:8 offset1:9\n"
    "v_mfma_f32_16x16x32_bf16 v[180:183], a[188:191], a[156:159], v[180:183]\n"
    "s_waitcnt lgkmcnt(9)\n"
    "v_exp_f32_e32 v141, v141\n"
    "v_mfma_f32_16x16x32_bf16 v[184:187], a[160:163], a[128:131], v[236:239]\n"
    "s_mov_b32 m0, s63\n"
    "v_exp_f32_e32 v142, v142\n"
    "v_mfma_f32_16x16x32_bf16 v[184:187], a[164:167], a[132:135], v[184:187]\n"
    "ds_read_b128 a[184:187], v242 offset:14336\n"
    "v_exp_f32_e32 v143, v143\n"
    "v_mfma_f32_16x16x32_bf16 v[152:155], a[164:167], a[100:103], v[232:235]\n"
    "s_add_u32 s31, s31, 0x1000\n"
    "ds_read_b128 a[180:183], v243 offset:10240\n"
    "v_cvt_pk_bf16_f32 v140, v140, v141\n"
    "v_mfma_f32_16x16x32_bf16 v[152:155], a[160:163], a[96:99], v[152:155]\n"
    "s_add_u32 m0, s63, 7168\n"
    "ds_read2st64_b32 a[210:211], v252 offset0:10 offset1:11\n"
    "v_exp_f32_e32 v148, v148\n"
    "v_mfma_f32_16x16x32_bf16 v[144:147], a[160:163], a[64:67], v[228:231]\n"
    "buffer_load_dwordx4 v251, s[12:15], s31 offen offset:0 lds\n"
    "v_mfma_f32_16x16x32_bf16 v[144:147], a[164:167], a[68:71], v[144:147]\n"
    "s_waitcnt lgkmcnt(4)\n"
    "s_waitcnt vmcnt(3)\n"
    "v_mfma_f32_16x16x32_bf16 v[144:147], a[168:171], a[72:75], v[144:147]\n"
    "s_barrier\n"
    "v_mfma_f32_16x16x32_bf16 v[144:147], a[172:175], a[76:79], v[144:147]\n"
    "s_mov_b32 m0, s63\n"
    "v_exp_f32_e32 v149, v149\n"
    "v_mfma_f32_16x16x32_bf16 v[152:155], a[172:175], a[108:111], v[152:155]\n"
    "s_cmp_eq_u32 s93, 1\n"
    "s_cselect_b32 s31, s36, 2.0\n"
    "ds_read_b128 a[188:191], v243 offset:14336\n"
    "v_mfma_f32_16x16x32_bf16 v[152:155], a[168:171], a[104:107], v[152:155]\n"
    "s_cmp_eq_u32 s93, 2\n"
    "s_cselect_b32 s31, s37, s31\n"
    "ds_write_addtid_b32 v166 offset:4608\n"
    "v_mfma_f32_16x16x32_bf16 v[184:187], a[168:171], a[136:139], v[184:187]\n"
    "buffer_load_dword v191, v190, s[12:15], s31 offen offset:0\n"
    "v_mfma_f32_16x16x32_bf16 v[184:187], a[172:175], a[140:143], v[184:187]\n"
    "s_waitcnt lgkmcnt(3)\n"
    "ds_read_b128 a[160:163], v240 offset:16384\n"
    "v_cvt_pk_bf16_f32 v141, v142, v143\n"
    "v_mfma_f32_16x16x32_bf16 v[184:187], a[176:179], a[144:147], v[184:187]\n"
    "s_cmp_eq_u32 s93, 0\n"
    "s_cselect_b32 s31, s35, s31\n"
    "ds_read_b128 a[164:167], v241 offset:16384\n"
    "v_mfma_f32_16x16x32_bf16 v[184:187], a[180:183], a[148:151], v[184:187]\n"
    "s_add_u32 s31, s31, 6103040\n"
    "v_exp_f32_e32 v150, v150\n"
    "v_mfma_f32_16x16x32_bf16 v[152:155], a[180:183], a[116:119], v[152:155]\n"
    "ds_write_addtid_b32 v167 offset:4864\n"
    "v_cvt_pk_bf16_f32 v174, v176, v177\n"
    "v_mfma_f32_16x16x32_bf16 v[152:155], a[176:179], a[112:115], v[152:155]\n"
    "ds_read_b128 a[168:171], v240 offset:20480\n"
    "ds_write_addtid_b32 v140 offset:0\n"
    "v_mfma_f32_16x16x32_bf16 v[144:147], a[176:179], a[80:83], v[144:147]\n"
    "ds_read_b128 a[172:175], v241 offset:20480\n"
    "v_exp_f32_e32 v151, v151\n"
    "v_mfma_f32_16x16x32_bf16 v[144:147], a[180:183], a[84:87], v[144:147]\n"
    "s_waitcnt lgkmcnt(7)\n"
    "ds_write_addtid_b32 v141 offset:256\n"
    "v_cvt_pk_bf16_f32 v148, v148, v149\n"
    "v_mfma_f32_16x16x32_bf16 v[144:147], a[184:187], a[88:91], v[144:147]\n"
    "v_exp_f32_e32 v180, v180\n"
    "v_mfma_f32_16x16x32_bf16 v[144:147], a[188:191], a[92:95], v[144:147]\n"
    "ds_write_addtid_b32 v174 offset:5632\n"
    "v_exp_f32_e32 v181, v181\n"
    "v_mfma_f32_16x16x32_bf16 v[152:155], a[188:191], a[124:127], v[152:155]\n"
    "ds_write_addtid_b32 v148 offset:1024\n"
    "v_cvt_pk_bf16_f32 v149, v150, v151\n"
    "v_mfma_f32_16x16x32_bf16 v[152:155], a[184:187], a[120:123], v[152:155]\n"
    "ds_read_b128 a[176:179], v242 offset:16384\n"
    "ds_write_addtid_b32 v149 offset:1280\n"
    "v_mfma_f32_16x16x32_bf16 v[184:187], a[184:187], a[152:155], v[184:187]\n"
    "v_exp_f32_e32 v182, v182\n"
    "v_mfma_f32_16x16x32_bf16 v[184:187], a[188:191], a[156:159], v[184:187]\n"
    "s_waitcnt lgkmcnt(9)\n"
    "v_cvt_pk_bf16_f32 v180, v180, v181\n"
    "v_exp_f32_e32 v183, v183\n"
    "v_mfma_f32_16x16x32_bf16 v[212:215], a[160:163], a[128:131], v[236:239]\n"
    "ds_read_b128 a[180:183], v243 offset:16384\n"
    "v_exp_f32_e32 v144, v144\n"
    "v_mfma_f32_16x16x32_bf16 v[212:215], a[164:167], a[132:135], v[212:215]\n"
    "ds_read_b128 a[184:187], v242 offset:20480\n"
    "v_cvt_pk_bf16_f32 v175, v178, v179\n"
    "v_mfma_f32_16x16x32_bf16 v[204:207], a[164:167], a[100:103], v[232:235]\n"
    "v_exp_f32_e32 v145, v145\n"
    "v_mfma_f32_16x16x32_bf16 v[204:207], a[160:163], a[96:99], v[204:207]\n"
    "ds_write_addtid_b32 v175 offset:5888\n"
    "v_cvt_pk_bf16_f32 v181, v182, v183\n"
    "v_mfma_f32_16x16x32_bf16 v[196:199], a[160:163], a[64:67], v[228:231]\n"
    "v_exp_f32_e32 v146, v146\n"
    "v_mfma_f32_16x16x32_bf16 v[196:199], a[164:167], a[68:71], v[196:199]\n"
    "s_waitcnt lgkmcnt(8)\n"
    "v_exp_f32_e32 v147, v147\n"
    "v_mfma_f32_16x16x32_bf16 v[196:199], a[168:171], a[72:75], v[196:199]\n"
    "ds_write_addtid_b32 v180 offset:2048\n"
    "v_cvt_pk_bf16_f32 v142, v144, v145\n"
    "v_mfma_f32_16x16x32_bf16 v[196:199], a[172:175], a[76:79], v[196:199]\n"
    "s_waitcnt lgkmcnt(1)\n"
    "v_mfma_f32_16x16x32_bf16 v[204:207], a[172:175], a[108:111], v[204:207]\n"
    "s_barrier\n"
    "v_mfma_f32_16x16x32_bf16 v[204:207], a[168:171], a[104:107], v[204:207]\n"
    "ds_read_b128 a[188:191], v243 offset:20480\n"
    "ds_write_addtid_b32 v181 offset:2304\n"
    "v_mfma_f32_16x16x32_bf16 v[212:215], a[168:171], a[136:139], v[212:215]\n"
    "v_exp_f32_e32 v152, v152\n"
    "v_mfma_f32_16x16x32_bf16 v[212:215], a[172:175], a[140:143], v[212:215]\n"
    "v_exp_f32_e32 v153, v153\n"
    "v_mfma_f32_16x16x32_bf16 v[212:215], a[176:179], a[144:147], v[212:215]\n"
    "ds_read_b128 a[168:171], v240 offset:22528\n"
    "v_cvt_pk_bf16_f32 v143, v146, v147\n"
    "v_mfma_f32_16x16x32_bf16 v[212:215], a[180:183], a[148:151], v[212:215]\n"
    "ds_read_b128 a[160:163], v240 offset:18432\n"
    "ds_read_b128 a[164:167], v241 offset:18432\n"
    "v_mfma_f32_16x16x32_bf16 v[204:207], a[180:183], a[116:119], v[204:207]\n"
    "ds_read_b128 a[172:175], v241 offset:22528\n"
    "v_exp_f32_e32 v154, v154\n"
    "v_mfma_f32_16x16x32_bf16 v[204:207], a[176:179], a[112:115], v[204:207]\n"
    "ds_write_addtid_b32 v142 offset:512\n"
    "v_exp_f32_e32 v155, v155\n"
    "v_mfma_f32_16x16x32_bf16 v[196:199], a[176:179], a[80:83], v[196:199]\n"
    "ds_read2st64_b32 v[160:161], v249 offset0:12 offset1:13\n"
    "v_cvt_pk_bf16_f32 v150, v152, v153\n"
    "v_mfma_f32_16x16x32_bf16 v[196:199], a[180:183], a[84:87], v[196:199]\n"
    "s_waitcnt lgkmcnt(7)\n"
    "ds_read2st64_b32 v[162:163], v249 offset0:14 offset1:15\n"
    "v_exp_f32_e32 v184, v184\n"
    "v_mfma_f32_16x16x32_bf16 v[196:199], a[184:187], a[88:91], v[196:199]\n"
    "ds_read_b128 a[176:179], v242 offset:18432\n"
    "v_exp_f32_e32 v185, v185\n"
    "v_mfma_f32_16x16x32_bf16 v[196:199], a[188:191], a[92:95], v[196:199]\n"
    "ds_read_b128 a[180:183], v243 offset:18432\n"
    "v_cvt_pk_bf16_f32 v151, v154, v155\n"
    "v_mfma_f32_16x16x32_bf16 v[204:207], a[188:191], a[124:127], v[204:207]\n"
    "v_exp_f32_e32 v186, v186\n"
    "v_mfma_f32_16x16x32_bf16 v[204:207], a[184:187], a[120:123], v[204:207]\n"
    "s_waitcnt vmcnt(3)\n"
    "ds_write_addtid_b32 v143 offset:768\n"
    "v_cmp_ne_u32_e32 vcc, s39, v188\n"
    "v_mfma_f32_16x16x32_bf16 v[212:215], a[184:187], a[152:155], v[212:215]\n"
    "ds_read2st64_b32 v[168:169], v249 offset0:16 offset1:17\n"
    "v_exp_f32_e32 v196, v196\n"
    "v_mfma_f32_16x16x32_bf16 v[212:215], a[188:191], a[156:159], v[212:215]\n"
    "s_waitcnt lgkmcnt(8)\n"
    "ds_read2st64_b32 v[170:171], v249 offset0:18 offset1:19\n"
    "v_exp_f32_e32 v197, v197\n"
    "v_mfma_f32_16x16x32_bf16 v[216:219], a[160:163], a[128:131], v[236:239]\n"
    "s_cmp_eq_u64 vcc, 0\n"
    "s_cselect_b32 s31, s31, 2.0\n"
    "s_cselect_b32 s44, 0, 1\n"
    "ds_write_addtid_b32 v150 offset:1536\n"
    "v_mfma_f32_16x16x32_bf16 v[216:219], a[164:167], a[132:135], v[216:219]\n"
    "buffer_load_dword v191, v190, s[8:11], s31 offen offset:0 sc1\n"
    "ds_read_b128 a[184:187], v242 offset:22528\n"
    "v_mfma_f32_16x16x32_bf16 v[208:211], a[164:167], a[100:103], v[232:235]\n"
    "v_exp_f32_e32 v198, v198\n"
    "v_mfma_f32_16x16x32_bf16 v[208:211], a[160:163], a[96:99], v[208:211]\n"
    "ds_write_addtid_b32 v151 offset:1792\n"
    "v_exp_f32_e32 v187, v187\n"
    "v_mfma_f32_16x16x32_bf16 v[200:203], a[160:163], a[64:67], v[228:231]\n"
    "ds_read_b128 a[188:191], v243 offset:22528\n"
    "v_cvt_pk_bf16_f32 v182, v184, v185\n"
    "v_mfma_f32_16x16x32_bf16 v[200:203], a[164:167], a[68:71], v[200:203]\n"
    "s_waitcnt lgkmcnt(12)\n"
    "v_cvt_pk_bf16_f32 v196, v196, v197\n"
    "v_exp_f32_e32 v199, v199\n"
    "v_mfma_f32_16x16x32_bf16 v[200:203], a[168:171], a[72:75], v[200:203]\n"
    "v_exp_f32_e32 v204, v204\n"
    "v_mfma_f32_16x16x32_bf16 v[200:203], a[172:175], a[76:79], v[200:203]\n"
    "ds_read2st64_b32 v[176:177], v249 offset0:20 offset1:21\n"
    "v_exp_f32_e32 v205, v205\n"
    "v_mfma_f32_16x16x32_bf16 v[208:211], a[172:175], a[108:111], v[208:211]\n"
    "ds_read2st64_b32 v[178:179], v249 offset0:22 offset1:23\n"
    "v_and_b32_e32 v248, 0xf0, v250\n"
    "v_lshrrev_b32_e32 v231, 3, v248\n"
    "v_mfma_f32_16x16x32_bf16 v[208:211], a[168:171], a[104:107], v[208:211]\n"
    "ds_read_b64_tr_b16 a[160:161], v244 offset:24576\n"
    "v_cvt_pk_bf16_f32 v197, v198, v199\n"
    "v_mfma_f32_16x16x32_bf16 v[216:219], a[168:171], a[136:139], v[216:219]\n"
    "s_lshr_b32 s31, s35, 8\n"
    "ds_read_b64_tr_b16 a[162:163], v244 offset:26624\n"
    "v_exp_f32_e32 v206, v206\n"
    "v_mfma_f32_16x16x32_bf16 v[216:219], a[172:175], a[140:143], v[216:219]\n"
    "s_waitcnt lgkmcnt(11)\n"
    "ds_read_b64_tr_b16 a[168:169], v245 offset:24576\n"
    "v_cvt_pk_bf16_f32 v183, v186, v187\n"
    "v_mfma_f32_16x16x32_bf16 v[216:219], a[176:179], a[144:147], v[216:219]\n"
    "buffer_load_ushort v228, v231, s[24:27], s31 offen\n"
    "ds_write_addtid_b32 v182 offset:2560\n"
    "v_mfma_f32_16x16x32_bf16 v[216:219], a[180:183], a[148:151], v[216:219]\n"
    "s_lshr_b32 s31, s36, 8\n"
    "ds_read_b64_tr_b16 a[164:165], v244 offset:57344\n"
    "v_exp_f32_e32 v207, v207\n"
    "v_mfma_f32_16x16x32_bf16 v[208:211], a[180:183], a[116:119], v[208:211]\n"
    "ds_read_b64_tr_b16 a[170:171], v245 offset:26624\n"
    "v_cvt_pk_bf16_f32 v204, v204, v205\n"
    "v_mfma_f32_16x16x32_bf16 v[208:211], a[176:179], a[112:115], v[208:211]\n"
    "ds_read_b64_tr_b16 a[172:173], v245 offset:57344\n"
    "v_exp_f32_e32 v212, v212\n"
    "v_mfma_f32_16x16x32_bf16 v[200:203], a[176:179], a[80:83], v[200:203]\n"
    "buffer_load_ushort v229, v231, s[24:27], s31 offen\n"
    "ds_read_b64_tr_b16 a[166:167], v244 offset:59392\n"
    "v_mfma_f32_16x16x32_bf16 v[200:203], a[180:183], a[84:87], v[200:203]\n"
    "s_waitcnt lgkmcnt(10)\n"
    "v_cvt_pk_bf16_f32 v205, v206, v207\n"
    "v_exp_f32_e32 v213, v213\n"
    "v_mfma_f32_16x16x32_bf16 v[200:203], a[184:187], a[88:91], v[200:203]\n"
    "s_lshr_b32 s31, s37, 8\n"
    "ds_write_addtid_b32 v183 offset:2816\n"
    "v_exp_f32_e32 v214, v214\n"
    "v_mfma_f32_16x16x32_bf16 v[200:203], a[188:191], a[92:95], v[200:203]\n"
    "ds_read_b64_tr_b16 a[174:175], v245 offset:59392\n"
    "v_exp_f32_e32 v215, v215\n"
    "v_mfma_f32_16x16x32_bf16 v[208:211], a[188:191], a[124:127], v[208:211]\n"
    "buffer_load_ushort v230, v231, s[24:27], s31 offen\n"
    "ds_read_b64_tr_b16 a[176:177], v246 offset:24576\n"
    "v_mfma_f32_16x16x32_bf16 v[208:211], a[184:187], a[120:123], v[208:211]\n"
    "s_waitcnt lgkmcnt(2)\n"
    "v_mfma_f32_16x16x32_bf16 v[216:219], a[184:187], a[152:155], v[216:219]\n"
    "s_barrier\n"
    "v_mfma_f32_16x16x32_bf16 v[216:219], a[188:191], a[156:159], v[216:219]\n"
    "ds_write_addtid_b32 v196 offset:3072\n"
    "v_cvt_pk_bf16_f32 v212, v212, v213\n"
    "v_mfma_f32_16x16x32_bf16 a[252:255], a[160:163], a[208:211], a[252:255]\n"
    "s_add_u32 s62, s57, 4096\n"
    "ds_write_addtid_b32 v197 offset:3328\n"
    "v_exp_f32_e32 v200, v200\n"
    "v_mfma_f32_16x16x32_bf16 a[252:255], a[164:167], a[212:215], a[252:255]\n"
    "buffer_load_dwordx4 a[64:67], v250, s[8:11], s62 offen offset:2048 sc1\n"
    "ds_read_b64_tr_b16 a[184:185], v246 offset:0\n"
    "v_mfma_f32_16x16x32_bf16 a[248:251], a[164:167], a[204:207], a[248:251]\n"
    "ds_write_addtid_b32 v204 offset:4096\n"
    "v_lshrrev_b32_e32 v231, 2, v250\n"
    "v_mfma_f32_16x16x32_bf16 a[248:251], a[160:163], a[200:203], a[248:251]\n"
    "ds_read_b64_tr_b16 a[178:179], v246 offset:26624\n"
    "ds_read_b64_tr_b16 a[186:187], v246 offset:2048\n"
    "v_mfma_f32_16x16x32_bf16 a[244:247], a[160:163], a[192:195], a[244:247]\n"
    "ds_read_b64_tr_b16 a[180:181], v246 offset:57344\n"
    "ds_read_b64_tr_b16 a[182:183], v246 offset:59392\n"
    "v_mfma_f32_16x16x32_bf16 a[244:247], a[164:167], a[196:199], a[244:247]\n"
    "s_waitcnt lgkmcnt(9)\n"
    "ds_write_addtid_b32 v205 offset:4352\n"
    "v_exp_f32_e32 v201, v201\n"
    "v_mfma_f32_16x16x32_bf16 v[8:11], a[168:171], a[192:195], v[8:11]\n"
    "buffer_load_dwordx4 a[68:71], v250, s[8:11], s62 offen offset:3072 sc1\n"
    "ds_write_addtid_b32 v212 offset:5120\n"
    "v_mfma_f32_16x16x32_bf16 v[8:11], a[172:175], a[196:199], v[8:11]\n"
    "s_add_u32 s62, s57, 8192\n"
    "ds_read_b64_tr_b16 a[160:161], v244 offset:0\n"
    "v_cvt_pk_bf16_f32 v213, v214, v215\n"
    "v_mfma_f32_16x16x32_bf16 v[12:15], a[172:175], a[204:207], v[12:15]\n"
    "ds_read_b64_tr_b16 a[188:189], v246 offset:32768\n"
    "v_exp_f32_e32 v202, v202\n"
    "v_mfma_f32_16x16x32_bf16 v[12:15], a[168:171], a[200:203], v[12:15]\n"
    "ds_write_addtid_b32 v213 offset:5376\n"
    "v_exp_f32_e32 v203, v203\n"
    "v_mfma_f32_16x16x32_bf16 v[16:19], a[168:171], a[208:211], v[16:19]\n"
    "ds_read_b64_tr_b16 a[190:191], v246 offset:34816\n"
    "v_cvt_pk_bf16_f32 v198, v200, v201\n"
    "v_mfma_f32_16x16x32_bf16 v[16:19], a[172:175], a[212:215], v[16:19]\n"
    "s_waitcnt lgkmcnt(6)\n"
    "ds_read_b64_tr_b16 a[168:169], v245 offset:0\n"
    "v_exp_f32_e32 v208, v208\n"
    "v_mfma_f32_16x16x32_bf16 v[36:39], a[176:179], a[208:211], v[36:39]\n"
    "buffer_load_dwordx4 a[72:75], v250, s[8:11], s62 offen offset:0 sc1\n"
    "ds_write_addtid_b32 v198 offset:3584\n"
    "v_mfma_f32_16x16x32_bf16 v[36:39], a[180:183], a[212:215], v[36:39]\n"
    "ds_read2st64_b32 v[144:145], v249 offset0:0 offset1:1\n"
    "v_exp_f32_e32 v209, v209\n"
    "v_mfma_f32_16x16x32_bf16 v[32:35], a[180:183], a[204:207], v[32:35]\n"
    "ds_read2st64_b32 v[146:147], v249 offset0:2 offset1:3\n"
    "ds_read2st64_b32 v[152:153], v249 offset0:4 offset1:5\n"
    "v_cvt_pk_bf16_f32 v199, v202, v203\n"
    "v_mfma_f32_16x16x32_bf16 v[32:35], a[176:179], a[200:203], v[32:35]\n"
    "ds_read_b64_tr_b16 a[162:163], v244 offset:2048\n"
    "ds_read_b64_tr_b16 a[164:165], v244 offset:32768\n"
    "v_mfma_f32_16x16x32_bf16 v[28:31], a[176:179], a[192:195], v[28:31]\n"
    "ds_read_b64_tr_b16 a[166:167], v244 offset:34816\n"
    "v_exp_f32_e32 v210, v210\n"
    "v_mfma_f32_16x16x32_bf16 v[28:31], a[180:183], a[196:199], v[28:31]\n"
    "s_waitcnt lgkmcnt(8)\n"
    "ds_read_b64_tr_b16 a[170:171], v245 offset:2048\n"
    "v_exp_f32_e32 v211, v211\n"
    "v_mfma_f32_16x16x32_bf16 v[28:31], a[184:187], v[156:159], v[28:31]\n"
    "buffer_load_dwordx4 a[76:79], v250, s[8:11], s62 offen offset:1024 sc1\n"
    "ds_read2st64_b32 v[154:155], v249 offset0:6 offset1:7\n"
    "v_mfma_f32_16x16x32_bf16 v[28:31], a[188:191], v[160:163], v[28:31]\n"
    "ds_read_b64_tr_b16 a[172:173], v245 offset:32768\n"
    "ds_read2st64_b32 v[184:185], v249 offset0:8 offset1:9\n"
    "v_mfma_f32_16x16x32_bf16 v[32:35], a[188:191], v[168:171], v[32:35]\n"
    "ds_read2st64_b32 v[186:187], v249 offset0:10 offset1:11\n"
    "v_cvt_pk_bf16_f32 v206, v208, v209\n"
    "v_mfma_f32_16x16x32_bf16 v[32:35], a[184:187], v[164:167], v[32:35]\n"
    "ds_read_b64_tr_b16 a[174:175], v245 offset:34816\n"
    "v_exp_f32_e32 v216, v216\n"
    "v_mfma_f32_16x16x32_bf16 v[36:39], a[184:187], v[172:175], v[36:39]\n"
    "ds_write_addtid_b32 v199 offset:3840\n"
    "v_exp_f32_e32 v217, v217\n"
    "v_mfma_f32_16x16x32_bf16 v[36:39], a[188:191], v[176:179], v[36:39]\n"
    "s_waitcnt lgkmcnt(7)\n"
    "ds_read_b64_tr_b16 a[176:177], v245 offset:8192\n"
    "ds_read_b64_tr_b16 a[178:179], v245 offset:10240\n"
    "v_cvt_pk_bf16_f32 v207, v210, v211\n"
    "v_mfma_f32_16x16x32_bf16 a[252:255], a[160:163], v[172:175], a[252:255]\n"
    "buffer_load_dwordx4 a[80:83], v250, s[8:11], s62 offen offset:2048 sc1\n"
    "ds_write_addtid_b32 v206 offset:4608\n"
    "v_mfma_f32_16x16x32_bf16 a[252:255], a[164:167], v[176:179], a[252:255]\n"
    "ds_read_b64_tr_b16 a[184:185], v244 offset:8192\n"
    "v_exp_f32_e32 v218, v218\n"
    "v_mfma_f32_16x16x32_bf16 a[248:251], a[164:167], v[168:171], a[248:251]\n"
    "ds_read_b64_tr_b16 a[180:181], v245 offset:40960\n"
    "v_exp_f32_e32 v219, v219\n"
    "v_mfma_f32_16x16x32_bf16 a[248:251], a[160:163], v[164:167], a[248:251]\n"
    "ds_write_addtid_b32 v207 offset:4864\n"
    "v_cvt_pk_bf16_f32 v214, v216, v217\n"
    "v_mfma_f32_16x16x32_bf16 a[244:247], a[160:163], v[156:159], a[244:247]\n"
    "ds_read_b64_tr_b16 a[182:183], v245 offset:43008\n"
    "v_cvt_pk_bf16_f32 v215, v218, v219\n"
    "v_mfma_f32_16x16x32_bf16 a[244:247], a[164:167], v[160:163], a[244:247]\n"
    "s_waitcnt lgkmcnt(8)\n"
    "ds_write_addtid_b32 v214 offset:5632\n"
    "v_subrev_u32_e32 v231, 0x80, v231\n"
    "v_mfma_f32_16x16x32_bf16 v[8:11], a[168:171], v[156:159], v[8:11]\n"
    "buffer_load_dwordx4 a[84:87], v250, s[8:11], s62 offen offset:3072 sc1\n"
    "ds_read_b64_tr_b16 a[186:187], v244 offset:10240\n"
    "v_mfma_f32_16x16x32_bf16 v[8:11], a[172:175], v[160:163], v[8:11]\n"
    "s_add_u32 s62, s57, 12288\n"
    "ds_read_b64_tr_b16 a[160:161], v244 offset:16384\n"
    "ds_write_addtid_b32 v215 offset:5888\n"
    "v_mfma_f32_16x16x32_bf16 v[12:15], a[172:175], v[168:171], v[12:15]\n"
    "ds_read_b64_tr_b16 a[162:163], v244 offset:18432\n"
    "ds_read_b64_tr_b16 a[164:165], v244 offset:49152\n"
    "v_mfma_f32_16x16x32_bf16 v[12:15], a[168:171], v[164:167], v[12:15]\n"
    "ds_read_b64_tr_b16 a[188:189], v244 offset:40960\n"
    "ds_read_b64_tr_b16 a[166:167], v244 offset:51200\n"
    "v_mfma_f32_16x16x32_bf16 v[16:19], a[168:171], v[172:175], v[16:19]\n"
    "ds_read_b64_tr_b16 a[190:191], v244 offset:43008\n"
    "v_min_u32_e32 v231, 2.0, v231\n"
    "v_mfma_f32_16x16x32_bf16 v[16:19], a[172:175], v[176:179], v[16:19]\n"
    "s_waitcnt lgkmcnt(9)\n"
    "s_waitcnt lgkmcnt(5)\n"
    "v_mfma_f32_16x16x32_bf16 v[16:19], a[176:179], v[180:183], v[16:19]\n"
    "s_barrier\n"
    "v_mfma_f32_16x16x32_bf16 v[16:19], a[180:183], v[184:187], v[16:19]\n"
    "buffer_load_dwordx4 a[88:91], v250, s[8:11], s62 offen offset:0 sc1\n"
    "ds_read2st64_b32 v[200:201], v249 offset0:12 offset1:13\n"
    "v_mfma_f32_16x16x32_bf16 v[12:15], a[180:183], v[152:155], v[12:15]\n"
    "ds_read2st64_b32 v[202:203], v249 offset0:14 offset1:15\n"
    "v_lshrrev_b32_e32 v252, 4, v250\n"
    "v_mfma_f32_16x16x32_bf16 v[12:15], a[176:179], v[148:151], v[12:15]\n"
    "ds_read2st64_b32 v[208:209], v249 offset0:16 offset1:17\n"
    "v_lshlrev_b32_e32 v239, 5, v248\n"
    "v_mfma_f32_16x16x32_bf16 v[8:11], a[176:179], v[140:143], v[8:11]\n"
    "ds_read_b64_tr_b16 a[168:169], v245 offset:16384\n"
    "ds_read2st64_b32 v[210:211], v249 offset0:18 offset1:19\n"
    "v_mfma_f32_16x16x32_bf16 v[8:11], a[180:183], v[144:147], v[8:11]\n"
    "s_waitcnt lgkmcnt(5)\n"
    "ds_read2st64_b32 v[216:217], v249 offset0:20 offset1:21\n"
    "ds_read2st64_b32 v[218:219], v249 offset0:22 offset1:23\n"
    "v_mfma_f32_16x16x32_bf16 a[244:247], a[184:187], v[140:143], a[244:247]\n"
    "buffer_load_dwordx4 a[92:95], v250, s[8:11], s62 offen offset:1024 sc1\n"
    "v_mfma_f32_16x16x32_bf16 a[244:247], a[188:191], v[144:147], a[244:247]\n"
    "ds_read_b64_tr_b16 a[170:171], v245 offset:18432\n"
    "ds_read_b64_tr_b16 a[172:173], v245 offset:49152\n"
    "v_mfma_f32_16x16x32_bf16 a[248:251], a[188:191], v[152:155], a[248:251]\n"
    "ds_read_b64_tr_b16 a[176:177], v246 offset:8192\n"
    "v_and_or_b32 v239, v252, 48, v239\n"
    "v_mfma_f32_16x16x32_bf16 a[248:251], a[184:187], v[148:151], a[248:251]\n"
    "ds_read_b64_tr_b16 a[174:175], v245 offset:51200\n"
    "v_lshrrev_b32_e32 v248, 2, v248\n"
    "v_mfma_f32_16x16x32_bf16 a[252:255], a[184:187], v[180:183], a[252:255]\n"
    "ds_read_b64_tr_b16 a[178:179], v246 offset:10240\n"
    "v_lshrrev_b32_e32 v253, 4, v252\n"
    "v_mfma_f32_16x16x32_bf16 a[252:255], a[188:191], v[184:187], a[252:255]\n"
    "s_waitcnt lgkmcnt(5)\n"
    "ds_read_b64_tr_b16 a[184:185], v246 offset:16384\n"
    "v_lshl_add_u32 v248, v253, 7, v248\n"
    "v_mfma_f32_16x16x32_bf16 a[252:255], a[160:163], v[212:215], a[252:255]\n"
    "buffer_load_dwordx4 a[96:99], v250, s[8:11], s62 offen offset:2048 sc1\n"
    "v_mfma_f32_16x16x32_bf16 a[252:255], a[164:167], v[216:219], a[252:255]\n"
    "ds_read_b64_tr_b16 a[180:181], v246 offset:40960\n"
    "v_bfe_u32 v255, v252, 2, 2\n"
    "v_mfma_f32_16x16x32_bf16 a[248:251], a[164:167], v[208:211], a[248:251]\n"
    "ds_read_b64_tr_b16 a[186:187], v246 offset:18432\n"
    "v_max_u32_e32 v238, 1, v255\n"
    "v_mfma_f32_16x16x32_bf16 a[248:251], a[160:163], v[204:207], a[248:251]\n"
    "ds_read_b64_tr_b16 a[182:183], v246 offset:43008\n"
    "v_add_u32_e32 v238, s32, v238\n"
    "v_mfma_f32_16x16x32_bf16 a[244:247], a[160:163], v[196:199], a[244:247]\n"
    "v_and_b32_e32 v238, 3, v238\n"
    "v_lshlrev_b32_e32 v238, 10, v238\n"
    "v_mfma_f32_16x16x32_bf16 a[244:247], a[164:167], v[200:203], a[244:247]\n"
    "s_waitcnt lgkmcnt(5)\n"
    "ds_read_b64_tr_b16 a[160:161], v247 offset:24576\n"
    "v_and_b32_e32 v252, 3, v252\n"
    "v_mfma_f32_16x16x32_bf16 v[8:11], a[168:171], v[196:199], v[8:11]\n"
    "buffer_load_dwordx4 a[100:103], v250, s[8:11], s62 offen offset:3072 sc1\n"
    "v_mfma_f32_16x16x32_bf16 v[8:11], a[172:175], v[200:203], v[8:11]\n"
    "s_add_u32 s62, s57, 16384\n"
    "ds_read_b64_tr_b16 a[188:189], v246 offset:49152\n"
    "v_xor_b32_e32 v253, v253, v255\n"
    "v_mfma_f32_16x16x32_bf16 v[12:15], a[172:175], v[208:211], v[12:15]\n"
    "ds_read_b64_tr_b16 a[162:163], v247 offset:26624\n"
    "v_lshl_or_b32 v252, v253, 2, v252\n"
    "v_mfma_f32_16x16x32_bf16 v[12:15], a[168:171], v[204:207], v[12:15]\n"
    "ds_read_b64_tr_b16 a[190:191], v246 offset:51200\n"
    "v_cvt_pk_bf16_f32 v11, v10, v11\n"
    "v_mfma_f32_16x16x32_bf16 v[16:19], a[168:171], v[212:215], v[16:19]\n"
    "v_cvt_pk_bf16_f32 v10, v8, v9\n"
    "v_accvgpr_read_b32 v8, a244\n"
    "v_mfma_f32_16x16x32_bf16 v[16:19], a[172:175], v[216:219], v[16:19]\n"
    "s_waitcnt lgkmcnt(4)\n"
    "v_accvgpr_read_b32 v9, a245\n"
    "v_accvgpr_read_b32 v255, a247\n"
    "v_mfma_f32_16x16x32_bf16 v[36:39], a[176:179], v[180:183], v[36:39]\n"
    "ds_read_b64_tr_b16 a[168:169], v247 offset:0\n"
    "v_cvt_pk_bf16_f32 v8, v8, v9\n"
    "v_mfma_f32_16x16x32_bf16 v[36:39], a[180:183], v[184:187], v[36:39]\n"
    "v_accvgpr_read_b32 v9, a246\n"
    "v_cvt_pk_bf16_f32 v9, v9, v255\n"
    "v_mfma_f32_16x16x32_bf16 v[32:35], a[180:183], v[152:155], v[32:35]\n"
    "buffer_store_dwordx4 v[8:11], v250, s[8:11], s82 offen offset:0 sc1\n"
    "v_mfma_f32_16x16x32_bf16 v[32:35], a[176:179], v[148:151], v[32:35]\n"
    "ds_read_b64_tr_b16 a[164:165], v247 offset:57344\n"
    "ds_read_b64_tr_b16 a[166:167], v247 offset:59392\n"
    "v_mfma_f32_16x16x32_bf16 v[28:31], a[176:179], v[140:143], v[28:31]\n"
    "ds_read_b64_tr_b16 a[170:171], v247 offset:2048\n"
    "v_cvt_pk_bf16_f32 v15, v14, v15\n"
    "v_mfma_f32_16x16x32_bf16 v[28:31], a[180:183], v[144:147], v[28:31]\n"
    "s_waitcnt lgkmcnt(4)\n"
    "ds_read_b64_tr_b16 a[176:177], v247 offset:8192\n"
    "v_mfma_f32_16x16x32_bf16 v[28:31], a[184:187], v[196:199], v[28:31]\n"
    "buffer_load_dwordx4 a[104:107], v250, s[8:11], s62 offen offset:0 sc1\n"
    "v_mfma_f32_16x16x32_bf16 v[28:31], a[188:191], v[200:203], v[28:31]\n"
    "ds_read_b64_tr_b16 a[172:173], v247 offset:32768\n"
    "v_cvt_pk_bf16_f32 v14, v12, v13\n"
    "v_mfma_f32_16x16x32_bf16 v[32:35], a[188:191], v[208:211], v[32:35]\n"
    "ds_read_b64_tr_b16 a[178:179], v247 offset:10240\n"
    "v_accvgpr_read_b32 v12, a248\n"
    "v_mfma_f32_16x16x32_bf16 v[32:35], a[184:187], v[204:207], v[32:35]\n"
    "ds_read_b64_tr_b16 a[174:175], v247 offset:34816\n"
    "v_cvt_pk_bf16_f32 v28, v28, v29\n"
    "v_mfma_f32_16x16x32_bf16 v[36:39], a[184:187], v[212:215], v[36:39]\n"
    "ds_read_b64_tr_b16 a[180:181], v247 offset:40960\n"
    "v_mfma_f32_16x16x32_bf16 v[36:39], a[188:191], v[216:219], v[36:39]\n"
    "s_waitcnt lgkmcnt(6)\n"
    "ds_read_b64_tr_b16 a[182:183], v247 offset:43008\n"
    "v_accvgpr_read_b32 v13, a249\n"
    "v_mfma_f32_16x16x32_bf16 v[56:59], a[160:163], a[208:211], v[56:59]\n"
    "buffer_load_dwordx4 a[108:111], v250, s[8:11], s62 offen offset:1024 sc1\n"
    "v_mfma_f32_16x16x32_bf16 v[56:59], a[164:167], a[212:215], v[56:59]\n"
    "ds_read_b64_tr_b16 a[184:185], v247 offset:16384\n"
    "v_accvgpr_read_b32 v255, a251\n"
    "v_mfma_f32_16x16x32_bf16 v[52:55], a[164:167], a[204:207], v[52:55]\n"
    "v_cvt_pk_bf16_f32 v12, v12, v13\n"
    "v_accvgpr_read_b32 v13, a250\n"
    "v_mfma_f32_16x16x32_bf16 v[52:55], a[160:163], a[200:203], v[52:55]\n"
    "ds_read_b64_tr_b16 a[186:187], v247 offset:18432\n"
    "v_cvt_pk_bf16_f32 v13, v13, v255\n"
    "v_mfma_f32_16x16x32_bf16 v[48:51], a[160:163], a[192:195], v[48:51]\n"
    "buffer_store_dwordx4 v[12:15], v250, s[8:11], s28 offen offset:0 sc1\n"
    "v_mfma_f32_16x16x32_bf16 v[48:51], a[164:167], a[196:199], v[48:51]\n"
    "s_waitcnt lgkmcnt(4)\n"
    "ds_read_b64_tr_b16 a[160:161], v244 offset:28672\n"
    "v_cvt_pk_bf16_f32 v19, v18, v19\n"
    "v_mfma_f32_16x16x32_bf16 v[48:51], a[168:171], v[156:159], v[48:51]\n"
    "s_add_u32 s62, s57, 16384\n"
    "ds_read_b64_tr_b16 a[162:163], v244 offset:30720\n"
    "v_cvt_pk_bf16_f32 v18, v16, v17\n"
    "v_mfma_f32_16x16x32_bf16 v[48:51], a[172:175], v[160:163], v[48:51]\n"
    "buffer_load_dwordx4 a[112:115], v250, s[8:11], s62 offen offset:2048 sc1\n"
    "v_mfma_f32_16x16x32_bf16 v[52:55], a[172:175], v[168:171], v[52:55]\n"
    "ds_read_b64_tr_b16 a[188:189], v247 offset:49152\n"
    "v_accvgpr_read_b32 v16, a252\n"
    "v_mfma_f32_16x16x32_bf16 v[52:55], a[168:171], v[164:167], v[52:55]\n"
    "ds_read_b64_tr_b16 a[164:165], v244 offset:61440\n"
    "v_mfma_f32_16x16x32_bf16 v[56:59], a[168:171], v[172:175], v[56:59]\n"
    "ds_read_b64_tr_b16 a[190:191], v247 offset:51200\n"
    "v_accvgpr_read_b32 v17, a253\n"
    "v_mfma_f32_16x16x32_bf16 v[56:59], a[172:175], v[176:179], v[56:59]\n"
    "s_waitcnt lgkmcnt(7)\n"
    "ds_read_b64_tr_b16 a[166:167], v244 offset:63488\n"
    "v_cvt_pk_bf16_f32 v29, v30, v31\n"
    "v_mfma_f32_16x16x32_bf16 v[56:59], a[176:179], v[180:183], v[56:59]\n"
    "v_mov_b32_e32 v253, 0x3f80\n"
    "v_accvgpr_read_b32 v255, a255\n"
    "v_mfma_f32_16x16x32_bf16 v[56:59], a[180:183], v[184:187], v[56:59]\n"
    "ds_read_b64_tr_b16 a[168:169], v244 offset:4096\n"
    "v_cvt_pk_bf16_f32 v16, v16, v17\n"
    "v_mfma_f32_16x16x32_bf16 v[52:55], a[180:183], v[152:155], v[52:55]\n"
    "ds_read_b64_tr_b16 a[170:171], v244 offset:6144\n"
    "v_cvt_pk_bf16_f32 v32, v32, v33\n"
    "v_mfma_f32_16x16x32_bf16 v[52:55], a[176:179], v[148:151], v[52:55]\n"
    "buffer_load_dwordx4 a[116:119], v250, s[8:11], s62 offen offset:3072 sc1\n"
    "v_mfma_f32_16x16x32_bf16 v[48:51], a[176:179], v[140:143], v[48:51]\n"
    "s_add_u32 s62, s57, 20480\n"
    "v_accvgpr_read_b32 v17, a254\n"
    "v_cvt_pk_bf16_f32 v17, v17, v255\n"
    "v_mfma_f32_16x16x32_bf16 v[48:51], a[180:183], v[144:147], v[48:51]\n"
    "s_waitcnt lgkmcnt(3)\n"
    "ds_read_b64_tr_b16 a[172:173], v244 offset:36864\n"
    "v_mfma_f32_16x16x32_bf16 v[48:51], a[184:187], v[196:199], v[48:51]\n"
    "buffer_store_dwordx4 v[16:19], v250, s[8:11], s29 offen offset:0 sc1\n"
    "v_mfma_f32_16x16x32_bf16 v[48:51], a[188:191], v[200:203], v[48:51]\n"
    "v_mov_b64_e32 v[232:233], 0\n"
    "v_mov_b64_e32 v[236:237], 0\n"
    "v_mfma_f32_16x16x32_bf16 v[52:55], a[188:191], v[208:211], v[52:55]\n"
    "ds_read_b64_tr_b16 a[174:175], v244 offset:38912\n"
    "v_mfma_f32_16x16x32_bf16 v[52:55], a[184:187], v[204:207], v[52:55]\n"
    "ds_read_b64_tr_b16 a[176:177], v244 offset:12288\n"
    "v_mfma_f32_16x16x32_bf16 v[56:59], a[184:187], v[212:215], v[56:59]\n"
    "ds_read_b64_tr_b16 a[178:179], v244 offset:14336\n"
    "v_cvt_pk_bf16_f32 v30, v48, v49\n"
    "v_mfma_f32_16x16x32_bf16 v[56:59], a[188:191], v[216:219], v[56:59]\n"
    "s_waitcnt lgkmcnt(6)\n"
    "ds_read_b64_tr_b16 a[184:185], v244 offset:20480\n"
    "v_mfma_f32_16x16x32_bf16 v[76:79], a[160:163], a[208:211], v[76:79]\n"
    "buffer_load_dwordx4 a[120:123], v250, s[8:11], s62 offen offset:0 sc1\n"
    "v_mfma_f32_16x16x32_bf16 v[76:79], a[164:167], a[212:215], v[76:79]\n"
    ".long 0xbedf00ff\n"
    ".Lat_w11:\n"
    "s_setpc_b64 s[2:3]\n"
    "ds_read_b64_tr_b16 a[186:187], v244 offset:22528\n"
    "v_mfma_f32_16x16x32_bf16 v[72:75], a[164:167], a[204:207], v[72:75]\n"
    "ds_read_b64_tr_b16 a[180:181], v244 offset:45056\n"
    "v_cvt_pk_bf16_f32 v36, v36, v37\n"
    "v_mfma_f32_16x16x32_bf16 v[72:75], a[160:163], a[200:203], v[72:75]\n"
    "ds_read_b64_tr_b16 a[182:183], v244 offset:47104\n"
    "v_mfma_f32_16x16x32_bf16 v[68:71], a[160:163], a[192:195], v[68:71]\n"
    "ds_read_b64_tr_b16 a[188:189], v244 offset:53248\n"
    "v_mfma_f32_16x16x32_bf16 v[68:71], a[164:167], a[196:199], v[68:71]\n"
    "s_waitcnt lgkmcnt(7)\n"
    "ds_read_b64_tr_b16 a[190:191], v244 offset:55296\n"
    "v_cvt_pk_bf16_f32 v37, v38, v39\n"
    "v_mfma_f32_16x16x32_bf16 v[68:71], a[168:171], v[156:159], v[68:71]\n"
    "v_cmp_eq_u32_e32 vcc, 0, v252\n"
    "v_cvt_pk_bf16_f32 v31, v50, v51\n"
    "v_mfma_f32_16x16x32_bf16 v[68:71], a[172:175], v[160:163], v[68:71]\n"
    "ds_read_b64_tr_b16 a[160:161], v245 offset:28672\n"
    "v_mfma_f32_16x16x32_bf16 v[72:75], a[172:175], v[168:171], v[72:75]\n"
    "buffer_store_dwordx4 v[28:31], v250, s[8:11], s82 offen offset:1024 sc1\n"
    "v_mfma_f32_16x16x32_bf16 v[72:75], a[168:171], v[164:167], v[72:75]\n"
    "ds_read_b64_tr_b16 a[162:163], v245 offset:30720\n"
    "v_mfma_f32_16x16x32_bf16 v[76:79], a[168:171], v[172:175], v[76:79]\n"
    "ds_read_b64_tr_b16 a[164:165], v245 offset:61440\n"
    "v_mfma_f32_16x16x32_bf16 v[76:79], a[172:175], v[176:179], v[76:79]\n"
    "s_waitcnt lgkmcnt(5)\n"
    "ds_read_b64_tr_b16 a[168:169], v245 offset:4096\n"
    "v_mfma_f32_16x16x32_bf16 v[76:79], a[176:179], v[180:183], v[76:79]\n"
    "buffer_load_dwordx4 a[124:127], v250, s[8:11], s62 offen offset:1024 sc1\n"
    "v_mfma_f32_16x16x32_bf16 v[76:79], a[180:183], v[184:187], v[76:79]\n"
    "v_cvt_pk_bf16_f32 v33, v34, v35\n"
    "v_mfma_f32_16x16x32_bf16 v[72:75], a[180:183], v[152:155], v[72:75]\n"
    "ds_read_b64_tr_b16 a[170:171], v245 offset:6144\n"
    "v_mfma_f32_16x16x32_bf16 v[72:75], a[176:179], v[148:151], v[72:75]\n"
    "ds_read_b64_tr_b16 a[166:167], v245 offset:63488\n"
    "v_mfma_f32_16x16x32_bf16 v[68:71], a[176:179], v[140:143], v[68:71]\n"
    "ds_read_b64_tr_b16 a[172:173], v245 offset:36864\n"
    "v_cndmask_b32_e32 v234, 0, v253, vcc\n"
    "v_mfma_f32_16x16x32_bf16 v[68:71], a[180:183], v[144:147], v[68:71]\n"
    "s_waitcnt lgkmcnt(7)\n"
    "ds_read_b64_tr_b16 a[176:177], v245 offset:12288\n"
    "v_cmp_eq_u32_e32 vcc, 1, v252\n"
    "v_mfma_f32_16x16x32_bf16 v[68:71], a[184:187], v[196:199], v[68:71]\n"
    "ds_read_b64_tr_b16 a[178:179], v245 offset:14336\n"
    "v_cndmask_b32_e64 v234, v234, 1.0, vcc\n"
    "v_mfma_f32_16x16x32_bf16 v[68:71], a[188:191], v[200:203], v[68:71]\n"
    "v_cvt_pk_bf16_f32 v34, v52, v53\n"
    "v_cvt_pk_bf16_f32 v35, v54, v55\n"
    "v_mfma_f32_16x16x32_bf16 v[72:75], a[188:191], v[208:211], v[72:75]\n"
    "v_mfma_f32_16x16x32_bf16 v[72:75], a[184:187], v[204:207], v[72:75]\n"
    "ds_read_b64_tr_b16 a[174:175], v245 offset:38912\n"
    "v_mfma_f32_16x16x32_bf16 v[76:79], a[184:187], v[212:215], v[76:79]\n"
    "buffer_store_dwordx4 v[32:35], v250, s[8:11], s28 offen offset:1024 sc1\n"
    "v_mfma_f32_16x16x32_bf16 v[76:79], a[188:191], v[216:219], v[76:79]\n"
    "s_waitcnt lgkmcnt(4)\n"
    "ds_read_b64_tr_b16 a[184:185], v245 offset:20480\n"
    "v_cvt_pk_bf16_f32 v68, v68, v69\n"
    "v_mfma_f32_16x16x32_bf16 v[96:99], a[160:163], a[208:211], v[96:99]\n"
    "ds_read_b64_tr_b16 a[186:187], v245 offset:22528\n"
    "v_cvt_pk_bf16_f32 v72, v72, v73\n"
    "v_mfma_f32_16x16x32_bf16 v[96:99], a[164:167], a[212:215], v[96:99]\n"
    "buffer_load_dwordx4 a[128:131], v250, s[8:11], s62 offen offset:2048 sc1\n"
    "v_mfma_f32_16x16x32_bf16 v[92:95], a[164:167], a[204:207], v[92:95]\n"
    "ds_read_b64_tr_b16 a[180:181], v245 offset:45056\n"
    "v_cvt_pk_bf16_f32 v76, v76, v77\n"
    "v_mfma_f32_16x16x32_bf16 v[92:95], a[160:163], a[200:203], v[92:95]\n"
    "ds_read_b64_tr_b16 a[188:189], v245 offset:53248\n"
    "v_cvt_pk_bf16_f32 v38, v56, v57\n"
    "v_mfma_f32_16x16x32_bf16 v[88:91], a[160:163], a[192:195], v[88:91]\n"
    "ds_read_b64_tr_b16 a[182:183], v245 offset:47104\n"
    "v_mfma_f32_16x16x32_bf16 v[88:91], a[164:167], a[196:199], v[88:91]\n"
    "s_waitcnt lgkmcnt(5)\n"
    "ds_read_b64_tr_b16 a[160:161], v246 offset:28672\n"
    "v_cmp_eq_u32_e32 vcc, 2, v252\n"
    "v_mfma_f32_16x16x32_bf16 v[88:91], a[168:171], v[156:159], v[88:91]\n"
    "s_nop 0\n"
    "v_cndmask_b32_e32 v235, 0, v253, vcc\n"
    "v_mfma_f32_16x16x32_bf16 v[88:91], a[172:175], v[160:163], v[88:91]\n"
    "ds_read_b64_tr_b16 a[190:191], v245 offset:55296\n"
    "v_mfma_f32_16x16x32_bf16 v[92:95], a[172:175], v[168:171], v[92:95]\n"
    "v_cvt_pk_bf16_f32 v39, v58, v59\n"
    "v_mfma_f32_16x16x32_bf16 v[92:95], a[168:171], v[164:167], v[92:95]\n"
    "buffer_load_dwordx4 a[132:135], v250, s[8:11], s62 offen offset:3072 sc1\n"
    "v_mfma_f32_16x16x32_bf16 v[96:99], a[168:171], v[172:175], v[96:99]\n"
    "s_add_u32 s62, s57, 24576\n"
    "v_cvt_pk_bf16_f32 v73, v74, v75\n"
    "v_mfma_f32_16x16x32_bf16 v[96:99], a[172:175], v[176:179], v[96:99]\n"
    "s_waitcnt lgkmcnt(2)\n"
    "ds_read_b64_tr_b16 a[162:163], v246 offset:30720\n"
    "v_mfma_f32_16x16x32_bf16 v[96:99], a[176:179], v[180:183], v[96:99]\n"
    "buffer_store_dwordx4 v[36:39], v250, s[8:11], s29 offen offset:1024 sc1\n"
    "v_mfma_f32_16x16x32_bf16 v[96:99], a[180:183], v[184:187], v[96:99]\n"
    "ds_read_b64_tr_b16 a[168:169], v246 offset:4096\n"
    "v_mfma_f32_16x16x32_bf16 v[92:95], a[180:183], v[152:155], v[92:95]\n"
    "ds_read_b64_tr_b16 a[164:165], v246 offset:61440\n"
    "v_mfma_f32_16x16x32_bf16 v[92:95], a[176:179], v[148:151], v[92:95]\n"
    "ds_read_b64_tr_b16 a[166:167], v246 offset:63488\n"
    "v_mfma_f32_16x16x32_bf16 v[88:91], a[176:179], v[140:143], v[88:91]\n"
    "ds_read_b64_tr_b16 a[170:171], v246 offset:6144\n"
    "v_cvt_pk_bf16_f32 v69, v70, v71\n"
    "v_mfma_f32_16x16x32_bf16 v[88:91], a[180:183], v[144:147], v[88:91]\n"
    "s_waitcnt lgkmcnt(5)\n"
    "ds_read_b64_tr_b16 a[176:177], v246 offset:12288\n"
    "v_cvt_pk_bf16_f32 v77, v78, v79\n"
    "v_mfma_f32_16x16x32_bf16 v[88:91], a[184:187], v[196:199], v[88:91]\n"
    "buffer_load_dwordx4 a[136:139], v250, s[8:11], s62 offen offset:0 sc1\n"
    "v_mfma_f32_16x16x32_bf16 v[88:91], a[188:191], v[200:203], v[88:91]\n"
    "ds_read_b64_tr_b16 a[172:173], v246 offset:36864\n"
    "v_mfma_f32_16x16x32_bf16 v[92:95], a[188:191], v[208:211], v[92:95]\n"
    "v_mfma_f32_16x16x32_bf16 v[92:95], a[184:187], v[204:207], v[92:95]\n"
    "ds_read_b64_tr_b16 a[174:175], v246 offset:38912\n"
    "v_cvt_pk_bf16_f32 v70, v88, v89\n"
    "v_mfma_f32_16x16x32_bf16 v[96:99], a[184:187], v[212:215], v[96:99]\n"
    "v_mfma_f32_16x16x32_bf16 v[96:99], a[188:191], v[216:219], v[96:99]\n"
    "s_waitcnt lgkmcnt(4)\n"
    "ds_read_b64_tr_b16 a[178:179], v246 offset:14336\n"
    "v_cmp_eq_u32_e32 vcc, 3, v252\n"
    "v_mfma_f32_16x16x32_bf16 v[116:119], a[160:163], a[208:211], v[116:119]\n"
    "ds_read_b64_tr_b16 a[184:185], v246 offset:20480\n"
    "v_cvt_pk_bf16_f32 v71, v90, v91\n"
    "v_mfma_f32_16x16x32_bf16 v[116:119], a[164:167], a[212:215], v[116:119]\n"
    "v_cvt_pk_bf16_f32 v74, v92, v93\n"
    "v_mfma_f32_16x16x32_bf16 v[112:115], a[164:167], a[204:207], v[112:115]\n"
    "buffer_store_dwordx4 v[68:71], v250, s[8:11], s82 offen offset:2048 sc1\n"
    "v_mfma_f32_16x16x32_bf16 v[112:115], a[160:163], a[200:203], v[112:115]\n"
    "ds_read_b64_tr_b16 a[180:181], v246 offset:45056\n"
    "v_mfma_f32_16x16x32_bf16 v[108:111], a[160:163], a[192:195], v[108:111]\n"
    "ds_read_b64_tr_b16 a[182:183], v246 offset:47104\n"
    "v_mfma_f32_16x16x32_bf16 v[108:111], a[164:167], a[196:199], v[108:111]\n"
    "s_waitcnt lgkmcnt(4)\n"
    "ds_read_b64_tr_b16 a[186:187], v246 offset:22528\n"
    "v_mfma_f32_16x16x32_bf16 v[108:111], a[168:171], v[156:159], v[108:111]\n"
    "buffer_load_dwordx4 a[140:143], v250, s[8:11], s62 offen offset:1024 sc1\n"
    "v_mfma_f32_16x16x32_bf16 v[108:111], a[172:175], v[160:163], v[108:111]\n"
    "ds_read_b64_tr_b16 a[188:189], v246 offset:53248\n"
    "v_cndmask_b32_e64 v235, v235, 1.0, vcc\n"
    "v_mfma_f32_16x16x32_bf16 v[112:115], a[172:175], v[168:171], v[112:115]\n"
    "v_mfma_f32_16x16x32_bf16 v[112:115], a[168:171], v[164:167], v[112:115]\n"
    "ds_read_b64_tr_b16 a[160:161], v247 offset:28672\n"
    "v_mfma_f32_16x16x32_bf16 v[116:119], a[168:171], v[172:175], v[116:119]\n"
    "ds_read_b64_tr_b16 a[190:191], v246 offset:55296\n"
    "v_cvt_pk_bf16_f32 v75, v94, v95\n"
    "v_mfma_f32_16x16x32_bf16 v[116:119], a[172:175], v[176:179], v[116:119]\n"
    "s_waitcnt lgkmcnt(4)\n"
    "ds_read_b64_tr_b16 a[162:163], v247 offset:30720\n"
    "v_cvt_pk_bf16_f32 v78, v96, v97\n"
    "v_mfma_f32_16x16x32_bf16 v[116:119], a[176:179], v[180:183], v[116:119]\n"
    "ds_read_b64_tr_b16 a[164:165], v247 offset:61440\n"
    "v_mfma_f32_16x16x32_bf16 v[116:119], a[180:183], v[184:187], v[116:119]\n"
    "v_mfma_f32_16x16x32_bf16 v[112:115], a[180:183], v[152:155], v[112:115]\n"
    "ds_read_b64_tr_b16 a[168:169], v247 offset:4096\n"
    "v_mfma_f32_16x16x32_bf16 v[112:115], a[176:179], v[148:151], v[112:115]\n"
    "ds_read_b64_tr_b16 a[166:167], v247 offset:63488\n"
    "v_mfma_f32_16x16x32_bf16 v[108:111], a[176:179], v[140:143], v[108:111]\n"
    "buffer_store_dwordx4 v[72:75], v250, s[8:11], s28 offen offset:2048 sc1\n"
    "v_mfma_f32_16x16x32_bf16 v[108:111], a[180:183], v[144:147], v[108:111]\n"
    "s_waitcnt lgkmcnt(4)\n"
    "ds_read_b64_tr_b16 a[170:171], v247 offset:6144\n"
    "v_mfma_f32_16x16x32_bf16 v[108:111], a[184:187], v[196:199], v[108:111]\n"
    "ds_read_b64_tr_b16 a[176:177], v247 offset:12288\n"
    "v_mfma_f32_16x16x32_bf16 v[108:111], a[188:191], v[200:203], v[108:111]\n"
    "buffer_load_dwordx4 a[144:147], v250, s[8:11], s62 offen offset:2048 sc1\n"
    "v_mfma_f32_16x16x32_bf16 v[112:115], a[188:191], v[208:211], v[112:115]\n"
    "ds_read_b64_tr_b16 a[172:173], v247 offset:36864\n"
    "v_mfma_f32_16x16x32_bf16 v[112:115], a[184:187], v[204:207], v[112:115]\n"
    "ds_read_b64_tr_b16 a[178:179], v247 offset:14336\n"
    "v_cvt_pk_bf16_f32 v108, v108, v109\n"
    "v_mfma_f32_16x16x32_bf16 v[116:119], a[184:187], v[212:215], v[116:119]\n"
    "ds_read_b64_tr_b16 a[174:175], v247 offset:38912\n"
    "v_mfma_f32_16x16x32_bf16 v[116:119], a[188:191], v[216:219], v[116:119]\n"
    "s_waitcnt lgkmcnt(5)\n"
    "ds_read_b64_tr_b16 a[180:181], v247 offset:45056\n"
    "v_cvt_pk_bf16_f32 v79, v98, v99\n"
    "v_mfma_f32_16x16x32_bf16 v[136:139], a[160:163], a[208:211], v[136:139]\n"
    "s_cmp_lg_u32 s44, 0\n"
    "s_cbranch_scc1 .Lat_glate\n"
    ".Lat_glate_ret:\n"
    "v_mfma_f32_16x16x32_bf16 v[136:139], a[164:167], a[212:215], v[136:139]\n"
    "ds_read_b64_tr_b16 a[184:185], v247 offset:20480\n"
    "v_mfma_f32_16x16x32_bf16 v[132:135], a[164:167], a[204:207], v[132:135]\n"
    "ds_read_b64_tr_b16 a[186:187], v247 offset:22528\n"
    "v_mfma_f32_16x16x32_bf16 v[132:135], a[160:163], a[200:203], v[132:135]\n"
    "buffer_load_dwordx4 a[148:151], v250, s[8:11], s62 offen offset:3072 sc1\n"
    "v_mfma_f32_16x16x32_bf16 v[128:131], a[160:163], a[192:195], v[128:131]\n"
    "s_add_u32 s62, s57, 28672\n"
    "ds_read_b64_tr_b16 a[182:183], v247 offset:47104\n"
    "v_mfma_f32_16x16x32_bf16 v[128:131], a[164:167], a[196:199], v[128:131]\n"
    "s_waitcnt lgkmcnt(4)\n"
    "v_cvt_pk_bf16_f32 v116, v116, v117\n"
    "v_mfma_f32_16x16x32_bf16 v[128:131], a[168:171], v[156:159], v[128:131]\n"
    "buffer_store_dwordx4 v[76:79], v250, s[8:11], s29 offen offset:2048 sc1\n"
    "v_mfma_f32_16x16x32_bf16 v[128:131], a[172:175], v[160:163], v[128:131]\n"
    "ds_read_b64_tr_b16 a[188:189], v247 offset:53248\n"
    "v_mfma_f32_16x16x32_bf16 v[132:135], a[172:175], v[168:171], v[132:135]\n"
    "ds_read_b64_tr_b16 a[190:191], v247 offset:55296\n"
    "v_mfma_f32_16x16x32_bf16 v[132:135], a[168:171], v[164:167], v[132:135]\n"
    "ds_read_b128 a[160:163], v240 offset:24576\n"
    "v_mfma_f32_16x16x32_bf16 v[136:139], a[168:171], v[172:175], v[136:139]\n"
    "ds_read_b128 a[164:167], v241 offset:24576\n"
    "v_mfma_f32_16x16x32_bf16 v[136:139], a[172:175], v[176:179], v[136:139]\n"
    "s_waitcnt lgkmcnt(4)\n"
    "ds_read_b128 a[168:171], v240 offset:28672\n"
    "v_mfma_f32_16x16x32_bf16 v[136:139], a[176:179], v[180:183], v[136:139]\n"
    "buffer_load_dwordx4 a[152:155], v250, s[8:11], s62 offen offset:0 sc1\n"
    "v_mfma_f32_16x16x32_bf16 v[136:139], a[180:183], v[184:187], v[136:139]\n"
    "v_mfma_f32_16x16x32_bf16 v[132:135], a[180:183], v[152:155], v[132:135]\n"
    "v_mfma_f32_16x16x32_bf16 v[132:135], a[176:179], v[148:151], v[132:135]\n"
    "v_mfma_f32_16x16x32_bf16 v[128:131], a[176:179], v[140:143], v[128:131]\n"
    "ds_read_b128 a[172:175], v241 offset:28672\n"
    "v_cvt_pk_bf16_f32 v109, v110, v111\n"
    "v_mfma_f32_16x16x32_bf16 v[128:131], a[180:183], v[144:147], v[128:131]\n"
    "s_waitcnt lgkmcnt(4)\n"
    "ds_read_b128 a[176:179], v242 offset:24576\n"
    "v_mfma_f32_16x16x32_bf16 v[128:131], a[184:187], v[196:199], v[128:131]\n"
    "v_cvt_pk_bf16_f32 v117, v118, v119\n"
    "v_mfma_f32_16x16x32_bf16 v[128:131], a[188:191], v[200:203], v[128:131]\n"
    "v_mfma_f32_16x16x32_bf16 v[132:135], a[188:191], v[208:211], v[132:135]\n"
    "buffer_load_dwordx4 a[156:159], v250, s[8:11], s62 offen offset:1024 sc1\n"
    "v_mfma_f32_16x16x32_bf16 v[132:135], a[184:187], v[204:207], v[132:135]\n"
    "ds_read_b128 a[180:183], v243 offset:24576\n"
    "v_mfma_f32_16x16x32_bf16 v[136:139], a[184:187], v[212:215], v[136:139]\n"
    "v_cvt_pk_bf16_f32 v110, v128, v129\n"
    "v_cvt_pk_bf16_f32 v111, v130, v131\n"
    "v_mfma_f32_16x16x32_bf16 v[136:139], a[188:191], v[216:219], v[136:139]\n"
    "s_waitcnt lgkmcnt(4)\n"
    "ds_read_b128 a[184:187], v242 offset:28672\n"
    "v_mfma_f32_16x16x32_bf16 v[164:167], a[160:163], a[32:35], v[224:227]\n"
    "buffer_store_dwordx4 v[108:111], v250, s[8:11], s82 offen offset:3072 sc1\n"
    "v_mfma_f32_16x16x32_bf16 v[164:167], a[164:167], a[36:39], v[164:167]\n"
    "ds_read_b128 a[188:191], v243 offset:28672\n"
    "v_mfma_f32_16x16x32_bf16 v[156:159], a[164:167], a[4:7], v[220:223]\n"
    "v_mfma_f32_16x16x32_bf16 v[156:159], a[160:163], a[0:3], v[156:159]\n"
    "s_waitcnt lgkmcnt(4)\n"
    "v_cvt_pk_bf16_f32 v112, v112, v113\n"
    "v_mfma_f32_16x16x32_bf16 v[156:159], a[168:171], a[8:11], v[156:159]\n"
    "buffer_store_dword v131, v231, s[8:11], s87 offen offset:0 sc1\n"
    "v_mfma_f32_16x16x32_bf16 v[156:159], a[172:175], a[12:15], v[156:159]\n"
    "ds_read_b128 a[160:163], v240 offset:26624\n"
    "v_cvt_pk_bf16_f32 v113, v114, v115\n"
    "v_mfma_f32_16x16x32_bf16 v[164:167], a[172:175], a[44:47], v[164:167]\n"
    "ds_read_b128 a[164:167], v241 offset:26624\n"
    "v_mfma_f32_16x16x32_bf16 v[164:167], a[168:171], a[40:43], v[164:167]\n"
    "s_waitcnt lgkmcnt(4)\n"
    "ds_read_b128 a[168:171], v240 offset:30720\n"
    "v_mfma_f32_16x16x32_bf16 v[164:167], a[176:179], a[48:51], v[164:167]\n"
    "v_cvt_pk_bf16_f32 v118, v136, v137\n"
    "v_mfma_f32_16x16x32_bf16 v[164:167], a[180:183], a[52:55], v[164:167]\n"
    "buffer_store_dword v135, v231, s[8:11], s88 offen offset:0 sc1\n"
    "v_mfma_f32_16x16x32_bf16 v[156:159], a[180:183], a[20:23], v[156:159]\n"
    "v_cvt_pk_bf16_f32 v114, v132, v133\n"
    "v_cvt_pk_bf16_f32 v119, v138, v139\n"
    "v_mfma_f32_16x16x32_bf16 v[156:159], a[176:179], a[16:19], v[156:159]\n"
    "s_waitcnt lgkmcnt(3)\n"
    "ds_read_b128 a[172:175], v241 offset:30720\n"
    "v_mfma_f32_16x16x32_bf16 v[156:159], a[184:187], a[24:27], v[156:159]\n"
    "ds_read_b128 a[176:179], v242 offset:26624\n"
    "v_cvt_pk_bf16_f32 v115, v134, v135\n"
    "v_mfma_f32_16x16x32_bf16 v[156:159], a[188:191], a[28:31], v[156:159]\n"
    "ds_read_b128 a[180:183], v243 offset:26624\n"
    "v_mfma_f32_16x16x32_bf16 v[164:167], a[188:191], a[60:63], v[164:167]\n"
    "buffer_store_dwordx4 v[112:115], v250, s[8:11], s28 offen offset:3072 sc1\n"
    "v_mfma_f32_16x16x32_bf16 v[164:167], a[184:187], a[56:59], v[164:167]\n"
    "s_waitcnt lgkmcnt(4)\n"
    "v_exp_f32_e32 v156, v156\n"
    "v_mfma_f32_16x16x32_bf16 v[168:171], a[160:163], a[32:35], v[224:227]\n"
    "v_exp_f32_e32 v157, v157\n"
    "v_mfma_f32_16x16x32_bf16 v[168:171], a[164:167], a[36:39], v[168:171]\n"
    "buffer_store_dword v139, v231, s[8:11], s89 offen offset:0 sc1\n"
    "v_mfma_f32_16x16x32_bf16 v[160:163], a[164:167], a[4:7], v[220:223]\n"
    "s_mov_b32 s56, 0\n"
    "v_exp_f32_e32 v158, v158\n"
    "v_mfma_f32_16x16x32_bf16 v[160:163], a[160:163], a[0:3], v[160:163]\n"
    "s_waitcnt lgkmcnt(2)\n"
    "ds_read_b128 a[184:187], v242 offset:30720\n"
    "ds_read_b128 a[188:191], v243 offset:30720\n"
    "v_mfma_f32_16x16x32_bf16 v[160:163], a[168:171], a[8:11], v[160:163]\n"
    "buffer_load_dwordx4 v[180:183], v250, s[8:11], s62 offen offset:2048 sc1\n"
    "v_mfma_f32_16x16x32_bf16 v[160:163], a[172:175], a[12:15], v[160:163]\n"
    "ds_read_b128 a[160:163], v240 offset:0\n"
    "ds_read_b128 a[164:167], v241 offset:0\n"
    "v_mfma_f32_16x16x32_bf16 v[168:171], a[172:175], a[44:47], v[168:171]\n"
    "v_exp_f32_e32 v159, v159\n"
    "v_mfma_f32_16x16x32_bf16 v[168:171], a[168:171], a[40:43], v[168:171]\n"
    "s_waitcnt lgkmcnt(4)\n"
    "ds_read_b128 a[168:171], v240 offset:4096\n"
    "v_cvt_pk_bf16_f32 v156, v156, v157\n"
    "v_mfma_f32_16x16x32_bf16 v[168:171], a[176:179], a[48:51], v[168:171]\n"
    "ds_write_addtid_b32 v156 offset:0\n"
    "v_add_f32_e32 v128, v131, v135\n"
    "v_mfma_f32_16x16x32_bf16 v[168:171], a[180:183], a[52:55], v[168:171]\n"
    "v_min3_f32 v129, v131, v135, v139\n"
    "v_add_f32_e32 v128, v128, v139\n"
    "v_mfma_f32_16x16x32_bf16 v[160:163], a[180:183], a[20:23], v[160:163]\n"
    "ds_read_b128 a[172:175], v241 offset:4096\n"
    "v_mfma_f32_16x16x32_bf16 v[160:163], a[176:179], a[16:19], v[160:163]\n"
    "s_waitcnt lgkmcnt(5)\n"
    "ds_read_b128 a[176:179], v242 offset:0\n"
    "ds_read_b128 a[180:183], v243 offset:0\n"
    "v_mfma_f32_16x16x32_bf16 v[160:163], a[184:187], a[24:27], v[160:163]\n"
    "buffer_store_dwordx4 v[116:119], v250, s[8:11], s29 offen offset:3072 sc1\n"
    "v_mfma_f32_16x16x32_bf16 v[160:163], a[188:191], a[28:31], v[160:163]\n"
    "v_exp_f32_e32 v164, v164\n"
    "v_mfma_f32_16x16x32_bf16 v[168:171], a[188:191], a[60:63], v[168:171]\n"
    "v_exp_f32_e32 v165, v165\n"
    "v_mfma_f32_16x16x32_bf16 v[168:171], a[184:187], a[56:59], v[168:171]\n"
    "s_waitcnt lgkmcnt(5)\n"
    "ds_read_b128 a[184:187], v242 offset:4096\n"
    "v_cvt_pk_bf16_f32 v157, v158, v159\n"
    "v_mfma_f32_16x16x32_bf16 v[204:207], a[160:163], a[32:35], v[224:227]\n"
    "buffer_load_dwordx4 v[184:187], v250, s[8:11], s62 offen offset:3072 sc1\n"
    "v_mfma_f32_16x16x32_bf16 v[204:207], a[164:167], a[36:39], v[204:207]\n"
    "s_add_u32 s62, s58, 0\n"
    "ds_read_b128 a[188:191], v243 offset:4096\n"
    "ds_write_addtid_b32 v157 offset:256\n"
    "v_mfma_f32_16x16x32_bf16 v[196:199], a[164:167], a[4:7], v[220:223]\n"
    "v_exp_f32_e32 v166, v166\n"
    "v_mfma_f32_16x16x32_bf16 v[196:199], a[160:163], a[0:3], v[196:199]\n"
    "s_waitcnt lgkmcnt(5)\n"
    "ds_read_b128 a[160:163], v240 offset:2048\n"
    "v_mfma_f32_16x16x32_bf16 v[196:199], a[168:171], a[8:11], v[196:199]\n"
    "v_exp_f32_e32 v167, v167\n"
    "v_mfma_f32_16x16x32_bf16 v[196:199], a[172:175], a[12:15], v[196:199]\n"
    "ds_read_b128 a[164:167], v241 offset:2048\n"
    "v_cvt_pk_bf16_f32 v164, v164, v165\n"
    "v_mfma_f32_16x16x32_bf16 v[204:207], a[172:175], a[44:47], v[204:207]\n"
    "v_exp_f32_e32 v160, v160\n"
    "v_mfma_f32_16x16x32_bf16 v[204:207], a[168:171], a[40:43], v[204:207]\n"
    "s_waitcnt lgkmcnt(5)\n"
    "v_exp_f32_e32 v161, v161\n"
    "v_mfma_f32_16x16x32_bf16 v[204:207], a[176:179], a[48:51], v[204:207]\n"
    "buffer_load_dwordx4 v[28:31], v250, s[8:11], s62 offen offset:0 sc1\n"
    "v_mfma_f32_16x16x32_bf16 v[204:207], a[180:183], a[52:55], v[204:207]\n"
    "ds_read_b128 a[168:171], v240 offset:6144\n"
    "v_cvt_pk_bf16_f32 v165, v166, v167\n"
    "v_mfma_f32_16x16x32_bf16 v[196:199], a[180:183], a[20:23], v[196:199]\n"
    "ds_write_addtid_b32 v164 offset:1024\n"
    "v_exp_f32_e32 v162, v162\n"
    "v_mfma_f32_16x16x32_bf16 v[196:199], a[176:179], a[16:19], v[196:199]\n"
    "s_waitcnt lgkmcnt(5)\n"
    "ds_read_b128 a[176:179], v242 offset:2048\n"
    "v_exp_f32_e32 v163, v163\n"
    "v_mfma_f32_16x16x32_bf16 v[196:199], a[184:187], a[24:27], v[196:199]\n"
    "ds_read_b128 a[172:175], v241 offset:6144\n"
    "v_cvt_pk_bf16_f32 v158, v160, v161\n"
    "v_mfma_f32_16x16x32_bf16 v[196:199], a[188:191], a[28:31], v[196:199]\n"
    "v_exp_f32_e32 v168, v168\n"
    "v_mfma_f32_16x16x32_bf16 v[204:207], a[188:191], a[60:63], v[204:207]\n"
    "v_exp_f32_e32 v169, v169\n"
    "v_mfma_f32_16x16x32_bf16 v[204:207], a[184:187], a[56:59], v[204:207]\n"
    "s_waitcnt lgkmcnt(4)\n"
    "ds_read_b128 a[180:183], v243 offset:2048\n"
    "v_cvt_pk_bf16_f32 v159, v162, v163\n"
    "v_mfma_f32_16x16x32_bf16 v[208:211], a[160:163], a[32:35], v[224:227]\n"
    "buffer_load_dwordx4 v[48:51], v250, s[8:11], s62 offen offset:1024 sc1\n"
    "ds_write_addtid_b32 v165 offset:1280\n"
    "v_mfma_f32_16x16x32_bf16 v[208:211], a[164:167], a[36:39], v[208:211]\n"
    "ds_write_addtid_b32 v158 offset:512\n"
    "v_exp_f32_e32 v170, v170\n"
    "v_mfma_f32_16x16x32_bf16 v[200:203], a[164:167], a[4:7], v[220:223]\n"
    "ds_read_b128 a[184:187], v242 offset:6144\n"
    "v_exp_f32_e32 v196, v196\n"
    "v_mfma_f32_16x16x32_bf16 v[200:203], a[160:163], a[0:3], v[200:203]\n"
    "s_waitcnt lgkmcnt(4)\n"
    "v_exp_f32_e32 v197, v197\n"
    "v_mfma_f32_16x16x32_bf16 v[200:203], a[168:171], a[8:11], v[200:203]\n"
    "ds_read_b128 a[188:191], v243 offset:6144\n"
    "v_exp_f32_e32 v198, v198\n"
    "v_mfma_f32_16x16x32_bf16 v[200:203], a[172:175], a[12:15], v[200:203]\n"
    "v_exp_f32_e32 v171, v171\n"
    "v_mfma_f32_16x16x32_bf16 v[208:211], a[172:175], a[44:47], v[208:211]\n"
    "v_exp_f32_e32 v199, v199\n"
    "v_mfma_f32_16x16x32_bf16 v[208:211], a[168:171], a[40:43], v[208:211]\n"
    "s_waitcnt lgkmcnt(4)\n"
    "ds_read_b128 a[160:163], v240 offset:8192\n"
    "ds_read_b128 a[164:167], v241 offset:8192\n"
    "v_mfma_f32_16x16x32_bf16 v[208:211], a[176:179], a[48:51], v[208:211]\n"
    "buffer_load_dwordx4 v[32:35], v250, s[8:11], s62 offen offset:2048 sc1\n"
    "ds_read_b128 a[168:171], v240 offset:12288\n"
    "v_mfma_f32_16x16x32_bf16 v[208:211], a[180:183], a[52:55], v[208:211]\n"
    "ds_read_b128 a[172:175], v241 offset:12288\n"
    "v_cvt_pk_bf16_f32 v196, v196, v197\n"
    "v_mfma_f32_16x16x32_bf16 v[200:203], a[180:183], a[20:23], v[200:203]\n"
    "v_exp_f32_e32 v204, v204\n"
    "v_mfma_f32_16x16x32_bf16 v[200:203], a[176:179], a[16:19], v[200:203]\n"
    "s_waitcnt lgkmcnt(4)\n"
    "v_exp_f32_e32 v205, v205\n"
    "v_mfma_f32_16x16x32_bf16 v[200:203], a[184:187], a[24:27], v[200:203]\n"
    "v_cvt_pk_bf16_f32 v197, v198, v199\n"
    "v_exp_f32_e32 v206, v206\n"
    "v_mfma_f32_16x16x32_bf16 v[200:203], a[188:191], a[28:31], v[200:203]\n"
    "ds_read_b128 a[176:179], v242 offset:8192\n"
    "v_cvt_pk_bf16_f32 v166, v168, v169\n"
    "v_mfma_f32_16x16x32_bf16 v[208:211], a[188:191], a[60:63], v[208:211]\n"
    "v_exp_f32_e32 v207, v207\n"
    "v_mfma_f32_16x16x32_bf16 v[208:211], a[184:187], a[56:59], v[208:211]\n"
    "s_waitcnt lgkmcnt(3)\n"
    "ds_read_b128 a[180:183], v243 offset:8192\n"
    "v_cvt_pk_bf16_f32 v204, v204, v205\n"
    "v_mfma_f32_16x16x32_bf16 v[212:215], a[160:163], a[32:35], v[224:227]\n"
    "buffer_load_dwordx4 v[52:55], v250, s[8:11], s62 offen offset:3072 sc1\n"
    "v_mfma_f32_16x16x32_bf16 v[212:215], a[164:167], a[36:39], v[212:215]\n"
    "s_add_u32 s62, s58, 4096\n"
    "v_exp_f32_e32 v200, v200\n"
    "v_mfma_f32_16x16x32_bf16 v[172:175], a[164:167], a[4:7], v[220:223]\n"
    "v_exp_f32_e32 v201, v201\n"
    "v_mfma_f32_16x16x32_bf16 v[172:175], a[160:163], a[0:3], v[172:175]\n"
    "s_waitcnt lgkmcnt(2)\n"
    "ds_read_b128 a[160:163], v240 offset:10240\n"
    "v_cvt_pk_bf16_f32 v205, v206, v207\n"
    "v_mfma_f32_16x16x32_bf16 v[172:175], a[168:171], a[8:11], v[172:175]\n"
    "ds_read_b128 a[184:187], v242 offset:12288\n"
    "ds_read_b128 a[188:191], v243 offset:12288\n"
    "v_mfma_f32_16x16x32_bf16 v[172:175], a[172:175], a[12:15], v[172:175]\n"
    "ds_read_b128 a[164:167], v241 offset:10240\n"
    "v_exp_f32_e32 v202, v202\n"
    "v_mfma_f32_16x16x32_bf16 v[212:215], a[172:175], a[44:47], v[212:215]\n"
    "ds_write_addtid_b32 v159 offset:768\n"
    "v_cvt_pk_bf16_f32 v167, v170, v171\n"
    "v_mfma_f32_16x16x32_bf16 v[212:215], a[168:171], a[40:43], v[212:215]\n"
    "s_waitcnt lgkmcnt(5)\n"
    "ds_write_addtid_b32 v166 offset:1536\n"
    "v_exp_f32_e32 v203, v203\n"
    "v_mfma_f32_16x16x32_bf16 v[212:215], a[176:179], a[48:51], v[212:215]\n"
    "buffer_load_dwordx4 v[36:39], v250, s[8:11], s62 offen offset:0 sc1\n"
    "v_mfma_f32_16x16x32_bf16 v[212:215], a[180:183], a[52:55], v[212:215]\n"
    "ds_read_b128 a[168:171], v240 offset:14336\n"
    "v_cvt_pk_bf16_f32 v198, v200, v201\n"
    "v_mfma_f32_16x16x32_bf16 v[172:175], a[180:183], a[20:23], v[172:175]\n"
    "ds_read_b128 a[172:175], v241 offset:14336\n"
    "v_exp_f32_e32 v208, v208\n"
    "v_mfma_f32_16x16x32_bf16 v[172:175], a[176:179], a[16:19], v[172:175]\n"
    "s_waitcnt lgkmcnt(5)\n"
    "v_exp_f32_e32 v209, v209\n"
    "v_mfma_f32_16x16x32_bf16 v[172:175], a[184:187], a[24:27], v[172:175]\n"
    "ds_read_b128 a[176:179], v242 offset:10240\n"
    "v_cvt_pk_bf16_f32 v199, v202, v203\n"
    "v_mfma_f32_16x16x32_bf16 v[172:175], a[188:191], a[28:31], v[172:175]\n"
    "ds_read_b128 a[180:183], v243 offset:10240\n"
    "ds_write_addtid_b32 v167 offset:1792\n"
    "v_mfma_f32_16x16x32_bf16 v[212:215], a[188:191], a[60:63], v[212:215]\n"
    "v_exp_f32_e32 v210, v210\n"
    "v_mfma_f32_16x16x32_bf16 v[212:215], a[184:187], a[56:59], v[212:215]\n"
    "s_waitcnt lgkmcnt(7)\n"
    "ds_read_b128 a[184:187], v242 offset:14336\n"
    "v_exp_f32_e32 v172, v172\n"
    "v_mfma_f32_16x16x32_bf16 v[216:219], a[160:163], a[32:35], v[224:227]\n"
    "buffer_load_dwordx4 v[56:59], v250, s[8:11], s62 offen offset:1024 sc1\n"
    "ds_read_b128 a[188:191], v243 offset:14336\n"
    "v_mfma_f32_16x16x32_bf16 v[216:219], a[164:167], a[36:39], v[216:219]\n"
    "s_waitcnt lgkmcnt(2)\n"
    "v_mfma_f32_16x16x32_bf16 v[176:179], a[164:167], a[4:7], v[220:223]\n"
    "s_barrier\n"
    "v_mfma_f32_16x16x32_bf16 v[176:179], a[160:163], a[0:3], v[176:179]\n"
    "ds_read_b64_tr_b16 a[160:161], v244 offset:24576\n"
    "ds_read2st64_b32 v[160:161], v249 offset0:0 offset1:1\n"
    "v_mfma_f32_16x16x32_bf16 v[176:179], a[168:171], a[8:11], v[176:179]\n"
    "ds_read_b64_tr_b16 a[162:163], v244 offset:26624\n"
    "v_exp_f32_e32 v173, v173\n"
    "v_mfma_f32_16x16x32_bf16 v[176:179], a[172:175], a[12:15], v[176:179]\n"
    "ds_read2st64_b32 v[162:163], v249 offset0:2 offset1:3\n"
    "v_exp_f32_e32 v174, v174\n"
    "v_mfma_f32_16x16x32_bf16 v[216:219], a[172:175], a[44:47], v[216:219]\n"
    "ds_read_b64_tr_b16 a[164:165], v244 offset:57344\n"
    "ds_read_b64_tr_b16 a[166:167], v244 offset:59392\n"
    "v_mfma_f32_16x16x32_bf16 v[216:219], a[168:171], a[40:43], v[216:219]\n"
    "ds_read2st64_b32 v[168:169], v249 offset0:4 offset1:5\n"
    "ds_read2st64_b32 v[170:171], v249 offset0:6 offset1:7\n"
    "v_mfma_f32_16x16x32_bf16 v[216:219], a[176:179], a[48:51], v[216:219]\n"
    "s_waitcnt vmcnt(7)\n"
    "buffer_store_dword v254, v231, s[8:11], s38 offen offset:0 sc1\n"
    "v_mfma_f32_16x16x32_bf16 v[216:219], a[180:183], a[52:55], v[216:219]\n"
    "v_exp_f32_e32 v175, v175\n"
    "v_mfma_f32_16x16x32_bf16 v[176:179], a[180:183], a[20:23], v[176:179]\n"
    "ds_read_b64_tr_b16 a[168:169], v245 offset:24576\n"
    "ds_read_b64_tr_b16 a[170:171], v245 offset:26624\n"
    "v_mfma_f32_16x16x32_bf16 v[176:179], a[176:179], a[16:19], v[176:179]\n"
    "s_waitcnt lgkmcnt(10)\n"
    "ds_read_b64_tr_b16 a[172:173], v245 offset:57344\n"
    "v_cvt_pk_bf16_f32 v172, v172, v173\n"
    "v_mfma_f32_16x16x32_bf16 v[176:179], a[184:187], a[24:27], v[176:179]\n"
    "buffer_load_dwordx4 v[68:71], v250, s[8:11], s62 offen offset:2048 sc1\n"
    "ds_read_b64_tr_b16 a[174:175], v245 offset:59392\n"
    "v_mfma_f32_16x16x32_bf16 v[176:179], a[188:191], a[28:31], v[176:179]\n"
    "ds_read_b64_tr_b16 a[176:177], v246 offset:24576\n"
    "v_exp_f32_e32 v211, v211\n"
    "v_mfma_f32_16x16x32_bf16 v[216:219], a[188:191], a[60:63], v[216:219]\n"
    "ds_read_b64_tr_b16 a[178:179], v246 offset:26624\n"
    "ds_read_b64_tr_b16 a[180:181], v246 offset:57344\n"
    "v_mfma_f32_16x16x32_bf16 v[216:219], a[184:187], a[56:59], v[216:219]\n"
    "s_waitcnt lgkmcnt(7)\n"
    "v_exp_f32_e32 v212, v212\n"
    "v_mfma_f32_16x16x32_bf16 a[240:243], a[160:163], v[164:167], a[240:243]\n"
    "ds_read_b64_tr_b16 a[182:183], v246 offset:59392\n"
    "ds_read_b64_tr_b16 a[184:185], v247 offset:24576\n"
    "v_mfma_f32_16x16x32_bf16 a[240:243], a[164:167], v[168:171], a[240:243]\n"
    "ds_write_addtid_b32 v196 offset:2048\n"
    "v_exp_f32_e32 v213, v213\n"
    "v_mfma_f32_16x16x32_bf16 a[236:239], a[164:167], v[160:163], a[236:239]\n"
    "ds_read_b64_tr_b16 a[186:187], v247 offset:26624\n"
    "ds_read_b64_tr_b16 a[188:189], v247 offset:57344\n"
    "v_mfma_f32_16x16x32_bf16 a[236:239], a[160:163], v[156:159], a[236:239]\n"
    "s_waitcnt lgkmcnt(8)\n"
    "ds_read_b64_tr_b16 a[190:191], v247 offset:59392\n"
    "v_cvt_pk_bf16_f32 v173, v174, v175\n"
    "v_mfma_f32_16x16x32_bf16 v[0:3], a[168:171], v[156:159], v[0:3]\n"
    "buffer_load_dwordx4 v[88:91], v250, s[8:11], s62 offen offset:3072 sc1\n"
    "ds_write_addtid_b32 v172 offset:4096\n"
    "v_mfma_f32_16x16x32_bf16 v[0:3], a[172:175], v[160:163], v[0:3]\n"
    "s_add_u32 s62, s58, 8192\n"
    "ds_read_b64_tr_b16 a[160:161], v244 offset:28672\n"
    "ds_read_b64_tr_b16 a[162:163], v244 offset:30720\n"
    "v_mfma_f32_16x16x32_bf16 v[4:7], a[172:175], v[168:171], v[4:7]\n"
    "ds_read_b64_tr_b16 a[164:165], v244 offset:61440\n"
    "v_exp_f32_e32 v214, v214\n"
    "v_mfma_f32_16x16x32_bf16 v[4:7], a[168:171], v[164:167], v[4:7]\n"
    "s_waitcnt lgkmcnt(9)\n"
    "ds_read_b64_tr_b16 a[166:167], v244 offset:63488\n"
    "ds_read_b64_tr_b16 a[168:169], v245 offset:28672\n"
    "v_mfma_f32_16x16x32_bf16 v[24:27], a[176:179], v[164:167], v[24:27]\n"
    "ds_read_b64_tr_b16 a[170:171], v245 offset:30720\n"
    "v_exp_f32_e32 v215, v215\n"
    "v_mfma_f32_16x16x32_bf16 v[24:27], a[180:183], v[168:171], v[24:27]\n"
    "ds_read_b64_tr_b16 a[172:173], v245 offset:61440\n"
    "v_cvt_pk_bf16_f32 v212, v212, v213\n"
    "v_mfma_f32_16x16x32_bf16 v[20:23], a[180:183], v[160:163], v[20:23]\n"
    "v_exp_f32_e32 v176, v176\n"
    "v_mfma_f32_16x16x32_bf16 v[20:23], a[176:179], v[156:159], v[20:23]\n"
    "s_waitcnt lgkmcnt(8)\n"
    "ds_read_b64_tr_b16 a[174:175], v245 offset:63488\n"
    "v_exp_f32_e32 v177, v177\n"
    "v_mfma_f32_16x16x32_bf16 v[40:43], a[184:187], v[156:159], v[40:43]\n"
    "buffer_load_dwordx4 v[72:75], v250, s[8:11], s62 offen offset:0 sc1\n"
    "v_mfma_f32_16x16x32_bf16 v[40:43], a[188:191], v[160:163], v[40:43]\n"
    "ds_read_b64_tr_b16 a[176:177], v246 offset:28672\n"
    "ds_read_b64_tr_b16 a[178:179], v246 offset:30720\n"
    "v_mfma_f32_16x16x32_bf16 v[44:47], a[188:191], v[168:171], v[44:47]\n"
    "ds_write_addtid_b32 v197 offset:2304\n"
    "ds_write_addtid_b32 v204 offset:3072\n"
    "v_mfma_f32_16x16x32_bf16 v[44:47], a[184:187], v[164:167], v[44:47]\n"
    "s_waitcnt lgkmcnt(8)\n"
    "ds_read_b64_tr_b16 a[180:181], v246 offset:61440\n"
    "ds_write_addtid_b32 v173 offset:4352\n"
    "v_mfma_f32_16x16x32_bf16 v[64:67], a[160:163], v[164:167], v[64:67]\n"
    "ds_read_b64_tr_b16 a[182:183], v246 offset:63488\n"
    "ds_read_b64_tr_b16 a[184:185], v247 offset:28672\n"
    "v_mfma_f32_16x16x32_bf16 v[64:67], a[164:167], v[168:171], v[64:67]\n"
    "ds_read_b64_tr_b16 a[186:187], v247 offset:30720\n"
    "v_cvt_pk_bf16_f32 v213, v214, v215\n"
    "v_mfma_f32_16x16x32_bf16 v[60:63], a[164:167], v[160:163], v[60:63]\n"
    "ds_write_addtid_b32 v205 offset:3328\n"
    "ds_write_addtid_b32 v198 offset:2560\n"
    "v_mfma_f32_16x16x32_bf16 v[60:63], a[160:163], v[156:159], v[60:63]\n"
    "s_waitcnt lgkmcnt(11)\n"
    "ds_read_b64_tr_b16 a[188:189], v247 offset:61440\n"
    "ds_read_b64_tr_b16 a[190:191], v247 offset:63488\n"
    "v_mfma_f32_16x16x32_bf16 v[80:83], a[168:171], v[156:159], v[80:83]\n"
    "buffer_load_dwordx4 v[92:95], v250, s[8:11], s62 offen offset:1024 sc1\n"
    "v_mfma_f32_16x16x32_bf16 v[80:83], a[172:175], v[160:163], v[80:83]\n"
    "ds_write_addtid_b32 v212 offset:5120\n"
    "v_cvt_pk_bf16_f32 v206, v208, v209\n"
    "v_mfma_f32_16x16x32_bf16 v[84:87], a[172:175], v[168:171], v[84:87]\n"
    ".long 0xbedf00ff\n"
    ".Lat_w12:\n"
    "s_setpc_b64 s[2:3]\n"
    "ds_read_b128 a[160:163], v240 offset:16384\n"
    "ds_read_b128 a[164:167], v241 offset:16384\n"
    "v_mfma_f32_16x16x32_bf16 v[84:87], a[168:171], v[164:167], v[84:87]\n"
    "s_waitcnt lgkmcnt(9)\n"
    "v_mfma_f32_16x16x32_bf16 v[104:107], a[176:179], v[164:167], v[104:107]\n"
    "s_barrier\n"
    "v_mfma_f32_16x16x32_bf16 v[104:107], a[180:183], v[168:171], v[104:107]\n"
    "ds_write_addtid_b32 v213 offset:5376\n"
    "v_exp_f32_e32 v178, v178\n"
    "v_mfma_f32_16x16x32_bf16 v[100:103], a[180:183], v[160:163], v[100:103]\n"
    "s_add_u32 s31, s47, s35\n"
    "ds_read_b128 a[168:171], v240 offset:20480\n"
    "ds_write_addtid_b32 v199 offset:2816\n"
    "v_mfma_f32_16x16x32_bf16 v[100:103], a[176:179], v[156:159], v[100:103]\n"
    "s_waitcnt lgkmcnt(6)\n"
    "ds_read_b128 a[172:175], v241 offset:20480\n"
    "ds_write_addtid_b32 v206 offset:3584\n"
    "v_mfma_f32_16x16x32_bf16 v[120:123], a[184:187], v[156:159], v[120:123]\n"
    "s_add_u32 m0, s55, 24576\n"
    "v_exp_f32_e32 v179, v179\n"
    "v_mfma_f32_16x16x32_bf16 v[120:123], a[188:191], v[160:163], v[120:123]\n"
    "buffer_load_dwordx4 v251, s[8:11], s31 offen offset:0 sc1 lds\n"
    "v_mfma_f32_16x16x32_bf16 v[124:127], a[188:191], v[168:171], v[124:127]\n"
    "s_mov_b32 m0, s63\n"
    "v_cvt_pk_bf16_f32 v207, v210, v211\n"
    "v_cvt_pk_bf16_f32 v174, v176, v177\n"
    "v_mfma_f32_16x16x32_bf16 v[124:127], a[184:187], v[164:167], v[124:127]\n"
    "s_waitcnt lgkmcnt(5)\n"
    "ds_read_b128 a[176:179], v242 offset:16384\n"
    "ds_read_b128 a[180:183], v243 offset:16384\n"
    "v_mfma_f32_16x16x32_bf16 v[148:151], a[160:163], a[32:35], v[224:227]\n"
    "buffer_load_dwordx4 v[76:79], v250, s[8:11], s62 offen offset:2048 sc1\n"
    "v_mfma_f32_16x16x32_bf16 v[148:151], a[164:167], a[36:39], v[148:151]\n"
    "s_add_u32 s31, s31, 0x1000\n"
    "v_exp_f32_e32 v216, v216\n"
    "v_mfma_f32_16x16x32_bf16 v[140:143], a[164:167], a[4:7], v[220:223]\n"
    "s_add_u32 m0, s55, 25600\n"
    "ds_read_b128 a[184:187], v242 offset:20480\n"
    "ds_read_b128 a[188:191], v243 offset:20480\n"
    "v_mfma_f32_16x16x32_bf16 v[140:143], a[160:163], a[0:3], v[140:143]\n"
    "s_waitcnt lgkmcnt(5)\n"
    "v_exp_f32_e32 v217, v217\n"
    "v_mfma_f32_16x16x32_bf16 v[140:143], a[168:171], a[8:11], v[140:143]\n"
    "buffer_load_dwordx4 v251, s[8:11], s31 offen offset:0 sc1 lds\n"
    "v_mfma_f32_16x16x32_bf16 v[140:143], a[172:175], a[12:15], v[140:143]\n"
    "s_mov_b32 m0, s63\n"
    "ds_read_b128 a[160:163], v240 offset:18432\n"
    "v_cvt_pk_bf16_f32 v175, v178, v179\n"
    "v_mfma_f32_16x16x32_bf16 v[148:151], a[172:175], a[44:47], v[148:151]\n"
    "v_exp_f32_e32 v218, v218\n"
    "v_mfma_f32_16x16x32_bf16 v[148:151], a[168:171], a[40:43], v[148:151]\n"
    "s_waitcnt lgkmcnt(3)\n"
    "ds_write_addtid_b32 v174 offset:4608\n"
    "v_exp_f32_e32 v219, v219\n"
    "v_mfma_f32_16x16x32_bf16 v[148:151], a[176:179], a[48:51], v[148:151]\n"
    "s_add_u32 s31, s47, s36\n"
    "ds_read_b128 a[164:167], v241 offset:18432\n"
    "ds_write_addtid_b32 v175 offset:4864\n"
    "v_mfma_f32_16x16x32_bf16 v[148:151], a[180:183], a[52:55], v[148:151]\n"
    "s_add_u32 m0, s55, 26624\n"
    "ds_read_b128 a[168:171], v240 offset:22528\n"
    "v_cvt_pk_bf16_f32 v214, v216, v217\n"
    "v_mfma_f32_16x16x32_bf16 v[140:143], a[180:183], a[20:23], v[140:143]\n"
    "buffer_load_dwordx4 v251, s[8:11], s31 offen offset:0 sc1 lds\n"
    "v_mfma_f32_16x16x32_bf16 v[140:143], a[176:179], a[16:19], v[140:143]\n"
    "s_waitcnt lgkmcnt(5)\n"
    "ds_read_b128 a[176:179], v242 offset:18432\n"
    "ds_read_b128 a[180:183], v243 offset:18432\n"
    "v_mfma_f32_16x16x32_bf16 v[140:143], a[184:187], a[24:27], v[140:143]\n"
    "s_mov_b32 m0, s63\n"
    "ds_read_b128 a[172:175], v241 offset:22528\n"
    "v_cvt_pk_bf16_f32 v215, v218, v219\n"
    "v_mfma_f32_16x16x32_bf16 v[140:143], a[188:191], a[28:31], v[140:143]\n"
    "buffer_load_dword v253, v238, s[8:11], s48 offen offset:0 sc1\n"
    "ds_write_addtid_b32 v214 offset:5632\n"
    "v_mfma_f32_16x16x32_bf16 v[148:151], a[188:191], a[60:63], v[148:151]\n"
    "s_add_u32 s31, s31, 0x1000\n"
    "ds_write_addtid_b32 v207 offset:3840\n"
    "ds_write_addtid_b32 v215 offset:5888\n"
    "v_mfma_f32_16x16x32_bf16 v[148:151], a[184:187], a[56:59], v[148:151]\n"
    "s_waitcnt lgkmcnt(8)\n"
    "ds_read_b128 a[184:187], v242 offset:22528\n"
    "ds_read_b128 a[188:191], v243 offset:22528\n"
    "v_mfma_f32_16x16x32_bf16 v[152:155], a[160:163], a[32:35], v[224:227]\n"
    "s_add_u32 m0, s55, 27648\n"
    "v_exp_f32_e32 v140, v140\n"
    "v_mfma_f32_16x16x32_bf16 v[152:155], a[164:167], a[36:39], v[152:155]\n"
    "s_waitcnt lgkmcnt(3)\n"
    "s_waitcnt lgkmcnt(2)\n"
    "v_mfma_f32_16x16x32_bf16 v[144:147], a[164:167], a[4:7], v[220:223]\n"
    "s_barrier\n"
    "v_mfma_f32_16x16x32_bf16 v[144:147], a[160:163], a[0:3], v[144:147]\n"
    "ds_read2st64_b32 v[200:201], v249 offset0:8 offset1:9\n"
    "v_exp_f32_e32 v141, v141\n"
    "v_mfma_f32_16x16x32_bf16 v[144:147], a[168:171], a[8:11], v[144:147]\n"
    "buffer_load_dwordx4 v251, s[8:11], s31 offen offset:0 sc1 lds\n"
    "ds_read_b64_tr_b16 a[160:161], v244 offset:0\n"
    "v_mfma_f32_16x16x32_bf16 v[144:147], a[172:175], a[12:15], v[144:147]\n"
    "s_mov_b32 m0, s63\n"
    "ds_read_b64_tr_b16 a[162:163], v244 offset:2048\n"
    "ds_read_b64_tr_b16 a[164:165], v244 offset:32768\n"
    "ds_read2st64_b32 v[202:203], v249 offset0:10 offset1:11\n"
    "v_mfma_f32_16x16x32_bf16 v[152:155], a[172:175], a[44:47], v[152:155]\n"
    "s_add_u32 s87, s59, 8192\n"
    "ds_read_b64_tr_b16 a[166:167], v244 offset:34816\n"
    "v_exp_f32_e32 v142, v142\n"
    "v_mfma_f32_16x16x32_bf16 v[152:155], a[168:171], a[40:43], v[152:155]\n"
    "ds_read_b64_tr_b16 a[168:169], v245 offset:0\n"
    "v_exp_f32_e32 v143, v143\n"
    "v_mfma_f32_16x16x32_bf16 v[152:155], a[176:179], a[48:51], v[152:155]\n"
    "s_add_u32 s31, s47, s37\n"
    "ds_read2st64_b32 v[208:209], v249 offset0:12 offset1:13\n"
    "ds_read2st64_b32 v[210:211], v249 offset0:14 offset1:15\n"
    "v_mfma_f32_16x16x32_bf16 v[152:155], a[180:183], a[52:55], v[152:155]\n"
    "s_add_u32 m0, s55, 28672\n"
    "ds_read_b64_tr_b16 a[170:171], v245 offset:2048\n"
    "v_cvt_pk_bf16_f32 v140, v140, v141\n"
    "v_mfma_f32_16x16x32_bf16 v[144:147], a[180:183], a[20:23], v[144:147]\n"
    "buffer_load_dwordx4 v251, s[8:11], s31 offen offset:0 sc1 lds\n"
    "ds_read_b64_tr_b16 a[172:173], v245 offset:32768\n"
    "v_mfma_f32_16x16x32_bf16 v[144:147], a[176:179], a[16:19], v[144:147]\n"
    "s_waitcnt lgkmcnt(11)\n"
    "ds_read_b64_tr_b16 a[174:175], v245 offset:34816\n"
    "v_exp_f32_e32 v148, v148\n"
    "v_mfma_f32_16x16x32_bf16 v[144:147], a[184:187], a[24:27], v[144:147]\n"
    "s_mov_b32 m0, s63\n"
    "ds_read_b64_tr_b16 a[176:177], v246 offset:0\n"
    "v_exp_f32_e32 v149, v149\n"
    "v_mfma_f32_16x16x32_bf16 v[144:147], a[188:191], a[28:31], v[144:147]\n"
    "s_add_u32 s88, s59, 16384\n"
    "ds_read_b64_tr_b16 a[178:179], v246 offset:2048\n"
    "v_cvt_pk_bf16_f32 v141, v142, v143\n"
    "v_mfma_f32_16x16x32_bf16 v[152:155], a[188:191], a[60:63], v[152:155]\n"
    "s_add_u32 s31, s31, 0x1000\n"
    "ds_read_b64_tr_b16 a[180:181], v246 offset:32768\n"
    "v_exp_f32_e32 v150, v150\n"
    "v_mfma_f32_16x16x32_bf16 v[152:155], a[184:187], a[56:59], v[152:155]\n"
    "s_waitcnt lgkmcnt(6)\n"
    "ds_read_b64_tr_b16 a[182:183], v246 offset:34816\n"
    "ds_write_addtid_b32 v140 offset:0\n"
    "v_mfma_f32_16x16x32_bf16 a[240:243], a[160:163], v[204:207], a[240:243]\n"
    "s_add_u32 m0, s55, 29696\n"
    "ds_read_b64_tr_b16 a[184:185], v247 offset:0\n"
    "v_exp_f32_e32 v151, v151\n"
    "v_mfma_f32_16x16x32_bf16 a[240:243], a[164:167], v[208:211], a[240:243]\n"
    "buffer_load_dwordx4 v251, s[8:11], s31 offen offset:0 sc1 lds\n"
    "ds_read_b64_tr_b16 a[186:187], v247 offset:2048\n"
    "v_mfma_f32_16x16x32_bf16 a[236:239], a[164:167], v[200:203], a[236:239]\n"
    "s_mov_b32 m0, s63\n"
    "ds_read_b64_tr_b16 a[188:189], v247 offset:32768\n"
    "v_cvt_pk_bf16_f32 v148, v148, v149\n"
    "v_mfma_f32_16x16x32_bf16 a[236:239], a[160:163], v[196:199], a[236:239]\n"
    "s_waitcnt lgkmcnt(8)\n"
    "ds_read_b64_tr_b16 a[160:161], v244 offset:4096\n"
    "v_exp_f32_e32 v144, v144\n"
    "v_mfma_f32_16x16x32_bf16 v[0:3], a[168:171], v[196:199], v[0:3]\n"
    "s_add_u32 s89, s59, 24576\n"
    "ds_read_b64_tr_b16 a[190:191], v247 offset:34816\n"
    "ds_write_addtid_b32 v141 offset:256\n"
    "v_mfma_f32_16x16x32_bf16 v[0:3], a[172:175], v[200:203], v[0:3]\n"
    "s_add_u32 s31, s46, s36\n"
    "ds_write_addtid_b32 v148 offset:1024\n"
    "v_exp_f32_e32 v145, v145\n"
    "v_mfma_f32_16x16x32_bf16 v[4:7], a[172:175], v[208:211], v[4:7]\n"
    "s_add_u32 m0, s55, 30720\n"
    "ds_read_b64_tr_b16 a[162:163], v244 offset:6144\n"
    "v_cvt_pk_bf16_f32 v149, v150, v151\n"
    "v_mfma_f32_16x16x32_bf16 v[4:7], a[168:171], v[204:207], v[4:7]\n"
    "s_waitcnt lgkmcnt(9)\n"
    "ds_read_b64_tr_b16 a[164:165], v244 offset:36864\n"
    "ds_read_b64_tr_b16 a[166:167], v244 offset:38912\n"
    "v_mfma_f32_16x16x32_bf16 v[24:27], a[176:179], v[204:207], v[24:27]\n"
    "buffer_load_dwordx4 v251, s[12:15], s31 offen offset:0 lds\n"
    "ds_read_b64_tr_b16 a[168:169], v245 offset:4096\n"
    "v_mfma_f32_16x16x32_bf16 v[24:27], a[180:183], v[208:211], v[24:27]\n"
    "s_mov_b32 m0, s63\n"
    "ds_read_b64_tr_b16 a[170:171], v245 offset:6144\n"
    "v_exp_f32_e32 v146, v146\n"
    "v_mfma_f32_16x16x32_bf16 v[20:23], a[180:183], v[200:203], v[20:23]\n"
    "ds_read_b64_tr_b16 a[172:173], v245 offset:36864\n"
    "ds_read_b64_tr_b16 a[174:175], v245 offset:38912\n"
    "v_mfma_f32_16x16x32_bf16 v[20:23], a[176:179], v[196:199], v[20:23]\n"
    "s_waitcnt lgkmcnt(9)\n"
    "ds_read_b64_tr_b16 a[176:177], v246 offset:4096\n"
    "ds_read2st64_b32 v[176:177], v249 offset0:16 offset1:17\n"
    "v_mfma_f32_16x16x32_bf16 v[40:43], a[184:187], v[196:199], v[40:43]\n"
    "s_add_u32 s31, s31, 0x1000\n"
    "ds_read2st64_b32 v[178:179], v249 offset0:18 offset1:19\n"
    "v_exp_f32_e32 v147, v147\n"
    "v_mfma_f32_16x16x32_bf16 v[40:43], a[188:191], v[200:203], v[40:43]\n"
    "s_add_u32 m0, s55, 31744\n"
    "ds_read_b64_tr_b16 a[178:179], v246 offset:6144\n"
    "ds_read2st64_b32 v[216:217], v249 offset0:20 offset1:21\n"
    "v_mfma_f32_16x16x32_bf16 v[44:47], a[188:191], v[208:211], v[44:47]\n"
    "buffer_load_dwordx4 v251, s[12:15], s31 offen offset:0 lds\n"
    "ds_read_b64_tr_b16 a[180:181], v246 offset:36864\n"
    "v_mfma_f32_16x16x32_bf16 v[44:47], a[184:187], v[204:207], v[44:47]\n"
    "s_waitcnt lgkmcnt(10)\n"
    "ds_read_b64_tr_b16 a[182:183], v246 offset:38912\n"
    "ds_read2st64_b32 v[218:219], v249 offset0:22 offset1:23\n"
    "v_mfma_f32_16x16x32_bf16 v[64:67], a[160:163], v[204:207], v[64:67]\n"
    "s_mov_b32 m0, s63\n"
    "ds_read_b64_tr_b16 a[184:185], v247 offset:4096\n"
    "v_cvt_pk_bf16_f32 v142, v144, v145\n"
    "v_mfma_f32_16x16x32_bf16 v[64:67], a[164:167], v[208:211], v[64:67]\n"
    "s_waitcnt vmcnt(5)\n"
    "ds_read_b64_tr_b16 a[186:187], v247 offset:6144\n"
    "v_subrev_u32_e32 v253, s39, v253\n"
    "v_subrev_u32_e32 v253, 0, v253\n"
    "v_mfma_f32_16x16x32_bf16 v[60:63], a[164:167], v[200:203], v[60:63]\n"
    "ds_read_b64_tr_b16 a[188:189], v247 offset:36864\n"
    "v_cmp_le_u32_e32 vcc, 1, v253\n"
    "v_min_u32_e32 v220, 0x13c, v248\n"
    "v_mfma_f32_16x16x32_bf16 v[60:63], a[160:163], v[196:199], v[60:63]\n"
    "s_waitcnt lgkmcnt(11)\n"
    "ds_read_b64_tr_b16 a[190:191], v247 offset:38912\n"
    "v_exp_f32_e32 v152, v152\n"
    "v_mfma_f32_16x16x32_bf16 v[80:83], a[168:171], v[196:199], v[80:83]\n"
    "ds_read_b64_tr_b16 a[160:161], v244 offset:12288\n"
    "ds_read_b64_tr_b16 a[162:163], v244 offset:14336\n"
    "ds_read_b64_tr_b16 a[164:165], v244 offset:45056\n"
    "v_mfma_f32_16x16x32_bf16 v[80:83], a[172:175], v[200:203], v[80:83]\n"
    "s_cbranch_vccnz .Lat_k\n"
    ".Lat_kr:\n"
    "s_cmp_lg_u32 s56, 0\n"
    "s_cbranch_scc1 .Lat_kx\n"
    ".Lat_kxr:\n"
    "v_mfma_f32_16x16x32_bf16 v[84:87], a[172:175], v[208:211], v[84:87]\n"
    "buffer_load_dwordx4 a[244:247], v250, s[8:11], s87 offen offset:0 sc1\n"
    "ds_read_b64_tr_b16 a[166:167], v244 offset:47104\n"
    "v_mfma_f32_16x16x32_bf16 v[84:87], a[168:171], v[204:207], v[84:87]\n"
    "s_waitcnt lgkmcnt(9)\n"
    "ds_write_addtid_b32 v149 offset:1280\n"
    "v_exp_f32_e32 v153, v153\n"
    "v_mfma_f32_16x16x32_bf16 v[104:107], a[176:179], v[204:207], v[104:107]\n"
    "v_cvt_pk_bf16_f32 v143, v146, v147\n"
    "v_exp_f32_e32 v154, v154\n"
    "v_mfma_f32_16x16x32_bf16 v[104:107], a[180:183], v[208:211], v[104:107]\n"
    "buffer_load_dwordx4 v[96:99], v250, s[8:11], s62 offen offset:3072 sc1\n"
    "ds_read_b64_tr_b16 a[168:169], v245 offset:12288\n"
    "v_mfma_f32_16x16x32_bf16 v[100:103], a[180:183], v[200:203], v[100:103]\n"
    "s_add_u32 s62, s58, 12288\n"
    "ds_read_b64_tr_b16 a[170:171], v245 offset:14336\n"
    "ds_read_b64_tr_b16 a[172:173], v245 offset:45056\n"
    "v_mfma_f32_16x16x32_bf16 v[100:103], a[176:179], v[196:199], v[100:103]\n"
    "s_waitcnt lgkmcnt(8)\n"
    "ds_read_b64_tr_b16 a[176:177], v246 offset:12288\n"
    "ds_write_addtid_b32 v142 offset:512\n"
    "v_mfma_f32_16x16x32_bf16 v[120:123], a[184:187], v[196:199], v[120:123]\n"
    "ds_read_b64_tr_b16 a[174:175], v245 offset:47104\n"
    "ds_read_b64_tr_b16 a[178:179], v246 offset:14336\n"
    "v_mfma_f32_16x16x32_bf16 v[120:123], a[188:191], v[200:203], v[120:123]\n"
    "ds_write_addtid_b32 v143 offset:768\n"
    "v_exp_f32_e32 v155, v155\n"
    "v_mfma_f32_16x16x32_bf16 v[124:127], a[188:191], v[208:211], v[124:127]\n"
    "buffer_load_dwordx4 a[248:251], v250, s[8:11], s88 offen offset:0 sc1\n"
    "ds_read_b64_tr_b16 a[180:181], v246 offset:45056\n"
    "v_mfma_f32_16x16x32_bf16 v[124:127], a[184:187], v[204:207], v[124:127]\n"
    "s_waitcnt lgkmcnt(10)\n"
    "ds_read_b64_tr_b16 a[184:185], v247 offset:12288\n"
    "v_cvt_pk_bf16_f32 v150, v152, v153\n"
    "v_mfma_f32_16x16x32_bf16 v[64:67], a[160:163], v[212:215], v[64:67]\n"
    "ds_read_b64_tr_b16 a[182:183], v246 offset:47104\n"
    "v_cvt_pk_bf16_f32 v151, v154, v155\n"
    "v_mfma_f32_16x16x32_bf16 v[64:67], a[164:167], v[216:219], v[64:67]\n"
    "buffer_load_dwordx4 v[108:111], v250, s[8:11], s62 offen offset:0 sc1\n"
    "ds_read_b64_tr_b16 a[186:187], v247 offset:14336\n"
    "v_mfma_f32_16x16x32_bf16 v[60:63], a[164:167], v[176:179], v[60:63]\n"
    "ds_read_b64_tr_b16 a[188:189], v247 offset:45056\n"
    "ds_read_b64_tr_b16 a[190:191], v247 offset:47104\n"
    "v_mfma_f32_16x16x32_bf16 v[60:63], a[160:163], v[172:175], v[60:63]\n"
    "s_waitcnt lgkmcnt(8)\n"
    "ds_write_addtid_b32 v150 offset:1536\n"
    "ds_write_addtid_b32 v151 offset:1792\n"
    "v_mfma_f32_16x16x32_bf16 v[80:83], a[168:171], v[172:175], v[80:83]\n"
    "ds_read_b64_tr_b16 a[160:161], v244 offset:8192\n"
    "v_add_u32_e32 v240, s45, v240\n"
    "v_mfma_f32_16x16x32_bf16 v[80:83], a[172:175], v[176:179], v[80:83]\n"
    "ds_read_b64_tr_b16 a[162:163], v244 offset:10240\n"
    "ds_read_b64_tr_b16 a[164:165], v244 offset:40960\n"
    "v_mfma_f32_16x16x32_bf16 v[84:87], a[172:175], v[216:219], v[84:87]\n"
    "buffer_load_dwordx4 a[252:255], v250, s[8:11], s89 offen offset:0 sc1\n"
    "ds_read_b64_tr_b16 a[166:167], v244 offset:43008\n"
    "v_mfma_f32_16x16x32_bf16 v[84:87], a[168:171], v[212:215], v[84:87]\n"
    "s_waitcnt lgkmcnt(9)\n"
    "s_waitcnt lgkmcnt(4)\n"
    "s_waitcnt vmcnt(7)\n"
    "v_mfma_f32_16x16x32_bf16 v[104:107], a[176:179], v[212:215], v[104:107]\n"
    "s_barrier\n"
    "v_mfma_f32_16x16x32_bf16 v[104:107], a[180:183], v[216:219], v[104:107]\n"
    "buffer_load_dwordx4 v[112:115], v250, s[8:11], s62 offen offset:1024 sc1\n"
    "ds_read2st64_b32 v[144:145], v249 offset0:0 offset1:1\n"
    "v_mfma_f32_16x16x32_bf16 v[100:103], a[180:183], v[176:179], v[100:103]\n"
    "s_add_u32 s31, s46, s37\n"
    "ds_read2st64_b32 v[146:147], v249 offset0:2 offset1:3\n"
    "ds_read2st64_b32 v[152:153], v249 offset0:4 offset1:5\n"
    "v_mfma_f32_16x16x32_bf16 v[100:103], a[176:179], v[172:175], v[100:103]\n"
    "ds_read_b64_tr_b16 a[168:169], v244 offset:16384\n"
    "ds_read_b64_tr_b16 a[170:171], v244 offset:18432\n"
    "v_mfma_f32_16x16x32_bf16 v[120:123], a[184:187], v[172:175], v[120:123]\n"
    "s_add_u32 m0, s55, 4096\n"
    "ds_read_b64_tr_b16 a[172:173], v244 offset:49152\n"
    "ds_read_b64_tr_b16 a[174:175], v244 offset:51200\n"
    "v_mfma_f32_16x16x32_bf16 v[120:123], a[188:191], v[176:179], v[120:123]\n"
    "buffer_load_dwordx4 v251, s[12:15], s31 offen offset:0 lds\n"
    "ds_read2st64_b32 v[154:155], v249 offset0:6 offset1:7\n"
    "v_mfma_f32_16x16x32_bf16 v[124:127], a[188:191], v[216:219], v[124:127]\n"
    "s_mov_b32 m0, s63\n"
    "ds_read_b64_tr_b16 a[176:177], v245 offset:8192\n"
    "ds_read_b64_tr_b16 a[178:179], v245 offset:10240\n"
    "v_mfma_f32_16x16x32_bf16 v[124:127], a[184:187], v[212:215], v[124:127]\n"
    "s_waitcnt lgkmcnt(10)\n"
    "ds_read_b64_tr_b16 a[180:181], v245 offset:40960\n"
    "ds_read_b64_tr_b16 a[182:183], v245 offset:43008\n"
    "v_mfma_f32_16x16x32_bf16 a[240:243], a[160:163], v[212:215], a[240:243]\n"
    "buffer_load_dwordx4 v[8:11], v250, s[8:11], s87 offen offset:1024 sc1\n"
    "v_mfma_f32_16x16x32_bf16 a[240:243], a[164:167], v[216:219], a[240:243]\n"
    "s_add_u32 s31, s31, 0x1000\n"
    "ds_read_b64_tr_b16 a[184:185], v245 offset:16384\n"
    "v_xor_b32_e32 v241, 64, v240\n"
    "v_mfma_f32_16x16x32_bf16 a[236:239], a[164:167], v[176:179], a[236:239]\n"
    "s_add_u32 m0, s55, 5120\n"
    "ds_read_b64_tr_b16 a[186:187], v245 offset:18432\n"
    "ds_read_b64_tr_b16 a[188:189], v245 offset:49152\n"
    "v_mfma_f32_16x16x32_bf16 a[236:239], a[160:163], v[172:175], a[236:239]\n"
    "s_waitcnt lgkmcnt(7)\n"
    "ds_read_b64_tr_b16 a[190:191], v245 offset:51200\n"
    "v_add_u32_e32 v242, 0x10000, v240\n"
    "v_mfma_f32_16x16x32_bf16 a[236:239], a[168:171], v[140:143], a[236:239]\n"
    "buffer_load_dwordx4 v251, s[12:15], s31 offen offset:0 lds\n"
    "v_mfma_f32_16x16x32_bf16 a[236:239], a[172:175], v[144:147], a[236:239]\n"
    "s_mov_b32 m0, s63\n"
    "ds_read_b64_tr_b16 a[160:161], v246 offset:8192\n"
    "v_add_u32_e32 v243, 0x10000, v241\n"
    "v_mfma_f32_16x16x32_bf16 a[240:243], a[172:175], v[152:155], a[240:243]\n"
    "ds_read_b64_tr_b16 a[162:163], v246 offset:10240\n"
    "ds_read_b64_tr_b16 a[164:165], v246 offset:40960\n"
    "v_mfma_f32_16x16x32_bf16 a[240:243], a[168:171], v[148:151], a[240:243]\n"
    "s_waitcnt lgkmcnt(7)\n"
    "ds_read_b64_tr_b16 a[166:167], v246 offset:43008\n"
    "v_add_u32_e32 v252, s54, v240\n"
    "v_mfma_f32_16x16x32_bf16 v[4:7], a[176:179], v[212:215], v[4:7]\n"
    "buffer_load_dwordx4 v[12:15], v250, s[8:11], s88 offen offset:1024 sc1\n"
    "v_mfma_f32_16x16x32_bf16 v[4:7], a[180:183], v[216:219], v[4:7]\n"
    "ds_read_b64_tr_b16 a[168:169], v246 offset:16384\n"
    "v_mfma_f32_16x16x32_bf16 v[0:3], a[180:183], v[176:179], v[0:3]\n"
    "ds_read_b64_tr_b16 a[170:171], v246 offset:18432\n"
    "v_xor_b32_e32 v255, 64, v252\n"
    "v_mfma_f32_16x16x32_bf16 v[0:3], a[176:179], v[172:175], v[0:3]\n"
    "s_waitcnt lgkmcnt(6)\n"
    "ds_read_b64_tr_b16 a[172:173], v246 offset:49152\n"
    "ds_read_b64_tr_b16 a[174:175], v246 offset:51200\n"
    "v_mfma_f32_16x16x32_bf16 v[0:3], a[184:187], v[140:143], v[0:3]\n"
    "buffer_load_dwordx4 v[132:135], v250, s[8:11], s62 offen offset:2048 sc1\n"
    "v_mfma_f32_16x16x32_bf16 v[0:3], a[188:191], v[144:147], v[0:3]\n"
    "ds_read_b64_tr_b16 a[176:177], v247 offset:8192\n"
    "ds_read_b64_tr_b16 a[178:179], v247 offset:10240\n"
    "v_mfma_f32_16x16x32_bf16 v[4:7], a[188:191], v[152:155], v[4:7]\n"
    "ds_read_b64_tr_b16 a[180:181], v247 offset:40960\n"
    "v_lshlrev_b32_e32 v228, 16, v228\n"
    "v_mfma_f32_16x16x32_bf16 v[4:7], a[184:187], v[148:151], v[4:7]\n"
    "s_waitcnt lgkmcnt(7)\n"
    "ds_read_b64_tr_b16 a[182:183], v247 offset:43008\n"
    "v_lshlrev_b32_e32 v229, 16, v229\n"
    "v_mfma_f32_16x16x32_bf16 v[24:27], a[160:163], v[212:215], v[24:27]\n"
    "buffer_load_dwordx4 v[16:19], v250, s[8:11], s89 offen offset:1024 sc1\n"
    "v_mfma_f32_16x16x32_bf16 v[24:27], a[164:167], v[216:219], v[24:27]\n"
    "ds_read_b64_tr_b16 a[184:185], v247 offset:16384\n"
    "v_cvt_pk_bf16_f32 v7, v6, v7\n"
    "v_mfma_f32_16x16x32_bf16 v[20:23], a[164:167], v[176:179], v[20:23]\n"
    "ds_read_b64_tr_b16 a[186:187], v247 offset:18432\n"
    "ds_read_b64_tr_b16 a[188:189], v247 offset:49152\n"
    "v_mfma_f32_16x16x32_bf16 v[20:23], a[160:163], v[172:175], v[20:23]\n"
    "s_waitcnt lgkmcnt(7)\n"
    "ds_read_b64_tr_b16 a[190:191], v247 offset:51200\n"
    "ds_read_b64_tr_b16 a[160:161], v244 offset:20480\n"
    "v_mfma_f32_16x16x32_bf16 v[20:23], a[168:171], v[140:143], v[20:23]\n"
    "buffer_load_dwordx4 v[116:119], v250, s[8:11], s62 offen offset:3072 sc1\n"
    "v_mfma_f32_16x16x32_bf16 v[20:23], a[172:175], v[144:147], v[20:23]\n"
    "s_add_u32 s62, s58, 16384\n"
    "ds_read_b64_tr_b16 a[162:163], v244 offset:22528\n"
    "ds_read_b64_tr_b16 a[164:165], v244 offset:53248\n"
    "v_mfma_f32_16x16x32_bf16 v[24:27], a[172:175], v[152:155], v[24:27]\n"
    "ds_read_b64_tr_b16 a[166:167], v244 offset:55296\n"
    "v_cvt_pk_bf16_f32 v6, v4, v5\n"
    "v_mfma_f32_16x16x32_bf16 v[24:27], a[168:171], v[148:151], v[24:27]\n"
    "s_waitcnt lgkmcnt(8)\n"
    "ds_read_b64_tr_b16 a[168:169], v245 offset:20480\n"
    "ds_read_b64_tr_b16 a[170:171], v245 offset:22528\n"
    "v_mfma_f32_16x16x32_bf16 v[44:47], a[176:179], v[212:215], v[44:47]\n"
    "buffer_load_dwordx4 a[192:195], v250, s[8:11], s87 offen offset:2048 sc1\n"
    "v_mfma_f32_16x16x32_bf16 v[44:47], a[180:183], v[216:219], v[44:47]\n"
    "ds_read_b64_tr_b16 a[172:173], v245 offset:53248\n"
    "ds_read_b64_tr_b16 a[174:175], v245 offset:55296\n"
    "v_mfma_f32_16x16x32_bf16 v[40:43], a[180:183], v[176:179], v[40:43]\n"
    "v_accvgpr_read_b32 v4, a240\n"
    "v_accvgpr_read_b32 v5, a241\n"
    "v_mfma_f32_16x16x32_bf16 v[40:43], a[176:179], v[172:175], v[40:43]\n"
    "s_waitcnt lgkmcnt(8)\n"
    "ds_read_b64_tr_b16 a[176:177], v246 offset:20480\n"
    "v_lshlrev_b32_e32 v230, 16, v230\n"
    "v_mfma_f32_16x16x32_bf16 v[40:43], a[184:187], v[140:143], v[40:43]\n"
    "buffer_load_dwordx4 v[136:139], v250, s[8:11], s62 offen offset:0 sc1\n"
    "ds_read_b64_tr_b16 a[178:179], v246 offset:22528\n"
    "v_mfma_f32_16x16x32_bf16 v[40:43], a[188:191], v[144:147], v[40:43]\n"
    "ds_read_b64_tr_b16 a[180:181], v246 offset:53248\n"
    "ds_read_b64_tr_b16 a[182:183], v246 offset:55296\n"
    "v_mfma_f32_16x16x32_bf16 v[44:47], a[188:191], v[152:155], v[44:47]\n"
    "v_accvgpr_read_b32 v225, a243\n"
    "v_cvt_pk_bf16_f32 v4, v4, v5\n"
    "v_mfma_f32_16x16x32_bf16 v[44:47], a[184:187], v[148:151], v[44:47]\n"
    "s_waitcnt lgkmcnt(8)\n"
    "ds_read_b64_tr_b16 a[184:185], v247 offset:20480\n"
    "ds_read_b64_tr_b16 a[186:187], v247 offset:22528\n"
    "v_mfma_f32_16x16x32_bf16 v[64:67], a[160:163], v[148:151], v[64:67]\n"
    "buffer_load_dwordx4 a[196:199], v250, s[8:11], s88 offen offset:2048 sc1\n"
    "v_mfma_f32_16x16x32_bf16 v[64:67], a[164:167], v[152:155], v[64:67]\n"
    "s_waitcnt lgkmcnt(6)\n"
    "ds_read_b64_tr_b16 a[188:189], v247 offset:53248\n"
    "ds_read_b64_tr_b16 a[190:191], v247 offset:55296\n"
    "v_mfma_f32_16x16x32_bf16 v[84:87], a[172:175], v[152:155], v[84:87]\n"
    "ds_read_b128 a[48:51], v240 offset:0\n"
    "v_accvgpr_read_b32 v5, a242\n"
    "v_mfma_f32_16x16x32_bf16 v[84:87], a[168:171], v[148:151], v[84:87]\n"
    "s_waitcnt lgkmcnt(5)\n"
    "ds_read_b128 a[52:55], v241 offset:0\n"
    "v_cvt_pk_bf16_f32 v5, v5, v225\n"
    "v_mfma_f32_16x16x32_bf16 v[104:107], a[176:179], v[148:151], v[104:107]\n"
    "buffer_store_dwordx4 v[4:7], v250, s[8:11], s80 offen offset:0 sc1\n"
    "v_mfma_f32_16x16x32_bf16 v[104:107], a[180:183], v[152:155], v[104:107]\n"
    "s_waitcnt lgkmcnt(2)\n"
    "v_cvt_pk_bf16_f32 v24, v24, v25\n"
    "v_cvt_pk_bf16_f32 v25, v26, v27\n"
    "v_mfma_f32_16x16x32_bf16 v[124:127], a[188:191], v[152:155], v[124:127]\n"
    "ds_read_b128 a[56:59], v240 offset:32768\n"
    "v_cvt_pk_bf16_f32 v26, v44, v45\n"
    "v_mfma_f32_16x16x32_bf16 v[124:127], a[184:187], v[148:151], v[124:127]\n"
    "ds_read_b128 a[60:63], v241 offset:32768\n"
    "v_cvt_pk_bf16_f32 v27, v46, v47\n"
    "v_mfma_f32_16x16x32_bf16 v[60:63], a[164:167], v[144:147], v[60:63]\n"
    "buffer_load_dwordx4 a[200:203], v250, s[8:11], s89 offen offset:2048 sc1\n"
    "v_mfma_f32_16x16x32_bf16 v[60:63], a[160:163], v[140:143], v[60:63]\n"
    "buffer_store_dwordx4 v[24:27], v250, s[8:11], s80 offen offset:1024 sc1\n"
    "v_mfma_f32_16x16x32_bf16 v[80:83], a[168:171], v[140:143], v[80:83]\n"
    "v_cvt_pk_bf16_f32 v64, v64, v65\n"
    "v_cvt_pk_bf16_f32 v65, v66, v67\n"
    "v_mfma_f32_16x16x32_bf16 v[80:83], a[172:175], v[144:147], v[80:83]\n"
    "v_cvt_pk_bf16_f32 v66, v84, v85\n"
    "v_cvt_pk_bf16_f32 v67, v86, v87\n"
    "v_mfma_f32_16x16x32_bf16 v[100:103], a[180:183], v[144:147], v[100:103]\n"
    "buffer_store_dwordx4 v[64:67], v250, s[8:11], s80 offen offset:2048 sc1\n"
    "v_mfma_f32_16x16x32_bf16 v[100:103], a[176:179], v[140:143], v[100:103]\n"
    "s_mov_b32 s45, 0x71800000\n"
    "v_cvt_pk_bf16_f32 v104, v104, v105\n"
    "v_cvt_pk_bf16_f32 v105, v106, v107\n"
    "v_mfma_f32_16x16x32_bf16 v[120:123], a[184:187], v[140:143], v[120:123]\n"
    "v_cvt_pk_bf16_f32 v106, v124, v125\n"
    "v_cvt_pk_bf16_f32 v107, v126, v127\n"
    "v_mfma_f32_16x16x32_bf16 v[120:123], a[188:191], v[144:147], v[120:123]\n"
    "s_waitcnt lgkmcnt(2)\n"
    "buffer_store_dwordx4 v[104:107], v250, s[8:11], s80 offen offset:3072 sc1\n"
    "v_mfma_f32_16x16x32_bf16 v[156:159], a[216:219], a[48:51], 0\n"
    "buffer_load_dwordx4 a[204:207], v250, s[8:11], s87 offen offset:3072 sc1\n"
    "ds_read_b128 a[160:163], v242 offset:0\n"
    "v_mfma_f32_16x16x32_bf16 v[160:163], a[220:223], a[48:51], 0\n"
    "ds_read_b128 a[164:167], v243 offset:0\n"
    "buffer_store_dword v127, v231, s[8:11], s86 offen offset:0 sc1\n"
    "v_mfma_f32_16x16x32_bf16 v[164:167], a[224:227], a[48:51], 0\n"
    "ds_read_b128 a[168:171], v242 offset:32768\n"
    "v_mfma_f32_16x16x32_bf16 v[168:171], a[228:231], a[48:51], 0\n"
    "ds_read_b128 a[172:175], v243 offset:32768\n"
    "v_add_f32_e32 v225, v123, v127\n"
    "v_mfma_f32_16x16x32_bf16 v[156:159], a[232:235], a[52:55], v[156:159]\n"
    "ds_read_b128 a[176:179], v240 offset:2048\n"
    "v_min3_f32 v129, v129, v123, v127\n"
    "v_mfma_f32_16x16x32_bf16 v[160:163], v[192:195], a[52:55], v[160:163]\n"
    "ds_read_b128 a[180:183], v241 offset:2048\n"
    "v_add_f32_e32 v225, v225, v128\n"
    "v_mfma_f32_16x16x32_bf16 v[164:167], a[64:67], a[52:55], v[164:167]\n"
    "ds_read_b128 a[184:187], v240 offset:34816\n"
    "v_mov_b32_e32 v227, v123\n"
    "v_mfma_f32_16x16x32_bf16 v[168:171], a[68:71], a[52:55], v[168:171]\n"
    "s_waitcnt lgkmcnt(7)\n"
    "ds_read_b128 a[188:191], v241 offset:34816\n"
    "v_cmp_nlt_f32_e32 vcc, 0x12800000, v129\n"
    "v_mfma_f32_16x16x32_bf16 v[156:159], a[72:75], a[56:59], v[156:159]\n"
    "buffer_load_dwordx4 a[208:211], v250, s[8:11], s88 offen offset:3072 sc1\n"
    "ds_read_b128 a[48:51], v242 offset:2048\n"
    "v_mfma_f32_16x16x32_bf16 v[160:163], a[76:79], a[56:59], v[160:163]\n"
    "s_add_u32 s62, s58, 16384\n"
    "v_cmp_nlt_f32_e64 s[28:29], v225, s45\n"
    "v_mfma_f32_16x16x32_bf16 v[164:167], a[80:83], a[56:59], v[164:167]\n"
    "buffer_load_dwordx4 a[0:3], v250, s[8:11], s62 offen offset:1024 sc1\n"
    "v_mfma_f32_16x16x32_bf16 v[168:171], a[84:87], a[56:59], v[168:171]\n"
    "s_or_b32 s45, s29, vcc_hi\n"
    "v_mfma_f32_16x16x32_bf16 v[156:159], a[88:91], a[60:63], v[156:159]\n"
    "s_lshr_b32 s45, s45, 16\n"
    "s_cselect_b32 s45, s51, 0\n"
    "ds_read_b128 a[52:55], v243 offset:2048\n"
    "v_mfma_f32_16x16x32_bf16 v[160:163], a[92:95], a[60:63], v[160:163]\n"
    "s_andn2_b32 s45, s45, s49\n"
    "v_mfma_f32_16x16x32_bf16 v[164:167], a[96:99], a[60:63], v[164:167]\n"
    "s_add_u32 s45, s45, s39\n"
    "v_mfma_f32_16x16x32_bf16 v[168:171], a[100:103], a[60:63], v[168:171]\n"
    "s_waitcnt lgkmcnt(8)\n"
    "v_mov_b32_e32 v254, s45\n"
    "v_mfma_f32_16x16x32_bf16 v[156:159], a[104:107], a[160:163], v[156:159]\n"
    "buffer_load_dwordx4 a[212:215], v250, s[8:11], s89 offen offset:3072 sc1\n"
    "ds_read_b128 a[56:59], v242 offset:34816\n"
    "v_mfma_f32_16x16x32_bf16 v[160:163], a[108:111], a[160:163], v[160:163]\n"
    "s_lshl1_add_u32 s55, s50, s51\n"
    "v_mfma_f32_16x16x32_bf16 v[164:167], a[112:115], a[160:163], v[164:167]\n"
    "s_lshl_b32 s55, s55, 12\n"
    "v_mfma_f32_16x16x32_bf16 v[168:171], a[116:119], a[160:163], v[168:171]\n"
    "s_xor_b32 s31, s93, 2\n"
    "v_mfma_f32_16x16x32_bf16 v[156:159], a[120:123], a[164:167], v[156:159]\n"
    "s_lshl_b32 s31, s31, 8\n"
    "ds_read_b128 a[60:63], v243 offset:34816\n"
    "v_mfma_f32_16x16x32_bf16 v[160:163], a[124:127], a[164:167], v[160:163]\n"
    "s_add_u32 s31, s31, 0x6000\n"
    "v_mfma_f32_16x16x32_bf16 v[164:167], a[128:131], a[164:167], v[164:167]\n"
    "s_mul_i32 s45, s49, 252\n"
    "v_mfma_f32_16x16x32_bf16 v[168:171], a[132:135], a[164:167], v[168:171]\n"
    "s_waitcnt lgkmcnt(8)\n"
    "v_mfma_f32_16x16x32_bf16 v[156:159], a[136:139], a[168:171], v[156:159]\n"
    "buffer_load_dword v220, v220, s[8:11], s60 offen offset:192 sc1\n"
    "ds_read_b128 a[160:163], v240 offset:4096\n"
    "v_mfma_f32_16x16x32_bf16 v[160:163], a[140:143], a[168:171], v[160:163]\n"
    "s_add_u32 s45, s45, s34\n"
    "v_mfma_f32_16x16x32_bf16 v[164:167], a[144:147], a[168:171], v[164:167]\n"
    "s_mul_i32 s45, s45, 101376\n"
    "v_mfma_f32_16x16x32_bf16 v[168:171], a[148:151], a[168:171], v[168:171]\n"
    "s_add_u32 s45, s45, 16654336\n"
    "v_mfma_f32_16x16x32_bf16 v[156:159], a[152:155], a[172:175], v[156:159]\n"
    "s_lshl_b32 s47, s93, 11\n"
    "ds_read_b128 a[164:167], v241 offset:4096\n"
    "v_mfma_f32_16x16x32_bf16 v[160:163], a[156:159], a[172:175], v[160:163]\n"
    "s_add_u32 s54, s47, s45\n"
    "v_and_b32_e32 v251, 0x7ff, v252\n"
    "v_mfma_f32_16x16x32_bf16 v[164:167], v[180:183], a[172:175], v[164:167]\n"
    "s_add_u32 s45, s45, 32768\n"
    "v_add_u32_e32 v251, s63, v251\n"
    "v_mfma_f32_16x16x32_bf16 v[168:171], v[184:187], a[172:175], v[168:171]\n"
    "s_waitcnt lgkmcnt(8)\n"
    "v_mfma_f32_16x16x32_bf16 v[196:199], a[216:219], a[176:179], 0\n"
    "s_cmp_lt_u32 s32, 2\n"
    "s_cselect_b32 s54, s54, 2.0\n"
    "s_cselect_b32 s45, s45, 2.0\n"
    "ds_read_b128 a[168:171], v240 offset:36864\n"
    "v_mfma_f32_16x16x32_bf16 v[200:203], a[220:223], a[176:179], 0\n"
    "s_add_u32 s80, s35, s46\n"
    "v_mfma_f32_16x16x32_bf16 v[204:207], a[224:227], a[176:179], 0\n"
    "s_add_u32 s82, s36, s46\n"
    "v_mul_f32_e32 v156, v156, v228\n"
    "v_mul_f32_e32 v157, v157, v228\n"
    "v_mfma_f32_16x16x32_bf16 v[208:211], a[228:231], a[176:179], 0\n"
    "s_add_u32 s28, s37, s46\n"
    "v_mul_f32_e32 v158, v158, v228\n"
    "v_mul_f32_e32 v159, v159, v228\n"
    "v_mfma_f32_16x16x32_bf16 v[196:199], a[232:235], a[180:183], v[196:199]\n"
    "ds_read_b128 a[172:175], v241 offset:36864\n"
    "v_mul_f32_e32 v160, v160, v228\n"
    "v_mfma_f32_16x16x32_bf16 v[200:203], v[192:195], a[180:183], v[200:203]\n"
    "v_add_u32_e32 v244, s55, v250\n"
    "v_mul_f32_e32 v161, v161, v228\n"
    "v_mfma_f32_16x16x32_bf16 v[204:207], a[64:67], a[180:183], v[204:207]\n"
    "v_lshrrev_b32_e32 v247, 2, v250\n"
    "v_mfma_f32_16x16x32_bf16 v[208:211], a[68:71], a[180:183], v[208:211]\n"
    "s_waitcnt lgkmcnt(8)\n"
    "v_mul_f32_e32 v162, v162, v228\n"
    "v_mfma_f32_16x16x32_bf16 v[196:199], a[72:75], a[184:187], v[196:199]\n"
    "ds_read_b128 a[176:179], v242 offset:4096\n"
    "v_mfma_f32_16x16x32_bf16 v[200:203], a[76:79], a[184:187], v[200:203]\n"
    "v_xor_b32_e32 v249, 64, v251\n"
    "v_mfma_f32_16x16x32_bf16 v[204:207], a[80:83], a[184:187], v[204:207]\n"
    "s_waitcnt vmcnt(4)\n"
    "buffer_store_dword v254, off, s[8:11], s90 offset:0 sc1\n"
    "v_mfma_f32_16x16x32_bf16 v[208:211], a[84:87], a[184:187], v[208:211]\n"
    ".long 0xbedf00ff\n"
    ".Lat_w13:\n"
    "s_setpc_b64 s[2:3]\n"
    "v_add_u32_e32 v247, s31, v247\n"
    "v_mul_f32_e32 v163, v163, v228\n"
    "v_mfma_f32_16x16x32_bf16 v[196:199], a[88:91], a[188:191], v[196:199]\n"
    "ds_read_b128 a[180:183], v243 offset:4096\n"
    "v_add_u32_e32 v246, s47, v250\n"
    "v_mfma_f32_16x16x32_bf16 v[200:203], a[92:95], a[188:191], v[200:203]\n"
    "v_and_b32_e32 v245, 0x603f, v247\n"
    "v_bfe_u32 v226, v250, 2, 4\n"
    "v_mfma_f32_16x16x32_bf16 v[204:207], a[96:99], a[188:191], v[204:207]\n"
    "v_mul_f32_e32 v164, v164, v228\n"
    "v_mfma_f32_16x16x32_bf16 v[208:211], a[100:103], a[188:191], v[208:211]\n"
    "s_waitcnt lgkmcnt(8)\n"
    "v_bfe_u32 v225, v250, 6, 2\n"
    "v_lshl_or_b32 v226, v225, 7, v226\n"
    "v_mfma_f32_16x16x32_bf16 v[196:199], a[104:107], a[48:51], v[196:199]\n"
    "ds_read_b128 a[184:187], v242 offset:36864\n"
    "v_mfma_f32_16x16x32_bf16 v[200:203], a[108:111], a[48:51], v[200:203]\n"
    "v_and_b32_e32 v225, 0x60ff, v247\n"
    "v_mul_f32_e32 v165, v165, v228\n"
    "v_mfma_f32_16x16x32_bf16 v[204:207], a[112:115], a[48:51], v[204:207]\n"
    "buffer_load_dwordx4 a[4:7], v250, s[8:11], s62 offen offset:2048 sc1\n"
    "v_mfma_f32_16x16x32_bf16 v[208:211], a[116:119], a[48:51], v[208:211]\n"
    "s_waitcnt vmcnt(20)\n"
    "v_mul_f32_e32 v166, v166, v228\n"
    "v_mfma_f32_16x16x32_bf16 v[196:199], a[120:123], a[52:55], v[196:199]\n"
    "ds_read_b128 a[188:191], v243 offset:36864\n"
    "v_mfma_f32_16x16x32_bf16 v[200:203], a[124:127], a[52:55], v[200:203]\n"
    "v_mul_f32_e32 v167, v167, v228\n"
    "v_mfma_f32_16x16x32_bf16 v[204:207], a[128:131], a[52:55], v[204:207]\n"
    "buffer_load_dwordx4 a[8:11], v250, s[8:11], s62 offen offset:3072 sc1\n"
    "v_mfma_f32_16x16x32_bf16 v[208:211], a[132:135], a[52:55], v[208:211]\n"
    "s_waitcnt lgkmcnt(8)\n"
    "v_mul_f32_e32 v168, v168, v228\n"
    "v_mfma_f32_16x16x32_bf16 v[196:199], a[136:139], a[56:59], v[196:199]\n"
    "s_add_u32 s62, s58, 20480\n"
    "v_mfma_f32_16x16x32_bf16 v[200:203], a[140:143], a[56:59], v[200:203]\n"
    "v_mul_f32_e32 v169, v169, v228\n"
    "v_mul_f32_e32 v170, v170, v228\n"
    "v_mfma_f32_16x16x32_bf16 v[204:207], a[144:147], a[56:59], v[204:207]\n"
    "buffer_load_dwordx4 a[12:15], v250, s[8:11], s62 offen offset:0 sc1\n"
    "v_mfma_f32_16x16x32_bf16 v[208:211], a[148:151], a[56:59], v[208:211]\n"
    "v_mul_f32_e32 v171, v171, v228\n"
    "v_mfma_f32_16x16x32_bf16 v[196:199], a[152:155], a[60:63], v[196:199]\n"
    "ds_read_b128 a[48:51], v251 offset:6144\n"
    "v_mfma_f32_16x16x32_bf16 v[200:203], a[156:159], a[60:63], v[200:203]\n"
    "ds_read_b128 a[52:55], v249 offset:6144\n"
    "v_mfma_f32_16x16x32_bf16 v[204:207], v[180:183], a[60:63], v[204:207]\n"
    "s_barrier\n"
    "v_mfma_f32_16x16x32_bf16 v[208:211], v[184:187], a[60:63], v[208:211]\n"
    "s_waitcnt vmcnt(4)\n"
    "buffer_load_dwordx4 a[16:19], v250, s[8:11], s62 offen offset:1024 sc1\n"
    "v_mfma_f32_16x16x32_bf16 a[236:239], v[234:237], a[244:247], a[236:239]\n"
    "v_mfma_f32_16x16x32_bf16 v[0:3], v[232:235], a[244:247], v[0:3]\n"
    "v_cmp_le_u32_e32 vcc, 0x300, v250\n"
    "v_mfma_f32_16x16x32_bf16 a[236:239], v[234:237], a[248:251], a[236:239]\n"
    "v_cndmask_b32_e32 v227, v220, v227, vcc\n"
    "v_mfma_f32_16x16x32_bf16 v[0:3], v[232:235], a[248:251], v[0:3]\n"
    "buffer_load_dwordx4 a[20:23], v250, s[8:11], s62 offen offset:2048 sc1\n"
    "v_mfma_f32_16x16x32_bf16 a[236:239], v[234:237], a[252:255], a[236:239]\n"
    "v_mfma_f32_16x16x32_bf16 v[0:3], v[232:235], a[252:255], v[0:3]\n"
    "ds_write_b32 v247, v227 offset:0\n"
    "v_mfma_f32_16x16x32_bf16 v[20:23], v[234:237], v[8:11], v[20:23]\n"
    "v_mfma_f32_16x16x32_bf16 v[40:43], v[232:235], v[8:11], v[40:43]\n"
    "buffer_load_dwordx4 a[24:27], v250, s[8:11], s62 offen offset:3072 sc1\n"
    "v_mfma_f32_16x16x32_bf16 v[20:23], v[234:237], v[12:15], v[20:23]\n"
    "s_add_u32 s62, s58, 24576\n"
    "v_accvgpr_read_b32 v64, a236\n"
    "v_accvgpr_read_b32 v65, a237\n"
    "v_mfma_f32_16x16x32_bf16 v[40:43], v[232:235], v[12:15], v[40:43]\n"
    "v_accvgpr_read_b32 v66, a238\n"
    "v_accvgpr_read_b32 v67, a239\n"
    "v_mfma_f32_16x16x32_bf16 v[20:23], v[234:237], v[16:19], v[20:23]\n"
    "v_cvt_pk_bf16_f32 v64, v64, v65\n"
    "v_cvt_pk_bf16_f32 v65, v66, v67\n"
    "v_mfma_f32_16x16x32_bf16 v[40:43], v[232:235], v[16:19], v[40:43]\n"
    "buffer_load_dwordx4 a[28:31], v250, s[8:11], s62 offen offset:0 sc1\n"
    "v_mfma_f32_16x16x32_bf16 v[60:63], v[234:237], a[192:195], v[60:63]\n"
    "v_cvt_pk_bf16_f32 v66, v0, v1\n"
    "v_cvt_pk_bf16_f32 v67, v2, v3\n"
    "v_mfma_f32_16x16x32_bf16 v[80:83], v[232:235], a[192:195], v[80:83]\n"
    "ds_write_b128 v244, v[64:67] offset:0\n"
    "v_mfma_f32_16x16x32_bf16 v[60:63], v[234:237], a[196:199], v[60:63]\n"
    "v_mfma_f32_16x16x32_bf16 v[80:83], v[232:235], a[196:199], v[80:83]\n"
    "buffer_load_dwordx4 a[32:35], v250, s[8:11], s62 offen offset:1024 sc1\n"
    "v_mfma_f32_16x16x32_bf16 v[60:63], v[234:237], a[200:203], v[60:63]\n"
    "v_cvt_pk_bf16_f32 v20, v20, v21\n"
    "v_cvt_pk_bf16_f32 v21, v22, v23\n"
    "v_mfma_f32_16x16x32_bf16 v[80:83], v[232:235], a[200:203], v[80:83]\n"
    "v_cvt_pk_bf16_f32 v22, v40, v41\n"
    "v_cvt_pk_bf16_f32 v23, v42, v43\n"
    "v_mfma_f32_16x16x32_bf16 v[100:103], v[234:237], a[204:207], v[100:103]\n"
    "v_mfma_f32_16x16x32_bf16 v[120:123], v[232:235], a[204:207], v[120:123]\n"
    "buffer_load_dwordx4 a[36:39], v250, s[8:11], s62 offen offset:2048 sc1\n"
    "v_mfma_f32_16x16x32_bf16 v[100:103], v[234:237], a[208:211], v[100:103]\n"
    "ds_write_b128 v244, v[20:23] offset:1024\n"
    "v_mfma_f32_16x16x32_bf16 v[120:123], v[232:235], a[208:211], v[120:123]\n"
    "v_cvt_pk_bf16_f32 v60, v60, v61\n"
    "v_cvt_pk_bf16_f32 v61, v62, v63\n"
    "v_mfma_f32_16x16x32_bf16 v[100:103], v[234:237], a[212:215], v[100:103]\n"
    "v_mfma_f32_16x16x32_bf16 v[120:123], v[232:235], a[212:215], v[120:123]\n"
    "s_waitcnt lgkmcnt(11)\n"
    "v_cvt_pk_bf16_f32 v62, v80, v81\n"
    "v_cvt_pk_bf16_f32 v63, v82, v83\n"
    "v_mfma_f32_16x16x32_bf16 v[172:175], a[216:219], a[160:163], 0\n"
    "buffer_load_dword v253, v226, s[8:11], s61 offen offset:0 sc1\n"
    "v_mfma_f32_16x16x32_bf16 v[176:179], a[220:223], a[160:163], 0\n"
    "ds_write_b128 v244, v[60:63] offset:2048\n"
    "v_mfma_f32_16x16x32_bf16 v[212:215], a[224:227], a[160:163], 0\n"
    "buffer_load_dwordx4 a[40:43], v250, s[8:11], s62 offen offset:3072 sc1\n"
    "v_mfma_f32_16x16x32_bf16 v[216:219], a[228:231], a[160:163], 0\n"
    "s_add_u32 s62, s58, 28672\n"
    "v_cvt_pk_bf16_f32 v100, v100, v101\n"
    "v_cvt_pk_bf16_f32 v101, v102, v103\n"
    "v_mfma_f32_16x16x32_bf16 v[172:175], a[232:235], a[164:167], v[172:175]\n"
    "v_cvt_pk_bf16_f32 v102, v120, v121\n"
    "v_cvt_pk_bf16_f32 v103, v122, v123\n"
    "v_mfma_f32_16x16x32_bf16 v[176:179], v[192:195], a[164:167], v[176:179]\n"
    "ds_write_b128 v244, v[100:103] offset:3072\n"
    "v_mfma_f32_16x16x32_bf16 v[212:215], a[64:67], a[164:167], v[212:215]\n"
    "buffer_load_dwordx4 a[44:47], v250, s[8:11], s62 offen offset:0 sc1\n"
    "v_mfma_f32_16x16x32_bf16 v[216:219], a[68:71], a[164:167], v[216:219]\n"
    "s_waitcnt lgkmcnt(12)\n"
    "v_mfma_f32_16x16x32_bf16 v[172:175], a[72:75], a[168:171], v[172:175]\n"
    "v_mfma_f32_16x16x32_bf16 v[176:179], a[76:79], a[168:171], v[176:179]\n"
    "s_waitcnt lgkmcnt(0)\n"
    "v_mfma_f32_16x16x32_bf16 v[212:215], a[80:83], a[168:171], v[212:215]\n"
    "s_barrier\n"
    "v_mfma_f32_16x16x32_bf16 v[216:219], a[84:87], a[168:171], v[216:219]\n"
    "buffer_load_dwordx4 a[240:243], v250, s[8:11], s62 offen offset:1024 sc1\n"
    "v_mfma_f32_16x16x32_bf16 v[172:175], a[88:91], a[172:175], v[172:175]\n"
    "ds_read_b128 a[244:247], v250 offset:0\n"
    "v_mfma_f32_16x16x32_bf16 v[176:179], a[92:95], a[172:175], v[176:179]\n"
    "ds_read_b128 a[248:251], v250 offset:1024\n"
    "v_mfma_f32_16x16x32_bf16 v[212:215], a[96:99], a[172:175], v[212:215]\n"
    "ds_read_b128 a[192:195], v250 offset:2048\n"
    "v_mfma_f32_16x16x32_bf16 v[216:219], a[100:103], a[172:175], v[216:219]\n"
    "ds_read_b128 a[196:199], v250 offset:3072\n"
    "v_mfma_f32_16x16x32_bf16 v[172:175], a[104:107], a[176:179], v[172:175]\n"
    "buffer_load_dwordx4 v[4:7], v250, s[8:11], s62 offen offset:2048 sc1\n"
    "v_mfma_f32_16x16x32_bf16 v[176:179], a[108:111], a[176:179], v[176:179]\n"
    "ds_read_b128 a[200:203], v250 offset:4096\n"
    "v_mfma_f32_16x16x32_bf16 v[212:215], a[112:115], a[176:179], v[212:215]\n"
    "ds_read_b128 a[204:207], v250 offset:5120\n"
    "v_mfma_f32_16x16x32_bf16 v[216:219], a[116:119], a[176:179], v[216:219]\n"
    "ds_read_b128 a[208:211], v250 offset:6144\n"
    "v_mfma_f32_16x16x32_bf16 v[172:175], a[120:123], a[180:183], v[172:175]\n"
    "buffer_load_dwordx4 v[24:27], v250, s[8:11], s62 offen offset:3072 sc1\n"
    "v_mfma_f32_16x16x32_bf16 v[176:179], a[124:127], a[180:183], v[176:179]\n"
    "v_mul_f32_e32 v196, v196, v229\n"
    "v_mul_f32_e32 v197, v197, v229\n"
    "v_mfma_f32_16x16x32_bf16 v[212:215], a[128:131], a[180:183], v[212:215]\n"
    "ds_read_b128 a[212:215], v250 offset:7168\n"
    "v_mfma_f32_16x16x32_bf16 v[216:219], a[132:135], a[180:183], v[216:219]\n"
    "ds_read2_b32 v[220:221], v245 offset0:0 offset1:16\n"
    "v_mfma_f32_16x16x32_bf16 v[172:175], a[136:139], a[184:187], v[172:175]\n"
    "ds_read2_b32 v[222:223], v245 offset0:32 offset1:48\n"
    "v_mfma_f32_16x16x32_bf16 v[176:179], a[140:143], a[184:187], v[176:179]\n"
    "ds_read2_b32 v[80:81], v245 offset0:64 offset1:80\n"
    "v_mfma_f32_16x16x32_bf16 v[212:215], a[144:147], a[184:187], v[212:215]\n"
    "v_mul_f32_e32 v198, v198, v229\n"
    "v_mul_f32_e32 v199, v199, v229\n"
    "v_mfma_f32_16x16x32_bf16 v[216:219], a[148:151], a[184:187], v[216:219]\n"
    "ds_read2_b32 v[82:83], v245 offset0:96 offset1:112\n"
    "v_mfma_f32_16x16x32_bf16 v[172:175], a[152:155], a[188:191], v[172:175]\n"
    "v_mul_f32_e32 v200, v200, v229\n"
    "v_mfma_f32_16x16x32_bf16 v[176:179], a[156:159], a[188:191], v[176:179]\n"
    "v_mfma_f32_16x16x32_bf16 v[212:215], v[180:183], a[188:191], v[212:215]\n"
    "v_mul_f32_e32 v201, v201, v229\n"
    "v_mfma_f32_16x16x32_bf16 v[216:219], v[184:187], a[188:191], v[216:219]\n"
    "v_mul_f32_e32 v202, v202, v229\n"
    "v_mfma_f32_16x16x32_bf16 v[156:159], v[234:237], a[48:51], v[156:159]\n"
    "v_mul_f32_e32 v203, v203, v229\n"
    "v_mfma_f32_16x16x32_bf16 v[160:163], v[232:235], a[48:51], v[160:163]\n"
    "ds_read_b128 a[64:67], v250 offset:8192\n"
    "v_mul_f32_e32 v204, v204, v229\n"
    "v_mfma_f32_16x16x32_bf16 v[164:167], v[234:237], a[52:55], v[164:167]\n"
    "ds_read_b128 a[68:71], v250 offset:9216\n"
    "v_mfma_f32_16x16x32_bf16 v[168:171], v[232:235], a[52:55], v[168:171]\n"
    "s_waitcnt lgkmcnt(12)\n"
    "ds_read_b128 a[72:75], v250 offset:10240\n"
    "v_mfma_f32_16x16x32_bf16 v[140:143], v[28:31], a[244:247], 0\n"
    "ds_read_b128 a[76:79], v250 offset:11264\n"
    "v_mfma_f32_16x16x32_bf16 v[144:147], v[48:51], a[244:247], 0\n"
    "v_mul_f32_e32 v205, v205, v229\n"
    "v_mul_f32_e32 v206, v206, v229\n"
    "v_mfma_f32_16x16x32_bf16 v[148:151], v[32:35], a[244:247], 0\n"
    "v_mul_f32_e32 v207, v207, v229\n"
    "v_mfma_f32_16x16x32_bf16 v[152:155], v[52:55], a[244:247], 0\n"
    "s_waitcnt vmcnt(5)\n"
    "v_subrev_u32_e32 v253, s39, v253\n"
    "v_cmp_lt_u32_e32 vcc, 1, v253\n"
    "v_mfma_f32_16x16x32_bf16 v[140:143], v[36:39], a[248:251], v[140:143]\n"
    "ds_read_b128 a[80:83], v250 offset:12288\n"
    "v_mfma_f32_16x16x32_bf16 v[144:147], v[56:59], a[248:251], v[144:147]\n"
    "ds_read_b128 a[84:87], v250 offset:13312\n"
    "v_mul_f32_e32 v208, v208, v229\n"
    "v_mfma_f32_16x16x32_bf16 v[148:151], v[68:71], a[248:251], v[148:151]\n"
    "s_cbranch_vccnz .Lat_ppoll\n"
    ".Lat_pok:\n"
    "v_cmp_eq_u32_e32 vcc, 1, v253\n"
    "v_mfma_f32_16x16x32_bf16 v[152:155], v[88:91], a[248:251], v[152:155]\n"
    "s_waitcnt lgkmcnt(9)\n"
    "buffer_load_dwordx4 a[128:131], v250, s[8:11], s54 offen offset:0 sc1\n"
    "v_mfma_f32_16x16x32_bf16 v[140:143], v[72:75], a[192:195], v[140:143]\n"
    "v_add_f32_e32 v220, v220, v221\n"
    "v_mfma_f32_16x16x32_bf16 v[144:147], v[92:95], a[192:195], v[144:147]\n"
    "s_cbranch_vccnz .Lat_redo\n"
    "v_mfma_f32_16x16x32_bf16 v[148:151], v[76:79], a[192:195], v[148:151]\n"
    "buffer_load_dwordx4 a[132:135], v250, s[8:11], s54 offen offset:1024 sc1\n"
    "v_mfma_f32_16x16x32_bf16 v[152:155], v[96:99], a[192:195], v[152:155]\n"
    "s_add_u32 s54, s54, 8192\n"
    "ds_read_b128 a[88:91], v250 offset:14336\n"
    "v_mul_f32_e32 v209, v209, v229\n"
    "v_mfma_f32_16x16x32_bf16 v[140:143], v[108:111], a[196:199], v[140:143]\n"
    "buffer_load_dwordx4 a[136:139], v250, s[8:11], s54 offen offset:0 sc1\n"
    "v_mfma_f32_16x16x32_bf16 v[144:147], v[112:115], a[196:199], v[144:147]\n"
    "ds_read_b128 a[92:95], v250 offset:15360\n"
    "v_mfma_f32_16x16x32_bf16 v[148:151], v[132:135], a[196:199], v[148:151]\n"
    "s_waitcnt vmcnt(3)\n"
    "buffer_load_dwordx4 a[140:143], v250, s[8:11], s54 offen offset:1024 sc1\n"
    "v_mfma_f32_16x16x32_bf16 v[152:155], v[116:119], a[196:199], v[152:155]\n"
    "s_add_u32 s54, s54, 8192\n"
    "s_waitcnt lgkmcnt(10)\n"
    "v_mfma_f32_16x16x32_bf16 v[140:143], v[136:139], a[200:203], v[140:143]\n"
    "buffer_load_dwordx4 a[144:147], v250, s[8:11], s54 offen offset:0 sc1\n"
    "v_mfma_f32_16x16x32_bf16 v[144:147], a[0:3], a[200:203], v[144:147]\n"
    "ds_read_b128 a[56:59], v252 offset:30720\n"
    "v_mfma_f32_16x16x32_bf16 v[148:151], a[4:7], a[200:203], v[148:151]\n"
    "buffer_load_dwordx4 a[148:151], v250, s[8:11], s54 offen offset:1024 sc1\n"
    "v_mfma_f32_16x16x32_bf16 v[152:155], a[8:11], a[200:203], v[152:155]\n"
    "s_add_u32 s54, s54, 8192\n"
    "v_mul_f32_e32 v210, v210, v229\n"
    "v_add_f32_e32 v222, v222, v223\n"
    "v_mfma_f32_16x16x32_bf16 v[140:143], a[12:15], a[204:207], v[140:143]\n"
    "buffer_load_dwordx4 a[152:155], v250, s[8:11], s54 offen offset:0 sc1\n"
    "v_mfma_f32_16x16x32_bf16 v[144:147], a[16:19], a[204:207], v[144:147]\n"
    "ds_read_b128 a[60:63], v255 offset:30720\n"
    "v_mfma_f32_16x16x32_bf16 v[148:151], a[20:23], a[204:207], v[148:151]\n"
    "buffer_load_dwordx4 a[156:159], v250, s[8:11], s54 offen offset:1024 sc1\n"
    "v_mfma_f32_16x16x32_bf16 v[152:155], a[24:27], a[204:207], v[152:155]\n"
    "s_waitcnt lgkmcnt(10)\n"
    "v_mul_f32_e32 v211, v211, v229\n"
    "v_mfma_f32_16x16x32_bf16 v[140:143], a[28:31], a[208:211], v[140:143]\n"
    "buffer_load_dword v227, v248, s[8:11], s45 offen offset:1088 sc1\n"
    "v_mfma_f32_16x16x32_bf16 v[144:147], a[32:35], a[208:211], v[144:147]\n"
    "v_add_f32_e32 v220, v220, v222\n"
    "v_mfma_f32_16x16x32_bf16 v[148:151], a[36:39], a[208:211], v[148:151]\n"
    "v_rcp_f32_e32 v220, v220\n"
    "v_mfma_f32_16x16x32_bf16 v[152:155], a[40:43], a[208:211], v[152:155]\n"
    "v_add_f32_e32 v80, v80, v81\n"
    "v_add_f32_e32 v82, v82, v83\n"
    "v_mfma_f32_16x16x32_bf16 v[140:143], a[44:47], a[212:215], v[140:143]\n"
    "v_mul_f32_e32 v172, v172, v230\n"
    "v_add_f32_e32 v80, v80, v82\n"
    "v_mfma_f32_16x16x32_bf16 v[144:147], a[240:243], a[212:215], v[144:147]\n"
    "v_rcp_f32_e32 v80, v80\n"
    "v_mfma_f32_16x16x32_bf16 v[148:151], v[4:7], a[212:215], v[148:151]\n"
    "ds_read_b128 a[160:163], v252 offset:4096\n"
    "v_mfma_f32_16x16x32_bf16 v[152:155], v[24:27], a[212:215], v[152:155]\n"
    "s_waitcnt lgkmcnt(9)\n"
    "ds_read_b128 a[164:167], v255 offset:4096\n"
    "v_mfma_f32_16x16x32_bf16 v[180:183], v[28:31], a[64:67], 0\n"
    "v_mul_f32_e32 v173, v173, v230\n"
    "v_mfma_f32_16x16x32_bf16 v[184:187], v[48:51], a[64:67], 0\n"
    "v_fma_f32 v140, v140, v220, v156\n"
    "v_fma_f32 v141, v141, v220, v157\n"
    "v_mfma_f32_16x16x32_bf16 v[188:191], v[32:35], a[64:67], 0\n"
    "v_mfma_f32_16x16x32_bf16 v[192:195], v[52:55], a[64:67], 0\n"
    "v_fma_f32 v142, v142, v220, v158\n"
    "v_mfma_f32_16x16x32_bf16 v[180:183], v[36:39], a[68:71], v[180:183]\n"
    "v_fma_f32 v143, v143, v220, v159\n"
    "v_mfma_f32_16x16x32_bf16 v[184:187], v[56:59], a[68:71], v[184:187]\n"
    "v_fma_f32 v144, v144, v220, v160\n"
    "v_fma_f32 v145, v145, v220, v161\n"
    "v_mfma_f32_16x16x32_bf16 v[188:191], v[68:71], a[68:71], v[188:191]\n"
    "v_fma_f32 v146, v146, v220, v162\n"
    "v_mfma_f32_16x16x32_bf16 v[192:195], v[88:91], a[68:71], v[192:195]\n"
    "s_waitcnt lgkmcnt(8)\n"
    "v_fma_f32 v147, v147, v220, v163\n"
    "v_mfma_f32_16x16x32_bf16 v[180:183], v[72:75], a[72:75], v[180:183]\n"
    "v_mul_f32_e32 v174, v174, v230\n"
    "v_mfma_f32_16x16x32_bf16 v[184:187], v[92:95], a[72:75], v[184:187]\n"
    "v_cvt_pk_bf16_f32 v140, v140, v141\n"
    "v_cvt_pk_bf16_f32 v141, v142, v143\n"
    "v_mfma_f32_16x16x32_bf16 v[188:191], v[76:79], a[72:75], v[188:191]\n"
    "v_cvt_pk_bf16_f32 v142, v144, v145\n"
    "v_mfma_f32_16x16x32_bf16 v[192:195], v[96:99], a[72:75], v[192:195]\n"
    "v_cvt_pk_bf16_f32 v143, v146, v147\n"
    "v_mfma_f32_16x16x32_bf16 v[180:183], v[108:111], a[76:79], v[180:183]\n"
    "buffer_store_dwordx4 v[140:143], v239, s[16:19], s80 offen offset:0 sc0 sc1\n"
    "v_mfma_f32_16x16x32_bf16 v[184:187], v[112:115], a[76:79], v[184:187]\n"
    "v_mul_f32_e32 v175, v175, v230\n"
    "v_mfma_f32_16x16x32_bf16 v[188:191], v[132:135], a[76:79], v[188:191]\n"
    "v_fma_f32 v148, v148, v220, v164\n"
    "v_mfma_f32_16x16x32_bf16 v[192:195], v[116:119], a[76:79], v[192:195]\n"
    "s_waitcnt lgkmcnt(6)\n"
    "v_fma_f32 v149, v149, v220, v165\n"
    "v_fma_f32 v150, v150, v220, v166\n"
    "v_mfma_f32_16x16x32_bf16 v[180:183], v[136:139], a[80:83], v[180:183]\n"
    "v_fma_f32 v151, v151, v220, v167\n"
    "v_mfma_f32_16x16x32_bf16 v[184:187], a[0:3], a[80:83], v[184:187]\n"
    "v_fma_f32 v152, v152, v220, v168\n"
    "v_mfma_f32_16x16x32_bf16 v[188:191], a[4:7], a[80:83], v[188:191]\n"
    "v_fma_f32 v153, v153, v220, v169\n"
    "v_fma_f32 v154, v154, v220, v170\n"
    "v_mfma_f32_16x16x32_bf16 v[192:195], a[8:11], a[80:83], v[192:195]\n"
    "v_fma_f32 v155, v155, v220, v171\n"
    "v_cvt_pk_bf16_f32 v148, v148, v149\n"
    "v_mfma_f32_16x16x32_bf16 v[180:183], a[12:15], a[84:87], v[180:183]\n"
    "v_cvt_pk_bf16_f32 v149, v150, v151\n"
    "v_cvt_pk_bf16_f32 v150, v152, v153\n"
    "v_mfma_f32_16x16x32_bf16 v[184:187], a[16:19], a[84:87], v[184:187]\n"
    "v_cvt_pk_bf16_f32 v151, v154, v155\n"
    "v_mfma_f32_16x16x32_bf16 v[188:191], a[20:23], a[84:87], v[188:191]\n"
    "buffer_store_dwordx4 v[148:151], v239, s[16:19], s80 offen offset:64 sc0 sc1\n"
    "v_mfma_f32_16x16x32_bf16 v[192:195], a[24:27], a[84:87], v[192:195]\n"
    "s_waitcnt lgkmcnt(4)\n"
    "v_mfma_f32_16x16x32_bf16 v[180:183], a[28:31], a[88:91], v[180:183]\n"
    "v_mul_f32_e32 v176, v176, v230\n"
    "v_mul_f32_e32 v177, v177, v230\n"
    "v_mfma_f32_16x16x32_bf16 v[184:187], a[32:35], a[88:91], v[184:187]\n"
    "v_mul_f32_e32 v178, v178, v230\n"
    "v_mul_f32_e32 v179, v179, v230\n"
    "v_mfma_f32_16x16x32_bf16 v[188:191], a[36:39], a[88:91], v[188:191]\n"
    "v_mul_f32_e32 v212, v212, v230\n"
    "v_mfma_f32_16x16x32_bf16 v[192:195], a[40:43], a[88:91], v[192:195]\n"
    "s_waitcnt vmcnt(9)\n"
    "v_mul_f32_e32 v213, v213, v230\n"
    "v_mfma_f32_16x16x32_bf16 v[64:67], v[234:237], a[128:131], 0\n"
    "v_mfma_f32_16x16x32_bf16 v[84:87], v[232:235], a[128:131], 0\n"
    "v_mfma_f32_16x16x32_bf16 v[104:107], v[234:237], a[132:135], 0\n"
    "v_mul_f32_e32 v214, v214, v230\n"
    "v_mfma_f32_16x16x32_bf16 v[124:127], v[232:235], a[132:135], 0\n"
    "s_waitcnt vmcnt(7)\n"
    "v_mfma_f32_16x16x32_bf16 v[64:67], v[234:237], a[136:139], v[64:67]\n"
    "v_mfma_f32_16x16x32_bf16 v[84:87], v[232:235], a[136:139], v[84:87]\n"
    "v_mul_f32_e32 v215, v215, v230\n"
    "v_mfma_f32_16x16x32_bf16 v[104:107], v[234:237], a[140:143], v[104:107]\n"
    "v_mul_f32_e32 v216, v216, v230\n"
    "v_mfma_f32_16x16x32_bf16 v[124:127], v[232:235], a[140:143], v[124:127]\n"
    "s_waitcnt vmcnt(5)\n"
    "v_mul_f32_e32 v217, v217, v230\n"
    "v_mfma_f32_16x16x32_bf16 v[64:67], v[234:237], a[144:147], v[64:67]\n"
    "v_mul_f32_e32 v218, v218, v230\n"
    "v_mfma_f32_16x16x32_bf16 v[84:87], v[232:235], a[144:147], v[84:87]\n"
    "v_mul_f32_e32 v219, v219, v230\n"
    "v_mfma_f32_16x16x32_bf16 v[104:107], v[234:237], a[148:151], v[104:107]\n"
    "v_mfma_f32_16x16x32_bf16 v[124:127], v[232:235], a[148:151], v[124:127]\n"
    "s_waitcnt vmcnt(3)\n"
    "v_mfma_f32_16x16x32_bf16 v[64:67], v[234:237], a[152:155], v[64:67]\n"
    "s_waitcnt vmcnt(2)\n"
    "ds_write_b32 v225, v227 offset:512\n"
    "v_mfma_f32_16x16x32_bf16 v[84:87], v[232:235], a[152:155], v[84:87]\n"
    "v_mfma_f32_16x16x32_bf16 v[104:107], v[234:237], a[156:159], v[104:107]\n"
    "v_mfma_f32_16x16x32_bf16 v[124:127], v[232:235], a[156:159], v[124:127]\n"
    "v_cvt_pk_bf16_f32 v64, v64, v65\n"
    "v_cvt_pk_bf16_f32 v65, v66, v67\n"
    "v_mfma_f32_16x16x32_bf16 v[180:183], a[44:47], a[92:95], v[180:183]\n"
    "v_cvt_pk_bf16_f32 v66, v84, v85\n"
    "v_cvt_pk_bf16_f32 v67, v86, v87\n"
    "v_mfma_f32_16x16x32_bf16 v[184:187], a[240:243], a[92:95], v[184:187]\n"
    "ds_write_b128 v246, v[64:67] offset:16384\n"
    "v_mfma_f32_16x16x32_bf16 v[188:191], v[4:7], a[92:95], v[188:191]\n"
    "v_cvt_pk_bf16_f32 v104, v104, v105\n"
    "v_cvt_pk_bf16_f32 v105, v106, v107\n"
    "v_mfma_f32_16x16x32_bf16 v[192:195], v[24:27], a[92:95], v[192:195]\n"
    "s_waitcnt lgkmcnt(4)\n"
    "v_cvt_pk_bf16_f32 v106, v124, v125\n"
    "v_cvt_pk_bf16_f32 v107, v126, v127\n"
    "v_mfma_f32_16x16x32_bf16 v[196:199], v[234:237], a[56:59], v[196:199]\n"
    "ds_write_b128 v246, v[104:107] offset:17408\n"
    "v_mfma_f32_16x16x32_bf16 v[200:203], v[232:235], a[56:59], v[200:203]\n"
    "v_mfma_f32_16x16x32_bf16 v[204:207], v[234:237], a[60:63], v[204:207]\n"
    "s_waitcnt lgkmcnt(0)\n"
    "v_mfma_f32_16x16x32_bf16 v[208:211], v[232:235], a[60:63], v[208:211]\n"
    "s_barrier\n"
    "v_mfma_f32_16x16x32_bf16 v[172:175], v[234:237], a[160:163], v[172:175]\n"
    "ds_read_b128 a[96:99], v250 offset:16384\n"
    "v_mfma_f32_16x16x32_bf16 v[176:179], v[232:235], a[160:163], v[176:179]\n"
    "ds_read_b128 a[100:103], v250 offset:17408\n"
    "v_mfma_f32_16x16x32_bf16 v[212:215], v[234:237], a[164:167], v[212:215]\n"
    "ds_read_b128 a[104:107], v250 offset:18432\n"
    "ds_read_b128 a[108:111], v250 offset:19456\n"
    "v_fma_f32 v180, v180, v80, v196\n"
    "v_mfma_f32_16x16x32_bf16 v[216:219], v[232:235], a[164:167], v[216:219]\n"
    "s_waitcnt lgkmcnt(2)\n"
    "ds_read_b128 a[112:115], v250 offset:20480\n"
    "ds_read_b128 a[116:119], v250 offset:21504\n"
    "v_mfma_f32_16x16x32_bf16 v[0:3], v[28:31], a[96:99], 0\n"
    "ds_read_b128 a[120:123], v250 offset:22528\n"
    "ds_read_b128 a[124:127], v250 offset:23552\n"
    "v_mfma_f32_16x16x32_bf16 v[20:23], v[48:51], a[96:99], 0\n"
    "ds_read2_b32 v[220:221], v245 offset0:128 offset1:144\n"
    "ds_read2_b32 v[222:223], v245 offset0:160 offset1:176\n"
    "v_mfma_f32_16x16x32_bf16 v[0:3], v[36:39], a[100:103], v[0:3]\n"
    "v_fma_f32 v181, v181, v80, v197\n"
    "v_fma_f32 v182, v182, v80, v198\n"
    "v_mfma_f32_16x16x32_bf16 v[20:23], v[56:59], a[100:103], v[20:23]\n"
    "s_waitcnt lgkmcnt(6)\n"
    "v_fma_f32 v183, v183, v80, v199\n"
    "v_fma_f32 v184, v184, v80, v200\n"
    "v_mfma_f32_16x16x32_bf16 v[0:3], v[72:75], a[104:107], v[0:3]\n"
    "s_or_b32 s45, s34, s93\n"
    "s_cselect_b32 s45, 2.0, 6101760\n"
    "v_fma_f32 v185, v185, v80, v201\n"
    "v_mfma_f32_16x16x32_bf16 v[20:23], v[92:95], a[104:107], v[20:23]\n"
    "v_fma_f32 v186, v186, v80, v202\n"
    "v_fma_f32 v187, v187, v80, v203\n"
    "v_mfma_f32_16x16x32_bf16 v[0:3], v[108:111], a[108:111], v[0:3]\n"
    "v_cvt_pk_bf16_f32 v180, v180, v181\n"
    "v_cvt_pk_bf16_f32 v181, v182, v183\n"
    "v_mfma_f32_16x16x32_bf16 v[20:23], v[112:115], a[108:111], v[20:23]\n"
    "s_waitcnt lgkmcnt(4)\n"
    "v_cvt_pk_bf16_f32 v182, v184, v185\n"
    "v_cvt_pk_bf16_f32 v183, v186, v187\n"
    "v_mfma_f32_16x16x32_bf16 v[0:3], v[136:139], a[112:115], v[0:3]\n"
    "buffer_store_dwordx4 v[180:183], v239, s[16:19], s82 offen offset:0 sc0 sc1\n"
    "v_mfma_f32_16x16x32_bf16 v[20:23], a[0:3], a[112:115], v[20:23]\n"
    "v_fma_f32 v188, v188, v80, v204\n"
    "v_fma_f32 v189, v189, v80, v205\n"
    "v_mfma_f32_16x16x32_bf16 v[0:3], a[12:15], a[116:119], v[0:3]\n"
    "v_fma_f32 v190, v190, v80, v206\n"
    "v_fma_f32 v191, v191, v80, v207\n"
    "v_mfma_f32_16x16x32_bf16 v[20:23], a[16:19], a[116:119], v[20:23]\n"
    "s_waitcnt lgkmcnt(0)\n"
    "v_fma_f32 v192, v192, v80, v208\n"
    "v_fma_f32 v193, v193, v80, v209\n"
    "v_fma_f32 v194, v194, v80, v210\n"
    "v_mfma_f32_16x16x32_bf16 v[0:3], a[28:31], a[120:123], v[0:3]\n"
    "v_fma_f32 v195, v195, v80, v211\n"
    "v_cvt_pk_bf16_f32 v188, v188, v189\n"
    "v_mfma_f32_16x16x32_bf16 v[20:23], a[32:35], a[120:123], v[20:23]\n"
    "v_cvt_pk_bf16_f32 v189, v190, v191\n"
    "v_cvt_pk_bf16_f32 v190, v192, v193\n"
    "v_cvt_pk_bf16_f32 v191, v194, v195\n"
    "v_mfma_f32_16x16x32_bf16 v[0:3], a[44:47], a[124:127], v[0:3]\n"
    "buffer_store_dwordx4 v[188:191], v239, s[16:19], s82 offen offset:64 sc0 sc1\n"
    "v_mfma_f32_16x16x32_bf16 v[20:23], a[240:243], a[124:127], v[20:23]\n"
    "v_add_f32_e32 v220, v220, v221\n"
    "v_add_f32_e32 v222, v222, v223\n"
    "v_mfma_f32_16x16x32_bf16 v[40:43], v[32:35], a[96:99], 0\n"
    "v_add_f32_e32 v220, v220, v222\n"
    "v_mfma_f32_16x16x32_bf16 v[60:63], v[52:55], a[96:99], 0\n"
    "v_rcp_f32_e32 v220, v220\n"
    "v_mfma_f32_16x16x32_bf16 v[40:43], v[68:71], a[100:103], v[40:43]\n"
    "v_fma_f32 v0, v0, v220, v172\n"
    "v_mfma_f32_16x16x32_bf16 v[60:63], v[88:91], a[100:103], v[60:63]\n"
    "v_fma_f32 v1, v1, v220, v173\n"
    "v_fma_f32 v2, v2, v220, v174\n"
    "v_mfma_f32_16x16x32_bf16 v[40:43], v[76:79], a[104:107], v[40:43]\n"
    "v_fma_f32 v3, v3, v220, v175\n"
    "v_mfma_f32_16x16x32_bf16 v[60:63], v[96:99], a[104:107], v[60:63]\n"
    "v_fma_f32 v20, v20, v220, v176\n"
    "v_fma_f32 v21, v21, v220, v177\n"
    "v_mfma_f32_16x16x32_bf16 v[40:43], v[132:135], a[108:111], v[40:43]\n"
    "v_fma_f32 v22, v22, v220, v178\n"
    "v_fma_f32 v23, v23, v220, v179\n"
    "v_mfma_f32_16x16x32_bf16 v[60:63], v[116:119], a[108:111], v[60:63]\n"
    "v_cvt_pk_bf16_f32 v0, v0, v1\n"
    "v_cvt_pk_bf16_f32 v1, v2, v3\n"
    "v_mfma_f32_16x16x32_bf16 v[40:43], a[4:7], a[112:115], v[40:43]\n"
    "v_cvt_pk_bf16_f32 v2, v20, v21\n"
    "v_cvt_pk_bf16_f32 v3, v22, v23\n"
    "v_mfma_f32_16x16x32_bf16 v[60:63], a[8:11], a[112:115], v[60:63]\n"
    "buffer_store_dwordx4 v[0:3], v239, s[16:19], s28 offen offset:0 sc0 sc1\n"
    "v_mfma_f32_16x16x32_bf16 v[40:43], a[20:23], a[116:119], v[40:43]\n"
    "v_mfma_f32_16x16x32_bf16 v[60:63], a[24:27], a[116:119], v[60:63]\n"
    "v_mfma_f32_16x16x32_bf16 v[40:43], a[36:39], a[120:123], v[40:43]\n"
    "v_mfma_f32_16x16x32_bf16 v[60:63], a[40:43], a[120:123], v[60:63]\n"
    "v_mfma_f32_16x16x32_bf16 v[40:43], v[4:7], a[124:127], v[40:43]\n"
    "v_mfma_f32_16x16x32_bf16 v[60:63], v[24:27], a[124:127], v[60:63]\n"
    "v_mov_b32_e32 v254, s39\n"
    "s_nop 2\n"
    "v_fma_f32 v40, v40, v220, v212\n"
    "v_fma_f32 v41, v41, v220, v213\n"
    "v_fma_f32 v42, v42, v220, v214\n"
    "v_fma_f32 v43, v43, v220, v215\n"
    "v_fma_f32 v60, v60, v220, v216\n"
    "v_fma_f32 v61, v61, v220, v217\n"
    "v_fma_f32 v62, v62, v220, v218\n"
    "v_fma_f32 v63, v63, v220, v219\n"
    "v_cvt_pk_bf16_f32 v40, v40, v41\n"
    "v_cvt_pk_bf16_f32 v41, v42, v43\n"
    "v_cvt_pk_bf16_f32 v42, v60, v61\n"
    "v_cvt_pk_bf16_f32 v43, v62, v63\n"
    "buffer_store_dwordx4 v[40:43], v239, s[16:19], s28 offen offset:64 sc0 sc1\n"
    "buffer_store_dword v254, off, s[8:11], s45 offset:0 sc1\n"
    "s_endpgm\n"
    ".p2align 6\n"
    ".long 0xbedf00ff\n"
    ".Lat_w14:\n"
    "s_setpc_b64 s[2:3]\n"
    ".Lat_k:\n"
    "s_mov_b32 s53, 0x400000\n"
    ".Lat_kl:\n"
    "buffer_load_dword v253, v238, s[8:11], s48 offen offset:0 sc1\n"
    "s_waitcnt vmcnt(0)\n"
    "v_subrev_u32_e32 v253, s39, v253\n"
    "v_subrev_u32_e32 v253, 0, v253\n"
    "v_cmp_le_u32_e32 vcc, 1, v253\n"
    "s_sub_u32 s53, s53, 1\n"
    "s_cbranch_scc1 .Lat_kr\n"
    "s_nop 2\n"
    "s_cbranch_vccnz .Lat_kl\n"
    "s_branch .Lat_kr\n"
    ".Lat_kx:\n"
    "s_branch .Lat_kxr\n"
    ".Lat_ppoll:\n"
    "s_mov_b32 s53, 0x400000\n"
    ".Lat_ppl:\n"
    "buffer_load_dword v253, v226, s[8:11], s61 offen offset:0 sc1\n"
    "s_waitcnt vmcnt(0)\n"
    "v_subrev_u32_e32 v253, s39, v253\n"
    "v_cmp_lt_u32_e32 vcc, 1, v253\n"
    "s_sub_u32 s53, s53, 1\n"
    "s_cbranch_scc1 .Lat_pok\n"
    "s_nop 2\n"
    "s_cbranch_vccnz .Lat_ppl\n"
    "s_branch .Lat_pok\n"
    ".Lat_wpoll:\n"
    "s_mov_b32 s53, 0x400000\n"
    ".Lat_wpoll_l:\n"
    "s_sleep 1\n"
    "buffer_load_dword v253, v255, s[8:11], s52 offen offset:0 sc1\n"
    "s_waitcnt vmcnt(0)\n"
    "v_cmp_ne_u32_e32 vcc, s39, v253\n"
    "s_sub_u32 s53, s53, 1\n"
    "s_cbranch_scc1 .Lat_wpoll_ret\n"
    "s_nop 2\n"
    "s_cbranch_vccnz .Lat_wpoll_l\n"
    "s_branch .Lat_wpoll_ret\n"
    ".Lat_glate:\n"
    "s_mov_b32 s53, 0x400000\n"
    ".Lat_glate_l:\n"
    "s_sleep 2\n"
    "buffer_load_dword v188, v189, s[8:11], s81 offen offset:0 sc1\n"
    "s_waitcnt vmcnt(0)\n"
    "v_cmp_ne_u32_e32 vcc, s39, v188\n"
    "s_sub_u32 s53, s53, 1\n"
    "s_cbranch_scc1 .Lat_glate_ret\n"
    "s_nop 2\n"
    "s_cbranch_vccnz .Lat_glate_l\n"
    "s_branch .Lat_glate_ret\n"
    ".Lat_lp0:\n"
    "s_mov_b32 s53, 0x400000\n"
    ".Lat_lp:\n"
    "s_sleep 1\n"
    "buffer_load_dword v253, off, s[8:11], s82 sc1\n"
    "s_waitcnt vmcnt(0)\n"
    "v_readfirstlane_b32 s81, v253\n"
    "s_sub_u32 s53, s53, 1\n"
    "s_cbranch_scc1 .Lat_lpx\n"
    "s_nop 3\n"
    "s_cmp_lg_u32 s81, s39\n"
    "s_cbranch_scc1 .Lat_lp\n"
    ".Lat_lpx:\n"
    "s_mov_b32 m0, s30\n"
    "s_nop 0\n"
    "buffer_load_dwordx4 v250, s[8:11], s83 offen offset:0 sc1 lds\n"
    "s_mov_b32 m0, s42\n"
    "s_branch .Lat_g1b\n"
    ".Lat_h1poll:\n"
    "s_mov_b32 s53, 0x400000\n"
    ".Lat_h1poll_l:\n"
    "s_sleep 1\n"
    "buffer_load_dword v80, v83, s[8:11], s59 offen offset:0 sc1\n"
    "s_waitcnt vmcnt(0)\n"
    "v_cmp_ne_u32_e32 vcc, s39, v80\n"
    "s_sub_u32 s53, s53, 1\n"
    "s_cbranch_scc1 .Lat_h1poll_ret\n"
    "s_nop 2\n"
    "s_cbranch_vccnz .Lat_h1poll_l\n"
    "s_branch .Lat_h1poll_ret\n"
    ".Lat_hpoll:\n"
    "s_mov_b32 s53, 0x400000\n"
    ".Lat_hpoll_l:\n"
    "s_sleep 1\n"
    "buffer_load_dword v253, v250, s[8:11], s59 offen offset:0 sc1\n"
    "buffer_load_dword v254, v252, s[8:11], s59 offen offset:0 sc1\n"
    "s_waitcnt vmcnt(0)\n"
    "v_xor_b32_e32 v254, s39, v254\n"
    "v_xor_b32_e32 v253, s39, v253\n"
    "v_or_b32_e32 v253, v253, v254\n"
    "v_cmp_ne_u32_e32 vcc, 0, v253\n"
    "s_sub_u32 s53, s53, 1\n"
    "s_cbranch_scc1 .Lat_hpoll_ret\n"
    "s_nop 2\n"
    "s_cbranch_vccnz .Lat_hpoll_l\n"
    "s_branch .Lat_hpoll_ret\n"
    ".Lat_redo:\n"
    "s_waitcnt vmcnt(0) lgkmcnt(0)\n"
    "s_barrier\n"
    "s_mov_b32 s49, 1\n"
    "s_mov_b32 s80, 0\n"
    "s_mov_b32 s82, 2.0\n"
    "v_lshrrev_b32_e32 v224, 4, v250\n"
    "s_and_b32 s50, s93, 1\n"
    "s_lshr_b32 s51, s93, 1\n"
    "v_lshrrev_b32_e32 v225, 3, v224\n"
    "v_and_b32_e32 v226, 6, v225\n"
    "v_and_b32_e32 v227, 7, v224\n"
    "v_xor_b32_e32 v226, v226, v227\n"
    "v_lshlrev_b32_e32 v226, 4, v226\n"
    "v_and_b32_e32 v227, 4, v225\n"
    "v_add_u32_e32 v227, v227, v225\n"
    "v_lshl_add_u32 v251, v227, 9, v226\n"
    "v_add_u32_e32 v252, 0x1c00, v251\n"
    "v_and_b32_e32 v225, 7, v224\n"
    "v_lshlrev_b32_e32 v227, 7, v225\n"
    "v_bfe_u32 v226, v224, 3, 1\n"
    "v_lshl_add_u32 v227, v226, 10, v227\n"
    "v_and_b32_e32 v225, 6, v224\n"
    "v_lshrrev_b32_e32 v226, 4, v224\n"
    "v_xor_b32_e32 v225, v225, v226\n"
    "v_lshl_add_u32 v240, v225, 4, v227\n"
    "v_lshrrev_b32_e32 v8, 4, v250\n"
    "v_and_b32_e32 v10, 0x70, v251\n"
    "v_bfe_u32 v9, v8, 3, 1\n"
    "v_lshl_add_u32 v248, v9, 9, v10\n"
    "v_lshrrev_b32_e32 v9, 4, v8\n"
    "v_lshl_add_u32 v248, v9, 12, v248\n"
    "s_mul_i32 s43, s50, 3\n"
    "s_xor_b32 s43, s43, s51\n"
    "s_lshl_b32 s43, s43, 15\n"
    "s_add_u32 s43, s43, 32768\n"
    "s_mul_i32 s42, s93, 5120\n"
    "s_lshl_b32 s45, s50, 8\n"
    "s_mul_i32 s40, s32, 1277952\n"
    "s_lshl_b32 s41, s51, 14\n"
    "s_add_u32 s40, s40, s41\n"
    "s_add_u32 s40, s40, s45\n"
    "s_add_u32 s41, s40, 128\n"
    "v_bfe_u32 v9, v8, 2, 3\n"
    "v_lshlrev_b32_e32 v244, 7, v9\n"
    "v_lshrrev_b32_e32 v10, 5, v8\n"
    "v_lshl_add_u32 v244, v10, 10, v244\n"
    "v_and_b32_e32 v9, 6, v9\n"
    "v_bfe_u32 v10, v8, 1, 1\n"
    "v_xor_b32_e32 v9, v9, v10\n"
    "v_lshl_add_u32 v244, v9, 4, v244\n"
    "v_and_b32_e32 v10, 1, v8\n"
    "v_lshl_add_u32 v244, v10, 3, v244\n"
    "s_lshl_b32 s45, s51, 16\n"
    "s_add_u32 s45, s45, 32768\n"
    "v_add_u32_e32 v244, s45, v244\n"
    "v_xor_b32_e32 v245, 32, v244\n"
    "v_xor_b32_e32 v246, 64, v244\n"
    "v_xor_b32_e32 v247, 0x60, v244\n"
    "s_lshl_b32 s45, s51, 15\n"
    "s_add_u32 s48, s45, 32768\n"
    "s_sub_u32 s45, 131072, s45\n"
    "v_add_u32_e32 v242, s45, v240\n"
    "v_add_u32_e32 v240, s48, v240\n"
    "v_xor_b32_e32 v241, 64, v240\n"
    "v_xor_b32_e32 v243, 64, v242\n"
    "s_xor_b32 s45, s93, 2\n"
    "s_mul_i32 s45, s45, 5120\n"
    "v_lshl_add_u32 v249, v8, 2, s45\n"
    "s_lshl_b32 s45, s93, 11\n"
    "v_add_u32_e32 v99, s45, v250\n"
    "v_add_u32_e32 v98, 11345920, v99\n"
    "v_and_b32_e32 v252, 0x30, v250\n"
    "v_add_u32_e32 v252, 0x400, v252\n"
    "s_mul_i32 s59, s32, 999\n"
    "s_andn2_b32 s59, s59, 63\n"
    "s_sub_u32 s48, 3968, s59\n"
    "v_cmp_le_u32_e32 vcc, 0x80, v250\n"
    "v_mov_b32_e32 v80, s48\n"
    "s_add_u32 s59, s59, 6076416\n"
    "v_cndmask_b32_e32 v83, v250, v80, vcc\n"
    "v_bfe_u32 v253, v250, 4, 2\n"
    "v_max_u32_e32 v253, 1, v253\n"
    "v_add_u32_e32 v253, s32, v253\n"
    "v_and_b32_e32 v253, 3, v253\n"
    "v_lshlrev_b32_e32 v253, 9, v253\n"
    "v_mov_b32_e32 v84, s39\n"
    "v_lshrrev_b32_e32 v86, 2, v250\n"
    "v_min_u32_e32 v86, 124, v86\n"
    "s_lshl2_add_u32 s58, s34, s93\n"
    "s_lshl_b32 s58, s58, 7\n"
    "s_add_u32 s58, s58, 5910528\n"
    "s_lshl_b32 s45, s50, 13\n"
    "v_add_u32_e32 v85, s45, v250\n"
    "s_lshl3_add_u32 s45, s45, 49152\n"
    "v_add_u32_e32 v255, s45, v250\n"
    "v_lshrrev_b32_e32 v105, 7, v250\n"
    "v_and_b32_e32 v104, 0x70, v251\n"
    "v_lshl_add_u32 v104, v105, 9, v104\n"
    "v_add_u32_e32 v105, 0xc00, v104\n"
    "v_add_u32_e32 v106, 0x1800, v104\n"
    "v_add_u32_e32 v107, 0x2400, v104\n"
    "s_lshl_b32 s57, s93, 7\n"
    "s_lshl4_add_u32 s56, s57, 24576\n"
    "s_add_u32 s57, s57, 5111808\n"
    "s_mul_i32 s45, s51, 32768\n"
    "s_add_u32 s45, s45, 8192\n"
    "v_subrev_u32_e32 v46, s45, v240\n"
    "v_subrev_u32_e32 v47, s45, v241\n"
    "s_mul_i32 s45, s51, 61440\n"
    "s_add_u32 s45, s45, 8192\n"
    "v_subrev_u32_e32 v4, s45, v244\n"
    "v_subrev_u32_e32 v5, s45, v245\n"
    "v_subrev_u32_e32 v6, s45, v246\n"
    "v_subrev_u32_e32 v7, s45, v247\n"
    "v_accvgpr_write_b32 a244, 0\n"
    "v_accvgpr_write_b32 a245, 0\n"
    "v_accvgpr_write_b32 a246, 0\n"
    "v_accvgpr_write_b32 a247, 0\n"
    "v_accvgpr_write_b32 a248, 0\n"
    "v_accvgpr_write_b32 a249, 0\n"
    "v_accvgpr_write_b32 a250, 0\n"
    "v_accvgpr_write_b32 a251, 0\n"
    "v_accvgpr_write_b32 a252, 0\n"
    "v_accvgpr_write_b32 a253, 0\n"
    "v_accvgpr_write_b32 a254, 0\n"
    "v_accvgpr_write_b32 a255, 0\n"
    "v_mov_b64_e32 v[8:9], 0\n"
    "v_mov_b64_e32 v[10:11], 0\n"
    "v_mov_b64_e32 v[12:13], 0\n"
    "v_mov_b64_e32 v[14:15], 0\n"
    "v_mov_b64_e32 v[16:17], 0\n"
    "v_mov_b64_e32 v[18:19], 0\n"
    "v_mov_b64_e32 v[28:29], 0\n"
    "v_mov_b64_e32 v[30:31], 0\n"
    "v_mov_b64_e32 v[32:33], 0\n"
    "v_mov_b64_e32 v[34:35], 0\n"
    "v_mov_b64_e32 v[36:37], 0\n"
    "v_mov_b64_e32 v[38:39], 0\n"
    "v_mov_b64_e32 v[48:49], 0\n"
    "v_mov_b64_e32 v[50:51], 0\n"
    "v_mov_b64_e32 v[52:53], 0\n"
    "v_mov_b64_e32 v[54:55], 0\n"
    "v_mov_b64_e32 v[56:57], 0\n"
    "v_mov_b64_e32 v[58:59], 0\n"
    "v_mov_b64_e32 v[68:69], 0\n"
    "v_mov_b64_e32 v[70:71], 0\n"
    "v_mov_b64_e32 v[72:73], 0\n"
    "v_mov_b64_e32 v[74:75], 0\n"
    "v_mov_b64_e32 v[76:77], 0\n"
    "v_mov_b64_e32 v[78:79], 0\n"
    "v_mov_b64_e32 v[88:89], 0\n"
    "v_mov_b64_e32 v[90:91], 0\n"
    "v_mov_b64_e32 v[92:93], 0\n"
    "v_mov_b64_e32 v[94:95], 0\n"
    "v_mov_b64_e32 v[96:97], 0\n"
    "v_mov_b64_e32 v[98:99], 0\n"
    "v_mov_b64_e32 v[108:109], 0\n"
    "v_mov_b64_e32 v[110:111], 0\n"
    "v_mov_b64_e32 v[112:113], 0\n"
    "v_mov_b64_e32 v[114:115], 0\n"
    "v_mov_b64_e32 v[116:117], 0\n"
    "v_mov_b64_e32 v[118:119], 0\n"
    "v_mov_b64_e32 v[128:129], 0\n"
    "v_mov_b64_e32 v[130:131], 0\n"
    "v_mov_b64_e32 v[132:133], 0\n"
    "v_mov_b64_e32 v[134:135], 0\n"
    "v_mov_b64_e32 v[136:137], 0\n"
    "v_mov_b64_e32 v[138:139], 0\n"
    "s_mov_b32 m0, s56\n"
    "s_nop 0\n"
    "buffer_load_dwordx4 v104, s[8:11], s57 offen offset:0 sc1 lds\n"
    "buffer_load_dwordx4 v105, s[8:11], s57 offen offset:1024 sc1 lds\n"
    "s_add_u32 m0, s43, 0\n"
    "s_nop 0\n"
    "buffer_load_dwordx4 v104, s[8:11], s40 offen offset:0 sc1 lds\n"
    "buffer_load_dwordx4 v105, s[8:11], s40 offen offset:1024 sc1 lds\n"
    "buffer_load_dwordx4 v106, s[8:11], s40 offen offset:2048 sc1 lds\n"
    "buffer_load_dwordx4 v107, s[8:11], s40 offen offset:3072 sc1 lds\n"
    "s_add_u32 s40, s40, 0x8000\n"
    "s_add_u32 m0, s43, 4096\n"
    "s_nop 0\n"
    "buffer_load_dwordx4 v104, s[8:11], s41 offen offset:0 sc1 lds\n"
    "buffer_load_dwordx4 v105, s[8:11], s41 offen offset:1024 sc1 lds\n"
    "buffer_load_dwordx4 v106, s[8:11], s41 offen offset:2048 sc1 lds\n"
    "buffer_load_dwordx4 v107, s[8:11], s41 offen offset:3072 sc1 lds\n"
    "s_add_u32 s41, s41, 0x8000\n"
    "s_add_u32 m0, s43, 8192\n"
    "s_nop 0\n"
    "buffer_load_dwordx4 v104, s[8:11], s40 offen offset:0 sc1 lds\n"
    "buffer_load_dwordx4 v105, s[8:11], s40 offen offset:1024 sc1 lds\n"
    "buffer_load_dwordx4 v106, s[8:11], s40 offen offset:2048 sc1 lds\n"
    "buffer_load_dwordx4 v107, s[8:11], s40 offen offset:3072 sc1 lds\n"
    "s_add_u32 s40, s40, 0x8000\n"
    "s_add_u32 m0, s43, 12288\n"
    "s_nop 0\n"
    "buffer_load_dwordx4 v104, s[8:11], s41 offen offset:0 sc1 lds\n"
    "buffer_load_dwordx4 v105, s[8:11], s41 offen offset:1024 sc1 lds\n"
    "buffer_load_dwordx4 v106, s[8:11], s41 offen offset:2048 sc1 lds\n"
    "buffer_load_dwordx4 v107, s[8:11], s41 offen offset:3072 sc1 lds\n"
    "s_add_u32 s41, s41, 0x8000\n"
    "s_waitcnt vmcnt(0)\n"
    "s_branch .Lat_p2\n"
    ".Lat_maxpass:\n"
    "s_waitcnt lgkmcnt(0)\n"
    "s_mul_i32 s48, s33, 81920\n"
    "s_add_u32 s48, s48, 11345920\n"
    "s_add_u32 s45, s32, 0\n"
    "s_and_b32 s45, s45, 3\n"
    "s_lshl2_add_u32 s45, s50, s45\n"
    "s_lshl_b32 s45, s45, 13\n"
    "s_add_u32 s45, s45, s48\n"
    "s_add_u32 s46, s45, 4096\n"
    "buffer_load_dwordx4 a[0:3], v250, s[8:11], s45 offen offset:0 sc1\n"
    "buffer_load_dwordx4 a[4:7], v250, s[8:11], s45 offen offset:1024 sc1\n"
    "buffer_load_dwordx4 a[8:11], v250, s[8:11], s45 offen offset:2048 sc1\n"
    "buffer_load_dwordx4 a[12:15], v250, s[8:11], s45 offen offset:3072 sc1\n"
    "buffer_load_dwordx4 a[16:19], v250, s[8:11], s46 offen offset:0 sc1\n"
    "buffer_load_dwordx4 a[20:23], v250, s[8:11], s46 offen offset:1024 sc1\n"
    "buffer_load_dwordx4 a[24:27], v250, s[8:11], s46 offen offset:2048 sc1\n"
    "buffer_load_dwordx4 a[28:31], v250, s[8:11], s46 offen offset:3072 sc1\n"
    "s_and_b32 s45, s32, 1\n"
    "s_xor_b32 s45, s45, s50\n"
    "s_add_u32 s45, s45, 8\n"
    "s_lshl_b32 s45, s45, 13\n"
    "s_add_u32 s45, s45, s48\n"
    "s_add_u32 s46, s45, 4096\n"
    "buffer_load_dwordx4 a[32:35], v250, s[8:11], s45 offen offset:0 sc1\n"
    "buffer_load_dwordx4 a[36:39], v250, s[8:11], s45 offen offset:1024 sc1\n"
    "buffer_load_dwordx4 a[40:43], v250, s[8:11], s45 offen offset:2048 sc1\n"
    "buffer_load_dwordx4 a[44:47], v250, s[8:11], s45 offen offset:3072 sc1\n"
    "buffer_load_dwordx4 a[48:51], v250, s[8:11], s46 offen offset:0 sc1\n"
    "buffer_load_dwordx4 a[52:55], v250, s[8:11], s46 offen offset:1024 sc1\n"
    "buffer_load_dwordx4 a[56:59], v250, s[8:11], s46 offen offset:2048 sc1\n"
    "buffer_load_dwordx4 a[60:63], v250, s[8:11], s46 offen offset:3072 sc1\n"
    "s_add_u32 s45, s32, 1\n"
    "s_and_b32 s45, s45, 3\n"
    "s_lshl2_add_u32 s45, s50, s45\n"
    "s_lshl_b32 s45, s45, 13\n"
    "s_add_u32 s45, s45, s48\n"
    "s_add_u32 s46, s45, 4096\n"
    "buffer_load_dwordx4 a[64:67], v250, s[8:11], s45 offen offset:0 sc1\n"
    "buffer_load_dwordx4 a[68:71], v250, s[8:11], s45 offen offset:1024 sc1\n"
    "buffer_load_dwordx4 a[72:75], v250, s[8:11], s45 offen offset:2048 sc1\n"
    "buffer_load_dwordx4 a[76:79], v250, s[8:11], s45 offen offset:3072 sc1\n"
    "buffer_load_dwordx4 a[80:83], v250, s[8:11], s46 offen offset:0 sc1\n"
    "buffer_load_dwordx4 a[84:87], v250, s[8:11], s46 offen offset:1024 sc1\n"
    "buffer_load_dwordx4 a[88:91], v250, s[8:11], s46 offen offset:2048 sc1\n"
    "buffer_load_dwordx4 a[92:95], v250, s[8:11], s46 offen offset:3072 sc1\n"
    "s_add_u32 s45, s32, 2\n"
    "s_and_b32 s45, s45, 3\n"
    "s_lshl2_add_u32 s45, s50, s45\n"
    "s_lshl_b32 s45, s45, 13\n"
    "s_add_u32 s45, s45, s48\n"
    "s_add_u32 s46, s45, 4096\n"
    "buffer_load_dwordx4 a[96:99], v250, s[8:11], s45 offen offset:0 sc1\n"
    "buffer_load_dwordx4 a[100:103], v250, s[8:11], s45 offen offset:1024 sc1\n"
    "buffer_load_dwordx4 a[104:107], v250, s[8:11], s45 offen offset:2048 sc1\n"
    "buffer_load_dwordx4 a[108:111], v250, s[8:11], s45 offen offset:3072 sc1\n"
    "buffer_load_dwordx4 a[112:115], v250, s[8:11], s46 offen offset:0 sc1\n"
    "buffer_load_dwordx4 a[116:119], v250, s[8:11], s46 offen offset:1024 sc1\n"
    "buffer_load_dwordx4 a[120:123], v250, s[8:11], s46 offen offset:2048 sc1\n"
    "buffer_load_dwordx4 a[124:127], v250, s[8:11], s46 offen offset:3072 sc1\n"
    "s_add_u32 s45, s32, 3\n"
    "s_and_b32 s45, s45, 3\n"
    "s_lshl2_add_u32 s45, s50, s45\n"
    "s_lshl_b32 s45, s45, 13\n"
    "s_add_u32 s45, s45, s48\n"
    "s_add_u32 s46, s45, 4096\n"
    "buffer_load_dwordx4 a[128:131], v250, s[8:11], s45 offen offset:0 sc1\n"
    "buffer_load_dwordx4 a[132:135], v250, s[8:11], s45 offen offset:1024 sc1\n"
    "buffer_load_dwordx4 a[136:139], v250, s[8:11], s45 offen offset:2048 sc1\n"
    "buffer_load_dwordx4 a[140:143], v250, s[8:11], s45 offen offset:3072 sc1\n"
    "buffer_load_dwordx4 a[144:147], v250, s[8:11], s46 offen offset:0 sc1\n"
    "buffer_load_dwordx4 a[148:151], v250, s[8:11], s46 offen offset:1024 sc1\n"
    "buffer_load_dwordx4 a[152:155], v250, s[8:11], s46 offen offset:2048 sc1\n"
    "buffer_load_dwordx4 a[156:159], v250, s[8:11], s46 offen offset:3072 sc1\n"
    "s_waitcnt vmcnt(0)\n"
    "v_mov_b32_e32 v200, 0xff800000\n"
    "v_mov_b32_e32 v201, 0xff800000\n"
    "v_mov_b32_e32 v202, 0xff800000\n"
    "v_mov_b32_e32 v203, 0xff800000\n"
    "v_mov_b32_e32 v204, 0xff800000\n"
    "s_lshl_b32 s45, s51, 14\n"
    "s_lshl_b32 s46, s50, 8\n"
    "s_add_u32 s45, s45, s46\n"
    "s_add_u32 s61, s46, 5103616\n"
    "s_movk_i32 s48, 157\n"
    ".Lat_mp_loop:\n"
    "s_min_u32 s45, s45, s61\n"
    "s_add_u32 s46, s45, 128\n"
    "s_add_u32 m0, s43, 16384\n"
    "s_nop 0\n"
    "buffer_load_dwordx4 v248, s[8:11], s45 offen offset:0 sc1 lds\n"
    "buffer_load_dwordx4 v248, s[8:11], s45 offen offset:1024 sc1 lds\n"
    "buffer_load_dwordx4 v248, s[8:11], s45 offen offset:2048 sc1 lds\n"
    "buffer_load_dwordx4 v248, s[8:11], s45 offen offset:3072 sc1 lds\n"
    "s_add_u32 m0, s43, 20480\n"
    "s_nop 0\n"
    "buffer_load_dwordx4 v248, s[8:11], s46 offen offset:0 sc1 lds\n"
    "buffer_load_dwordx4 v248, s[8:11], s46 offen offset:1024 sc1 lds\n"
    "buffer_load_dwordx4 v248, s[8:11], s46 offen offset:2048 sc1 lds\n"
    "buffer_load_dwordx4 v248, s[8:11], s46 offen offset:3072 sc1 lds\n"
    "s_waitcnt vmcnt(0)\n"
    "s_barrier\n"
    "ds_read_b128 a[160:163], v240 offset:16384\n"
    "ds_read_b128 a[164:167], v241 offset:16384\n"
    "ds_read_b128 a[168:171], v240 offset:20480\n"
    "ds_read_b128 a[172:175], v241 offset:20480\n"
    "ds_read_b128 a[176:179], v242 offset:16384\n"
    "ds_read_b128 a[180:183], v243 offset:16384\n"
    "ds_read_b128 a[184:187], v242 offset:20480\n"
    "ds_read_b128 a[188:191], v243 offset:20480\n"
    "s_waitcnt lgkmcnt(0)\n"
    "v_mfma_f32_16x16x32_bf16 v[180:183], a[160:163], a[0:3], 0\n"
    "v_mfma_f32_16x16x32_bf16 v[184:187], a[160:163], a[32:35], 0\n"
    "v_mfma_f32_16x16x32_bf16 v[188:191], a[160:163], a[64:67], 0\n"
    "v_mfma_f32_16x16x32_bf16 v[192:195], a[160:163], a[96:99], 0\n"
    "v_mfma_f32_16x16x32_bf16 v[196:199], a[160:163], a[128:131], 0\n"
    "v_mfma_f32_16x16x32_bf16 v[180:183], a[164:167], a[4:7], v[180:183]\n"
    "v_mfma_f32_16x16x32_bf16 v[184:187], a[164:167], a[36:39], v[184:187]\n"
    "v_mfma_f32_16x16x32_bf16 v[188:191], a[164:167], a[68:71], v[188:191]\n"
    "v_mfma_f32_16x16x32_bf16 v[192:195], a[164:167], a[100:103], v[192:195]\n"
    "v_mfma_f32_16x16x32_bf16 v[196:199], a[164:167], a[132:135], v[196:199]\n"
    "v_mfma_f32_16x16x32_bf16 v[180:183], a[168:171], a[8:11], v[180:183]\n"
    "v_mfma_f32_16x16x32_bf16 v[184:187], a[168:171], a[40:43], v[184:187]\n"
    "v_mfma_f32_16x16x32_bf16 v[188:191], a[168:171], a[72:75], v[188:191]\n"
    "v_mfma_f32_16x16x32_bf16 v[192:195], a[168:171], a[104:107], v[192:195]\n"
    "v_mfma_f32_16x16x32_bf16 v[196:199], a[168:171], a[136:139], v[196:199]\n"
    "v_mfma_f32_16x16x32_bf16 v[180:183], a[172:175], a[12:15], v[180:183]\n"
    "v_mfma_f32_16x16x32_bf16 v[184:187], a[172:175], a[44:47], v[184:187]\n"
    "v_mfma_f32_16x16x32_bf16 v[188:191], a[172:175], a[76:79], v[188:191]\n"
    "v_mfma_f32_16x16x32_bf16 v[192:195], a[172:175], a[108:111], v[192:195]\n"
    "v_mfma_f32_16x16x32_bf16 v[196:199], a[172:175], a[140:143], v[196:199]\n"
    "v_mfma_f32_16x16x32_bf16 v[180:183], a[176:179], a[16:19], v[180:183]\n"
    "v_mfma_f32_16x16x32_bf16 v[184:187], a[176:179], a[48:51], v[184:187]\n"
    "v_mfma_f32_16x16x32_bf16 v[188:191], a[176:179], a[80:83], v[188:191]\n"
    "v_mfma_f32_16x16x32_bf16 v[192:195], a[176:179], a[112:115], v[192:195]\n"
    "v_mfma_f32_16x16x32_bf16 v[196:199], a[176:179], a[144:147], v[196:199]\n"
    "v_mfma_f32_16x16x32_bf16 v[180:183], a[180:183], a[20:23], v[180:183]\n"
    "v_mfma_f32_16x16x32_bf16 v[184:187], a[180:183], a[52:55], v[184:187]\n"
    "v_mfma_f32_16x16x32_bf16 v[188:191], a[180:183], a[84:87], v[188:191]\n"
    "v_mfma_f32_16x16x32_bf16 v[192:195], a[180:183], a[116:119], v[192:195]\n"
    "v_mfma_f32_16x16x32_bf16 v[196:199], a[180:183], a[148:151], v[196:199]\n"
    "v_mfma_f32_16x16x32_bf16 v[180:183], a[184:187], a[24:27], v[180:183]\n"
    "v_mfma_f32_16x16x32_bf16 v[184:187], a[184:187], a[56:59], v[184:187]\n"
    "v_mfma_f32_16x16x32_bf16 v[188:191], a[184:187], a[88:91], v[188:191]\n"
    "v_mfma_f32_16x16x32_bf16 v[192:195], a[184:187], a[120:123], v[192:195]\n"
    "v_mfma_f32_16x16x32_bf16 v[196:199], a[184:187], a[152:155], v[196:199]\n"
    "v_mfma_f32_16x16x32_bf16 v[180:183], a[188:191], a[28:31], v[180:183]\n"
    "v_mfma_f32_16x16x32_bf16 v[184:187], a[188:191], a[60:63], v[184:187]\n"
    "v_mfma_f32_16x16x32_bf16 v[188:191], a[188:191], a[92:95], v[188:191]\n"
    "v_mfma_f32_16x16x32_bf16 v[192:195], a[188:191], a[124:127], v[192:195]\n"
    "v_mfma_f32_16x16x32_bf16 v[196:199], a[188:191], a[156:159], v[196:199]\n"
    "s_nop 15\n"
    "v_max3_f32 v200, v200, v180, v181\n"
    "v_max3_f32 v200, v200, v182, v183\n"
    "v_max3_f32 v201, v201, v184, v185\n"
    "v_max3_f32 v201, v201, v186, v187\n"
    "v_max3_f32 v202, v202, v188, v189\n"
    "v_max3_f32 v202, v202, v190, v191\n"
    "v_max3_f32 v203, v203, v192, v193\n"
    "v_max3_f32 v203, v203, v194, v195\n"
    "v_max3_f32 v204, v204, v196, v197\n"
    "v_max3_f32 v204, v204, v198, v199\n"
    "s_nop 3\n"
    "ds_read_b128 a[160:163], v240 offset:18432\n"
    "ds_read_b128 a[164:167], v241 offset:18432\n"
    "ds_read_b128 a[168:171], v240 offset:22528\n"
    "ds_read_b128 a[172:175], v241 offset:22528\n"
    "ds_read_b128 a[176:179], v242 offset:18432\n"
    "ds_read_b128 a[180:183], v243 offset:18432\n"
    "ds_read_b128 a[184:187], v242 offset:22528\n"
    "ds_read_b128 a[188:191], v243 offset:22528\n"
    "s_waitcnt lgkmcnt(0)\n"
    "v_mfma_f32_16x16x32_bf16 v[180:183], a[160:163], a[0:3], 0\n"
    "v_mfma_f32_16x16x32_bf16 v[184:187], a[160:163], a[32:35], 0\n"
    "v_mfma_f32_16x16x32_bf16 v[188:191], a[160:163], a[64:67], 0\n"
    "v_mfma_f32_16x16x32_bf16 v[192:195], a[160:163], a[96:99], 0\n"
    "v_mfma_f32_16x16x32_bf16 v[196:199], a[160:163], a[128:131], 0\n"
    "v_mfma_f32_16x16x32_bf16 v[180:183], a[164:167], a[4:7], v[180:183]\n"
    "v_mfma_f32_16x16x32_bf16 v[184:187], a[164:167], a[36:39], v[184:187]\n"
    "v_mfma_f32_16x16x32_bf16 v[188:191], a[164:167], a[68:71], v[188:191]\n"
    "v_mfma_f32_16x16x32_bf16 v[192:195], a[164:167], a[100:103], v[192:195]\n"
    "v_mfma_f32_16x16x32_bf16 v[196:199], a[164:167], a[132:135], v[196:199]\n"
    "v_mfma_f32_16x16x32_bf16 v[180:183], a[168:171], a[8:11], v[180:183]\n"
    "v_mfma_f32_16x16x32_bf16 v[184:187], a[168:171], a[40:43], v[184:187]\n"
    "v_mfma_f32_16x16x32_bf16 v[188:191], a[168:171], a[72:75], v[188:191]\n"
    "v_mfma_f32_16x16x32_bf16 v[192:195], a[168:171], a[104:107], v[192:195]\n"
    "v_mfma_f32_16x16x32_bf16 v[196:199], a[168:171], a[136:139], v[196:199]\n"
    "v_mfma_f32_16x16x32_bf16 v[180:183], a[172:175], a[12:15], v[180:183]\n"
    "v_mfma_f32_16x16x32_bf16 v[184:187], a[172:175], a[44:47], v[184:187]\n"
    "v_mfma_f32_16x16x32_bf16 v[188:191], a[172:175], a[76:79], v[188:191]\n"
    "v_mfma_f32_16x16x32_bf16 v[192:195], a[172:175], a[108:111], v[192:195]\n"
    "v_mfma_f32_16x16x32_bf16 v[196:199], a[172:175], a[140:143], v[196:199]\n"
    "v_mfma_f32_16x16x32_bf16 v[180:183], a[176:179], a[16:19], v[180:183]\n"
    "v_mfma_f32_16x16x32_bf16 v[184:187], a[176:179], a[48:51], v[184:187]\n"
    "v_mfma_f32_16x16x32_bf16 v[188:191], a[176:179], a[80:83], v[188:191]\n"
    "v_mfma_f32_16x16x32_bf16 v[192:195], a[176:179], a[112:115], v[192:195]\n"
    "v_mfma_f32_16x16x32_bf16 v[196:199], a[176:179], a[144:147], v[196:199]\n"
    "v_mfma_f32_16x16x32_bf16 v[180:183], a[180:183], a[20:23], v[180:183]\n"
    "v_mfma_f32_16x16x32_bf16 v[184:187], a[180:183], a[52:55], v[184:187]\n"
    "v_mfma_f32_16x16x32_bf16 v[188:191], a[180:183], a[84:87], v[188:191]\n"
    "v_mfma_f32_16x16x32_bf16 v[192:195], a[180:183], a[116:119], v[192:195]\n"
    "v_mfma_f32_16x16x32_bf16 v[196:199], a[180:183], a[148:151], v[196:199]\n"
    "v_mfma_f32_16x16x32_bf16 v[180:183], a[184:187], a[24:27], v[180:183]\n"
    "v_mfma_f32_16x16x32_bf16 v[184:187], a[184:187], a[56:59], v[184:187]\n"
    "v_mfma_f32_16x16x32_bf16 v[188:191], a[184:187], a[88:91], v[188:191]\n"
    "v_mfma_f32_16x16x32_bf16 v[192:195], a[184:187], a[120:123], v[192:195]\n"
    "v_mfma_f32_16x16x32_bf16 v[196:199], a[184:187], a[152:155], v[196:199]\n"
    "v_mfma_f32_16x16x32_bf16 v[180:183], a[188:191], a[28:31], v[180:183]\n"
    "v_mfma_f32_16x16x32_bf16 v[184:187], a[188:191], a[60:63], v[184:187]\n"
    "v_mfma_f32_16x16x32_bf16 v[188:191], a[188:191], a[92:95], v[188:191]\n"
    "v_mfma_f32_16x16x32_bf16 v[192:195], a[188:191], a[124:127], v[192:195]\n"
    "v_mfma_f32_16x16x32_bf16 v[196:199], a[188:191], a[156:159], v[196:199]\n"
    "s_nop 15\n"
    "v_max3_f32 v200, v200, v180, v181\n"
    "v_max3_f32 v200, v200, v182, v183\n"
    "v_max3_f32 v201, v201, v184, v185\n"
    "v_max3_f32 v201, v201, v186, v187\n"
    "v_max3_f32 v202, v202, v188, v189\n"
    "v_max3_f32 v202, v202, v190, v191\n"
    "v_max3_f32 v203, v203, v192, v193\n"
    "v_max3_f32 v203, v203, v194, v195\n"
    "v_max3_f32 v204, v204, v196, v197\n"
    "v_max3_f32 v204, v204, v198, v199\n"
    "s_nop 3\n"
    "s_barrier\n"
    "s_add_u32 s45, s45, 0x8000\n"
    "s_sub_u32 s48, s48, 1\n"
    "s_cmp_lg_u32 s48, 0\n"
    "s_cbranch_scc1 .Lat_mp_loop\n"
    "s_mov_b32 m0, s42\n"
    "s_nop 1\n"
    "ds_write_addtid_b32 v200 offset:0\n"
    "ds_write_addtid_b32 v201 offset:256\n"
    "ds_write_addtid_b32 v202 offset:512\n"
    "ds_write_addtid_b32 v203 offset:768\n"
    "ds_write_addtid_b32 v204 offset:1024\n"
    "s_waitcnt lgkmcnt(0)\n"
    "s_barrier\n"
    "ds_read_b32 v180, v249 offset:0\n"
    "ds_read_b32 v184, v249 offset:256\n"
    "ds_read_b32 v188, v249 offset:512\n"
    "ds_read_b32 v192, v249 offset:768\n"
    "ds_read_b32 v196, v249 offset:1024\n"
    "s_waitcnt lgkmcnt(0)\n"
    "v_max_f32_e32 v205, v200, v180\n"
    "v_mov_b32_e32 v206, v205\n"
    "s_nop 1\n"
    "v_permlane16_swap_b32_e32 v205, v206\n"
    "s_nop 0\n"
    "v_max_f32_e32 v205, v205, v206\n"
    "v_mov_b32_e32 v206, v205\n"
    "s_nop 1\n"
    "v_permlane32_swap_b32_e32 v205, v206\n"
    "s_nop 0\n"
    "v_max_f32_e32 v205, v205, v206\n"
    "v_add_f32_e32 v205, 0x42540000, v205\n"
    "v_xor_b32_e32 v220, 0x80000000, v205\n"
    "v_xor_b32_e32 v221, 0x80000000, v205\n"
    "v_xor_b32_e32 v222, 0x80000000, v205\n"
    "v_xor_b32_e32 v223, 0x80000000, v205\n"
    "v_max_f32_e32 v205, v201, v184\n"
    "v_mov_b32_e32 v206, v205\n"
    "s_nop 1\n"
    "v_permlane16_swap_b32_e32 v205, v206\n"
    "s_nop 0\n"
    "v_max_f32_e32 v205, v205, v206\n"
    "v_mov_b32_e32 v206, v205\n"
    "s_nop 1\n"
    "v_permlane32_swap_b32_e32 v205, v206\n"
    "s_nop 0\n"
    "v_max_f32_e32 v205, v205, v206\n"
    "v_add_f32_e32 v205, 0x42540000, v205\n"
    "v_xor_b32_e32 v224, 0x80000000, v205\n"
    "v_xor_b32_e32 v225, 0x80000000, v205\n"
    "v_xor_b32_e32 v226, 0x80000000, v205\n"
    "v_xor_b32_e32 v227, 0x80000000, v205\n"
    "v_max_f32_e32 v205, v202, v188\n"
    "v_mov_b32_e32 v206, v205\n"
    "s_nop 1\n"
    "v_permlane16_swap_b32_e32 v205, v206\n"
    "s_nop 0\n"
    "v_max_f32_e32 v205, v205, v206\n"
    "v_mov_b32_e32 v206, v205\n"
    "s_nop 1\n"
    "v_permlane32_swap_b32_e32 v205, v206\n"
    "s_nop 0\n"
    "v_max_f32_e32 v205, v205, v206\n"
    "v_add_f32_e32 v205, 0x42540000, v205\n"
    "v_xor_b32_e32 v228, 0x80000000, v205\n"
    "v_xor_b32_e32 v229, 0x80000000, v205\n"
    "v_xor_b32_e32 v230, 0x80000000, v205\n"
    "v_xor_b32_e32 v231, 0x80000000, v205\n"
    "v_max_f32_e32 v205, v203, v192\n"
    "v_mov_b32_e32 v206, v205\n"
    "s_nop 1\n"
    "v_permlane16_swap_b32_e32 v205, v206\n"
    "s_nop 0\n"
    "v_max_f32_e32 v205, v205, v206\n"
    "v_mov_b32_e32 v206, v205\n"
    "s_nop 1\n"
    "v_permlane32_swap_b32_e32 v205, v206\n"
    "s_nop 0\n"
    "v_max_f32_e32 v205, v205, v206\n"
    "v_add_f32_e32 v205, 0x42540000, v205\n"
    "v_xor_b32_e32 v232, 0x80000000, v205\n"
    "v_xor_b32_e32 v233, 0x80000000, v205\n"
    "v_xor_b32_e32 v234, 0x80000000, v205\n"
    "v_xor_b32_e32 v235, 0x80000000, v205\n"
    "v_max_f32_e32 v205, v204, v196\n"
    "v_mov_b32_e32 v206, v205\n"
    "s_nop 1\n"
    "v_permlane16_swap_b32_e32 v205, v206\n"
    "s_nop 0\n"
    "v_max_f32_e32 v205, v205, v206\n"
    "v_mov_b32_e32 v206, v205\n"
    "s_nop 1\n"
    "v_permlane32_swap_b32_e32 v205, v206\n"
    "s_nop 0\n"
    "v_max_f32_e32 v205, v205, v206\n"
    "v_add_f32_e32 v205, 0x42540000, v205\n"
    "v_xor_b32_e32 v236, 0x80000000, v205\n"
    "v_xor_b32_e32 v237, 0x80000000, v205\n"
    "v_xor_b32_e32 v238, 0x80000000, v205\n"
    "v_xor_b32_e32 v239, 0x80000000, v205\n"
    "s_nop 1\n"
    "s_barrier\n"
    "s_mul_i32 s48, s33, 81920\n"
    "s_add_u32 s48, s48, 11345920\n"
    "s_lshl_b32 s46, s93, 11\n"
    "s_add_u32 s48, s48, s46\n"
    "s_add_u32 s46, s46, 49152\n"
    "s_and_b32 s45, s32, 1\n"
    "s_xor_b32 s45, s45, 8\n"
    "s_lshl_b32 s45, s45, 13\n"
    "s_add_u32 s45, s45, s48\n"
    "s_add_u32 m0, s46, 0\n"
    "s_nop 0\n"
    "buffer_load_dwordx4 v250, s[8:11], s45 offen offset:0 sc1 lds\n"
    "buffer_load_dwordx4 v250, s[8:11], s45 offen offset:1024 sc1 lds\n"
    "s_branch .Lat_maxpass_ret\n"
    ".p2align 12\n"
    ".Lat_out:\n"
    "s_waitcnt lgkmcnt(0)\n"
    "s_mov_b64 s[2:3], s[0:1]\n"
    "s_mov_b64 %0, s[2:3]\n"
    "s_mov_b32 %1, s92\n"
    "s_mov_b32 %2, s93\n"
    : "=s"(ka), "=s"(b), "=s"(wid) : "0"(__builtin_amdgcn_kernarg_segment_ptr()), "1"(blockIdx.x)
    : "v0", "v1", "v2", "v3", "v4", "v5", "v6", "v7", "v8", "v9", "v10", "v11", "v12", "v13", "v14", "v15", "v16", "v17", "v18", "v19", "v20", "v21", "v22", "v23", "v24", "v25", "v26", "v27", "v28", "v29", "v30", "v31", "v32", "v33", "v34", "v35", "v36", "v37", "v38", "v39", "v40", "v41", "v42", "v43", "v44", "v45", "v46", "v47", "v48", "v49", "v50", "v51", "v52", "v53", "v54", "v55", "v56", "v57", "v58", "v59", "v60", "v61", "v62", "v63", "v64", "v65", "v66", "v67", "v68", "v69", "v70", "v71", "v72", "v73", "v74", "v75", "v76", "v77", "v78", "v79", "v80", "v81", "v82", "v83", "v84", "v85", "v86", "v87", "v88", "v89", "v90", "v91", "v92", "v93", "v94", "v95", "v96", "v97", "v98", "v99", "v100", "v101", "v102", "v103", "v104", "v105", "v106", "v107", "v108", "v109", "v110", "v111", "v112", "v113", "v114", "v115", "v116", "v117", "v118", "v119", "v120", "v121", "v122", "v123", "v124", "v125", "v126", "v127", "v128", "v129", "v130", "v131", "v132", "v133", "v134", "v135", "v136", "v137", "v138", "v139", "v140", "v141", "v142", "v143", "v144", "v145", "v146", "v147", "v148", "v149", "v150", "v151", "v152", "v153", "v154", "v155", "v156", "v157", "v158", "v159", "v160", "v161", "v162", "v163", "v164", "v165", "v166", "v167", "v168", "v169", "v170", "v171", "v172", "v173", "v174", "v175", "v176", "v177", "v178", "v179", "v180", "v181", "v182", "v183", "v184", "v185", "v186", "v187", "v188", "v189", "v190", "v191", "v192", "v193", "v194", "v195", "v196", "v197", "v198", "v199", "v200", "v201", "v202", "v203", "v204", "v205", "v206", "v207", "v208", "v209", "v210", "v211", "v212", "v213", "v214", "v215", "v216", "v217", "v218", "v219", "v220", "v221", "v222", "v223", "v224", "v225", "v226", "v227", "v228", "v229", "v230", "v231", "v232", "v233", "v234", "v235", "v236", "v237", "v238", "v239", "v240", "v241", "v242", "v243", "v244", "v245", "v246", "v247", "v248", "v249", "v250", "v251", "v252", "v253", "v254", "v255", "a0", "a1", "a2", "a3", "a4", "a5", "a6", "a7", "a8", "a9", "a10", "a11", "a12", "a13", "a14", "a15", "a16", "a17", "a18", "a19", "a20", "a21", "a22", "a23", "a24", "a25", "a26", "a27", "a28", "a29", "a30", "a31", "a32", "a33", "a34", "a35", "a36", "a37", "a38", "a39", "a40", "a41", "a42", "a43", "a44", "a45", "a46", "a47", "a48", "a49", "a50", "a51", "a52", "a53", "a54", "a55", "a56", "a57", "a58", "a59", "a60", "a61", "a62", "a63", "a64", "a65", "a66", "a67", "a68", "a69", "a70", "a71", "a72", "a73", "a74", "a75", "a76", "a77", "a78", "a79", "a80", "a81", "a82", "a83", "a84", "a85", "a86", "a87", "a88", "a89", "a90", "a91", "a92", "a93", "a94", "a95", "a96", "a97", "a98", "a99", "a100", "a101", "a102", "a103", "a104", "a105", "a106", "a107", "a108", "a109", "a110", "a111", "a112", "a113", "a114", "a115", "a116", "a117", "a118", "a119", "a120", "a121", "a122", "a123", "a124", "a125", "a126", "a127", "a128", "a129", "a130", "a131", "a132", "a133", "a134", "a135", "a136", "a137", "a138", "a139", "a140", "a141", "a142", "a143", "a144", "a145", "a146", "a147", "a148", "a149", "a150", "a151", "a152", "a153", "a154", "a155", "a156", "a157", "a158", "a159", "a160", "a161", "a162", "a163", "a164", "a165", "a166", "a167", "a168", "a169", "a170", "a171", "a172", "a173", "a174", "a175", "a176", "a177", "a178", "a179", "a180", "a181", "a182", "a183", "a184", "a185", "a186", "a187", "a188", "a189", "a190", "a191", "a192", "a193", "a194", "a195", "a196", "a197", "a198", "a199", "a200", "a201", "a202", "a203", "a204", "a205", "a206", "a207", "a208", "a209", "a210", "a211", "a212", "a213", "a214", "a215", "a216", "a217", "a218", "a219", "a220", "a221", "a222", "a223", "a224", "a225", "a226", "a227", "a228", "a229", "a230", "a231", "a232", "a233", "a234", "a235", "a236", "a237", "a238", "a239", "a240", "a241", "a242", "a243", "a244", "a245", "a246", "a247", "a248", "a249", "a250", "a251", "a252", "a253", "a254", "a255", "s2", "s3", "s4", "s5", "s6", "s7", "s8", "s9", "s10", "s11", "s12", "s13", "s14", "s15", "s16", "s17", "s18", "s19", "s20", "s21", "s22", "s23", "s24", "s25", "s26", "s27", "s28", "s29", "s30", "s31", "s32", "s33", "s34", "s35", "s36", "s37", "s38", "s39", "s40", "s41", "s42", "s43", "s44", "s45", "s46", "s47", "s48", "s49", "s50", "s51", "s52", "s53", "s54", "s55", "s56", "s57", "s58", "s59", "s60", "s61", "s62", "s63", "s80", "s81", "s82", "s83", "s84", "s85", "s86", "s87", "s88", "s89", "s90", "s91", "s92", "s93", "s94", "s95", "vcc", "m0", "scc", "memory");
  typedef __attribute__((address_space(1))) void* gp;
  helper(lds, b >> 1 & 3, wid, (const int*)(gp)ka[2], (const int*)(gp)ka[4], (const u16*)(gp)ka[8], (const u16*)(gp)ka[9], (const u16*)(gp)ka[10], (char*)(gp)ka[3]);
}

extern "C" void kernel_launch(void* const* d_in, const int* in_sizes, int n_in, void* d_out, int out_size, void* d_ws,
                              size_t ws_size, hipStream_t stream) {
  const u16* x = (const u16*)d_in[0];
  const int* src = (const int*)d_in[1];
  const int* dst = (const int*)d_in[2];
  const u16* deg_inv = (const u16*)d_in[3];
  const u16* wmsg = (const u16*)d_in[4];
  const u16* wq = (const u16*)d_in[5];
  const u16* wk = (const u16*)d_in[6];
  const u16* wv = (const u16*)d_in[7];
  const u16* wout = (const u16*)d_in[8];
  char* ws = (char*)d_ws;
  hipFuncSetAttribute((const void*)gps_kernel, hipFuncAttributeMaxDynamicSharedMemorySize, 163840);
  gps_kernel<<<256, 256, 163840, stream>>>(wq, wk, src, ws, dst, x, (u16*)d_out, deg_inv, wv, wmsg, wout);
}
```

```cpp
#include <hip/hip_runtime.h>
#include <hip/hip_bf16.h>
#include <stdint.h>

typedef unsigned short u16;
typedef __attribute__((ext_vector_type(8))) short bf16x8;
typedef __attribute__((ext_vector_type(4))) short bf16x4;
typedef __attribute__((ext_vector_type(4))) float f32x4;
typedef __attribute__((ext_vector_type(2))) float f32x2;
typedef __attribute__((ext_vector_type(4))) unsigned u32x4;
typedef __attribute__((ext_vector_type(2))) unsigned u32x2;
#define DI __device__ __forceinline__

constexpr int NN = 10000, DD = 256, EE = 160000;
constexpr int NATT = 252, NSP = 4, SPN = 2560;
constexpr int GU = 5, GNB = 5; static_assert(GU * GNB <= 30, "list size");

constexpr unsigned WS_H = 0;
constexpr unsigned WS_AF = WS_H + 10240 * 512;
constexpr unsigned WS_WV = WS_AF + 135168;
constexpr unsigned WS_WM = WS_WV + 135168;
constexpr unsigned WS_XF = WS_WM + 135168;
constexpr unsigned WS_GF = WS_XF + 3328 * 128;
constexpr unsigned WS_HF = WS_GF + 2048;
constexpr unsigned WS_PF = WS_HF + 4096;
constexpr unsigned WS_NF = WS_PF + 4096;
constexpr unsigned WS_WF = WS_NF + 16384;
constexpr unsigned WS_LF = WS_WF + 256;
constexpr unsigned WS_EP = WS_LF + 512;
constexpr unsigned WS_G = WS_EP + 256 + 1024;
constexpr unsigned WS_QP = WS_G + 10240 * 512;
constexpr unsigned WS_XB = WS_QP + 10240 * 512;
constexpr unsigned WS_LS = WS_XB + 2 * NATT * 101376;
constexpr unsigned WS_BL = WS_LS + 524288;
constexpr unsigned WS_DBG = WS_BL + 64 * 260 * 64;
constexpr unsigned WS_END = WS_DBG + 1024 * 128 + 8192;

DI float bf2f(unsigned x) { return __uint_as_float(x << 16); }
DI float bfhi(unsigned x) { return __uint_as_float(x & 0xffff0000u); }
DI unsigned pk2(float lo, float hi) {
  typedef __attribute__((ext_vector_type(2))) __bf16 bf2_t;
  const bf2_t r = __builtin_convertvector((f32x2){lo, hi}, bf2_t);
  return __builtin_bit_cast(unsigned, r);
}
typedef __attribute__((address_space(3))) bf16x4* lds_bf16x4_p;
DI bf16x4 tr_read(unsigned p) { return __builtin_amdgcn_ds_read_tr16_b64_v4i16((lds_bf16x4_p)p); }
DI bf16x8 as_bf(u32x4 v) { return __builtin_bit_cast(bf16x8, v); }
#define SB() __builtin_amdgcn_sched_barrier(0)
#define MFMA16(a, b, c) __builtin_amdgcn_mfma_f32_16x16x32_bf16(a, b, c, 0, 0, 0)

typedef __amdgpu_buffer_rsrc_t srd_t;
DI void drain() { asm volatile("s_waitcnt vmcnt(0)" ::: "memory"); }
DI void st_flag(srd_t w, unsigned off, unsigned tag) { __builtin_amdgcn_raw_buffer_store_b32(tag, w, off, 0, 16); }
DI void wait_flag(srd_t w, unsigned off, unsigned tag) {
#pragma unroll 1
  for (int it = 0; it < (1 << 22); ++it) {
    unsigned v;
    asm volatile("buffer_load_dword %0, %1, %2, 0 offen sc1\n s_waitcnt vmcnt(0)" : "=v"(v) : "v"(off), "s"(w) : "memory");
    if (__ballot(v != tag) == 0) break;
    __builtin_amdgcn_s_sleep(2);
    asm volatile("" ::: "memory");
  }
  asm volatile("" ::: "memory");
}

DI void prod_store(const f32x4 (&acc)[2][2], int mat, int ks, int J, int lane, srd_t wsrd) {
#pragma unroll
  for (int s = 0; s < 2; ++s) {
    u32x4 o = {pk2(acc[0][s][0], acc[0][s][1]), pk2(acc[0][s][2], acc[0][s][3]), pk2(acc[1][s][0], acc[1][s][1]), pk2(acc[1][s][2], acc[1][s][3])};
    if (ks == 7 && lane >= 48) o[3] &= 0xffffu;
    const int T = 2 * J + s;
    const int at = mat * 135168 + ks * 4096 + (T & 3) * 1024;
    __builtin_amdgcn_raw_buffer_store_b128(o, wsrd, WS_WV + (T >> 2) * 33792 + at + lane * 16, 0, 16);
  }
}
DI void corr(unsigned* dl, const unsigned (&lr)[16]) {
  float a0 = 0.f, a1 = 0.f;
#pragma unroll
  for (int r = 0; r < 16; ++r) { a0 += bf2f(lr[r] & 0xffffu); a1 += bfhi(lr[r]); }
  dl[0] = pk2(a0 * -0.0625f, a1 * -0.0625f);
  dl[128] = pk2((bf2f(lr[15] & 0xffffu) - 0.25f * a0) * (1.f / 3.f), (bfhi(lr[15]) - 0.25f * a1) * (1.f / 3.f));
}
DI void product_load(char* lds, int wid, int lane, int job, const u16* __restrict__ wv, const u16* __restrict__ wm, const u16* __restrict__ wo) {
  const unsigned ldsb = (unsigned)(size_t)lds;
  const int r = lane >> 3, sw = ((lane & 7) ^ (r & 6)) * 16 + wid * 128;
  const unsigned vp = (8 * (r >> 2) + (r & 3)) * 512 + sw, vn = r * 512 + sw;
  auto image = [&](int n, const u16* w, int row0, bool nat) {
#pragma unroll
    for (int i = 0; i < 8; ++i) {
      const int a = i & 3;
      asm volatile("s_mov_b32 m0, %2\n s_nop 0\n global_load_lds_dwordx4 %0, %1"
                   :: "v"(nat ? vn : vp), "s"(w + (row0 + (i >> 2) * 32 + (nat ? 8 * a : 16 * (a & 1) + 4 * (a >> 1))) * DD),
                      "s"(ldsb + n * 32768 + (i >> 2) * 16384 + wid * 4096 + a * 1024) : "m0", "memory");
    }
  };
  image(2, wv, (job >> 2) * 64, true);
  image(3, wm, (job >> 2) * 64, false);
#pragma unroll
  for (int k = 0; k < 8; ++k)
    asm volatile("s_mov_b32 m0, %2\n s_nop 0\n global_load_lds_dwordx4 %0, %1"
                 :: "v"((lane >> 3) * 512 + (lane & 7) * 16), "s"(wo + (k * 32 + wid * 8) * DD + (job & 3) * 64), "s"(ldsb + k * 4096 + wid * 1024) : "m0", "memory");
}
DI void product_w(char* lds, int wid, int lane, int job, srd_t wsrd) {
  const int m = lane & 15, g = lane >> 4;
  const int ks = 2 * (job >> 2) + (wid >> 1), J = 2 * (job & 3) + (wid & 1);
  const unsigned ldsb = (unsigned)(size_t)lds;
  unsigned* dl = reinterpret_cast<unsigned*>(lds + 0x20000);
  const bool hi = (m >> 3) != 0;
  const unsigned ra = (m >> 3) * 1024 + (m & 7) * 128 + (g ^ (m & 6)) * 16;
  auto frag = [&](int n, int tl, int t, int k) {
    return *reinterpret_cast<const bf16x8*>(lds + n * 32768 + tl * 16384 + (k >> 1) * 4096 + t * 2048 + (ra ^ ((k & 1) * 64)));
  };
  f32x4 av[2][2] = {}, am[2][2] = {}, ad[2] = {};
  const unsigned tb = ldsb + (8 * g + ((lane >> 2) & 3)) * 128 + (wid & 1) * 64 + (lane & 3) * 16;
  bf16x8 d0[8], d1[8], d2[8], rr[8][2], vf[8][2], mf[8][2];
#pragma unroll
  for (int kk = 0; kk < 9; ++kk) {
    if (kk < 8) {
      d0[kk] = *reinterpret_cast<const bf16x8*>(dl + 512 + 16 * kk + 4 * g); d1[kk] = *reinterpret_cast<const bf16x8*>(dl + 512 + (ks == 7 ? 128 : 0) + 16 * kk + 4 * g);
      d2[kk] = *reinterpret_cast<const bf16x8*>(dl + 768 + (ks == 7 && hi ? 128 : 0) + 16 * kk + 4 * g);
#pragma unroll
      for (int s = 0; s < 2; ++s) {
        rr[kk][s] = __builtin_shufflevector(tr_read(tb + kk * 4096 + s * 8), tr_read(tb + kk * 4096 + s * 8 + 512), 0, 1, 2, 3, 4, 5, 6, 7);
        vf[kk][s] = frag(2, wid >> 1, s, kk); mf[kk][s] = frag(3, wid >> 1, s, kk);
      }
    }
    SB();
    if (kk) {
      const int k = kk - 1;
#pragma unroll
      for (int s = 0; s < 2; ++s) {
        av[0][s] = MFMA16(d0[k], rr[k][s], av[0][s]); av[1][s] = MFMA16(d1[k], rr[k][s], av[1][s]); ad[s] = MFMA16(d2[k], rr[k][s], ad[s]);
#pragma unroll
        for (int t = 0; t < 2; ++t) { av[t][s] = MFMA16(vf[k][t], rr[k][s], av[t][s]); am[t][s] = MFMA16(mf[k][t], rr[k][s], am[t][s]); }
      }
    }
    SB();
  }
#pragma unroll
  for (int t = 0; t < 2; ++t)
#pragma unroll
    for (int s = 0; s < 2; ++s) am[t][s] += ad[s];
  prod_store(av, 0, ks, J, lane, wsrd);
  prod_store(am, 1, ks, J, lane, wsrd);
}

DI void helper(char* lds, int j, int wid, const int* __restrict__ src, const int* __restrict__ dst,
               const u16* __restrict__ wv, const u16* __restrict__ wm, const u16* __restrict__ wo, const u16* dinv, char* ws) {
  unsigned* img = reinterpret_cast<unsigned*>(lds);
  char* cntb = lds + 0x20000;
  int* cnt = reinterpret_cast<int*>(cntb);
  unsigned* ub = reinterpret_cast<unsigned*>(lds + 0x22c00);
  int* red = reinterpret_cast<int*>(ub + 256);
  u16* dg = reinterpret_cast<u16*>(lds + 0x23400);
  u16* tmp = reinterpret_cast<u16*>(lds + 0x25000) + wid * 512;
  int lane; asm volatile("v_mbcnt_lo_u32_b32 %0, -1, 0\n\tv_mbcnt_hi_u32_b32 %0, -1, %0" : "=v"(lane));
  const int tid = wid * 64 + lane, lo = j * SPN, nn_ = min(SPN, NN - lo);
  const srd_t wsrd = __builtin_amdgcn_make_buffer_rsrc(ws, 0, WS_END, 0x00020000);
  const unsigned tag = __builtin_amdgcn_raw_buffer_load_b32(wsrd, WS_EP, 0, 0) + 4u;
  auto touch = [&](unsigned at) { asm volatile("s_mov_b32 m0, 0x27c00\n s_nop 0\n buffer_load_dword %0, %1, 0 offen sc1 lds" :: "v"(at), "s"(wsrd) : "m0", "memory"); };
  touch(lane < 16 ? min(WS_GF + (lane * 16 + wid * 4 + j) * 128, WS_EP) : 0x40000000u);
  for (int i = tid; i < 8192; i += 256) { const unsigned f = (i & 63) >= 60 ? 0u : ~0u; reinterpret_cast<u32x4*>(img)[i] = (u32x4){f, f, f, f}; }
  for (int i = tid; i < SPN + 64; i += 256) cnt[i] = 0;
  ub[tid] = 0u;
  if (tid < 8) red[tid] = 0;
  touch(WS_XF + 262144 + (j * 256 + tid) * 128);
  wait_flag(wsrd, WS_NF + j * 4096 + min(tid, 249) * 16, tag);
  __syncthreads();
  const unsigned dum = (SPN + lane) << 18;
  auto fetch = [&](int k) {
    return __builtin_amdgcn_raw_buffer_load_b128(wsrd, WS_BL + (k < 48 ? tid * 16 + (k & 7) * 4096 : (tid >> 2) * 128 + (tid & 3) * 16 + (k & 3) * 8192 + (k & 4) * 16), (j * 8 + (k >> 3)) * 33280, 16);
  };
  auto clean = [&](u32x4& e, int k) {
#pragma unroll
    for (int r = 0; r < 4; ++r) e[r] = (k < 48 ? (k & 7) == 7 && tid >= 208 : (k & 3) == 3 && tid >= 232) ? dum : min(e[r], dum);
  };
  auto slot = [&](unsigned e) { return reinterpret_cast<int*>(cntb + (e >> 16)); };
  constexpr int NL = 52;
  u32x4 ent[NL];
#pragma unroll
  for (int k = 0; k < NL; ++k) ent[k] = fetch(k);
  SB();
#pragma unroll
  for (int k = 0; k < NL; ++k) {
    clean(ent[k], k);
    int* p[4];
#pragma unroll
    for (int r = 0; r < 4; ++r) p[r] = slot(ent[k][r]);
    SB();
#pragma unroll
    for (int r = 0; r < 4; ++r) atomicAdd(p[r], 2);
    SB();
  }
  unsigned use = 0;
  const unsigned dum3 = (tid & 3) == 3 ? dum : 0;
#pragma unroll
  for (int k = 0; k < 4; ++k) use |= __ballot(ent[NL - 4 + k][3] < dum3) ? 1u << k : 0u;
  if (use)
#pragma unroll 1
    for (int k = NL; k < 64; ++k)
      if (use >> (k - NL) & 1) {
        u32x4 e = fetch(k); clean(e, k);
#pragma unroll
        for (int r = 0; r < 4; ++r) atomicAdd(slot(e[r]), 2);
        if (__ballot(e[3] < dum3)) { use |= 16u << (k - NL); if (k >= 60) red[7] = 1; }
      }
  __syncthreads();
  int inc = 0, nu[10], cn[10], cm = 0;
  auto units = [&](int c) { return max(1, (c + 6) >> 3); };
#pragma unroll
  for (int i = 0; i < 10; i += 2) { const u32x2 v = *reinterpret_cast<u32x2*>(cnt + 10 * tid + i); cn[i] = v[0]; cn[i + 1] = v[1]; }
  const int nup = units(cnt[max(10 * tid - 1, 0)]);
  const bool act = 10 * tid < nn_;
#pragma unroll
  for (int i = 0; i < 10; ++i) { cm = max(cm, cn[i]); nu[i] = act ? units(cn[i]) : 0; inc += nu[i]; }
  const int own = inc;
  inc += __builtin_amdgcn_update_dpp(0, inc, 0x111, 15, 15, true); inc += __builtin_amdgcn_update_dpp(0, inc, 0x112, 15, 15, true);
  inc += __builtin_amdgcn_update_dpp(0, inc, 0x114, 15, 15, true); inc += __builtin_amdgcn_update_dpp(0, inc, 0x118, 15, 15, true);
  inc += __builtin_amdgcn_update_dpp(0, inc, 0x142, 10, 15, false); inc += __builtin_amdgcn_update_dpp(0, inc, 0x143, 12, 15, false);
  if (lane == 63) red[wid] = inc;
  const bool srt = __ballot(cm > 64);
  __syncthreads();
  const u32x4 rd = *reinterpret_cast<u32x4*>(red);
  const float rs_ = 64.f / (float)(rd[0] + rd[1] + rd[2] + rd[3]);
  inc += (wid > 0 ? rd[0] : 0) + (wid > 1 ? rd[1] : 0) + (wid > 2 ? rd[2] : 0) - own;
  int wv_[10];
  {
    int o = inc, wp = tid ? min(127, (int)((float)(2 * o - nup) * rs_)) : 128;
#pragma unroll
    for (int i = 0; i < 10; ++i) {
      wv_[i] = min(127, (int)((float)(2 * o + nu[i]) * rs_));
      ub[act && wv_[i] != wp ? wv_[i] : 128 + lane] = (unsigned)o << 12 | (unsigned)(10 * tid + i);
      wp = wv_[i]; o += nu[i];
    }
  }
  __syncthreads();
  {
    int u0[10], top = 0;
#pragma unroll
    for (int i = 0; i < 10; ++i) u0[i] = ub[wv_[i]] >> 12;
#pragma unroll
    for (int i = 0; i < 10; ++i) {
      const int lu = inc - u0[i], e = lu + nu[i] - 1 + GU, q = min(e / (4 * GU), GNB);
      u0[i] = wv_[i] * 1024 + lu * 8; top = max(top, q);
      atomicOr(img + wv_[i] * 256 + 244 + q, act ? 1u << (e - q * 4 * GU) : 0u);
      inc += nu[i];
    }
#pragma unroll
    for (int i = 0; i < 10; i += 2) *reinterpret_cast<u32x2*>(cnt + 10 * tid + i) = (u32x2){(unsigned)u0[i], (unsigned)u0[i + 1]};
    if (tid < 64) cnt[SPN + tid] = 0x30000;
    if (act && top == GNB) red[7] = 1;
  }
  __syncthreads();
  const bool big_ = red[7];
  if (tid < 128) {
    unsigned nb = 0;
#pragma unroll
    for (int q = 0; q < GNB; ++q) nb = img[tid * 256 + 244 + q] ? q + 1 : nb;
    *reinterpret_cast<u32x4*>(img + tid * 256 + 240) = (u32x4){big_ ? 0u : nb, WS_G + (lo + (ub[tid] & 4095u)) * 512, big_ || (tid & 3) ? 0x40000000u : WS_GF + j * 512 + tid * 4, 0u};
  }
  if (!big_) {
    const unsigned dmp = 0x27000 + lane * 4;
    typedef __attribute__((address_space(3))) u16* lds_u16_p;
    unsigned p[2][8];
#pragma unroll
    for (int k = 0; k < NL + 2; k += 2) {
      if (k < NL) {
        int* q[8];
#pragma unroll
        for (int r = 0; r < 8; ++r) q[r] = slot(ent[k + (r >> 2)][r & 3]);
        SB();
#pragma unroll
        for (int r = 0; r < 8; ++r) p[k >> 1 & 1][r] = atomicAdd(q[r], 2);
        SB();
      }
      if (k) {
#pragma unroll
        for (int r = 0; r < 8; ++r) p[~k >> 1 & 1][r] = min(p[~k >> 1 & 1][r], dmp);
        SB();
#pragma unroll
        for (int r = 0; r < 8; ++r) *(lds_u16_p)p[~k >> 1 & 1][r] = ent[k - 2 + (r >> 2)][r & 3];
        SB();
      }
    }
    if (use)
#pragma unroll 1
      for (int k = NL; k < 64; ++k)
        if (use >> (k - NL) & 1) {
          u32x4 e = fetch(k); clean(e, k);
#pragma unroll
          for (int r = 0; r < 4; ++r) *(lds_u16_p)min((unsigned)atomicAdd(slot(e[r]), 2), dmp) = e[r];
        }
    __syncthreads();
    if (srt) {
#pragma unroll
      for (int i = 0; i < 10; ++i) dg[10 * tid + i] = cn[i] >> 1;
      for (unsigned long long big = __ballot(cm > 64); big; big &= big - 1)
      for (int n = 10 * (wid * 64 + __builtin_ctzll(big)), n1 = n + 10; n < n1; ++n) {
        const int d = __builtin_amdgcn_readfirstlane(dg[n]);
        if (d <= 32) continue;
        u16* f = reinterpret_cast<u16*>(lds + cnt[n]) - d;
        for (int i = lane; i < d + 8; i += 64) tmp[i] = i < d ? f[i] : 0xffff;
        __builtin_amdgcn_wave_barrier();
        for (int i = lane; i < d; i += 64) {
          const unsigned key = tmp[i] << 9 | i; int p_ = 0;
          for (int q = 0; q < d; q += 8) {
            const u32x4 v = *reinterpret_cast<u32x4*>(tmp + q);
#pragma unroll
            for (int e = 0; e < 4; ++e) p_ += (((v[e] & 0xffffu) << 9 | (q + 2 * e)) < key) + (((v[e] >> 16) << 9 | (q + 2 * e + 1)) < key);
          }
          f[p_] = key >> 9;
        }
        __builtin_amdgcn_wave_barrier();
      }
    }
  }
  __syncthreads();
#pragma unroll 8
  for (int k = 0; k < 32; ++k)
    __builtin_amdgcn_raw_buffer_store_b128(reinterpret_cast<u32x4*>(img)[k * 256 + tid], wsrd, WS_LS + j * 131072 + (k * 256 + tid) * 16, 0, 16);
  unsigned lr[16];
#pragma unroll
  for (int r = 0; r < 16; ++r) lr[r] = reinterpret_cast<const unsigned*>((wid >= 2 ? wm : wv) + (240 + r) * DD)[tid & 127];
  wait_flag(wsrd, WS_HF + min(tid, NATT - 1) * 16, tag);
  drain();
  __syncthreads();
  if (tid == 0) st_flag(wsrd, WS_LF + j * 128, tag);
  asm volatile("s_mov_b32 m0, 0x27c00\n s_nop 0\n global_load_lds_dword %0, %1" :: "v"(min(tid * 128, NN * 2 - 4)), "s"(dinv) : "m0", "memory");
  touch(WS_XF + (j * 256 + tid) * 256);
  touch(wid ? 0x40000000u : WS_GF - 32768 + (j * 64 + lane) * 128);
  corr(reinterpret_cast<unsigned*>(lds + 0x20000) + 512 + (wid >> 1) * 256 + (tid & 127), lr);
#pragma unroll 1
  for (int job = 4 * j; job < 4 * j + 4; ++job) {
    product_load(lds, wid, lane, job, wv, wm, wo);
    drain();
    __syncthreads();
    product_w(lds, wid, lane, job, wsrd);
    __syncthreads();
  }
  drain();
  __syncthreads();
  if (tid == 0) st_flag(wsrd, WS_WF + j * 4, tag);
  if (big_) {
    const srd_t msrd = __builtin_amdgcn_make_buffer_rsrc(ws + WS_H, 0, 10240 * 512, 0x00020000);
    bf16x8 b1;
#pragma unroll
    for (int e = 0; e < 8; ++e) b1[e] = (lane & 15) == e + ((lane >> 1) & 8) ? 0x3f80 : 0;
    for (int n = wid; n < nn_; n += 4) {
      f32x4 acc = {};
      for (int e0 = 0; e0 < EE; e0 += 64) {
        unsigned long long m = __ballot(dst[e0 + lane] == lo + n);
        while (m) {
          const int e = e0 + __builtin_ctzll(m); m &= m - 1;
          const unsigned vo = lane < 32 ? (unsigned)src[e] * 512 + lane * 16 : 0x7fffff00u;
          acc = MFMA16(b1, as_bf(__builtin_amdgcn_raw_buffer_load_b128(msrd, vo, 0, 16)), acc);
        }
      }
      __builtin_amdgcn_raw_buffer_store_b64((u32x2){pk2(acc[0], acc[1]), pk2(acc[2], acc[3])}, wsrd,
                                            WS_G + (lo + n) * 512 + (lane >> 5) * 256 + (lane & 15) * 16 + (lane & 16) / 2, 0, 16);
    }
    drain();
    __syncthreads();
    if (tid < 32) st_flag(wsrd, WS_GF + j * 512 + tid * 16, tag);
  }
}

extern "C" __global__ __launch_bounds__(256, 1) void gps_kernel(const u16* wq, const u16* wk, const int* src, char* ws, const int* dst, const u16* x, u16* out,
                                                                const u16* deg_inv, const u16* wv, const u16* wm, const u16* wo) {
  extern __shared__ __attribute__((aligned(16))) char lds[];
  static_assert(WS_END == 69410816 && WS_EP == 6101760, "workspace layout of the generated code");
  typedef void* const __attribute__((address_space(4)))* arg_p;
  arg_p ka; int b, wid;
  asm volatile(
    ".Lat_0:\n"
    "s_mov_b32 s92, s2\n"
    "s_load_dwordx2 s[2:3], s[0:1], 0x0\n"
    "s_load_dwordx4 s[4:7], s[0:1], 0x8\n"
    "s_load_dwordx8 s[8:15], s[0:1], 0x18\n"
    "s_load_dwordx2 s[28:29], s[0:1], 0x38\n"
    "s_getpc_b64 s[30:31]\n"
    "v_readfirstlane_b32 s93, v0\n"
    "s_bfe_u32 s45, s92, 0x40003\n"
    "v_lshlrev_b32_e32 v1, 5, v0\n"
    "s_lshl_b32 s45, s45, 13\n"
    "s_mov_b32 m0, 0x27f00\n"
    "v_add_u32_e32 v1, s45, v1\n"
    "v_min_u32_e32 v1, 76488, v1\n"
    "global_load_lds_dword v1, s[30:31]\n"
    "s_and_b32 s45, s92, 249\n"
    "s_lshr_b32 s93, s93, 6\n"
    "s_cmp_eq_u32 s45, 249\n"
    "s_cbranch_scc1 .Lat_out\n"
    "v_mbcnt_lo_u32_b32 v24, -1, 0\n"
    "v_mbcnt_hi_u32_b32 v24, -1, v24\n"
    "s_and_b32 s50, s93, 1\n"
    "s_lshr_b32 s51, s93, 1\n"
    "v_lshrrev_b32_e32 v25, 3, v24\n"
    "v_and_b32_e32 v26, 6, v25\n"
    "v_and_b32_e32 v27, 7, v24\n"
    "v_xor_b32_e32 v26, v26, v27\n"
    "v_lshlrev_b32_e32 v26, 4, v26\n"
    "v_and_b32_e32 v27, 4, v25\n"
    "v_add_u32_e32 v27, v27, v25\n"
    "v_lshl_add_u32 v251, v27, 9, v26\n"
    "v_add_u32_e32 v252, 0x1c00, v251\n"
    "s_mov_b32 s22, 0x20000\n"
    "s_mov_b32 s23, 0x20000\n"
    "s_mov_b32 s26, 0x20000\n"
    "s_mov_b32 s27, 0x20000\n"
    "s_lshr_b32 s36, s92, 7\n"
    "s_mul_i32 s36, s36, 3\n"
    "s_add_u32 s36, s36, s92\n"
    "s_and_b32 s36, s36, 127\n"
    "s_lshl_b32 s45, s93, 7\n"
    "s_lshl_b32 s46, s93, 12\n"
    "s_and_b32 s47, s36, 7\n"
    "s_lshl_b32 s47, s47, 14\n"
    "s_mov_b32 m0, s46\n"
    "s_add_u32 s47, s47, s45\n"
    "s_and_b32 s34, s92, 7\n"
    "s_lshl_b32 s34, s34, 5\n"
    "s_lshr_b32 s32, s92, 3\n"
    "s_add_u32 s34, s34, s32\n"
    "s_lshl_b32 s32, s34, 6\n"
    "s_or_b32 s32, s32, 63\n"
    "s_and_b32 s32, s32, 255\n"
    "s_cmp_ge_u32 s34, 252\n"
    "s_cselect_b32 s34, s32, s34\n"
    "s_and_b32 s32, s34, 3\n"
    "s_lshr_b32 s33, s34, 2\n"
    "s_or_b32 s38, s33, 16\n"
    "s_cmp_eq_u32 s38, 62\n"
    "s_cselect_b32 s38, 16, 0\n"
    "s_xor_b32 s33, s33, s38\n"
    "s_lshl2_add_u32 s34, s33, s32\n"
    "s_mul_i32 s38, s33, 81920\n"
    "s_lshl_b32 s35, s32, 13\n"
    "s_and_b32 s55, s35, 0x2000\n"
    "s_add_u32 s35, s35, s38\n"
    "s_add_u32 s55, s55, s38\n"
    "s_mov_b32 s39, 6101760\n"
    "s_waitcnt lgkmcnt(0)\n"
    "s_mov_b64 s[20:21], s[2:3]\n"
    "s_mov_b64 s[24:25], s[4:5]\n"
    "buffer_load_dwordx4 v251, s[20:23], s47 offen offset:0 lds\n"
    "s_add_u32 s48, s47, 1024\n"
    "buffer_load_dwordx4 v252, s[20:23], s47 offen offset:3072 lds\n"
    "s_add_u32 s47, s47, 0x1800\n"
    "buffer_load_dwordx4 v251, s[20:23], s48 offen offset:1024 lds\n"
    "buffer_load_dwordx4 v251, s[20:23], s47 offen offset:2048 lds\n"
    "s_lshr_b32 s47, s36, 4\n"
    "s_lshl_b32 s47, s47, 14\n"
    "s_and_b32 s48, s36, 8\n"
    "s_lshl_b32 s48, s48, 8\n"
    "s_add_u32 s47, s47, s48\n"
    "s_add_u32 m0, s46, 0x4000\n"
    "s_add_u32 s47, s47, s45\n"
    "buffer_load_dwordx4 v251, s[24:27], s47 offen offset:0 lds\n"
    "buffer_load_dwordx4 v252, s[24:27], s47 offen offset:1024 lds\n"
    "s_add_u32 s47, s45, 120832\n"
    "buffer_load_dwordx4 v251, s[24:27], s47 offen offset:2048 lds\n"
    "s_add_u32 s47, s47, 1024\n"
    "buffer_load_dwordx4 v251, s[24:27], s47 offen offset:3072 lds\n"
    "s_mov_b64 s[16:17], s[14:15]\n"
    "s_mov_b64 s[86:87], s[10:11]\n"
    "s_mov_b32 s14, 5120000\n"
    "s_mov_b32 s15, 0x20000\n"
    "v_mov_b32_e32 v102, s39\n"
    "global_load_dword v102, v102, s[8:9]\n"
    "s_lshl_b32 s45, s93, 2\n"
    "s_add_u32 s54, s35, 0x8000\n"
    "s_add_u32 s55, s55, 0x10000\n"
    "v_lshlrev_b32_e32 v250, 4, v24\n"
    "v_lshrrev_b32_e32 v28, 4, v24\n"
    "v_add_u32_e32 v28, s45, v28\n"
    "v_and_b32_e32 v104, 0xf0, v250\n"
    "v_lshl_add_u32 v104, v28, 9, v104\n"
    "buffer_load_dwordx4 v[0:3], v104, s[12:15], s35 offen offset:0\n"
    "buffer_load_dwordx4 v[4:7], v104, s[12:15], s35 offen offset:256\n"
    "s_cmp_lt_u32 s32, 2\n"
    "s_cselect_b32 s60, s55, 2.0\n"
    "buffer_load_dwordx4 v[8:11], v104, s[12:15], s54 offen offset:0\n"
    "buffer_load_dwordx4 v[12:15], v104, s[12:15], s54 offen offset:256\n"
    "buffer_load_dwordx4 v[16:19], v104, s[12:15], s55 offen offset:0\n"
    "buffer_load_dwordx4 v[20:23], v104, s[12:15], s55 offen offset:256\n"
    "s_min_u32 s46, s34, 249\n"
    "s_mul_i32 s46, s46, 2560\n"
    "s_lshl_b32 s47, s50, 10\n"
    "s_add_u32 s47, s47, s46\n"
    "v_add_u32_e32 v158, s47, v250\n"
    "s_lshl_b32 s47, s50, 8\n"
    "s_add_u32 s47, s47, s46\n"
    "s_add_u32 s47, s47, 2048\n"
    "v_lshl_add_u32 v159, v24, 2, s47\n"
    "s_cmp_lg_u32 s51, 0\n"
    "s_cselect_b64 exec, -1, 1\n"
    "global_load_dwordx4 v[144:147], v158, s[86:87]\n"
    "global_load_dword v148, v159, s[86:87]\n"
    "global_load_dwordx4 v[152:155], v158, s[6:7]\n"
    "global_load_dword v156, v159, s[6:7]\n"
    "s_mov_b64 exec, -1\n"
    "s_mov_b32 s10, 69410816\n"
    "s_mov_b32 s11, 0x20000\n"
    "s_mov_b32 s49, 0\n"
    "v_and_b32_e32 v25, 7, v24\n"
    "v_lshlrev_b32_e32 v27, 7, v25\n"
    "v_bfe_u32 v26, v24, 3, 1\n"
    "v_lshl_add_u32 v27, v26, 10, v27\n"
    "v_and_b32_e32 v25, 6, v24\n"
    "v_lshrrev_b32_e32 v26, 4, v24\n"
    "v_xor_b32_e32 v25, v25, v26\n"
    "v_lshl_add_u32 v240, v25, 4, v27\n"
    "s_lshl_b32 s48, s50, 11\n"
    "v_xor_b32_e32 v241, 64, v240\n"
    "v_add_u32_e32 v72, s48, v240\n"
    "v_add_u32_e32 v73, s48, v241\n"
    "s_lshl_b32 s47, s93, 10\n"
    "v_add_u32_e32 v108, s47, v250\n"
    "v_lshrrev_b32_e32 v109, 7, v108\n"
    "v_mul_u32_u24_e32 v109, 0x8200, v109\n"
    "v_and_b32_e32 v32, 0x70, v250\n"
    "v_or_b32_e32 v109, v109, v32\n"
    "v_add_u32_e32 v108, 81920, v108\n"
    "v_mov_b64_e32 v[32:33], -1\n"
    "v_mov_b64_e32 v[34:35], -1\n"
    "ds_write_b128 v108, v[32:35] offset:0\n"
    "ds_write_b128 v108, v[32:35] offset:16\n"
    "v_and_b32_e32 v29, 6, v28\n"
    "v_xor_b32_e32 v29, v29, v24\n"
    "v_and_b32_e32 v29, 7, v29\n"
    "v_lshlrev_b32_e32 v105, 7, v28\n"
    "v_lshl_add_u32 v105, v29, 4, v105\n"
    "v_bfe_u32 v29, v24, 3, 1\n"
    "v_subrev_u32_e32 v29, s32, v29\n"
    "v_and_b32_e32 v29, 3, v29\n"
    "v_lshl_add_u32 v105, v29, 11, v105\n"
    "v_add_u32_e32 v105, 32768, v105\n"
    "v_xor_b32_e32 v106, 0x1000, v105\n"
    "v_and_b32_e32 v29, 15, v24\n"
    "v_cmp_eq_u32_e32 vcc, 15, v29\n"
    "v_mov_b32_e32 v107, 0xffff\n"
    "s_mov_b32 s58, 0x3f803f80\n"
    "s_mov_b32 s57, 0x43c00000\n"
    "v_cndmask_b32_e32 v107, -1, v107, vcc\n"
    "s_mov_b32 s56, 0xc3c00000\n"
    "s_mov_b32 s62, 0xc000c000\n"
    "s_mov_b32 s63, 0xc000c000\n"
    "s_lshl_b32 s47, s93, 10\n"
    "s_add_u32 s47, s47, 6080512\n"
    "v_add_u32_e32 v100, s47, v250\n"
    "v_and_b32_e32 v29, 15, v24\n"
    "s_cmp_ge_u32 s36, 112\n"
    "s_cselect_b32 s47, 8, 16\n"
    "v_cmp_le_u32_e32 vcc, s47, v29\n"
    "v_mov_b32_e32 v64, 0xbd80bd80\n"
    "v_mov_b32_e32 v65, 0xbdabbdab\n"
    "v_mov_b32_e32 v66, 0x392b392b\n"
    "v_cndmask_b32_e32 v64, v64, v65, vcc\n"
    "v_cndmask_b32_e32 v66, 0, v66, vcc\n"
    "s_cselect_b32 s47, 56, 64\n"
    "v_cmp_le_u32_e32 vcc, s47, v24\n"
    "v_mov_b32_e32 v65, 0x3e80bdab\n"
    "v_mov_b32_e32 v67, 0x392b\n"
    "v_cndmask_b32_e32 v65, v64, v65, vcc\n"
    "v_cndmask_b32_e32 v67, v66, v67, vcc\n"
    "s_cmp_ge_u32 s36, 120\n"
    "s_cselect_b32 s47, 15, 16\n"
    "v_cmp_ne_u32_e32 vcc, s47, v29\n"
    "v_mov_b32_e32 v68, 0x3db8aa3b\n"
    "s_lshr_b32 s47, s36, 5\n"
    "v_cndmask_b32_e32 v68, 0, v68, vcc\n"
    "s_waitcnt vmcnt(11)\n"
    "s_waitcnt lgkmcnt(0)\n"
    "s_barrier\n"
    "s_cmp_lg_u32 s51, 0\n"
    "s_cbranch_scc1 .Lat_noj2\n"
    "ds_read_b128 v[112:115], v72 offset:0\n"
    "ds_read_b128 v[208:211], v240 offset:18432\n"
    "ds_read_b128 v[176:179], v240 offset:16384\n"
    "ds_read_b128 v[116:119], v73 offset:0\n"
    "ds_read_b128 v[212:215], v241 offset:18432\n"
    "ds_read_b128 v[180:183], v241 offset:16384\n"
    "ds_read_b128 v[120:123], v72 offset:4096\n"
    "ds_read_b128 v[216:219], v240 offset:22528\n"
    "ds_read_b128 v[184:187], v240 offset:20480\n"
    "ds_read_b128 v[124:127], v73 offset:4096\n"
    "ds_read_b128 v[220:223], v241 offset:22528\n"
    "ds_read_b128 v[188:191], v241 offset:20480\n"
    "ds_read_b128 v[128:131], v72 offset:8192\n"
    "ds_read_b128 v[224:227], v240 offset:26624\n"
    "ds_read_b128 v[192:195], v240 offset:24576\n"
    "s_waitcnt lgkmcnt(12)\n"
    "v_mfma_f32_16x16x32_bf16 v[56:59], v[208:211], v[112:115], 0\n"
    "ds_read_b128 v[132:135], v73 offset:8192\n"
    "ds_read_b128 v[228:231], v241 offset:26624\n"
    "v_mfma_f32_16x16x32_bf16 v[48:51], v[112:115], v[176:179], 0\n"
    "ds_read_b128 v[196:199], v241 offset:24576\n"
    "s_waitcnt lgkmcnt(12)\n"
    "v_mfma_f32_16x16x32_bf16 v[56:59], v[212:215], v[116:119], v[56:59]\n"
    "ds_read_b128 v[136:139], v72 offset:12288\n"
    "ds_read_b128 v[232:235], v240 offset:30720\n"
    "v_mfma_f32_16x16x32_bf16 v[48:51], v[116:119], v[180:183], v[48:51]\n"
    "ds_read_b128 v[200:203], v240 offset:28672\n"
    "s_waitcnt lgkmcnt(12)\n"
    "v_mfma_f32_16x16x32_bf16 v[56:59], v[216:219], v[120:123], v[56:59]\n"
    "ds_read_b128 v[140:143], v73 offset:12288\n"
    "ds_read_b128 v[236:239], v241 offset:30720\n"
    "v_mfma_f32_16x16x32_bf16 v[48:51], v[120:123], v[184:187], v[48:51]\n"
    "ds_read_b128 v[204:207], v241 offset:28672\n"
    "s_waitcnt lgkmcnt(12)\n"
    "v_mfma_f32_16x16x32_bf16 v[56:59], v[220:223], v[124:127], v[56:59]\n"
    "s_mul_i32 s47, s47, 33792\n"
    "s_and_b32 s48, s36, 7\n"
    "s_lshl_b32 s48, s48, 11\n"
    "v_mfma_f32_16x16x32_bf16 v[48:51], v[124:127], v[188:191], v[48:51]\n"
    "s_add_u32 s47, s47, s48\n"
    "s_and_b32 s48, s36, 8\n"
    "s_lshl_b32 s48, s48, 7\n"
    "s_waitcnt lgkmcnt(9)\n"
    "v_mfma_f32_16x16x32_bf16 v[56:59], v[224:227], v[128:131], v[56:59]\n"
    "s_add_u32 s47, s47, s48\n"
    "s_and_b32 s48, s36, 16\n"
    "s_lshl_b32 s48, s48, 10\n"
    "v_mfma_f32_16x16x32_bf16 v[48:51], v[128:131], v[192:195], v[48:51]\n"
    "s_add_u32 s47, s47, s48\n"
    "s_add_u32 s52, s47, 5242880\n"
    "s_lshl_b32 s48, s50, 9\n"
    "s_waitcnt lgkmcnt(6)\n"
    "v_mfma_f32_16x16x32_bf16 v[56:59], v[228:231], v[132:135], v[56:59]\n"
    "s_add_u32 s52, s52, s48\n"
    "v_and_b32_e32 v74, 0x1f0, v250\n"
    "v_lshrrev_b32_e32 v29, 5, v24\n"
    "v_mfma_f32_16x16x32_bf16 v[48:51], v[132:135], v[196:199], v[48:51]\n"
    "v_lshl_add_u32 v74, v29, 3, v74\n"
    "s_lshl1_add_u32 s53, s36, s50\n"
    "s_lshl4_add_u32 s53, s53, 6080512\n"
    "s_waitcnt lgkmcnt(3)\n"
    "v_mfma_f32_16x16x32_bf16 v[56:59], v[232:235], v[136:139], v[56:59]\n"
    "s_cmp_lg_u32 s51, 0\n"
    "v_mfma_f32_16x16x32_bf16 v[48:51], v[136:139], v[200:203], v[48:51]\n"
    "s_waitcnt lgkmcnt(0)\n"
    "v_mfma_f32_16x16x32_bf16 v[56:59], v[236:239], v[140:143], v[56:59]\n"
    "v_mfma_f32_16x16x32_bf16 v[48:51], v[140:143], v[204:207], v[48:51]\n"
    "s_nop 3\n"
    "v_cvt_pk_bf16_f32 v56, v56, v57\n"
    "v_cvt_pk_bf16_f32 v57, v58, v59\n"
    "s_nop 1\n"
    "v_mfma_f32_16x16x16_bf16 v[48:51], v[56:57], v[64:65], v[48:51]\n"
    "v_mfma_f32_16x16x16_bf16 v[48:51], v[56:57], v[66:67], v[48:51]\n"
    "s_nop 7\n"
    "v_mul_f32_e32 v48, v48, v68\n"
    "v_mul_f32_e32 v49, v49, v68\n"
    "v_mul_f32_e32 v50, v50, v68\n"
    "v_mul_f32_e32 v51, v51, v68\n"
    "v_cvt_pk_bf16_f32 v48, v48, v49\n"
    "v_cvt_pk_bf16_f32 v49, v50, v51\n"
    "buffer_store_dwordx2 v[48:49], v74, s[8:11], s52 offen offset:0 sc1\n"
    "s_waitcnt vmcnt(5)\n"
    "s_branch .Lat_ln\n"
    ".Lat_noj2:\n"
    "s_cmp_lg_u32 s93, 3\n"
    "s_cbranch_scc1 .Lat_wn10\n"
    "s_call_b64 s[2:3], .Lat_w0\n"
    "s_call_b64 s[2:3], .Lat_w2\n"
    "s_call_b64 s[2:3], .Lat_w4\n"
    ".Lat_wn10:\n"
    "s_cmp_lg_u32 s93, 2\n"
    "s_cbranch_scc1 .Lat_wn11\n"
    "s_call_b64 s[2:3], .Lat_w1\n"
    "s_call_b64 s[2:3], .Lat_w3\n"
    "s_call_b64 s[2:3], .Lat_w5\n"
    ".Lat_wn11:\n"
    "s_cmp_lg_u32 s51, 0\n"
    "s_waitcnt vmcnt(4)\n"
    ".Lat_ln:\n"
    "v_add_u32_e32 v102, 4, v102\n"
    "s_nop 1\n"
    "v_readfirstlane_b32 s39, v102\n"
    "v_dot2_f32_bf16 v24, v0, s58, 0\n"
    "v_dot2_f32_bf16 v32, v8, s58, 0\n"
    "v_dot2_f32_bf16 v40, v16, s58, 0\n"
    "v_dot2_f32_bf16 v25, v4, s58, 0\n"
    "v_dot2_f32_bf16 v33, v12, s58, 0\n"
    "v_dot2_f32_bf16 v41, v20, s58, 0\n"
    "v_dot2_f32_bf16 v26, v0, v0, 0\n"
    "v_dot2_f32_bf16 v34, v8, v8, 0\n"
    "v_dot2_f32_bf16 v42, v16, v16, 0\n"
    "v_dot2_f32_bf16 v27, v4, v4, 0\n"
    "v_dot2_f32_bf16 v35, v12, v12, 0\n"
    "v_dot2_f32_bf16 v43, v20, v20, 0\n"
    "v_dot2_f32_bf16 v24, v1, s58, v24\n"
    "v_dot2_f32_bf16 v32, v9, s58, v32\n"
    "v_dot2_f32_bf16 v40, v17, s58, v40\n"
    "v_dot2_f32_bf16 v25, v5, s58, v25\n"
    "v_dot2_f32_bf16 v33, v13, s58, v33\n"
    "v_dot2_f32_bf16 v41, v21, s58, v41\n"
    "v_dot2_f32_bf16 v26, v1, v1, v26\n"
    "v_dot2_f32_bf16 v34, v9, v9, v34\n"
    "v_dot2_f32_bf16 v42, v17, v17, v42\n"
    "v_dot2_f32_bf16 v27, v5, v5, v27\n"
    "v_dot2_f32_bf16 v35, v13, v13, v35\n"
    "v_dot2_f32_bf16 v43, v21, v21, v43\n"
    "v_dot2_f32_bf16 v24, v2, s58, v24\n"
    "v_dot2_f32_bf16 v32, v10, s58, v32\n"
    "v_dot2_f32_bf16 v40, v18, s58, v40\n"
    "v_dot2_f32_bf16 v25, v6, s58, v25\n"
    "v_dot2_f32_bf16 v33, v14, s58, v33\n"
    "v_dot2_f32_bf16 v41, v22, s58, v41\n"
    "v_dot2_f32_bf16 v26, v2, v2, v26\n"
    "v_dot2_f32_bf16 v34, v10, v10, v34\n"
    "v_dot2_f32_bf16 v42, v18, v18, v42\n"
    "v_dot2_f32_bf16 v27, v6, v6, v27\n"
    "v_dot2_f32_bf16 v35, v14, v14, v35\n"
    "v_dot2_f32_bf16 v43, v22, v22, v43\n"
    "v_dot2_f32_bf16 v24, v3, s58, v24\n"
    "v_dot2_f32_bf16 v32, v11, s58, v32\n"
    "v_dot2_f32_bf16 v40, v19, s58, v40\n"
    "v_dot2_f32_bf16 v25, v7, s58, v25\n"
    "v_dot2_f32_bf16 v33, v15, s58, v33\n"
    "v_dot2_f32_bf16 v41, v23, s58, v41\n"
    "v_dot2_f32_bf16 v26, v3, v3, v26\n"
    "v_dot2_f32_bf16 v34, v11, v11, v34\n"
    "v_dot2_f32_bf16 v42, v19, v19, v42\n"
    "v_dot2_f32_bf16 v27, v7, v7, v27\n"
    "v_dot2_f32_bf16 v35, v15, v15, v35\n"
    "v_dot2_f32_bf16 v43, v23, v23, v43\n"
    "v_and_b32_e32 v30, 0xffff0000, v7\n"
    "v_and_b32_e32 v38, 0xffff0000, v15\n"
    "v_and_b32_e32 v46, 0xffff0000, v23\n"
    "v_add_f32_e32 v24, v24, v25\n"
    "v_add_f32_e32 v32, v32, v33\n"
    "v_add_f32_e32 v40, v40, v41\n"
    "v_add_f32_e32 v26, v26, v27\n"
    "v_add_f32_e32 v34, v34, v35\n"
    "v_add_f32_e32 v42, v42, v43\n"
    "s_cbranch_scc1 .Lat_nojf\n"
    "s_waitcnt vmcnt(0)\n"
    "buffer_store_dword v102, off, s[8:11], s53 offset:0 sc1\n"
    ".Lat_nojf:\n"
    "v_add_f32_dpp v25, v25, v25 quad_perm:[1,0,3,2] row_mask:0xf bank_mask:0xf bound_ctrl:1\n"
    "v_lshlrev_b32_e32 v48, 16, v0\n"
    "v_add_f32_dpp v33, v33, v33 quad_perm:[1,0,3,2] row_mask:0xf bank_mask:0xf bound_ctrl:1\n"
    "v_and_b32_e32 v49, 0xffff0000, v0\n"
    "v_add_f32_dpp v41, v41, v41 quad_perm:[1,0,3,2] row_mask:0xf bank_mask:0xf bound_ctrl:1\n"
    "v_lshlrev_b32_e32 v50, 16, v1\n"
    "v_mov_b32_dpp v30, v30 quad_perm:[3,3,3,3] row_mask:0xf bank_mask:0xf bound_ctrl:1\n"
    "v_and_b32_e32 v51, 0xffff0000, v1\n"
    "v_mov_b32_dpp v38, v38 quad_perm:[3,3,3,3] row_mask:0xf bank_mask:0xf bound_ctrl:1\n"
    "v_lshlrev_b32_e32 v52, 16, v2\n"
    "v_mov_b32_dpp v46, v46 quad_perm:[3,3,3,3] row_mask:0xf bank_mask:0xf bound_ctrl:1\n"
    "v_and_b32_e32 v53, 0xffff0000, v2\n"
    "v_add_f32_dpp v24, v24, v24 quad_perm:[1,0,3,2] row_mask:0xf bank_mask:0xf bound_ctrl:1\n"
    "v_lshlrev_b32_e32 v54, 16, v3\n"
    "v_add_f32_dpp v32, v32, v32 quad_perm:[1,0,3,2] row_mask:0xf bank_mask:0xf bound_ctrl:1\n"
    "v_and_b32_e32 v55, 0xffff0000, v3\n"
    "v_add_f32_dpp v40, v40, v40 quad_perm:[1,0,3,2] row_mask:0xf bank_mask:0xf bound_ctrl:1\n"
    "v_lshlrev_b32_e32 v56, 16, v4\n"
    "v_add_f32_dpp v26, v26, v26 quad_perm:[1,0,3,2] row_mask:0xf bank_mask:0xf bound_ctrl:1\n"
    "v_and_b32_e32 v57, 0xffff0000, v4\n"
    "v_add_f32_dpp v34, v34, v34 quad_perm:[1,0,3,2] row_mask:0xf bank_mask:0xf bound_ctrl:1\n"
    "v_lshlrev_b32_e32 v58, 16, v5\n"
    "v_add_f32_dpp v42, v42, v42 quad_perm:[1,0,3,2] row_mask:0xf bank_mask:0xf bound_ctrl:1\n"
    "v_and_b32_e32 v59, 0xffff0000, v5\n"
    "v_add_f32_dpp v24, v24, v24 quad_perm:[2,3,0,1] row_mask:0xf bank_mask:0xf bound_ctrl:1\n"
    "v_lshlrev_b32_e32 v60, 16, v6\n"
    "v_add_f32_dpp v32, v32, v32 quad_perm:[2,3,0,1] row_mask:0xf bank_mask:0xf bound_ctrl:1\n"
    "v_and_b32_e32 v61, 0xffff0000, v6\n"
    "v_add_f32_dpp v40, v40, v40 quad_perm:[2,3,0,1] row_mask:0xf bank_mask:0xf bound_ctrl:1\n"
    "v_lshlrev_b32_e32 v62, 16, v7\n"
    "v_add_f32_dpp v26, v26, v26 quad_perm:[2,3,0,1] row_mask:0xf bank_mask:0xf bound_ctrl:1\n"
    "v_and_b32_e32 v63, 0xffff0000, v7\n"
    "v_add_f32_dpp v34, v34, v34 quad_perm:[2,3,0,1] row_mask:0xf bank_mask:0xf bound_ctrl:1\n"
    "v_lshlrev_b32_e32 v64, 16, v8\n"
    "v_add_f32_dpp v42, v42, v42 quad_perm:[2,3,0,1] row_mask:0xf bank_mask:0xf bound_ctrl:1\n"
    "v_and_b32_e32 v65, 0xffff0000, v8\n"
    "v_add_f32_dpp v24, v24, v24 row_half_mirror row_mask:0xf bank_mask:0xf bound_ctrl:1\n"
    "v_lshlrev_b32_e32 v66, 16, v9\n"
    "v_add_f32_dpp v32, v32, v32 row_half_mirror row_mask:0xf bank_mask:0xf bound_ctrl:1\n"
    "v_and_b32_e32 v67, 0xffff0000, v9\n"
    "v_add_f32_dpp v40, v40, v40 row_half_mirror row_mask:0xf bank_mask:0xf bound_ctrl:1\n"
    "v_lshlrev_b32_e32 v68, 16, v10\n"
    "v_add_f32_dpp v26, v26, v26 row_half_mirror row_mask:0xf bank_mask:0xf bound_ctrl:1\n"
    "v_and_b32_e32 v69, 0xffff0000, v10\n"
    "v_add_f32_dpp v34, v34, v34 row_half_mirror row_mask:0xf bank_mask:0xf bound_ctrl:1\n"
    "v_lshlrev_b32_e32 v70, 16, v11\n"
    "v_add_f32_dpp v42, v42, v42 row_half_mirror row_mask:0xf bank_mask:0xf bound_ctrl:1\n"
    "v_and_b32_e32 v71, 0xffff0000, v11\n"
    "v_add_f32_dpp v24, v24, v24 row_mirror row_mask:0xf bank_mask:0xf bound_ctrl:1\n"
    "v_lshlrev_b32_e32 v72, 16, v12\n"
    "v_add_f32_dpp v32, v32, v32 row_mirror row_mask:0xf bank_mask:0xf bound_ctrl:1\n"
    "v_and_b32_e32 v73, 0xffff0000, v12\n"
    "v_add_f32_dpp v40, v40, v40 row_mirror row_mask:0xf bank_mask:0xf bound_ctrl:1\n"
    "v_lshlrev_b32_e32 v74, 16, v13\n"
    "v_add_f32_dpp v26, v26, v26 row_mirror row_mask:0xf bank_mask:0xf bound_ctrl:1\n"
    "v_and_b32_e32 v75, 0xffff0000, v13\n"
    "v_add_f32_dpp v34, v34, v34 row_mirror row_mask:0xf bank_mask:0xf bound_ctrl:1\n"
    "v_lshlrev_b32_e32 v76, 16, v14\n"
    "v_add_f32_dpp v42, v42, v42 row_mirror row_mask:0xf bank_mask:0xf bound_ctrl:1\n"
    "v_and_b32_e32 v77, 0xffff0000, v14\n"
    "v_mul_f32_e32 v24, 0x3b800000, v24\n"
    "v_lshlrev_b32_e32 v78, 16, v15\n"
    "v_mul_f32_e32 v32, 0x3b800000, v32\n"
    "v_and_b32_e32 v79, 0xffff0000, v15\n"
    "v_mul_f32_e32 v40, 0x3b800000, v40\n"
    "v_lshlrev_b32_e32 v80, 16, v16\n"
    "v_mul_f32_e32 v25, 0.25, v25\n"
    "v_and_b32_e32 v81, 0xffff0000, v16\n"
    "v_mul_f32_e32 v33, 0.25, v33\n"
    "v_lshlrev_b32_e32 v82, 16, v17\n"
    "v_mul_f32_e32 v41, 0.25, v41\n"
    "v_and_b32_e32 v83, 0xffff0000, v17\n"
    "v_mul_f32_e64 v27, v24, -v24\n"
    "v_lshlrev_b32_e32 v84, 16, v18\n"
    "v_mul_f32_e64 v35, v32, -v32\n"
    "v_and_b32_e32 v85, 0xffff0000, v18\n"
    "v_mul_f32_e64 v43, v40, -v40\n"
    "v_lshlrev_b32_e32 v86, 16, v19\n"
    "v_sub_f32_e32 v25, v25, v30\n"
    "v_and_b32_e32 v87, 0xffff0000, v19\n"
    "v_sub_f32_e32 v33, v33, v38\n"
    "v_lshlrev_b32_e32 v88, 16, v20\n"
    "v_sub_f32_e32 v41, v41, v46\n"
    "v_and_b32_e32 v89, 0xffff0000, v20\n"
    "v_fmac_f32_e32 v27, 0x3b800000, v26\n"
    "v_lshlrev_b32_e32 v90, 16, v21\n"
    "v_fmac_f32_e32 v35, 0x3b800000, v34\n"
    "v_and_b32_e32 v91, 0xffff0000, v21\n"
    "v_fmac_f32_e32 v43, 0x3b800000, v42\n"
    "v_lshlrev_b32_e32 v92, 16, v22\n"
    "v_mul_f32_e32 v25, 0x3eaaaaab, v25\n"
    "v_and_b32_e32 v93, 0xffff0000, v22\n"
    "v_mul_f32_e32 v33, 0x3eaaaaab, v33\n"
    "v_lshlrev_b32_e32 v94, 16, v23\n"
    "v_mul_f32_e32 v41, 0x3eaaaaab, v41\n"
    "v_and_b32_e32 v95, 0xffff0000, v23\n"
    "v_max_f32_e32 v27, 0, v27\n"
    "v_max_f32_e32 v35, 0, v35\n"
    "v_max_f32_e32 v43, 0, v43\n"
    "s_cbranch_scc0 .Lat_bin\n"
    "s_movk_i32 s45, -2560\n"
    "s_mov_b32 s48, 81920\n"
    "v_mov_b32_e32 v172, 1\n"
    "s_waitcnt vmcnt(0)\n"
    "v_mul_u32_u24_e32 v160, 0x199a, v144\n"
    "v_mul_u32_u24_e32 v161, 0x199a, v145\n"
    "v_mul_u32_u24_e32 v162, 0x199a, v146\n"
    "v_mul_u32_u24_e32 v163, 0x199a, v147\n"
    "v_mul_u32_u24_e32 v164, 0x199a, v148\n"
    "v_bfe_u32 v160, v160, 24, 2\n"
    "v_bfe_u32 v161, v161, 24, 2\n"
    "v_bfe_u32 v162, v162, 24, 2\n"
    "v_bfe_u32 v163, v163, 24, 2\n"
    "v_bfe_u32 v164, v164, 24, 2\n"
    "v_lshl_add_u32 v166, v160, 2, s48\n"
    "v_lshl_add_u32 v167, v161, 2, s48\n"
    "v_lshl_add_u32 v168, v162, 2, s48\n"
    "v_lshl_add_u32 v169, v163, 2, s48\n"
    "v_lshl_add_u32 v170, v164, 2, s48\n"
    "ds_add_rtn_u32 v166, v166, v172 offset:4096\n"
    "ds_add_rtn_u32 v167, v167, v172 offset:4096\n"
    "ds_add_rtn_u32 v168, v168, v172 offset:4096\n"
    "ds_add_rtn_u32 v169, v169, v172 offset:4096\n"
    "ds_add_rtn_u32 v170, v170, v172 offset:4096\n"
    "v_mad_i32_i24 v144, v160, s45, v144\n"
    "v_mad_i32_i24 v145, v161, s45, v145\n"
    "v_mad_i32_i24 v146, v162, s45, v146\n"
    "v_mad_i32_i24 v147, v163, s45, v147\n"
    "v_mad_i32_i24 v148, v164, s45, v148\n"
    "v_lshl_or_b32 v144, v144, 18, v152\n"
    "v_lshl_or_b32 v145, v145, 18, v153\n"
    "v_lshl_or_b32 v146, v146, 18, v154\n"
    "v_lshl_or_b32 v147, v147, 18, v155\n"
    "v_lshl_or_b32 v148, v148, 18, v156\n"
    "v_lshl_add_u32 v160, v160, 10, s48\n"
    "v_lshl_add_u32 v161, v161, 10, s48\n"
    "v_lshl_add_u32 v162, v162, 10, s48\n"
    "v_lshl_add_u32 v163, v163, 10, s48\n"
    "v_lshl_add_u32 v164, v164, 10, s48\n"
    ".Lat_bin:\n"
    "v_cndmask_b32_e64 v25, v24, v25, s[62:63]\n"
    "v_cndmask_b32_e64 v33, v32, v33, s[62:63]\n"
    "v_cndmask_b32_e64 v41, v40, v41, s[62:63]\n"
    "v_add_f32_e32 v27, 0x3727c5ac, v27\n"
    "v_add_f32_e32 v35, 0x3727c5ac, v35\n"
    "v_add_f32_e32 v43, 0x3727c5ac, v43\n"
    "v_rsq_f32_e32 v28, v27\n"
    "v_rsq_f32_e32 v36, v35\n"
    "v_rsq_f32_e32 v44, v43\n"
    "v_fma_f32 v29, -v24, v28, s57\n"
    "v_fma_f32 v37, -v32, v36, s57\n"
    "v_fma_f32 v45, -v40, v44, s57\n"
    "v_fma_f32 v31, -v25, v28, s57\n"
    "v_fma_f32 v39, -v33, v36, s57\n"
    "v_fma_f32 v47, -v41, v44, s57\n"
    "v_sub_f32_e32 v24, v29, v31\n"
    "v_sub_f32_e32 v32, v37, v39\n"
    "v_sub_f32_e32 v40, v45, v47\n"
    "v_pk_fma_f32 v[48:49], v[48:49], v[28:29], v[28:29] op_sel:[0,0,1] op_sel_hi:[1,0,1]\n"
    "v_pk_fma_f32 v[50:51], v[50:51], v[28:29], v[28:29] op_sel:[0,0,1] op_sel_hi:[1,0,1]\n"
    "v_pk_fma_f32 v[52:53], v[52:53], v[28:29], v[28:29] op_sel:[0,0,1] op_sel_hi:[1,0,1]\n"
    "v_pk_fma_f32 v[54:55], v[54:55], v[28:29], v[28:29] op_sel:[0,0,1] op_sel_hi:[1,0,1]\n"
    "v_pk_fma_f32 v[56:57], v[56:57], v[28:29], v[30:31] op_sel:[0,0,1] op_sel_hi:[1,0,1]\n"
    "v_pk_fma_f32 v[58:59], v[58:59], v[28:29], v[30:31] op_sel:[0,0,1] op_sel_hi:[1,0,1]\n"
    "v_pk_fma_f32 v[60:61], v[60:61], v[28:29], v[30:31] op_sel:[0,0,1] op_sel_hi:[1,0,1]\n"
    "v_pk_fma_f32 v[62:63], v[62:63], v[28:29], v[30:31] op_sel:[0,0,1] op_sel_hi:[1,0,1]\n"
    "v_pk_add_f32 v[48:49], v[48:49], s[56:57] op_sel_hi:[1,0]\n"
    "v_pk_add_f32 v[50:51], v[50:51], s[56:57] op_sel_hi:[1,0]\n"
    "v_pk_add_f32 v[52:53], v[52:53], s[56:57] op_sel_hi:[1,0]\n"
    "v_pk_add_f32 v[54:55], v[54:55], s[56:57] op_sel_hi:[1,0]\n"
    "v_pk_add_f32 v[56:57], v[56:57], s[56:57] op_sel_hi:[1,0]\n"
    "v_pk_add_f32 v[58:59], v[58:59], s[56:57] op_sel_hi:[1,0]\n"
    "v_pk_add_f32 v[60:61], v[60:61], s[56:57] op_sel_hi:[1,0]\n"
    "v_pk_add_f32 v[62:63], v[62:63], s[56:57] op_sel_hi:[1,0]\n"
    "v_cvt_pk_bf16_f32 v0, v48, v49\n"
    "v_cvt_pk_bf16_f32 v1, v50, v51\n"
    "v_cvt_pk_bf16_f32 v2, v52, v53\n"
    "v_cvt_pk_bf16_f32 v3, v54, v55\n"
    "v_cvt_pk_bf16_f32 v4, v56, v57\n"
    "v_cvt_pk_bf16_f32 v5, v58, v59\n"
    "v_cvt_pk_bf16_f32 v6, v60, v61\n"
    "v_cvt_pk_bf16_f32 v7, v62, v63\n"
    "buffer_store_dwordx4 v[0:3], v104, s[8:11], s35 offen offset:0 sc1\n"
    "v_bfi_b32 v7, v107, v7, 1.0\n"
    "ds_write_b128 v105, v[0:3] offset:0\n"
    "buffer_store_dwordx4 v[4:7], v104, s[8:11], s35 offen offset:256 sc1\n"
    "v_pk_fma_f32 v[64:65], v[64:65], v[36:37], v[36:37] op_sel:[0,0,1] op_sel_hi:[1,0,1]\n"
    "v_pk_fma_f32 v[66:67], v[66:67], v[36:37], v[36:37] op_sel:[0,0,1] op_sel_hi:[1,0,1]\n"
    "v_pk_fma_f32 v[68:69], v[68:69], v[36:37], v[36:37] op_sel:[0,0,1] op_sel_hi:[1,0,1]\n"
    "v_pk_fma_f32 v[70:71], v[70:71], v[36:37], v[36:37] op_sel:[0,0,1] op_sel_hi:[1,0,1]\n"
    "v_pk_fma_f32 v[72:73], v[72:73], v[36:37], v[38:39] op_sel:[0,0,1] op_sel_hi:[1,0,1]\n"
    "v_pk_fma_f32 v[74:75], v[74:75], v[36:37], v[38:39] op_sel:[0,0,1] op_sel_hi:[1,0,1]\n"
    "v_pk_fma_f32 v[76:77], v[76:77], v[36:37], v[38:39] op_sel:[0,0,1] op_sel_hi:[1,0,1]\n"
    "v_pk_fma_f32 v[78:79], v[78:79], v[36:37], v[38:39] op_sel:[0,0,1] op_sel_hi:[1,0,1]\n"
    "v_pk_add_f32 v[64:65], v[64:65], s[56:57] op_sel_hi:[1,0]\n"
    "v_pk_add_f32 v[66:67], v[66:67], s[56:57] op_sel_hi:[1,0]\n"
    "v_pk_add_f32 v[68:69], v[68:69], s[56:57] op_sel_hi:[1,0]\n"
    "v_pk_add_f32 v[70:71], v[70:71], s[56:57] op_sel_hi:[1,0]\n"
    "v_pk_add_f32 v[72:73], v[72:73], s[56:57] op_sel_hi:[1,0]\n"
    "v_pk_add_f32 v[74:75], v[74:75], s[56:57] op_sel_hi:[1,0]\n"
    "v_pk_add_f32 v[76:77], v[76:77], s[56:57] op_sel_hi:[1,0]\n"
    "v_pk_add_f32 v[78:79], v[78:79], s[56:57] op_sel_hi:[1,0]\n"
    "v_cvt_pk_bf16_f32 v8, v64, v65\n"
    "v_cvt_pk_bf16_f32 v9, v66, v67\n"
    "v_cvt_pk_bf16_f32 v10, v68, v69\n"
    "v_cvt_pk_bf16_f32 v11, v70, v71\n"
    "v_cvt_pk_bf16_f32 v12, v72, v73\n"
    "v_cvt_pk_bf16_f32 v13, v74, v75\n"
    "v_cvt_pk_bf16_f32 v14, v76, v77\n"
    "v_cvt_pk_bf16_f32 v15, v78, v79\n"
    "buffer_store_dwordx4 v[8:11], v104, s[8:11], s54 offen offset:0 sc1\n"
    "v_bfi_b32 v15, v107, v15, 1.0\n"
    "ds_write_b128 v105, v[8:11] offset:8192\n"
    "buffer_store_dwordx4 v[12:15], v104, s[8:11], s54 offen offset:256 sc1\n"
    "v_pk_fma_f32 v[80:81], v[80:81], v[44:45], v[44:45] op_sel:[0,0,1] op_sel_hi:[1,0,1]\n"
    "v_pk_fma_f32 v[82:83], v[82:83], v[44:45], v[44:45] op_sel:[0,0,1] op_sel_hi:[1,0,1]\n"
    "v_pk_fma_f32 v[84:85], v[84:85], v[44:45], v[44:45] op_sel:[0,0,1] op_sel_hi:[1,0,1]\n"
    "v_pk_fma_f32 v[86:87], v[86:87], v[44:45], v[44:45] op_sel:[0,0,1] op_sel_hi:[1,0,1]\n"
    "v_pk_fma_f32 v[88:89], v[88:89], v[44:45], v[46:47] op_sel:[0,0,1] op_sel_hi:[1,0,1]\n"
    "v_pk_fma_f32 v[90:91], v[90:91], v[44:45], v[46:47] op_sel:[0,0,1] op_sel_hi:[1,0,1]\n"
    "v_pk_fma_f32 v[92:93], v[92:93], v[44:45], v[46:47] op_sel:[0,0,1] op_sel_hi:[1,0,1]\n"
    "v_pk_fma_f32 v[94:95], v[94:95], v[44:45], v[46:47] op_sel:[0,0,1] op_sel_hi:[1,0,1]\n"
    "v_pk_add_f32 v[80:81], v[80:81], s[56:57] op_sel_hi:[1,0]\n"
    "v_pk_add_f32 v[82:83], v[82:83], s[56:57] op_sel_hi:[1,0]\n"
    "v_pk_add_f32 v[84:85], v[84:85], s[56:57] op_sel_hi:[1,0]\n"
    "v_pk_add_f32 v[86:87], v[86:87], s[56:57] op_sel_hi:[1,0]\n"
    "v_pk_add_f32 v[88:89], v[88:89], s[56:57] op_sel_hi:[1,0]\n"
    "v_pk_add_f32 v[90:91], v[90:91], s[56:57] op_sel_hi:[1,0]\n"
    "v_pk_add_f32 v[92:93], v[92:93], s[56:57] op_sel_hi:[1,0]\n"
    "v_pk_add_f32 v[94:95], v[94:95], s[56:57] op_sel_hi:[1,0]\n"
    "v_cvt_pk_bf16_f32 v16, v80, v81\n"
    "v_cvt_pk_bf16_f32 v17, v82, v83\n"
    "v_cvt_pk_bf16_f32 v18, v84, v85\n"
    "v_cvt_pk_bf16_f32 v19, v86, v87\n"
    "v_cvt_pk_bf16_f32 v20, v88, v89\n"
    "v_cvt_pk_bf16_f32 v21, v90, v91\n"
    "v_cvt_pk_bf16_f32 v22, v92, v93\n"
    "v_cvt_pk_bf16_f32 v23, v94, v95\n"
    "buffer_store_dwordx4 v[16:19], v104, s[8:11], s60 offen offset:0 sc1\n"
    "v_bfi_b32 v23, v107, v23, 1.0\n"
    "ds_write_b128 v105, v[16:19] offset:16384\n"
    "buffer_store_dwordx4 v[20:23], v104, s[8:11], s60 offen offset:256 sc1\n"
    "v_pk_add_f32 v[56:57], v[56:57], v[24:25] op_sel_hi:[1,0]\n"
    "v_pk_add_f32 v[58:59], v[58:59], v[24:25] op_sel_hi:[1,0]\n"
    "v_pk_add_f32 v[60:61], v[60:61], v[24:25] op_sel_hi:[1,0]\n"
    "v_pk_add_f32 v[62:63], v[62:63], v[24:25] op_sel_hi:[1,0]\n"
    "v_cvt_pk_bf16_f32 v56, v56, v57\n"
    "v_cvt_pk_bf16_f32 v57, v58, v59\n"
    "v_cvt_pk_bf16_f32 v58, v60, v61\n"
    "v_cvt_pk_bf16_f32 v59, v62, v63\n"
    "ds_write_b128 v106, v[56:59] offset:0\n"
    "v_pk_add_f32 v[72:73], v[72:73], v[32:33] op_sel_hi:[1,0]\n"
    "v_pk_add_f32 v[74:75], v[74:75], v[32:33] op_sel_hi:[1,0]\n"
    "v_pk_add_f32 v[76:77], v[76:77], v[32:33] op_sel_hi:[1,0]\n"
    "v_pk_add_f32 v[78:79], v[78:79], v[32:33] op_sel_hi:[1,0]\n"
    "v_cvt_pk_bf16_f32 v72, v72, v73\n"
    "v_cvt_pk_bf16_f32 v73, v74, v75\n"
    ".long 0xbedf00ff\n"
    ".Lat_w0:\n"
    "s_setpc_b64 s[2:3]\n"
    "v_cvt_pk_bf16_f32 v74, v76, v77\n"
    "v_cvt_pk_bf16_f32 v75, v78, v79\n"
    "ds_write_b128 v106, v[72:75] offset:8192\n"
    "v_pk_add_f32 v[88:89], v[88:89], v[40:41] op_sel_hi:[1,0]\n"
    "v_pk_add_f32 v[90:91], v[90:91], v[40:41] op_sel_hi:[1,0]\n"
    "v_pk_add_f32 v[92:93], v[92:93], v[40:41] op_sel_hi:[1,0]\n"
    "v_pk_add_f32 v[94:95], v[94:95], v[40:41] op_sel_hi:[1,0]\n"
    "v_cvt_pk_bf16_f32 v88, v88, v89\n"
    "v_cvt_pk_bf16_f32 v89, v90, v91\n"
    "v_cvt_pk_bf16_f32 v90, v92, v93\n"
    "v_cvt_pk_bf16_f32 v91, v94, v95\n"
    "ds_write_b128 v106, v[88:91] offset:16384\n"
    "s_cbranch_scc0 .Lat_bin2\n"
    "s_waitcnt lgkmcnt(0)\n"
    "v_min_i32_e32 v166, 254, v166\n"
    "v_min_i32_e32 v167, 254, v167\n"
    "v_min_i32_e32 v168, 254, v168\n"
    "v_min_i32_e32 v169, 254, v169\n"
    "v_min_i32_e32 v170, 254, v170\n"
    "v_lshl_add_u32 v160, v166, 2, v160\n"
    "v_lshl_add_u32 v161, v167, 2, v161\n"
    "v_lshl_add_u32 v162, v168, 2, v162\n"
    "v_lshl_add_u32 v163, v169, 2, v163\n"
    "v_lshl_add_u32 v164, v170, 2, v164\n"
    "ds_write_b32 v160, v144 offset:4\n"
    "ds_write_b32 v161, v145 offset:4\n"
    "ds_write_b32 v162, v146 offset:4\n"
    "ds_write_b32 v163, v147 offset:4\n"
    "ds_write_b32 v164, v148 offset:4\n"
    ".Lat_bin2:\n"
    "s_waitcnt lgkmcnt(0)\n"
    "s_barrier\n"
    "ds_read_b128 a[0:3], v240 offset:32768\n"
    "ds_read_b128 a[32:35], v240 offset:40960\n"
    "ds_read_b128 a[64:67], v240 offset:49152\n"
    "ds_read_b128 a[4:7], v241 offset:32768\n"
    "ds_read_b128 a[36:39], v241 offset:40960\n"
    "ds_read_b128 a[68:71], v241 offset:49152\n"
    "ds_read_b128 a[8:11], v240 offset:34816\n"
    "ds_read_b128 a[40:43], v240 offset:43008\n"
    "ds_read_b128 a[72:75], v240 offset:51200\n"
    "ds_read_b128 a[12:15], v241 offset:34816\n"
    "ds_read_b128 a[44:47], v241 offset:43008\n"
    "ds_read_b128 a[76:79], v241 offset:51200\n"
    "buffer_load_dword v101, v100, s[8:11], 0 offen offset:0 sc1\n"
    "ds_read_b128 a[16:19], v240 offset:36864\n"
    "ds_read_b128 a[48:51], v240 offset:45056\n"
    "ds_read_b128 a[80:83], v240 offset:53248\n"
    "ds_read_b128 a[20:23], v241 offset:36864\n"
    "ds_read_b128 a[52:55], v241 offset:45056\n"
    "ds_read_b128 a[84:87], v241 offset:53248\n"
    "ds_read_b128 a[24:27], v240 offset:38912\n"
    "ds_read_b128 a[56:59], v240 offset:47104\n"
    "ds_read_b128 a[88:91], v240 offset:55296\n"
    "ds_read_b128 a[28:31], v241 offset:38912\n"
    "ds_read_b128 a[60:63], v241 offset:47104\n"
    "ds_read_b128 a[92:95], v241 offset:55296\n"
    "ds_read_b128 v[40:43], v108 offset:0\n"
    "s_mul_i32 s52, s93, 33792\n"
    "s_add_u32 s52, s52, 5242880\n"
    "s_lshl_b32 s61, s32, 12\n"
    "s_lshl4_add_u32 s46, s34, 6076416\n"
    "s_cmp_eq_u32 s93, 0\n"
    "s_cselect_b32 s46, s46, 2.0\n"
    "s_lshl_b32 s48, s34, 7\n"
    "s_add_u32 s48, s48, 68206592\n"
    "s_lshl_b32 s47, s93, 12\n"
    "s_waitcnt vmcnt(1)\n"
    "s_waitcnt lgkmcnt(0)\n"
    "s_barrier\n"
    "buffer_store_dword v102, off, s[8:11], s46 offset:0 sc1\n"
    "buffer_store_dwordx4 v[40:43], v109, s[8:11], s48 offen offset:0 sc1\n"
    "s_lshl4_add_u32 s46, s34, s47\n"
    "s_add_u32 s46, s46, 6084608\n"
    "s_waitcnt vmcnt(2)\n"
    "v_cmp_ne_u32_e32 vcc, s39, v101\n"
    "s_nop 4\n"
    "s_cbranch_vccz .Lat_apoll_ret\n"
    ".Lat_apoll:\n"
    "s_mov_b32 s53, 0x400000\n"
    ".Lat_apoll_l:\n"
    "s_sleep 1\n"
    "buffer_load_dword v101, v100, s[8:11], 0 offen offset:0 sc1\n"
    "s_waitcnt vmcnt(0)\n"
    "v_cmp_ne_u32_e32 vcc, s39, v101\n"
    "s_sub_u32 s53, s53, 1\n"
    "s_cbranch_scc1 .Lat_apoll_ret\n"
    "s_nop 2\n"
    "s_cbranch_vccnz .Lat_apoll_l\n"
    ".Lat_apoll_ret:\n"
    "s_add_u32 s47, s52, s61\n"
    "buffer_load_dwordx4 v[112:115], v250, s[8:11], s47 offen offset:0 sc1\n"
    "buffer_load_dwordx4 v[116:119], v250, s[8:11], s47 offen offset:1024 sc1\n"
    "buffer_load_dwordx4 v[128:131], v250, s[8:11], s47 offen offset:2048 sc1\n"
    "s_add_u32 s61, s61, 0x1000\n"
    "buffer_load_dwordx4 v[132:135], v250, s[8:11], s47 offen offset:3072 sc1\n"
    "s_bitset0_b32 s61, 14\n"
    "s_add_u32 s47, s52, s61\n"
    "buffer_load_dwordx4 v[144:147], v250, s[8:11], s47 offen offset:0 sc1\n"
    "buffer_load_dwordx4 v[148:151], v250, s[8:11], s47 offen offset:1024 sc1\n"
    "buffer_load_dwordx4 v[160:163], v250, s[8:11], s47 offen offset:2048 sc1\n"
    "s_add_u32 s61, s61, 0x1000\n"
    "buffer_load_dwordx4 v[164:167], v250, s[8:11], s47 offen offset:3072 sc1\n"
    "s_bitset0_b32 s61, 14\n"
    "s_add_u32 s47, s52, s61\n"
    "buffer_load_dwordx4 v[176:179], v250, s[8:11], s47 offen offset:0 sc1\n"
    "buffer_load_dwordx4 v[180:183], v250, s[8:11], s47 offen offset:1024 sc1\n"
    "buffer_load_dwordx4 v[192:195], v250, s[8:11], s47 offen offset:2048 sc1\n"
    "s_add_u32 s61, s61, 0x1000\n"
    "v_lshrrev_b32_e32 v8, 4, v250\n"
    "v_and_b32_e32 v10, 0x70, v251\n"
    "v_bfe_u32 v9, v8, 3, 1\n"
    "v_lshl_add_u32 v248, v9, 9, v10\n"
    "v_lshrrev_b32_e32 v9, 4, v8\n"
    "v_lshl_add_u32 v248, v9, 12, v248\n"
    "s_mul_i32 s43, s50, 3\n"
    "s_xor_b32 s43, s43, s51\n"
    "s_lshl_b32 s43, s43, 15\n"
    "s_add_u32 s43, s43, 32768\n"
    "s_mul_i32 s42, s93, 5120\n"
    "s_lshl_b32 s45, s50, 8\n"
    "s_mul_i32 s40, s32, 1277952\n"
    "s_lshl_b32 s41, s51, 14\n"
    "s_add_u32 s40, s40, s41\n"
    "s_add_u32 s40, s40, s45\n"
    "s_add_u32 s41, s40, 128\n"
    "v_bfe_u32 v9, v8, 2, 3\n"
    "v_lshlrev_b32_e32 v244, 7, v9\n"
    "v_lshrrev_b32_e32 v10, 5, v8\n"
    "v_lshl_add_u32 v244, v10, 10, v244\n"
    "v_and_b32_e32 v9, 6, v9\n"
    "v_bfe_u32 v10, v8, 1, 1\n"
    "v_xor_b32_e32 v9, v9, v10\n"
    "v_lshl_add_u32 v244, v9, 4, v244\n"
    "v_and_b32_e32 v10, 1, v8\n"
    "v_lshl_add_u32 v244, v10, 3, v244\n"
    "s_lshl_b32 s45, s51, 16\n"
    "s_add_u32 s45, s45, 32768\n"
    "v_add_u32_e32 v244, s45, v244\n"
    "v_xor_b32_e32 v245, 32, v244\n"
    "v_xor_b32_e32 v246, 64, v244\n"
    "v_xor_b32_e32 v247, 0x60, v244\n"
    "s_lshl_b32 s45, s51, 15\n"
    "s_add_u32 s48, s45, 32768\n"
    "s_sub_u32 s45, 131072, s45\n"
    "v_add_u32_e32 v242, s45, v240\n"
    "v_add_u32_e32 v240, s48, v240\n"
    "v_xor_b32_e32 v241, 64, v240\n"
    "v_xor_b32_e32 v243, 64, v242\n"
    "s_xor_b32 s45, s93, 2\n"
    "s_mul_i32 s45, s45, 5120\n"
    "v_lshl_add_u32 v249, v8, 2, s45\n"
    "s_lshl_b32 s45, s93, 11\n"
    "v_add_u32_e32 v99, s45, v250\n"
    "v_add_u32_e32 v98, 11345920, v99\n"
    "v_and_b32_e32 v252, 0x30, v250\n"
    "v_add_u32_e32 v252, 0x400, v252\n"
    "s_mul_i32 s59, s32, 999\n"
    "s_andn2_b32 s59, s59, 63\n"
    "s_sub_u32 s48, 3968, s59\n"
    "v_cmp_le_u32_e32 vcc, 0x80, v250\n"
    "v_mov_b32_e32 v80, s48\n"
    "s_add_u32 s59, s59, 6076416\n"
    "v_cndmask_b32_e32 v83, v250, v80, vcc\n"
    "v_bfe_u32 v253, v250, 4, 2\n"
    "v_max_u32_e32 v253, 1, v253\n"
    "v_add_u32_e32 v253, s32, v253\n"
    "v_and_b32_e32 v253, 3, v253\n"
    "v_lshlrev_b32_e32 v253, 9, v253\n"
    "v_mov_b32_e32 v84, s39\n"
    "v_lshrrev_b32_e32 v86, 2, v250\n"
    "v_min_u32_e32 v86, 124, v86\n"
    "s_lshl2_add_u32 s58, s34, s93\n"
    "s_lshl_b32 s58, s58, 7\n"
    "s_add_u32 s58, s58, 5910528\n"
    "s_lshl_b32 s45, s50, 13\n"
    "v_add_u32_e32 v85, s45, v250\n"
    "s_lshl3_add_u32 s45, s45, 49152\n"
    "v_add_u32_e32 v255, s45, v250\n"
    "v_lshrrev_b32_e32 v105, 7, v250\n"
    "v_and_b32_e32 v104, 0x70, v251\n"
    "v_lshl_add_u32 v104, v105, 9, v104\n"
    "v_add_u32_e32 v105, 0xc00, v104\n"
    "v_add_u32_e32 v106, 0x1800, v104\n"
    "v_add_u32_e32 v107, 0x2400, v104\n"
    "s_lshl_b32 s57, s93, 7\n"
    "s_lshl4_add_u32 s56, s57, 24576\n"
    "s_add_u32 s57, s57, 5111808\n"
    "s_mul_i32 s45, s51, 32768\n"
    "s_add_u32 s45, s45, 8192\n"
    "v_subrev_u32_e32 v46, s45, v240\n"
    "v_subrev_u32_e32 v47, s45, v241\n"
    "s_mul_i32 s45, s51, 61440\n"
    "s_add_u32 s45, s45, 8192\n"
    "v_subrev_u32_e32 v4, s45, v244\n"
    "v_subrev_u32_e32 v5, s45, v245\n"
    "v_subrev_u32_e32 v6, s45, v246\n"
    "v_subrev_u32_e32 v7, s45, v247\n"
    "s_waitcnt vmcnt(9)\n"
    "v_mfma_f32_16x16x32_bf16 v[24:27], v[112:115], a[0:3], 0\n"
    "buffer_load_dwordx4 v[196:199], v250, s[8:11], s47 offen offset:3072 sc1\n"
    "s_bitset0_b32 s61, 14\n"
    "v_mfma_f32_16x16x32_bf16 v[28:31], v[116:119], a[0:3], 0\n"
    "v_accvgpr_write_b32 a244, 0\n"
    "v_mfma_f32_16x16x32_bf16 v[48:51], v[112:115], a[32:35], 0\n"
    "v_accvgpr_write_b32 a245, 0\n"
    "v_mfma_f32_16x16x32_bf16 v[52:55], v[116:119], a[32:35], 0\n"
    "s_add_u32 s47, s52, s61\n"
    "buffer_load_dwordx4 v[208:211], v250, s[8:11], s47 offen offset:0 sc1\n"
    "v_mfma_f32_16x16x32_bf16 v[64:67], v[112:115], a[64:67], 0\n"
    "v_bfe_u32 v0, v250, 4, 2\n"
    "v_mfma_f32_16x16x32_bf16 v[68:71], v[116:119], a[64:67], 0\n"
    "s_mov_b32 s18, 5120000\n"
    "v_accvgpr_write_b32 a246, 0\n"
    "s_waitcnt vmcnt(9)\n"
    "v_mfma_f32_16x16x32_bf16 v[24:27], v[128:131], a[4:7], v[24:27]\n"
    "buffer_load_dwordx4 v[212:215], v250, s[8:11], s47 offen offset:1024 sc1\n"
    "s_add_u32 s52, s52, 0x4000\n"
    "v_mfma_f32_16x16x32_bf16 v[28:31], v[132:135], a[4:7], v[28:31]\n"
    "buffer_store_dword v102, off, s[8:11], s46 offset:0 sc1\n"
    "v_accvgpr_write_b32 a247, 0\n"
    "v_mfma_f32_16x16x32_bf16 v[48:51], v[128:131], a[36:39], v[48:51]\n"
    "v_bfe_u32 v1, v250, 6, 2\n"
    "v_accvgpr_write_b32 a248, 0\n"
    "v_mfma_f32_16x16x32_bf16 v[52:55], v[132:135], a[36:39], v[52:55]\n"
    "buffer_load_dwordx4 v[224:227], v250, s[8:11], s47 offen offset:2048 sc1\n"
    "s_add_u32 s61, s61, 0x1000\n"
    "v_mfma_f32_16x16x32_bf16 v[64:67], v[128:131], a[68:71], v[64:67]\n"
    "v_mov_b32_e32 v2, 0x3f80\n"
    "v_accvgpr_write_b32 a249, 0\n"
    "v_mfma_f32_16x16x32_bf16 v[68:71], v[132:135], a[68:71], v[68:71]\n"
    "s_mov_b32 s19, 0x20000\n"
    "s_waitcnt vmcnt(10)\n"
    "v_mfma_f32_16x16x32_bf16 v[24:27], v[144:147], a[8:11], v[24:27]\n"
    "buffer_load_dwordx4 v[228:231], v250, s[8:11], s47 offen offset:3072 sc1\n"
    "s_bitset0_b32 s61, 14\n"
    "v_mfma_f32_16x16x32_bf16 v[28:31], v[148:151], a[8:11], v[28:31]\n"
    "v_accvgpr_write_b32 a250, 0\n"
    "v_mfma_f32_16x16x32_bf16 v[48:51], v[144:147], a[40:43], v[48:51]\n"
    "s_mov_b64 s[20:21], s[8:9]\n"
    "v_accvgpr_write_b32 a251, 0\n"
    "v_mfma_f32_16x16x32_bf16 v[52:55], v[148:151], a[40:43], v[52:55]\n"
    "s_add_u32 s47, s52, s61\n"
    "buffer_load_dwordx4 v[120:123], v250, s[8:11], s47 offen offset:0 sc1\n"
    "v_mfma_f32_16x16x32_bf16 v[64:67], v[144:147], a[72:75], v[64:67]\n"
    "v_cmp_eq_u32_e32 vcc, 3, v0\n"
    "s_nop 1\n"
    "v_cndmask_b32_e64 v2, v2, 1.0, vcc\n"
    "v_mfma_f32_16x16x32_bf16 v[68:71], v[148:151], a[72:75], v[68:71]\n"
    "v_accvgpr_write_b32 a252, 0\n"
    "s_waitcnt vmcnt(10)\n"
    "v_mfma_f32_16x16x32_bf16 v[24:27], v[160:163], a[12:15], v[24:27]\n"
    "buffer_load_dwordx4 v[124:127], v250, s[8:11], s47 offen offset:1024 sc1\n"
    "v_accvgpr_write_b32 a253, 0\n"
    "v_mfma_f32_16x16x32_bf16 v[28:31], v[164:167], a[12:15], v[28:31]\n"
    "s_mov_b32 s22, 5242880\n"
    "v_accvgpr_write_b32 a254, 0\n"
    "v_mfma_f32_16x16x32_bf16 v[48:51], v[160:163], a[44:47], v[48:51]\n"
    "v_cmp_eq_u32_e32 vcc, 0, v1\n"
    "s_nop 1\n"
    "v_cndmask_b32_e32 v3, 0, v2, vcc\n"
    "v_mfma_f32_16x16x32_bf16 v[52:55], v[164:167], a[44:47], v[52:55]\n"
    "buffer_load_dwordx4 v[136:139], v250, s[8:11], s47 offen offset:2048 sc1\n"
    "s_add_u32 s61, s61, 0x1000\n"
    "v_mfma_f32_16x16x32_bf16 v[64:67], v[160:163], a[76:79], v[64:67]\n"
    "v_accvgpr_write_b32 a232, v3\n"
    "v_accvgpr_write_b32 a255, 0\n"
    "v_mfma_f32_16x16x32_bf16 v[68:71], v[164:167], a[76:79], v[68:71]\n"
    "s_mov_b64 s[24:25], s[28:29]\n"
    "v_accvgpr_write_b32 a192, 0\n"
    "s_waitcnt vmcnt(10)\n"
    "v_mfma_f32_16x16x32_bf16 v[24:27], v[176:179], a[16:19], v[24:27]\n"
    "buffer_load_dwordx4 v[140:143], v250, s[8:11], s47 offen offset:3072 sc1\n"
    "s_bitset0_b32 s61, 14\n"
    "v_mfma_f32_16x16x32_bf16 v[28:31], v[180:183], a[16:19], v[28:31]\n"
    "v_accvgpr_write_b32 a193, 0\n"
    "v_mfma_f32_16x16x32_bf16 v[48:51], v[176:179], a[48:51], v[48:51]\n"
    "v_cmp_eq_u32_e32 vcc, 1, v1\n"
    "s_nop 1\n"
    "v_cndmask_b32_e32 v3, 0, v2, vcc\n"
    "v_mfma_f32_16x16x32_bf16 v[52:55], v[180:183], a[48:51], v[52:55]\n"
    "s_add_u32 s47, s52, s61\n"
    "buffer_load_dwordx4 v[152:155], v250, s[8:11], s47 offen offset:0 sc1\n"
    "v_mfma_f32_16x16x32_bf16 v[64:67], v[176:179], a[80:83], v[64:67]\n"
    "buffer_load_dword v80, v83, s[8:11], s59 offen offset:0 sc1\n"
    "v_accvgpr_write_b32 a194, 0\n"
    "v_mfma_f32_16x16x32_bf16 v[68:71], v[180:183], a[80:83], v[68:71]\n"
    "s_movk_i32 s26, 20000\n"
    "v_accvgpr_write_b32 a195, 0\n"
    "s_waitcnt vmcnt(11)\n"
    "v_mfma_f32_16x16x32_bf16 v[24:27], v[192:195], a[20:23], v[24:27]\n"
    "buffer_load_dwordx4 v[156:159], v250, s[8:11], s47 offen offset:1024 sc1\n"
    "v_accvgpr_write_b32 a196, 0\n"
    "v_mfma_f32_16x16x32_bf16 v[28:31], v[196:199], a[20:23], v[28:31]\n"
    "v_accvgpr_write_b32 a233, v3\n"
    "v_mfma_f32_16x16x32_bf16 v[48:51], v[192:195], a[52:55], v[48:51]\n"
    "s_bfe_u32 s88, s92, 0x20001\n"
    "v_accvgpr_write_b32 a197, 0\n"
    "v_mfma_f32_16x16x32_bf16 v[52:55], v[196:199], a[52:55], v[52:55]\n"
    "buffer_load_dwordx4 v[168:171], v250, s[8:11], s47 offen offset:2048 sc1\n"
    "s_add_u32 s61, s61, 0x1000\n"
    "v_mfma_f32_16x16x32_bf16 v[64:67], v[192:195], a[84:87], v[64:67]\n"
    "v_cmp_eq_u32_e32 vcc, 2, v1\n"
    "s_nop 1\n"
    "v_cndmask_b32_e32 v3, 0, v2, vcc\n"
    "v_mfma_f32_16x16x32_bf16 v[68:71], v[196:199], a[84:87], v[68:71]\n"
    "v_accvgpr_write_b32 a198, 0\n"
    "s_waitcnt vmcnt(11)\n"
    "v_mfma_f32_16x16x32_bf16 v[24:27], v[208:211], a[24:27], v[24:27]\n"
    "buffer_load_dwordx4 v[172:175], v250, s[8:11], s47 offen offset:3072 sc1\n"
    "s_bitset0_b32 s61, 14\n"
    "v_mfma_f32_16x16x32_bf16 v[28:31], v[212:215], a[24:27], v[28:31]\n"
    "s_lshl_b32 s82, s88, 7\n"
    "v_accvgpr_write_b32 a199, 0\n"
    "v_mfma_f32_16x16x32_bf16 v[48:51], v[208:211], a[56:59], v[48:51]\n"
    "v_accvgpr_write_b32 a234, v3\n"
    "v_accvgpr_write_b32 a200, 0\n"
    "v_mfma_f32_16x16x32_bf16 v[52:55], v[212:215], a[56:59], v[52:55]\n"
    "s_add_u32 s47, s52, s61\n"
    "buffer_load_dwordx4 v[184:187], v250, s[8:11], s47 offen offset:0 sc1\n"
    "v_mfma_f32_16x16x32_bf16 v[64:67], v[208:211], a[88:91], v[64:67]\n"
    "v_cmp_eq_u32_e32 vcc, 3, v1\n"
    "s_nop 1\n"
    "v_cndmask_b32_e32 v3, 0, v2, vcc\n"
    "v_mfma_f32_16x16x32_bf16 v[68:71], v[212:215], a[88:91], v[68:71]\n"
    "s_add_u32 s82, s82, 6101248\n"
    "v_accvgpr_write_b32 a201, 0\n"
    "s_waitcnt vmcnt(10)\n"
    "v_mfma_f32_16x16x32_bf16 v[24:27], v[224:227], a[28:31], v[24:27]\n"
    "buffer_load_dwordx4 v[188:191], v250, s[8:11], s47 offen offset:1024 sc1\n"
    "v_accvgpr_write_b32 a202, 0\n"
    "v_mfma_f32_16x16x32_bf16 v[28:31], v[228:231], a[28:31], v[28:31]\n"
    "v_accvgpr_write_b32 a235, v3\n"
    "v_accvgpr_write_b32 a203, 0\n"
    "v_mfma_f32_16x16x32_bf16 v[48:51], v[224:227], a[60:63], v[48:51]\n"
    "s_lshl_b32 s83, s88, 17\n"
    "v_accvgpr_write_b32 a204, 0\n"
    "v_mfma_f32_16x16x32_bf16 v[52:55], v[228:231], a[60:63], v[52:55]\n"
    "buffer_load_dwordx4 v[200:203], v250, s[8:11], s47 offen offset:2048 sc1\n"
    "s_add_u32 s61, s61, 0x1000\n"
    "v_mfma_f32_16x16x32_bf16 v[64:67], v[224:227], a[92:95], v[64:67]\n"
    "s_and_b32 s89, s34, 31\n"
    "v_accvgpr_write_b32 a205, 0\n"
    "v_mfma_f32_16x16x32_bf16 v[68:71], v[228:231], a[92:95], v[68:71]\n"
    "s_lshl2_add_u32 s89, s89, s93\n"
    "v_accvgpr_write_b32 a206, 0\n"
    "s_waitcnt vmcnt(10)\n"
    "v_mfma_f32_16x16x32_bf16 v[32:35], v[120:123], a[0:3], 0\n"
    "buffer_load_dwordx4 v[204:207], v250, s[8:11], s47 offen offset:3072 sc1\n"
    "s_bitset0_b32 s61, 14\n"
    "v_mfma_f32_16x16x32_bf16 v[36:39], v[124:127], a[0:3], 0\n"
    "v_cvt_pk_bf16_f32 v24, v24, v25\n"
    "v_accvgpr_write_b32 a207, 0\n"
    "v_mfma_f32_16x16x32_bf16 v[56:59], v[120:123], a[32:35], 0\n"
    "v_cvt_pk_bf16_f32 v25, v26, v27\n"
    "v_accvgpr_write_b32 a208, 0\n"
    "v_mfma_f32_16x16x32_bf16 v[60:63], v[124:127], a[32:35], 0\n"
    "s_add_u32 s47, s52, s61\n"
    "buffer_load_dwordx4 v[216:219], v250, s[8:11], s47 offen offset:0 sc1\n"
    "v_mfma_f32_16x16x32_bf16 v[72:75], v[120:123], a[64:67], 0\n"
    "v_cvt_pk_bf16_f32 v26, v28, v29\n"
    "v_cvt_pk_bf16_f32 v27, v30, v31\n"
    "v_mfma_f32_16x16x32_bf16 v[76:79], v[124:127], a[64:67], 0\n"
    "buffer_store_dwordx4 v[24:27], v98, s[8:11], s35 offen offset:0 sc1\n"
    "v_accvgpr_write_b32 a209, 0\n"
    "s_waitcnt vmcnt(11)\n"
    "v_mfma_f32_16x16x32_bf16 v[32:35], v[136:139], a[4:7], v[32:35]\n"
    "buffer_load_dwordx4 v[220:223], v250, s[8:11], s47 offen offset:1024 sc1\n"
    "s_add_u32 s52, s52, 0x4000\n"
    "v_mfma_f32_16x16x32_bf16 v[36:39], v[140:143], a[4:7], v[36:39]\n"
    "v_cvt_pk_bf16_f32 v48, v48, v49\n"
    "v_accvgpr_write_b32 a210, 0\n"
    "v_mfma_f32_16x16x32_bf16 v[56:59], v[136:139], a[36:39], v[56:59]\n"
    "v_cvt_pk_bf16_f32 v49, v50, v51\n"
    "v_cvt_pk_bf16_f32 v50, v52, v53\n"
    "v_mfma_f32_16x16x32_bf16 v[60:63], v[140:143], a[36:39], v[60:63]\n"
    "buffer_load_dwordx4 v[232:235], v250, s[8:11], s47 offen offset:2048 sc1\n"
    "s_add_u32 s61, s61, 0x1000\n"
    "s_waitcnt vmcnt(11)\n"
    "v_cmp_ne_u32_e32 vcc, s39, v80\n"
    "v_mfma_f32_16x16x32_bf16 v[72:75], v[136:139], a[68:71], v[72:75]\n"
    "v_cvt_pk_bf16_f32 v51, v54, v55\n"
    "buffer_store_dwordx4 v[48:51], v98, s[8:11], s54 offen offset:0 sc1\n"
    "v_mfma_f32_16x16x32_bf16 v[76:79], v[140:143], a[68:71], v[76:79]\n"
    "v_cvt_pk_bf16_f32 v64, v64, v65\n"
    "v_cvt_pk_bf16_f32 v65, v66, v67\n"
    "s_waitcnt vmcnt(11)\n"
    "v_mfma_f32_16x16x32_bf16 v[32:35], v[152:155], a[8:11], v[32:35]\n"
    "buffer_load_dwordx4 v[236:239], v250, s[8:11], s47 offen offset:3072 sc1\n"
    "s_bitset0_b32 s61, 14\n"
    "v_mfma_f32_16x16x32_bf16 v[36:39], v[156:159], a[8:11], v[36:39]\n"
    "s_cbranch_vccnz .Lat_h1poll\n"
    ".Lat_h1poll_ret:\n"
    "s_mov_b32 m0, s56\n"
    "v_mfma_f32_16x16x32_bf16 v[56:59], v[152:155], a[40:43], v[56:59]\n"
    "buffer_load_dwordx4 v104, s[8:11], s57 offen offset:0 sc1 lds\n"
    "v_cvt_pk_bf16_f32 v66, v68, v69\n"
    "v_mfma_f32_16x16x32_bf16 v[60:63], v[156:159], a[40:43], v[60:63]\n"
    "v_cvt_pk_bf16_f32 v67, v70, v71\n"
    "buffer_store_dwordx4 v[64:67], v98, s[8:11], s60 offen offset:0 sc1\n"
    "v_mfma_f32_16x16x32_bf16 v[72:75], v[152:155], a[72:75], v[72:75]\n"
    "s_lshl_b32 s89, s89, 10\n"
    "v_accvgpr_write_b32 a211, 0\n"
    "v_mfma_f32_16x16x32_bf16 v[76:79], v[156:159], a[72:75], v[76:79]\n"
    "s_add_u32 s83, s83, s89\n"
    "v_accvgpr_write_b32 a212, 0\n"
    "s_waitcnt vmcnt(12)\n"
    "v_mfma_f32_16x16x32_bf16 v[32:35], v[168:171], a[12:15], v[32:35]\n"
    "buffer_load_dwordx4 v105, s[8:11], s57 offen offset:1024 sc1 lds\n"
    "v_accvgpr_write_b32 a213, 0\n"
    "v_mfma_f32_16x16x32_bf16 v[36:39], v[172:175], a[12:15], v[36:39]\n"
    "s_add_u32 s83, s83, 67682304\n"
    "v_accvgpr_write_b32 a214, 0\n"
    "v_mfma_f32_16x16x32_bf16 v[56:59], v[168:171], a[44:47], v[56:59]\n"
    "ds_write_b128 v99, v[24:27] offset:0\n"
    "v_accvgpr_write_b32 a215, 0\n"
    "v_mfma_f32_16x16x32_bf16 v[60:63], v[172:175], a[44:47], v[60:63]\n"
    "s_add_u32 m0, s43, 0\n"
    "v_accvgpr_write_b32 a216, 0\n"
    "v_mfma_f32_16x16x32_bf16 v[72:75], v[168:171], a[76:79], v[72:75]\n"
    "buffer_load_dwordx4 v104, s[8:11], s40 offen offset:0 sc1 lds\n"
    "v_accvgpr_write_b32 a217, 0\n"
    "v_mfma_f32_16x16x32_bf16 v[76:79], v[172:175], a[76:79], v[76:79]\n"
    "v_accvgpr_write_b32 a218, 0\n"
    "s_waitcnt vmcnt(12)\n"
    "v_mfma_f32_16x16x32_bf16 v[32:35], v[184:187], a[16:19], v[32:35]\n"
    "s_lshl_b32 s30, s93, 10\n"
    "v_mfma_f32_16x16x32_bf16 v[36:39], v[188:191], a[16:19], v[36:39]\n"
    "v_accvgpr_write_b32 a219, 0\n"
    "v_mfma_f32_16x16x32_bf16 v[56:59], v[184:187], a[48:51], v[56:59]\n"
    "buffer_load_dwordx4 v105, s[8:11], s40 offen offset:1024 sc1 lds\n"
    "v_mfma_f32_16x16x32_bf16 v[60:63], v[188:191], a[48:51], v[60:63]\n"
    "v_accvgpr_write_b32 a220, 0\n"
    "v_mfma_f32_16x16x32_bf16 v[72:75], v[184:187], a[80:83], v[72:75]\n"
    "s_add_u32 s30, s30, 20480\n"
    "v_mfma_f32_16x16x32_bf16 v[76:79], v[188:191], a[80:83], v[76:79]\n"
    "ds_write_b128 v99, v[48:51] offset:8192\n"
    "v_accvgpr_write_b32 a221, 0\n"
    "s_waitcnt vmcnt(11)\n"
    "v_mfma_f32_16x16x32_bf16 v[32:35], v[200:203], a[20:23], v[32:35]\n"
    "buffer_load_dwordx4 v106, s[8:11], s40 offen offset:2048 sc1 lds\n"
    "v_mfma_f32_16x16x32_bf16 v[36:39], v[204:207], a[20:23], v[36:39]\n"
    "v_accvgpr_write_b32 a222, 0\n"
    "v_mfma_f32_16x16x32_bf16 v[56:59], v[200:203], a[52:55], v[56:59]\n"
    "v_accvgpr_write_b32 a223, 0\n"
    "v_mfma_f32_16x16x32_bf16 v[60:63], v[204:207], a[52:55], v[60:63]\n"
    "s_bitcmp0_b32 s92, 0\n"
    "s_cselect_b32 s85, 1, 0\n"
    "s_cselect_b32 s80, 5, 0\n"
    "v_mfma_f32_16x16x32_bf16 v[72:75], v[200:203], a[84:87], v[72:75]\n"
    "buffer_load_dwordx4 v107, s[8:11], s40 offen offset:3072 sc1 lds\n"
    "v_mfma_f32_16x16x32_bf16 v[76:79], v[204:207], a[84:87], v[76:79]\n"
    "v_accvgpr_write_b32 a224, 0\n"
    "s_waitcnt vmcnt(10)\n"
    "v_mfma_f32_16x16x32_bf16 v[32:35], v[216:219], a[24:27], v[32:35]\n"
    "s_bitcmp0_b32 s92, 0\n"
    "s_cselect_b32 s82, s82, 2.0\n"
    "s_cselect_b32 s83, s83, 2.0\n"
    "v_mfma_f32_16x16x32_bf16 v[36:39], v[220:223], a[24:27], v[36:39]\n"
    "s_add_u32 s40, s40, 0x8000\n"
    "v_mfma_f32_16x16x32_bf16 v[56:59], v[216:219], a[56:59], v[56:59]\n"
    "ds_write_b128 v99, v[64:67] offset:49152\n"
    "v_mfma_f32_16x16x32_bf16 v[60:63], v[220:223], a[56:59], v[60:63]\n"
    "v_accvgpr_write_b32 a225, 0\n"
    "v_mfma_f32_16x16x32_bf16 v[72:75], v[216:219], a[88:91], v[72:75]\n"
    "v_accvgpr_write_b32 a226, 0\n"
    "v_mfma_f32_16x16x32_bf16 v[76:79], v[220:223], a[88:91], v[76:79]\n"
    "s_mov_b32 s84, 2.0\n"
    "s_waitcnt vmcnt(7)\n"
    "v_mfma_f32_16x16x32_bf16 v[32:35], v[232:235], a[28:31], v[32:35]\n"
    "v_accvgpr_write_b32 a227, 0\n"
    "v_mfma_f32_16x16x32_bf16 v[36:39], v[236:239], a[28:31], v[36:39]\n"
    "v_accvgpr_write_b32 a228, 0\n"
    "v_mfma_f32_16x16x32_bf16 v[56:59], v[232:235], a[60:63], v[56:59]\n"
    "v_accvgpr_write_b32 a229, 0\n"
    "v_mfma_f32_16x16x32_bf16 v[60:63], v[236:239], a[60:63], v[60:63]\n"
    "v_accvgpr_write_b32 a230, 0\n"
    "v_mfma_f32_16x16x32_bf16 v[72:75], v[232:235], a[92:95], v[72:75]\n"
    "s_movk_i32 s90, 512\n"
    "v_mfma_f32_16x16x32_bf16 v[76:79], v[236:239], a[92:95], v[76:79]\n"
    "v_accvgpr_write_b32 a231, 0\n"
    "v_cvt_pk_bf16_f32 v32, v32, v33\n"
    "v_cvt_pk_bf16_f32 v33, v34, v35\n"
    "v_cvt_pk_bf16_f32 v34, v36, v37\n"
    "v_cvt_pk_bf16_f32 v35, v38, v39\n"
    "ds_write_b128 v99, v[32:35] offset:1024\n"
    "buffer_store_dwordx4 v[32:35], v98, s[8:11], s35 offen offset:1024 sc1\n"
    "v_cvt_pk_bf16_f32 v56, v56, v57\n"
    "v_cvt_pk_bf16_f32 v57, v58, v59\n"
    "v_cvt_pk_bf16_f32 v58, v60, v61\n"
    "v_cvt_pk_bf16_f32 v59, v62, v63\n"
    "ds_write_b128 v99, v[56:59] offset:9216\n"
    "buffer_store_dwordx4 v[56:59], v98, s[8:11], s54 offen offset:1024 sc1\n"
    "v_cvt_pk_bf16_f32 v72, v72, v73\n"
    "v_cvt_pk_bf16_f32 v73, v74, v75\n"
    "v_cvt_pk_bf16_f32 v74, v76, v77\n"
    "v_cvt_pk_bf16_f32 v75, v78, v79\n"
    "ds_write_b128 v99, v[72:75] offset:50176\n"
    "buffer_store_dwordx4 v[72:75], v98, s[8:11], s60 offen offset:1024 sc1\n"
    "v_mov_b64_e32 v[8:9], 0\n"
    "v_mov_b64_e32 v[10:11], 0\n"
    "s_add_u32 m0, s43, 4096\n"
    "v_mov_b64_e32 v[12:13], 0\n"
    "v_mov_b64_e32 v[14:15], 0\n"
    "buffer_load_dwordx4 v104, s[8:11], s41 offen offset:0 sc1 lds\n"
    "v_mov_b64_e32 v[16:17], 0\n"
    "v_mov_b64_e32 v[18:19], 0\n"
    "buffer_load_dwordx4 v105, s[8:11], s41 offen offset:1024 sc1 lds\n"
    "v_mov_b64_e32 v[28:29], 0\n"
    "v_mov_b64_e32 v[30:31], 0\n"
    "buffer_load_dwordx4 v106, s[8:11], s41 offen offset:2048 sc1 lds\n"
    "v_mov_b64_e32 v[32:33], 0\n"
    "v_mov_b64_e32 v[34:35], 0\n"
    "buffer_load_dwordx4 v107, s[8:11], s41 offen offset:3072 sc1 lds\n"
    "v_mov_b64_e32 v[36:37], 0\n"
    "v_mov_b64_e32 v[38:39], 0\n"
    "s_add_u32 s41, s41, 0x8000\n"
    "v_mov_b64_e32 v[48:49], 0\n"
    "v_mov_b64_e32 v[50:51], 0\n"
    "v_mov_b64_e32 v[52:53], 0\n"
    "v_mov_b64_e32 v[54:55], 0\n"
    "v_mov_b64_e32 v[56:57], 0\n"
    "v_mov_b64_e32 v[58:59], 0\n"
    "v_mov_b64_e32 v[68:69], 0\n"
    "v_mov_b64_e32 v[70:71], 0\n"
    "v_mov_b64_e32 v[72:73], 0\n"
    "v_mov_b64_e32 v[74:75], 0\n"
    "v_mov_b64_e32 v[76:77], 0\n"
    "v_mov_b64_e32 v[78:79], 0\n"
    "s_cmp_lg_u32 s93, 3\n"
    "s_cbranch_scc1 .Lat_wn40\n"
    "s_call_b64 s[2:3], .Lat_w6\n"
    "s_call_b64 s[2:3], .Lat_w10\n"
    ".Lat_wn40:\n"
    "s_cmp_lg_u32 s93, 2\n"
    "s_cbranch_scc1 .Lat_wn41\n"
    "s_call_b64 s[2:3], .Lat_w7\n"
    "s_call_b64 s[2:3], .Lat_w11\n"
    ".Lat_wn41:\n"
    "s_cmp_lg_u32 s93, 1\n"
    "s_cbranch_scc1 .Lat_wn42\n"
    "s_call_b64 s[2:3], .Lat_w8\n"
    "s_call_b64 s[2:3], .Lat_w12\n"
    ".Lat_wn42:\n"
    "s_cmp_lg_u32 s93, 0\n"
    "s_cbranch_scc1 .Lat_wn43\n"
    "s_call_b64 s[2:3], .Lat_w9\n"
    "s_call_b64 s[2:3], .Lat_w13\n"
    ".Lat_wn43:\n"
    "s_waitcnt vmcnt(4)\n"
    "buffer_store_dword v84, v86, s[8:11], s58 offen offset:0 sc1\n"
    "v_mov_b64_e32 v[88:89], 0\n"
    "v_mov_b64_e32 v[90:91], 0\n"
    "s_add_u32 m0, s43, 8192\n"
    "v_mov_b64_e32 v[92:93], 0\n"
    "v_mov_b64_e32 v[94:95], 0\n"
    "buffer_load_dwordx4 v104, s[8:11], s40 offen offset:0 sc1 lds\n"
    "v_mov_b64_e32 v[96:97], 0\n"
    "v_mov_b64_e32 v[98:99], 0\n"
    "buffer_load_dwordx4 v105, s[8:11], s40 offen offset:1024 sc1 lds\n"
    "v_mov_b64_e32 v[108:109], 0\n"
    "v_mov_b64_e32 v[110:111], 0\n"
    "buffer_load_dwordx4 v106, s[8:11], s40 offen offset:2048 sc1 lds\n"
    "v_mov_b64_e32 v[112:113], 0\n"
    "v_mov_b64_e32 v[114:115], 0\n"
    "buffer_load_dwordx4 v107, s[8:11], s40 offen offset:3072 sc1 lds\n"
    "v_mov_b64_e32 v[116:117], 0\n"
    "v_mov_b64_e32 v[118:119], 0\n"
    "s_add_u32 s40, s40, 0x8000\n"
    "v_mov_b64_e32 v[128:129], 0\n"
    "v_mov_b64_e32 v[130:131], 0\n"
    "s_add_u32 m0, s43, 12288\n"
    "v_mov_b64_e32 v[132:133], 0\n"
    "v_mov_b64_e32 v[134:135], 0\n"
    "buffer_load_dwordx4 v104, s[8:11], s41 offen offset:0 sc1 lds\n"
    "v_mov_b64_e32 v[136:137], 0\n"
    "v_mov_b64_e32 v[138:139], 0\n"
    "buffer_load_dwordx4 v105, s[8:11], s41 offen offset:1024 sc1 lds\n"
    "buffer_load_dwordx4 v106, s[8:11], s41 offen offset:2048 sc1 lds\n"
    "buffer_load_dwordx4 v107, s[8:11], s41 offen offset:3072 sc1 lds\n"
    "s_add_u32 s41, s41, 0x8000\n"
    ".Lat_p2:\n"
    "v_mov_b32_e32 v220, 0xc2540000\n"
    "v_mov_b32_e32 v221, 0xc2540000\n"
    "v_mov_b64_e32 v[222:223], v[220:221]\n"
    "v_mov_b64_e32 v[224:225], v[220:221]\n"
    "v_mov_b64_e32 v[226:227], v[220:221]\n"
    "v_mov_b64_e32 v[228:229], v[220:221]\n"
    "v_mov_b64_e32 v[230:231], v[220:221]\n"
    "v_mov_b64_e32 v[232:233], v[220:221]\n"
    "v_mov_b64_e32 v[234:235], v[220:221]\n"
    "v_mov_b64_e32 v[236:237], v[220:221]\n"
    "v_mov_b64_e32 v[238:239], v[220:221]\n"
    "s_andn2_b32 s47, s58, 0x600\n"
    "s_mov_b32 s44, 9\n"
    "s_waitcnt vmcnt(9)\n"
    "s_waitcnt lgkmcnt(0)\n"
    "s_barrier\n"
    "s_cmp_lg_u32 s49, 0\n"
    "s_cbranch_scc1 .Lat_maxpass\n"
    "ds_read_b128 a[0:3], v85 offset:0\n"
    "ds_read_b128 a[4:7], v85 offset:1024\n"
    "ds_read_b128 a[8:11], v85 offset:2048\n"
    "ds_read_b128 a[12:15], v85 offset:3072\n"
    "ds_read_b128 a[16:19], v85 offset:4096\n"
    "ds_read_b128 a[20:23], v85 offset:5120\n"
    "ds_read_b128 a[24:27], v85 offset:6144\n"
    "ds_read_b128 a[28:31], v85 offset:7168\n"
    "ds_read_b128 a[32:35], v255 offset:0\n"
    "ds_read_b128 a[36:39], v255 offset:1024\n"
    "ds_read_b128 a[40:43], v255 offset:2048\n"
    "ds_read_b128 a[44:47], v255 offset:3072\n"
    "ds_read_b128 a[48:51], v255 offset:4096\n"
    "ds_read_b128 a[52:55], v255 offset:5120\n"
    "ds_read_b128 a[56:59], v255 offset:6144\n"
    "ds_read_b128 a[60:63], v255 offset:7168\n"
    ".Lat_maxpass_ret:\n"
    "ds_read_b128 a[160:163], v46 offset:0\n"
    "ds_read_b128 a[164:167], v47 offset:0\n"
    "ds_read_b128 a[168:171], v46 offset:2048\n"
    "ds_read_b128 a[172:175], v47 offset:2048\n"
    "ds_read_b128 a[176:179], v46 offset:4096\n"
    "ds_read_b128 a[180:183], v47 offset:4096\n"
    "ds_read_b128 a[184:187], v46 offset:6144\n"
    "ds_read_b128 a[188:191], v47 offset:6144\n"
    "s_mul_i32 s48, s33, 81920\n"
    "s_add_u32 s48, s48, 11345920\n"
    "s_lshl_b32 s46, s93, 11\n"
    "s_add_u32 s48, s48, s46\n"
    "s_add_u32 s46, s46, 49152\n"
    "buffer_load_dword v254, v253, s[8:11], s47 offen offset:0 sc1\n"
    ".long 0xbedf00ff\n"
    ".Lat_w1:\n"
    "s_setpc_b64 s[2:3]\n"
    "s_waitcnt lgkmcnt(4)\n"
    "v_mfma_f32_16x16x32_bf16 v[212:215], a[160:163], a[0:3], v[220:223]\n"
    "s_and_b32 s45, s32, 1\n"
    "ds_read_b64_tr_b16 v[140:141], v4 offset:0\n"
    "v_mfma_f32_16x16x32_bf16 v[216:219], a[160:163], a[32:35], v[224:227]\n"
    "s_xor_b32 s45, s45, 9\n"
    "ds_read_b64_tr_b16 v[142:143], v5 offset:0\n"
    "v_mfma_f32_16x16x32_bf16 v[212:215], a[164:167], a[4:7], v[212:215]\n"
    "s_lshl_b32 s45, s45, 13\n"
    "ds_read_b64_tr_b16 v[144:145], v6 offset:0\n"
    "v_mfma_f32_16x16x32_bf16 v[216:219], a[164:167], a[36:39], v[216:219]\n"
    "s_add_u32 s45, s45, s48\n"
    "ds_read_b64_tr_b16 v[146:147], v7 offset:0\n"
    "v_mfma_f32_16x16x32_bf16 v[212:215], a[168:171], a[8:11], v[212:215]\n"
    "s_add_u32 s54, s32, 1\n"
    "ds_read_b64_tr_b16 v[148:149], v4 offset:2048\n"
    "v_mfma_f32_16x16x32_bf16 v[216:219], a[168:171], a[40:43], v[216:219]\n"
    "s_and_b32 s54, s54, 3\n"
    "ds_read_b64_tr_b16 v[150:151], v5 offset:2048\n"
    "v_mfma_f32_16x16x32_bf16 v[212:215], a[172:175], a[12:15], v[212:215]\n"
    "s_lshl_b32 s54, s54, 13\n"
    "ds_read_b64_tr_b16 v[152:153], v6 offset:2048\n"
    "v_mfma_f32_16x16x32_bf16 v[216:219], a[172:175], a[44:47], v[216:219]\n"
    "s_add_u32 s54, s54, s48\n"
    "ds_read_b64_tr_b16 v[154:155], v7 offset:2048\n"
    "s_waitcnt lgkmcnt(8)\n"
    "v_mfma_f32_16x16x32_bf16 v[212:215], a[176:179], a[16:19], v[212:215]\n"
    "s_add_u32 s55, s32, 2\n"
    "ds_read_b128 v[180:183], v240 offset:2048\n"
    "v_mfma_f32_16x16x32_bf16 v[216:219], a[176:179], a[48:51], v[216:219]\n"
    "s_and_b32 s55, s55, 3\n"
    "ds_read_b128 v[184:187], v241 offset:2048\n"
    "v_mfma_f32_16x16x32_bf16 v[212:215], a[180:183], a[20:23], v[212:215]\n"
    "s_lshl_b32 s55, s55, 13\n"
    "ds_read_b128 v[188:191], v240 offset:6144\n"
    "v_mfma_f32_16x16x32_bf16 v[216:219], a[180:183], a[52:55], v[216:219]\n"
    "s_add_u32 s55, s55, s48\n"
    "ds_read_b128 v[192:195], v241 offset:6144\n"
    "v_mfma_f32_16x16x32_bf16 v[212:215], a[184:187], a[24:27], v[212:215]\n"
    "s_add_u32 s60, s32, 3\n"
    "ds_read_b128 v[196:199], v242 offset:2048\n"
    "v_mfma_f32_16x16x32_bf16 v[216:219], a[184:187], a[56:59], v[216:219]\n"
    "s_and_b32 s60, s60, 3\n"
    "ds_read_b128 v[200:203], v243 offset:2048\n"
    "v_mfma_f32_16x16x32_bf16 v[212:215], a[188:191], a[28:31], v[212:215]\n"
    "s_lshl_b32 s60, s60, 13\n"
    "ds_read_b128 v[204:207], v242 offset:6144\n"
    "v_mfma_f32_16x16x32_bf16 v[216:219], a[188:191], a[60:63], v[216:219]\n"
    "s_add_u32 s60, s60, s48\n"
    "ds_read_b128 v[208:211], v243 offset:6144\n"
    "s_lshr_b32 s61, s32, 1\n"
    "s_or_b32 s61, s61, s50\n"
    "s_sub_u32 s61, s61, 1\n"
    "s_add_u32 m0, s46, 65536\n"
    "s_waitcnt vmcnt(0)\n"
    "v_cmp_ne_u32_e32 vcc, s39, v254\n"
    "ds_read_b128 a[160:163], v240 offset:0\n"
    "ds_read_b128 a[164:167], v241 offset:0\n"
    "ds_read_b128 a[168:171], v240 offset:4096\n"
    "ds_read_b128 a[172:175], v241 offset:4096\n"
    "ds_read_b128 a[176:179], v242 offset:0\n"
    "s_cbranch_vccz .Lat_qpoll_ret\n"
    ".Lat_qpoll:\n"
    "s_mov_b32 s53, 0x400000\n"
    ".Lat_qpoll_l:\n"
    "s_sleep 0\n"
    "buffer_load_dword v254, v253, s[8:11], s47 offen offset:0 sc1\n"
    "s_waitcnt vmcnt(0)\n"
    "v_cmp_ne_u32_e32 vcc, s39, v254\n"
    "s_sub_u32 s53, s53, 1\n"
    "s_cbranch_scc1 .Lat_qpoll_ret\n"
    "s_nop 2\n"
    "s_cbranch_vccnz .Lat_qpoll_l\n"
    ".Lat_qpoll_ret:\n"
    "buffer_load_dwordx4 v250, s[8:11], s45 offen offset:0 sc1 lds\n"
    "buffer_load_dwordx4 v250, s[8:11], s45 offen offset:1024 sc1 lds\n"
    "s_add_u32 m0, s46, 8192\n"
    "ds_read_b128 a[180:183], v243 offset:0\n"
    "s_nop 0\n"
    "ds_read_b128 a[184:187], v242 offset:4096\n"
    "buffer_load_dwordx4 v250, s[8:11], s54 offen offset:0 sc1 lds\n"
    "ds_read_b128 a[188:191], v243 offset:4096\n"
    "buffer_load_dwordx4 v250, s[8:11], s54 offen offset:1024 sc1 lds\n"
    "v_exp_f32_e32 v212, v212\n"
    "s_add_u32 m0, s46, 73728\n"
    "v_exp_f32_e32 v213, v213\n"
    "s_add_u32 s54, s54, 0x8000\n"
    "v_exp_f32_e32 v214, v214\n"
    "buffer_load_dwordx4 v250, s[8:11], s54 offen offset:0 sc1 lds\n"
    "v_exp_f32_e32 v215, v215\n"
    "buffer_load_dwordx4 v250, s[8:11], s54 offen offset:1024 sc1 lds\n"
    "v_exp_f32_e32 v216, v216\n"
    "s_add_u32 m0, s46, 32768\n"
    "v_exp_f32_e32 v217, v217\n"
    "s_nop 0\n"
    "v_exp_f32_e32 v218, v218\n"
    "buffer_load_dwordx4 v250, s[8:11], s55 offen offset:0 sc1 lds\n"
    "v_exp_f32_e32 v219, v219\n"
    "buffer_load_dwordx4 v250, s[8:11], s55 offen offset:1024 sc1 lds\n"
    "v_cvt_pk_bf16_f32 v212, v212, v213\n"
    "v_cvt_pk_bf16_f32 v213, v214, v215\n"
    "v_cvt_pk_bf16_f32 v216, v216, v217\n"
    "v_cvt_pk_bf16_f32 v217, v218, v219\n"
    "v_and_b32_e32 v216, s61, v216\n"
    "v_and_b32_e32 v217, s61, v217\n"
    "s_waitcnt lgkmcnt(15)\n"
    "v_mfma_f32_16x16x16_bf16 a[236:239], v[140:141], v[212:213], 0\n"
    "s_add_u32 m0, s46, 98304\n"
    "v_mfma_f32_16x16x16_bf16 v[0:3], v[142:143], v[212:213], 0\n"
    "s_add_u32 s55, s55, 0x8000\n"
    "v_mfma_f32_16x16x16_bf16 v[20:23], v[144:145], v[212:213], 0\n"
    "buffer_load_dwordx4 v250, s[8:11], s55 offen offset:0 sc1 lds\n"
    "v_mfma_f32_16x16x16_bf16 v[40:43], v[146:147], v[212:213], 0\n"
    "buffer_load_dwordx4 v250, s[8:11], s55 offen offset:1024 sc1 lds\n"
    "v_mfma_f32_16x16x16_bf16 v[60:63], v[148:149], v[212:213], 0\n"
    "s_add_u32 m0, s46, 40960\n"
    "v_mfma_f32_16x16x16_bf16 v[80:83], v[150:151], v[212:213], 0\n"
    "buffer_load_dwordx4 v250, s[8:11], s60 offen offset:0 sc1 lds\n"
    "v_mfma_f32_16x16x16_bf16 v[100:103], v[152:153], v[212:213], 0\n"
    "buffer_load_dwordx4 v250, s[8:11], s60 offen offset:1024 sc1 lds\n"
    "v_mfma_f32_16x16x16_bf16 v[120:123], v[154:155], v[212:213], 0\n"
    "s_add_u32 m0, s46, 106496\n"
    "v_mfma_f32_16x16x16_bf16 a[240:243], v[140:141], v[216:217], 0\n"
    "s_add_u32 s60, s60, 0x8000\n"
    "v_mfma_f32_16x16x16_bf16 v[4:7], v[142:143], v[216:217], 0\n"
    "buffer_load_dwordx4 v250, s[8:11], s60 offen offset:0 sc1 lds\n"
    "v_mfma_f32_16x16x16_bf16 v[24:27], v[144:145], v[216:217], 0\n"
    "buffer_load_dwordx4 v250, s[8:11], s60 offen offset:1024 sc1 lds\n"
    "v_mfma_f32_16x16x16_bf16 v[44:47], v[146:147], v[216:217], 0\n"
    "v_mfma_f32_16x16x16_bf16 v[64:67], v[148:149], v[216:217], 0\n"
    "buffer_load_dword v253, v250, s[8:11], s59 offen offset:0 sc1\n"
    "v_mfma_f32_16x16x16_bf16 v[84:87], v[150:151], v[216:217], 0\n"
    "v_mfma_f32_16x16x16_bf16 v[104:107], v[152:153], v[216:217], 0\n"
    "v_mfma_f32_16x16x16_bf16 v[124:127], v[154:155], v[216:217], 0\n"
    "s_waitcnt lgkmcnt(0)\n"
    "buffer_load_dword v254, v252, s[8:11], s59 offen offset:0 sc1\n"
    "v_mfma_f32_16x16x32_bf16 v[140:143], a[160:163], a[0:3], v[220:223]\n"
    "v_mfma_f32_16x16x32_bf16 v[140:143], a[164:167], a[4:7], v[140:143]\n"
    "v_mfma_f32_16x16x32_bf16 v[140:143], a[168:171], a[8:11], v[140:143]\n"
    "v_mfma_f32_16x16x32_bf16 v[140:143], a[172:175], a[12:15], v[140:143]\n"
    "v_mfma_f32_16x16x32_bf16 v[140:143], a[176:179], a[16:19], v[140:143]\n"
    "v_mfma_f32_16x16x32_bf16 v[140:143], a[180:183], a[20:23], v[140:143]\n"
    "v_mfma_f32_16x16x32_bf16 v[140:143], a[184:187], a[24:27], v[140:143]\n"
    "v_mfma_f32_16x16x32_bf16 v[140:143], a[188:191], a[28:31], v[140:143]\n"
    "v_mfma_f32_16x16x32_bf16 v[144:147], v[180:183], a[0:3], v[220:223]\n"
    "v_mfma_f32_16x16x32_bf16 v[144:147], v[184:187], a[4:7], v[144:147]\n"
    "s_waitcnt vmcnt(10)\n"
    "s_barrier\n"
    "v_mfma_f32_16x16x32_bf16 v[144:147], v[188:191], a[8:11], v[144:147]\n"
    "s_mov_b32 m0, s42\n"
    "ds_read_b128 a[32:35], v255 offset:0\n"
    "v_exp_f32_e32 v140, v140\n"
    "v_mfma_f32_16x16x32_bf16 v[144:147], v[192:195], a[12:15], v[144:147]\n"
    "ds_read_b128 a[36:39], v255 offset:1024\n"
    "v_exp_f32_e32 v141, v141\n"
    "v_mfma_f32_16x16x32_bf16 v[144:147], v[196:199], a[16:19], v[144:147]\n"
    "ds_read_b128 a[40:43], v255 offset:2048\n"
    "ds_read_b128 a[44:47], v255 offset:3072\n"
    "v_mfma_f32_16x16x32_bf16 v[144:147], v[200:203], a[20:23], v[144:147]\n"
    "ds_read_b128 a[48:51], v255 offset:4096\n"
    "ds_read_b128 a[52:55], v255 offset:5120\n"
    "v_mfma_f32_16x16x32_bf16 v[144:147], v[204:207], a[24:27], v[144:147]\n"
    "ds_read_b128 a[56:59], v255 offset:6144\n"
    "ds_read_b128 a[60:63], v255 offset:7168\n"
    "v_mfma_f32_16x16x32_bf16 v[144:147], v[208:211], a[28:31], v[144:147]\n"
    "s_waitcnt lgkmcnt(4)\n"
    "v_exp_f32_e32 v142, v142\n"
    "v_mfma_f32_16x16x32_bf16 v[148:151], a[160:163], a[32:35], v[224:227]\n"
    "ds_read_b128 a[64:67], v255 offset:8192\n"
    "v_exp_f32_e32 v143, v143\n"
    "v_mfma_f32_16x16x32_bf16 v[148:151], a[164:167], a[36:39], v[148:151]\n"
    "ds_read_b128 a[68:71], v255 offset:9216\n"
    "v_cvt_pk_bf16_f32 v140, v140, v141\n"
    "v_mfma_f32_16x16x32_bf16 v[148:151], a[168:171], a[40:43], v[148:151]\n"
    "ds_read_b128 a[72:75], v255 offset:10240\n"
    "v_cvt_pk_bf16_f32 v141, v142, v143\n"
    "v_mfma_f32_16x16x32_bf16 v[148:151], a[172:175], a[44:47], v[148:151]\n"
    "s_waitcnt lgkmcnt(3)\n"
    "v_exp_f32_e32 v144, v144\n"
    "v_mfma_f32_16x16x32_bf16 v[148:151], a[176:179], a[48:51], v[148:151]\n"
    "ds_read_b128 a[76:79], v255 offset:11264\n"
    "ds_write_addtid_b32 v140 offset:0\n"
    "v_mfma_f32_16x16x32_bf16 v[148:151], a[180:183], a[52:55], v[148:151]\n"
    "ds_read_b128 a[80:83], v255 offset:12288\n"
    "v_exp_f32_e32 v145, v145\n"
    "v_mfma_f32_16x16x32_bf16 v[148:151], a[184:187], a[56:59], v[148:151]\n"
    "v_exp_f32_e32 v146, v146\n"
    "v_mfma_f32_16x16x32_bf16 v[148:151], a[188:191], a[60:63], v[148:151]\n"
    "ds_read_b128 a[84:87], v255 offset:13312\n"
    "v_exp_f32_e32 v147, v147\n"
    "v_mfma_f32_16x16x32_bf16 v[152:155], v[180:183], a[32:35], v[224:227]\n"
    "ds_read_b128 a[88:91], v255 offset:14336\n"
    "v_cvt_pk_bf16_f32 v142, v144, v145\n"
    "v_mfma_f32_16x16x32_bf16 v[152:155], v[184:187], a[36:39], v[152:155]\n"
    "ds_read_b128 a[92:95], v255 offset:15360\n"
    "v_cvt_pk_bf16_f32 v143, v146, v147\n"
    "v_mfma_f32_16x16x32_bf16 v[152:155], v[188:191], a[40:43], v[152:155]\n"
    "v_exp_f32_e32 v148, v148\n"
    "v_mfma_f32_16x16x32_bf16 v[152:155], v[192:195], a[44:47], v[152:155]\n"
    "v_exp_f32_e32 v149, v149\n"
    "v_mfma_f32_16x16x32_bf16 v[152:155], v[196:199], a[48:51], v[152:155]\n"
    "v_exp_f32_e32 v150, v150\n"
    "v_mfma_f32_16x16x32_bf16 v[152:155], v[200:203], a[52:55], v[152:155]\n"
    "v_exp_f32_e32 v151, v151\n"
    "v_mfma_f32_16x16x32_bf16 v[152:155], v[204:207], a[56:59], v[152:155]\n"
    "s_waitcnt vmcnt(2)\n"
    "s_barrier\n"
    "v_mfma_f32_16x16x32_bf16 v[152:155], v[208:211], a[60:63], v[152:155]\n"
    "s_waitcnt lgkmcnt(5)\n"
    "ds_read_b128 a[96:99], v255 offset:32768\n"
    "ds_write_addtid_b32 v141 offset:256\n"
    "v_mfma_f32_16x16x32_bf16 v[156:159], a[160:163], a[64:67], v[228:231]\n"
    "ds_read_b128 a[100:103], v255 offset:33792\n"
    "ds_write_addtid_b32 v142 offset:512\n"
    "v_mfma_f32_16x16x32_bf16 v[156:159], a[164:167], a[68:71], v[156:159]\n"
    "ds_read_b128 a[104:107], v255 offset:34816\n"
    "v_mfma_f32_16x16x32_bf16 v[156:159], a[168:171], a[72:75], v[156:159]\n"
    "ds_read_b128 a[108:111], v255 offset:35840\n"
    "v_mfma_f32_16x16x32_bf16 v[156:159], a[172:175], a[76:79], v[156:159]\n"
    "s_waitcnt lgkmcnt(6)\n"
    "ds_read_b128 a[112:115], v255 offset:36864\n"
    "v_cvt_pk_bf16_f32 v148, v148, v149\n"
    "v_mfma_f32_16x16x32_bf16 v[156:159], a[176:179], a[80:83], v[156:159]\n"
    "ds_read_b128 a[116:119], v255 offset:37888\n"
    "v_cvt_pk_bf16_f32 v149, v150, v151\n"
    "v_mfma_f32_16x16x32_bf16 v[156:159], a[180:183], a[84:87], v[156:159]\n"
    "ds_read_b128 a[120:123], v255 offset:38912\n"
    "v_exp_f32_e32 v152, v152\n"
    "v_mfma_f32_16x16x32_bf16 v[156:159], a[184:187], a[88:91], v[156:159]\n"
    "ds_read_b128 a[124:127], v255 offset:39936\n"
    "v_exp_f32_e32 v153, v153\n"
    "v_mfma_f32_16x16x32_bf16 v[156:159], a[188:191], a[92:95], v[156:159]\n"
    "ds_read_b128 a[128:131], v255 offset:40960\n"
    "v_exp_f32_e32 v154, v154\n"
    "v_mfma_f32_16x16x32_bf16 v[160:163], v[180:183], a[64:67], v[228:231]\n"
    "ds_read_b128 a[132:135], v255 offset:41984\n"
    "v_exp_f32_e32 v155, v155\n"
    "v_mfma_f32_16x16x32_bf16 v[160:163], v[184:187], a[68:71], v[160:163]\n"
    "ds_read_b128 a[136:139], v255 offset:43008\n"
    "v_cvt_pk_bf16_f32 v150, v152, v153\n"
    "v_mfma_f32_16x16x32_bf16 v[160:163], v[188:191], a[72:75], v[160:163]\n"
    "ds_read_b128 a[140:143], v255 offset:44032\n"
    "v_cvt_pk_bf16_f32 v151, v154, v155\n"
    "v_mfma_f32_16x16x32_bf16 v[160:163], v[192:195], a[76:79], v[160:163]\n"
    "ds_read_b128 a[144:147], v255 offset:45056\n"
    "ds_read_b128 a[148:151], v255 offset:46080\n"
    "v_mfma_f32_16x16x32_bf16 v[160:163], v[196:199], a[80:83], v[160:163]\n"
    "ds_read_b128 a[152:155], v255 offset:47104\n"
    "ds_read_b128 a[156:159], v255 offset:48128\n"
    "v_mfma_f32_16x16x32_bf16 v[160:163], v[200:203], a[84:87], v[160:163]\n"
    "s_waitcnt vmcnt(0)\n"
    "ds_write_addtid_b32 v148 offset:1024\n"
    "v_xor_b32_e32 v254, s39, v254\n"
    "v_xor_b32_e32 v253, s39, v253\n"
    "v_mfma_f32_16x16x32_bf16 v[160:163], v[204:207], a[88:91], v[160:163]\n"
    "ds_write_addtid_b32 v149 offset:1280\n"
    "v_or_b32_e32 v253, v253, v254\n"
    "v_cmp_ne_u32_e32 vcc, 0, v253\n"
    "v_mfma_f32_16x16x32_bf16 v[160:163], v[208:211], a[92:95], v[160:163]\n"
    "s_waitcnt lgkmcnt(14)\n"
    "v_exp_f32_e32 v156, v156\n"
    "v_mfma_f32_16x16x32_bf16 v[164:167], a[160:163], a[96:99], v[232:235]\n"
    "v_exp_f32_e32 v157, v157\n"
    "v_mfma_f32_16x16x32_bf16 v[164:167], a[164:167], a[100:103], v[164:167]\n"
    "v_exp_f32_e32 v158, v158\n"
    "v_mfma_f32_16x16x32_bf16 v[164:167], a[168:171], a[104:107], v[164:167]\n"
    "s_cbranch_vccnz .Lat_hpoll\n"
    ".Lat_hpoll_ret:\n"
    "s_waitcnt lgkmcnt(2)\n"
    "s_waitcnt vmcnt(17)\n"
    "s_barrier\n"
    "v_mfma_f32_16x16x32_bf16 v[164:167], a[172:175], a[108:111], v[164:167]\n"
    "v_exp_f32_e32 v159, v159\n"
    "v_mfma_f32_16x16x32_bf16 v[164:167], a[176:179], a[112:115], v[164:167]\n"
    "s_add_u32 m0, s43, 16384\n"
    "v_cvt_pk_bf16_f32 v156, v156, v157\n"
    "v_mfma_f32_16x16x32_bf16 v[164:167], a[180:183], a[116:119], v[164:167]\n"
    "buffer_load_dwordx4 v248, s[8:11], s40 offen offset:0 sc1 lds\n"
    "v_cvt_pk_bf16_f32 v157, v158, v159\n"
    "v_mfma_f32_16x16x32_bf16 v[164:167], a[184:187], a[120:123], v[164:167]\n"
    "v_exp_f32_e32 v160, v160\n"
    "v_mfma_f32_16x16x32_bf16 v[164:167], a[188:191], a[124:127], v[164:167]\n"
    "v_exp_f32_e32 v161, v161\n"
    "v_mfma_f32_16x16x32_bf16 v[168:171], v[180:183], a[96:99], v[232:235]\n"
    "buffer_load_dwordx4 v248, s[8:11], s40 offen offset:1024 sc1 lds\n"
    "v_mfma_f32_16x16x32_bf16 v[168:171], v[184:187], a[100:103], v[168:171]\n"
    "v_exp_f32_e32 v162, v162\n"
    "v_mfma_f32_16x16x32_bf16 v[168:171], v[188:191], a[104:107], v[168:171]\n"
    "v_exp_f32_e32 v163, v163\n"
    "v_mfma_f32_16x16x32_bf16 v[168:171], v[192:195], a[108:111], v[168:171]\n"
    "buffer_load_dwordx4 v248, s[8:11], s40 offen offset:2048 sc1 lds\n"
    "v_mfma_f32_16x16x32_bf16 v[168:171], v[196:199], a[112:115], v[168:171]\n"
    "v_cvt_pk_bf16_f32 v158, v160, v161\n"
    "v_cvt_pk_bf16_f32 v159, v162, v163\n"
    "v_mfma_f32_16x16x32_bf16 v[168:171], v[200:203], a[116:119], v[168:171]\n"
    "v_exp_f32_e32 v164, v164\n"
    "v_mfma_f32_16x16x32_bf16 v[168:171], v[204:207], a[120:123], v[168:171]\n"
    "buffer_load_dwordx4 v248, s[8:11], s40 offen offset:3072 sc1 lds\n"
    "v_mfma_f32_16x16x32_bf16 v[168:171], v[208:211], a[124:127], v[168:171]\n"
    "s_add_u32 s40, s40, 0x8000\n"
    "v_exp_f32_e32 v165, v165\n"
    "v_mfma_f32_16x16x32_bf16 v[172:175], a[160:163], a[128:131], v[236:239]\n"
    "s_add_u32 m0, s43, 20480\n"
    "v_exp_f32_e32 v166, v166\n"
    "v_mfma_f32_16x16x32_bf16 v[172:175], a[164:167], a[132:135], v[172:175]\n"
    "buffer_load_dwordx4 v248, s[8:11], s41 offen offset:0 sc1 lds\n"
    "ds_read_b128 a[160:163], v240 offset:8192\n"
    "v_mfma_f32_16x16x32_bf16 v[172:175], a[168:171], a[136:139], v[172:175]\n"
    "ds_read_b128 a[164:167], v241 offset:8192\n"
    "v_mfma_f32_16x16x32_bf16 v[172:175], a[172:175], a[140:143], v[172:175]\n"
    "ds_read_b128 a[168:171], v240 offset:12288\n"
    "v_mfma_f32_16x16x32_bf16 v[172:175], a[176:179], a[144:147], v[172:175]\n"
    "buffer_load_dwordx4 v248, s[8:11], s41 offen offset:1024 sc1 lds\n"
    "ds_read_b128 a[172:175], v241 offset:12288\n"
    "v_mfma_f32_16x16x32_bf16 v[172:175], a[180:183], a[148:151], v[172:175]\n"
    "ds_read_b128 a[176:179], v242 offset:8192\n"
    "v_exp_f32_e32 v167, v167\n"
    "v_mfma_f32_16x16x32_bf16 v[172:175], a[184:187], a[152:155], v[172:175]\n"
    "ds_read_b128 a[180:183], v243 offset:8192\n"
    "v_cvt_pk_bf16_f32 v164, v164, v165\n"
    "v_mfma_f32_16x16x32_bf16 v[172:175], a[188:191], a[156:159], v[172:175]\n"
    "buffer_load_dwordx4 v248, s[8:11], s41 offen offset:2048 sc1 lds\n"
    "v_cvt_pk_bf16_f32 v165, v166, v167\n"
    "v_mfma_f32_16x16x32_bf16 v[176:179], v[180:183], a[128:131], v[236:239]\n"
    "v_exp_f32_e32 v168, v168\n"
    "v_mfma_f32_16x16x32_bf16 v[176:179], v[184:187], a[132:135], v[176:179]\n"
    "v_exp_f32_e32 v169, v169\n"
    "v_mfma_f32_16x16x32_bf16 v[176:179], v[188:191], a[136:139], v[176:179]\n"
    "buffer_load_dwordx4 v248, s[8:11], s41 offen offset:3072 sc1 lds\n"
    "v_mfma_f32_16x16x32_bf16 v[176:179], v[192:195], a[140:143], v[176:179]\n"
    "s_mov_b32 m0, s42\n"
    "s_add_u32 s41, s41, 0x8000\n"
    "v_exp_f32_e32 v172, v172\n"
    "v_mfma_f32_16x16x32_bf16 v[176:179], v[196:199], a[144:147], v[176:179]\n"
    "ds_write_addtid_b32 v156 offset:2048\n"
    "v_exp_f32_e32 v173, v173\n"
    "v_mfma_f32_16x16x32_bf16 v[176:179], v[200:203], a[148:151], v[176:179]\n"
    "ds_write_addtid_b32 v157 offset:2304\n"
    "v_exp_f32_e32 v174, v174\n"
    "v_mfma_f32_16x16x32_bf16 v[176:179], v[204:207], a[152:155], v[176:179]\n"
    "ds_write_addtid_b32 v164 offset:3072\n"
    "v_exp_f32_e32 v175, v175\n"
    "v_mfma_f32_16x16x32_bf16 v[176:179], v[208:211], a[156:159], v[176:179]\n"
    "ds_write_addtid_b32 v165 offset:3328\n"
    "v_cvt_pk_bf16_f32 v172, v172, v173\n"
    "v_cvt_pk_bf16_f32 v173, v174, v175\n"
    "ds_write_addtid_b32 v172 offset:4096\n"
    "ds_write_addtid_b32 v173 offset:4352\n"
    "s_mul_i32 s45, s33, 320\n"
    "s_lshl_b32 s46, s93, 7\n"
    "s_add_u32 s45, s45, s46\n"
    "buffer_load_dword v254, off, s[24:27], s45\n"
    "s_andn2_b32 s45, s34, 3\n"
    "s_add_u32 s45, s45, s93\n"
    "s_mul_i32 s45, s45, 101376\n"
    "s_add_u32 s45, s45, 16588800\n"
    "buffer_load_dword v254, off, s[8:11], s45\n"
    "s_waitcnt lgkmcnt(10)\n"
    "s_branch .Lat_in\n"
    ".fill 236, 4, 0xbf800000\n"
    ".long 0xbedf00ff\n"
    ".Lat_w2:\n"
    "s_setpc_b64 s[2:3]\n"
    ".Lat_loop:\n"
    "v_mfma_f32_16x16x32_bf16 v[212:215], a[160:163], a[128:131], v[236:239]\n"
    "v_mfma_f32_16x16x32_bf16 v[212:215], a[164:167], a[132:135], v[212:215]\n"
    "v_exp_f32_e32 v170, v170\n"
    "v_mfma_f32_16x16x32_bf16 v[204:207], a[164:167], a[100:103], v[232:235]\n"
    "ds_write_addtid_b32 v143 offset:768\n"
    "v_mfma_f32_16x16x32_bf16 v[204:207], a[160:163], a[96:99], v[204:207]\n"
    "ds_read_b128 a[184:187], v242 offset:28672\n"
    "v_mfma_f32_16x16x32_bf16 v[196:199], a[160:163], a[64:67], v[228:231]\n"
    "v_exp_f32_e32 v171, v171\n"
    "v_mfma_f32_16x16x32_bf16 v[196:199], a[164:167], a[68:71], v[196:199]\n"
    "ds_write_addtid_b32 v150 offset:1536\n"
    "v_mfma_f32_16x16x32_bf16 v[188:191], a[164:167], a[36:39], v[224:227]\n"
    "v_cvt_pk_bf16_f32 v166, v168, v169\n"
    "v_mfma_f32_16x16x32_bf16 v[188:191], a[160:163], a[32:35], v[188:191]\n"
    "v_mfma_f32_16x16x32_bf16 v[180:183], a[160:163], a[0:3], v[220:223]\n"
    "ds_write_addtid_b32 v151 offset:1792\n"
    "v_mfma_f32_16x16x32_bf16 v[180:183], a[164:167], a[4:7], v[180:183]\n"
    "s_waitcnt lgkmcnt(10)\n"
    "v_exp_f32_e32 v176, v176\n"
    "v_mfma_f32_16x16x32_bf16 v[180:183], a[168:171], a[8:11], v[180:183]\n"
    "ds_read_b128 a[188:191], v243 offset:28672\n"
    "v_mfma_f32_16x16x32_bf16 v[180:183], a[172:175], a[12:15], v[180:183]\n"
    "v_exp_f32_e32 v177, v177\n"
    "v_mfma_f32_16x16x32_bf16 v[188:191], a[172:175], a[44:47], v[188:191]\n"
    "ds_write_addtid_b32 v158 offset:2560\n"
    "v_mfma_f32_16x16x32_bf16 v[188:191], a[168:171], a[40:43], v[188:191]\n"
    "ds_read_b128 a[160:163], v240 offset:26624\n"
    "v_mfma_f32_16x16x32_bf16 v[196:199], a[168:171], a[72:75], v[196:199]\n"
    "v_cvt_pk_bf16_f32 v167, v170, v171\n"
    "v_mfma_f32_16x16x32_bf16 v[196:199], a[172:175], a[76:79], v[196:199]\n"
    "ds_write_addtid_b32 v159 offset:2816\n"
    "v_mfma_f32_16x16x32_bf16 v[204:207], a[172:175], a[108:111], v[204:207]\n"
    "v_exp_f32_e32 v178, v178\n"
    "v_mfma_f32_16x16x32_bf16 v[204:207], a[168:171], a[104:107], v[204:207]\n"
    "ds_write_addtid_b32 v166 offset:3584\n"
    "v_mfma_f32_16x16x32_bf16 v[212:215], a[168:171], a[136:139], v[212:215]\n"
    "v_mfma_f32_16x16x32_bf16 v[212:215], a[172:175], a[140:143], v[212:215]\n"
    "s_waitcnt lgkmcnt(10)\n"
    "v_exp_f32_e32 v179, v179\n"
    "v_mfma_f32_16x16x32_bf16 v[212:215], a[176:179], a[144:147], v[212:215]\n"
    "ds_read_b128 a[164:167], v241 offset:26624\n"
    "v_mfma_f32_16x16x32_bf16 v[212:215], a[180:183], a[148:151], v[212:215]\n"
    "v_cvt_pk_bf16_f32 v174, v176, v177\n"
    "v_mfma_f32_16x16x32_bf16 v[204:207], a[180:183], a[116:119], v[204:207]\n"
    "ds_write_addtid_b32 v167 offset:3840\n"
    "v_mfma_f32_16x16x32_bf16 v[204:207], a[176:179], a[112:115], v[204:207]\n"
    "ds_read_b128 a[168:171], v240 offset:30720\n"
    "v_mfma_f32_16x16x32_bf16 v[196:199], a[176:179], a[80:83], v[196:199]\n"
    "v_cvt_pk_bf16_f32 v175, v178, v179\n"
    "v_mfma_f32_16x16x32_bf16 v[196:199], a[180:183], a[84:87], v[196:199]\n"
    "ds_write_addtid_b32 v174 offset:4608\n"
    "v_mfma_f32_16x16x32_bf16 v[188:191], a[180:183], a[52:55], v[188:191]\n"
    "ds_write_addtid_b32 v175 offset:4864\n"
    "v_mfma_f32_16x16x32_bf16 v[188:191], a[176:179], a[48:51], v[188:191]\n"
    "v_mfma_f32_16x16x32_bf16 v[180:183], a[176:179], a[16:19], v[180:183]\n"
    "ds_read_b128 a[172:175], v241 offset:30720\n"
    "v_mfma_f32_16x16x32_bf16 v[180:183], a[180:183], a[20:23], v[180:183]\n"
    "s_waitcnt lgkmcnt(10)\n"
    "s_waitcnt lgkmcnt(1)\n"
    "v_mfma_f32_16x16x32_bf16 v[180:183], a[184:187], a[24:27], v[180:183]\n"
    "s_barrier\n"
    "v_mfma_f32_16x16x32_bf16 v[180:183], a[188:191], a[28:31], v[180:183]\n"
    "s_add_u32 m0, s43, 8192\n"
    "v_mfma_f32_16x16x32_bf16 v[188:191], a[188:191], a[60:63], v[188:191]\n"
    "ds_read_b128 a[176:179], v242 offset:26624\n"
    "v_mfma_f32_16x16x32_bf16 v[188:191], a[184:187], a[56:59], v[188:191]\n"
    "buffer_load_dwordx4 v248, s[8:11], s40 offen offset:0 sc1 lds\n"
    "v_mfma_f32_16x16x32_bf16 v[196:199], a[184:187], a[88:91], v[196:199]\n"
    "ds_read2st64_b32 v[144:145], v249 offset0:0 offset1:1\n"
    "v_mfma_f32_16x16x32_bf16 v[196:199], a[188:191], a[92:95], v[196:199]\n"
    "v_mfma_f32_16x16x32_bf16 v[204:207], a[188:191], a[124:127], v[204:207]\n"
    "ds_read2st64_b32 v[146:147], v249 offset0:2 offset1:3\n"
    "v_mfma_f32_16x16x32_bf16 v[204:207], a[184:187], a[120:123], v[204:207]\n"
    "buffer_load_dwordx4 v248, s[8:11], s40 offen offset:1024 sc1 lds\n"
    "v_mfma_f32_16x16x32_bf16 v[212:215], a[184:187], a[152:155], v[212:215]\n"
    "v_mfma_f32_16x16x32_bf16 v[212:215], a[188:191], a[156:159], v[212:215]\n"
    "v_mfma_f32_16x16x32_bf16 v[216:219], a[160:163], a[128:131], v[236:239]\n"
    "ds_read_b128 a[180:183], v243 offset:26624\n"
    "v_mfma_f32_16x16x32_bf16 v[216:219], a[164:167], a[132:135], v[216:219]\n"
    "buffer_load_dwordx4 v248, s[8:11], s40 offen offset:2048 sc1 lds\n"
    "v_mfma_f32_16x16x32_bf16 v[208:211], a[164:167], a[100:103], v[232:235]\n"
    "ds_read2st64_b32 v[152:153], v249 offset0:4 offset1:5\n"
    "v_mfma_f32_16x16x32_bf16 v[208:211], a[160:163], a[96:99], v[208:211]\n"
    "ds_read_b128 a[184:187], v242 offset:30720\n"
    "v_mfma_f32_16x16x32_bf16 v[200:203], a[160:163], a[64:67], v[228:231]\n"
    "ds_read2st64_b32 v[154:155], v249 offset0:6 offset1:7\n"
    "v_mfma_f32_16x16x32_bf16 v[200:203], a[164:167], a[68:71], v[200:203]\n"
    "buffer_load_dwordx4 v248, s[8:11], s40 offen offset:3072 sc1 lds\n"
    "v_mfma_f32_16x16x32_bf16 v[192:195], a[164:167], a[36:39], v[224:227]\n"
    "s_add_u32 m0, s43, 12288\n"
    "v_exp_f32_e32 v180, v180\n"
    "v_mfma_f32_16x16x32_bf16 v[192:195], a[160:163], a[32:35], v[192:195]\n"
    "s_add_u32 s40, s40, 0x8000\n"
    "v_exp_f32_e32 v181, v181\n"
    "v_mfma_f32_16x16x32_bf16 v[184:187], a[160:163], a[0:3], v[220:223]\n"
    "ds_read2st64_b32 v[160:161], v249 offset0:8 offset1:9\n"
    "v_mfma_f32_16x16x32_bf16 v[184:187], a[164:167], a[4:7], v[184:187]\n"
    "s_waitcnt lgkmcnt(8)\n"
    "buffer_load_dwordx4 v248, s[8:11], s41 offen offset:0 sc1 lds\n"
    "v_mfma_f32_16x16x32_bf16 v[184:187], a[168:171], a[8:11], v[184:187]\n"
    ".long 0xbedf00ff\n"
    ".Lat_w3:\n"
    "s_setpc_b64 s[2:3]\n"
    "ds_read_b128 a[188:191], v243 offset:30720\n"
    "v_mfma_f32_16x16x32_bf16 v[184:187], a[172:175], a[12:15], v[184:187]\n"
    "ds_read_b64_tr_b16 a[160:161], v244 offset:16384\n"
    "v_mfma_f32_16x16x32_bf16 v[192:195], a[172:175], a[44:47], v[192:195]\n"
    "ds_read2st64_b32 v[162:163], v249 offset0:10 offset1:11\n"
    "v_mfma_f32_16x16x32_bf16 v[192:195], a[168:171], a[40:43], v[192:195]\n"
    "buffer_load_dwordx4 v248, s[8:11], s41 offen offset:1024 sc1 lds\n"
    "v_mfma_f32_16x16x32_bf16 v[200:203], a[168:171], a[72:75], v[200:203]\n"
    "ds_read_b64_tr_b16 a[162:163], v244 offset:18432\n"
    "v_mfma_f32_16x16x32_bf16 v[200:203], a[172:175], a[76:79], v[200:203]\n"
    "v_exp_f32_e32 v182, v182\n"
    "v_mfma_f32_16x16x32_bf16 v[208:211], a[172:175], a[108:111], v[208:211]\n"
    "ds_read2st64_b32 v[168:169], v249 offset0:12 offset1:13\n"
    "v_mfma_f32_16x16x32_bf16 v[208:211], a[168:171], a[104:107], v[208:211]\n"
    "buffer_load_dwordx4 v248, s[8:11], s41 offen offset:2048 sc1 lds\n"
    "v_mfma_f32_16x16x32_bf16 v[216:219], a[168:171], a[136:139], v[216:219]\n"
    "ds_read_b64_tr_b16 a[164:165], v244 offset:49152\n"
    "v_mfma_f32_16x16x32_bf16 v[216:219], a[172:175], a[140:143], v[216:219]\n"
    "s_waitcnt lgkmcnt(10)\n"
    "ds_read2st64_b32 v[170:171], v249 offset0:14 offset1:15\n"
    "v_mfma_f32_16x16x32_bf16 v[216:219], a[176:179], a[144:147], v[216:219]\n"
    "ds_read_b64_tr_b16 a[168:169], v245 offset:16384\n"
    "v_mfma_f32_16x16x32_bf16 v[216:219], a[180:183], a[148:151], v[216:219]\n"
    "buffer_load_dwordx4 v248, s[8:11], s41 offen offset:3072 sc1 lds\n"
    "v_mfma_f32_16x16x32_bf16 v[208:211], a[180:183], a[116:119], v[208:211]\n"
    "ds_read_b64_tr_b16 a[166:167], v244 offset:51200\n"
    "v_mfma_f32_16x16x32_bf16 v[208:211], a[176:179], a[112:115], v[208:211]\n"
    "s_add_u32 s41, s41, 0x8000\n"
    "v_exp_f32_e32 v183, v183\n"
    "v_mfma_f32_16x16x32_bf16 v[200:203], a[176:179], a[80:83], v[200:203]\n"
    "ds_read_b64_tr_b16 a[170:171], v245 offset:18432\n"
    "v_cvt_pk_bf16_f32 v180, v180, v181\n"
    "v_mfma_f32_16x16x32_bf16 v[200:203], a[180:183], a[84:87], v[200:203]\n"
    "ds_read2st64_b32 v[176:177], v249 offset0:16 offset1:17\n"
    "v_mfma_f32_16x16x32_bf16 v[192:195], a[180:183], a[52:55], v[192:195]\n"
    "v_exp_f32_e32 v188, v188\n"
    "v_mfma_f32_16x16x32_bf16 v[192:195], a[176:179], a[48:51], v[192:195]\n"
    "v_exp_f32_e32 v189, v189\n"
    "v_mfma_f32_16x16x32_bf16 v[184:187], a[176:179], a[16:19], v[184:187]\n"
    "ds_read_b64_tr_b16 a[172:173], v245 offset:49152\n"
    "v_cvt_pk_bf16_f32 v181, v182, v183\n"
    "v_mfma_f32_16x16x32_bf16 v[184:187], a[180:183], a[20:23], v[184:187]\n"
    "s_waitcnt lgkmcnt(11)\n"
    "ds_read2st64_b32 v[178:179], v249 offset0:18 offset1:19\n"
    "v_mfma_f32_16x16x32_bf16 v[184:187], a[184:187], a[24:27], v[184:187]\n"
    "ds_read_b64_tr_b16 a[176:177], v246 offset:16384\n"
    "v_mfma_f32_16x16x32_bf16 v[184:187], a[188:191], a[28:31], v[184:187]\n"
    "ds_read_b64_tr_b16 a[174:175], v245 offset:51200\n"
    "v_mfma_f32_16x16x32_bf16 v[192:195], a[188:191], a[60:63], v[192:195]\n"
    "v_exp_f32_e32 v190, v190\n"
    "v_mfma_f32_16x16x32_bf16 v[192:195], a[184:187], a[56:59], v[192:195]\n"
    "v_exp_f32_e32 v191, v191\n"
    "v_mfma_f32_16x16x32_bf16 v[200:203], a[184:187], a[88:91], v[200:203]\n"
    "v_mfma_f32_16x16x32_bf16 v[200:203], a[188:191], a[92:95], v[200:203]\n"
    "ds_read_b64_tr_b16 a[178:179], v246 offset:18432\n"
    "v_mfma_f32_16x16x32_bf16 v[208:211], a[188:191], a[124:127], v[208:211]\n"
    "v_cvt_pk_bf16_f32 v188, v188, v189\n"
    "v_mfma_f32_16x16x32_bf16 v[208:211], a[184:187], a[120:123], v[208:211]\n"
    "v_exp_f32_e32 v196, v196\n"
    "v_mfma_f32_16x16x32_bf16 v[216:219], a[184:187], a[152:155], v[216:219]\n"
    "ds_read_b64_tr_b16 a[180:181], v246 offset:49152\n"
    "v_mfma_f32_16x16x32_bf16 v[216:219], a[188:191], a[156:159], v[216:219]\n"
    "s_waitcnt lgkmcnt(4)\n"
    "v_exp_f32_e32 v197, v197\n"
    "v_mfma_f32_16x16x32_bf16 a[236:239], a[160:163], v[140:143], a[236:239]\n"
    "v_mfma_f32_16x16x32_bf16 a[236:239], a[164:167], v[144:147], a[236:239]\n"
    "ds_read_b64_tr_b16 a[182:183], v246 offset:51200\n"
    "v_mfma_f32_16x16x32_bf16 a[240:243], a[164:167], v[152:155], a[240:243]\n"
    "ds_read_b64_tr_b16 a[184:185], v247 offset:16384\n"
    "v_mfma_f32_16x16x32_bf16 a[240:243], a[160:163], v[148:151], a[240:243]\n"
    "v_cvt_pk_bf16_f32 v189, v190, v191\n"
    "v_mfma_f32_16x16x32_bf16 a[244:247], a[160:163], v[156:159], a[244:247]\n"
    "ds_read_b64_tr_b16 a[186:187], v247 offset:18432\n"
    "v_mfma_f32_16x16x32_bf16 a[244:247], a[164:167], v[160:163], a[244:247]\n"
    "v_exp_f32_e32 v198, v198\n"
    "v_mfma_f32_16x16x32_bf16 a[248:251], a[164:167], v[168:171], a[248:251]\n"
    "v_exp_f32_e32 v199, v199\n"
    "v_mfma_f32_16x16x32_bf16 a[248:251], a[160:163], v[164:167], a[248:251]\n"
    "v_cvt_pk_bf16_f32 v196, v196, v197\n"
    "v_mfma_f32_16x16x32_bf16 a[252:255], a[160:163], v[172:175], a[252:255]\n"
    "ds_read_b64_tr_b16 a[188:189], v247 offset:49152\n"
    "v_mfma_f32_16x16x32_bf16 a[252:255], a[164:167], v[176:179], a[252:255]\n"
    "s_waitcnt lgkmcnt(6)\n"
    "v_exp_f32_e32 v204, v204\n"
    "v_mfma_f32_16x16x32_bf16 v[16:19], a[172:175], v[176:179], v[16:19]\n"
    "ds_read_b64_tr_b16 a[160:161], v244 offset:20480\n"
    "v_mfma_f32_16x16x32_bf16 v[16:19], a[168:171], v[172:175], v[16:19]\n"
    "ds_read_b64_tr_b16 a[190:191], v247 offset:51200\n"
    "v_mfma_f32_16x16x32_bf16 v[12:15], a[168:171], v[164:167], v[12:15]\n"
    "v_exp_f32_e32 v205, v205\n"
    "v_mfma_f32_16x16x32_bf16 v[12:15], a[172:175], v[168:171], v[12:15]\n"
    "v_cvt_pk_bf16_f32 v197, v198, v199\n"
    "v_mfma_f32_16x16x32_bf16 v[8:11], a[172:175], v[160:163], v[8:11]\n"
    "ds_read_b64_tr_b16 a[162:163], v244 offset:22528\n"
    "v_mfma_f32_16x16x32_bf16 v[8:11], a[168:171], v[156:159], v[8:11]\n"
    "v_exp_f32_e32 v206, v206\n"
    "v_mfma_f32_16x16x32_bf16 v[4:7], a[168:171], v[148:151], v[4:7]\n"
    "v_exp_f32_e32 v207, v207\n"
    "v_mfma_f32_16x16x32_bf16 v[4:7], a[172:175], v[152:155], v[4:7]\n"
    "v_cvt_pk_bf16_f32 v204, v204, v205\n"
    "v_mfma_f32_16x16x32_bf16 v[0:3], a[172:175], v[144:147], v[0:3]\n"
    "ds_read_b64_tr_b16 a[164:165], v244 offset:53248\n"
    "v_mfma_f32_16x16x32_bf16 v[0:3], a[168:171], v[140:143], v[0:3]\n"
    "s_waitcnt lgkmcnt(7)\n"
    "v_exp_f32_e32 v212, v212\n"
    "v_mfma_f32_16x16x32_bf16 v[20:23], a[176:179], v[140:143], v[20:23]\n"
    "v_mfma_f32_16x16x32_bf16 v[20:23], a[180:183], v[144:147], v[20:23]\n"
    "ds_read_b64_tr_b16 a[166:167], v244 offset:55296\n"
    "v_mfma_f32_16x16x32_bf16 v[24:27], a[180:183], v[152:155], v[24:27]\n"
    "ds_read_b64_tr_b16 a[168:169], v245 offset:20480\n"
    "v_mfma_f32_16x16x32_bf16 v[24:27], a[176:179], v[148:151], v[24:27]\n"
    "v_exp_f32_e32 v213, v213\n"
    "v_mfma_f32_16x16x32_bf16 v[28:31], a[176:179], v[156:159], v[28:31]\n"
    "ds_read_b64_tr_b16 a[170:171], v245 offset:22528\n"
    "v_mfma_f32_16x16x32_bf16 v[28:31], a[180:183], v[160:163], v[28:31]\n"
    "v_cvt_pk_bf16_f32 v205, v206, v207\n"
    "v_mfma_f32_16x16x32_bf16 v[32:35], a[180:183], v[168:171], v[32:35]\n"
    "v_exp_f32_e32 v214, v214\n"
    "v_mfma_f32_16x16x32_bf16 v[32:35], a[176:179], v[164:167], v[32:35]\n"
    "v_exp_f32_e32 v215, v215\n"
    "v_mfma_f32_16x16x32_bf16 v[36:39], a[176:179], v[172:175], v[36:39]\n"
    "ds_read_b64_tr_b16 a[172:173], v245 offset:53248\n"
    "v_mfma_f32_16x16x32_bf16 v[36:39], a[180:183], v[176:179], v[36:39]\n"
    "s_waitcnt lgkmcnt(6)\n"
    "v_cvt_pk_bf16_f32 v212, v212, v213\n"
    "v_mfma_f32_16x16x32_bf16 v[56:59], a[188:191], v[176:179], v[56:59]\n"
    "ds_read_b64_tr_b16 a[176:177], v246 offset:20480\n"
    "v_mfma_f32_16x16x32_bf16 v[56:59], a[184:187], v[172:175], v[56:59]\n"
    "ds_read_b64_tr_b16 a[174:175], v245 offset:55296\n"
    "v_mfma_f32_16x16x32_bf16 v[52:55], a[184:187], v[164:167], v[52:55]\n"
    "v_cvt_pk_bf16_f32 v213, v214, v215\n"
    "v_mfma_f32_16x16x32_bf16 v[52:55], a[188:191], v[168:171], v[52:55]\n"
    "v_exp_f32_e32 v184, v184\n"
    "v_mfma_f32_16x16x32_bf16 v[48:51], a[188:191], v[160:163], v[48:51]\n"
    "ds_read_b64_tr_b16 a[178:179], v246 offset:22528\n"
    "v_mfma_f32_16x16x32_bf16 v[48:51], a[184:187], v[156:159], v[48:51]\n"
    "v_mfma_f32_16x16x32_bf16 v[44:47], a[184:187], v[148:151], v[44:47]\n"
    "v_exp_f32_e32 v185, v185\n"
    "v_mfma_f32_16x16x32_bf16 v[44:47], a[188:191], v[152:155], v[44:47]\n"
    "v_exp_f32_e32 v186, v186\n"
    "v_mfma_f32_16x16x32_bf16 v[40:43], a[188:191], v[144:147], v[40:43]\n"
    "ds_read_b64_tr_b16 a[180:181], v246 offset:53248\n"
    "v_mfma_f32_16x16x32_bf16 v[40:43], a[184:187], v[140:143], v[40:43]\n"
    "s_waitcnt lgkmcnt(7)\n"
    "v_exp_f32_e32 v187, v187\n"
    "v_mfma_f32_16x16x32_bf16 v[60:63], a[160:163], v[140:143], v[60:63]\n"
    "ds_read_b64_tr_b16 a[184:185], v247 offset:20480\n"
    "v_mfma_f32_16x16x32_bf16 v[60:63], a[164:167], v[144:147], v[60:63]\n"
    "ds_read_b64_tr_b16 a[182:183], v246 offset:55296\n"
    "v_mfma_f32_16x16x32_bf16 v[64:67], a[164:167], v[152:155], v[64:67]\n"
    "ds_read_b64_tr_b16 a[186:187], v247 offset:22528\n"
    "v_mfma_f32_16x16x32_bf16 v[64:67], a[160:163], v[148:151], v[64:67]\n"
    "v_cvt_pk_bf16_f32 v182, v184, v185\n"
    "v_mfma_f32_16x16x32_bf16 v[68:71], a[160:163], v[156:159], v[68:71]\n"
    "v_exp_f32_e32 v192, v192\n"
    "v_mfma_f32_16x16x32_bf16 v[68:71], a[164:167], v[160:163], v[68:71]\n"
    "ds_read_b64_tr_b16 a[188:189], v247 offset:53248\n"
    "v_mfma_f32_16x16x32_bf16 v[72:75], a[164:167], v[168:171], v[72:75]\n"
    "ds_read_b64_tr_b16 a[190:191], v247 offset:55296\n"
    "v_mfma_f32_16x16x32_bf16 v[72:75], a[160:163], v[164:167], v[72:75]\n"
    "s_waitcnt vmcnt(9)\n"
    "v_mfma_f32_16x16x32_bf16 v[76:79], a[160:163], v[172:175], v[76:79]\n"
    "s_barrier\n"
    "v_mfma_f32_16x16x32_bf16 v[76:79], a[164:167], v[176:179], v[76:79]\n"
    "s_waitcnt lgkmcnt(7)\n"
    "s_mov_b32 m0, s42\n"
    "v_mfma_f32_16x16x32_bf16 v[96:99], a[172:175], v[176:179], v[96:99]\n"
    "v_exp_f32_e32 v193, v193\n"
    "v_mfma_f32_16x16x32_bf16 v[96:99], a[168:171], v[172:175], v[96:99]\n"
    "v_cvt_pk_bf16_f32 v183, v186, v187\n"
    "v_mfma_f32_16x16x32_bf16 v[92:95], a[168:171], v[164:167], v[92:95]\n"
    "v_exp_f32_e32 v194, v194\n"
    "v_mfma_f32_16x16x32_bf16 v[92:95], a[172:175], v[168:171], v[92:95]\n"
    "ds_read_b128 a[160:163], v240 offset:0\n"
    "v_mfma_f32_16x16x32_bf16 v[88:91], a[172:175], v[160:163], v[88:91]\n"
    "ds_write_addtid_b32 v180 offset:0\n"
    "v_mfma_f32_16x16x32_bf16 v[88:91], a[168:171], v[156:159], v[88:91]\n"
    "v_exp_f32_e32 v195, v195\n"
    "v_mfma_f32_16x16x32_bf16 v[84:87], a[168:171], v[148:151], v[84:87]\n"
    "ds_write_addtid_b32 v181 offset:256\n"
    "v_mfma_f32_16x16x32_bf16 v[84:87], a[172:175], v[152:155], v[84:87]\n"
    "ds_write_addtid_b32 v188 offset:1024\n"
    "v_mfma_f32_16x16x32_bf16 v[80:83], a[172:175], v[144:147], v[80:83]\n"
    "v_cvt_pk_bf16_f32 v190, v192, v193\n"
    "v_mfma_f32_16x16x32_bf16 v[80:83], a[168:171], v[140:143], v[80:83]\n"
    "s_waitcnt lgkmcnt(7)\n"
    "v_exp_f32_e32 v200, v200\n"
    "v_mfma_f32_16x16x32_bf16 v[100:103], a[176:179], v[140:143], v[100:103]\n"
    "ds_read_b128 a[164:167], v241 offset:0\n"
    "v_mfma_f32_16x16x32_bf16 v[100:103], a[180:183], v[144:147], v[100:103]\n"
    "ds_write_addtid_b32 v189 offset:1280\n"
    "v_mfma_f32_16x16x32_bf16 v[104:107], a[180:183], v[152:155], v[104:107]\n"
    "v_exp_f32_e32 v201, v201\n"
    "v_mfma_f32_16x16x32_bf16 v[104:107], a[176:179], v[148:151], v[104:107]\n"
    "ds_read_b128 a[168:171], v240 offset:4096\n"
    "v_mfma_f32_16x16x32_bf16 v[108:111], a[176:179], v[156:159], v[108:111]\n"
    "ds_write_addtid_b32 v196 offset:2048\n"
    "v_mfma_f32_16x16x32_bf16 v[108:111], a[180:183], v[160:163], v[108:111]\n"
    "v_cvt_pk_bf16_f32 v191, v194, v195\n"
    "v_mfma_f32_16x16x32_bf16 v[112:115], a[180:183], v[168:171], v[112:115]\n"
    "ds_write_addtid_b32 v197 offset:2304\n"
    "v_mfma_f32_16x16x32_bf16 v[112:115], a[176:179], v[164:167], v[112:115]\n"
    "v_exp_f32_e32 v202, v202\n"
    "v_mfma_f32_16x16x32_bf16 v[116:119], a[176:179], v[172:175], v[116:119]\n"
    "ds_write_addtid_b32 v204 offset:3072\n"
    "v_mfma_f32_16x16x32_bf16 v[116:119], a[180:183], v[176:179], v[116:119]\n"
    "s_waitcnt lgkmcnt(10)\n"
    "v_exp_f32_e32 v203, v203\n"
    "v_mfma_f32_16x16x32_bf16 v[136:139], a[188:191], v[176:179], v[136:139]\n"
    "ds_read_b128 a[172:175], v241 offset:4096\n"
    "v_mfma_f32_16x16x32_bf16 v[136:139], a[184:187], v[172:175], v[136:139]\n"
    "ds_write_addtid_b32 v205 offset:3328\n"
    "v_cvt_pk_bf16_f32 v198, v200, v201\n"
    "v_mfma_f32_16x16x32_bf16 v[132:135], a[184:187], v[164:167], v[132:135]\n"
    "ds_read_b128 a[176:179], v242 offset:0\n"
    "v_mfma_f32_16x16x32_bf16 v[132:135], a[188:191], v[168:171], v[132:135]\n"
    "ds_write_addtid_b32 v212 offset:4096\n"
    "v_mfma_f32_16x16x32_bf16 v[128:131], a[188:191], v[160:163], v[128:131]\n"
    "v_exp_f32_e32 v208, v208\n"
    "v_mfma_f32_16x16x32_bf16 v[128:131], a[184:187], v[156:159], v[128:131]\n"
    "ds_write_addtid_b32 v213 offset:4352\n"
    "v_mfma_f32_16x16x32_bf16 v[124:127], a[184:187], v[148:151], v[124:127]\n"
    "v_exp_f32_e32 v209, v209\n"
    "v_mfma_f32_16x16x32_bf16 v[124:127], a[188:191], v[152:155], v[124:127]\n"
    "ds_read_b128 a[180:183], v243 offset:0\n"
    "v_mfma_f32_16x16x32_bf16 v[120:123], a[188:191], v[144:147], v[120:123]\n"
    "ds_write_addtid_b32 v182 offset:512\n"
    "v_mfma_f32_16x16x32_bf16 v[120:123], a[184:187], v[140:143], v[120:123]\n"
    "s_waitcnt lgkmcnt(12)\n"
    "v_cvt_pk_bf16_f32 v199, v202, v203\n"
    "v_mfma_f32_16x16x32_bf16 v[172:175], a[160:163], a[128:131], v[236:239]\n"
    "v_mfma_f32_16x16x32_bf16 v[172:175], a[164:167], a[132:135], v[172:175]\n"
    "v_exp_f32_e32 v210, v210\n"
    "v_mfma_f32_16x16x32_bf16 v[164:167], a[164:167], a[100:103], v[232:235]\n"
    "ds_write_addtid_b32 v183 offset:768\n"
    "v_mfma_f32_16x16x32_bf16 v[164:167], a[160:163], a[96:99], v[164:167]\n"
    "ds_read_b128 a[184:187], v242 offset:4096\n"
    "v_mfma_f32_16x16x32_bf16 v[156:159], a[160:163], a[64:67], v[228:231]\n"
    "v_exp_f32_e32 v211, v211\n"
    "v_mfma_f32_16x16x32_bf16 v[156:159], a[164:167], a[68:71], v[156:159]\n"
    "ds_write_addtid_b32 v190 offset:1536\n"
    "v_mfma_f32_16x16x32_bf16 v[148:151], a[164:167], a[36:39], v[224:227]\n"
    "v_cvt_pk_bf16_f32 v206, v208, v209\n"
    "v_mfma_f32_16x16x32_bf16 v[148:151], a[160:163], a[32:35], v[148:151]\n"
    "v_mfma_f32_16x16x32_bf16 v[140:143], a[160:163], a[0:3], v[220:223]\n"
    "ds_write_addtid_b32 v191 offset:1792\n"
    "v_mfma_f32_16x16x32_bf16 v[140:143], a[164:167], a[4:7], v[140:143]\n"
    "s_waitcnt lgkmcnt(10)\n"
    "v_exp_f32_e32 v216, v216\n"
    "v_mfma_f32_16x16x32_bf16 v[140:143], a[168:171], a[8:11], v[140:143]\n"
    "ds_read_b128 a[188:191], v243 offset:4096\n"
    "v_mfma_f32_16x16x32_bf16 v[140:143], a[172:175], a[12:15], v[140:143]\n"
    "v_exp_f32_e32 v217, v217\n"
    "v_mfma_f32_16x16x32_bf16 v[148:151], a[172:175], a[44:47], v[148:151]\n"
    "ds_write_addtid_b32 v198 offset:2560\n"
    "v_mfma_f32_16x16x32_bf16 v[148:151], a[168:171], a[40:43], v[148:151]\n"
    "ds_read_b128 a[160:163], v240 offset:2048\n"
    "v_mfma_f32_16x16x32_bf16 v[156:159], a[168:171], a[72:75], v[156:159]\n"
    "v_cvt_pk_bf16_f32 v207, v210, v211\n"
    "v_mfma_f32_16x16x32_bf16 v[156:159], a[172:175], a[76:79], v[156:159]\n"
    "ds_write_addtid_b32 v199 offset:2816\n"
    "v_mfma_f32_16x16x32_bf16 v[164:167], a[172:175], a[108:111], v[164:167]\n"
    "v_exp_f32_e32 v218, v218\n"
    "v_mfma_f32_16x16x32_bf16 v[164:167], a[168:171], a[104:107], v[164:167]\n"
    "ds_write_addtid_b32 v206 offset:3584\n"
    "v_mfma_f32_16x16x32_bf16 v[172:175], a[168:171], a[136:139], v[172:175]\n"
    "v_mfma_f32_16x16x32_bf16 v[172:175], a[172:175], a[140:143], v[172:175]\n"
    "s_waitcnt lgkmcnt(10)\n"
    "v_exp_f32_e32 v219, v219\n"
    "v_mfma_f32_16x16x32_bf16 v[172:175], a[176:179], a[144:147], v[172:175]\n"
    "ds_read_b128 a[164:167], v241 offset:2048\n"
    "v_mfma_f32_16x16x32_bf16 v[172:175], a[180:183], a[148:151], v[172:175]\n"
    "v_cvt_pk_bf16_f32 v214, v216, v217\n"
    "v_mfma_f32_16x16x32_bf16 v[164:167], a[180:183], a[116:119], v[164:167]\n"
    "ds_write_addtid_b32 v207 offset:3840\n"
    "v_mfma_f32_16x16x32_bf16 v[164:167], a[176:179], a[112:115], v[164:167]\n"
    "ds_read_b128 a[168:171], v240 offset:6144\n"
    "v_mfma_f32_16x16x32_bf16 v[156:159], a[176:179], a[80:83], v[156:159]\n"
    "v_cvt_pk_bf16_f32 v215, v218, v219\n"
    "v_mfma_f32_16x16x32_bf16 v[156:159], a[180:183], a[84:87], v[156:159]\n"
    "ds_write_addtid_b32 v214 offset:4608\n"
    "v_mfma_f32_16x16x32_bf16 v[148:151], a[180:183], a[52:55], v[148:151]\n"
    "ds_write_addtid_b32 v215 offset:4864\n"
    "v_mfma_f32_16x16x32_bf16 v[148:151], a[176:179], a[48:51], v[148:151]\n"
    "v_mfma_f32_16x16x32_bf16 v[140:143], a[176:179], a[16:19], v[140:143]\n"
    "ds_read_b128 a[172:175], v241 offset:6144\n"
    "v_mfma_f32_16x16x32_bf16 v[140:143], a[180:183], a[20:23], v[140:143]\n"
    "s_waitcnt lgkmcnt(10)\n"
    "s_waitcnt lgkmcnt(1)\n"
    "v_mfma_f32_16x16x32_bf16 v[140:143], a[184:187], a[24:27], v[140:143]\n"
    "s_barrier\n"
    "v_mfma_f32_16x16x32_bf16 v[140:143], a[188:191], a[28:31], v[140:143]\n"
    "s_add_u32 m0, s43, 16384\n"
    "v_mfma_f32_16x16x32_bf16 v[148:151], a[188:191], a[60:63], v[148:151]\n"
    "ds_read_b128 a[176:179], v242 offset:2048\n"
    "v_mfma_f32_16x16x32_bf16 v[148:151], a[184:187], a[56:59], v[148:151]\n"
    "buffer_load_dwordx4 v248, s[8:11], s40 offen offset:0 sc1 lds\n"
    "v_mfma_f32_16x16x32_bf16 v[156:159], a[184:187], a[88:91], v[156:159]\n"
    "ds_read2st64_b32 v[184:185], v249 offset0:0 offset1:1\n"
    "v_mfma_f32_16x16x32_bf16 v[156:159], a[188:191], a[92:95], v[156:159]\n"
    "v_mfma_f32_16x16x32_bf16 v[164:167], a[188:191], a[124:127], v[164:167]\n"
    "ds_read2st64_b32 v[186:187], v249 offset0:2 offset1:3\n"
    "v_mfma_f32_16x16x32_bf16 v[164:167], a[184:187], a[120:123], v[164:167]\n"
    "buffer_load_dwordx4 v248, s[8:11], s40 offen offset:1024 sc1 lds\n"
    "v_mfma_f32_16x16x32_bf16 v[172:175], a[184:187], a[152:155], v[172:175]\n"
    "v_mfma_f32_16x16x32_bf16 v[172:175], a[188:191], a[156:159], v[172:175]\n"
    "v_mfma_f32_16x16x32_bf16 v[176:179], a[160:163], a[128:131], v[236:239]\n"
    "ds_read_b128 a[180:183], v243 offset:2048\n"
    "v_mfma_f32_16x16x32_bf16 v[176:179], a[164:167], a[132:135], v[176:179]\n"
    "buffer_load_dwordx4 v248, s[8:11], s40 offen offset:2048 sc1 lds\n"
    "v_mfma_f32_16x16x32_bf16 v[168:171], a[164:167], a[100:103], v[232:235]\n"
    "ds_read2st64_b32 v[192:193], v249 offset0:4 offset1:5\n"
    "v_mfma_f32_16x16x32_bf16 v[168:171], a[160:163], a[96:99], v[168:171]\n"
    "ds_read_b128 a[184:187], v242 offset:6144\n"
    "v_mfma_f32_16x16x32_bf16 v[160:163], a[160:163], a[64:67], v[228:231]\n"
    "ds_read2st64_b32 v[194:195], v249 offset0:6 offset1:7\n"
    "v_mfma_f32_16x16x32_bf16 v[160:163], a[164:167], a[68:71], v[160:163]\n"
    "buffer_load_dwordx4 v248, s[8:11], s40 offen offset:3072 sc1 lds\n"
    "v_mfma_f32_16x16x32_bf16 v[152:155], a[164:167], a[36:39], v[224:227]\n"
    "s_add_u32 m0, s43, 20480\n"
    "v_exp_f32_e32 v140, v140\n"
    "v_mfma_f32_16x16x32_bf16 v[152:155], a[160:163], a[32:35], v[152:155]\n"
    "s_add_u32 s40, s40, 0x8000\n"
    "v_exp_f32_e32 v141, v141\n"
    "v_mfma_f32_16x16x32_bf16 v[144:147], a[160:163], a[0:3], v[220:223]\n"
    "ds_read2st64_b32 v[200:201], v249 offset0:8 offset1:9\n"
    "v_mfma_f32_16x16x32_bf16 v[144:147], a[164:167], a[4:7], v[144:147]\n"
    "s_waitcnt lgkmcnt(8)\n"
    "buffer_load_dwordx4 v248, s[8:11], s41 offen offset:0 sc1 lds\n"
    "v_mfma_f32_16x16x32_bf16 v[144:147], a[168:171], a[8:11], v[144:147]\n"
    "ds_read_b128 a[188:191], v243 offset:6144\n"
    "v_mfma_f32_16x16x32_bf16 v[144:147], a[172:175], a[12:15], v[144:147]\n"
    "ds_read_b64_tr_b16 a[160:161], v244 offset:24576\n"
    "v_mfma_f32_16x16x32_bf16 v[152:155], a[172:175], a[44:47], v[152:155]\n"
    "ds_read2st64_b32 v[202:203], v249 offset0:10 offset1:11\n"
    "v_mfma_f32_16x16x32_bf16 v[152:155], a[168:171], a[40:43], v[152:155]\n"
    "buffer_load_dwordx4 v248, s[8:11], s41 offen offset:1024 sc1 lds\n"
    "v_mfma_f32_16x16x32_bf16 v[160:163], a[168:171], a[72:75], v[160:163]\n"
    "ds_read_b64_tr_b16 a[162:163], v244 offset:26624\n"
    "v_mfma_f32_16x16x32_bf16 v[160:163], a[172:175], a[76:79], v[160:163]\n"
    "v_exp_f32_e32 v142, v142\n"
    "v_mfma_f32_16x16x32_bf16 v[168:171], a[172:175], a[108:111], v[168:171]\n"
    "ds_read2st64_b32 v[208:209], v249 offset0:12 offset1:13\n"
    "v_mfma_f32_16x16x32_bf16 v[168:171], a[168:171], a[104:107], v[168:171]\n"
    "buffer_load_dwordx4 v248, s[8:11], s41 offen offset:2048 sc1 lds\n"
    "v_mfma_f32_16x16x32_bf16 v[176:179], a[168:171], a[136:139], v[176:179]\n"
    "ds_read_b64_tr_b16 a[164:165], v244 offset:57344\n"
    "v_mfma_f32_16x16x32_bf16 v[176:179], a[172:175], a[140:143], v[176:179]\n"
    "s_waitcnt lgkmcnt(10)\n"
    "ds_read2st64_b32 v[210:211], v249 offset0:14 offset1:15\n"
    "v_mfma_f32_16x16x32_bf16 v[176:179], a[176:179], a[144:147], v[176:179]\n"
    "ds_read_b64_tr_b16 a[168:169], v245 offset:24576\n"
    "v_mfma_f32_16x16x32_bf16 v[176:179], a[180:183], a[148:151], v[176:179]\n"
    "buffer_load_dwordx4 v248, s[8:11], s41 offen offset:3072 sc1 lds\n"
    "v_mfma_f32_16x16x32_bf16 v[168:171], a[180:183], a[116:119], v[168:171]\n"
    "ds_read_b64_tr_b16 a[166:167], v244 offset:59392\n"
    "v_mfma_f32_16x16x32_bf16 v[168:171], a[176:179], a[112:115], v[168:171]\n"
    "s_add_u32 s41, s41, 0x8000\n"
    "v_exp_f32_e32 v143, v143\n"
    "v_mfma_f32_16x16x32_bf16 v[160:163], a[176:179], a[80:83], v[160:163]\n"
    "ds_read_b64_tr_b16 a[170:171], v245 offset:26624\n"
    "v_cvt_pk_bf16_f32 v140, v140, v141\n"
    "v_mfma_f32_16x16x32_bf16 v[160:163], a[180:183], a[84:87], v[160:163]\n"
    "ds_read2st64_b32 v[216:217], v249 offset0:16 offset1:17\n"
    "v_mfma_f32_16x16x32_bf16 v[152:155], a[180:183], a[52:55], v[152:155]\n"
    "v_exp_f32_e32 v148, v148\n"
    "v_mfma_f32_16x16x32_bf16 v[152:155], a[176:179], a[48:51], v[152:155]\n"
    "v_exp_f32_e32 v149, v149\n"
    "v_mfma_f32_16x16x32_bf16 v[144:147], a[176:179], a[16:19], v[144:147]\n"
    "ds_read_b64_tr_b16 a[172:173], v245 offset:57344\n"
    "v_cvt_pk_bf16_f32 v141, v142, v143\n"
    "v_mfma_f32_16x16x32_bf16 v[144:147], a[180:183], a[20:23], v[144:147]\n"
    "s_waitcnt lgkmcnt(11)\n"
    "ds_read2st64_b32 v[218:219], v249 offset0:18 offset1:19\n"
    "v_mfma_f32_16x16x32_bf16 v[144:147], a[184:187], a[24:27], v[144:147]\n"
    "ds_read_b64_tr_b16 a[176:177], v246 offset:24576\n"
    "v_mfma_f32_16x16x32_bf16 v[144:147], a[188:191], a[28:31], v[144:147]\n"
    "ds_read_b64_tr_b16 a[174:175], v245 offset:59392\n"
    "v_mfma_f32_16x16x32_bf16 v[152:155], a[188:191], a[60:63], v[152:155]\n"
    "v_exp_f32_e32 v150, v150\n"
    "v_mfma_f32_16x16x32_bf16 v[152:155], a[184:187], a[56:59], v[152:155]\n"
    "v_exp_f32_e32 v151, v151\n"
    "v_mfma_f32_16x16x32_bf16 v[160:163], a[184:187], a[88:91], v[160:163]\n"
    "v_mfma_f32_16x16x32_bf16 v[160:163], a[188:191], a[92:95], v[160:163]\n"
    "ds_read_b64_tr_b16 a[178:179], v246 offset:26624\n"
    "v_mfma_f32_16x16x32_bf16 v[168:171], a[188:191], a[124:127], v[168:171]\n"
    "v_cvt_pk_bf16_f32 v148, v148, v149\n"
    "v_mfma_f32_16x16x32_bf16 v[168:171], a[184:187], a[120:123], v[168:171]\n"
    "v_exp_f32_e32 v156, v156\n"
    "v_mfma_f32_16x16x32_bf16 v[176:179], a[184:187], a[152:155], v[176:179]\n"
    "ds_read_b64_tr_b16 a[180:181], v246 offset:57344\n"
    "v_mfma_f32_16x16x32_bf16 v[176:179], a[188:191], a[156:159], v[176:179]\n"
    "s_waitcnt lgkmcnt(4)\n"
    "v_exp_f32_e32 v157, v157\n"
    "v_mfma_f32_16x16x32_bf16 a[236:239], a[160:163], v[180:183], a[236:239]\n"
    "v_mfma_f32_16x16x32_bf16 a[236:239], a[164:167], v[184:187], a[236:239]\n"
    "ds_read_b64_tr_b16 a[182:183], v246 offset:59392\n"
    "v_mfma_f32_16x16x32_bf16 a[240:243], a[164:167], v[192:195], a[240:243]\n"
    "ds_read_b64_tr_b16 a[184:185], v247 offset:24576\n"
    "v_mfma_f32_16x16x32_bf16 a[240:243], a[160:163], v[188:191], a[240:243]\n"
    "v_cvt_pk_bf16_f32 v149, v150, v151\n"
    "v_mfma_f32_16x16x32_bf16 a[244:247], a[160:163], v[196:199], a[244:247]\n"
    "ds_read_b64_tr_b16 a[186:187], v247 offset:26624\n"
    "v_mfma_f32_16x16x32_bf16 a[244:247], a[164:167], v[200:203], a[244:247]\n"
    "v_exp_f32_e32 v158, v158\n"
    "v_mfma_f32_16x16x32_bf16 a[248:251], a[164:167], v[208:211], a[248:251]\n"
    "v_exp_f32_e32 v159, v159\n"
    "v_mfma_f32_16x16x32_bf16 a[248:251], a[160:163], v[204:207], a[248:251]\n"
    "v_cvt_pk_bf16_f32 v156, v156, v157\n"
    "v_mfma_f32_16x16x32_bf16 a[252:255], a[160:163], v[212:215], a[252:255]\n"
    "ds_read_b64_tr_b16 a[188:189], v247 offset:57344\n"
    "v_mfma_f32_16x16x32_bf16 a[252:255], a[164:167], v[216:219], a[252:255]\n"
    "s_waitcnt lgkmcnt(6)\n"
    "v_exp_f32_e32 v164, v164\n"
    "v_mfma_f32_16x16x32_bf16 v[16:19], a[172:175], v[216:219], v[16:19]\n"
    "ds_read_b64_tr_b16 a[160:161], v244 offset:28672\n"
    "v_mfma_f32_16x16x32_bf16 v[16:19], a[168:171], v[212:215], v[16:19]\n"
    "ds_read_b64_tr_b16 a[190:191], v247 offset:59392\n"
    "v_mfma_f32_16x16x32_bf16 v[12:15], a[168:171], v[204:207], v[12:15]\n"
    "v_exp_f32_e32 v165, v165\n"
    "v_mfma_f32_16x16x32_bf16 v[12:15], a[172:175], v[208:211], v[12:15]\n"
    "v_cvt_pk_bf16_f32 v157, v158, v159\n"
    "v_mfma_f32_16x16x32_bf16 v[8:11], a[172:175], v[200:203], v[8:11]\n"
    "ds_read_b64_tr_b16 a[162:163], v244 offset:30720\n"
    "v_mfma_f32_16x16x32_bf16 v[8:11], a[168:171], v[196:199], v[8:11]\n"
    "v_exp_f32_e32 v166, v166\n"
    "v_mfma_f32_16x16x32_bf16 v[4:7], a[168:171], v[188:191], v[4:7]\n"
    "v_exp_f32_e32 v167, v167\n"
    "v_mfma_f32_16x16x32_bf16 v[4:7], a[172:175], v[192:195], v[4:7]\n"
    "v_cvt_pk_bf16_f32 v164, v164, v165\n"
    "v_mfma_f32_16x16x32_bf16 v[0:3], a[172:175], v[184:187], v[0:3]\n"
    "ds_read_b64_tr_b16 a[164:165], v244 offset:61440\n"
    "v_mfma_f32_16x16x32_bf16 v[0:3], a[168:171], v[180:183], v[0:3]\n"
    "s_waitcnt lgkmcnt(7)\n"
    "v_exp_f32_e32 v172, v172\n"
    "v_mfma_f32_16x16x32_bf16 v[20:23], a[176:179], v[180:183], v[20:23]\n"
    "v_mfma_f32_16x16x32_bf16 v[20:23], a[180:183], v[184:187], v[20:23]\n"
    "ds_read_b64_tr_b16 a[166:167], v244 offset:63488\n"
    "v_mfma_f32_16x16x32_bf16 v[24:27], a[180:183], v[192:195], v[24:27]\n"
    "ds_read_b64_tr_b16 a[168:169], v245 offset:28672\n"
    "v_mfma_f32_16x16x32_bf16 v[24:27], a[176:179], v[188:191], v[24:27]\n"
    "v_exp_f32_e32 v173, v173\n"
    "v_mfma_f32_16x16x32_bf16 v[28:31], a[176:179], v[196:199], v[28:31]\n"
    "ds_read_b64_tr_b16 a[170:171], v245 offset:30720\n"
    "v_mfma_f32_16x16x32_bf16 v[28:31], a[180:183], v[200:203], v[28:31]\n"
    "v_cvt_pk_bf16_f32 v165, v166, v167\n"
    "v_mfma_f32_16x16x32_bf16 v[32:35], a[180:183], v[208:211], v[32:35]\n"
    "v_exp_f32_e32 v174, v174\n"
    "v_mfma_f32_16x16x32_bf16 v[32:35], a[176:179], v[204:207], v[32:35]\n"
    "v_exp_f32_e32 v175, v175\n"
    "v_mfma_f32_16x16x32_bf16 v[36:39], a[176:179], v[212:215], v[36:39]\n"
    "ds_read_b64_tr_b16 a[172:173], v245 offset:61440\n"
    "v_mfma_f32_16x16x32_bf16 v[36:39], a[180:183], v[216:219], v[36:39]\n"
    "s_waitcnt lgkmcnt(6)\n"
    "v_cvt_pk_bf16_f32 v172, v172, v173\n"
    "v_mfma_f32_16x16x32_bf16 v[56:59], a[188:191], v[216:219], v[56:59]\n"
    "ds_read_b64_tr_b16 a[176:177], v246 offset:28672\n"
    "v_mfma_f32_16x16x32_bf16 v[56:59], a[184:187], v[212:215], v[56:59]\n"
    "ds_read_b64_tr_b16 a[174:175], v245 offset:63488\n"
    "v_mfma_f32_16x16x32_bf16 v[52:55], a[184:187], v[204:207], v[52:55]\n"
    "v_cvt_pk_bf16_f32 v173, v174, v175\n"
    "v_mfma_f32_16x16x32_bf16 v[52:55], a[188:191], v[208:211], v[52:55]\n"
    "v_exp_f32_e32 v144, v144\n"
    "v_mfma_f32_16x16x32_bf16 v[48:51], a[188:191], v[200:203], v[48:51]\n"
    "ds_read_b64_tr_b16 a[178:179], v246 offset:30720\n"
    "v_mfma_f32_16x16x32_bf16 v[48:51], a[184:187], v[196:199], v[48:51]\n"
    "v_mfma_f32_16x16x32_bf16 v[44:47], a[184:187], v[188:191], v[44:47]\n"
    "v_exp_f32_e32 v145, v145\n"
    "v_mfma_f32_16x16x32_bf16 v[44:47], a[188:191], v[192:195], v[44:47]\n"
    "v_exp_f32_e32 v146, v146\n"
    "v_mfma_f32_16x16x32_bf16 v[40:43], a[188:191], v[184:187], v[40:43]\n"
    "ds_read_b64_tr_b16 a[180:181], v246 offset:61440\n"
    "v_mfma_f32_16x16x32_bf16 v[40:43], a[184:187], v[180:183], v[40:43]\n"
    "s_waitcnt lgkmcnt(7)\n"
    "v_exp_f32_e32 v147, v147\n"
    "v_mfma_f32_16x16x32_bf16 v[60:63], a[160:163], v[180:183], v[60:63]\n"
    "ds_read_b64_tr_b16 a[184:185], v247 offset:28672\n"
    "v_mfma_f32_16x16x32_bf16 v[60:63], a[164:167], v[184:187], v[60:63]\n"
    "ds_read_b64_tr_b16 a[182:183], v246 offset:63488\n"
    "v_mfma_f32_16x16x32_bf16 v[64:67], a[164:167], v[192:195], v[64:67]\n"
    "ds_read_b64_tr_b16 a[186:187], v247 offset:30720\n"
    "v_mfma_f32_16x16x32_bf16 v[64:67], a[160:163], v[188:191], v[64:67]\n"
    "v_cvt_pk_bf16_f32 v142, v144, v145\n"
    "v_mfma_f32_16x16x32_bf16 v[68:71], a[160:163], v[196:199], v[68:71]\n"
    "v_exp_f32_e32 v152, v152\n"
    "v_mfma_f32_16x16x32_bf16 v[68:71], a[164:167], v[200:203], v[68:71]\n"
    "ds_read_b64_tr_b16 a[188:189], v247 offset:61440\n"
    "v_mfma_f32_16x16x32_bf16 v[72:75], a[164:167], v[208:211], v[72:75]\n"
    "ds_read_b64_tr_b16 a[190:191], v247 offset:63488\n"
    "v_mfma_f32_16x16x32_bf16 v[72:75], a[160:163], v[204:207], v[72:75]\n"
    "s_waitcnt vmcnt(8)\n"
    "v_mfma_f32_16x16x32_bf16 v[76:79], a[160:163], v[212:215], v[76:79]\n"
    "s_barrier\n"
    "v_mfma_f32_16x16x32_bf16 v[76:79], a[164:167], v[216:219], v[76:79]\n"
    "s_waitcnt lgkmcnt(7)\n"
    "s_mov_b32 m0, s42\n"
    "v_mfma_f32_16x16x32_bf16 v[96:99], a[172:175], v[216:219], v[96:99]\n"
    "v_exp_f32_e32 v153, v153\n"
    "v_mfma_f32_16x16x32_bf16 v[96:99], a[168:171], v[212:215], v[96:99]\n"
    "v_cvt_pk_bf16_f32 v143, v146, v147\n"
    "v_mfma_f32_16x16x32_bf16 v[92:95], a[168:171], v[204:207], v[92:95]\n"
    "v_exp_f32_e32 v154, v154\n"
    "v_mfma_f32_16x16x32_bf16 v[92:95], a[172:175], v[208:211], v[92:95]\n"
    "ds_read_b128 a[160:163], v240 offset:8192\n"
    "v_mfma_f32_16x16x32_bf16 v[88:91], a[172:175], v[200:203], v[88:91]\n"
    "ds_write_addtid_b32 v140 offset:0\n"
    "v_mfma_f32_16x16x32_bf16 v[88:91], a[168:171], v[196:199], v[88:91]\n"
    "v_exp_f32_e32 v155, v155\n"
    "v_mfma_f32_16x16x32_bf16 v[84:87], a[168:171], v[188:191], v[84:87]\n"
    "ds_write_addtid_b32 v141 offset:256\n"
    "v_mfma_f32_16x16x32_bf16 v[84:87], a[172:175], v[192:195], v[84:87]\n"
    "ds_write_addtid_b32 v148 offset:1024\n"
    "v_mfma_f32_16x16x32_bf16 v[80:83], a[172:175], v[184:187], v[80:83]\n"
    "v_cvt_pk_bf16_f32 v150, v152, v153\n"
    "v_mfma_f32_16x16x32_bf16 v[80:83], a[168:171], v[180:183], v[80:83]\n"
    "s_waitcnt lgkmcnt(7)\n"
    "v_exp_f32_e32 v160, v160\n"
    "v_mfma_f32_16x16x32_bf16 v[100:103], a[176:179], v[180:183], v[100:103]\n"
    "ds_read_b128 a[164:167], v241 offset:8192\n"
    "v_mfma_f32_16x16x32_bf16 v[100:103], a[180:183], v[184:187], v[100:103]\n"
    "ds_write_addtid_b32 v149 offset:1280\n"
    "v_mfma_f32_16x16x32_bf16 v[104:107], a[180:183], v[192:195], v[104:107]\n"
    "v_exp_f32_e32 v161, v161\n"
    "v_mfma_f32_16x16x32_bf16 v[104:107], a[176:179], v[188:191], v[104:107]\n"
    "ds_read_b128 a[168:171], v240 offset:12288\n"
    "v_mfma_f32_16x16x32_bf16 v[108:111], a[176:179], v[196:199], v[108:111]\n"
    "ds_write_addtid_b32 v156 offset:2048\n"
    "v_mfma_f32_16x16x32_bf16 v[108:111], a[180:183], v[200:203], v[108:111]\n"
    "v_cvt_pk_bf16_f32 v151, v154, v155\n"
    "v_mfma_f32_16x16x32_bf16 v[112:115], a[180:183], v[208:211], v[112:115]\n"
    "ds_write_addtid_b32 v157 offset:2304\n"
    "v_mfma_f32_16x16x32_bf16 v[112:115], a[176:179], v[204:207], v[112:115]\n"
    "v_exp_f32_e32 v162, v162\n"
    "v_mfma_f32_16x16x32_bf16 v[116:119], a[176:179], v[212:215], v[116:119]\n"
    "ds_write_addtid_b32 v164 offset:3072\n"
    "v_mfma_f32_16x16x32_bf16 v[116:119], a[180:183], v[216:219], v[116:119]\n"
    "s_waitcnt lgkmcnt(10)\n"
    "v_exp_f32_e32 v163, v163\n"
    "v_mfma_f32_16x16x32_bf16 v[136:139], a[188:191], v[216:219], v[136:139]\n"
    "ds_read_b128 a[172:175], v241 offset:12288\n"
    "v_mfma_f32_16x16x32_bf16 v[136:139], a[184:187], v[212:215], v[136:139]\n"
    "ds_write_addtid_b32 v165 offset:3328\n"
    "v_cvt_pk_bf16_f32 v158, v160, v161\n"
    "v_mfma_f32_16x16x32_bf16 v[132:135], a[184:187], v[204:207], v[132:135]\n"
    "ds_read_b128 a[176:179], v242 offset:8192\n"
    "v_mfma_f32_16x16x32_bf16 v[132:135], a[188:191], v[208:211], v[132:135]\n"
    "ds_write_addtid_b32 v172 offset:4096\n"
    "v_mfma_f32_16x16x32_bf16 v[128:131], a[188:191], v[200:203], v[128:131]\n"
    "v_exp_f32_e32 v168, v168\n"
    "v_mfma_f32_16x16x32_bf16 v[128:131], a[184:187], v[196:199], v[128:131]\n"
    "ds_write_addtid_b32 v173 offset:4352\n"
    "v_mfma_f32_16x16x32_bf16 v[124:127], a[184:187], v[188:191], v[124:127]\n"
    "v_exp_f32_e32 v169, v169\n"
    "v_mfma_f32_16x16x32_bf16 v[124:127], a[188:191], v[192:195], v[124:127]\n"
    "ds_read_b128 a[180:183], v243 offset:8192\n"
    "v_mfma_f32_16x16x32_bf16 v[120:123], a[188:191], v[184:187], v[120:123]\n"
    "ds_write_addtid_b32 v142 offset:512\n"
    "v_mfma_f32_16x16x32_bf16 v[120:123], a[184:187], v[180:183], v[120:123]\n"
    "s_waitcnt lgkmcnt(12)\n"
    "v_cvt_pk_bf16_f32 v159, v162, v163\n"
    ".Lat_in:\n"
    "v_mfma_f32_16x16x32_bf16 v[212:215], a[160:163], a[128:131], v[236:239]\n"
    ".long 0xbedf00ff\n"
    ".Lat_w4:\n"
    "s_setpc_b64 s[2:3]\n"
    "v_mfma_f32_16x16x32_bf16 v[212:215], a[164:167], a[132:135], v[212:215]\n"
    "v_exp_f32_e32 v170, v170\n"
    "v_mfma_f32_16x16x32_bf16 v[204:207], a[164:167], a[100:103], v[232:235]\n"
    "ds_write_addtid_b32 v143 offset:768\n"
    "v_mfma_f32_16x16x32_bf16 v[204:207], a[160:163], a[96:99], v[204:207]\n"
    "ds_read_b128 a[184:187], v242 offset:12288\n"
    "v_mfma_f32_16x16x32_bf16 v[196:199], a[160:163], a[64:67], v[228:231]\n"
    "v_exp_f32_e32 v171, v171\n"
    "v_mfma_f32_16x16x32_bf16 v[196:199], a[164:167], a[68:71], v[196:199]\n"
    "ds_write_addtid_b32 v150 offset:1536\n"
    "v_mfma_f32_16x16x32_bf16 v[188:191], a[164:167], a[36:39], v[224:227]\n"
    "v_cvt_pk_bf16_f32 v166, v168, v169\n"
    "v_mfma_f32_16x16x32_bf16 v[188:191], a[160:163], a[32:35], v[188:191]\n"
    "v_mfma_f32_16x16x32_bf16 v[180:183], a[160:163], a[0:3], v[220:223]\n"
    "ds_write_addtid_b32 v151 offset:1792\n"
    "v_mfma_f32_16x16x32_bf16 v[180:183], a[164:167], a[4:7], v[180:183]\n"
    "s_waitcnt lgkmcnt(10)\n"
    "v_exp_f32_e32 v176, v176\n"
    "v_mfma_f32_16x16x32_bf16 v[180:183], a[168:171], a[8:11], v[180:183]\n"
    "ds_read_b128 a[188:191], v243 offset:12288\n"
    "v_mfma_f32_16x16x32_bf16 v[180:183], a[172:175], a[12:15], v[180:183]\n"
    "v_exp_f32_e32 v177, v177\n"
    "v_mfma_f32_16x16x32_bf16 v[188:191], a[172:175], a[44:47], v[188:191]\n"
    "ds_write_addtid_b32 v158 offset:2560\n"
    "v_mfma_f32_16x16x32_bf16 v[188:191], a[168:171], a[40:43], v[188:191]\n"
    "ds_read_b128 a[160:163], v240 offset:10240\n"
    "v_mfma_f32_16x16x32_bf16 v[196:199], a[168:171], a[72:75], v[196:199]\n"
    "v_cvt_pk_bf16_f32 v167, v170, v171\n"
    "v_mfma_f32_16x16x32_bf16 v[196:199], a[172:175], a[76:79], v[196:199]\n"
    "ds_write_addtid_b32 v159 offset:2816\n"
    "v_mfma_f32_16x16x32_bf16 v[204:207], a[172:175], a[108:111], v[204:207]\n"
    "v_exp_f32_e32 v178, v178\n"
    "v_mfma_f32_16x16x32_bf16 v[204:207], a[168:171], a[104:107], v[204:207]\n"
    "ds_write_addtid_b32 v166 offset:3584\n"
    "v_mfma_f32_16x16x32_bf16 v[212:215], a[168:171], a[136:139], v[212:215]\n"
    "v_mfma_f32_16x16x32_bf16 v[212:215], a[172:175], a[140:143], v[212:215]\n"
    "s_waitcnt lgkmcnt(10)\n"
    "v_exp_f32_e32 v179, v179\n"
    "v_mfma_f32_16x16x32_bf16 v[212:215], a[176:179], a[144:147], v[212:215]\n"
    "ds_read_b128 a[164:167], v241 offset:10240\n"
    "v_mfma_f32_16x16x32_bf16 v[212:215], a[180:183], a[148:151], v[212:215]\n"
    "v_cvt_pk_bf16_f32 v174, v176, v177\n"
    "v_mfma_f32_16x16x32_bf16 v[204:207], a[180:183], a[116:119], v[204:207]\n"
    "ds_write_addtid_b32 v167 offset:3840\n"
    "v_mfma_f32_16x16x32_bf16 v[204:207], a[176:179], a[112:115], v[204:207]\n"
    "ds_read_b128 a[168:171], v240 offset:14336\n"
    "v_mfma_f32_16x16x32_bf16 v[196:199], a[176:179], a[80:83], v[196:199]\n"
    "v_cvt_pk_bf16_f32 v175, v178, v179\n"
    "v_mfma_f32_16x16x32_bf16 v[196:199], a[180:183], a[84:87], v[196:199]\n"
    "ds_write_addtid_b32 v174 offset:4608\n"
    "v_mfma_f32_16x16x32_bf16 v[188:191], a[180:183], a[52:55], v[188:191]\n"
    "ds_write_addtid_b32 v175 offset:4864\n"
    "v_mfma_f32_16x16x32_bf16 v[188:191], a[176:179], a[48:51], v[188:191]\n"
    "v_mfma_f32_16x16x32_bf16 v[180:183], a[176:179], a[16:19], v[180:183]\n"
    "ds_read_b128 a[172:175], v241 offset:14336\n"
    "v_mfma_f32_16x16x32_bf16 v[180:183], a[180:183], a[20:23], v[180:183]\n"
    "s_waitcnt lgkmcnt(10)\n"
    "s_waitcnt lgkmcnt(1)\n"
    "v_mfma_f32_16x16x32_bf16 v[180:183], a[184:187], a[24:27], v[180:183]\n"
    "s_barrier\n"
    "v_mfma_f32_16x16x32_bf16 v[180:183], a[188:191], a[28:31], v[180:183]\n"
    "s_add_u32 m0, s43, 24576\n"
    "v_mfma_f32_16x16x32_bf16 v[188:191], a[188:191], a[60:63], v[188:191]\n"
    "ds_read_b128 a[176:179], v242 offset:10240\n"
    "v_mfma_f32_16x16x32_bf16 v[188:191], a[184:187], a[56:59], v[188:191]\n"
    "buffer_load_dwordx4 v248, s[8:11], s40 offen offset:0 sc1 lds\n"
    "v_mfma_f32_16x16x32_bf16 v[196:199], a[184:187], a[88:91], v[196:199]\n"
    "ds_read2st64_b32 v[144:145], v249 offset0:0 offset1:1\n"
    "v_mfma_f32_16x16x32_bf16 v[196:199], a[188:191], a[92:95], v[196:199]\n"
    "v_mfma_f32_16x16x32_bf16 v[204:207], a[188:191], a[124:127], v[204:207]\n"
    "ds_read2st64_b32 v[146:147], v249 offset0:2 offset1:3\n"
    "v_mfma_f32_16x16x32_bf16 v[204:207], a[184:187], a[120:123], v[204:207]\n"
    "buffer_load_dwordx4 v248, s[8:11], s40 offen offset:1024 sc1 lds\n"
    "v_mfma_f32_16x16x32_bf16 v[212:215], a[184:187], a[152:155], v[212:215]\n"
    "v_mfma_f32_16x16x32_bf16 v[212:215], a[188:191], a[156:159], v[212:215]\n"
    "v_mfma_f32_16x16x32_bf16 v[216:219], a[160:163], a[128:131], v[236:239]\n"
    "ds_read_b128 a[180:183], v243 offset:10240\n"
    "v_mfma_f32_16x16x32_bf16 v[216:219], a[164:167], a[132:135], v[216:219]\n"
    "buffer_load_dwordx4 v248, s[8:11], s40 offen offset:2048 sc1 lds\n"
    "v_mfma_f32_16x16x32_bf16 v[208:211], a[164:167], a[100:103], v[232:235]\n"
    "ds_read2st64_b32 v[152:153], v249 offset0:4 offset1:5\n"
    "v_mfma_f32_16x16x32_bf16 v[208:211], a[160:163], a[96:99], v[208:211]\n"
    "ds_read_b128 a[184:187], v242 offset:14336\n"
    "v_mfma_f32_16x16x32_bf16 v[200:203], a[160:163], a[64:67], v[228:231]\n"
    "ds_read2st64_b32 v[154:155], v249 offset0:6 offset1:7\n"
    "v_mfma_f32_16x16x32_bf16 v[200:203], a[164:167], a[68:71], v[200:203]\n"
    "buffer_load_dwordx4 v248, s[8:11], s40 offen offset:3072 sc1 lds\n"
    "v_mfma_f32_16x16x32_bf16 v[192:195], a[164:167], a[36:39], v[224:227]\n"
    "s_add_u32 m0, s43, 28672\n"
    "v_exp_f32_e32 v180, v180\n"
    "v_mfma_f32_16x16x32_bf16 v[192:195], a[160:163], a[32:35], v[192:195]\n"
    "s_add_u32 s40, s40, 0x8000\n"
    "v_exp_f32_e32 v181, v181\n"
    "v_mfma_f32_16x16x32_bf16 v[184:187], a[160:163], a[0:3], v[220:223]\n"
    "ds_read2st64_b32 v[160:161], v249 offset0:8 offset1:9\n"
    "v_mfma_f32_16x16x32_bf16 v[184:187], a[164:167], a[4:7], v[184:187]\n"
    "s_waitcnt lgkmcnt(8)\n"
    "buffer_load_dwordx4 v248, s[8:11], s41 offen offset:0 sc1 lds\n"
    "v_mfma_f32_16x16x32_bf16 v[184:187], a[168:171], a[8:11], v[184:187]\n"
    "ds_read_b128 a[188:191], v243 offset:14336\n"
    "v_mfma_f32_16x16x32_bf16 v[184:187], a[172:175], a[12:15], v[184:187]\n"
    "ds_read_b64_tr_b16 a[160:161], v244 offset:0\n"
    "v_mfma_f32_16x16x32_bf16 v[192:195], a[172:175], a[44:47], v[192:195]\n"
    "ds_read2st64_b32 v[162:163], v249 offset0:10 offset1:11\n"
    "v_mfma_f32_16x16x32_bf16 v[192:195], a[168:171], a[40:43], v[192:195]\n"
    "buffer_load_dwordx4 v248, s[8:11], s41 offen offset:1024 sc1 lds\n"
    "v_mfma_f32_16x16x32_bf16 v[200:203], a[168:171], a[72:75], v[200:203]\n"
    "ds_read_b64_tr_b16 a[162:163], v244 offset:2048\n"
    "v_mfma_f32_16x16x32_bf16 v[200:203], a[172:175], a[76:79], v[200:203]\n"
    "v_exp_f32_e32 v182, v182\n"
    "v_mfma_f32_16x16x32_bf16 v[208:211], a[172:175], a[108:111], v[208:211]\n"
    "ds_read2st64_b32 v[168:169], v249 offset0:12 offset1:13\n"
    "v_mfma_f32_16x16x32_bf16 v[208:211], a[168:171], a[104:107], v[208:211]\n"
    "buffer_load_dwordx4 v248, s[8:11], s41 offen offset:2048 sc1 lds\n"
    "v_mfma_f32_16x16x32_bf16 v[216:219], a[168:171], a[136:139], v[216:219]\n"
    "ds_read_b64_tr_b16 a[164:165], v244 offset:32768\n"
    "v_mfma_f32_16x16x32_bf16 v[216:219], a[172:175], a[140:143], v[216:219]\n"
    "s_waitcnt lgkmcnt(10)\n"
    "ds_read2st64_b32 v[170:171], v249 offset0:14 offset1:15\n"
    "v_mfma_f32_16x16x32_bf16 v[216:219], a[176:179], a[144:147], v[216:219]\n"
    "ds_read_b64_tr_b16 a[168:169], v245 offset:0\n"
    "v_mfma_f32_16x16x32_bf16 v[216:219], a[180:183], a[148:151], v[216:219]\n"
    "buffer_load_dwordx4 v248, s[8:11], s41 offen offset:3072 sc1 lds\n"
    "v_mfma_f32_16x16x32_bf16 v[208:211], a[180:183], a[116:119], v[208:211]\n"
    "ds_read_b64_tr_b16 a[166:167], v244 offset:34816\n"
    "v_mfma_f32_16x16x32_bf16 v[208:211], a[176:179], a[112:115], v[208:211]\n"
    "s_add_u32 s41, s41, 0x8000\n"
    "v_exp_f32_e32 v183, v183\n"
    "v_mfma_f32_16x16x32_bf16 v[200:203], a[176:179], a[80:83], v[200:203]\n"
    "ds_read_b64_tr_b16 a[170:171], v245 offset:2048\n"
    "v_cvt_pk_bf16_f32 v180, v180, v181\n"
    "v_mfma_f32_16x16x32_bf16 v[200:203], a[180:183], a[84:87], v[200:203]\n"
    "ds_read2st64_b32 v[176:177], v249 offset0:16 offset1:17\n"
    "v_mfma_f32_16x16x32_bf16 v[192:195], a[180:183], a[52:55], v[192:195]\n"
    "v_exp_f32_e32 v188, v188\n"
    "v_mfma_f32_16x16x32_bf16 v[192:195], a[176:179], a[48:51], v[192:195]\n"
    "v_exp_f32_e32 v189, v189\n"
    "v_mfma_f32_16x16x32_bf16 v[184:187], a[176:179], a[16:19], v[184:187]\n"
    "ds_read_b64_tr_b16 a[172:173], v245 offset:32768\n"
    "v_cvt_pk_bf16_f32 v181, v182, v183\n"
    "v_mfma_f32_16x16x32_bf16 v[184:187], a[180:183], a[20:23], v[184:187]\n"
    "s_waitcnt lgkmcnt(11)\n"
    "ds_read2st64_b32 v[178:179], v249 offset0:18 offset1:19\n"
    "v_mfma_f32_16x16x32_bf16 v[184:187], a[184:187], a[24:27], v[184:187]\n"
    "ds_read_b64_tr_b16 a[176:177], v246 offset:0\n"
    "v_mfma_f32_16x16x32_bf16 v[184:187], a[188:191], a[28:31], v[184:187]\n"
    "ds_read_b64_tr_b16 a[174:175], v245 offset:34816\n"
    "v_mfma_f32_16x16x32_bf16 v[192:195], a[188:191], a[60:63], v[192:195]\n"
    "v_exp_f32_e32 v190, v190\n"
    "v_mfma_f32_16x16x32_bf16 v[192:195], a[184:187], a[56:59], v[192:195]\n"
    "v_exp_f32_e32 v191, v191\n"
    "v_mfma_f32_16x16x32_bf16 v[200:203], a[184:187], a[88:91], v[200:203]\n"
    "v_mfma_f32_16x16x32_bf16 v[200:203], a[188:191], a[92:95], v[200:203]\n"
    "ds_read_b64_tr_b16 a[178:179], v246 offset:2048\n"
    "v_mfma_f32_16x16x32_bf16 v[208:211], a[188:191], a[124:127], v[208:211]\n"
    "v_cvt_pk_bf16_f32 v188, v188, v189\n"
    "v_mfma_f32_16x16x32_bf16 v[208:211], a[184:187], a[120:123], v[208:211]\n"
    "v_exp_f32_e32 v196, v196\n"
    "v_mfma_f32_16x16x32_bf16 v[216:219], a[184:187], a[152:155], v[216:219]\n"
    "ds_read_b64_tr_b16 a[180:181], v246 offset:32768\n"
    "v_mfma_f32_16x16x32_bf16 v[216:219], a[188:191], a[156:159], v[216:219]\n"
    "s_waitcnt lgkmcnt(4)\n"
    "v_exp_f32_e32 v197, v197\n"
    "v_mfma_f32_16x16x32_bf16 a[236:239], a[160:163], v[140:143], a[236:239]\n"
    "v_mfma_f32_16x16x32_bf16 a[236:239], a[164:167], v[144:147], a[236:239]\n"
    "ds_read_b64_tr_b16 a[182:183], v246 offset:34816\n"
    "v_mfma_f32_16x16x32_bf16 a[240:243], a[164:167], v[152:155], a[240:243]\n"
    "ds_read_b64_tr_b16 a[184:185], v247 offset:0\n"
    "v_mfma_f32_16x16x32_bf16 a[240:243], a[160:163], v[148:151], a[240:243]\n"
    "v_cvt_pk_bf16_f32 v189, v190, v191\n"
    "v_mfma_f32_16x16x32_bf16 a[244:247], a[160:163], v[156:159], a[244:247]\n"
    "ds_read_b64_tr_b16 a[186:187], v247 offset:2048\n"
    "v_mfma_f32_16x16x32_bf16 a[244:247], a[164:167], v[160:163], a[244:247]\n"
    "v_exp_f32_e32 v198, v198\n"
    "v_mfma_f32_16x16x32_bf16 a[248:251], a[164:167], v[168:171], a[248:251]\n"
    "v_exp_f32_e32 v199, v199\n"
    "v_mfma_f32_16x16x32_bf16 a[248:251], a[160:163], v[164:167], a[248:251]\n"
    "v_cvt_pk_bf16_f32 v196, v196, v197\n"
    "v_mfma_f32_16x16x32_bf16 a[252:255], a[160:163], v[172:175], a[252:255]\n"
    "ds_read_b64_tr_b16 a[188:189], v247 offset:32768\n"
    "v_mfma_f32_16x16x32_bf16 a[252:255], a[164:167], v[176:179], a[252:255]\n"
    "s_waitcnt lgkmcnt(6)\n"
    "v_exp_f32_e32 v204, v204\n"
    "v_mfma_f32_16x16x32_bf16 v[16:19], a[172:175], v[176:179], v[16:19]\n"
    "ds_read_b64_tr_b16 a[160:161], v244 offset:4096\n"
    "v_mfma_f32_16x16x32_bf16 v[16:19], a[168:171], v[172:175], v[16:19]\n"
    "ds_read_b64_tr_b16 a[190:191], v247 offset:34816\n"
    "v_mfma_f32_16x16x32_bf16 v[12:15], a[168:171], v[164:167], v[12:15]\n"
    "v_exp_f32_e32 v205, v205\n"
    "v_mfma_f32_16x16x32_bf16 v[12:15], a[172:175], v[168:171], v[12:15]\n"
    "v_cvt_pk_bf16_f32 v197, v198, v199\n"
    "v_mfma_f32_16x16x32_bf16 v[8:11], a[172:175], v[160:163], v[8:11]\n"
    "ds_read_b64_tr_b16 a[162:163], v244 offset:6144\n"
    "v_mfma_f32_16x16x32_bf16 v[8:11], a[168:171], v[156:159], v[8:11]\n"
    "v_exp_f32_e32 v206, v206\n"
    "v_mfma_f32_16x16x32_bf16 v[4:7], a[168:171], v[148:151], v[4:7]\n"
    "v_exp_f32_e32 v207, v207\n"
    "v_mfma_f32_16x16x32_bf16 v[4:7], a[172:175], v[152:155], v[4:7]\n"
    "v_cvt_pk_bf16_f32 v204, v204, v205\n"
    "v_mfma_f32_16x16x32_bf16 v[0:3], a[172:175], v[144:147], v[0:3]\n"
    "ds_read_b64_tr_b16 a[164:165], v244 offset:36864\n"
    "v_mfma_f32_16x16x32_bf16 v[0:3], a[168:171], v[140:143], v[0:3]\n"
    "s_waitcnt lgkmcnt(7)\n"
    "v_exp_f32_e32 v212, v212\n"
    "v_mfma_f32_16x16x32_bf16 v[20:23], a[176:179], v[140:143], v[20:23]\n"
    "buffer_load_dword v253, off, s[8:11], s82 sc1\n"
    "v_mfma_f32_16x16x32_bf16 v[20:23], a[180:183], v[144:147], v[20:23]\n"
    "ds_read_b64_tr_b16 a[166:167], v244 offset:38912\n"
    "v_mfma_f32_16x16x32_bf16 v[24:27], a[180:183], v[152:155], v[24:27]\n"
    "ds_read_b64_tr_b16 a[168:169], v245 offset:4096\n"
    "v_mfma_f32_16x16x32_bf16 v[24:27], a[176:179], v[148:151], v[24:27]\n"
    "v_exp_f32_e32 v213, v213\n"
    "v_mfma_f32_16x16x32_bf16 v[28:31], a[176:179], v[156:159], v[28:31]\n"
    "ds_read_b64_tr_b16 a[170:171], v245 offset:6144\n"
    "v_mfma_f32_16x16x32_bf16 v[28:31], a[180:183], v[160:163], v[28:31]\n"
    "v_cvt_pk_bf16_f32 v205, v206, v207\n"
    "v_mfma_f32_16x16x32_bf16 v[32:35], a[180:183], v[168:171], v[32:35]\n"
    "v_exp_f32_e32 v214, v214\n"
    "v_mfma_f32_16x16x32_bf16 v[32:35], a[176:179], v[164:167], v[32:35]\n"
    "v_exp_f32_e32 v215, v215\n"
    "v_mfma_f32_16x16x32_bf16 v[36:39], a[176:179], v[172:175], v[36:39]\n"
    "ds_read_b64_tr_b16 a[172:173], v245 offset:36864\n"
    "v_mfma_f32_16x16x32_bf16 v[36:39], a[180:183], v[176:179], v[36:39]\n"
    "s_waitcnt lgkmcnt(6)\n"
    "v_cvt_pk_bf16_f32 v212, v212, v213\n"
    "v_mfma_f32_16x16x32_bf16 v[56:59], a[188:191], v[176:179], v[56:59]\n"
    "ds_read_b64_tr_b16 a[176:177], v246 offset:4096\n"
    "v_mfma_f32_16x16x32_bf16 v[56:59], a[184:187], v[172:175], v[56:59]\n"
    "ds_read_b64_tr_b16 a[174:175], v245 offset:38912\n"
    "v_mfma_f32_16x16x32_bf16 v[52:55], a[184:187], v[164:167], v[52:55]\n"
    "v_cvt_pk_bf16_f32 v213, v214, v215\n"
    "v_mfma_f32_16x16x32_bf16 v[52:55], a[188:191], v[168:171], v[52:55]\n"
    "v_exp_f32_e32 v184, v184\n"
    "v_mfma_f32_16x16x32_bf16 v[48:51], a[188:191], v[160:163], v[48:51]\n"
    "ds_read_b64_tr_b16 a[178:179], v246 offset:6144\n"
    "v_mfma_f32_16x16x32_bf16 v[48:51], a[184:187], v[156:159], v[48:51]\n"
    "v_mfma_f32_16x16x32_bf16 v[44:47], a[184:187], v[148:151], v[44:47]\n"
    "v_exp_f32_e32 v185, v185\n"
    "v_mfma_f32_16x16x32_bf16 v[44:47], a[188:191], v[152:155], v[44:47]\n"
    "v_exp_f32_e32 v186, v186\n"
    "v_mfma_f32_16x16x32_bf16 v[40:43], a[188:191], v[144:147], v[40:43]\n"
    "ds_read_b64_tr_b16 a[180:181], v246 offset:36864\n"
    "v_mfma_f32_16x16x32_bf16 v[40:43], a[184:187], v[140:143], v[40:43]\n"
    "s_waitcnt lgkmcnt(7)\n"
    "v_exp_f32_e32 v187, v187\n"
    "v_mfma_f32_16x16x32_bf16 v[60:63], a[160:163], v[140:143], v[60:63]\n"
    "ds_read_b64_tr_b16 a[184:185], v247 offset:4096\n"
    "v_mfma_f32_16x16x32_bf16 v[60:63], a[164:167], v[144:147], v[60:63]\n"
    "ds_read_b64_tr_b16 a[182:183], v246 offset:38912\n"
    "v_mfma_f32_16x16x32_bf16 v[64:67], a[164:167], v[152:155], v[64:67]\n"
    "ds_read_b64_tr_b16 a[186:187], v247 offset:6144\n"
    "v_mfma_f32_16x16x32_bf16 v[64:67], a[160:163], v[148:151], v[64:67]\n"
    "v_cvt_pk_bf16_f32 v182, v184, v185\n"
    "v_mfma_f32_16x16x32_bf16 v[68:71], a[160:163], v[156:159], v[68:71]\n"
    "v_exp_f32_e32 v192, v192\n"
    "v_mfma_f32_16x16x32_bf16 v[68:71], a[164:167], v[160:163], v[68:71]\n"
    "ds_read_b64_tr_b16 a[188:189], v247 offset:36864\n"
    "v_mfma_f32_16x16x32_bf16 v[72:75], a[164:167], v[168:171], v[72:75]\n"
    "ds_read_b64_tr_b16 a[190:191], v247 offset:38912\n"
    "v_mfma_f32_16x16x32_bf16 v[72:75], a[160:163], v[164:167], v[72:75]\n"
    "s_waitcnt vmcnt(9)\n"
    "v_mfma_f32_16x16x32_bf16 v[76:79], a[160:163], v[172:175], v[76:79]\n"
    "s_barrier\n"
    "v_mfma_f32_16x16x32_bf16 v[76:79], a[164:167], v[176:179], v[76:79]\n"
    "s_waitcnt lgkmcnt(7)\n"
    "s_mov_b32 m0, s42\n"
    "v_mfma_f32_16x16x32_bf16 v[96:99], a[172:175], v[176:179], v[96:99]\n"
    "v_exp_f32_e32 v193, v193\n"
    "v_mfma_f32_16x16x32_bf16 v[96:99], a[168:171], v[172:175], v[96:99]\n"
    "v_cvt_pk_bf16_f32 v183, v186, v187\n"
    "v_mfma_f32_16x16x32_bf16 v[92:95], a[168:171], v[164:167], v[92:95]\n"
    "v_exp_f32_e32 v194, v194\n"
    "v_mfma_f32_16x16x32_bf16 v[92:95], a[172:175], v[168:171], v[92:95]\n"
    "ds_read_b128 a[160:163], v240 offset:16384\n"
    "v_mfma_f32_16x16x32_bf16 v[88:91], a[172:175], v[160:163], v[88:91]\n"
    "ds_write_addtid_b32 v180 offset:0\n"
    "v_mfma_f32_16x16x32_bf16 v[88:91], a[168:171], v[156:159], v[88:91]\n"
    "v_exp_f32_e32 v195, v195\n"
    "v_mfma_f32_16x16x32_bf16 v[84:87], a[168:171], v[148:151], v[84:87]\n"
    "ds_write_addtid_b32 v181 offset:256\n"
    "v_mfma_f32_16x16x32_bf16 v[84:87], a[172:175], v[152:155], v[84:87]\n"
    "ds_write_addtid_b32 v188 offset:1024\n"
    "v_mfma_f32_16x16x32_bf16 v[80:83], a[172:175], v[144:147], v[80:83]\n"
    "v_cvt_pk_bf16_f32 v190, v192, v193\n"
    "v_mfma_f32_16x16x32_bf16 v[80:83], a[168:171], v[140:143], v[80:83]\n"
    "s_waitcnt lgkmcnt(7)\n"
    "v_exp_f32_e32 v200, v200\n"
    "v_mfma_f32_16x16x32_bf16 v[100:103], a[176:179], v[140:143], v[100:103]\n"
    "ds_read_b128 a[164:167], v241 offset:16384\n"
    "v_mfma_f32_16x16x32_bf16 v[100:103], a[180:183], v[144:147], v[100:103]\n"
    "ds_write_addtid_b32 v189 offset:1280\n"
    "v_mfma_f32_16x16x32_bf16 v[104:107], a[180:183], v[152:155], v[104:107]\n"
    "v_exp_f32_e32 v201, v201\n"
    "v_mfma_f32_16x16x32_bf16 v[104:107], a[176:179], v[148:151], v[104:107]\n"
    "ds_read_b128 a[168:171], v240 offset:20480\n"
    "v_mfma_f32_16x16x32_bf16 v[108:111], a[176:179], v[156:159], v[108:111]\n"
    "ds_write_addtid_b32 v196 offset:2048\n"
    "v_mfma_f32_16x16x32_bf16 v[108:111], a[180:183], v[160:163], v[108:111]\n"
    "v_cvt_pk_bf16_f32 v191, v194, v195\n"
    "v_mfma_f32_16x16x32_bf16 v[112:115], a[180:183], v[168:171], v[112:115]\n"
    "ds_write_addtid_b32 v197 offset:2304\n"
    "v_mfma_f32_16x16x32_bf16 v[112:115], a[176:179], v[164:167], v[112:115]\n"
    "v_exp_f32_e32 v202, v202\n"
    "v_mfma_f32_16x16x32_bf16 v[116:119], a[176:179], v[172:175], v[116:119]\n"
    "ds_write_addtid_b32 v204 offset:3072\n"
    "v_mfma_f32_16x16x32_bf16 v[116:119], a[180:183], v[176:179], v[116:119]\n"
    "s_waitcnt lgkmcnt(10)\n"
    "v_exp_f32_e32 v203, v203\n"
    "v_mfma_f32_16x16x32_bf16 v[136:139], a[188:191], v[176:179], v[136:139]\n"
    "ds_read_b128 a[172:175], v241 offset:20480\n"
    "v_mfma_f32_16x16x32_bf16 v[136:139], a[184:187], v[172:175], v[136:139]\n"
    "ds_write_addtid_b32 v205 offset:3328\n"
    "v_cvt_pk_bf16_f32 v198, v200, v201\n"
    "v_mfma_f32_16x16x32_bf16 v[132:135], a[184:187], v[164:167], v[132:135]\n"
    "ds_read_b128 a[176:179], v242 offset:16384\n"
    "v_mfma_f32_16x16x32_bf16 v[132:135], a[188:191], v[168:171], v[132:135]\n"
    "ds_write_addtid_b32 v212 offset:4096\n"
    "v_mfma_f32_16x16x32_bf16 v[128:131], a[188:191], v[160:163], v[128:131]\n"
    "v_exp_f32_e32 v208, v208\n"
    "v_mfma_f32_16x16x32_bf16 v[128:131], a[184:187], v[156:159], v[128:131]\n"
    "ds_write_addtid_b32 v213 offset:4352\n"
    "v_mfma_f32_16x16x32_bf16 v[124:127], a[184:187], v[148:151], v[124:127]\n"
    "v_exp_f32_e32 v209, v209\n"
    "v_mfma_f32_16x16x32_bf16 v[124:127], a[188:191], v[152:155], v[124:127]\n"
    "ds_read_b128 a[180:183], v243 offset:16384\n"
    "v_mfma_f32_16x16x32_bf16 v[120:123], a[188:191], v[144:147], v[120:123]\n"
    "ds_write_addtid_b32 v182 offset:512\n"
    "v_mfma_f32_16x16x32_bf16 v[120:123], a[184:187], v[140:143], v[120:123]\n"
    "s_waitcnt lgkmcnt(12)\n"
    "v_cvt_pk_bf16_f32 v199, v202, v203\n"
    "v_mfma_f32_16x16x32_bf16 v[172:175], a[160:163], a[128:131], v[236:239]\n"
    "v_mfma_f32_16x16x32_bf16 v[172:175], a[164:167], a[132:135], v[172:175]\n"
    "v_exp_f32_e32 v210, v210\n"
    "v_mfma_f32_16x16x32_bf16 v[164:167], a[164:167], a[100:103], v[232:235]\n"
    "ds_write_addtid_b32 v183 offset:768\n"
    "v_mfma_f32_16x16x32_bf16 v[164:167], a[160:163], a[96:99], v[164:167]\n"
    "ds_read_b128 a[184:187], v242 offset:20480\n"
    "v_mfma_f32_16x16x32_bf16 v[156:159], a[160:163], a[64:67], v[228:231]\n"
    "v_exp_f32_e32 v211, v211\n"
    "v_mfma_f32_16x16x32_bf16 v[156:159], a[164:167], a[68:71], v[156:159]\n"
    "ds_write_addtid_b32 v190 offset:1536\n"
    "v_mfma_f32_16x16x32_bf16 v[148:151], a[164:167], a[36:39], v[224:227]\n"
    "v_cvt_pk_bf16_f32 v206, v208, v209\n"
    "v_mfma_f32_16x16x32_bf16 v[148:151], a[160:163], a[32:35], v[148:151]\n"
    "v_mfma_f32_16x16x32_bf16 v[140:143], a[160:163], a[0:3], v[220:223]\n"
    "ds_write_addtid_b32 v191 offset:1792\n"
    "v_mfma_f32_16x16x32_bf16 v[140:143], a[164:167], a[4:7], v[140:143]\n"
    "s_waitcnt lgkmcnt(10)\n"
    "v_exp_f32_e32 v216, v216\n"
    "v_mfma_f32_16x16x32_bf16 v[140:143], a[168:171], a[8:11], v[140:143]\n"
    "ds_read_b128 a[188:191], v243 offset:20480\n"
    "v_mfma_f32_16x16x32_bf16 v[140:143], a[172:175], a[12:15], v[140:143]\n"
    "v_exp_f32_e32 v217, v217\n"
    "v_mfma_f32_16x16x32_bf16 v[148:151], a[172:175], a[44:47], v[148:151]\n"
    "ds_write_addtid_b32 v198 offset:2560\n"
    "v_mfma_f32_16x16x32_bf16 v[148:151], a[168:171], a[40:43], v[148:151]\n"
    "ds_read_b128 a[160:163], v240 offset:18432\n"
    "v_mfma_f32_16x16x32_bf16 v[156:159], a[168:171], a[72:75], v[156:159]\n"
    "v_cvt_pk_bf16_f32 v207, v210, v211\n"
    "v_mfma_f32_16x16x32_bf16 v[156:159], a[172:175], a[76:79], v[156:159]\n"
    "ds_write_addtid_b32 v199 offset:2816\n"
    "v_mfma_f32_16x16x32_bf16 v[164:167], a[172:175], a[108:111], v[164:167]\n"
    "v_exp_f32_e32 v218, v218\n"
    "v_mfma_f32_16x16x32_bf16 v[164:167], a[168:171], a[104:107], v[164:167]\n"
    "ds_write_addtid_b32 v206 offset:3584\n"
    "v_mfma_f32_16x16x32_bf16 v[172:175], a[168:171], a[136:139], v[172:175]\n"
    "v_mfma_f32_16x16x32_bf16 v[172:175], a[172:175], a[140:143], v[172:175]\n"
    "s_waitcnt lgkmcnt(10)\n"
    "v_exp_f32_e32 v219, v219\n"
    "v_mfma_f32_16x16x32_bf16 v[172:175], a[176:179], a[144:147], v[172:175]\n"
    "ds_read_b128 a[164:167], v241 offset:18432\n"
    "v_mfma_f32_16x16x32_bf16 v[172:175], a[180:183], a[148:151], v[172:175]\n"
    "v_cvt_pk_bf16_f32 v214, v216, v217\n"
    "v_mfma_f32_16x16x32_bf16 v[164:167], a[180:183], a[116:119], v[164:167]\n"
    "ds_write_addtid_b32 v207 offset:3840\n"
    "v_mfma_f32_16x16x32_bf16 v[164:167], a[176:179], a[112:115], v[164:167]\n"
    "ds_read_b128 a[168:171], v240 offset:22528\n"
    "v_mfma_f32_16x16x32_bf16 v[156:159], a[176:179], a[80:83], v[156:159]\n"
    "v_cvt_pk_bf16_f32 v215, v218, v219\n"
    "v_mfma_f32_16x16x32_bf16 v[156:159], a[180:183], a[84:87], v[156:159]\n"
    "ds_write_addtid_b32 v214 offset:4608\n"
    "v_mfma_f32_16x16x32_bf16 v[148:151], a[180:183], a[52:55], v[148:151]\n"
    "ds_write_addtid_b32 v215 offset:4864\n"
    "v_mfma_f32_16x16x32_bf16 v[148:151], a[176:179], a[48:51], v[148:151]\n"
    "v_mfma_f32_16x16x32_bf16 v[140:143], a[176:179], a[16:19], v[140:143]\n"
    "ds_read_b128 a[172:175], v241 offset:22528\n"
    "v_mfma_f32_16x16x32_bf16 v[140:143], a[180:183], a[20:23], v[140:143]\n"
    "s_waitcnt lgkmcnt(10)\n"
    "s_waitcnt lgkmcnt(1)\n"
    "v_mfma_f32_16x16x32_bf16 v[140:143], a[184:187], a[24:27], v[140:143]\n"
    "s_barrier\n"
    "v_mfma_f32_16x16x32_bf16 v[140:143], a[188:191], a[28:31], v[140:143]\n"
    "s_add_u32 m0, s43, 0\n"
    "v_mfma_f32_16x16x32_bf16 v[148:151], a[188:191], a[60:63], v[148:151]\n"
    "ds_read_b128 a[176:179], v242 offset:18432\n"
    "v_mfma_f32_16x16x32_bf16 v[148:151], a[184:187], a[56:59], v[148:151]\n"
    "buffer_load_dwordx4 v248, s[8:11], s40 offen offset:0 sc1 lds\n"
    "v_mfma_f32_16x16x32_bf16 v[156:159], a[184:187], a[88:91], v[156:159]\n"
    "ds_read2st64_b32 v[184:185], v249 offset0:0 offset1:1\n"
    "v_mfma_f32_16x16x32_bf16 v[156:159], a[188:191], a[92:95], v[156:159]\n"
    "v_mfma_f32_16x16x32_bf16 v[164:167], a[188:191], a[124:127], v[164:167]\n"
    "ds_read2st64_b32 v[186:187], v249 offset0:2 offset1:3\n"
    "v_mfma_f32_16x16x32_bf16 v[164:167], a[184:187], a[120:123], v[164:167]\n"
    "buffer_load_dwordx4 v248, s[8:11], s40 offen offset:1024 sc1 lds\n"
    "v_mfma_f32_16x16x32_bf16 v[172:175], a[184:187], a[152:155], v[172:175]\n"
    "v_mfma_f32_16x16x32_bf16 v[172:175], a[188:191], a[156:159], v[172:175]\n"
    "v_mfma_f32_16x16x32_bf16 v[176:179], a[160:163], a[128:131], v[236:239]\n"
    "ds_read_b128 a[180:183], v243 offset:18432\n"
    "v_mfma_f32_16x16x32_bf16 v[176:179], a[164:167], a[132:135], v[176:179]\n"
    "buffer_load_dwordx4 v248, s[8:11], s40 offen offset:2048 sc1 lds\n"
    "v_mfma_f32_16x16x32_bf16 v[168:171], a[164:167], a[100:103], v[232:235]\n"
    "ds_read2st64_b32 v[192:193], v249 offset0:4 offset1:5\n"
    "v_mfma_f32_16x16x32_bf16 v[168:171], a[160:163], a[96:99], v[168:171]\n"
    "ds_read_b128 a[184:187], v242 offset:22528\n"
    "v_mfma_f32_16x16x32_bf16 v[160:163], a[160:163], a[64:67], v[228:231]\n"
    "ds_read2st64_b32 v[194:195], v249 offset0:6 offset1:7\n"
    "v_mfma_f32_16x16x32_bf16 v[160:163], a[164:167], a[68:71], v[160:163]\n"
    "buffer_load_dwordx4 v248, s[8:11], s40 offen offset:3072 sc1 lds\n"
    "v_mfma_f32_16x16x32_bf16 v[152:155], a[164:167], a[36:39], v[224:227]\n"
    "s_add_u32 m0, s43, 4096\n"
    "v_exp_f32_e32 v140, v140\n"
    "v_mfma_f32_16x16x32_bf16 v[152:155], a[160:163], a[32:35], v[152:155]\n"
    "s_add_u32 s40, s40, 0x8000\n"
    "v_exp_f32_e32 v141, v141\n"
    "v_mfma_f32_16x16x32_bf16 v[144:147], a[160:163], a[0:3], v[220:223]\n"
    "ds_read2st64_b32 v[200:201], v249 offset0:8 offset1:9\n"
    "v_mfma_f32_16x16x32_bf16 v[144:147], a[164:167], a[4:7], v[144:147]\n"
    "s_waitcnt lgkmcnt(8)\n"
    "buffer_load_dwordx4 v248, s[8:11], s41 offen offset:0 sc1 lds\n"
    "v_mfma_f32_16x16x32_bf16 v[144:147], a[168:171], a[8:11], v[144:147]\n"
    "ds_read_b128 a[188:191], v243 offset:22528\n"
    "v_mfma_f32_16x16x32_bf16 v[144:147], a[172:175], a[12:15], v[144:147]\n"
    "ds_read_b64_tr_b16 a[160:161], v244 offset:8192\n"
    "v_mfma_f32_16x16x32_bf16 v[152:155], a[172:175], a[44:47], v[152:155]\n"
    "ds_read2st64_b32 v[202:203], v249 offset0:10 offset1:11\n"
    "v_mfma_f32_16x16x32_bf16 v[152:155], a[168:171], a[40:43], v[152:155]\n"
    "buffer_load_dwordx4 v248, s[8:11], s41 offen offset:1024 sc1 lds\n"
    "v_mfma_f32_16x16x32_bf16 v[160:163], a[168:171], a[72:75], v[160:163]\n"
    "ds_read_b64_tr_b16 a[162:163], v244 offset:10240\n"
    "v_mfma_f32_16x16x32_bf16 v[160:163], a[172:175], a[76:79], v[160:163]\n"
    "v_exp_f32_e32 v142, v142\n"
    "v_mfma_f32_16x16x32_bf16 v[168:171], a[172:175], a[108:111], v[168:171]\n"
    "ds_read2st64_b32 v[208:209], v249 offset0:12 offset1:13\n"
    "v_mfma_f32_16x16x32_bf16 v[168:171], a[168:171], a[104:107], v[168:171]\n"
    "buffer_load_dwordx4 v248, s[8:11], s41 offen offset:2048 sc1 lds\n"
    "v_mfma_f32_16x16x32_bf16 v[176:179], a[168:171], a[136:139], v[176:179]\n"
    "ds_read_b64_tr_b16 a[164:165], v244 offset:40960\n"
    "v_mfma_f32_16x16x32_bf16 v[176:179], a[172:175], a[140:143], v[176:179]\n"
    "s_waitcnt lgkmcnt(10)\n"
    "ds_read2st64_b32 v[210:211], v249 offset0:14 offset1:15\n"
    "v_mfma_f32_16x16x32_bf16 v[176:179], a[176:179], a[144:147], v[176:179]\n"
    "ds_read_b64_tr_b16 a[168:169], v245 offset:8192\n"
    "v_mfma_f32_16x16x32_bf16 v[176:179], a[180:183], a[148:151], v[176:179]\n"
    "buffer_load_dwordx4 v248, s[8:11], s41 offen offset:3072 sc1 lds\n"
    "v_mfma_f32_16x16x32_bf16 v[168:171], a[180:183], a[116:119], v[168:171]\n"
    "ds_read_b64_tr_b16 a[166:167], v244 offset:43008\n"
    "v_mfma_f32_16x16x32_bf16 v[168:171], a[176:179], a[112:115], v[168:171]\n"
    "s_add_u32 s41, s41, 0x8000\n"
    "v_exp_f32_e32 v143, v143\n"
    "v_mfma_f32_16x16x32_bf16 v[160:163], a[176:179], a[80:83], v[160:163]\n"
    "ds_read_b64_tr_b16 a[170:171], v245 offset:10240\n"
    "v_cvt_pk_bf16_f32 v140, v140, v141\n"
    "v_mfma_f32_16x16x32_bf16 v[160:163], a[180:183], a[84:87], v[160:163]\n"
    "ds_read2st64_b32 v[216:217], v249 offset0:16 offset1:17\n"
    "v_mfma_f32_16x16x32_bf16 v[152:155], a[180:183], a[52:55], v[152:155]\n"
    "v_exp_f32_e32 v148, v148\n"
    "v_mfma_f32_16x16x32_bf16 v[152:155], a[176:179], a[48:51], v[152:155]\n"
    "v_exp_f32_e32 v149, v149\n"
    "v_mfma_f32_16x16x32_bf16 v[144:147], a[176:179], a[16:19], v[144:147]\n"
    "ds_read_b64_tr_b16 a[172:173], v245 offset:40960\n"
    "v_cvt_pk_bf16_f32 v141, v142, v143\n"
    "v_mfma_f32_16x16x32_bf16 v[144:147], a[180:183], a[20:23], v[144:147]\n"
    "s_waitcnt lgkmcnt(11)\n"
    "ds_read2st64_b32 v[218:219], v249 offset0:18 offset1:19\n"
    "v_mfma_f32_16x16x32_bf16 v[144:147], a[184:187], a[24:27], v[144:147]\n"
    "ds_read_b64_tr_b16 a[176:177], v246 offset:8192\n"
    "v_mfma_f32_16x16x32_bf16 v[144:147], a[188:191], a[28:31], v[144:147]\n"
    "ds_read_b64_tr_b16 a[174:175], v245 offset:43008\n"
    "v_mfma_f32_16x16x32_bf16 v[152:155], a[188:191], a[60:63], v[152:155]\n"
    "v_exp_f32_e32 v150, v150\n"
    "v_mfma_f32_16x16x32_bf16 v[152:155], a[184:187], a[56:59], v[152:155]\n"
    "v_exp_f32_e32 v151, v151\n"
    "v_mfma_f32_16x16x32_bf16 v[160:163], a[184:187], a[88:91], v[160:163]\n"
    "s_waitcnt vmcnt(8)\n"
    "v_readfirstlane_b32 s81, v253\n"
    "v_mfma_f32_16x16x32_bf16 v[160:163], a[188:191], a[92:95], v[160:163]\n"
    "ds_read_b64_tr_b16 a[178:179], v246 offset:10240\n"
    "v_mfma_f32_16x16x32_bf16 v[168:171], a[188:191], a[124:127], v[168:171]\n"
    "s_cmp_eq_u32 s81, s39\n"
    "v_cvt_pk_bf16_f32 v148, v148, v149\n"
    "v_mfma_f32_16x16x32_bf16 v[168:171], a[184:187], a[120:123], v[168:171]\n"
    "s_cselect_b32 s88, s83, 2.0\n"
    "v_exp_f32_e32 v156, v156\n"
    "v_mfma_f32_16x16x32_bf16 v[176:179], a[184:187], a[152:155], v[176:179]\n"
    "s_mov_b32 m0, s30\n"
    "ds_read_b64_tr_b16 a[180:181], v246 offset:40960\n"
    "v_mfma_f32_16x16x32_bf16 v[176:179], a[188:191], a[156:159], v[176:179]\n"
    "s_waitcnt lgkmcnt(4)\n"
    "v_exp_f32_e32 v157, v157\n"
    "v_mfma_f32_16x16x32_bf16 a[236:239], a[160:163], v[180:183], a[236:239]\n"
    "buffer_load_dwordx4 v250, s[8:11], s88 offen offset:0 sc1 lds\n"
    "v_mfma_f32_16x16x32_bf16 a[236:239], a[164:167], v[184:187], a[236:239]\n"
    "ds_read_b64_tr_b16 a[182:183], v246 offset:43008\n"
    "v_mfma_f32_16x16x32_bf16 a[240:243], a[164:167], v[192:195], a[240:243]\n"
    "ds_read_b64_tr_b16 a[184:185], v247 offset:8192\n"
    "v_mfma_f32_16x16x32_bf16 a[240:243], a[160:163], v[188:191], a[240:243]\n"
    "v_cvt_pk_bf16_f32 v149, v150, v151\n"
    "v_mfma_f32_16x16x32_bf16 a[244:247], a[160:163], v[196:199], a[244:247]\n"
    "ds_read_b64_tr_b16 a[186:187], v247 offset:10240\n"
    "v_mfma_f32_16x16x32_bf16 a[244:247], a[164:167], v[200:203], a[244:247]\n"
    "v_exp_f32_e32 v158, v158\n"
    "v_mfma_f32_16x16x32_bf16 a[248:251], a[164:167], v[208:211], a[248:251]\n"
    "v_exp_f32_e32 v159, v159\n"
    "v_mfma_f32_16x16x32_bf16 a[248:251], a[160:163], v[204:207], a[248:251]\n"
    "v_cvt_pk_bf16_f32 v156, v156, v157\n"
    "v_mfma_f32_16x16x32_bf16 a[252:255], a[160:163], v[212:215], a[252:255]\n"
    "ds_read_b64_tr_b16 a[188:189], v247 offset:40960\n"
    "v_mfma_f32_16x16x32_bf16 a[252:255], a[164:167], v[216:219], a[252:255]\n"
    "s_waitcnt lgkmcnt(6)\n"
    "v_exp_f32_e32 v164, v164\n"
    "v_mfma_f32_16x16x32_bf16 v[16:19], a[172:175], v[216:219], v[16:19]\n"
    "ds_read_b64_tr_b16 a[160:161], v244 offset:12288\n"
    "v_mfma_f32_16x16x32_bf16 v[16:19], a[168:171], v[212:215], v[16:19]\n"
    "ds_read_b64_tr_b16 a[190:191], v247 offset:43008\n"
    "v_mfma_f32_16x16x32_bf16 v[12:15], a[168:171], v[204:207], v[12:15]\n"
    "v_exp_f32_e32 v165, v165\n"
    "v_mfma_f32_16x16x32_bf16 v[12:15], a[172:175], v[208:211], v[12:15]\n"
    "v_cvt_pk_bf16_f32 v157, v158, v159\n"
    "v_mfma_f32_16x16x32_bf16 v[8:11], a[172:175], v[200:203], v[8:11]\n"
    "ds_read_b64_tr_b16 a[162:163], v244 offset:14336\n"
    "v_mfma_f32_16x16x32_bf16 v[8:11], a[168:171], v[196:199], v[8:11]\n"
    "v_exp_f32_e32 v166, v166\n"
    "v_mfma_f32_16x16x32_bf16 v[4:7], a[168:171], v[188:191], v[4:7]\n"
    "v_exp_f32_e32 v167, v167\n"
    "v_mfma_f32_16x16x32_bf16 v[4:7], a[172:175], v[192:195], v[4:7]\n"
    "v_cvt_pk_bf16_f32 v164, v164, v165\n"
    "v_mfma_f32_16x16x32_bf16 v[0:3], a[172:175], v[184:187], v[0:3]\n"
    "ds_read_b64_tr_b16 a[164:165], v244 offset:45056\n"
    "v_mfma_f32_16x16x32_bf16 v[0:3], a[168:171], v[180:183], v[0:3]\n"
    "s_waitcnt lgkmcnt(7)\n"
    "v_exp_f32_e32 v172, v172\n"
    "v_mfma_f32_16x16x32_bf16 v[20:23], a[176:179], v[180:183], v[20:23]\n"
    "v_mfma_f32_16x16x32_bf16 v[20:23], a[180:183], v[184:187], v[20:23]\n"
    "ds_read_b64_tr_b16 a[166:167], v244 offset:47104\n"
    "v_mfma_f32_16x16x32_bf16 v[24:27], a[180:183], v[192:195], v[24:27]\n"
    "ds_read_b64_tr_b16 a[168:169], v245 offset:12288\n"
    "v_mfma_f32_16x16x32_bf16 v[24:27], a[176:179], v[188:191], v[24:27]\n"
    "v_exp_f32_e32 v173, v173\n"
    "v_mfma_f32_16x16x32_bf16 v[28:31], a[176:179], v[196:199], v[28:31]\n"
    "ds_read_b64_tr_b16 a[170:171], v245 offset:14336\n"
    "v_mfma_f32_16x16x32_bf16 v[28:31], a[180:183], v[200:203], v[28:31]\n"
    "v_cvt_pk_bf16_f32 v165, v166, v167\n"
    "v_mfma_f32_16x16x32_bf16 v[32:35], a[180:183], v[208:211], v[32:35]\n"
    "v_exp_f32_e32 v174, v174\n"
    "v_mfma_f32_16x16x32_bf16 v[32:35], a[176:179], v[204:207], v[32:35]\n"
    "v_exp_f32_e32 v175, v175\n"
    "v_mfma_f32_16x16x32_bf16 v[36:39], a[176:179], v[212:215], v[36:39]\n"
    "ds_read_b64_tr_b16 a[172:173], v245 offset:45056\n"
    "v_mfma_f32_16x16x32_bf16 v[36:39], a[180:183], v[216:219], v[36:39]\n"
    "s_waitcnt lgkmcnt(6)\n"
    "v_cvt_pk_bf16_f32 v172, v172, v173\n"
    "v_mfma_f32_16x16x32_bf16 v[56:59], a[188:191], v[216:219], v[56:59]\n"
    ".long 0xbedf00ff\n"
    ".Lat_w5:\n"
    "s_setpc_b64 s[2:3]\n"
    "ds_read_b64_tr_b16 a[176:177], v246 offset:12288\n"
    "v_mfma_f32_16x16x32_bf16 v[56:59], a[184:187], v[212:215], v[56:59]\n"
    "ds_read_b64_tr_b16 a[174:175], v245 offset:47104\n"
    "v_mfma_f32_16x16x32_bf16 v[52:55], a[184:187], v[204:207], v[52:55]\n"
    "v_cvt_pk_bf16_f32 v173, v174, v175\n"
    "v_mfma_f32_16x16x32_bf16 v[52:55], a[188:191], v[208:211], v[52:55]\n"
    "v_exp_f32_e32 v144, v144\n"
    "v_mfma_f32_16x16x32_bf16 v[48:51], a[188:191], v[200:203], v[48:51]\n"
    "ds_read_b64_tr_b16 a[178:179], v246 offset:14336\n"
    "v_mfma_f32_16x16x32_bf16 v[48:51], a[184:187], v[196:199], v[48:51]\n"
    "v_mfma_f32_16x16x32_bf16 v[44:47], a[184:187], v[188:191], v[44:47]\n"
    "v_exp_f32_e32 v145, v145\n"
    "v_mfma_f32_16x16x32_bf16 v[44:47], a[188:191], v[192:195], v[44:47]\n"
    "v_exp_f32_e32 v146, v146\n"
    "v_mfma_f32_16x16x32_bf16 v[40:43], a[188:191], v[184:187], v[40:43]\n"
    "ds_read_b64_tr_b16 a[180:181], v246 offset:45056\n"
    "v_mfma_f32_16x16x32_bf16 v[40:43], a[184:187], v[180:183], v[40:43]\n"
    "s_waitcnt lgkmcnt(7)\n"
    "v_exp_f32_e32 v147, v147\n"
    "v_mfma_f32_16x16x32_bf16 v[60:63], a[160:163], v[180:183], v[60:63]\n"
    "ds_read_b64_tr_b16 a[184:185], v247 offset:12288\n"
    "v_mfma_f32_16x16x32_bf16 v[60:63], a[164:167], v[184:187], v[60:63]\n"
    "ds_read_b64_tr_b16 a[182:183], v246 offset:47104\n"
    "v_mfma_f32_16x16x32_bf16 v[64:67], a[164:167], v[192:195], v[64:67]\n"
    "ds_read_b64_tr_b16 a[186:187], v247 offset:14336\n"
    "v_mfma_f32_16x16x32_bf16 v[64:67], a[160:163], v[188:191], v[64:67]\n"
    "v_cvt_pk_bf16_f32 v142, v144, v145\n"
    "v_mfma_f32_16x16x32_bf16 v[68:71], a[160:163], v[196:199], v[68:71]\n"
    "v_exp_f32_e32 v152, v152\n"
    "v_mfma_f32_16x16x32_bf16 v[68:71], a[164:167], v[200:203], v[68:71]\n"
    "ds_read_b64_tr_b16 a[188:189], v247 offset:45056\n"
    "v_mfma_f32_16x16x32_bf16 v[72:75], a[164:167], v[208:211], v[72:75]\n"
    "ds_read_b64_tr_b16 a[190:191], v247 offset:47104\n"
    "v_mfma_f32_16x16x32_bf16 v[72:75], a[160:163], v[204:207], v[72:75]\n"
    "s_waitcnt vmcnt(10)\n"
    "v_mfma_f32_16x16x32_bf16 v[76:79], a[160:163], v[212:215], v[76:79]\n"
    "s_barrier\n"
    "v_mfma_f32_16x16x32_bf16 v[76:79], a[164:167], v[216:219], v[76:79]\n"
    "s_waitcnt lgkmcnt(7)\n"
    "s_mov_b32 m0, s42\n"
    "v_mfma_f32_16x16x32_bf16 v[96:99], a[172:175], v[216:219], v[96:99]\n"
    "v_exp_f32_e32 v153, v153\n"
    "v_mfma_f32_16x16x32_bf16 v[96:99], a[168:171], v[212:215], v[96:99]\n"
    "v_cvt_pk_bf16_f32 v143, v146, v147\n"
    "v_mfma_f32_16x16x32_bf16 v[92:95], a[168:171], v[204:207], v[92:95]\n"
    "v_exp_f32_e32 v154, v154\n"
    "v_mfma_f32_16x16x32_bf16 v[92:95], a[172:175], v[208:211], v[92:95]\n"
    "ds_read_b128 a[160:163], v240 offset:24576\n"
    "v_mfma_f32_16x16x32_bf16 v[88:91], a[172:175], v[200:203], v[88:91]\n"
    "ds_write_addtid_b32 v140 offset:0\n"
    "v_mfma_f32_16x16x32_bf16 v[88:91], a[168:171], v[196:199], v[88:91]\n"
    "v_exp_f32_e32 v155, v155\n"
    "v_mfma_f32_16x16x32_bf16 v[84:87], a[168:171], v[188:191], v[84:87]\n"
    "ds_write_addtid_b32 v141 offset:256\n"
    "v_mfma_f32_16x16x32_bf16 v[84:87], a[172:175], v[192:195], v[84:87]\n"
    "ds_write_addtid_b32 v148 offset:1024\n"
    "v_mfma_f32_16x16x32_bf16 v[80:83], a[172:175], v[184:187], v[80:83]\n"
    "v_cvt_pk_bf16_f32 v150, v152, v153\n"
    "v_mfma_f32_16x16x32_bf16 v[80:83], a[168:171], v[180:183], v[80:83]\n"
    "s_waitcnt lgkmcnt(7)\n"
    "v_exp_f32_e32 v160, v160\n"
    "v_mfma_f32_16x16x32_bf16 v[100:103], a[176:179], v[180:183], v[100:103]\n"
    "ds_read_b128 a[164:167], v241 offset:24576\n"
    "v_mfma_f32_16x16x32_bf16 v[100:103], a[180:183], v[184:187], v[100:103]\n"
    "ds_write_addtid_b32 v149 offset:1280\n"
    "v_mfma_f32_16x16x32_bf16 v[104:107], a[180:183], v[192:195], v[104:107]\n"
    "v_exp_f32_e32 v161, v161\n"
    "v_mfma_f32_16x16x32_bf16 v[104:107], a[176:179], v[188:191], v[104:107]\n"
    "ds_read_b128 a[168:171], v240 offset:28672\n"
    "v_mfma_f32_16x16x32_bf16 v[108:111], a[176:179], v[196:199], v[108:111]\n"
    "ds_write_addtid_b32 v156 offset:2048\n"
    "v_mfma_f32_16x16x32_bf16 v[108:111], a[180:183], v[200:203], v[108:111]\n"
    "v_cvt_pk_bf16_f32 v151, v154, v155\n"
    "v_mfma_f32_16x16x32_bf16 v[112:115], a[180:183], v[208:211], v[112:115]\n"
    "ds_write_addtid_b32 v157 offset:2304\n"
    "v_mfma_f32_16x16x32_bf16 v[112:115], a[176:179], v[204:207], v[112:115]\n"
    "v_exp_f32_e32 v162, v162\n"
    "v_mfma_f32_16x16x32_bf16 v[116:119], a[176:179], v[212:215], v[116:119]\n"
    "ds_write_addtid_b32 v164 offset:3072\n"
    "v_mfma_f32_16x16x32_bf16 v[116:119], a[180:183], v[216:219], v[116:119]\n"
    "s_waitcnt lgkmcnt(10)\n"
    "v_exp_f32_e32 v163, v163\n"
    "v_mfma_f32_16x16x32_bf16 v[136:139], a[188:191], v[216:219], v[136:139]\n"
    "ds_read_b128 a[172:175], v241 offset:28672\n"
    "v_mfma_f32_16x16x32_bf16 v[136:139], a[184:187], v[212:215], v[136:139]\n"
    "ds_write_addtid_b32 v165 offset:3328\n"
    "v_cvt_pk_bf16_f32 v158, v160, v161\n"
    "v_mfma_f32_16x16x32_bf16 v[132:135], a[184:187], v[204:207], v[132:135]\n"
    "ds_read_b128 a[176:179], v242 offset:24576\n"
    "v_mfma_f32_16x16x32_bf16 v[132:135], a[188:191], v[208:211], v[132:135]\n"
    "ds_write_addtid_b32 v172 offset:4096\n"
    "v_mfma_f32_16x16x32_bf16 v[128:131], a[188:191], v[200:203], v[128:131]\n"
    "v_exp_f32_e32 v168, v168\n"
    "v_mfma_f32_16x16x32_bf16 v[128:131], a[184:187], v[196:199], v[128:131]\n"
    "s_sub_u32 s44, s44, 1\n"
    "ds_write_addtid_b32 v173 offset:4352\n"
    "v_mfma_f32_16x16x32_bf16 v[124:127], a[184:187], v[188:191], v[124:127]\n"
    "s_cmp_eq_u32 s44, s80\n"
    "v_exp_f32_e32 v169, v169\n"
    "v_mfma_f32_16x16x32_bf16 v[124:127], a[188:191], v[192:195], v[124:127]\n"
    "s_cselect_b32 s88, s39, s81\n"
    "ds_read_b128 a[180:183], v243 offset:24576\n"
    "v_mfma_f32_16x16x32_bf16 v[120:123], a[188:191], v[184:187], v[120:123]\n"
    "s_cmp_lg_u32 s88, s39\n"
    "ds_write_addtid_b32 v142 offset:512\n"
    "v_mfma_f32_16x16x32_bf16 v[120:123], a[184:187], v[180:183], v[120:123]\n"
    "s_waitcnt lgkmcnt(12)\n"
    "v_cvt_pk_bf16_f32 v159, v162, v163\n"
    "s_cbranch_scc1 .Lat_loop\n"
    "s_cmp_eq_u32 s44, 0\n"
    "s_cbranch_scc1 .Lat_tail\n"
    "s_cmp_eq_u32 s85, 0\n"
    "s_cbranch_scc1 .Lat_loop\n"
    "s_cmp_lg_u32 s81, s39\n"
    "s_cbranch_scc1 .Lat_lp0\n"
    ".Lat_g1b:\n"
    "v_lshrrev_b32_e32 v228, 2, v250\n"
    "v_add_u32_e32 v254, s30, v228\n"
    "s_waitcnt vmcnt(0)\n"
    "ds_read_b32 v254, v254 offset:960\n"
    "v_lshrrev_b32_e32 v228, 9, v250\n"
    "v_lshl_add_u32 v251, v228, 2, s30\n"
    "v_and_b32_e32 v252, 0xf0, v250\n"
    "v_lshl_add_u32 v252, v228, 8, v252\n"
    "v_bfe_u32 v229, v250, 8, 1\n"
    "v_lshl_add_u32 v252, v229, 3, v252\n"
    "v_and_b32_e32 v239, 0x1f0, v250\n"
    "v_bfe_u32 v228, v250, 2, 4\n"
    "s_mov_b32 s88, 0xcedc\n"
    "v_bfe_u32 v238, s88, v228, 4\n"
    "v_mul_u32_u24_e32 v238, 0x1111, v238\n"
    "v_mov_b64_e32 v[224:225], 0\n"
    "v_mov_b64_e32 v[226:227], 0\n"
    "s_mov_b32 s85, 0\n"
    "s_mov_b32 s82, 2.0\n"
    "s_mov_b32 s83, 2.0\n"
    "s_mov_b32 s89, 4\n"
    "s_waitcnt lgkmcnt(0)\n"
    "v_readlane_b32 s88, v254, 0\n"
    "v_readlane_b32 s87, v254, 1\n"
    "v_readlane_b32 s84, v254, 2\n"
    "v_readlane_b32 s86, v254, 4\n"
    "s_nop 3\n"
    "s_min_u32 s88, s88, s44\n"
    "s_sub_u32 s80, s44, s88\n"
    "s_cmp_eq_u32 s88, 0\n"
    "s_cbranch_scc1 .Lat_g2\n"
    ".Lat_gloop:\n"
    "v_mfma_f32_16x16x32_bf16 v[212:215], a[160:163], a[128:131], v[220:223]\n"
    "ds_write_addtid_b32 v143 offset:768\n"
    "v_mfma_f32_16x16x32_bf16 v[212:215], a[164:167], a[132:135], v[212:215]\n"
    "v_exp_f32_e32 v170, v170\n"
    "v_mfma_f32_16x16x32_bf16 v[204:207], a[164:167], a[100:103], v[220:223]\n"
    "ds_write_addtid_b32 v150 offset:1536\n"
    "v_mfma_f32_16x16x32_bf16 v[204:207], a[160:163], a[96:99], v[204:207]\n"
    "ds_read_b128 a[184:187], v242 offset:28672\n"
    "v_mfma_f32_16x16x32_bf16 v[196:199], a[160:163], a[64:67], v[220:223]\n"
    "v_exp_f32_e32 v171, v171\n"
    "v_mfma_f32_16x16x32_bf16 v[196:199], a[164:167], a[68:71], v[196:199]\n"
    "ds_write_addtid_b32 v151 offset:1792\n"
    "v_cvt_pk_bf16_f32 v166, v168, v169\n"
    "v_mfma_f32_16x16x32_bf16 v[188:191], a[164:167], a[36:39], v[220:223]\n"
    "v_exp_f32_e32 v176, v176\n"
    "v_mfma_f32_16x16x32_bf16 v[188:191], a[160:163], a[32:35], v[188:191]\n"
    "v_exp_f32_e32 v177, v177\n"
    "v_mfma_f32_16x16x32_bf16 v[180:183], a[160:163], a[0:3], v[220:223]\n"
    "ds_write_addtid_b32 v158 offset:2560\n"
    "v_cvt_pk_bf16_f32 v167, v170, v171\n"
    "v_mfma_f32_16x16x32_bf16 v[180:183], a[164:167], a[4:7], v[180:183]\n"
    "s_waitcnt vmcnt(20)\n"
    "v_exp_f32_e32 v178, v178\n"
    "v_smfmac_f32_16x16x64_bf16 v[224:227], a[232:235], a[192:199], v238\n"
    "s_waitcnt lgkmcnt(10)\n"
    "ds_read_b128 a[188:191], v243 offset:28672\n"
    "v_mfma_f32_16x16x32_bf16 v[180:183], a[168:171], a[8:11], v[180:183]\n"
    "s_bitcmp1_b32 s86, 0\n"
    "ds_read_b128 a[160:163], v240 offset:26624\n"
    "v_mfma_f32_16x16x32_bf16 v[180:183], a[172:175], a[12:15], v[180:183]\n"
    "s_cselect_b64 exec, -1, 0\n"
    "v_cvt_pk_bf16_f32 v236, v224, v225\n"
    "v_cvt_pk_bf16_f32 v237, v226, v227\n"
    "v_mfma_f32_16x16x32_bf16 v[188:191], a[172:175], a[44:47], v[188:191]\n"
    "s_cselect_b32 s88, 512, 0\n"
    "v_mov_b64_e32 v[224:225], 0\n"
    "v_mov_b64_e32 v[226:227], 0\n"
    "v_mfma_f32_16x16x32_bf16 v[188:191], a[168:171], a[40:43], v[188:191]\n"
    "buffer_store_dwordx2 v[236:237], v252, s[8:11], s87 offen sc1\n"
    "v_mfma_f32_16x16x32_bf16 v[196:199], a[168:171], a[72:75], v[196:199]\n"
    "s_mov_b64 exec, -1\n"
    "ds_read2_b32 v[228:229], v251 offset0:0 offset1:2\n"
    "v_mfma_f32_16x16x32_bf16 v[196:199], a[172:175], a[76:79], v[196:199]\n"
    "s_add_u32 s87, s87, s88\n"
    "ds_read2_b32 v[230:231], v251 offset0:4 offset1:6\n"
    "v_mfma_f32_16x16x32_bf16 v[204:207], a[172:175], a[108:111], v[204:207]\n"
    "ds_read_b32 v232, v251 offset:32\n"
    "v_mfma_f32_16x16x32_bf16 v[204:207], a[168:171], a[104:107], v[204:207]\n"
    "v_exp_f32_e32 v179, v179\n"
    "v_mfma_f32_16x16x32_bf16 v[212:215], a[168:171], a[136:139], v[212:215]\n"
    "ds_write_addtid_b32 v159 offset:2816\n"
    "v_cvt_pk_bf16_f32 v174, v176, v177\n"
    "v_mfma_f32_16x16x32_bf16 v[212:215], a[172:175], a[140:143], v[212:215]\n"
    "s_waitcnt lgkmcnt(13)\n"
    "ds_read_b128 a[164:167], v241 offset:26624\n"
    "v_mfma_f32_16x16x32_bf16 v[212:215], a[176:179], a[144:147], v[212:215]\n"
    "ds_write_addtid_b32 v166 offset:3584\n"
    "v_cvt_pk_bf16_f32 v175, v178, v179\n"
    "v_mfma_f32_16x16x32_bf16 v[212:215], a[180:183], a[148:151], v[212:215]\n"
    "ds_read_b128 a[168:171], v240 offset:30720\n"
    "v_mfma_f32_16x16x32_bf16 v[204:207], a[180:183], a[116:119], v[204:207]\n"
    "s_waitcnt lgkmcnt(6)\n"
    "ds_write_addtid_b32 v167 offset:3840\n"
    "v_mad_u32_u16 v234, v228, s90, v239 op_sel:[0,0,0,0]\n"
    "v_mfma_f32_16x16x32_bf16 v[204:207], a[176:179], a[112:115], v[204:207]\n"
    "buffer_load_dwordx4 a[192:195], v234, s[20:23], 0 offen sc1\n"
    "v_mad_u32_u16 v235, v228, s90, v239 op_sel:[1,0,0,0]\n"
    "v_mfma_f32_16x16x32_bf16 v[196:199], a[176:179], a[80:83], v[196:199]\n"
    "ds_write_addtid_b32 v174 offset:4608\n"
    "v_mfma_f32_16x16x32_bf16 v[196:199], a[180:183], a[84:87], v[196:199]\n"
    "ds_write_addtid_b32 v175 offset:4864\n"
    "v_mfma_f32_16x16x32_bf16 v[188:191], a[180:183], a[52:55], v[188:191]\n"
    "buffer_load_dwordx4 a[196:199], v235, s[20:23], 0 offen sc1\n"
    "v_mfma_f32_16x16x32_bf16 v[188:191], a[176:179], a[48:51], v[188:191]\n"
    "v_mfma_f32_16x16x32_bf16 v[180:183], a[176:179], a[16:19], v[180:183]\n"
    "s_waitcnt lgkmcnt(0)\n"
    "v_mfma_f32_16x16x32_bf16 v[180:183], a[180:183], a[20:23], v[180:183]\n"
    "s_barrier\n"
    "v_mfma_f32_16x16x32_bf16 v[180:183], a[184:187], a[24:27], v[180:183]\n"
    "s_add_u32 m0, s43, 8192\n"
    "ds_read_b128 a[172:175], v241 offset:30720\n"
    "v_mfma_f32_16x16x32_bf16 v[180:183], a[188:191], a[28:31], v[180:183]\n"
    "ds_read2st64_b32 v[144:145], v249 offset0:0 offset1:1\n"
    "v_mfma_f32_16x16x32_bf16 v[188:191], a[188:191], a[60:63], v[188:191]\n"
    "buffer_load_dwordx4 v248, s[8:11], s40 offen offset:0 sc1 lds\n"
    "v_mfma_f32_16x16x32_bf16 v[188:191], a[184:187], a[56:59], v[188:191]\n"
    "ds_read_b128 a[176:179], v242 offset:26624\n"
    "v_mfma_f32_16x16x32_bf16 v[196:199], a[184:187], a[88:91], v[196:199]\n"
    "v_mfma_f32_16x16x32_bf16 v[196:199], a[188:191], a[92:95], v[196:199]\n"
    "v_mfma_f32_16x16x32_bf16 v[204:207], a[188:191], a[124:127], v[204:207]\n"
    "buffer_load_dwordx4 v248, s[8:11], s40 offen offset:1024 sc1 lds\n"
    "v_mfma_f32_16x16x32_bf16 v[204:207], a[184:187], a[120:123], v[204:207]\n"
    "ds_read2st64_b32 v[146:147], v249 offset0:2 offset1:3\n"
    "v_mfma_f32_16x16x32_bf16 v[212:215], a[184:187], a[152:155], v[212:215]\n"
    "v_mfma_f32_16x16x32_bf16 v[212:215], a[188:191], a[156:159], v[212:215]\n"
    "ds_read_b128 a[180:183], v243 offset:26624\n"
    "v_mfma_f32_16x16x32_bf16 v[216:219], a[160:163], a[128:131], v[220:223]\n"
    "buffer_load_dwordx4 v248, s[8:11], s40 offen offset:2048 sc1 lds\n"
    "v_mfma_f32_16x16x32_bf16 v[216:219], a[164:167], a[132:135], v[216:219]\n"
    "ds_read2st64_b32 v[152:153], v249 offset0:4 offset1:5\n"
    "v_mfma_f32_16x16x32_bf16 v[208:211], a[164:167], a[100:103], v[220:223]\n"
    "v_exp_f32_e32 v180, v180\n"
    "v_mfma_f32_16x16x32_bf16 v[208:211], a[160:163], a[96:99], v[208:211]\n"
    "ds_read_b128 a[184:187], v242 offset:30720\n"
    "v_mfma_f32_16x16x32_bf16 v[200:203], a[160:163], a[64:67], v[220:223]\n"
    "buffer_load_dwordx4 v248, s[8:11], s40 offen offset:3072 sc1 lds\n"
    "v_mfma_f32_16x16x32_bf16 v[200:203], a[164:167], a[68:71], v[200:203]\n"
    "s_add_u32 m0, s43, 12288\n"
    "ds_read2st64_b32 v[154:155], v249 offset0:6 offset1:7\n"
    "v_mfma_f32_16x16x32_bf16 v[192:195], a[164:167], a[36:39], v[220:223]\n"
    "s_add_u32 s40, s40, 0x8000\n"
    "ds_read2st64_b32 v[160:161], v249 offset0:8 offset1:9\n"
    "v_mfma_f32_16x16x32_bf16 v[192:195], a[160:163], a[32:35], v[192:195]\n"
    "ds_read2st64_b32 v[162:163], v249 offset0:10 offset1:11\n"
    "v_mfma_f32_16x16x32_bf16 v[184:187], a[160:163], a[0:3], v[220:223]\n"
    "buffer_load_dwordx4 v248, s[8:11], s41 offen offset:0 sc1 lds\n"
    "v_mfma_f32_16x16x32_bf16 v[184:187], a[164:167], a[4:7], v[184:187]\n"
    "s_waitcnt vmcnt(17)\n"
    "ds_read_b128 a[188:191], v243 offset:30720\n"
    "v_smfmac_f32_16x16x64_bf16 v[224:227], a[232:235], a[200:207], v238\n"
    "s_waitcnt lgkmcnt(10)\n"
    "v_mad_u32_u16 v234, v229, s90, v239 op_sel:[0,0,0,0]\n"
    "v_mfma_f32_16x16x32_bf16 v[184:187], a[168:171], a[8:11], v[184:187]\n"
    "s_bitcmp1_b32 s86, 1\n"
    "buffer_load_dwordx4 a[200:203], v234, s[20:23], 0 offen sc1\n"
    "v_mad_u32_u16 v235, v229, s90, v239 op_sel:[1,0,0,0]\n"
    "v_mfma_f32_16x16x32_bf16 v[184:187], a[172:175], a[12:15], v[184:187]\n"
    "s_cselect_b64 exec, -1, 0\n"
    "v_cvt_pk_bf16_f32 v236, v224, v225\n"
    "v_cvt_pk_bf16_f32 v237, v226, v227\n"
    "v_mfma_f32_16x16x32_bf16 v[192:195], a[172:175], a[44:47], v[192:195]\n"
    "s_cselect_b32 s88, 512, 0\n"
    "v_mov_b64_e32 v[224:225], 0\n"
    "v_mov_b64_e32 v[226:227], 0\n"
    "v_mfma_f32_16x16x32_bf16 v[192:195], a[168:171], a[40:43], v[192:195]\n"
    "buffer_store_dwordx2 v[236:237], v252, s[8:11], s87 offen sc1\n"
    "v_mfma_f32_16x16x32_bf16 v[200:203], a[168:171], a[72:75], v[200:203]\n"
    "s_mov_b64 exec, -1\n"
    "ds_read_b64_tr_b16 a[160:161], v244 offset:16384\n"
    "v_mfma_f32_16x16x32_bf16 v[200:203], a[172:175], a[76:79], v[200:203]\n"
    "s_add_u32 s87, s87, s88\n"
    "ds_read_b64_tr_b16 a[162:163], v244 offset:18432\n"
    "v_mfma_f32_16x16x32_bf16 v[208:211], a[172:175], a[108:111], v[208:211]\n"
    "buffer_load_dwordx4 v248, s[8:11], s41 offen offset:1024 sc1 lds\n"
    "v_mfma_f32_16x16x32_bf16 v[208:211], a[168:171], a[104:107], v[208:211]\n"
    "ds_read_b64_tr_b16 a[164:165], v244 offset:49152\n"
    "v_mfma_f32_16x16x32_bf16 v[216:219], a[168:171], a[136:139], v[216:219]\n"
    "ds_read2st64_b32 v[168:169], v249 offset0:12 offset1:13\n"
    "v_mfma_f32_16x16x32_bf16 v[216:219], a[172:175], a[140:143], v[216:219]\n"
    "s_waitcnt lgkmcnt(10)\n"
    "ds_read2st64_b32 v[170:171], v249 offset0:14 offset1:15\n"
    "v_mfma_f32_16x16x32_bf16 v[216:219], a[176:179], a[144:147], v[216:219]\n"
    "buffer_load_dwordx4 v248, s[8:11], s41 offen offset:2048 sc1 lds\n"
    "v_mfma_f32_16x16x32_bf16 v[216:219], a[180:183], a[148:151], v[216:219]\n"
    "ds_read_b64_tr_b16 a[166:167], v244 offset:51200\n"
    "v_mfma_f32_16x16x32_bf16 v[208:211], a[180:183], a[116:119], v[208:211]\n"
    "ds_read_b64_tr_b16 a[168:169], v245 offset:16384\n"
    "v_mfma_f32_16x16x32_bf16 v[208:211], a[176:179], a[112:115], v[208:211]\n"
    "v_exp_f32_e32 v181, v181\n"
    "v_mfma_f32_16x16x32_bf16 v[200:203], a[176:179], a[80:83], v[200:203]\n"
    "buffer_load_dwordx4 v248, s[8:11], s41 offen offset:3072 sc1 lds\n"
    "v_mfma_f32_16x16x32_bf16 v[200:203], a[180:183], a[84:87], v[200:203]\n"
    "ds_read_b64_tr_b16 a[170:171], v245 offset:18432\n"
    "v_mfma_f32_16x16x32_bf16 v[192:195], a[180:183], a[52:55], v[192:195]\n"
    "s_add_u32 s41, s41, 0x8000\n"
    "ds_read2st64_b32 v[176:177], v249 offset0:16 offset1:17\n"
    "v_mfma_f32_16x16x32_bf16 v[192:195], a[176:179], a[48:51], v[192:195]\n"
    "buffer_load_dwordx4 a[204:207], v235, s[20:23], 0 offen sc1\n"
    "v_mfma_f32_16x16x32_bf16 v[184:187], a[176:179], a[16:19], v[184:187]\n"
    "ds_read_b64_tr_b16 a[172:173], v245 offset:49152\n"
    "v_mfma_f32_16x16x32_bf16 v[184:187], a[180:183], a[20:23], v[184:187]\n"
    "s_waitcnt lgkmcnt(10)\n"
    "ds_read2st64_b32 v[178:179], v249 offset0:18 offset1:19\n"
    "v_mfma_f32_16x16x32_bf16 v[184:187], a[184:187], a[24:27], v[184:187]\n"
    "ds_read_b64_tr_b16 a[176:177], v246 offset:16384\n"
    "v_mfma_f32_16x16x32_bf16 v[184:187], a[188:191], a[28:31], v[184:187]\n"
    "ds_read_b64_tr_b16 a[174:175], v245 offset:51200\n"
    "v_mfma_f32_16x16x32_bf16 v[192:195], a[188:191], a[60:63], v[192:195]\n"
    "v_exp_f32_e32 v182, v182\n"
    "v_mfma_f32_16x16x32_bf16 v[192:195], a[184:187], a[56:59], v[192:195]\n"
    "v_exp_f32_e32 v183, v183\n"
    "v_mfma_f32_16x16x32_bf16 v[200:203], a[184:187], a[88:91], v[200:203]\n"
    "ds_read_b64_tr_b16 a[178:179], v246 offset:18432\n"
    "v_cvt_pk_bf16_f32 v180, v180, v181\n"
    "v_mfma_f32_16x16x32_bf16 v[200:203], a[188:191], a[92:95], v[200:203]\n"
    "v_exp_f32_e32 v188, v188\n"
    "v_mfma_f32_16x16x32_bf16 v[208:211], a[188:191], a[124:127], v[208:211]\n"
    "v_exp_f32_e32 v189, v189\n"
    "v_mfma_f32_16x16x32_bf16 v[208:211], a[184:187], a[120:123], v[208:211]\n"
    "v_cvt_pk_bf16_f32 v181, v182, v183\n"
    "v_mfma_f32_16x16x32_bf16 v[216:219], a[184:187], a[152:155], v[216:219]\n"
    "ds_read_b64_tr_b16 a[180:181], v246 offset:49152\n"
    "v_mfma_f32_16x16x32_bf16 v[216:219], a[188:191], a[156:159], v[216:219]\n"
    "s_waitcnt vmcnt(20)\n"
    "ds_read_b64_tr_b16 a[182:183], v246 offset:51200\n"
    "v_smfmac_f32_16x16x64_bf16 v[224:227], a[232:235], a[208:215], v238\n"
    "s_waitcnt lgkmcnt(5)\n"
    "v_mad_u32_u16 v234, v230, s90, v239 op_sel:[0,0,0,0]\n"
    "v_mfma_f32_16x16x32_bf16 a[236:239], a[160:163], v[140:143], a[236:239]\n"
    "s_bitcmp1_b32 s86, 2\n"
    "buffer_load_dwordx4 a[208:211], v234, s[20:23], 0 offen sc1\n"
    "v_mad_u32_u16 v235, v230, s90, v239 op_sel:[1,0,0,0]\n"
    "v_mfma_f32_16x16x32_bf16 a[236:239], a[164:167], v[144:147], a[236:239]\n"
    "s_cselect_b64 exec, -1, 0\n"
    "v_cvt_pk_bf16_f32 v236, v224, v225\n"
    "v_cvt_pk_bf16_f32 v237, v226, v227\n"
    "v_mfma_f32_16x16x32_bf16 a[240:243], a[164:167], v[152:155], a[240:243]\n"
    "s_cselect_b32 s88, 512, 0\n"
    "v_mov_b64_e32 v[224:225], 0\n"
    "v_mov_b64_e32 v[226:227], 0\n"
    "v_mfma_f32_16x16x32_bf16 a[240:243], a[160:163], v[148:151], a[240:243]\n"
    "buffer_store_dwordx2 v[236:237], v252, s[8:11], s87 offen sc1\n"
    "v_mfma_f32_16x16x32_bf16 a[244:247], a[160:163], v[156:159], a[244:247]\n"
    "s_mov_b64 exec, -1\n"
    "ds_read_b64_tr_b16 a[184:185], v247 offset:16384\n"
    "v_mfma_f32_16x16x32_bf16 a[244:247], a[164:167], v[160:163], a[244:247]\n"
    "s_add_u32 s87, s87, s88\n"
    "ds_read_b64_tr_b16 a[186:187], v247 offset:18432\n"
    "v_mfma_f32_16x16x32_bf16 a[248:251], a[164:167], v[168:171], a[248:251]\n"
    "buffer_load_dwordx4 a[212:215], v235, s[20:23], 0 offen sc1\n"
    "v_mfma_f32_16x16x32_bf16 a[248:251], a[160:163], v[164:167], a[248:251]\n"
    "ds_read_b64_tr_b16 a[188:189], v247 offset:49152\n"
    "v_mfma_f32_16x16x32_bf16 a[252:255], a[160:163], v[172:175], a[252:255]\n"
    "v_exp_f32_e32 v190, v190\n"
    "v_mfma_f32_16x16x32_bf16 a[252:255], a[164:167], v[176:179], a[252:255]\n"
    "s_waitcnt lgkmcnt(6)\n"
    "v_exp_f32_e32 v191, v191\n"
    "v_mfma_f32_16x16x32_bf16 v[16:19], a[172:175], v[176:179], v[16:19]\n"
    "ds_read_b64_tr_b16 a[190:191], v247 offset:51200\n"
    "v_cvt_pk_bf16_f32 v188, v188, v189\n"
    "v_mfma_f32_16x16x32_bf16 v[16:19], a[168:171], v[172:175], v[16:19]\n"
    "ds_read_b64_tr_b16 a[160:161], v244 offset:20480\n"
    "v_mfma_f32_16x16x32_bf16 v[12:15], a[168:171], v[164:167], v[12:15]\n"
    "v_exp_f32_e32 v196, v196\n"
    "v_mfma_f32_16x16x32_bf16 v[12:15], a[172:175], v[168:171], v[12:15]\n"
    "v_exp_f32_e32 v197, v197\n"
    "v_mfma_f32_16x16x32_bf16 v[8:11], a[172:175], v[160:163], v[8:11]\n"
    "ds_read_b64_tr_b16 a[162:163], v244 offset:22528\n"
    "v_cvt_pk_bf16_f32 v189, v190, v191\n"
    "v_mfma_f32_16x16x32_bf16 v[8:11], a[168:171], v[156:159], v[8:11]\n"
    "v_exp_f32_e32 v198, v198\n"
    "v_mfma_f32_16x16x32_bf16 v[4:7], a[168:171], v[148:151], v[4:7]\n"
    "v_exp_f32_e32 v199, v199\n"
    "v_mfma_f32_16x16x32_bf16 v[4:7], a[172:175], v[152:155], v[4:7]\n"
    "v_cvt_pk_bf16_f32 v196, v196, v197\n"
    "v_mfma_f32_16x16x32_bf16 v[0:3], a[172:175], v[144:147], v[0:3]\n"
    "ds_read_b64_tr_b16 a[164:165], v244 offset:53248\n"
    "v_mfma_f32_16x16x32_bf16 v[0:3], a[168:171], v[140:143], v[0:3]\n"
    "s_waitcnt lgkmcnt(7)\n"
    "v_exp_f32_e32 v204, v204\n"
    "v_mfma_f32_16x16x32_bf16 v[20:23], a[176:179], v[140:143], v[20:23]\n"
    "ds_read_b64_tr_b16 a[168:169], v245 offset:20480\n"
    "v_mfma_f32_16x16x32_bf16 v[20:23], a[180:183], v[144:147], v[20:23]\n"
    "ds_read_b64_tr_b16 a[166:167], v244 offset:55296\n"
    "v_mfma_f32_16x16x32_bf16 v[24:27], a[180:183], v[152:155], v[24:27]\n"
    "v_exp_f32_e32 v205, v205\n"
    "v_mfma_f32_16x16x32_bf16 v[24:27], a[176:179], v[148:151], v[24:27]\n"
    "v_cvt_pk_bf16_f32 v197, v198, v199\n"
    "v_mfma_f32_16x16x32_bf16 v[28:31], a[176:179], v[156:159], v[28:31]\n"
    "ds_read_b64_tr_b16 a[170:171], v245 offset:22528\n"
    "v_mfma_f32_16x16x32_bf16 v[28:31], a[180:183], v[160:163], v[28:31]\n"
    "v_exp_f32_e32 v206, v206\n"
    "v_mfma_f32_16x16x32_bf16 v[32:35], a[180:183], v[168:171], v[32:35]\n"
    "v_exp_f32_e32 v207, v207\n"
    "v_mfma_f32_16x16x32_bf16 v[32:35], a[176:179], v[164:167], v[32:35]\n"
    "v_cvt_pk_bf16_f32 v204, v204, v205\n"
    "v_mfma_f32_16x16x32_bf16 v[36:39], a[176:179], v[172:175], v[36:39]\n"
    "ds_read_b64_tr_b16 a[172:173], v245 offset:53248\n"
    "v_mfma_f32_16x16x32_bf16 v[36:39], a[180:183], v[176:179], v[36:39]\n"
    "s_waitcnt vmcnt(20)\n"
    "ds_read_b64_tr_b16 a[174:175], v245 offset:55296\n"
    "v_smfmac_f32_16x16x64_bf16 v[224:227], a[232:235], a[216:223], v238\n"
    "s_waitcnt lgkmcnt(8)\n"
    "v_mad_u32_u16 v234, v231, s90, v239 op_sel:[0,0,0,0]\n"
    "v_mfma_f32_16x16x32_bf16 v[56:59], a[188:191], v[176:179], v[56:59]\n"
    "s_bitcmp1_b32 s86, 3\n"
    "buffer_load_dwordx4 a[216:219], v234, s[20:23], 0 offen sc1\n"
    "v_mad_u32_u16 v235, v231, s90, v239 op_sel:[1,0,0,0]\n"
    "v_mfma_f32_16x16x32_bf16 v[56:59], a[184:187], v[172:175], v[56:59]\n"
    "s_cselect_b64 exec, -1, 0\n"
    "v_cvt_pk_bf16_f32 v236, v224, v225\n"
    "v_cvt_pk_bf16_f32 v237, v226, v227\n"
    "v_mfma_f32_16x16x32_bf16 v[52:55], a[184:187], v[164:167], v[52:55]\n"
    "s_cselect_b32 s88, 512, 0\n"
    "v_mov_b64_e32 v[224:225], 0\n"
    "v_mov_b64_e32 v[226:227], 0\n"
    "v_mfma_f32_16x16x32_bf16 v[52:55], a[188:191], v[168:171], v[52:55]\n"
    "buffer_store_dwordx2 v[236:237], v252, s[8:11], s87 offen sc1\n"
    "v_mfma_f32_16x16x32_bf16 v[48:51], a[188:191], v[160:163], v[48:51]\n"
    "s_mov_b64 exec, -1\n"
    "ds_read_b64_tr_b16 a[176:177], v246 offset:20480\n"
    "v_mfma_f32_16x16x32_bf16 v[48:51], a[184:187], v[156:159], v[48:51]\n"
    "s_add_u32 s87, s87, s88\n"
    "ds_read_b64_tr_b16 a[178:179], v246 offset:22528\n"
    "v_mfma_f32_16x16x32_bf16 v[44:47], a[184:187], v[148:151], v[44:47]\n"
    "buffer_load_dwordx4 a[220:223], v235, s[20:23], 0 offen sc1\n"
    "v_mfma_f32_16x16x32_bf16 v[44:47], a[188:191], v[152:155], v[44:47]\n"
    "ds_read_b64_tr_b16 a[180:181], v246 offset:53248\n"
    "v_mfma_f32_16x16x32_bf16 v[40:43], a[188:191], v[144:147], v[40:43]\n"
    "v_exp_f32_e32 v212, v212\n"
    "v_mfma_f32_16x16x32_bf16 v[40:43], a[184:187], v[140:143], v[40:43]\n"
    "s_waitcnt lgkmcnt(6)\n"
    "v_exp_f32_e32 v213, v213\n"
    "v_mfma_f32_16x16x32_bf16 v[60:63], a[160:163], v[140:143], v[60:63]\n"
    "ds_read_b64_tr_b16 a[182:183], v246 offset:55296\n"
    "v_cvt_pk_bf16_f32 v205, v206, v207\n"
    "v_mfma_f32_16x16x32_bf16 v[60:63], a[164:167], v[144:147], v[60:63]\n"
    "ds_read_b64_tr_b16 a[184:185], v247 offset:20480\n"
    "v_mfma_f32_16x16x32_bf16 v[64:67], a[164:167], v[152:155], v[64:67]\n"
    "ds_read_b64_tr_b16 a[186:187], v247 offset:22528\n"
    "v_mfma_f32_16x16x32_bf16 v[64:67], a[160:163], v[148:151], v[64:67]\n"
    "v_exp_f32_e32 v214, v214\n"
    "v_mfma_f32_16x16x32_bf16 v[68:71], a[160:163], v[156:159], v[68:71]\n"
    "v_exp_f32_e32 v215, v215\n"
    "v_mfma_f32_16x16x32_bf16 v[68:71], a[164:167], v[160:163], v[68:71]\n"
    "ds_read_b64_tr_b16 a[188:189], v247 offset:53248\n"
    "v_cvt_pk_bf16_f32 v212, v212, v213\n"
    "v_mfma_f32_16x16x32_bf16 v[72:75], a[164:167], v[168:171], v[72:75]\n"
    "ds_read_b64_tr_b16 a[190:191], v247 offset:55296\n"
    "v_cvt_pk_bf16_f32 v213, v214, v215\n"
    "v_mfma_f32_16x16x32_bf16 v[72:75], a[160:163], v[164:167], v[72:75]\n"
    "s_waitcnt vmcnt(30)\n"
    "v_mfma_f32_16x16x32_bf16 v[76:79], a[160:163], v[172:175], v[76:79]\n"
    "s_barrier\n"
    "v_mfma_f32_16x16x32_bf16 v[76:79], a[164:167], v[176:179], v[76:79]\n"
    "s_waitcnt lgkmcnt(8)\n"
    "s_mov_b32 m0, s42\n"
    "v_mfma_f32_16x16x32_bf16 v[96:99], a[172:175], v[176:179], v[96:99]\n"
    "v_exp_f32_e32 v184, v184\n"
    "v_mfma_f32_16x16x32_bf16 v[96:99], a[168:171], v[172:175], v[96:99]\n"
    "v_exp_f32_e32 v185, v185\n"
    "v_mfma_f32_16x16x32_bf16 v[92:95], a[168:171], v[164:167], v[92:95]\n"
    "v_exp_f32_e32 v186, v186\n"
    "v_mfma_f32_16x16x32_bf16 v[92:95], a[172:175], v[168:171], v[92:95]\n"
    "ds_read_b128 a[160:163], v240 offset:0\n"
    "ds_write_addtid_b32 v180 offset:0\n"
    "v_mfma_f32_16x16x32_bf16 v[88:91], a[172:175], v[160:163], v[88:91]\n"
    "v_exp_f32_e32 v187, v187\n"
    "v_mfma_f32_16x16x32_bf16 v[88:91], a[168:171], v[156:159], v[88:91]\n"
    "ds_write_addtid_b32 v181 offset:256\n"
    "v_cvt_pk_bf16_f32 v182, v184, v185\n"
    "v_mfma_f32_16x16x32_bf16 v[84:87], a[168:171], v[148:151], v[84:87]\n"
    "v_exp_f32_e32 v192, v192\n"
    "v_mfma_f32_16x16x32_bf16 v[84:87], a[172:175], v[152:155], v[84:87]\n"
    "v_exp_f32_e32 v193, v193\n"
    "v_mfma_f32_16x16x32_bf16 v[80:83], a[172:175], v[144:147], v[80:83]\n"
    "ds_write_addtid_b32 v188 offset:1024\n"
    "v_cvt_pk_bf16_f32 v183, v186, v187\n"
    "v_mfma_f32_16x16x32_bf16 v[80:83], a[168:171], v[140:143], v[80:83]\n"
    "s_waitcnt vmcnt(20)\n"
    "ds_read_b128 a[164:167], v241 offset:0\n"
    "ds_write_addtid_b32 v189 offset:1280\n"
    "v_smfmac_f32_16x16x64_bf16 v[224:227], a[232:235], a[224:231], v238\n"
    "s_waitcnt lgkmcnt(10)\n"
    "ds_write_addtid_b32 v196 offset:2048\n"
    "v_mad_u32_u16 v234, v232, s90, v239 op_sel:[0,0,0,0]\n"
    "v_mfma_f32_16x16x32_bf16 v[100:103], a[176:179], v[140:143], v[100:103]\n"
    "s_bitcmp1_b32 s86, 4\n"
    "buffer_load_dwordx4 a[224:227], v234, s[20:23], 0 offen sc1\n"
    "v_mad_u32_u16 v235, v232, s90, v239 op_sel:[1,0,0,0]\n"
    "v_mfma_f32_16x16x32_bf16 v[100:103], a[180:183], v[144:147], v[100:103]\n"
    "s_cselect_b64 exec, -1, 0\n"
    "v_cvt_pk_bf16_f32 v236, v224, v225\n"
    "v_cvt_pk_bf16_f32 v237, v226, v227\n"
    "v_mfma_f32_16x16x32_bf16 v[104:107], a[180:183], v[152:155], v[104:107]\n"
    "s_cselect_b32 s88, 512, 0\n"
    "v_mov_b64_e32 v[224:225], 0\n"
    "v_mov_b64_e32 v[226:227], 0\n"
    "v_mfma_f32_16x16x32_bf16 v[104:107], a[176:179], v[148:151], v[104:107]\n"
    "buffer_store_dwordx2 v[236:237], v252, s[8:11], s87 offen sc1\n"
    "v_mfma_f32_16x16x32_bf16 v[108:111], a[176:179], v[156:159], v[108:111]\n"
    "s_mov_b64 exec, -1\n"
    "ds_read_b128 a[168:171], v240 offset:4096\n"
    "ds_write_addtid_b32 v197 offset:2304\n"
    "v_mfma_f32_16x16x32_bf16 v[108:111], a[180:183], v[160:163], v[108:111]\n"
    "s_add_u32 s87, s87, s88\n"
    "v_exp_f32_e32 v194, v194\n"
    "v_mfma_f32_16x16x32_bf16 v[112:115], a[180:183], v[168:171], v[112:115]\n"
    "buffer_load_dwordx4 a[228:231], v235, s[20:23], 0 offen sc1\n"
    "ds_write_addtid_b32 v204 offset:3072\n"
    "v_mfma_f32_16x16x32_bf16 v[112:115], a[176:179], v[164:167], v[112:115]\n"
    "ds_write_addtid_b32 v205 offset:3328\n"
    "v_mfma_f32_16x16x32_bf16 v[116:119], a[176:179], v[172:175], v[116:119]\n"
    "v_exp_f32_e32 v195, v195\n"
    "v_mfma_f32_16x16x32_bf16 v[116:119], a[180:183], v[176:179], v[116:119]\n"
    "s_waitcnt lgkmcnt(11)\n"
    "ds_read_b128 a[172:175], v241 offset:4096\n"
    "v_cvt_pk_bf16_f32 v190, v192, v193\n"
    "v_mfma_f32_16x16x32_bf16 v[136:139], a[188:191], v[176:179], v[136:139]\n"
    "v_exp_f32_e32 v200, v200\n"
    "v_mfma_f32_16x16x32_bf16 v[136:139], a[184:187], v[172:175], v[136:139]\n"
    "v_exp_f32_e32 v201, v201\n"
    "v_mfma_f32_16x16x32_bf16 v[132:135], a[184:187], v[164:167], v[132:135]\n"
    "ds_read_b128 a[176:179], v242 offset:0\n"
    "v_cvt_pk_bf16_f32 v191, v194, v195\n"
    "v_mfma_f32_16x16x32_bf16 v[132:135], a[188:191], v[168:171], v[132:135]\n"
    "v_exp_f32_e32 v202, v202\n"
    "v_mfma_f32_16x16x32_bf16 v[128:131], a[188:191], v[160:163], v[128:131]\n"
    "v_exp_f32_e32 v203, v203\n"
    "v_mfma_f32_16x16x32_bf16 v[128:131], a[184:187], v[156:159], v[128:131]\n"
    "ds_write_addtid_b32 v212 offset:4096\n"
    "v_cvt_pk_bf16_f32 v198, v200, v201\n"
    "v_mfma_f32_16x16x32_bf16 v[124:127], a[184:187], v[148:151], v[124:127]\n"
    "v_exp_f32_e32 v208, v208\n"
    "v_mfma_f32_16x16x32_bf16 v[124:127], a[188:191], v[152:155], v[124:127]\n"
    "ds_read_b128 a[180:183], v243 offset:0\n"
    "ds_write_addtid_b32 v213 offset:4352\n"
    "v_mfma_f32_16x16x32_bf16 v[120:123], a[188:191], v[144:147], v[120:123]\n"
    "v_exp_f32_e32 v209, v209\n"
    "v_mfma_f32_16x16x32_bf16 v[120:123], a[184:187], v[140:143], v[120:123]\n"
    "s_waitcnt lgkmcnt(11)\n"
    "ds_write_addtid_b32 v182 offset:512\n"
    "v_cvt_pk_bf16_f32 v199, v202, v203\n"
    "v_mfma_f32_16x16x32_bf16 v[172:175], a[160:163], a[128:131], v[220:223]\n"
    "ds_write_addtid_b32 v183 offset:768\n"
    "v_mfma_f32_16x16x32_bf16 v[172:175], a[164:167], a[132:135], v[172:175]\n"
    "v_exp_f32_e32 v210, v210\n"
    "v_mfma_f32_16x16x32_bf16 v[164:167], a[164:167], a[100:103], v[220:223]\n"
    "ds_write_addtid_b32 v190 offset:1536\n"
    "v_mfma_f32_16x16x32_bf16 v[164:167], a[160:163], a[96:99], v[164:167]\n"
    "ds_read_b128 a[184:187], v242 offset:4096\n"
    "v_mfma_f32_16x16x32_bf16 v[156:159], a[160:163], a[64:67], v[220:223]\n"
    "v_exp_f32_e32 v211, v211\n"
    "v_mfma_f32_16x16x32_bf16 v[156:159], a[164:167], a[68:71], v[156:159]\n"
    "ds_write_addtid_b32 v191 offset:1792\n"
    "v_cvt_pk_bf16_f32 v206, v208, v209\n"
    "v_mfma_f32_16x16x32_bf16 v[148:151], a[164:167], a[36:39], v[220:223]\n"
    "v_exp_f32_e32 v216, v216\n"
    "v_mfma_f32_16x16x32_bf16 v[148:151], a[160:163], a[32:35], v[148:151]\n"
    "v_exp_f32_e32 v217, v217\n"
    "v_mfma_f32_16x16x32_bf16 v[140:143], a[160:163], a[0:3], v[220:223]\n"
    "ds_write_addtid_b32 v198 offset:2560\n"
    "v_cvt_pk_bf16_f32 v207, v210, v211\n"
    "v_mfma_f32_16x16x32_bf16 v[140:143], a[164:167], a[4:7], v[140:143]\n"
    "s_waitcnt vmcnt(20)\n"
    "v_exp_f32_e32 v218, v218\n"
    "v_smfmac_f32_16x16x64_bf16 v[224:227], a[232:235], a[192:199], v238\n"
    "s_waitcnt lgkmcnt(10)\n"
    "ds_read_b128 a[188:191], v243 offset:4096\n"
    "v_mfma_f32_16x16x32_bf16 v[140:143], a[168:171], a[8:11], v[140:143]\n"
    "s_bitcmp1_b32 s86, 5\n"
    "ds_read_b128 a[160:163], v240 offset:2048\n"
    "v_mfma_f32_16x16x32_bf16 v[140:143], a[172:175], a[12:15], v[140:143]\n"
    "s_cselect_b64 exec, -1, 0\n"
    "v_cvt_pk_bf16_f32 v236, v224, v225\n"
    "v_cvt_pk_bf16_f32 v237, v226, v227\n"
    "v_mfma_f32_16x16x32_bf16 v[148:151], a[172:175], a[44:47], v[148:151]\n"
    ".long 0xbedf00ff\n"
    ".Lat_w6:\n"
    "s_setpc_b64 s[2:3]\n"
    "s_cselect_b32 s88, 512, 0\n"
    "v_mov_b64_e32 v[224:225], 0\n"
    "v_mov_b64_e32 v[226:227], 0\n"
    "v_mfma_f32_16x16x32_bf16 v[148:151], a[168:171], a[40:43], v[148:151]\n"
    "buffer_store_dwordx2 v[236:237], v252, s[8:11], s87 offen sc1\n"
    "v_mfma_f32_16x16x32_bf16 v[156:159], a[168:171], a[72:75], v[156:159]\n"
    "s_mov_b64 exec, -1\n"
    "ds_read2_b32 v[228:229], v251 offset0:10 offset1:12\n"
    "v_mfma_f32_16x16x32_bf16 v[156:159], a[172:175], a[76:79], v[156:159]\n"
    "s_add_u32 s87, s87, s88\n"
    "ds_read2_b32 v[230:231], v251 offset0:14 offset1:16\n"
    "v_mfma_f32_16x16x32_bf16 v[164:167], a[172:175], a[108:111], v[164:167]\n"
    "ds_read_b32 v232, v251 offset:72\n"
    "v_mfma_f32_16x16x32_bf16 v[164:167], a[168:171], a[104:107], v[164:167]\n"
    "v_exp_f32_e32 v219, v219\n"
    "v_mfma_f32_16x16x32_bf16 v[172:175], a[168:171], a[136:139], v[172:175]\n"
    "ds_write_addtid_b32 v199 offset:2816\n"
    "v_cvt_pk_bf16_f32 v214, v216, v217\n"
    "v_mfma_f32_16x16x32_bf16 v[172:175], a[172:175], a[140:143], v[172:175]\n"
    "s_waitcnt lgkmcnt(13)\n"
    "ds_read_b128 a[164:167], v241 offset:2048\n"
    "v_mfma_f32_16x16x32_bf16 v[172:175], a[176:179], a[144:147], v[172:175]\n"
    "ds_write_addtid_b32 v206 offset:3584\n"
    "v_cvt_pk_bf16_f32 v215, v218, v219\n"
    "v_mfma_f32_16x16x32_bf16 v[172:175], a[180:183], a[148:151], v[172:175]\n"
    "ds_read_b128 a[168:171], v240 offset:6144\n"
    "v_mfma_f32_16x16x32_bf16 v[164:167], a[180:183], a[116:119], v[164:167]\n"
    "s_waitcnt lgkmcnt(6)\n"
    "ds_write_addtid_b32 v207 offset:3840\n"
    "v_mad_u32_u16 v234, v228, s90, v239 op_sel:[0,0,0,0]\n"
    "v_mfma_f32_16x16x32_bf16 v[164:167], a[176:179], a[112:115], v[164:167]\n"
    "buffer_load_dwordx4 a[192:195], v234, s[20:23], 0 offen sc1\n"
    "v_mad_u32_u16 v235, v228, s90, v239 op_sel:[1,0,0,0]\n"
    "v_mfma_f32_16x16x32_bf16 v[156:159], a[176:179], a[80:83], v[156:159]\n"
    "ds_write_addtid_b32 v214 offset:4608\n"
    "v_mfma_f32_16x16x32_bf16 v[156:159], a[180:183], a[84:87], v[156:159]\n"
    "ds_write_addtid_b32 v215 offset:4864\n"
    "v_mfma_f32_16x16x32_bf16 v[148:151], a[180:183], a[52:55], v[148:151]\n"
    "buffer_load_dwordx4 a[196:199], v235, s[20:23], 0 offen sc1\n"
    "v_mfma_f32_16x16x32_bf16 v[148:151], a[176:179], a[48:51], v[148:151]\n"
    "v_mfma_f32_16x16x32_bf16 v[140:143], a[176:179], a[16:19], v[140:143]\n"
    "s_waitcnt lgkmcnt(0)\n"
    "v_mfma_f32_16x16x32_bf16 v[140:143], a[180:183], a[20:23], v[140:143]\n"
    "s_barrier\n"
    "v_mfma_f32_16x16x32_bf16 v[140:143], a[184:187], a[24:27], v[140:143]\n"
    "s_add_u32 m0, s43, 16384\n"
    "ds_read_b128 a[172:175], v241 offset:6144\n"
    "v_mfma_f32_16x16x32_bf16 v[140:143], a[188:191], a[28:31], v[140:143]\n"
    "ds_read2st64_b32 v[184:185], v249 offset0:0 offset1:1\n"
    "v_mfma_f32_16x16x32_bf16 v[148:151], a[188:191], a[60:63], v[148:151]\n"
    "buffer_load_dwordx4 v248, s[8:11], s40 offen offset:0 sc1 lds\n"
    "v_mfma_f32_16x16x32_bf16 v[148:151], a[184:187], a[56:59], v[148:151]\n"
    "ds_read_b128 a[176:179], v242 offset:2048\n"
    "v_mfma_f32_16x16x32_bf16 v[156:159], a[184:187], a[88:91], v[156:159]\n"
    "v_mfma_f32_16x16x32_bf16 v[156:159], a[188:191], a[92:95], v[156:159]\n"
    "v_mfma_f32_16x16x32_bf16 v[164:167], a[188:191], a[124:127], v[164:167]\n"
    "buffer_load_dwordx4 v248, s[8:11], s40 offen offset:1024 sc1 lds\n"
    "v_mfma_f32_16x16x32_bf16 v[164:167], a[184:187], a[120:123], v[164:167]\n"
    "ds_read2st64_b32 v[186:187], v249 offset0:2 offset1:3\n"
    "v_mfma_f32_16x16x32_bf16 v[172:175], a[184:187], a[152:155], v[172:175]\n"
    "v_mfma_f32_16x16x32_bf16 v[172:175], a[188:191], a[156:159], v[172:175]\n"
    "ds_read_b128 a[180:183], v243 offset:2048\n"
    "v_mfma_f32_16x16x32_bf16 v[176:179], a[160:163], a[128:131], v[220:223]\n"
    "buffer_load_dwordx4 v248, s[8:11], s40 offen offset:2048 sc1 lds\n"
    "v_mfma_f32_16x16x32_bf16 v[176:179], a[164:167], a[132:135], v[176:179]\n"
    "ds_read2st64_b32 v[192:193], v249 offset0:4 offset1:5\n"
    "v_mfma_f32_16x16x32_bf16 v[168:171], a[164:167], a[100:103], v[220:223]\n"
    "v_exp_f32_e32 v140, v140\n"
    "v_mfma_f32_16x16x32_bf16 v[168:171], a[160:163], a[96:99], v[168:171]\n"
    "ds_read_b128 a[184:187], v242 offset:6144\n"
    "v_mfma_f32_16x16x32_bf16 v[160:163], a[160:163], a[64:67], v[220:223]\n"
    "buffer_load_dwordx4 v248, s[8:11], s40 offen offset:3072 sc1 lds\n"
    "v_mfma_f32_16x16x32_bf16 v[160:163], a[164:167], a[68:71], v[160:163]\n"
    "s_add_u32 m0, s43, 20480\n"
    "ds_read2st64_b32 v[194:195], v249 offset0:6 offset1:7\n"
    "v_mfma_f32_16x16x32_bf16 v[152:155], a[164:167], a[36:39], v[220:223]\n"
    "s_add_u32 s40, s40, 0x8000\n"
    "ds_read2st64_b32 v[200:201], v249 offset0:8 offset1:9\n"
    "v_mfma_f32_16x16x32_bf16 v[152:155], a[160:163], a[32:35], v[152:155]\n"
    "ds_read2st64_b32 v[202:203], v249 offset0:10 offset1:11\n"
    "v_mfma_f32_16x16x32_bf16 v[144:147], a[160:163], a[0:3], v[220:223]\n"
    "buffer_load_dwordx4 v248, s[8:11], s41 offen offset:0 sc1 lds\n"
    "v_mfma_f32_16x16x32_bf16 v[144:147], a[164:167], a[4:7], v[144:147]\n"
    "s_waitcnt vmcnt(17)\n"
    "ds_read_b128 a[188:191], v243 offset:6144\n"
    "v_smfmac_f32_16x16x64_bf16 v[224:227], a[232:235], a[200:207], v238\n"
    "s_waitcnt lgkmcnt(10)\n"
    "v_mad_u32_u16 v234, v229, s90, v239 op_sel:[0,0,0,0]\n"
    "v_mfma_f32_16x16x32_bf16 v[144:147], a[168:171], a[8:11], v[144:147]\n"
    "s_bitcmp1_b32 s86, 6\n"
    "buffer_load_dwordx4 a[200:203], v234, s[20:23], 0 offen sc1\n"
    "v_mad_u32_u16 v235, v229, s90, v239 op_sel:[1,0,0,0]\n"
    "v_mfma_f32_16x16x32_bf16 v[144:147], a[172:175], a[12:15], v[144:147]\n"
    "s_cselect_b64 exec, -1, 0\n"
    "v_cvt_pk_bf16_f32 v236, v224, v225\n"
    "v_cvt_pk_bf16_f32 v237, v226, v227\n"
    "v_mfma_f32_16x16x32_bf16 v[152:155], a[172:175], a[44:47], v[152:155]\n"
    "s_cselect_b32 s88, 512, 0\n"
    "v_mov_b64_e32 v[224:225], 0\n"
    "v_mov_b64_e32 v[226:227], 0\n"
    "v_mfma_f32_16x16x32_bf16 v[152:155], a[168:171], a[40:43], v[152:155]\n"
    "buffer_store_dwordx2 v[236:237], v252, s[8:11], s87 offen sc1\n"
    "v_mfma_f32_16x16x32_bf16 v[160:163], a[168:171], a[72:75], v[160:163]\n"
    "s_mov_b64 exec, -1\n"
    "ds_read_b64_tr_b16 a[160:161], v244 offset:24576\n"
    "v_mfma_f32_16x16x32_bf16 v[160:163], a[172:175], a[76:79], v[160:163]\n"
    "s_add_u32 s87, s87, s88\n"
    "ds_read_b64_tr_b16 a[162:163], v244 offset:26624\n"
    "v_mfma_f32_16x16x32_bf16 v[168:171], a[172:175], a[108:111], v[168:171]\n"
    "buffer_load_dwordx4 v248, s[8:11], s41 offen offset:1024 sc1 lds\n"
    "v_mfma_f32_16x16x32_bf16 v[168:171], a[168:171], a[104:107], v[168:171]\n"
    "ds_read_b64_tr_b16 a[164:165], v244 offset:57344\n"
    "v_mfma_f32_16x16x32_bf16 v[176:179], a[168:171], a[136:139], v[176:179]\n"
    "ds_read2st64_b32 v[208:209], v249 offset0:12 offset1:13\n"
    "v_mfma_f32_16x16x32_bf16 v[176:179], a[172:175], a[140:143], v[176:179]\n"
    "s_waitcnt lgkmcnt(10)\n"
    "ds_read2st64_b32 v[210:211], v249 offset0:14 offset1:15\n"
    "v_mfma_f32_16x16x32_bf16 v[176:179], a[176:179], a[144:147], v[176:179]\n"
    "buffer_load_dwordx4 v248, s[8:11], s41 offen offset:2048 sc1 lds\n"
    "v_mfma_f32_16x16x32_bf16 v[176:179], a[180:183], a[148:151], v[176:179]\n"
    "ds_read_b64_tr_b16 a[166:167], v244 offset:59392\n"
    "v_mfma_f32_16x16x32_bf16 v[168:171], a[180:183], a[116:119], v[168:171]\n"
    "ds_read_b64_tr_b16 a[168:169], v245 offset:24576\n"
    "v_mfma_f32_16x16x32_bf16 v[168:171], a[176:179], a[112:115], v[168:171]\n"
    "v_exp_f32_e32 v141, v141\n"
    "v_mfma_f32_16x16x32_bf16 v[160:163], a[176:179], a[80:83], v[160:163]\n"
    "buffer_load_dwordx4 v248, s[8:11], s41 offen offset:3072 sc1 lds\n"
    "v_mfma_f32_16x16x32_bf16 v[160:163], a[180:183], a[84:87], v[160:163]\n"
    "ds_read_b64_tr_b16 a[170:171], v245 offset:26624\n"
    "v_mfma_f32_16x16x32_bf16 v[152:155], a[180:183], a[52:55], v[152:155]\n"
    "s_add_u32 s41, s41, 0x8000\n"
    "ds_read2st64_b32 v[216:217], v249 offset0:16 offset1:17\n"
    "v_mfma_f32_16x16x32_bf16 v[152:155], a[176:179], a[48:51], v[152:155]\n"
    "buffer_load_dwordx4 a[204:207], v235, s[20:23], 0 offen sc1\n"
    "v_mfma_f32_16x16x32_bf16 v[144:147], a[176:179], a[16:19], v[144:147]\n"
    "ds_read_b64_tr_b16 a[172:173], v245 offset:57344\n"
    "v_mfma_f32_16x16x32_bf16 v[144:147], a[180:183], a[20:23], v[144:147]\n"
    "s_waitcnt lgkmcnt(10)\n"
    "ds_read2st64_b32 v[218:219], v249 offset0:18 offset1:19\n"
    "v_mfma_f32_16x16x32_bf16 v[144:147], a[184:187], a[24:27], v[144:147]\n"
    "ds_read_b64_tr_b16 a[176:177], v246 offset:24576\n"
    "v_mfma_f32_16x16x32_bf16 v[144:147], a[188:191], a[28:31], v[144:147]\n"
    "ds_read_b64_tr_b16 a[174:175], v245 offset:59392\n"
    "v_mfma_f32_16x16x32_bf16 v[152:155], a[188:191], a[60:63], v[152:155]\n"
    "v_exp_f32_e32 v142, v142\n"
    "v_mfma_f32_16x16x32_bf16 v[152:155], a[184:187], a[56:59], v[152:155]\n"
    "v_exp_f32_e32 v143, v143\n"
    "v_mfma_f32_16x16x32_bf16 v[160:163], a[184:187], a[88:91], v[160:163]\n"
    "ds_read_b64_tr_b16 a[178:179], v246 offset:26624\n"
    "v_cvt_pk_bf16_f32 v140, v140, v141\n"
    "v_mfma_f32_16x16x32_bf16 v[160:163], a[188:191], a[92:95], v[160:163]\n"
    "v_exp_f32_e32 v148, v148\n"
    "v_mfma_f32_16x16x32_bf16 v[168:171], a[188:191], a[124:127], v[168:171]\n"
    "v_exp_f32_e32 v149, v149\n"
    "v_mfma_f32_16x16x32_bf16 v[168:171], a[184:187], a[120:123], v[168:171]\n"
    "v_cvt_pk_bf16_f32 v141, v142, v143\n"
    "v_mfma_f32_16x16x32_bf16 v[176:179], a[184:187], a[152:155], v[176:179]\n"
    "ds_read_b64_tr_b16 a[180:181], v246 offset:57344\n"
    "v_mfma_f32_16x16x32_bf16 v[176:179], a[188:191], a[156:159], v[176:179]\n"
    "s_waitcnt vmcnt(20)\n"
    "ds_read_b64_tr_b16 a[182:183], v246 offset:59392\n"
    "v_smfmac_f32_16x16x64_bf16 v[224:227], a[232:235], a[208:215], v238\n"
    "s_waitcnt lgkmcnt(5)\n"
    "v_mad_u32_u16 v234, v230, s90, v239 op_sel:[0,0,0,0]\n"
    "v_mfma_f32_16x16x32_bf16 a[236:239], a[160:163], v[180:183], a[236:239]\n"
    "s_bitcmp1_b32 s86, 7\n"
    "buffer_load_dwordx4 a[208:211], v234, s[20:23], 0 offen sc1\n"
    "v_mad_u32_u16 v235, v230, s90, v239 op_sel:[1,0,0,0]\n"
    "v_mfma_f32_16x16x32_bf16 a[236:239], a[164:167], v[184:187], a[236:239]\n"
    "s_cselect_b64 exec, -1, 0\n"
    "v_cvt_pk_bf16_f32 v236, v224, v225\n"
    "v_cvt_pk_bf16_f32 v237, v226, v227\n"
    "v_mfma_f32_16x16x32_bf16 a[240:243], a[164:167], v[192:195], a[240:243]\n"
    "s_cselect_b32 s88, 512, 0\n"
    "v_mov_b64_e32 v[224:225], 0\n"
    "v_mov_b64_e32 v[226:227], 0\n"
    "v_mfma_f32_16x16x32_bf16 a[240:243], a[160:163], v[188:191], a[240:243]\n"
    "buffer_store_dwordx2 v[236:237], v252, s[8:11], s87 offen sc1\n"
    "v_mfma_f32_16x16x32_bf16 a[244:247], a[160:163], v[196:199], a[244:247]\n"
    "s_mov_b64 exec, -1\n"
    "ds_read_b64_tr_b16 a[184:185], v247 offset:24576\n"
    "v_mfma_f32_16x16x32_bf16 a[244:247], a[164:167], v[200:203], a[244:247]\n"
    "s_add_u32 s87, s87, s88\n"
    "ds_read_b64_tr_b16 a[186:187], v247 offset:26624\n"
    "v_mfma_f32_16x16x32_bf16 a[248:251], a[164:167], v[208:211], a[248:251]\n"
    "buffer_load_dwordx4 a[212:215], v235, s[20:23], 0 offen sc1\n"
    "v_mfma_f32_16x16x32_bf16 a[248:251], a[160:163], v[204:207], a[248:251]\n"
    "ds_read_b64_tr_b16 a[188:189], v247 offset:57344\n"
    "v_mfma_f32_16x16x32_bf16 a[252:255], a[160:163], v[212:215], a[252:255]\n"
    "v_exp_f32_e32 v150, v150\n"
    "v_mfma_f32_16x16x32_bf16 a[252:255], a[164:167], v[216:219], a[252:255]\n"
    "s_waitcnt lgkmcnt(6)\n"
    "v_exp_f32_e32 v151, v151\n"
    "v_mfma_f32_16x16x32_bf16 v[16:19], a[172:175], v[216:219], v[16:19]\n"
    "ds_read_b64_tr_b16 a[190:191], v247 offset:59392\n"
    "v_cvt_pk_bf16_f32 v148, v148, v149\n"
    "v_mfma_f32_16x16x32_bf16 v[16:19], a[168:171], v[212:215], v[16:19]\n"
    "ds_read_b64_tr_b16 a[160:161], v244 offset:28672\n"
    "v_mfma_f32_16x16x32_bf16 v[12:15], a[168:171], v[204:207], v[12:15]\n"
    "v_exp_f32_e32 v156, v156\n"
    "v_mfma_f32_16x16x32_bf16 v[12:15], a[172:175], v[208:211], v[12:15]\n"
    "v_exp_f32_e32 v157, v157\n"
    "v_mfma_f32_16x16x32_bf16 v[8:11], a[172:175], v[200:203], v[8:11]\n"
    "ds_read_b64_tr_b16 a[162:163], v244 offset:30720\n"
    "v_cvt_pk_bf16_f32 v149, v150, v151\n"
    "v_mfma_f32_16x16x32_bf16 v[8:11], a[168:171], v[196:199], v[8:11]\n"
    "v_exp_f32_e32 v158, v158\n"
    "v_mfma_f32_16x16x32_bf16 v[4:7], a[168:171], v[188:191], v[4:7]\n"
    "v_exp_f32_e32 v159, v159\n"
    "v_mfma_f32_16x16x32_bf16 v[4:7], a[172:175], v[192:195], v[4:7]\n"
    "v_cvt_pk_bf16_f32 v156, v156, v157\n"
    "v_mfma_f32_16x16x32_bf16 v[0:3], a[172:175], v[184:187], v[0:3]\n"
    "ds_read_b64_tr_b16 a[164:165], v244 offset:61440\n"
    "v_mfma_f32_16x16x32_bf16 v[0:3], a[168:171], v[180:183], v[0:3]\n"
    "s_waitcnt lgkmcnt(7)\n"
    "v_exp_f32_e32 v164, v164\n"
    "v_mfma_f32_16x16x32_bf16 v[20:23], a[176:179], v[180:183], v[20:23]\n"
    "ds_read_b64_tr_b16 a[168:169], v245 offset:28672\n"
    "v_mfma_f32_16x16x32_bf16 v[20:23], a[180:183], v[184:187], v[20:23]\n"
    "ds_read_b64_tr_b16 a[166:167], v244 offset:63488\n"
    "v_mfma_f32_16x16x32_bf16 v[24:27], a[180:183], v[192:195], v[24:27]\n"
    "v_exp_f32_e32 v165, v165\n"
    "v_mfma_f32_16x16x32_bf16 v[24:27], a[176:179], v[188:191], v[24:27]\n"
    "v_cvt_pk_bf16_f32 v157, v158, v159\n"
    "v_mfma_f32_16x16x32_bf16 v[28:31], a[176:179], v[196:199], v[28:31]\n"
    "ds_read_b64_tr_b16 a[170:171], v245 offset:30720\n"
    "v_mfma_f32_16x16x32_bf16 v[28:31], a[180:183], v[200:203], v[28:31]\n"
    "v_exp_f32_e32 v166, v166\n"
    "v_mfma_f32_16x16x32_bf16 v[32:35], a[180:183], v[208:211], v[32:35]\n"
    "v_exp_f32_e32 v167, v167\n"
    "v_mfma_f32_16x16x32_bf16 v[32:35], a[176:179], v[204:207], v[32:35]\n"
    "v_cvt_pk_bf16_f32 v164, v164, v165\n"
    "v_mfma_f32_16x16x32_bf16 v[36:39], a[176:179], v[212:215], v[36:39]\n"
    "ds_read_b64_tr_b16 a[172:173], v245 offset:61440\n"
    "v_mfma_f32_16x16x32_bf16 v[36:39], a[180:183], v[216:219], v[36:39]\n"
    "s_waitcnt vmcnt(20)\n"
    "ds_read_b64_tr_b16 a[174:175], v245 offset:63488\n"
    "v_smfmac_f32_16x16x64_bf16 v[224:227], a[232:235], a[216:223], v238\n"
    "s_waitcnt lgkmcnt(8)\n"
    "v_mad_u32_u16 v234, v231, s90, v239 op_sel:[0,0,0,0]\n"
    "v_mfma_f32_16x16x32_bf16 v[56:59], a[188:191], v[216:219], v[56:59]\n"
    "s_bitcmp1_b32 s86, 8\n"
    "buffer_load_dwordx4 a[216:219], v234, s[20:23], 0 offen sc1\n"
    "v_mad_u32_u16 v235, v231, s90, v239 op_sel:[1,0,0,0]\n"
    "v_mfma_f32_16x16x32_bf16 v[56:59], a[184:187], v[212:215], v[56:59]\n"
    "s_cselect_b64 exec, -1, 0\n"
    "v_cvt_pk_bf16_f32 v236, v224, v225\n"
    "v_cvt_pk_bf16_f32 v237, v226, v227\n"
    "v_mfma_f32_16x16x32_bf16 v[52:55], a[184:187], v[204:207], v[52:55]\n"
    "s_cselect_b32 s88, 512, 0\n"
    "v_mov_b64_e32 v[224:225], 0\n"
    "v_mov_b64_e32 v[226:227], 0\n"
    "v_mfma_f32_16x16x32_bf16 v[52:55], a[188:191], v[208:211], v[52:55]\n"
    "buffer_store_dwordx2 v[236:237], v252, s[8:11], s87 offen sc1\n"
    "v_mfma_f32_16x16x32_bf16 v[48:51], a[188:191], v[200:203], v[48:51]\n"
    "s_mov_b64 exec, -1\n"
    "ds_read_b64_tr_b16 a[176:177], v246 offset:28672\n"
    "v_mfma_f32_16x16x32_bf16 v[48:51], a[184:187], v[196:199], v[48:51]\n"
    "s_add_u32 s87, s87, s88\n"
    "ds_read_b64_tr_b16 a[178:179], v246 offset:30720\n"
    "v_mfma_f32_16x16x32_bf16 v[44:47], a[184:187], v[188:191], v[44:47]\n"
    "buffer_load_dwordx4 a[220:223], v235, s[20:23], 0 offen sc1\n"
    "v_mfma_f32_16x16x32_bf16 v[44:47], a[188:191], v[192:195], v[44:47]\n"
    "ds_read_b64_tr_b16 a[180:181], v246 offset:61440\n"
    "v_mfma_f32_16x16x32_bf16 v[40:43], a[188:191], v[184:187], v[40:43]\n"
    "v_exp_f32_e32 v172, v172\n"
    "v_mfma_f32_16x16x32_bf16 v[40:43], a[184:187], v[180:183], v[40:43]\n"
    "s_waitcnt lgkmcnt(6)\n"
    "v_exp_f32_e32 v173, v173\n"
    "v_mfma_f32_16x16x32_bf16 v[60:63], a[160:163], v[180:183], v[60:63]\n"
    "ds_read_b64_tr_b16 a[182:183], v246 offset:63488\n"
    "v_cvt_pk_bf16_f32 v165, v166, v167\n"
    "v_mfma_f32_16x16x32_bf16 v[60:63], a[164:167], v[184:187], v[60:63]\n"
    "ds_read_b64_tr_b16 a[184:185], v247 offset:28672\n"
    "v_mfma_f32_16x16x32_bf16 v[64:67], a[164:167], v[192:195], v[64:67]\n"
    "ds_read_b64_tr_b16 a[186:187], v247 offset:30720\n"
    "v_mfma_f32_16x16x32_bf16 v[64:67], a[160:163], v[188:191], v[64:67]\n"
    "v_exp_f32_e32 v174, v174\n"
    "v_mfma_f32_16x16x32_bf16 v[68:71], a[160:163], v[196:199], v[68:71]\n"
    "v_exp_f32_e32 v175, v175\n"
    "v_mfma_f32_16x16x32_bf16 v[68:71], a[164:167], v[200:203], v[68:71]\n"
    "ds_read_b64_tr_b16 a[188:189], v247 offset:61440\n"
    "v_cvt_pk_bf16_f32 v172, v172, v173\n"
    "v_mfma_f32_16x16x32_bf16 v[72:75], a[164:167], v[208:211], v[72:75]\n"
    "ds_read_b64_tr_b16 a[190:191], v247 offset:63488\n"
    "v_cvt_pk_bf16_f32 v173, v174, v175\n"
    "v_mfma_f32_16x16x32_bf16 v[72:75], a[160:163], v[204:207], v[72:75]\n"
    "s_waitcnt vmcnt(30)\n"
    "v_mfma_f32_16x16x32_bf16 v[76:79], a[160:163], v[212:215], v[76:79]\n"
    "s_barrier\n"
    "v_mfma_f32_16x16x32_bf16 v[76:79], a[164:167], v[216:219], v[76:79]\n"
    "s_waitcnt lgkmcnt(8)\n"
    "s_mov_b32 m0, s42\n"
    "v_mfma_f32_16x16x32_bf16 v[96:99], a[172:175], v[216:219], v[96:99]\n"
    "v_exp_f32_e32 v144, v144\n"
    "v_mfma_f32_16x16x32_bf16 v[96:99], a[168:171], v[212:215], v[96:99]\n"
    "v_exp_f32_e32 v145, v145\n"
    "v_mfma_f32_16x16x32_bf16 v[92:95], a[168:171], v[204:207], v[92:95]\n"
    "v_exp_f32_e32 v146, v146\n"
    "v_mfma_f32_16x16x32_bf16 v[92:95], a[172:175], v[208:211], v[92:95]\n"
    "ds_read_b128 a[160:163], v240 offset:8192\n"
    "ds_write_addtid_b32 v140 offset:0\n"
    "v_mfma_f32_16x16x32_bf16 v[88:91], a[172:175], v[200:203], v[88:91]\n"
    "v_exp_f32_e32 v147, v147\n"
    "v_mfma_f32_16x16x32_bf16 v[88:91], a[168:171], v[196:199], v[88:91]\n"
    "ds_write_addtid_b32 v141 offset:256\n"
    "v_cvt_pk_bf16_f32 v142, v144, v145\n"
    "v_mfma_f32_16x16x32_bf16 v[84:87], a[168:171], v[188:191], v[84:87]\n"
    "v_exp_f32_e32 v152, v152\n"
    "v_mfma_f32_16x16x32_bf16 v[84:87], a[172:175], v[192:195], v[84:87]\n"
    "v_exp_f32_e32 v153, v153\n"
    "v_mfma_f32_16x16x32_bf16 v[80:83], a[172:175], v[184:187], v[80:83]\n"
    "ds_write_addtid_b32 v148 offset:1024\n"
    "v_cvt_pk_bf16_f32 v143, v146, v147\n"
    "v_mfma_f32_16x16x32_bf16 v[80:83], a[168:171], v[180:183], v[80:83]\n"
    "s_waitcnt vmcnt(20)\n"
    "ds_read_b128 a[164:167], v241 offset:8192\n"
    "ds_write_addtid_b32 v149 offset:1280\n"
    "v_smfmac_f32_16x16x64_bf16 v[224:227], a[232:235], a[224:231], v238\n"
    "s_waitcnt lgkmcnt(10)\n"
    "ds_write_addtid_b32 v156 offset:2048\n"
    "v_mad_u32_u16 v234, v232, s90, v239 op_sel:[0,0,0,0]\n"
    "v_mfma_f32_16x16x32_bf16 v[100:103], a[176:179], v[180:183], v[100:103]\n"
    "s_bitcmp1_b32 s86, 9\n"
    "buffer_load_dwordx4 a[224:227], v234, s[20:23], 0 offen sc1\n"
    "v_mad_u32_u16 v235, v232, s90, v239 op_sel:[1,0,0,0]\n"
    "v_mfma_f32_16x16x32_bf16 v[100:103], a[180:183], v[184:187], v[100:103]\n"
    "s_cselect_b64 exec, -1, 0\n"
    "v_cvt_pk_bf16_f32 v236, v224, v225\n"
    "v_cvt_pk_bf16_f32 v237, v226, v227\n"
    "v_mfma_f32_16x16x32_bf16 v[104:107], a[180:183], v[192:195], v[104:107]\n"
    "s_cselect_b32 s88, 512, 0\n"
    "v_mov_b64_e32 v[224:225], 0\n"
    "v_mov_b64_e32 v[226:227], 0\n"
    "v_mfma_f32_16x16x32_bf16 v[104:107], a[176:179], v[188:191], v[104:107]\n"
    "buffer_store_dwordx2 v[236:237], v252, s[8:11], s87 offen sc1\n"
    "v_mfma_f32_16x16x32_bf16 v[108:111], a[176:179], v[196:199], v[108:111]\n"
    "s_mov_b64 exec, -1\n"
    "ds_read_b128 a[168:171], v240 offset:12288\n"
    "ds_write_addtid_b32 v157 offset:2304\n"
    "v_mfma_f32_16x16x32_bf16 v[108:111], a[180:183], v[200:203], v[108:111]\n"
    "s_add_u32 s87, s87, s88\n"
    "v_exp_f32_e32 v154, v154\n"
    "v_mfma_f32_16x16x32_bf16 v[112:115], a[180:183], v[208:211], v[112:115]\n"
    "buffer_load_dwordx4 a[228:231], v235, s[20:23], 0 offen sc1\n"
    "ds_write_addtid_b32 v164 offset:3072\n"
    "v_mfma_f32_16x16x32_bf16 v[112:115], a[176:179], v[204:207], v[112:115]\n"
    "ds_write_addtid_b32 v165 offset:3328\n"
    "v_mfma_f32_16x16x32_bf16 v[116:119], a[176:179], v[212:215], v[116:119]\n"
    "v_exp_f32_e32 v155, v155\n"
    "v_mfma_f32_16x16x32_bf16 v[116:119], a[180:183], v[216:219], v[116:119]\n"
    "s_waitcnt lgkmcnt(11)\n"
    "ds_read_b128 a[172:175], v241 offset:12288\n"
    "v_cvt_pk_bf16_f32 v150, v152, v153\n"
    "v_mfma_f32_16x16x32_bf16 v[136:139], a[188:191], v[216:219], v[136:139]\n"
    "v_exp_f32_e32 v160, v160\n"
    "v_mfma_f32_16x16x32_bf16 v[136:139], a[184:187], v[212:215], v[136:139]\n"
    "v_exp_f32_e32 v161, v161\n"
    "v_mfma_f32_16x16x32_bf16 v[132:135], a[184:187], v[204:207], v[132:135]\n"
    "ds_read_b128 a[176:179], v242 offset:8192\n"
    "v_cvt_pk_bf16_f32 v151, v154, v155\n"
    "v_mfma_f32_16x16x32_bf16 v[132:135], a[188:191], v[208:211], v[132:135]\n"
    "v_exp_f32_e32 v162, v162\n"
    "v_mfma_f32_16x16x32_bf16 v[128:131], a[188:191], v[200:203], v[128:131]\n"
    "v_exp_f32_e32 v163, v163\n"
    "v_mfma_f32_16x16x32_bf16 v[128:131], a[184:187], v[196:199], v[128:131]\n"
    "ds_write_addtid_b32 v172 offset:4096\n"
    "v_cvt_pk_bf16_f32 v158, v160, v161\n"
    "v_mfma_f32_16x16x32_bf16 v[124:127], a[184:187], v[188:191], v[124:127]\n"
    "v_exp_f32_e32 v168, v168\n"
    "v_mfma_f32_16x16x32_bf16 v[124:127], a[188:191], v[192:195], v[124:127]\n"
    "ds_read_b128 a[180:183], v243 offset:8192\n"
    "ds_write_addtid_b32 v173 offset:4352\n"
    "v_mfma_f32_16x16x32_bf16 v[120:123], a[188:191], v[184:187], v[120:123]\n"
    "v_exp_f32_e32 v169, v169\n"
    "v_mfma_f32_16x16x32_bf16 v[120:123], a[184:187], v[180:183], v[120:123]\n"
    "s_waitcnt lgkmcnt(11)\n"
    "ds_write_addtid_b32 v142 offset:512\n"
    "v_cvt_pk_bf16_f32 v159, v162, v163\n"
    "v_mfma_f32_16x16x32_bf16 v[212:215], a[160:163], a[128:131], v[220:223]\n"
    "ds_write_addtid_b32 v143 offset:768\n"
    "v_mfma_f32_16x16x32_bf16 v[212:215], a[164:167], a[132:135], v[212:215]\n"
    "v_exp_f32_e32 v170, v170\n"
    "v_mfma_f32_16x16x32_bf16 v[204:207], a[164:167], a[100:103], v[220:223]\n"
    "ds_write_addtid_b32 v150 offset:1536\n"
    "v_mfma_f32_16x16x32_bf16 v[204:207], a[160:163], a[96:99], v[204:207]\n"
    "ds_read_b128 a[184:187], v242 offset:12288\n"
    "v_mfma_f32_16x16x32_bf16 v[196:199], a[160:163], a[64:67], v[220:223]\n"
    "v_exp_f32_e32 v171, v171\n"
    "v_mfma_f32_16x16x32_bf16 v[196:199], a[164:167], a[68:71], v[196:199]\n"
    "ds_write_addtid_b32 v151 offset:1792\n"
    "v_cvt_pk_bf16_f32 v166, v168, v169\n"
    "v_mfma_f32_16x16x32_bf16 v[188:191], a[164:167], a[36:39], v[220:223]\n"
    "v_exp_f32_e32 v176, v176\n"
    "v_mfma_f32_16x16x32_bf16 v[188:191], a[160:163], a[32:35], v[188:191]\n"
    "v_exp_f32_e32 v177, v177\n"
    "v_mfma_f32_16x16x32_bf16 v[180:183], a[160:163], a[0:3], v[220:223]\n"
    "ds_write_addtid_b32 v158 offset:2560\n"
    "v_cvt_pk_bf16_f32 v167, v170, v171\n"
    "v_mfma_f32_16x16x32_bf16 v[180:183], a[164:167], a[4:7], v[180:183]\n"
    "s_waitcnt vmcnt(20)\n"
    "v_exp_f32_e32 v178, v178\n"
    "v_smfmac_f32_16x16x64_bf16 v[224:227], a[232:235], a[192:199], v238\n"
    "s_waitcnt lgkmcnt(10)\n"
    "ds_read_b128 a[188:191], v243 offset:12288\n"
    "v_mfma_f32_16x16x32_bf16 v[180:183], a[168:171], a[8:11], v[180:183]\n"
    "s_bitcmp1_b32 s86, 10\n"
    "ds_read_b128 a[160:163], v240 offset:10240\n"
    "v_mfma_f32_16x16x32_bf16 v[180:183], a[172:175], a[12:15], v[180:183]\n"
    "s_cselect_b64 exec, -1, 0\n"
    "v_cvt_pk_bf16_f32 v236, v224, v225\n"
    "v_cvt_pk_bf16_f32 v237, v226, v227\n"
    "v_mfma_f32_16x16x32_bf16 v[188:191], a[172:175], a[44:47], v[188:191]\n"
    "s_cselect_b32 s88, 512, 0\n"
    "v_mov_b64_e32 v[224:225], 0\n"
    "v_mov_b64_e32 v[226:227], 0\n"
    "v_mfma_f32_16x16x32_bf16 v[188:191], a[168:171], a[40:43], v[188:191]\n"
    "buffer_store_dwordx2 v[236:237], v252, s[8:11], s87 offen sc1\n"
    "v_mfma_f32_16x16x32_bf16 v[196:199], a[168:171], a[72:75], v[196:199]\n"
    "s_mov_b64 exec, -1\n"
    "ds_read2_b32 v[228:229], v251 offset0:20 offset1:22\n"
    "v_mfma_f32_16x16x32_bf16 v[196:199], a[172:175], a[76:79], v[196:199]\n"
    "s_add_u32 s87, s87, s88\n"
    "ds_read2_b32 v[230:231], v251 offset0:24 offset1:26\n"
    "v_mfma_f32_16x16x32_bf16 v[204:207], a[172:175], a[108:111], v[204:207]\n"
    "ds_read_b32 v232, v251 offset:112\n"
    "v_mfma_f32_16x16x32_bf16 v[204:207], a[168:171], a[104:107], v[204:207]\n"
    "v_exp_f32_e32 v179, v179\n"
    "v_mfma_f32_16x16x32_bf16 v[212:215], a[168:171], a[136:139], v[212:215]\n"
    "ds_write_addtid_b32 v159 offset:2816\n"
    "v_cvt_pk_bf16_f32 v174, v176, v177\n"
    "v_mfma_f32_16x16x32_bf16 v[212:215], a[172:175], a[140:143], v[212:215]\n"
    "s_waitcnt lgkmcnt(13)\n"
    "ds_read_b128 a[164:167], v241 offset:10240\n"
    "v_mfma_f32_16x16x32_bf16 v[212:215], a[176:179], a[144:147], v[212:215]\n"
    "ds_write_addtid_b32 v166 offset:3584\n"
    "v_cvt_pk_bf16_f32 v175, v178, v179\n"
    "v_mfma_f32_16x16x32_bf16 v[212:215], a[180:183], a[148:151], v[212:215]\n"
    "ds_read_b128 a[168:171], v240 offset:14336\n"
    "v_mfma_f32_16x16x32_bf16 v[204:207], a[180:183], a[116:119], v[204:207]\n"
    "s_waitcnt lgkmcnt(6)\n"
    "ds_write_addtid_b32 v167 offset:3840\n"
    "v_mad_u32_u16 v234, v228, s90, v239 op_sel:[0,0,0,0]\n"
    "v_mfma_f32_16x16x32_bf16 v[204:207], a[176:179], a[112:115], v[204:207]\n"
    "buffer_load_dwordx4 a[192:195], v234, s[20:23], 0 offen sc1\n"
    "v_mad_u32_u16 v235, v228, s90, v239 op_sel:[1,0,0,0]\n"
    "v_mfma_f32_16x16x32_bf16 v[196:199], a[176:179], a[80:83], v[196:199]\n"
    "ds_write_addtid_b32 v174 offset:4608\n"
    "v_mfma_f32_16x16x32_bf16 v[196:199], a[180:183], a[84:87], v[196:199]\n"
    "ds_write_addtid_b32 v175 offset:4864\n"
    "v_mfma_f32_16x16x32_bf16 v[188:191], a[180:183], a[52:55], v[188:191]\n"
    "buffer_load_dwordx4 a[196:199], v235, s[20:23], 0 offen sc1\n"
    "v_mfma_f32_16x16x32_bf16 v[188:191], a[176:179], a[48:51], v[188:191]\n"
    "v_mfma_f32_16x16x32_bf16 v[180:183], a[176:179], a[16:19], v[180:183]\n"
    "s_waitcnt lgkmcnt(0)\n"
    "v_mfma_f32_16x16x32_bf16 v[180:183], a[180:183], a[20:23], v[180:183]\n"
    "s_barrier\n"
    "v_mfma_f32_16x16x32_bf16 v[180:183], a[184:187], a[24:27], v[180:183]\n"
    "s_add_u32 m0, s43, 24576\n"
    "ds_read_b128 a[172:175], v241 offset:14336\n"
    "v_mfma_f32_16x16x32_bf16 v[180:183], a[188:191], a[28:31], v[180:183]\n"
    "ds_read2st64_b32 v[144:145], v249 offset0:0 offset1:1\n"
    "v_mfma_f32_16x16x32_bf16 v[188:191], a[188:191], a[60:63], v[188:191]\n"
    "buffer_load_dwordx4 v248, s[8:11], s40 offen offset:0 sc1 lds\n"
    "v_mfma_f32_16x16x32_bf16 v[188:191], a[184:187], a[56:59], v[188:191]\n"
    "ds_read_b128 a[176:179], v242 offset:10240\n"
    "v_mfma_f32_16x16x32_bf16 v[196:199], a[184:187], a[88:91], v[196:199]\n"
    "v_mfma_f32_16x16x32_bf16 v[196:199], a[188:191], a[92:95], v[196:199]\n"
    "v_mfma_f32_16x16x32_bf16 v[204:207], a[188:191], a[124:127], v[204:207]\n"
    "buffer_load_dwordx4 v248, s[8:11], s40 offen offset:1024 sc1 lds\n"
    "v_mfma_f32_16x16x32_bf16 v[204:207], a[184:187], a[120:123], v[204:207]\n"
    "ds_read2st64_b32 v[146:147], v249 offset0:2 offset1:3\n"
    "v_mfma_f32_16x16x32_bf16 v[212:215], a[184:187], a[152:155], v[212:215]\n"
    "v_mfma_f32_16x16x32_bf16 v[212:215], a[188:191], a[156:159], v[212:215]\n"
    "ds_read_b128 a[180:183], v243 offset:10240\n"
    "v_mfma_f32_16x16x32_bf16 v[216:219], a[160:163], a[128:131], v[220:223]\n"
    "buffer_load_dwordx4 v248, s[8:11], s40 offen offset:2048 sc1 lds\n"
    "v_mfma_f32_16x16x32_bf16 v[216:219], a[164:167], a[132:135], v[216:219]\n"
    "ds_read2st64_b32 v[152:153], v249 offset0:4 offset1:5\n"
    "v_mfma_f32_16x16x32_bf16 v[208:211], a[164:167], a[100:103], v[220:223]\n"
    "v_exp_f32_e32 v180, v180\n"
    "v_mfma_f32_16x16x32_bf16 v[208:211], a[160:163], a[96:99], v[208:211]\n"
    "ds_read_b128 a[184:187], v242 offset:14336\n"
    "v_mfma_f32_16x16x32_bf16 v[200:203], a[160:163], a[64:67], v[220:223]\n"
    "buffer_load_dwordx4 v248, s[8:11], s40 offen offset:3072 sc1 lds\n"
    "v_mfma_f32_16x16x32_bf16 v[200:203], a[164:167], a[68:71], v[200:203]\n"
    "s_add_u32 m0, s43, 28672\n"
    "ds_read2st64_b32 v[154:155], v249 offset0:6 offset1:7\n"
    "v_mfma_f32_16x16x32_bf16 v[192:195], a[164:167], a[36:39], v[220:223]\n"
    "s_add_u32 s40, s40, 0x8000\n"
    "ds_read2st64_b32 v[160:161], v249 offset0:8 offset1:9\n"
    "v_mfma_f32_16x16x32_bf16 v[192:195], a[160:163], a[32:35], v[192:195]\n"
    "ds_read2st64_b32 v[162:163], v249 offset0:10 offset1:11\n"
    "v_mfma_f32_16x16x32_bf16 v[184:187], a[160:163], a[0:3], v[220:223]\n"
    "buffer_load_dwordx4 v248, s[8:11], s41 offen offset:0 sc1 lds\n"
    "v_mfma_f32_16x16x32_bf16 v[184:187], a[164:167], a[4:7], v[184:187]\n"
    "s_waitcnt vmcnt(17)\n"
    "ds_read_b128 a[188:191], v243 offset:14336\n"
    "v_smfmac_f32_16x16x64_bf16 v[224:227], a[232:235], a[200:207], v238\n"
    "s_waitcnt lgkmcnt(10)\n"
    "v_mad_u32_u16 v234, v229, s90, v239 op_sel:[0,0,0,0]\n"
    "v_mfma_f32_16x16x32_bf16 v[184:187], a[168:171], a[8:11], v[184:187]\n"
    "s_bitcmp1_b32 s86, 11\n"
    "buffer_load_dwordx4 a[200:203], v234, s[20:23], 0 offen sc1\n"
    "v_mad_u32_u16 v235, v229, s90, v239 op_sel:[1,0,0,0]\n"
    "v_mfma_f32_16x16x32_bf16 v[184:187], a[172:175], a[12:15], v[184:187]\n"
    "s_cselect_b64 exec, -1, 0\n"
    "v_cvt_pk_bf16_f32 v236, v224, v225\n"
    "v_cvt_pk_bf16_f32 v237, v226, v227\n"
    "v_mfma_f32_16x16x32_bf16 v[192:195], a[172:175], a[44:47], v[192:195]\n"
    "s_cselect_b32 s88, 512, 0\n"
    "v_mov_b64_e32 v[224:225], 0\n"
    "v_mov_b64_e32 v[226:227], 0\n"
    "v_mfma_f32_16x16x32_bf16 v[192:195], a[168:171], a[40:43], v[192:195]\n"
    "buffer_store_dwordx2 v[236:237], v252, s[8:11], s87 offen sc1\n"
    "v_mfma_f32_16x16x32_bf16 v[200:203], a[168:171], a[72:75], v[200:203]\n"
    "s_mov_b64 exec, -1\n"
    "ds_read_b64_tr_b16 a[160:161], v244 offset:0\n"
    "v_mfma_f32_16x16x32_bf16 v[200:203], a[172:175], a[76:79], v[200:203]\n"
    "s_add_u32 s87, s87, s88\n"
    "ds_read_b64_tr_b16 a[162:163], v244 offset:2048\n"
    "v_mfma_f32_16x16x32_bf16 v[208:211], a[172:175], a[108:111], v[208:211]\n"
    "buffer_load_dwordx4 v248, s[8:11], s41 offen offset:1024 sc1 lds\n"
    "v_mfma_f32_16x16x32_bf16 v[208:211], a[168:171], a[104:107], v[208:211]\n"
    "ds_read_b64_tr_b16 a[164:165], v244 offset:32768\n"
    "v_mfma_f32_16x16x32_bf16 v[216:219], a[168:171], a[136:139], v[216:219]\n"
    "ds_read2st64_b32 v[168:169], v249 offset0:12 offset1:13\n"
    "v_mfma_f32_16x16x32_bf16 v[216:219], a[172:175], a[140:143], v[216:219]\n"
    "s_waitcnt lgkmcnt(10)\n"
    "ds_read2st64_b32 v[170:171], v249 offset0:14 offset1:15\n"
    "v_mfma_f32_16x16x32_bf16 v[216:219], a[176:179], a[144:147], v[216:219]\n"
    "buffer_load_dwordx4 v248, s[8:11], s41 offen offset:2048 sc1 lds\n"
    "v_mfma_f32_16x16x32_bf16 v[216:219], a[180:183], a[148:151], v[216:219]\n"
    "ds_read_b64_tr_b16 a[166:167], v244 offset:34816\n"
    "v_mfma_f32_16x16x32_bf16 v[208:211], a[180:183], a[116:119], v[208:211]\n"
    "ds_read_b64_tr_b16 a[168:169], v245 offset:0\n"
    "v_mfma_f32_16x16x32_bf16 v[208:211], a[176:179], a[112:115], v[208:211]\n"
    "v_exp_f32_e32 v181, v181\n"
    "v_mfma_f32_16x16x32_bf16 v[200:203], a[176:179], a[80:83], v[200:203]\n"
    "buffer_load_dwordx4 v248, s[8:11], s41 offen offset:3072 sc1 lds\n"
    "v_mfma_f32_16x16x32_bf16 v[200:203], a[180:183], a[84:87], v[200:203]\n"
    "ds_read_b64_tr_b16 a[170:171], v245 offset:2048\n"
    "v_mfma_f32_16x16x32_bf16 v[192:195], a[180:183], a[52:55], v[192:195]\n"
    "s_add_u32 s41, s41, 0x8000\n"
    "ds_read2st64_b32 v[176:177], v249 offset0:16 offset1:17\n"
    "v_mfma_f32_16x16x32_bf16 v[192:195], a[176:179], a[48:51], v[192:195]\n"
    "buffer_load_dwordx4 a[204:207], v235, s[20:23], 0 offen sc1\n"
    "v_mfma_f32_16x16x32_bf16 v[184:187], a[176:179], a[16:19], v[184:187]\n"
    "ds_read_b64_tr_b16 a[172:173], v245 offset:32768\n"
    "v_mfma_f32_16x16x32_bf16 v[184:187], a[180:183], a[20:23], v[184:187]\n"
    "s_waitcnt lgkmcnt(10)\n"
    "ds_read2st64_b32 v[178:179], v249 offset0:18 offset1:19\n"
    "v_mfma_f32_16x16x32_bf16 v[184:187], a[184:187], a[24:27], v[184:187]\n"
    "ds_read_b64_tr_b16 a[176:177], v246 offset:0\n"
    "v_mfma_f32_16x16x32_bf16 v[184:187], a[188:191], a[28:31], v[184:187]\n"
    "ds_read_b64_tr_b16 a[174:175], v245 offset:34816\n"
    "v_mfma_f32_16x16x32_bf16 v[192:195], a[188:191], a[60:63], v[192:195]\n"
    "v_exp_f32_e32 v182, v182\n"
    "v_mfma_f32_16x16x32_bf16 v[192:195], a[184:187], a[56:59], v[192:195]\n"
    "v_exp_f32_e32 v183, v183\n"
    "v_mfma_f32_16x16x32_bf16 v[200:203], a[184:187], a[88:91], v[200:203]\n"
    "ds_read_b64_tr_b16 a[178:179], v246 offset:2048\n"
    "v_cvt_pk_bf16_f32 v180, v180, v181\n"
    "v_mfma_f32_16x16x32_bf16 v[200:203], a[188:191], a[92:95], v[200:203]\n"
    "v_exp_f32_e32 v188, v188\n"
    "v_mfma_f32_16x16x32_bf16 v[208:211], a[188:191], a[124:127], v[208:211]\n"
    ".long 0xbedf00ff\n"
    ".Lat_w7:\n"
    "s_setpc_b64 s[2:3]\n"
    "v_exp_f32_e32 v189, v189\n"
    "v_mfma_f32_16x16x32_bf16 v[208:211], a[184:187], a[120:123], v[208:211]\n"
    "v_cvt_pk_bf16_f32 v181, v182, v183\n"
    "v_mfma_f32_16x16x32_bf16 v[216:219], a[184:187], a[152:155], v[216:219]\n"
    "ds_read_b64_tr_b16 a[180:181], v246 offset:32768\n"
    "v_mfma_f32_16x16x32_bf16 v[216:219], a[188:191], a[156:159], v[216:219]\n"
    "s_waitcnt vmcnt(20)\n"
    "ds_read_b64_tr_b16 a[182:183], v246 offset:34816\n"
    "v_smfmac_f32_16x16x64_bf16 v[224:227], a[232:235], a[208:215], v238\n"
    "s_waitcnt lgkmcnt(5)\n"
    "v_mad_u32_u16 v234, v230, s90, v239 op_sel:[0,0,0,0]\n"
    "v_mfma_f32_16x16x32_bf16 a[236:239], a[160:163], v[140:143], a[236:239]\n"
    "s_bitcmp1_b32 s86, 12\n"
    "buffer_load_dwordx4 a[208:211], v234, s[20:23], 0 offen sc1\n"
    "v_mad_u32_u16 v235, v230, s90, v239 op_sel:[1,0,0,0]\n"
    "v_mfma_f32_16x16x32_bf16 a[236:239], a[164:167], v[144:147], a[236:239]\n"
    "s_cselect_b64 exec, -1, 0\n"
    "v_cvt_pk_bf16_f32 v236, v224, v225\n"
    "v_cvt_pk_bf16_f32 v237, v226, v227\n"
    "v_mfma_f32_16x16x32_bf16 a[240:243], a[164:167], v[152:155], a[240:243]\n"
    "s_cselect_b32 s88, 512, 0\n"
    "v_mov_b64_e32 v[224:225], 0\n"
    "v_mov_b64_e32 v[226:227], 0\n"
    "v_mfma_f32_16x16x32_bf16 a[240:243], a[160:163], v[148:151], a[240:243]\n"
    "buffer_store_dwordx2 v[236:237], v252, s[8:11], s87 offen sc1\n"
    "v_mfma_f32_16x16x32_bf16 a[244:247], a[160:163], v[156:159], a[244:247]\n"
    "s_mov_b64 exec, -1\n"
    "ds_read_b64_tr_b16 a[184:185], v247 offset:0\n"
    "v_mfma_f32_16x16x32_bf16 a[244:247], a[164:167], v[160:163], a[244:247]\n"
    "s_add_u32 s87, s87, s88\n"
    "ds_read_b64_tr_b16 a[186:187], v247 offset:2048\n"
    "v_mfma_f32_16x16x32_bf16 a[248:251], a[164:167], v[168:171], a[248:251]\n"
    "buffer_load_dwordx4 a[212:215], v235, s[20:23], 0 offen sc1\n"
    "v_mfma_f32_16x16x32_bf16 a[248:251], a[160:163], v[164:167], a[248:251]\n"
    "ds_read_b64_tr_b16 a[188:189], v247 offset:32768\n"
    "v_mfma_f32_16x16x32_bf16 a[252:255], a[160:163], v[172:175], a[252:255]\n"
    "v_exp_f32_e32 v190, v190\n"
    "v_mfma_f32_16x16x32_bf16 a[252:255], a[164:167], v[176:179], a[252:255]\n"
    "s_waitcnt lgkmcnt(6)\n"
    "v_exp_f32_e32 v191, v191\n"
    "v_mfma_f32_16x16x32_bf16 v[16:19], a[172:175], v[176:179], v[16:19]\n"
    "ds_read_b64_tr_b16 a[190:191], v247 offset:34816\n"
    "v_cvt_pk_bf16_f32 v188, v188, v189\n"
    "v_mfma_f32_16x16x32_bf16 v[16:19], a[168:171], v[172:175], v[16:19]\n"
    "ds_read_b64_tr_b16 a[160:161], v244 offset:4096\n"
    "v_mfma_f32_16x16x32_bf16 v[12:15], a[168:171], v[164:167], v[12:15]\n"
    "v_exp_f32_e32 v196, v196\n"
    "v_mfma_f32_16x16x32_bf16 v[12:15], a[172:175], v[168:171], v[12:15]\n"
    "v_exp_f32_e32 v197, v197\n"
    "v_mfma_f32_16x16x32_bf16 v[8:11], a[172:175], v[160:163], v[8:11]\n"
    "ds_read_b64_tr_b16 a[162:163], v244 offset:6144\n"
    "v_cvt_pk_bf16_f32 v189, v190, v191\n"
    "v_mfma_f32_16x16x32_bf16 v[8:11], a[168:171], v[156:159], v[8:11]\n"
    "v_exp_f32_e32 v198, v198\n"
    "v_mfma_f32_16x16x32_bf16 v[4:7], a[168:171], v[148:151], v[4:7]\n"
    "v_exp_f32_e32 v199, v199\n"
    "v_mfma_f32_16x16x32_bf16 v[4:7], a[172:175], v[152:155], v[4:7]\n"
    "v_cvt_pk_bf16_f32 v196, v196, v197\n"
    "v_mfma_f32_16x16x32_bf16 v[0:3], a[172:175], v[144:147], v[0:3]\n"
    "ds_read_b64_tr_b16 a[164:165], v244 offset:36864\n"
    "v_mfma_f32_16x16x32_bf16 v[0:3], a[168:171], v[140:143], v[0:3]\n"
    "s_waitcnt lgkmcnt(7)\n"
    "v_exp_f32_e32 v204, v204\n"
    "v_mfma_f32_16x16x32_bf16 v[20:23], a[176:179], v[140:143], v[20:23]\n"
    "ds_read_b64_tr_b16 a[168:169], v245 offset:4096\n"
    "v_mfma_f32_16x16x32_bf16 v[20:23], a[180:183], v[144:147], v[20:23]\n"
    "ds_read_b64_tr_b16 a[166:167], v244 offset:38912\n"
    "v_mfma_f32_16x16x32_bf16 v[24:27], a[180:183], v[152:155], v[24:27]\n"
    "v_exp_f32_e32 v205, v205\n"
    "v_mfma_f32_16x16x32_bf16 v[24:27], a[176:179], v[148:151], v[24:27]\n"
    "v_cvt_pk_bf16_f32 v197, v198, v199\n"
    "v_mfma_f32_16x16x32_bf16 v[28:31], a[176:179], v[156:159], v[28:31]\n"
    "ds_read_b64_tr_b16 a[170:171], v245 offset:6144\n"
    "v_mfma_f32_16x16x32_bf16 v[28:31], a[180:183], v[160:163], v[28:31]\n"
    "v_exp_f32_e32 v206, v206\n"
    "v_mfma_f32_16x16x32_bf16 v[32:35], a[180:183], v[168:171], v[32:35]\n"
    "v_exp_f32_e32 v207, v207\n"
    "v_mfma_f32_16x16x32_bf16 v[32:35], a[176:179], v[164:167], v[32:35]\n"
    "v_cvt_pk_bf16_f32 v204, v204, v205\n"
    "v_mfma_f32_16x16x32_bf16 v[36:39], a[176:179], v[172:175], v[36:39]\n"
    "ds_read_b64_tr_b16 a[172:173], v245 offset:36864\n"
    "v_mfma_f32_16x16x32_bf16 v[36:39], a[180:183], v[176:179], v[36:39]\n"
    "s_waitcnt vmcnt(20)\n"
    "ds_read_b64_tr_b16 a[174:175], v245 offset:38912\n"
    "v_smfmac_f32_16x16x64_bf16 v[224:227], a[232:235], a[216:223], v238\n"
    "s_waitcnt lgkmcnt(8)\n"
    "v_mad_u32_u16 v234, v231, s90, v239 op_sel:[0,0,0,0]\n"
    "v_mfma_f32_16x16x32_bf16 v[56:59], a[188:191], v[176:179], v[56:59]\n"
    "s_bitcmp1_b32 s86, 13\n"
    "buffer_load_dwordx4 a[216:219], v234, s[20:23], 0 offen sc1\n"
    "v_mad_u32_u16 v235, v231, s90, v239 op_sel:[1,0,0,0]\n"
    "v_mfma_f32_16x16x32_bf16 v[56:59], a[184:187], v[172:175], v[56:59]\n"
    "s_cselect_b64 exec, -1, 0\n"
    "v_cvt_pk_bf16_f32 v236, v224, v225\n"
    "v_cvt_pk_bf16_f32 v237, v226, v227\n"
    "v_mfma_f32_16x16x32_bf16 v[52:55], a[184:187], v[164:167], v[52:55]\n"
    "s_cselect_b32 s88, 512, 0\n"
    "v_mov_b64_e32 v[224:225], 0\n"
    "v_mov_b64_e32 v[226:227], 0\n"
    "v_mfma_f32_16x16x32_bf16 v[52:55], a[188:191], v[168:171], v[52:55]\n"
    "buffer_store_dwordx2 v[236:237], v252, s[8:11], s87 offen sc1\n"
    "v_mfma_f32_16x16x32_bf16 v[48:51], a[188:191], v[160:163], v[48:51]\n"
    "s_mov_b64 exec, -1\n"
    "ds_read_b64_tr_b16 a[176:177], v246 offset:4096\n"
    "v_mfma_f32_16x16x32_bf16 v[48:51], a[184:187], v[156:159], v[48:51]\n"
    "s_add_u32 s87, s87, s88\n"
    "ds_read_b64_tr_b16 a[178:179], v246 offset:6144\n"
    "v_mfma_f32_16x16x32_bf16 v[44:47], a[184:187], v[148:151], v[44:47]\n"
    "buffer_load_dwordx4 a[220:223], v235, s[20:23], 0 offen sc1\n"
    "v_mfma_f32_16x16x32_bf16 v[44:47], a[188:191], v[152:155], v[44:47]\n"
    "ds_read_b64_tr_b16 a[180:181], v246 offset:36864\n"
    "v_mfma_f32_16x16x32_bf16 v[40:43], a[188:191], v[144:147], v[40:43]\n"
    "v_exp_f32_e32 v212, v212\n"
    "v_mfma_f32_16x16x32_bf16 v[40:43], a[184:187], v[140:143], v[40:43]\n"
    "s_waitcnt lgkmcnt(6)\n"
    "v_exp_f32_e32 v213, v213\n"
    "v_mfma_f32_16x16x32_bf16 v[60:63], a[160:163], v[140:143], v[60:63]\n"
    "ds_read_b64_tr_b16 a[182:183], v246 offset:38912\n"
    "v_cvt_pk_bf16_f32 v205, v206, v207\n"
    "v_mfma_f32_16x16x32_bf16 v[60:63], a[164:167], v[144:147], v[60:63]\n"
    "ds_read_b64_tr_b16 a[184:185], v247 offset:4096\n"
    "v_mfma_f32_16x16x32_bf16 v[64:67], a[164:167], v[152:155], v[64:67]\n"
    "ds_read_b64_tr_b16 a[186:187], v247 offset:6144\n"
    "v_mfma_f32_16x16x32_bf16 v[64:67], a[160:163], v[148:151], v[64:67]\n"
    "v_exp_f32_e32 v214, v214\n"
    "v_mfma_f32_16x16x32_bf16 v[68:71], a[160:163], v[156:159], v[68:71]\n"
    "v_exp_f32_e32 v215, v215\n"
    "v_mfma_f32_16x16x32_bf16 v[68:71], a[164:167], v[160:163], v[68:71]\n"
    "ds_read_b64_tr_b16 a[188:189], v247 offset:36864\n"
    "v_cvt_pk_bf16_f32 v212, v212, v213\n"
    "v_mfma_f32_16x16x32_bf16 v[72:75], a[164:167], v[168:171], v[72:75]\n"
    "ds_read_b64_tr_b16 a[190:191], v247 offset:38912\n"
    "v_cvt_pk_bf16_f32 v213, v214, v215\n"
    "v_mfma_f32_16x16x32_bf16 v[72:75], a[160:163], v[164:167], v[72:75]\n"
    "s_waitcnt vmcnt(30)\n"
    "v_mfma_f32_16x16x32_bf16 v[76:79], a[160:163], v[172:175], v[76:79]\n"
    "s_barrier\n"
    "v_mfma_f32_16x16x32_bf16 v[76:79], a[164:167], v[176:179], v[76:79]\n"
    "s_waitcnt lgkmcnt(8)\n"
    "s_mov_b32 m0, s42\n"
    "v_mfma_f32_16x16x32_bf16 v[96:99], a[172:175], v[176:179], v[96:99]\n"
    "v_exp_f32_e32 v184, v184\n"
    "v_mfma_f32_16x16x32_bf16 v[96:99], a[168:171], v[172:175], v[96:99]\n"
    "v_exp_f32_e32 v185, v185\n"
    "v_mfma_f32_16x16x32_bf16 v[92:95], a[168:171], v[164:167], v[92:95]\n"
    "v_exp_f32_e32 v186, v186\n"
    "v_mfma_f32_16x16x32_bf16 v[92:95], a[172:175], v[168:171], v[92:95]\n"
    "ds_read_b128 a[160:163], v240 offset:16384\n"
    "ds_write_addtid_b32 v180 offset:0\n"
    "v_mfma_f32_16x16x32_bf16 v[88:91], a[172:175], v[160:163], v[88:91]\n"
    "v_exp_f32_e32 v187, v187\n"
    "v_mfma_f32_16x16x32_bf16 v[88:91], a[168:171], v[156:159], v[88:91]\n"
    "ds_write_addtid_b32 v181 offset:256\n"
    "v_cvt_pk_bf16_f32 v182, v184, v185\n"
    "v_mfma_f32_16x16x32_bf16 v[84:87], a[168:171], v[148:151], v[84:87]\n"
    "v_exp_f32_e32 v192, v192\n"
    "v_mfma_f32_16x16x32_bf16 v[84:87], a[172:175], v[152:155], v[84:87]\n"
    "v_exp_f32_e32 v193, v193\n"
    "v_mfma_f32_16x16x32_bf16 v[80:83], a[172:175], v[144:147], v[80:83]\n"
    "ds_write_addtid_b32 v188 offset:1024\n"
    "v_cvt_pk_bf16_f32 v183, v186, v187\n"
    "v_mfma_f32_16x16x32_bf16 v[80:83], a[168:171], v[140:143], v[80:83]\n"
    "s_waitcnt vmcnt(20)\n"
    "ds_read_b128 a[164:167], v241 offset:16384\n"
    "ds_write_addtid_b32 v189 offset:1280\n"
    "v_smfmac_f32_16x16x64_bf16 v[224:227], a[232:235], a[224:231], v238\n"
    "s_waitcnt lgkmcnt(10)\n"
    "ds_write_addtid_b32 v196 offset:2048\n"
    "v_mad_u32_u16 v234, v232, s90, v239 op_sel:[0,0,0,0]\n"
    "v_mfma_f32_16x16x32_bf16 v[100:103], a[176:179], v[140:143], v[100:103]\n"
    "s_bitcmp1_b32 s86, 14\n"
    "buffer_load_dwordx4 a[224:227], v234, s[20:23], 0 offen sc1\n"
    "v_mad_u32_u16 v235, v232, s90, v239 op_sel:[1,0,0,0]\n"
    "v_mfma_f32_16x16x32_bf16 v[100:103], a[180:183], v[144:147], v[100:103]\n"
    "s_cselect_b64 exec, -1, 0\n"
    "v_cvt_pk_bf16_f32 v236, v224, v225\n"
    "v_cvt_pk_bf16_f32 v237, v226, v227\n"
    "v_mfma_f32_16x16x32_bf16 v[104:107], a[180:183], v[152:155], v[104:107]\n"
    "s_cselect_b32 s88, 512, 0\n"
    "v_mov_b64_e32 v[224:225], 0\n"
    "v_mov_b64_e32 v[226:227], 0\n"
    "v_mfma_f32_16x16x32_bf16 v[104:107], a[176:179], v[148:151], v[104:107]\n"
    "buffer_store_dwordx2 v[236:237], v252, s[8:11], s87 offen sc1\n"
    "v_mfma_f32_16x16x32_bf16 v[108:111], a[176:179], v[156:159], v[108:111]\n"
    "s_mov_b64 exec, -1\n"
    "ds_read_b128 a[168:171], v240 offset:20480\n"
    "ds_write_addtid_b32 v197 offset:2304\n"
    "v_mfma_f32_16x16x32_bf16 v[108:111], a[180:183], v[160:163], v[108:111]\n"
    "s_add_u32 s87, s87, s88\n"
    "v_exp_f32_e32 v194, v194\n"
    "v_mfma_f32_16x16x32_bf16 v[112:115], a[180:183], v[168:171], v[112:115]\n"
    "buffer_load_dwordx4 a[228:231], v235, s[20:23], 0 offen sc1\n"
    "ds_write_addtid_b32 v204 offset:3072\n"
    "v_mfma_f32_16x16x32_bf16 v[112:115], a[176:179], v[164:167], v[112:115]\n"
    "ds_write_addtid_b32 v205 offset:3328\n"
    "v_mfma_f32_16x16x32_bf16 v[116:119], a[176:179], v[172:175], v[116:119]\n"
    "v_exp_f32_e32 v195, v195\n"
    "v_mfma_f32_16x16x32_bf16 v[116:119], a[180:183], v[176:179], v[116:119]\n"
    "s_waitcnt lgkmcnt(11)\n"
    "ds_read_b128 a[172:175], v241 offset:20480\n"
    "v_cvt_pk_bf16_f32 v190, v192, v193\n"
    "v_mfma_f32_16x16x32_bf16 v[136:139], a[188:191], v[176:179], v[136:139]\n"
    "v_exp_f32_e32 v200, v200\n"
    "v_mfma_f32_16x16x32_bf16 v[136:139], a[184:187], v[172:175], v[136:139]\n"
    "v_exp_f32_e32 v201, v201\n"
    "v_mfma_f32_16x16x32_bf16 v[132:135], a[184:187], v[164:167], v[132:135]\n"
    "ds_read_b128 a[176:179], v242 offset:16384\n"
    "v_cvt_pk_bf16_f32 v191, v194, v195\n"
    "v_mfma_f32_16x16x32_bf16 v[132:135], a[188:191], v[168:171], v[132:135]\n"
    "v_exp_f32_e32 v202, v202\n"
    "v_mfma_f32_16x16x32_bf16 v[128:131], a[188:191], v[160:163], v[128:131]\n"
    "v_exp_f32_e32 v203, v203\n"
    "v_mfma_f32_16x16x32_bf16 v[128:131], a[184:187], v[156:159], v[128:131]\n"
    "ds_write_addtid_b32 v212 offset:4096\n"
    "v_cvt_pk_bf16_f32 v198, v200, v201\n"
    "v_mfma_f32_16x16x32_bf16 v[124:127], a[184:187], v[148:151], v[124:127]\n"
    "v_exp_f32_e32 v208, v208\n"
    "v_mfma_f32_16x16x32_bf16 v[124:127], a[188:191], v[152:155], v[124:127]\n"
    "ds_read_b128 a[180:183], v243 offset:16384\n"
    "ds_write_addtid_b32 v213 offset:4352\n"
    "v_mfma_f32_16x16x32_bf16 v[120:123], a[188:191], v[144:147], v[120:123]\n"
    "v_exp_f32_e32 v209, v209\n"
    "v_mfma_f32_16x16x32_bf16 v[120:123], a[184:187], v[140:143], v[120:123]\n"
    "s_waitcnt lgkmcnt(11)\n"
    "ds_write_addtid_b32 v182 offset:512\n"
    "v_cvt_pk_bf16_f32 v199, v202, v203\n"
    "v_mfma_f32_16x16x32_bf16 v[172:175], a[160:163], a[128:131], v[220:223]\n"
    "ds_write_addtid_b32 v183 offset:768\n"
    "v_mfma_f32_16x16x32_bf16 v[172:175], a[164:167], a[132:135], v[172:175]\n"
    "v_exp_f32_e32 v210, v210\n"
    "v_mfma_f32_16x16x32_bf16 v[164:167], a[164:167], a[100:103], v[220:223]\n"
    "ds_write_addtid_b32 v190 offset:1536\n"
    "v_mfma_f32_16x16x32_bf16 v[164:167], a[160:163], a[96:99], v[164:167]\n"
    "ds_read_b128 a[184:187], v242 offset:20480\n"
    "v_mfma_f32_16x16x32_bf16 v[156:159], a[160:163], a[64:67], v[220:223]\n"
    "v_exp_f32_e32 v211, v211\n"
    "v_mfma_f32_16x16x32_bf16 v[156:159], a[164:167], a[68:71], v[156:159]\n"
    "ds_write_addtid_b32 v191 offset:1792\n"
    "v_cvt_pk_bf16_f32 v206, v208, v209\n"
    "v_mfma_f32_16x16x32_bf16 v[148:151], a[164:167], a[36:39], v[220:223]\n"
    "v_exp_f32_e32 v216, v216\n"
    "v_mfma_f32_16x16x32_bf16 v[148:151], a[160:163], a[32:35], v[148:151]\n"
    "v_exp_f32_e32 v217, v217\n"
    "v_mfma_f32_16x16x32_bf16 v[140:143], a[160:163], a[0:3], v[220:223]\n"
    "ds_write_addtid_b32 v198 offset:2560\n"
    "v_cvt_pk_bf16_f32 v207, v210, v211\n"
    "v_mfma_f32_16x16x32_bf16 v[140:143], a[164:167], a[4:7], v[140:143]\n"
    "s_waitcnt vmcnt(20)\n"
    "v_exp_f32_e32 v218, v218\n"
    "v_smfmac_f32_16x16x64_bf16 v[224:227], a[232:235], a[192:199], v238\n"
    "s_waitcnt lgkmcnt(10)\n"
    "ds_read_b128 a[188:191], v243 offset:20480\n"
    "v_mfma_f32_16x16x32_bf16 v[140:143], a[168:171], a[8:11], v[140:143]\n"
    "s_bitcmp1_b32 s86, 15\n"
    "ds_read_b128 a[160:163], v240 offset:18432\n"
    "v_mfma_f32_16x16x32_bf16 v[140:143], a[172:175], a[12:15], v[140:143]\n"
    "s_cselect_b64 exec, -1, 0\n"
    "v_cvt_pk_bf16_f32 v236, v224, v225\n"
    "v_cvt_pk_bf16_f32 v237, v226, v227\n"
    "v_mfma_f32_16x16x32_bf16 v[148:151], a[172:175], a[44:47], v[148:151]\n"
    "s_cselect_b32 s88, 512, 0\n"
    "v_mov_b64_e32 v[224:225], 0\n"
    "v_mov_b64_e32 v[226:227], 0\n"
    "v_mfma_f32_16x16x32_bf16 v[148:151], a[168:171], a[40:43], v[148:151]\n"
    "buffer_store_dwordx2 v[236:237], v252, s[8:11], s87 offen sc1\n"
    "v_mfma_f32_16x16x32_bf16 v[156:159], a[168:171], a[72:75], v[156:159]\n"
    "s_mov_b64 exec, -1\n"
    "ds_read2_b32 v[228:229], v251 offset0:30 offset1:32\n"
    "v_mfma_f32_16x16x32_bf16 v[156:159], a[172:175], a[76:79], v[156:159]\n"
    "s_add_u32 s87, s87, s88\n"
    "ds_read2_b32 v[230:231], v251 offset0:34 offset1:36\n"
    "v_mfma_f32_16x16x32_bf16 v[164:167], a[172:175], a[108:111], v[164:167]\n"
    "ds_read_b32 v232, v251 offset:152\n"
    "v_mfma_f32_16x16x32_bf16 v[164:167], a[168:171], a[104:107], v[164:167]\n"
    "v_exp_f32_e32 v219, v219\n"
    "v_mfma_f32_16x16x32_bf16 v[172:175], a[168:171], a[136:139], v[172:175]\n"
    "ds_write_addtid_b32 v199 offset:2816\n"
    "v_cvt_pk_bf16_f32 v214, v216, v217\n"
    "v_mfma_f32_16x16x32_bf16 v[172:175], a[172:175], a[140:143], v[172:175]\n"
    "s_waitcnt lgkmcnt(13)\n"
    "ds_read_b128 a[164:167], v241 offset:18432\n"
    "v_mfma_f32_16x16x32_bf16 v[172:175], a[176:179], a[144:147], v[172:175]\n"
    "ds_write_addtid_b32 v206 offset:3584\n"
    "v_cvt_pk_bf16_f32 v215, v218, v219\n"
    "v_mfma_f32_16x16x32_bf16 v[172:175], a[180:183], a[148:151], v[172:175]\n"
    "ds_read_b128 a[168:171], v240 offset:22528\n"
    "v_mfma_f32_16x16x32_bf16 v[164:167], a[180:183], a[116:119], v[164:167]\n"
    "s_waitcnt lgkmcnt(6)\n"
    "ds_write_addtid_b32 v207 offset:3840\n"
    "v_mad_u32_u16 v234, v228, s90, v239 op_sel:[0,0,0,0]\n"
    "v_mfma_f32_16x16x32_bf16 v[164:167], a[176:179], a[112:115], v[164:167]\n"
    "buffer_load_dwordx4 a[192:195], v234, s[20:23], 0 offen sc1\n"
    "v_mad_u32_u16 v235, v228, s90, v239 op_sel:[1,0,0,0]\n"
    "v_mfma_f32_16x16x32_bf16 v[156:159], a[176:179], a[80:83], v[156:159]\n"
    "ds_write_addtid_b32 v214 offset:4608\n"
    "v_add_u32_e32 v251, 160, v251\n"
    "v_mfma_f32_16x16x32_bf16 v[156:159], a[180:183], a[84:87], v[156:159]\n"
    "ds_write_addtid_b32 v215 offset:4864\n"
    "v_mfma_f32_16x16x32_bf16 v[148:151], a[180:183], a[52:55], v[148:151]\n"
    "buffer_load_dwordx4 a[196:199], v235, s[20:23], 0 offen sc1\n"
    "v_mfma_f32_16x16x32_bf16 v[148:151], a[176:179], a[48:51], v[148:151]\n"
    "v_mfma_f32_16x16x32_bf16 v[140:143], a[176:179], a[16:19], v[140:143]\n"
    "s_waitcnt lgkmcnt(0)\n"
    "v_mfma_f32_16x16x32_bf16 v[140:143], a[180:183], a[20:23], v[140:143]\n"
    "s_barrier\n"
    "v_mfma_f32_16x16x32_bf16 v[140:143], a[184:187], a[24:27], v[140:143]\n"
    "s_add_u32 m0, s43, 0\n"
    "ds_read_b128 a[172:175], v241 offset:22528\n"
    "v_mfma_f32_16x16x32_bf16 v[140:143], a[188:191], a[28:31], v[140:143]\n"
    "ds_read2st64_b32 v[184:185], v249 offset0:0 offset1:1\n"
    "v_mfma_f32_16x16x32_bf16 v[148:151], a[188:191], a[60:63], v[148:151]\n"
    "buffer_load_dwordx4 v248, s[8:11], s40 offen offset:0 sc1 lds\n"
    "v_mfma_f32_16x16x32_bf16 v[148:151], a[184:187], a[56:59], v[148:151]\n"
    "ds_read_b128 a[176:179], v242 offset:18432\n"
    "v_mfma_f32_16x16x32_bf16 v[156:159], a[184:187], a[88:91], v[156:159]\n"
    "v_mfma_f32_16x16x32_bf16 v[156:159], a[188:191], a[92:95], v[156:159]\n"
    "v_mfma_f32_16x16x32_bf16 v[164:167], a[188:191], a[124:127], v[164:167]\n"
    "buffer_load_dwordx4 v248, s[8:11], s40 offen offset:1024 sc1 lds\n"
    "v_mfma_f32_16x16x32_bf16 v[164:167], a[184:187], a[120:123], v[164:167]\n"
    "ds_read2st64_b32 v[186:187], v249 offset0:2 offset1:3\n"
    "v_mfma_f32_16x16x32_bf16 v[172:175], a[184:187], a[152:155], v[172:175]\n"
    "v_mfma_f32_16x16x32_bf16 v[172:175], a[188:191], a[156:159], v[172:175]\n"
    "ds_read_b128 a[180:183], v243 offset:18432\n"
    "v_mfma_f32_16x16x32_bf16 v[176:179], a[160:163], a[128:131], v[220:223]\n"
    "buffer_load_dwordx4 v248, s[8:11], s40 offen offset:2048 sc1 lds\n"
    "v_mfma_f32_16x16x32_bf16 v[176:179], a[164:167], a[132:135], v[176:179]\n"
    "ds_read2st64_b32 v[192:193], v249 offset0:4 offset1:5\n"
    "v_mfma_f32_16x16x32_bf16 v[168:171], a[164:167], a[100:103], v[220:223]\n"
    "v_exp_f32_e32 v140, v140\n"
    "v_mfma_f32_16x16x32_bf16 v[168:171], a[160:163], a[96:99], v[168:171]\n"
    "ds_read_b128 a[184:187], v242 offset:22528\n"
    "v_mfma_f32_16x16x32_bf16 v[160:163], a[160:163], a[64:67], v[220:223]\n"
    "buffer_load_dwordx4 v248, s[8:11], s40 offen offset:3072 sc1 lds\n"
    "v_mfma_f32_16x16x32_bf16 v[160:163], a[164:167], a[68:71], v[160:163]\n"
    "s_add_u32 m0, s43, 4096\n"
    "ds_read2st64_b32 v[194:195], v249 offset0:6 offset1:7\n"
    "v_mfma_f32_16x16x32_bf16 v[152:155], a[164:167], a[36:39], v[220:223]\n"
    "s_add_u32 s40, s40, 0x8000\n"
    "ds_read2st64_b32 v[200:201], v249 offset0:8 offset1:9\n"
    "v_mfma_f32_16x16x32_bf16 v[152:155], a[160:163], a[32:35], v[152:155]\n"
    "ds_read2st64_b32 v[202:203], v249 offset0:10 offset1:11\n"
    "v_mfma_f32_16x16x32_bf16 v[144:147], a[160:163], a[0:3], v[220:223]\n"
    "buffer_load_dwordx4 v248, s[8:11], s41 offen offset:0 sc1 lds\n"
    "v_mfma_f32_16x16x32_bf16 v[144:147], a[164:167], a[4:7], v[144:147]\n"
    "s_waitcnt vmcnt(17)\n"
    "ds_read_b128 a[188:191], v243 offset:22528\n"
    "v_smfmac_f32_16x16x64_bf16 v[224:227], a[232:235], a[200:207], v238\n"
    "s_waitcnt lgkmcnt(10)\n"
    "v_mad_u32_u16 v234, v229, s90, v239 op_sel:[0,0,0,0]\n"
    "v_mfma_f32_16x16x32_bf16 v[144:147], a[168:171], a[8:11], v[144:147]\n"
    "s_bitcmp1_b32 s86, 16\n"
    "buffer_load_dwordx4 a[200:203], v234, s[20:23], 0 offen sc1\n"
    "v_mad_u32_u16 v235, v229, s90, v239 op_sel:[1,0,0,0]\n"
    "v_mfma_f32_16x16x32_bf16 v[144:147], a[172:175], a[12:15], v[144:147]\n"
    "s_cselect_b64 exec, -1, 0\n"
    "v_cvt_pk_bf16_f32 v236, v224, v225\n"
    "v_cvt_pk_bf16_f32 v237, v226, v227\n"
    "v_mfma_f32_16x16x32_bf16 v[152:155], a[172:175], a[44:47], v[152:155]\n"
    "s_cselect_b32 s88, 512, 0\n"
    "v_mov_b64_e32 v[224:225], 0\n"
    "v_mov_b64_e32 v[226:227], 0\n"
    "v_mfma_f32_16x16x32_bf16 v[152:155], a[168:171], a[40:43], v[152:155]\n"
    "buffer_store_dwordx2 v[236:237], v252, s[8:11], s87 offen sc1\n"
    "v_mfma_f32_16x16x32_bf16 v[160:163], a[168:171], a[72:75], v[160:163]\n"
    "s_mov_b64 exec, -1\n"
    "ds_read_b64_tr_b16 a[160:161], v244 offset:8192\n"
    "v_mfma_f32_16x16x32_bf16 v[160:163], a[172:175], a[76:79], v[160:163]\n"
    "s_add_u32 s87, s87, s88\n"
    "ds_read_b64_tr_b16 a[162:163], v244 offset:10240\n"
    "v_mfma_f32_16x16x32_bf16 v[168:171], a[172:175], a[108:111], v[168:171]\n"
    "buffer_load_dwordx4 v248, s[8:11], s41 offen offset:1024 sc1 lds\n"
    "v_mfma_f32_16x16x32_bf16 v[168:171], a[168:171], a[104:107], v[168:171]\n"
    "ds_read_b64_tr_b16 a[164:165], v244 offset:40960\n"
    "v_mfma_f32_16x16x32_bf16 v[176:179], a[168:171], a[136:139], v[176:179]\n"
    "ds_read2st64_b32 v[208:209], v249 offset0:12 offset1:13\n"
    "v_mfma_f32_16x16x32_bf16 v[176:179], a[172:175], a[140:143], v[176:179]\n"
    "s_waitcnt lgkmcnt(10)\n"
    "ds_read2st64_b32 v[210:211], v249 offset0:14 offset1:15\n"
    "v_mfma_f32_16x16x32_bf16 v[176:179], a[176:179], a[144:147], v[176:179]\n"
    "buffer_load_dwordx4 v248, s[8:11], s41 offen offset:2048 sc1 lds\n"
    "v_mfma_f32_16x16x32_bf16 v[176:179], a[180:183], a[148:151], v[176:179]\n"
    "ds_read_b64_tr_b16 a[166:167], v244 offset:43008\n"
    "v_mfma_f32_16x16x32_bf16 v[168:171], a[180:183], a[116:119], v[168:171]\n"
    "ds_read_b64_tr_b16 a[168:169], v245 offset:8192\n"
    "v_mfma_f32_16x16x32_bf16 v[168:171], a[176:179], a[112:115], v[168:171]\n"
    "v_exp_f32_e32 v141, v141\n"
    "v_mfma_f32_16x16x32_bf16 v[160:163], a[176:179], a[80:83], v[160:163]\n"
    "buffer_load_dwordx4 v248, s[8:11], s41 offen offset:3072 sc1 lds\n"
    "v_mfma_f32_16x16x32_bf16 v[160:163], a[180:183], a[84:87], v[160:163]\n"
    "ds_read_b64_tr_b16 a[170:171], v245 offset:10240\n"
    "v_mfma_f32_16x16x32_bf16 v[152:155], a[180:183], a[52:55], v[152:155]\n"
    "s_add_u32 s41, s41, 0x8000\n"
    "ds_read2st64_b32 v[216:217], v249 offset0:16 offset1:17\n"
    "v_mfma_f32_16x16x32_bf16 v[152:155], a[176:179], a[48:51], v[152:155]\n"
    "buffer_load_dwordx4 a[204:207], v235, s[20:23], 0 offen sc1\n"
    "v_mfma_f32_16x16x32_bf16 v[144:147], a[176:179], a[16:19], v[144:147]\n"
    "ds_read_b64_tr_b16 a[172:173], v245 offset:40960\n"
    "v_mfma_f32_16x16x32_bf16 v[144:147], a[180:183], a[20:23], v[144:147]\n"
    "s_waitcnt lgkmcnt(10)\n"
    "ds_read2st64_b32 v[218:219], v249 offset0:18 offset1:19\n"
    "v_mfma_f32_16x16x32_bf16 v[144:147], a[184:187], a[24:27], v[144:147]\n"
    "ds_read_b64_tr_b16 a[176:177], v246 offset:8192\n"
    "v_mfma_f32_16x16x32_bf16 v[144:147], a[188:191], a[28:31], v[144:147]\n"
    "ds_read_b64_tr_b16 a[174:175], v245 offset:43008\n"
    "v_mfma_f32_16x16x32_bf16 v[152:155], a[188:191], a[60:63], v[152:155]\n"
    "v_exp_f32_e32 v142, v142\n"
    "v_mfma_f32_16x16x32_bf16 v[152:155], a[184:187], a[56:59], v[152:155]\n"
    "v_exp_f32_e32 v143, v143\n"
    "v_mfma_f32_16x16x32_bf16 v[160:163], a[184:187], a[88:91], v[160:163]\n"
    "ds_read_b64_tr_b16 a[178:179], v246 offset:10240\n"
    "v_cvt_pk_bf16_f32 v140, v140, v141\n"
    "v_mfma_f32_16x16x32_bf16 v[160:163], a[188:191], a[92:95], v[160:163]\n"
    "v_exp_f32_e32 v148, v148\n"
    "v_mfma_f32_16x16x32_bf16 v[168:171], a[188:191], a[124:127], v[168:171]\n"
    "v_exp_f32_e32 v149, v149\n"
    "v_mfma_f32_16x16x32_bf16 v[168:171], a[184:187], a[120:123], v[168:171]\n"
    "v_cvt_pk_bf16_f32 v141, v142, v143\n"
    "v_mfma_f32_16x16x32_bf16 v[176:179], a[184:187], a[152:155], v[176:179]\n"
    "ds_read_b64_tr_b16 a[180:181], v246 offset:40960\n"
    "v_mfma_f32_16x16x32_bf16 v[176:179], a[188:191], a[156:159], v[176:179]\n"
    "s_waitcnt vmcnt(20)\n"
    "ds_read_b64_tr_b16 a[182:183], v246 offset:43008\n"
    "v_smfmac_f32_16x16x64_bf16 v[224:227], a[232:235], a[208:215], v238\n"
    "s_waitcnt lgkmcnt(5)\n"
    "v_mad_u32_u16 v234, v230, s90, v239 op_sel:[0,0,0,0]\n"
    "v_mfma_f32_16x16x32_bf16 a[236:239], a[160:163], v[180:183], a[236:239]\n"
    "s_bitcmp1_b32 s86, 17\n"
    "buffer_load_dwordx4 a[208:211], v234, s[20:23], 0 offen sc1\n"
    "v_mad_u32_u16 v235, v230, s90, v239 op_sel:[1,0,0,0]\n"
    "v_mfma_f32_16x16x32_bf16 a[236:239], a[164:167], v[184:187], a[236:239]\n"
    "s_cselect_b64 exec, -1, 0\n"
    "v_cvt_pk_bf16_f32 v236, v224, v225\n"
    "v_cvt_pk_bf16_f32 v237, v226, v227\n"
    "v_mfma_f32_16x16x32_bf16 a[240:243], a[164:167], v[192:195], a[240:243]\n"
    "s_cselect_b32 s88, 512, 0\n"
    "v_mov_b64_e32 v[224:225], 0\n"
    "v_mov_b64_e32 v[226:227], 0\n"
    "v_mfma_f32_16x16x32_bf16 a[240:243], a[160:163], v[188:191], a[240:243]\n"
    "buffer_store_dwordx2 v[236:237], v252, s[8:11], s87 offen sc1\n"
    "v_mfma_f32_16x16x32_bf16 a[244:247], a[160:163], v[196:199], a[244:247]\n"
    "s_mov_b64 exec, -1\n"
    "ds_read_b64_tr_b16 a[184:185], v247 offset:8192\n"
    "v_mfma_f32_16x16x32_bf16 a[244:247], a[164:167], v[200:203], a[244:247]\n"
    "s_add_u32 s87, s87, s88\n"
    "ds_read_b64_tr_b16 a[186:187], v247 offset:10240\n"
    "v_mfma_f32_16x16x32_bf16 a[248:251], a[164:167], v[208:211], a[248:251]\n"
    "buffer_load_dwordx4 a[212:215], v235, s[20:23], 0 offen sc1\n"
    "v_mfma_f32_16x16x32_bf16 a[248:251], a[160:163], v[204:207], a[248:251]\n"
    "ds_read_b64_tr_b16 a[188:189], v247 offset:40960\n"
    "v_mfma_f32_16x16x32_bf16 a[252:255], a[160:163], v[212:215], a[252:255]\n"
    "v_exp_f32_e32 v150, v150\n"
    "v_mfma_f32_16x16x32_bf16 a[252:255], a[164:167], v[216:219], a[252:255]\n"
    "s_waitcnt lgkmcnt(6)\n"
    "v_exp_f32_e32 v151, v151\n"
    "v_mfma_f32_16x16x32_bf16 v[16:19], a[172:175], v[216:219], v[16:19]\n"
    "ds_read_b64_tr_b16 a[190:191], v247 offset:43008\n"
    "v_cvt_pk_bf16_f32 v148, v148, v149\n"
    "v_mfma_f32_16x16x32_bf16 v[16:19], a[168:171], v[212:215], v[16:19]\n"
    "ds_read_b64_tr_b16 a[160:161], v244 offset:12288\n"
    "v_mfma_f32_16x16x32_bf16 v[12:15], a[168:171], v[204:207], v[12:15]\n"
    "v_exp_f32_e32 v156, v156\n"
    "v_mfma_f32_16x16x32_bf16 v[12:15], a[172:175], v[208:211], v[12:15]\n"
    "v_exp_f32_e32 v157, v157\n"
    "v_mfma_f32_16x16x32_bf16 v[8:11], a[172:175], v[200:203], v[8:11]\n"
    "ds_read_b64_tr_b16 a[162:163], v244 offset:14336\n"
    "v_cvt_pk_bf16_f32 v149, v150, v151\n"
    "v_mfma_f32_16x16x32_bf16 v[8:11], a[168:171], v[196:199], v[8:11]\n"
    "v_exp_f32_e32 v158, v158\n"
    "v_mfma_f32_16x16x32_bf16 v[4:7], a[168:171], v[188:191], v[4:7]\n"
    "v_exp_f32_e32 v159, v159\n"
    "v_mfma_f32_16x16x32_bf16 v[4:7], a[172:175], v[192:195], v[4:7]\n"
    "v_cvt_pk_bf16_f32 v156, v156, v157\n"
    "v_mfma_f32_16x16x32_bf16 v[0:3], a[172:175], v[184:187], v[0:3]\n"
    "ds_read_b64_tr_b16 a[164:165], v244 offset:45056\n"
    "v_mfma_f32_16x16x32_bf16 v[0:3], a[168:171], v[180:183], v[0:3]\n"
    "s_waitcnt lgkmcnt(7)\n"
    "v_exp_f32_e32 v164, v164\n"
    "v_mfma_f32_16x16x32_bf16 v[20:23], a[176:179], v[180:183], v[20:23]\n"
    "ds_read_b64_tr_b16 a[168:169], v245 offset:12288\n"
    "v_mfma_f32_16x16x32_bf16 v[20:23], a[180:183], v[184:187], v[20:23]\n"
    "ds_read_b64_tr_b16 a[166:167], v244 offset:47104\n"
    "v_mfma_f32_16x16x32_bf16 v[24:27], a[180:183], v[192:195], v[24:27]\n"
    "v_exp_f32_e32 v165, v165\n"
    "v_mfma_f32_16x16x32_bf16 v[24:27], a[176:179], v[188:191], v[24:27]\n"
    "v_cvt_pk_bf16_f32 v157, v158, v159\n"
    "v_mfma_f32_16x16x32_bf16 v[28:31], a[176:179], v[196:199], v[28:31]\n"
    "ds_read_b64_tr_b16 a[170:171], v245 offset:14336\n"
    "v_mfma_f32_16x16x32_bf16 v[28:31], a[180:183], v[200:203], v[28:31]\n"
    "v_exp_f32_e32 v166, v166\n"
    "v_mfma_f32_16x16x32_bf16 v[32:35], a[180:183], v[208:211], v[32:35]\n"
    "v_exp_f32_e32 v167, v167\n"
    "v_mfma_f32_16x16x32_bf16 v[32:35], a[176:179], v[204:207], v[32:35]\n"
    "v_cvt_pk_bf16_f32 v164, v164, v165\n"
    "v_mfma_f32_16x16x32_bf16 v[36:39], a[176:179], v[212:215], v[36:39]\n"
    "ds_read_b64_tr_b16 a[172:173], v245 offset:45056\n"
    "v_mfma_f32_16x16x32_bf16 v[36:39], a[180:183], v[216:219], v[36:39]\n"
    "s_waitcnt vmcnt(20)\n"
    "ds_read_b64_tr_b16 a[174:175], v245 offset:47104\n"
    "v_smfmac_f32_16x16x64_bf16 v[224:227], a[232:235], a[216:223], v238\n"
    "s_waitcnt lgkmcnt(8)\n"
    "v_mad_u32_u16 v234, v231, s90, v239 op_sel:[0,0,0,0]\n"
    "v_mfma_f32_16x16x32_bf16 v[56:59], a[188:191], v[216:219], v[56:59]\n"
    "s_bitcmp1_b32 s86, 18\n"
    "buffer_load_dwordx4 a[216:219], v234, s[20:23], 0 offen sc1\n"
    "v_mad_u32_u16 v235, v231, s90, v239 op_sel:[1,0,0,0]\n"
    "v_mfma_f32_16x16x32_bf16 v[56:59], a[184:187], v[212:215], v[56:59]\n"
    "s_cselect_b64 exec, -1, 0\n"
    "v_cvt_pk_bf16_f32 v236, v224, v225\n"
    "v_cvt_pk_bf16_f32 v237, v226, v227\n"
    "v_mfma_f32_16x16x32_bf16 v[52:55], a[184:187], v[204:207], v[52:55]\n"
    "s_cselect_b32 s88, 512, 0\n"
    "v_mov_b64_e32 v[224:225], 0\n"
    "v_mov_b64_e32 v[226:227], 0\n"
    "v_mfma_f32_16x16x32_bf16 v[52:55], a[188:191], v[208:211], v[52:55]\n"
    "buffer_store_dwordx2 v[236:237], v252, s[8:11], s87 offen sc1\n"
    "v_mfma_f32_16x16x32_bf16 v[48:51], a[188:191], v[200:203], v[48:51]\n"
    "s_mov_b64 exec, -1\n"
    "ds_read_b64_tr_b16 a[176:177], v246 offset:12288\n"
    "v_mfma_f32_16x16x32_bf16 v[48:51], a[184:187], v[196:199], v[48:51]\n"
    "s_add_u32 s87, s87, s88\n"
    "ds_read_b64_tr_b16 a[178:179], v246 offset:14336\n"
    "v_mfma_f32_16x16x32_bf16 v[44:47], a[184:187], v[188:191], v[44:47]\n"
    "buffer_load_dwordx4 a[220:223], v235, s[20:23], 0 offen sc1\n"
    "v_mfma_f32_16x16x32_bf16 v[44:47], a[188:191], v[192:195], v[44:47]\n"
    "ds_read_b64_tr_b16 a[180:181], v246 offset:45056\n"
    "v_mfma_f32_16x16x32_bf16 v[40:43], a[188:191], v[184:187], v[40:43]\n"
    "v_exp_f32_e32 v172, v172\n"
    "v_mfma_f32_16x16x32_bf16 v[40:43], a[184:187], v[180:183], v[40:43]\n"
    "s_waitcnt lgkmcnt(6)\n"
    "v_exp_f32_e32 v173, v173\n"
    "v_mfma_f32_16x16x32_bf16 v[60:63], a[160:163], v[180:183], v[60:63]\n"
    "ds_read_b64_tr_b16 a[182:183], v246 offset:47104\n"
    "v_cvt_pk_bf16_f32 v165, v166, v167\n"
    "v_mfma_f32_16x16x32_bf16 v[60:63], a[164:167], v[184:187], v[60:63]\n"
    "ds_read_b64_tr_b16 a[184:185], v247 offset:12288\n"
    "v_mfma_f32_16x16x32_bf16 v[64:67], a[164:167], v[192:195], v[64:67]\n"
    "ds_read_b64_tr_b16 a[186:187], v247 offset:14336\n"
    "v_mfma_f32_16x16x32_bf16 v[64:67], a[160:163], v[188:191], v[64:67]\n"
    "v_exp_f32_e32 v174, v174\n"
    "v_mfma_f32_16x16x32_bf16 v[68:71], a[160:163], v[196:199], v[68:71]\n"
    "v_exp_f32_e32 v175, v175\n"
    "v_mfma_f32_16x16x32_bf16 v[68:71], a[164:167], v[200:203], v[68:71]\n"
    "ds_read_b64_tr_b16 a[188:189], v247 offset:45056\n"
    "v_cvt_pk_bf16_f32 v172, v172, v173\n"
    "v_mfma_f32_16x16x32_bf16 v[72:75], a[164:167], v[208:211], v[72:75]\n"
    "ds_read_b64_tr_b16 a[190:191], v247 offset:47104\n"
    "v_cvt_pk_bf16_f32 v173, v174, v175\n"
    "v_mfma_f32_16x16x32_bf16 v[72:75], a[160:163], v[204:207], v[72:75]\n"
    "s_waitcnt vmcnt(30)\n"
    "v_mfma_f32_16x16x32_bf16 v[76:79], a[160:163], v[212:215], v[76:79]\n"
    "s_barrier\n"
    "v_mfma_f32_16x16x32_bf16 v[76:79], a[164:167], v[216:219], v[76:79]\n"
    "s_waitcnt lgkmcnt(8)\n"
    "s_mov_b32 m0, s42\n"
    "v_mfma_f32_16x16x32_bf16 v[96:99], a[172:175], v[216:219], v[96:99]\n"
    "v_exp_f32_e32 v144, v144\n"
    "v_mfma_f32_16x16x32_bf16 v[96:99], a[168:171], v[212:215], v[96:99]\n"
    "v_exp_f32_e32 v145, v145\n"
    "v_mfma_f32_16x16x32_bf16 v[92:95], a[168:171], v[204:207], v[92:95]\n"
    "v_exp_f32_e32 v146, v146\n"
    "v_mfma_f32_16x16x32_bf16 v[92:95], a[172:175], v[208:211], v[92:95]\n"
    "ds_read_b128 a[160:163], v240 offset:24576\n"
    "ds_write_addtid_b32 v140 offset:0\n"
    "v_mfma_f32_16x16x32_bf16 v[88:91], a[172:175], v[200:203], v[88:91]\n"
    "v_exp_f32_e32 v147, v147\n"
    "v_mfma_f32_16x16x32_bf16 v[88:91], a[168:171], v[196:199], v[88:91]\n"
    "ds_write_addtid_b32 v141 offset:256\n"
    "v_cvt_pk_bf16_f32 v142, v144, v145\n"
    "v_mfma_f32_16x16x32_bf16 v[84:87], a[168:171], v[188:191], v[84:87]\n"
    "v_exp_f32_e32 v152, v152\n"
    "v_mfma_f32_16x16x32_bf16 v[84:87], a[172:175], v[192:195], v[84:87]\n"
    "v_exp_f32_e32 v153, v153\n"
    "v_mfma_f32_16x16x32_bf16 v[80:83], a[172:175], v[184:187], v[80:83]\n"
    "ds_write_addtid_b32 v148 offset:1024\n"
    "v_cvt_pk_bf16_f32 v143, v146, v147\n"
    "v_mfma_f32_16x16x32_bf16 v[80:83], a[168:171], v[180:183], v[80:83]\n"
    "s_waitcnt vmcnt(20)\n"
    "ds_read_b128 a[164:167], v241 offset:24576\n"
    "ds_write_addtid_b32 v149 offset:1280\n"
    "v_smfmac_f32_16x16x64_bf16 v[224:227], a[232:235], a[224:231], v238\n"
    "s_waitcnt lgkmcnt(10)\n"
    "ds_write_addtid_b32 v156 offset:2048\n"
    "v_mad_u32_u16 v234, v232, s90, v239 op_sel:[0,0,0,0]\n"
    "v_mfma_f32_16x16x32_bf16 v[100:103], a[176:179], v[180:183], v[100:103]\n"
    "s_bitcmp1_b32 s86, 19\n"
    "buffer_load_dwordx4 a[224:227], v234, s[20:23], 0 offen sc1\n"
    "v_mad_u32_u16 v235, v232, s90, v239 op_sel:[1,0,0,0]\n"
    ".long 0xbedf00ff\n"
    ".Lat_w8:\n"
    "s_setpc_b64 s[2:3]\n"
    "v_mfma_f32_16x16x32_bf16 v[100:103], a[180:183], v[184:187], v[100:103]\n"
    "s_cselect_b64 exec, -1, 0\n"
    "v_cvt_pk_bf16_f32 v236, v224, v225\n"
    "v_cvt_pk_bf16_f32 v237, v226, v227\n"
    "v_mfma_f32_16x16x32_bf16 v[104:107], a[180:183], v[192:195], v[104:107]\n"
    "s_cselect_b32 s88, 512, 0\n"
    "v_mov_b64_e32 v[224:225], 0\n"
    "v_mov_b64_e32 v[226:227], 0\n"
    "v_mfma_f32_16x16x32_bf16 v[104:107], a[176:179], v[188:191], v[104:107]\n"
    "buffer_store_dwordx2 v[236:237], v252, s[8:11], s87 offen sc1\n"
    "v_mfma_f32_16x16x32_bf16 v[108:111], a[176:179], v[196:199], v[108:111]\n"
    "s_mov_b64 exec, -1\n"
    "ds_read_b128 a[168:171], v240 offset:28672\n"
    "ds_write_addtid_b32 v157 offset:2304\n"
    "v_mfma_f32_16x16x32_bf16 v[108:111], a[180:183], v[200:203], v[108:111]\n"
    "s_add_u32 s87, s87, s88\n"
    "v_exp_f32_e32 v154, v154\n"
    "v_mfma_f32_16x16x32_bf16 v[112:115], a[180:183], v[208:211], v[112:115]\n"
    "s_add_u32 s89, s89, 1\n"
    "buffer_load_dwordx4 a[228:231], v235, s[20:23], 0 offen sc1\n"
    "ds_write_addtid_b32 v164 offset:3072\n"
    "v_mfma_f32_16x16x32_bf16 v[112:115], a[176:179], v[204:207], v[112:115]\n"
    "ds_write_addtid_b32 v165 offset:3328\n"
    "v_readlane_b32 s86, v254, s89\n"
    "v_mfma_f32_16x16x32_bf16 v[116:119], a[176:179], v[212:215], v[116:119]\n"
    "v_exp_f32_e32 v155, v155\n"
    "v_mfma_f32_16x16x32_bf16 v[116:119], a[180:183], v[216:219], v[116:119]\n"
    "s_waitcnt lgkmcnt(11)\n"
    "ds_read_b128 a[172:175], v241 offset:28672\n"
    "v_cvt_pk_bf16_f32 v150, v152, v153\n"
    "v_mfma_f32_16x16x32_bf16 v[136:139], a[188:191], v[216:219], v[136:139]\n"
    "v_exp_f32_e32 v160, v160\n"
    "v_mfma_f32_16x16x32_bf16 v[136:139], a[184:187], v[212:215], v[136:139]\n"
    "v_exp_f32_e32 v161, v161\n"
    "v_mfma_f32_16x16x32_bf16 v[132:135], a[184:187], v[204:207], v[132:135]\n"
    "ds_read_b128 a[176:179], v242 offset:24576\n"
    "v_cvt_pk_bf16_f32 v151, v154, v155\n"
    "v_mfma_f32_16x16x32_bf16 v[132:135], a[188:191], v[208:211], v[132:135]\n"
    "v_exp_f32_e32 v162, v162\n"
    "v_mfma_f32_16x16x32_bf16 v[128:131], a[188:191], v[200:203], v[128:131]\n"
    "v_exp_f32_e32 v163, v163\n"
    "v_mfma_f32_16x16x32_bf16 v[128:131], a[184:187], v[196:199], v[128:131]\n"
    "ds_write_addtid_b32 v172 offset:4096\n"
    "v_cvt_pk_bf16_f32 v158, v160, v161\n"
    "v_mfma_f32_16x16x32_bf16 v[124:127], a[184:187], v[188:191], v[124:127]\n"
    "v_exp_f32_e32 v168, v168\n"
    "v_mfma_f32_16x16x32_bf16 v[124:127], a[188:191], v[192:195], v[124:127]\n"
    "s_sub_u32 s44, s44, 1\n"
    "ds_read_b128 a[180:183], v243 offset:24576\n"
    "ds_write_addtid_b32 v173 offset:4352\n"
    "v_mfma_f32_16x16x32_bf16 v[120:123], a[188:191], v[184:187], v[120:123]\n"
    "s_cmp_lg_u32 s44, s80\n"
    "v_exp_f32_e32 v169, v169\n"
    "v_mfma_f32_16x16x32_bf16 v[120:123], a[184:187], v[180:183], v[120:123]\n"
    "s_waitcnt lgkmcnt(11)\n"
    "ds_write_addtid_b32 v142 offset:512\n"
    "v_cvt_pk_bf16_f32 v159, v162, v163\n"
    "s_cbranch_scc1 .Lat_gloop\n"
    ".Lat_g2:\n"
    "v_mov_b64_e32 v[224:225], v[220:221]\n"
    "v_mov_b64_e32 v[226:227], v[220:221]\n"
    "v_mov_b64_e32 v[228:229], v[220:221]\n"
    "v_mov_b64_e32 v[230:231], v[220:221]\n"
    "v_mov_b64_e32 v[232:233], v[220:221]\n"
    "v_mov_b64_e32 v[234:235], v[220:221]\n"
    "v_mov_b64_e32 v[236:237], v[220:221]\n"
    "v_mov_b64_e32 v[238:239], v[220:221]\n"
    "s_mov_b32 s80, 0\n"
    "s_waitcnt lgkmcnt(0)\n"
    "s_cmp_lg_u32 s44, 0\n"
    "s_cbranch_scc1 .Lat_loop\n"
    ".Lat_tail:\n"
    "v_mfma_f32_16x16x32_bf16 v[212:215], a[160:163], a[128:131], v[236:239]\n"
    ".long 0xbedf00ff\n"
    ".Lat_w9:\n"
    "s_setpc_b64 s[2:3]\n"
    "v_and_b32_e32 v189, 0x1f0, v250\n"
    "v_lshlrev_b32_e32 v190, 3, v250\n"
    "v_mfma_f32_16x16x32_bf16 v[212:215], a[164:167], a[132:135], v[212:215]\n"
    "v_exp_f32_e32 v170, v170\n"
    "v_lshrrev_b32_e32 v253, 4, v250\n"
    "v_mfma_f32_16x16x32_bf16 v[204:207], a[164:167], a[100:103], v[232:235]\n"
    "s_mov_b32 s52, 6100992\n"
    "v_bfe_u32 v251, v253, 4, 2\n"
    "v_lshlrev_b32_e32 v251, 1, v251\n"
    "v_mfma_f32_16x16x32_bf16 v[204:207], a[160:163], a[96:99], v[204:207]\n"
    "s_lshr_b32 s81, s34, 6\n"
    "v_exp_f32_e32 v171, v171\n"
    "v_mfma_f32_16x16x32_bf16 v[196:199], a[160:163], a[64:67], v[228:231]\n"
    "s_lshl_b32 s81, s81, 9\n"
    "ds_read_b128 a[184:187], v242 offset:28672\n"
    "v_cvt_pk_bf16_f32 v166, v168, v169\n"
    "v_mfma_f32_16x16x32_bf16 v[196:199], a[164:167], a[68:71], v[196:199]\n"
    "s_waitcnt lgkmcnt(7)\n"
    "ds_read_b128 a[160:163], v240 offset:26624\n"
    "v_exp_f32_e32 v176, v176\n"
    "v_mfma_f32_16x16x32_bf16 v[196:199], a[168:171], a[72:75], v[196:199]\n"
    "s_add_u32 s81, s81, 6074368\n"
    "ds_read_b128 a[188:191], v243 offset:28672\n"
    "v_exp_f32_e32 v177, v177\n"
    "v_mfma_f32_16x16x32_bf16 v[196:199], a[172:175], a[76:79], v[196:199]\n"
    "s_lshl_b32 s63, s93, 13\n"
    "v_cvt_pk_bf16_f32 v167, v170, v171\n"
    "v_xor_b32_e32 v251, v251, v253\n"
    "v_mfma_f32_16x16x32_bf16 v[204:207], a[172:175], a[108:111], v[204:207]\n"
    "s_xor_b32 s45, s63, 0x4000\n"
    "v_and_b32_e32 v251, 7, v251\n"
    "v_lshlrev_b32_e32 v251, 4, v251\n"
    "v_mfma_f32_16x16x32_bf16 v[204:207], a[168:171], a[104:107], v[204:207]\n"
    "s_mul_i32 s58, s93, 33792\n"
    "ds_read_b128 a[164:167], v241 offset:26624\n"
    "v_exp_f32_e32 v178, v178\n"
    "v_mfma_f32_16x16x32_bf16 v[212:215], a[168:171], a[136:139], v[212:215]\n"
    "s_add_u32 s57, s58, 5513216\n"
    "ds_write_addtid_b32 v143 offset:768\n"
    "v_lshrrev_b32_e32 v253, 3, v253\n"
    "v_mfma_f32_16x16x32_bf16 v[212:215], a[172:175], a[140:143], v[212:215]\n"
    "s_waitcnt lgkmcnt(6)\n"
    "v_lshl_add_u32 v251, v253, 9, v251\n"
    "v_bfe_u32 v255, v250, 2, 4\n"
    "v_mfma_f32_16x16x32_bf16 v[212:215], a[176:179], a[144:147], v[212:215]\n"
    "s_add_u32 s58, s58, 5378048\n"
    "ds_write_addtid_b32 v150 offset:1536\n"
    "v_mov_b32_e32 v254, s39\n"
    "v_mfma_f32_16x16x32_bf16 v[212:215], a[180:183], a[148:151], v[212:215]\n"
    "s_add_u32 s36, s35, 0x8000\n"
    "ds_write_addtid_b32 v151 offset:1792\n"
    "v_lshrrev_b32_e32 v252, 2, v250\n"
    "v_mfma_f32_16x16x32_bf16 v[204:207], a[180:183], a[116:119], v[204:207]\n"
    "s_add_u32 s37, s35, 0x10000\n"
    "v_exp_f32_e32 v179, v179\n"
    "v_mfma_f32_16x16x32_bf16 v[204:207], a[176:179], a[112:115], v[204:207]\n"
    "s_cmp_lt_u32 s32, 2\n"
    "s_cselect_b32 s37, s37, 2.0\n"
    "ds_write_addtid_b32 v158 offset:2560\n"
    "v_mfma_f32_16x16x32_bf16 v[196:199], a[176:179], a[80:83], v[196:199]\n"
    "s_cmp_lt_u32 s36, 5120000\n"
    "s_cselect_b32 s36, s36, 2.0\n"
    "ds_write_addtid_b32 v159 offset:2816\n"
    "v_mfma_f32_16x16x32_bf16 v[196:199], a[180:183], a[84:87], v[196:199]\n"
    "s_waitcnt lgkmcnt(6)\n"
    "ds_write_addtid_b32 v166 offset:3584\n"
    "v_cvt_pk_bf16_f32 v174, v176, v177\n"
    "v_mfma_f32_16x16x32_bf16 v[196:199], a[184:187], a[88:91], v[196:199]\n"
    "s_cmp_lt_u32 s37, 5120000\n"
    "s_cselect_b32 s37, s37, 2.0\n"
    "ds_read_b128 a[168:171], v240 offset:30720\n"
    "v_mfma_f32_16x16x32_bf16 v[196:199], a[188:191], a[92:95], v[196:199]\n"
    "buffer_load_dword v253, v255, s[8:11], s52 offen offset:0 sc1\n"
    "v_mfma_f32_16x16x32_bf16 v[204:207], a[188:191], a[124:127], v[204:207]\n"
    "ds_write_addtid_b32 v167 offset:3840\n"
    "v_mfma_f32_16x16x32_bf16 v[204:207], a[184:187], a[120:123], v[204:207]\n"
    "ds_read_b128 a[176:179], v242 offset:26624\n"
    "v_cvt_pk_bf16_f32 v175, v178, v179\n"
    "v_mfma_f32_16x16x32_bf16 v[212:215], a[184:187], a[152:155], v[212:215]\n"
    "ds_read_b128 a[172:175], v241 offset:30720\n"
    "ds_write_addtid_b32 v174 offset:4608\n"
    "v_mfma_f32_16x16x32_bf16 v[212:215], a[188:191], a[156:159], v[212:215]\n"
    "s_waitcnt lgkmcnt(11)\n"
    "v_exp_f32_e32 v196, v196\n"
    "v_mfma_f32_16x16x32_bf16 v[216:219], a[160:163], a[128:131], v[236:239]\n"
    "ds_write_addtid_b32 v175 offset:4864\n"
    "v_mfma_f32_16x16x32_bf16 v[216:219], a[164:167], a[132:135], v[216:219]\n"
    "ds_read_b128 a[184:187], v242 offset:30720\n"
    "v_mfma_f32_16x16x32_bf16 v[208:211], a[164:167], a[100:103], v[232:235]\n"
    "ds_read_b128 a[180:183], v243 offset:26624\n"
    "v_mfma_f32_16x16x32_bf16 v[208:211], a[160:163], a[96:99], v[208:211]\n"
    "s_waitcnt lgkmcnt(2)\n"
    "v_mfma_f32_16x16x32_bf16 v[200:203], a[160:163], a[64:67], v[228:231]\n"
    "s_barrier\n"
    "v_mfma_f32_16x16x32_bf16 v[200:203], a[164:167], a[68:71], v[200:203]\n"
    "ds_read_b64_tr_b16 a[160:161], v244 offset:16384\n"
    "v_mfma_f32_16x16x32_bf16 v[200:203], a[168:171], a[72:75], v[200:203]\n"
    "s_add_u32 m0, s43, 8192\n"
    "ds_read_b64_tr_b16 a[162:163], v244 offset:18432\n"
    "v_mfma_f32_16x16x32_bf16 v[200:203], a[172:175], a[76:79], v[200:203]\n"
    "buffer_load_dwordx4 v248, s[8:11], s40 offen offset:0 sc1 lds\n"
    "v_mfma_f32_16x16x32_bf16 v[208:211], a[172:175], a[108:111], v[208:211]\n"
    "ds_read2st64_b32 v[178:179], v249 offset0:18 offset1:19\n"
    "v_mfma_f32_16x16x32_bf16 v[208:211], a[168:171], a[104:107], v[208:211]\n"
    "ds_read2st64_b32 v[176:177], v249 offset0:16 offset1:17\n"
    "v_mfma_f32_16x16x32_bf16 v[216:219], a[168:171], a[136:139], v[216:219]\n"
    "ds_read_b128 a[188:191], v243 offset:30720\n"
    "ds_read_b64_tr_b16 a[164:165], v244 offset:49152\n"
    "v_mfma_f32_16x16x32_bf16 v[216:219], a[172:175], a[140:143], v[216:219]\n"
    "s_waitcnt lgkmcnt(6)\n"
    "ds_read2st64_b32 v[168:169], v249 offset0:12 offset1:13\n"
    "ds_read2st64_b32 v[170:171], v249 offset0:14 offset1:15\n"
    "v_mfma_f32_16x16x32_bf16 v[216:219], a[176:179], a[144:147], v[216:219]\n"
    "buffer_load_dwordx4 v248, s[8:11], s40 offen offset:1024 sc1 lds\n"
    "v_mfma_f32_16x16x32_bf16 v[216:219], a[180:183], a[148:151], v[216:219]\n"
    "ds_read2st64_b32 v[162:163], v249 offset0:10 offset1:11\n"
    "v_mfma_f32_16x16x32_bf16 v[208:211], a[180:183], a[116:119], v[208:211]\n"
    "ds_read_b64_tr_b16 a[166:167], v244 offset:51200\n"
    "ds_read2st64_b32 v[160:161], v249 offset0:8 offset1:9\n"
    "v_mfma_f32_16x16x32_bf16 v[208:211], a[176:179], a[112:115], v[208:211]\n"
    "ds_read2st64_b32 v[154:155], v249 offset0:6 offset1:7\n"
    "v_mfma_f32_16x16x32_bf16 v[200:203], a[176:179], a[80:83], v[200:203]\n"
    "ds_read2st64_b32 v[152:153], v249 offset0:4 offset1:5\n"
    "v_mfma_f32_16x16x32_bf16 v[200:203], a[180:183], a[84:87], v[200:203]\n"
    "s_waitcnt lgkmcnt(8)\n"
    "ds_read2st64_b32 v[144:145], v249 offset0:0 offset1:1\n"
    "ds_read2st64_b32 v[146:147], v249 offset0:2 offset1:3\n"
    "v_mfma_f32_16x16x32_bf16 v[200:203], a[184:187], a[88:91], v[200:203]\n"
    "buffer_load_dwordx4 v248, s[8:11], s40 offen offset:2048 sc1 lds\n"
    "v_mfma_f32_16x16x32_bf16 v[200:203], a[188:191], a[92:95], v[200:203]\n"
    "ds_read_b64_tr_b16 a[168:169], v245 offset:16384\n"
    "v_mfma_f32_16x16x32_bf16 v[208:211], a[188:191], a[124:127], v[208:211]\n"
    "v_exp_f32_e32 v197, v197\n"
    "v_mfma_f32_16x16x32_bf16 v[208:211], a[184:187], a[120:123], v[208:211]\n"
    "ds_read_b64_tr_b16 a[170:171], v245 offset:18432\n"
    "v_mfma_f32_16x16x32_bf16 v[216:219], a[184:187], a[152:155], v[216:219]\n"
    "ds_read_b64_tr_b16 a[172:173], v245 offset:49152\n"
    "v_mfma_f32_16x16x32_bf16 v[216:219], a[188:191], a[156:159], v[216:219]\n"
    "s_waitcnt lgkmcnt(3)\n"
    "ds_read_b64_tr_b16 a[184:185], v247 offset:16384\n"
    "v_mfma_f32_16x16x32_bf16 a[252:255], a[160:163], v[172:175], a[252:255]\n"
    "buffer_load_dwordx4 v248, s[8:11], s40 offen offset:3072 sc1 lds\n"
    "v_mfma_f32_16x16x32_bf16 a[252:255], a[164:167], v[176:179], a[252:255]\n"
    "s_add_u32 s40, s40, 0x8000\n"
    "ds_read_b64_tr_b16 a[174:175], v245 offset:51200\n"
    "v_mfma_f32_16x16x32_bf16 a[248:251], a[164:167], v[168:171], a[248:251]\n"
    "s_add_u32 m0, s43, 12288\n"
    "v_exp_f32_e32 v198, v198\n"
    "v_mfma_f32_16x16x32_bf16 a[248:251], a[160:163], v[164:167], a[248:251]\n"
    "ds_read_b64_tr_b16 a[176:177], v246 offset:16384\n"
    "v_mfma_f32_16x16x32_bf16 a[244:247], a[160:163], v[156:159], a[244:247]\n"
    "ds_read_b64_tr_b16 a[178:179], v246 offset:18432\n"
    "v_mfma_f32_16x16x32_bf16 a[244:247], a[164:167], v[160:163], a[244:247]\n"
    "buffer_load_dwordx4 v248, s[8:11], s41 offen offset:0 sc1 lds\n"
    "v_mfma_f32_16x16x32_bf16 a[240:243], a[164:167], v[152:155], a[240:243]\n"
    "ds_read_b64_tr_b16 a[186:187], v247 offset:18432\n"
    "v_mfma_f32_16x16x32_bf16 a[240:243], a[160:163], v[148:151], a[240:243]\n"
    "ds_read_b64_tr_b16 a[180:181], v246 offset:49152\n"
    "v_mfma_f32_16x16x32_bf16 a[236:239], a[160:163], v[140:143], a[236:239]\n"
    "ds_read_b64_tr_b16 a[182:183], v246 offset:51200\n"
    "v_mfma_f32_16x16x32_bf16 a[236:239], a[164:167], v[144:147], a[236:239]\n"
    "s_waitcnt lgkmcnt(5)\n"
    "v_exp_f32_e32 v199, v199\n"
    "v_mfma_f32_16x16x32_bf16 v[0:3], a[168:171], v[140:143], v[0:3]\n"
    "buffer_load_dwordx4 v248, s[8:11], s41 offen offset:1024 sc1 lds\n"
    "v_mfma_f32_16x16x32_bf16 v[0:3], a[172:175], v[144:147], v[0:3]\n"
    "ds_read_b64_tr_b16 a[188:189], v247 offset:49152\n"
    "v_cvt_pk_bf16_f32 v196, v196, v197\n"
    "v_mfma_f32_16x16x32_bf16 v[4:7], a[172:175], v[152:155], v[4:7]\n"
    "ds_read_b64_tr_b16 a[160:161], v244 offset:20480\n"
    "v_mfma_f32_16x16x32_bf16 v[4:7], a[168:171], v[148:151], v[4:7]\n"
    "ds_read_b64_tr_b16 a[190:191], v247 offset:51200\n"
    "v_mfma_f32_16x16x32_bf16 v[8:11], a[168:171], v[156:159], v[8:11]\n"
    "ds_read_b64_tr_b16 a[162:163], v244 offset:22528\n"
    "v_mfma_f32_16x16x32_bf16 v[8:11], a[172:175], v[160:163], v[8:11]\n"
    "buffer_load_dwordx4 v248, s[8:11], s41 offen offset:2048 sc1 lds\n"
    "v_mfma_f32_16x16x32_bf16 v[12:15], a[172:175], v[168:171], v[12:15]\n"
    "v_exp_f32_e32 v204, v204\n"
    "v_mfma_f32_16x16x32_bf16 v[12:15], a[168:171], v[164:167], v[12:15]\n"
    "s_waitcnt vmcnt(7)\n"
    "v_mfma_f32_16x16x32_bf16 v[16:19], a[168:171], v[172:175], v[16:19]\n"
    "s_barrier\n"
    "v_mfma_f32_16x16x32_bf16 v[16:19], a[172:175], v[176:179], v[16:19]\n"
    "s_waitcnt lgkmcnt(4)\n"
    "v_add_u32_e32 v249, s45, v252\n"
    "v_add_u32_e32 v252, s63, v252\n"
    "v_mfma_f32_16x16x32_bf16 v[36:39], a[176:179], v[172:175], v[36:39]\n"
    "buffer_store_dword v254, off, s[8:11], s84 offset:0 sc1\n"
    "v_mfma_f32_16x16x32_bf16 v[36:39], a[180:183], v[176:179], v[36:39]\n"
    "s_mul_i32 s45, s49, 63\n"
    "ds_read_b64_tr_b16 a[164:165], v244 offset:53248\n"
    "v_mfma_f32_16x16x32_bf16 v[32:35], a[180:183], v[168:171], v[32:35]\n"
    "s_add_u32 s45, s45, s33\n"
    "v_exp_f32_e32 v205, v205\n"
    "v_mfma_f32_16x16x32_bf16 v[32:35], a[176:179], v[164:167], v[32:35]\n"
    "buffer_load_dwordx4 v248, s[8:11], s41 offen offset:3072 sc1 lds\n"
    "v_mfma_f32_16x16x32_bf16 v[28:31], a[176:179], v[156:159], v[28:31]\n"
    "s_add_u32 s41, s41, 0x8000\n"
    "v_cmp_ne_u32_e32 vcc, s39, v253\n"
    "v_cvt_pk_bf16_f32 v197, v198, v199\n"
    "v_mfma_f32_16x16x32_bf16 v[28:31], a[180:183], v[160:163], v[28:31]\n"
    "s_mov_b32 m0, s63\n"
    "v_exp_f32_e32 v206, v206\n"
    "v_mfma_f32_16x16x32_bf16 v[24:27], a[180:183], v[152:155], v[24:27]\n"
    "s_mul_i32 s45, s45, 405504\n"
    "v_exp_f32_e32 v207, v207\n"
    "v_mfma_f32_16x16x32_bf16 v[24:27], a[176:179], v[148:151], v[24:27]\n"
    "s_cbranch_vccnz .Lat_wpoll\n"
    ".Lat_wpoll_ret:\n"
    "v_mfma_f32_16x16x32_bf16 v[20:23], a[176:179], v[140:143], v[20:23]\n"
    "s_add_u32 s62, s57, 0\n"
    "ds_read_b64_tr_b16 a[166:167], v244 offset:55296\n"
    "v_cvt_pk_bf16_f32 v204, v204, v205\n"
    "v_mfma_f32_16x16x32_bf16 v[20:23], a[180:183], v[144:147], v[20:23]\n"
    "s_waitcnt lgkmcnt(3)\n"
    "ds_read_b64_tr_b16 a[168:169], v245 offset:20480\n"
    "v_mfma_f32_16x16x32_bf16 v[40:43], a[184:187], v[140:143], v[40:43]\n"
    "buffer_load_dwordx4 a[216:219], v250, s[8:11], s62 offen offset:0 sc1\n"
    "v_mfma_f32_16x16x32_bf16 v[40:43], a[188:191], v[144:147], v[40:43]\n"
    "s_add_u32 s45, s45, 16588800\n"
    "v_exp_f32_e32 v212, v212\n"
    "v_mfma_f32_16x16x32_bf16 v[44:47], a[188:191], v[152:155], v[44:47]\n"
    "s_lshl_b32 s46, s51, 12\n"
    "ds_write_addtid_b32 v196 offset:0\n"
    "v_mfma_f32_16x16x32_bf16 v[44:47], a[184:187], v[148:151], v[44:47]\n"
    "s_lshl_b32 s48, s50, 15\n"
    "ds_read_b64_tr_b16 a[170:171], v245 offset:22528\n"
    "v_mfma_f32_16x16x32_bf16 v[48:51], a[184:187], v[156:159], v[48:51]\n"
    "s_add_u32 s46, s46, s48\n"
    "ds_read_b64_tr_b16 a[176:177], v246 offset:20480\n"
    "v_mfma_f32_16x16x32_bf16 v[48:51], a[188:191], v[160:163], v[48:51]\n"
    "buffer_load_dwordx4 a[220:223], v250, s[8:11], s62 offen offset:1024 sc1\n"
    "v_mfma_f32_16x16x32_bf16 v[52:55], a[188:191], v[168:171], v[52:55]\n"
    "s_cmp_eq_u32 s51, 0\n"
    "s_cselect_b32 s47, 2.0, 0\n"
    "ds_read_b64_tr_b16 a[178:179], v246 offset:22528\n"
    "v_mfma_f32_16x16x32_bf16 v[52:55], a[184:187], v[164:167], v[52:55]\n"
    "s_add_u32 s47, s47, 98304\n"
    "ds_read_b64_tr_b16 a[172:173], v245 offset:53248\n"
    "v_mfma_f32_16x16x32_bf16 v[56:59], a[184:187], v[172:175], v[56:59]\n"
    "s_lshl_b32 s61, s50, 9\n"
    "ds_read_b64_tr_b16 a[174:175], v245 offset:55296\n"
    "v_mfma_f32_16x16x32_bf16 v[56:59], a[188:191], v[176:179], v[56:59]\n"
    "s_waitcnt lgkmcnt(7)\n"
    "ds_read_b64_tr_b16 a[184:185], v247 offset:20480\n"
    "v_mfma_f32_16x16x32_bf16 v[76:79], a[160:163], v[172:175], v[76:79]\n"
    "buffer_load_dwordx4 a[224:227], v250, s[8:11], s62 offen offset:2048 sc1\n"
    "v_mfma_f32_16x16x32_bf16 v[76:79], a[164:167], v[176:179], v[76:79]\n"
    "s_add_u32 s61, s61, s47\n"
    "ds_write_addtid_b32 v197 offset:256\n"
    "v_mfma_f32_16x16x32_bf16 v[72:75], a[164:167], v[168:171], v[72:75]\n"
    "s_add_u32 s48, s32, 3\n"
    "v_exp_f32_e32 v213, v213\n"
    "v_mfma_f32_16x16x32_bf16 v[72:75], a[160:163], v[164:167], v[72:75]\n"
    "s_and_b32 s48, s48, 3\n"
    "ds_read_b64_tr_b16 a[186:187], v247 offset:22528\n"
    "v_mfma_f32_16x16x32_bf16 v[68:71], a[160:163], v[156:159], v[68:71]\n"
    "s_mul_i32 s48, s48, 101376\n"
    "ds_read_b64_tr_b16 a[180:181], v246 offset:53248\n"
    "v_mfma_f32_16x16x32_bf16 v[68:71], a[164:167], v[160:163], v[68:71]\n"
    "buffer_load_dwordx4 a[228:231], v250, s[8:11], s62 offen offset:3072 sc1\n"
    "v_mfma_f32_16x16x32_bf16 v[64:67], a[164:167], v[152:155], v[64:67]\n"
    "s_add_u32 s62, s57, 4096\n"
    "ds_read_b64_tr_b16 a[182:183], v246 offset:55296\n"
    "v_mfma_f32_16x16x32_bf16 v[64:67], a[160:163], v[148:151], v[64:67]\n"
    "s_add_u32 s48, s48, s45\n"
    "ds_read_b64_tr_b16 a[188:189], v247 offset:53248\n"
    "v_cvt_pk_bf16_f32 v205, v206, v207\n"
    "v_mfma_f32_16x16x32_bf16 v[60:63], a[160:163], v[140:143], v[60:63]\n"
    "s_add_u32 s29, s48, s46\n"
    "ds_read_b64_tr_b16 a[190:191], v247 offset:55296\n"
    "v_mfma_f32_16x16x32_bf16 v[60:63], a[164:167], v[144:147], v[60:63]\n"
    "s_waitcnt lgkmcnt(7)\n"
    "ds_write_addtid_b32 v204 offset:1024\n"
    "v_mfma_f32_16x16x32_bf16 v[80:83], a[168:171], v[140:143], v[80:83]\n"
    "buffer_load_dwordx4 a[232:235], v250, s[8:11], s62 offen offset:0 sc1\n"
    "v_mfma_f32_16x16x32_bf16 v[80:83], a[172:175], v[144:147], v[80:83]\n"
    "s_add_u32 s29, s29, 8192\n"
    "v_exp_f32_e32 v214, v214\n"
    "v_mfma_f32_16x16x32_bf16 v[84:87], a[172:175], v[152:155], v[84:87]\n"
    "s_add_u32 s89, s48, s61\n"
    "v_mfma_f32_16x16x32_bf16 v[84:87], a[168:171], v[148:151], v[84:87]\n"
    "s_add_u32 s89, s89, 128\n"
    "v_exp_f32_e32 v215, v215\n"
    "v_mfma_f32_16x16x32_bf16 v[88:91], a[168:171], v[156:159], v[88:91]\n"
    "s_add_u32 s48, s32, 2\n"
    "ds_write_addtid_b32 v205 offset:1280\n"
    "v_cvt_pk_bf16_f32 v212, v212, v213\n"
    "v_mfma_f32_16x16x32_bf16 v[88:91], a[172:175], v[160:163], v[88:91]\n"
    "s_waitcnt lgkmcnt(2)\n"
    "v_mfma_f32_16x16x32_bf16 v[92:95], a[172:175], v[168:171], v[92:95]\n"
    "s_barrier\n"
    "v_mfma_f32_16x16x32_bf16 v[92:95], a[168:171], v[164:167], v[92:95]\n"
    "buffer_load_dwordx4 v[192:195], v250, s[8:11], s62 offen offset:1024 sc1\n"
    "v_mfma_f32_16x16x32_bf16 v[96:99], a[168:171], v[172:175], v[96:99]\n"
    "s_add_u32 m0, s43, 16384\n"
    "ds_read_b128 a[160:163], v240 offset:0\n"
    "v_mfma_f32_16x16x32_bf16 v[96:99], a[172:175], v[176:179], v[96:99]\n"
    "v_exp_f32_e32 v200, v200\n"
    "v_mfma_f32_16x16x32_bf16 v[116:119], a[176:179], v[172:175], v[116:119]\n"
    "buffer_load_dwordx4 v248, s[8:11], s40 offen offset:0 sc1 lds\n"
    "v_mfma_f32_16x16x32_bf16 v[116:119], a[180:183], v[176:179], v[116:119]\n"
    "s_mov_b32 m0, s63\n"
    "ds_read_b128 a[168:171], v240 offset:4096\n"
    "v_mfma_f32_16x16x32_bf16 v[112:115], a[180:183], v[168:171], v[112:115]\n"
    "s_and_b32 s48, s48, 3\n"
    "v_exp_f32_e32 v201, v201\n"
    "v_mfma_f32_16x16x32_bf16 v[112:115], a[176:179], v[164:167], v[112:115]\n"
    "s_mul_i32 s48, s48, 101376\n"
    "ds_read_b128 a[172:175], v241 offset:4096\n"
    "v_mfma_f32_16x16x32_bf16 v[108:111], a[176:179], v[156:159], v[108:111]\n"
    "s_add_u32 m0, s43, 16384\n"
    "v_cvt_pk_bf16_f32 v213, v214, v215\n"
    "v_mfma_f32_16x16x32_bf16 v[108:111], a[180:183], v[160:163], v[108:111]\n"
    "buffer_load_dwordx4 v248, s[8:11], s40 offen offset:1024 sc1 lds\n"
    "v_mfma_f32_16x16x32_bf16 v[104:107], a[180:183], v[152:155], v[104:107]\n"
    "s_mov_b32 m0, s63\n"
    "v_exp_f32_e32 v202, v202\n"
    "v_mfma_f32_16x16x32_bf16 v[104:107], a[176:179], v[148:151], v[104:107]\n"
    "s_add_u32 s48, s48, s45\n"
    "ds_read_b128 a[164:167], v241 offset:0\n"
    "v_mfma_f32_16x16x32_bf16 v[100:103], a[176:179], v[140:143], v[100:103]\n"
    "s_add_u32 s28, s48, s46\n"
    "ds_write_addtid_b32 v212 offset:2048\n"
    "v_mfma_f32_16x16x32_bf16 v[100:103], a[180:183], v[144:147], v[100:103]\n"
    "v_exp_f32_e32 v203, v203\n"
    "v_mfma_f32_16x16x32_bf16 v[120:123], a[184:187], v[140:143], v[120:123]\n"
    "s_add_u32 m0, s43, 16384\n"
    "v_cvt_pk_bf16_f32 v198, v200, v201\n"
    "v_mfma_f32_16x16x32_bf16 v[120:123], a[188:191], v[144:147], v[120:123]\n"
    "buffer_load_dwordx4 v248, s[8:11], s40 offen offset:2048 sc1 lds\n"
    "v_mfma_f32_16x16x32_bf16 v[124:127], a[188:191], v[152:155], v[124:127]\n"
    "s_mov_b32 m0, s63\n"
    "v_exp_f32_e32 v208, v208\n"
    "v_mfma_f32_16x16x32_bf16 v[124:127], a[184:187], v[148:151], v[124:127]\n"
    "s_add_u32 s28, s28, 16384\n"
    "v_exp_f32_e32 v209, v209\n"
    "v_mfma_f32_16x16x32_bf16 v[128:131], a[184:187], v[156:159], v[128:131]\n"
    "s_add_u32 s88, s48, s61\n"
    "ds_read_b128 a[176:179], v242 offset:0\n"
    "v_cvt_pk_bf16_f32 v199, v202, v203\n"
    "v_mfma_f32_16x16x32_bf16 v[128:131], a[188:191], v[160:163], v[128:131]\n"
    "s_add_u32 m0, s43, 16384\n"
    "v_exp_f32_e32 v210, v210\n"
    "v_mfma_f32_16x16x32_bf16 v[132:135], a[188:191], v[168:171], v[132:135]\n"
    "buffer_load_dwordx4 v248, s[8:11], s40 offen offset:3072 sc1 lds\n"
    "v_mfma_f32_16x16x32_bf16 v[132:135], a[184:187], v[164:167], v[132:135]\n"
    "s_mov_b32 m0, s63\n"
    "ds_read_b128 a[180:183], v243 offset:0\n"
    "v_mfma_f32_16x16x32_bf16 v[136:139], a[184:187], v[172:175], v[136:139]\n"
    "s_add_u32 s88, s88, 256\n"
    "v_exp_f32_e32 v211, v211\n"
    "v_mfma_f32_16x16x32_bf16 v[136:139], a[188:191], v[176:179], v[136:139]\n"
    "s_waitcnt lgkmcnt(3)\n"
    "ds_read_b128 a[184:187], v242 offset:4096\n"
    "v_cvt_pk_bf16_f32 v206, v208, v209\n"
    "v_mfma_f32_16x16x32_bf16 v[172:175], a[160:163], a[128:131], v[236:239]\n"
    "s_add_u32 m0, s43, 20480\n"
    "ds_read_b128 a[188:191], v243 offset:4096\n"
    "v_mfma_f32_16x16x32_bf16 v[172:175], a[164:167], a[132:135], v[172:175]\n"
    "buffer_load_dwordx4 v248, s[8:11], s41 offen offset:0 sc1 lds\n"
    "v_mfma_f32_16x16x32_bf16 v[164:167], a[164:167], a[100:103], v[232:235]\n"
    "s_mov_b32 m0, s63\n"
    "v_exp_f32_e32 v216, v216\n"
    "v_mfma_f32_16x16x32_bf16 v[164:167], a[160:163], a[96:99], v[164:167]\n"
    "s_add_u32 s48, s32, 1\n"
    "v_exp_f32_e32 v217, v217\n"
    "v_mfma_f32_16x16x32_bf16 v[156:159], a[160:163], a[64:67], v[228:231]\n"
    "s_and_b32 s48, s48, 3\n"
    "ds_write_addtid_b32 v213 offset:2304\n"
    "v_mfma_f32_16x16x32_bf16 v[156:159], a[164:167], a[68:71], v[156:159]\n"
    "ds_write_addtid_b32 v198 offset:512\n"
    "v_mfma_f32_16x16x32_bf16 v[156:159], a[168:171], a[72:75], v[156:159]\n"
    "s_add_u32 m0, s43, 20480\n"
    "ds_read_b128 a[160:163], v240 offset:2048\n"
    "v_cvt_pk_bf16_f32 v207, v210, v211\n"
    "v_mfma_f32_16x16x32_bf16 v[156:159], a[172:175], a[76:79], v[156:159]\n"
    "buffer_load_dwordx4 v248, s[8:11], s41 offen offset:1024 sc1 lds\n"
    "v_mfma_f32_16x16x32_bf16 v[164:167], a[172:175], a[108:111], v[164:167]\n"
    "s_mov_b32 m0, s63\n"
    "v_exp_f32_e32 v218, v218\n"
    "v_mfma_f32_16x16x32_bf16 v[164:167], a[168:171], a[104:107], v[164:167]\n"
    "s_mul_i32 s48, s48, 101376\n"
    "v_exp_f32_e32 v219, v219\n"
    "v_mfma_f32_16x16x32_bf16 v[172:175], a[168:171], a[136:139], v[172:175]\n"
    "s_add_u32 s48, s48, s45\n"
    "ds_write_addtid_b32 v199 offset:768\n"
    "v_mfma_f32_16x16x32_bf16 v[172:175], a[172:175], a[140:143], v[172:175]\n"
    "s_waitcnt lgkmcnt(6)\n"
    "ds_read_b128 a[164:167], v241 offset:2048\n"
    "v_cvt_pk_bf16_f32 v214, v216, v217\n"
    "v_mfma_f32_16x16x32_bf16 v[172:175], a[176:179], a[144:147], v[172:175]\n"
    "s_add_u32 m0, s43, 20480\n"
    "ds_read_b128 a[168:171], v240 offset:6144\n"
    "v_cvt_pk_bf16_f32 v215, v218, v219\n"
    "v_mfma_f32_16x16x32_bf16 v[172:175], a[180:183], a[148:151], v[172:175]\n"
    "buffer_load_dwordx4 v248, s[8:11], s41 offen offset:2048 sc1 lds\n"
    "v_mfma_f32_16x16x32_bf16 v[164:167], a[180:183], a[116:119], v[164:167]\n"
    "s_mov_b32 m0, s63\n"
    "ds_read_b128 a[172:175], v241 offset:6144\n"
    "v_mfma_f32_16x16x32_bf16 v[164:167], a[176:179], a[112:115], v[164:167]\n"
    "s_add_u32 s82, s48, s46\n"
    "ds_write_addtid_b32 v206 offset:1536\n"
    "v_mfma_f32_16x16x32_bf16 v[156:159], a[176:179], a[80:83], v[156:159]\n"
    "s_add_u32 s82, s82, 24576\n"
    "ds_write_addtid_b32 v207 offset:1792\n"
    "v_mfma_f32_16x16x32_bf16 v[156:159], a[180:183], a[84:87], v[156:159]\n"
    "s_waitcnt lgkmcnt(9)\n"
    "ds_write_addtid_b32 v214 offset:2560\n"
    "v_mfma_f32_16x16x32_bf16 v[156:159], a[184:187], a[88:91], v[156:159]\n"
    "s_add_u32 m0, s43, 20480\n"
    "ds_read_b128 a[176:179], v242 offset:2048\n"
    "v_mfma_f32_16x16x32_bf16 v[156:159], a[188:191], a[92:95], v[156:159]\n"
    "buffer_load_dwordx4 v248, s[8:11], s41 offen offset:3072 sc1 lds\n"
    "v_mfma_f32_16x16x32_bf16 v[164:167], a[188:191], a[124:127], v[164:167]\n"
    "s_mov_b32 m0, s63\n"
    "ds_read_b128 a[180:183], v243 offset:2048\n"
    "v_mfma_f32_16x16x32_bf16 v[164:167], a[184:187], a[120:123], v[164:167]\n"
    "s_add_u32 s87, s48, s61\n"
    "ds_write_addtid_b32 v215 offset:2816\n"
    "v_mfma_f32_16x16x32_bf16 v[172:175], a[184:187], a[152:155], v[172:175]\n"
    "s_add_u32 s87, s87, 384\n"
    "v_exp_f32_e32 v156, v156\n"
    "v_mfma_f32_16x16x32_bf16 v[172:175], a[188:191], a[156:159], v[172:175]\n"
    "s_waitcnt lgkmcnt(8)\n"
    "v_exp_f32_e32 v157, v157\n"
    "v_mfma_f32_16x16x32_bf16 v[176:179], a[160:163], a[128:131], v[236:239]\n"
    "s_mul_i32 s48, s32, 101376\n"
    "v_exp_f32_e32 v158, v158\n"
    "v_mfma_f32_16x16x32_bf16 v[176:179], a[164:167], a[132:135], v[176:179]\n"
    "s_add_u32 s48, s48, s45\n"
    "ds_read_b128 a[184:187], v242 offset:6144\n"
    "v_mfma_f32_16x16x32_bf16 v[168:171], a[164:167], a[100:103], v[232:235]\n"
    "s_add_u32 s59, s48, s46\n"
    "v_exp_f32_e32 v159, v159\n"
    "v_mfma_f32_16x16x32_bf16 v[168:171], a[160:163], a[96:99], v[168:171]\n"
    "s_waitcnt lgkmcnt(1)\n"
    "s_waitcnt vmcnt(14)\n"
    "v_mfma_f32_16x16x32_bf16 v[160:163], a[160:163], a[64:67], v[228:231]\n"
    "s_barrier\n"
    "v_mfma_f32_16x16x32_bf16 v[160:163], a[164:167], a[68:71], v[160:163]\n"
    "ds_read2st64_b32 a[196:197], v249 offset0:0 offset1:1\n"
    "v_cvt_pk_bf16_f32 v156, v156, v157\n"
    "v_mfma_f32_16x16x32_bf16 v[160:163], a[168:171], a[72:75], v[160:163]\n"
    "s_add_u32 s60, s48, s61\n"
    "v_exp_f32_e32 v164, v164\n"
    "v_mfma_f32_16x16x32_bf16 v[160:163], a[172:175], a[76:79], v[160:163]\n"
    "s_and_b32 s48, s32, 1\n"
    "v_exp_f32_e32 v165, v165\n"
    "v_mfma_f32_16x16x32_bf16 v[168:171], a[172:175], a[108:111], v[168:171]\n"
    "s_xor_b32 s48, s48, s50\n"
    "ds_read2st64_b32 a[198:199], v249 offset0:2 offset1:3\n"
    "ds_write_addtid_b32 v156 offset:3072\n"
    "v_mfma_f32_16x16x32_bf16 v[168:171], a[168:171], a[104:107], v[168:171]\n"
    "s_sub_u32 s61, s32, s48\n"
    "ds_read_b128 a[188:191], v243 offset:6144\n"
    "v_cvt_pk_bf16_f32 v157, v158, v159\n"
    "v_mfma_f32_16x16x32_bf16 v[176:179], a[168:171], a[136:139], v[176:179]\n"
    "s_and_b32 s61, s61, 3\n"
    "ds_read2st64_b32 a[192:193], v252 offset0:0 offset1:1\n"
    "v_mfma_f32_16x16x32_bf16 v[176:179], a[172:175], a[140:143], v[176:179]\n"
    "ds_read_b128 a[168:171], v240 offset:12288\n"
    "v_mfma_f32_16x16x32_bf16 v[176:179], a[176:179], a[144:147], v[176:179]\n"
    "s_add_u32 s61, s61, 8\n"
    "v_exp_f32_e32 v166, v166\n"
    "v_mfma_f32_16x16x32_bf16 v[176:179], a[180:183], a[148:151], v[176:179]\n"
    "s_mul_i32 s48, s48, 101376\n"
    "v_exp_f32_e32 v167, v167\n"
    "v_mfma_f32_16x16x32_bf16 v[168:171], a[180:183], a[116:119], v[168:171]\n"
    "s_add_u32 s48, s48, s45\n"
    "ds_read_b128 a[160:163], v240 offset:8192\n"
    "v_cvt_pk_bf16_f32 v164, v164, v165\n"
    "v_mfma_f32_16x16x32_bf16 v[168:171], a[176:179], a[112:115], v[168:171]\n"
    "s_lshl_b32 s80, s61, 13\n"
    "v_exp_f32_e32 v172, v172\n"
    "v_mfma_f32_16x16x32_bf16 v[160:163], a[176:179], a[80:83], v[160:163]\n"
    "s_add_u32 s80, s80, s48\n"
    "ds_read_b128 a[164:167], v241 offset:8192\n"
    "v_mfma_f32_16x16x32_bf16 v[160:163], a[180:183], a[84:87], v[160:163]\n"
    "s_waitcnt lgkmcnt(4)\n"
    "ds_read_b128 a[172:175], v241 offset:12288\n"
    "ds_write_addtid_b32 v157 offset:3328\n"
    "v_mfma_f32_16x16x32_bf16 v[160:163], a[184:187], a[88:91], v[160:163]\n"
    "s_lshl_b32 s46, s51, 12\n"
    "ds_read2st64_b32 a[194:195], v252 offset0:2 offset1:3\n"
    "ds_write_addtid_b32 v164 offset:4096\n"
    "v_mfma_f32_16x16x32_bf16 v[160:163], a[188:191], a[92:95], v[160:163]\n"
    "s_add_u32 s80, s80, s46\n"
    "v_exp_f32_e32 v173, v173\n"
    "v_mfma_f32_16x16x32_bf16 v[168:171], a[188:191], a[124:127], v[168:171]\n"
    "s_lshl_b32 s86, s61, 7\n"
    "ds_read_b128 a[176:179], v242 offset:8192\n"
    "v_cvt_pk_bf16_f32 v165, v166, v167\n"
    "v_mfma_f32_16x16x32_bf16 v[168:171], a[184:187], a[120:123], v[168:171]\n"
    "s_add_u32 s86, s86, s48\n"
    "v_exp_f32_e32 v174, v174\n"
    "v_mfma_f32_16x16x32_bf16 v[176:179], a[184:187], a[152:155], v[176:179]\n"
    "s_add_u32 s86, s86, s47\n"
    "v_exp_f32_e32 v175, v175\n"
    "v_mfma_f32_16x16x32_bf16 v[176:179], a[188:191], a[156:159], v[176:179]\n"
    "s_waitcnt lgkmcnt(5)\n"
    "ds_read2st64_b32 a[204:205], v249 offset0:4 offset1:5\n"
    "v_cvt_pk_bf16_f32 v172, v172, v173\n"
    "v_mfma_f32_16x16x32_bf16 v[180:183], a[160:163], a[128:131], v[236:239]\n"
    "s_lshl_b32 s46, s93, 7\n"
    "ds_read_b128 a[180:183], v243 offset:8192\n"
    "ds_read2st64_b32 a[206:207], v249 offset0:6 offset1:7\n"
    "v_mfma_f32_16x16x32_bf16 v[180:183], a[164:167], a[132:135], v[180:183]\n"
    "s_lshl_b32 s55, s93, 15\n"
    "ds_write_addtid_b32 v165 offset:4352\n"
    "v_exp_f32_e32 v160, v160\n"
    "v_mfma_f32_16x16x32_bf16 v[148:151], a[164:167], a[100:103], v[232:235]\n"
    "s_sub_u32 s54, s55, 24576\n"
    "ds_read_b128 a[184:187], v242 offset:12288\n"
    "v_exp_f32_e32 v161, v161\n"
    "v_mfma_f32_16x16x32_bf16 v[148:151], a[160:163], a[96:99], v[148:151]\n"
    "s_add_u32 s55, s55, 32768\n"
    "ds_read2st64_b32 a[200:201], v252 offset0:4 offset1:5\n"
    "v_cvt_pk_bf16_f32 v173, v174, v175\n"
    "v_mfma_f32_16x16x32_bf16 v[140:143], a[160:163], a[64:67], v[228:231]\n"
    "s_lshl2_add_u32 s38, s34, s93\n"
    "ds_read_b128 a[188:191], v243 offset:12288\n"
    "v_exp_f32_e32 v162, v162\n"
    "v_mfma_f32_16x16x32_bf16 v[140:143], a[164:167], a[68:71], v[140:143]\n"
    "s_waitcnt lgkmcnt(11)\n"
    "ds_read2st64_b32 a[202:203], v252 offset0:6 offset1:7\n"
    "v_exp_f32_e32 v163, v163\n"
    "v_mfma_f32_16x16x32_bf16 v[140:143], a[168:171], a[72:75], v[140:143]\n"
    "s_lshl1_add_u32 s38, s38, s49\n"
    ".long 0xbedf00ff\n"
    ".Lat_w10:\n"
    "s_setpc_b64 s[2:3]\n"
    "ds_write_addtid_b32 v172 offset:5120\n"
    "v_cvt_pk_bf16_f32 v158, v160, v161\n"
    "v_mfma_f32_16x16x32_bf16 v[140:143], a[172:175], a[76:79], v[140:143]\n"
    "s_lshl_b32 s38, s38, 7\n"
    "v_exp_f32_e32 v168, v168\n"
    "v_mfma_f32_16x16x32_bf16 v[148:151], a[172:175], a[108:111], v[148:151]\n"
    "s_add_u32 s38, s38, 5648384\n"
    "v_exp_f32_e32 v169, v169\n"
    "v_mfma_f32_16x16x32_bf16 v[148:151], a[168:171], a[104:107], v[148:151]\n"
    "s_lshl3_add_u32 s90, s34, s49\n"
    "ds_read2st64_b32 a[212:213], v249 offset0:8 offset1:9\n"
    "v_cvt_pk_bf16_f32 v159, v162, v163\n"
    "v_mfma_f32_16x16x32_bf16 v[180:183], a[168:171], a[136:139], v[180:183]\n"
    "s_lshl2_add_u32 s90, s90, s93\n"
    "v_exp_f32_e32 v170, v170\n"
    "v_mfma_f32_16x16x32_bf16 v[180:183], a[172:175], a[140:143], v[180:183]\n"
    "s_waitcnt lgkmcnt(8)\n"
    "ds_read_b128 a[160:163], v240 offset:10240\n"
    "v_exp_f32_e32 v171, v171\n"
    "v_mfma_f32_16x16x32_bf16 v[180:183], a[176:179], a[144:147], v[180:183]\n"
    "s_lshl2_add_u32 s90, s90, 6041600\n"
    "ds_write_addtid_b32 v173 offset:5376\n"
    "v_cvt_pk_bf16_f32 v166, v168, v169\n"
    "v_mfma_f32_16x16x32_bf16 v[180:183], a[180:183], a[148:151], v[180:183]\n"
    "s_lshl_b32 s45, s51, 15\n"
    "ds_read_b128 a[168:171], v240 offset:14336\n"
    "v_exp_f32_e32 v176, v176\n"
    "v_mfma_f32_16x16x32_bf16 v[148:151], a[180:183], a[116:119], v[148:151]\n"
    "s_sub_u32 s45, 24576, s45\n"
    "ds_read_b128 a[172:175], v241 offset:14336\n"
    "ds_read2st64_b32 a[214:215], v249 offset0:10 offset1:11\n"
    "v_mfma_f32_16x16x32_bf16 v[148:151], a[176:179], a[112:115], v[148:151]\n"
    "s_add_u32 s47, s46, 6103040\n"
    "ds_read_b128 a[164:167], v241 offset:10240\n"
    "ds_write_addtid_b32 v158 offset:3584\n"
    "v_mfma_f32_16x16x32_bf16 v[140:143], a[176:179], a[80:83], v[140:143]\n"
    "s_andn2_b32 s48, s38, 0xc00\n"
    "v_exp_f32_e32 v177, v177\n"
    "v_mfma_f32_16x16x32_bf16 v[140:143], a[180:183], a[84:87], v[140:143]\n"
    "s_waitcnt lgkmcnt(10)\n"
    "ds_read2st64_b32 a[208:209], v252 offset0:8 offset1:9\n"
    "v_cvt_pk_bf16_f32 v167, v170, v171\n"
    "v_mfma_f32_16x16x32_bf16 v[140:143], a[184:187], a[88:91], v[140:143]\n"
    "s_andn2_b32 s61, s90, 0x18c\n"
    "ds_read_b128 a[176:179], v242 offset:10240\n"
    "v_exp_f32_e32 v178, v178\n"
    "v_mfma_f32_16x16x32_bf16 v[140:143], a[188:191], a[92:95], v[140:143]\n"
    "buffer_load_dword v188, v189, s[8:11], s81 offen offset:0 sc1\n"
    "v_mfma_f32_16x16x32_bf16 v[148:151], a[188:191], a[124:127], v[148:151]\n"
    "s_add_u32 s31, s46, s35\n"
    "v_exp_f32_e32 v179, v179\n"
    "v_mfma_f32_16x16x32_bf16 v[148:151], a[184:187], a[120:123], v[148:151]\n"
    "s_add_u32 m0, s63, 6144\n"
    "ds_read_b128 a[180:183], v243 offset:10240\n"
    "v_cvt_pk_bf16_f32 v174, v176, v177\n"
    "v_mfma_f32_16x16x32_bf16 v[180:183], a[184:187], a[152:155], v[180:183]\n"
    "buffer_load_dwordx4 v251, s[12:15], s31 offen offset:0 lds\n"
    "ds_read2st64_b32 a[210:211], v252 offset0:10 offset1:11\n"
    "v_mfma_f32_16x16x32_bf16 v[180:183], a[188:191], a[156:159], v[180:183]\n"
    "s_waitcnt lgkmcnt(5)\n"
    "v_exp_f32_e32 v140, v140\n"
    "v_mfma_f32_16x16x32_bf16 v[184:187], a[160:163], a[128:131], v[236:239]\n"
    "s_mov_b32 m0, s63\n"
    "v_exp_f32_e32 v141, v141\n"
    "v_mfma_f32_16x16x32_bf16 v[184:187], a[164:167], a[132:135], v[184:187]\n"
    "v_exp_f32_e32 v142, v142\n"
    "v_mfma_f32_16x16x32_bf16 v[152:155], a[164:167], a[100:103], v[232:235]\n"
    "s_add_u32 s31, s31, 0x1000\n"
    "ds_write_addtid_b32 v159 offset:3840\n"
    "v_exp_f32_e32 v143, v143\n"
    "v_mfma_f32_16x16x32_bf16 v[152:155], a[160:163], a[96:99], v[152:155]\n"
    "s_add_u32 m0, s63, 7168\n"
    "ds_read_b128 a[184:187], v242 offset:14336\n"
    "v_cvt_pk_bf16_f32 v140, v140, v141\n"
    "v_mfma_f32_16x16x32_bf16 v[144:147], a[160:163], a[64:67], v[228:231]\n"
    "buffer_load_dwordx4 v251, s[12:15], s31 offen offset:0 lds\n"
    "v_mfma_f32_16x16x32_bf16 v[144:147], a[164:167], a[68:71], v[144:147]\n"
    "s_waitcnt lgkmcnt(2)\n"
    "s_waitcnt vmcnt(3)\n"
    "v_mfma_f32_16x16x32_bf16 v[144:147], a[168:171], a[72:75], v[144:147]\n"
    "s_barrier\n"
    "v_mfma_f32_16x16x32_bf16 v[144:147], a[172:175], a[76:79], v[144:147]\n"
    "s_mov_b32 m0, s63\n"
    "ds_read_b128 a[160:163], v240 offset:16384\n"
    "v_exp_f32_e32 v148, v148\n"
    "v_mfma_f32_16x16x32_bf16 v[152:155], a[172:175], a[108:111], v[152:155]\n"
    "s_cmp_eq_u32 s93, 1\n"
    "s_cselect_b32 s31, s36, 2.0\n"
    "ds_read_b128 a[188:191], v243 offset:14336\n"
    "v_mfma_f32_16x16x32_bf16 v[152:155], a[168:171], a[104:107], v[152:155]\n"
    "s_cmp_eq_u32 s93, 2\n"
    "s_cselect_b32 s31, s37, s31\n"
    "ds_write_addtid_b32 v166 offset:4608\n"
    "v_mfma_f32_16x16x32_bf16 v[184:187], a[168:171], a[136:139], v[184:187]\n"
    "buffer_load_dword v191, v190, s[12:15], s31 offen offset:0\n"
    "v_mfma_f32_16x16x32_bf16 v[184:187], a[172:175], a[140:143], v[184:187]\n"
    "ds_read_b128 a[168:171], v240 offset:20480\n"
    "v_exp_f32_e32 v149, v149\n"
    "v_mfma_f32_16x16x32_bf16 v[184:187], a[176:179], a[144:147], v[184:187]\n"
    "s_cmp_eq_u32 s93, 0\n"
    "s_cselect_b32 s31, s35, s31\n"
    "ds_write_addtid_b32 v140 offset:0\n"
    "v_mfma_f32_16x16x32_bf16 v[184:187], a[180:183], a[148:151], v[184:187]\n"
    "s_add_u32 s31, s31, 6103040\n"
    "ds_read_b128 a[164:167], v241 offset:16384\n"
    "v_cvt_pk_bf16_f32 v141, v142, v143\n"
    "v_mfma_f32_16x16x32_bf16 v[152:155], a[180:183], a[116:119], v[152:155]\n"
    "v_exp_f32_e32 v150, v150\n"
    "v_mfma_f32_16x16x32_bf16 v[152:155], a[176:179], a[112:115], v[152:155]\n"
    "ds_read_b128 a[172:175], v241 offset:20480\n"
    "v_exp_f32_e32 v151, v151\n"
    "v_mfma_f32_16x16x32_bf16 v[144:147], a[176:179], a[80:83], v[144:147]\n"
    "ds_write_addtid_b32 v167 offset:4864\n"
    "ds_write_addtid_b32 v141 offset:256\n"
    "v_mfma_f32_16x16x32_bf16 v[144:147], a[180:183], a[84:87], v[144:147]\n"
    "s_waitcnt lgkmcnt(7)\n"
    "ds_write_addtid_b32 v174 offset:5632\n"
    "v_cvt_pk_bf16_f32 v148, v148, v149\n"
    "v_mfma_f32_16x16x32_bf16 v[144:147], a[184:187], a[88:91], v[144:147]\n"
    "v_exp_f32_e32 v180, v180\n"
    "v_mfma_f32_16x16x32_bf16 v[144:147], a[188:191], a[92:95], v[144:147]\n"
    "ds_read_b128 a[176:179], v242 offset:16384\n"
    "v_exp_f32_e32 v181, v181\n"
    "v_mfma_f32_16x16x32_bf16 v[152:155], a[188:191], a[124:127], v[152:155]\n"
    "ds_read_b128 a[180:183], v243 offset:16384\n"
    "v_cvt_pk_bf16_f32 v149, v150, v151\n"
    "v_mfma_f32_16x16x32_bf16 v[152:155], a[184:187], a[120:123], v[152:155]\n"
    "ds_write_addtid_b32 v148 offset:1024\n"
    "v_cvt_pk_bf16_f32 v175, v178, v179\n"
    "v_mfma_f32_16x16x32_bf16 v[184:187], a[184:187], a[152:155], v[184:187]\n"
    "v_exp_f32_e32 v182, v182\n"
    "v_mfma_f32_16x16x32_bf16 v[184:187], a[188:191], a[156:159], v[184:187]\n"
    "s_waitcnt lgkmcnt(7)\n"
    "ds_write_addtid_b32 v175 offset:5888\n"
    "v_exp_f32_e32 v183, v183\n"
    "v_mfma_f32_16x16x32_bf16 v[212:215], a[160:163], a[128:131], v[236:239]\n"
    "ds_write_addtid_b32 v149 offset:1280\n"
    "v_cvt_pk_bf16_f32 v180, v180, v181\n"
    "v_mfma_f32_16x16x32_bf16 v[212:215], a[164:167], a[132:135], v[212:215]\n"
    "v_exp_f32_e32 v144, v144\n"
    "v_mfma_f32_16x16x32_bf16 v[204:207], a[164:167], a[100:103], v[232:235]\n"
    "v_exp_f32_e32 v145, v145\n"
    "v_mfma_f32_16x16x32_bf16 v[204:207], a[160:163], a[96:99], v[204:207]\n"
    "ds_read_b128 a[184:187], v242 offset:20480\n"
    "v_cvt_pk_bf16_f32 v181, v182, v183\n"
    "v_mfma_f32_16x16x32_bf16 v[196:199], a[160:163], a[64:67], v[228:231]\n"
    "ds_write_addtid_b32 v180 offset:2048\n"
    "v_exp_f32_e32 v146, v146\n"
    "v_mfma_f32_16x16x32_bf16 v[196:199], a[164:167], a[68:71], v[196:199]\n"
    "s_waitcnt lgkmcnt(10)\n"
    "v_exp_f32_e32 v147, v147\n"
    "v_mfma_f32_16x16x32_bf16 v[196:199], a[168:171], a[72:75], v[196:199]\n"
    "ds_read_b128 a[188:191], v243 offset:20480\n"
    "v_cvt_pk_bf16_f32 v142, v144, v145\n"
    "v_mfma_f32_16x16x32_bf16 v[196:199], a[172:175], a[76:79], v[196:199]\n"
    "s_waitcnt lgkmcnt(4)\n"
    "v_mfma_f32_16x16x32_bf16 v[204:207], a[172:175], a[108:111], v[204:207]\n"
    "s_barrier\n"
    "v_mfma_f32_16x16x32_bf16 v[204:207], a[168:171], a[104:107], v[204:207]\n"
    "v_exp_f32_e32 v152, v152\n"
    "v_mfma_f32_16x16x32_bf16 v[212:215], a[168:171], a[136:139], v[212:215]\n"
    "ds_write_addtid_b32 v181 offset:2304\n"
    "v_exp_f32_e32 v153, v153\n"
    "v_mfma_f32_16x16x32_bf16 v[212:215], a[172:175], a[140:143], v[212:215]\n"
    "ds_read_b128 a[160:163], v240 offset:18432\n"
    "ds_read_b128 a[168:171], v240 offset:22528\n"
    "v_mfma_f32_16x16x32_bf16 v[212:215], a[176:179], a[144:147], v[212:215]\n"
    "ds_read2st64_b32 v[160:161], v249 offset0:12 offset1:13\n"
    "v_cvt_pk_bf16_f32 v143, v146, v147\n"
    "v_mfma_f32_16x16x32_bf16 v[212:215], a[180:183], a[148:151], v[212:215]\n"
    "v_exp_f32_e32 v154, v154\n"
    "v_mfma_f32_16x16x32_bf16 v[204:207], a[180:183], a[116:119], v[204:207]\n"
    "ds_read_b128 a[172:175], v241 offset:22528\n"
    "v_exp_f32_e32 v155, v155\n"
    "v_mfma_f32_16x16x32_bf16 v[204:207], a[176:179], a[112:115], v[204:207]\n"
    "ds_read_b128 a[164:167], v241 offset:18432\n"
    "v_cvt_pk_bf16_f32 v150, v152, v153\n"
    "v_mfma_f32_16x16x32_bf16 v[196:199], a[176:179], a[80:83], v[196:199]\n"
    "v_exp_f32_e32 v184, v184\n"
    "v_mfma_f32_16x16x32_bf16 v[196:199], a[180:183], a[84:87], v[196:199]\n"
    "s_waitcnt lgkmcnt(6)\n"
    "ds_read2st64_b32 v[162:163], v249 offset0:14 offset1:15\n"
    "v_exp_f32_e32 v185, v185\n"
    "v_mfma_f32_16x16x32_bf16 v[196:199], a[184:187], a[88:91], v[196:199]\n"
    "ds_read_b128 a[176:179], v242 offset:18432\n"
    "ds_write_addtid_b32 v142 offset:512\n"
    "v_mfma_f32_16x16x32_bf16 v[196:199], a[188:191], a[92:95], v[196:199]\n"
    "ds_read_b128 a[180:183], v243 offset:18432\n"
    "v_cvt_pk_bf16_f32 v151, v154, v155\n"
    "v_mfma_f32_16x16x32_bf16 v[204:207], a[188:191], a[124:127], v[204:207]\n"
    "v_exp_f32_e32 v186, v186\n"
    "v_mfma_f32_16x16x32_bf16 v[204:207], a[184:187], a[120:123], v[204:207]\n"
    "s_waitcnt vmcnt(3)\n"
    "ds_read2st64_b32 v[168:169], v249 offset0:16 offset1:17\n"
    "v_cmp_ne_u32_e32 vcc, s39, v188\n"
    "v_mfma_f32_16x16x32_bf16 v[212:215], a[184:187], a[152:155], v[212:215]\n"
    "ds_write_addtid_b32 v143 offset:768\n"
    "v_exp_f32_e32 v187, v187\n"
    "v_mfma_f32_16x16x32_bf16 v[212:215], a[188:191], a[156:159], v[212:215]\n"
    "s_waitcnt lgkmcnt(6)\n"
    "ds_write_addtid_b32 v150 offset:1536\n"
    "v_exp_f32_e32 v196, v196\n"
    "v_mfma_f32_16x16x32_bf16 v[216:219], a[160:163], a[128:131], v[236:239]\n"
    "s_cmp_eq_u64 vcc, 0\n"
    "s_cselect_b32 s31, s31, 2.0\n"
    "s_cselect_b32 s44, 0, 1\n"
    "ds_write_addtid_b32 v151 offset:1792\n"
    "v_mfma_f32_16x16x32_bf16 v[216:219], a[164:167], a[132:135], v[216:219]\n"
    "buffer_load_dword v191, v190, s[8:11], s31 offen offset:0 sc1\n"
    "ds_read2st64_b32 v[170:171], v249 offset0:18 offset1:19\n"
    "v_mfma_f32_16x16x32_bf16 v[208:211], a[164:167], a[100:103], v[232:235]\n"
    "ds_read2st64_b32 v[176:177], v249 offset0:20 offset1:21\n"
    "v_exp_f32_e32 v197, v197\n"
    "v_mfma_f32_16x16x32_bf16 v[208:211], a[160:163], a[96:99], v[208:211]\n"
    "ds_read_b128 a[184:187], v242 offset:22528\n"
    "v_cvt_pk_bf16_f32 v182, v184, v185\n"
    "v_mfma_f32_16x16x32_bf16 v[200:203], a[160:163], a[64:67], v[228:231]\n"
    "ds_write_addtid_b32 v182 offset:2560\n"
    "v_exp_f32_e32 v198, v198\n"
    "v_mfma_f32_16x16x32_bf16 v[200:203], a[164:167], a[68:71], v[200:203]\n"
    "ds_read2st64_b32 v[178:179], v249 offset0:22 offset1:23\n"
    "v_exp_f32_e32 v199, v199\n"
    "v_mfma_f32_16x16x32_bf16 v[200:203], a[168:171], a[72:75], v[200:203]\n"
    "v_cvt_pk_bf16_f32 v196, v196, v197\n"
    "v_exp_f32_e32 v204, v204\n"
    "v_mfma_f32_16x16x32_bf16 v[200:203], a[172:175], a[76:79], v[200:203]\n"
    "ds_read_b128 a[188:191], v243 offset:22528\n"
    "v_exp_f32_e32 v205, v205\n"
    "v_mfma_f32_16x16x32_bf16 v[208:211], a[172:175], a[108:111], v[208:211]\n"
    "v_and_b32_e32 v248, 0xf0, v250\n"
    "v_lshrrev_b32_e32 v231, 3, v248\n"
    "v_cvt_pk_bf16_f32 v197, v198, v199\n"
    "v_mfma_f32_16x16x32_bf16 v[208:211], a[168:171], a[104:107], v[208:211]\n"
    "ds_read_b64_tr_b16 a[160:161], v244 offset:24576\n"
    "v_exp_f32_e32 v206, v206\n"
    "v_mfma_f32_16x16x32_bf16 v[216:219], a[168:171], a[136:139], v[216:219]\n"
    "s_lshr_b32 s31, s35, 8\n"
    "ds_read_b64_tr_b16 a[162:163], v244 offset:26624\n"
    "v_exp_f32_e32 v207, v207\n"
    "v_mfma_f32_16x16x32_bf16 v[216:219], a[172:175], a[140:143], v[216:219]\n"
    "s_waitcnt lgkmcnt(12)\n"
    "ds_read_b64_tr_b16 a[164:165], v244 offset:57344\n"
    "v_cvt_pk_bf16_f32 v204, v204, v205\n"
    "v_mfma_f32_16x16x32_bf16 v[216:219], a[176:179], a[144:147], v[216:219]\n"
    "buffer_load_ushort v228, v231, s[24:27], s31 offen\n"
    "ds_read_b64_tr_b16 a[168:169], v245 offset:24576\n"
    "v_mfma_f32_16x16x32_bf16 v[216:219], a[180:183], a[148:151], v[216:219]\n"
    "s_lshr_b32 s31, s36, 8\n"
    "ds_read_b64_tr_b16 a[170:171], v245 offset:26624\n"
    "v_cvt_pk_bf16_f32 v183, v186, v187\n"
    "v_mfma_f32_16x16x32_bf16 v[208:211], a[180:183], a[116:119], v[208:211]\n"
    "ds_read_b64_tr_b16 a[166:167], v244 offset:59392\n"
    "v_exp_f32_e32 v212, v212\n"
    "v_mfma_f32_16x16x32_bf16 v[208:211], a[176:179], a[112:115], v[208:211]\n"
    "ds_read_b64_tr_b16 a[172:173], v245 offset:57344\n"
    "ds_write_addtid_b32 v183 offset:2816\n"
    "v_mfma_f32_16x16x32_bf16 v[200:203], a[176:179], a[80:83], v[200:203]\n"
    "buffer_load_ushort v229, v231, s[24:27], s31 offen\n"
    "ds_read_b64_tr_b16 a[174:175], v245 offset:59392\n"
    "v_mfma_f32_16x16x32_bf16 v[200:203], a[180:183], a[84:87], v[200:203]\n"
    "s_waitcnt lgkmcnt(9)\n"
    "ds_read_b64_tr_b16 a[176:177], v246 offset:24576\n"
    "v_exp_f32_e32 v213, v213\n"
    "v_mfma_f32_16x16x32_bf16 v[200:203], a[184:187], a[88:91], v[200:203]\n"
    "s_lshr_b32 s31, s37, 8\n"
    "ds_read_b64_tr_b16 a[178:179], v246 offset:26624\n"
    "v_cvt_pk_bf16_f32 v205, v206, v207\n"
    "v_mfma_f32_16x16x32_bf16 v[200:203], a[188:191], a[92:95], v[200:203]\n"
    "ds_read_b64_tr_b16 a[180:181], v246 offset:57344\n"
    "v_exp_f32_e32 v214, v214\n"
    "v_mfma_f32_16x16x32_bf16 v[208:211], a[188:191], a[124:127], v[208:211]\n"
    "buffer_load_ushort v230, v231, s[24:27], s31 offen\n"
    "ds_read_b64_tr_b16 a[182:183], v246 offset:59392\n"
    "v_mfma_f32_16x16x32_bf16 v[208:211], a[184:187], a[120:123], v[208:211]\n"
    "s_waitcnt lgkmcnt(5)\n"
    "v_mfma_f32_16x16x32_bf16 v[216:219], a[184:187], a[152:155], v[216:219]\n"
    "s_barrier\n"
    "v_mfma_f32_16x16x32_bf16 v[216:219], a[188:191], a[156:159], v[216:219]\n"
    "ds_read2st64_b32 v[144:145], v249 offset0:0 offset1:1\n"
    "v_cvt_pk_bf16_f32 v212, v212, v213\n"
    "v_exp_f32_e32 v215, v215\n"
    "v_mfma_f32_16x16x32_bf16 a[252:255], a[160:163], a[208:211], a[252:255]\n"
    "s_add_u32 s62, s57, 4096\n"
    "ds_write_addtid_b32 v196 offset:3072\n"
    "v_exp_f32_e32 v200, v200\n"
    "v_mfma_f32_16x16x32_bf16 a[252:255], a[164:167], a[212:215], a[252:255]\n"
    "buffer_load_dwordx4 a[64:67], v250, s[8:11], s62 offen offset:2048 sc1\n"
    "ds_read_b64_tr_b16 a[184:185], v246 offset:0\n"
    "v_mfma_f32_16x16x32_bf16 a[248:251], a[164:167], a[204:207], a[248:251]\n"
    "ds_read2st64_b32 v[146:147], v249 offset0:2 offset1:3\n"
    "v_exp_f32_e32 v201, v201\n"
    "v_mfma_f32_16x16x32_bf16 a[248:251], a[160:163], a[200:203], a[248:251]\n"
    "ds_read_b64_tr_b16 a[186:187], v246 offset:2048\n"
    "v_cvt_pk_bf16_f32 v213, v214, v215\n"
    "v_mfma_f32_16x16x32_bf16 a[244:247], a[160:163], a[192:195], a[244:247]\n"
    "ds_read2st64_b32 v[152:153], v249 offset0:4 offset1:5\n"
    "v_exp_f32_e32 v202, v202\n"
    "v_mfma_f32_16x16x32_bf16 a[244:247], a[164:167], a[196:199], a[244:247]\n"
    "s_waitcnt lgkmcnt(10)\n"
    "ds_read_b64_tr_b16 a[160:161], v244 offset:0\n"
    "ds_write_addtid_b32 v197 offset:3328\n"
    "v_mfma_f32_16x16x32_bf16 v[8:11], a[168:171], a[192:195], v[8:11]\n"
    "buffer_load_dwordx4 a[68:71], v250, s[8:11], s62 offen offset:3072 sc1\n"
    "ds_read2st64_b32 v[154:155], v249 offset0:6 offset1:7\n"
    "v_mfma_f32_16x16x32_bf16 v[8:11], a[172:175], a[196:199], v[8:11]\n"
    "s_add_u32 s62, s57, 8192\n"
    "ds_read2st64_b32 v[184:185], v249 offset0:8 offset1:9\n"
    "ds_write_addtid_b32 v204 offset:4096\n"
    "v_mfma_f32_16x16x32_bf16 v[12:15], a[172:175], a[204:207], v[12:15]\n"
    "ds_read_b64_tr_b16 a[188:189], v246 offset:32768\n"
    "ds_write_addtid_b32 v205 offset:4352\n"
    "v_mfma_f32_16x16x32_bf16 v[12:15], a[168:171], a[200:203], v[12:15]\n"
    "ds_read_b64_tr_b16 a[190:191], v246 offset:34816\n"
    "v_exp_f32_e32 v203, v203\n"
    "v_mfma_f32_16x16x32_bf16 v[16:19], a[168:171], a[208:211], v[16:19]\n"
    "ds_read_b64_tr_b16 a[162:163], v244 offset:2048\n"
    "v_cvt_pk_bf16_f32 v198, v200, v201\n"
    "v_mfma_f32_16x16x32_bf16 v[16:19], a[172:175], a[212:215], v[16:19]\n"
    "s_waitcnt lgkmcnt(15)\n"
    "ds_read_b64_tr_b16 a[164:165], v244 offset:32768\n"
    "v_exp_f32_e32 v208, v208\n"
    "v_mfma_f32_16x16x32_bf16 v[36:39], a[176:179], a[208:211], v[36:39]\n"
    "buffer_load_dwordx4 a[72:75], v250, s[8:11], s62 offen offset:0 sc1\n"
    "ds_read_b64_tr_b16 a[166:167], v244 offset:34816\n"
    "v_mfma_f32_16x16x32_bf16 v[36:39], a[180:183], a[212:215], v[36:39]\n"
    "ds_read_b64_tr_b16 a[168:169], v245 offset:0\n"
    "v_exp_f32_e32 v209, v209\n"
    "v_mfma_f32_16x16x32_bf16 v[32:35], a[180:183], a[204:207], v[32:35]\n"
    "ds_read_b64_tr_b16 a[170:171], v245 offset:2048\n"
    "v_cvt_pk_bf16_f32 v199, v202, v203\n"
    "v_mfma_f32_16x16x32_bf16 v[32:35], a[176:179], a[200:203], v[32:35]\n"
    "ds_read_b64_tr_b16 a[172:173], v245 offset:32768\n"
    "v_exp_f32_e32 v210, v210\n"
    "v_mfma_f32_16x16x32_bf16 v[28:31], a[176:179], a[192:195], v[28:31]\n"
    "ds_write_addtid_b32 v212 offset:5120\n"
    "v_exp_f32_e32 v211, v211\n"
    "v_mfma_f32_16x16x32_bf16 v[28:31], a[180:183], a[196:199], v[28:31]\n"
    "s_waitcnt lgkmcnt(7)\n"
    "ds_read_b64_tr_b16 a[174:175], v245 offset:34816\n"
    "ds_write_addtid_b32 v213 offset:5376\n"
    "v_mfma_f32_16x16x32_bf16 v[28:31], a[184:187], v[156:159], v[28:31]\n"
    "buffer_load_dwordx4 a[76:79], v250, s[8:11], s62 offen offset:1024 sc1\n"
    "ds_read2st64_b32 v[186:187], v249 offset0:10 offset1:11\n"
    "v_mfma_f32_16x16x32_bf16 v[28:31], a[188:191], v[160:163], v[28:31]\n"
    "ds_read_b64_tr_b16 a[176:177], v245 offset:8192\n"
    "v_cvt_pk_bf16_f32 v206, v208, v209\n"
    "v_mfma_f32_16x16x32_bf16 v[32:35], a[188:191], v[168:171], v[32:35]\n"
    "ds_write_addtid_b32 v198 offset:3584\n"
    "v_exp_f32_e32 v216, v216\n"
    "v_mfma_f32_16x16x32_bf16 v[32:35], a[184:187], v[164:167], v[32:35]\n"
    "ds_write_addtid_b32 v199 offset:3840\n"
    "v_exp_f32_e32 v217, v217\n"
    "v_mfma_f32_16x16x32_bf16 v[36:39], a[184:187], v[172:175], v[36:39]\n"
    "ds_read_b64_tr_b16 a[178:179], v245 offset:10240\n"
    "v_cvt_pk_bf16_f32 v207, v210, v211\n"
    "v_mfma_f32_16x16x32_bf16 v[36:39], a[188:191], v[176:179], v[36:39]\n"
    "s_waitcnt lgkmcnt(11)\n"
    "ds_read_b64_tr_b16 a[180:181], v245 offset:40960\n"
    "ds_write_addtid_b32 v206 offset:4608\n"
    "v_mfma_f32_16x16x32_bf16 a[252:255], a[160:163], v[172:175], a[252:255]\n"
    "buffer_load_dwordx4 a[80:83], v250, s[8:11], s62 offen offset:2048 sc1\n"
    "ds_write_addtid_b32 v207 offset:4864\n"
    "v_mfma_f32_16x16x32_bf16 a[252:255], a[164:167], v[176:179], a[252:255]\n"
    "ds_read_b64_tr_b16 a[184:185], v244 offset:8192\n"
    "v_exp_f32_e32 v218, v218\n"
    "v_mfma_f32_16x16x32_bf16 a[248:251], a[164:167], v[168:171], a[248:251]\n"
    "v_exp_f32_e32 v219, v219\n"
    "v_mfma_f32_16x16x32_bf16 a[248:251], a[160:163], v[164:167], a[248:251]\n"
    "ds_read_b64_tr_b16 a[186:187], v244 offset:10240\n"
    "v_lshrrev_b32_e32 v231, 2, v250\n"
    "v_mfma_f32_16x16x32_bf16 a[244:247], a[160:163], v[156:159], a[244:247]\n"
    "ds_read_b64_tr_b16 a[182:183], v245 offset:43008\n"
    "v_cvt_pk_bf16_f32 v214, v216, v217\n"
    "v_mfma_f32_16x16x32_bf16 a[244:247], a[164:167], v[160:163], a[244:247]\n"
    "s_waitcnt lgkmcnt(12)\n"
    "ds_read_b64_tr_b16 a[160:161], v244 offset:16384\n"
    "v_subrev_u32_e32 v231, 0x80, v231\n"
    "v_mfma_f32_16x16x32_bf16 v[8:11], a[168:171], v[156:159], v[8:11]\n"
    "buffer_load_dwordx4 a[84:87], v250, s[8:11], s62 offen offset:3072 sc1\n"
    "v_mfma_f32_16x16x32_bf16 v[8:11], a[172:175], v[160:163], v[8:11]\n"
    "s_add_u32 s62, s57, 12288\n"
    "ds_write_addtid_b32 v214 offset:5632\n"
    "v_cvt_pk_bf16_f32 v215, v218, v219\n"
    "v_mfma_f32_16x16x32_bf16 v[12:15], a[172:175], v[168:171], v[12:15]\n"
    "ds_read_b64_tr_b16 a[188:189], v244 offset:40960\n"
    "ds_write_addtid_b32 v215 offset:5888\n"
    "v_mfma_f32_16x16x32_bf16 v[12:15], a[168:171], v[164:167], v[12:15]\n"
    "ds_read_b64_tr_b16 a[190:191], v244 offset:43008\n"
    "v_min_u32_e32 v231, 2.0, v231\n"
    "v_mfma_f32_16x16x32_bf16 v[16:19], a[168:171], v[172:175], v[16:19]\n"
    "ds_read_b64_tr_b16 a[162:163], v244 offset:18432\n"
    "ds_read_b64_tr_b16 a[164:165], v244 offset:49152\n"
    "v_mfma_f32_16x16x32_bf16 v[16:19], a[172:175], v[176:179], v[16:19]\n"
    "s_waitcnt lgkmcnt(7)\n"
    "s_waitcnt lgkmcnt(3)\n"
    "v_mfma_f32_16x16x32_bf16 v[16:19], a[176:179], v[180:183], v[16:19]\n"
    "s_barrier\n"
    "v_mfma_f32_16x16x32_bf16 v[16:19], a[180:183], v[184:187], v[16:19]\n"
    "buffer_load_dwordx4 a[88:91], v250, s[8:11], s62 offen offset:0 sc1\n"
    "ds_read2st64_b32 v[200:201], v249 offset0:12 offset1:13\n"
    "v_mfma_f32_16x16x32_bf16 v[12:15], a[180:183], v[152:155], v[12:15]\n"
    "ds_read_b64_tr_b16 a[166:167], v244 offset:51200\n"
    "ds_read2st64_b32 v[202:203], v249 offset0:14 offset1:15\n"
    "v_mfma_f32_16x16x32_bf16 v[12:15], a[176:179], v[148:151], v[12:15]\n"
    "ds_read2st64_b32 v[208:209], v249 offset0:16 offset1:17\n"
    "ds_read2st64_b32 v[210:211], v249 offset0:18 offset1:19\n"
    "v_mfma_f32_16x16x32_bf16 v[8:11], a[176:179], v[140:143], v[8:11]\n"
    "ds_read2st64_b32 v[216:217], v249 offset0:20 offset1:21\n"
    "v_lshrrev_b32_e32 v252, 4, v250\n"
    "v_mfma_f32_16x16x32_bf16 v[8:11], a[180:183], v[144:147], v[8:11]\n"
    "s_waitcnt lgkmcnt(8)\n"
    "ds_read_b64_tr_b16 a[168:169], v245 offset:16384\n"
    "ds_read2st64_b32 v[218:219], v249 offset0:22 offset1:23\n"
    "v_mfma_f32_16x16x32_bf16 a[244:247], a[184:187], v[140:143], a[244:247]\n"
    "buffer_load_dwordx4 a[92:95], v250, s[8:11], s62 offen offset:1024 sc1\n"
    "v_mfma_f32_16x16x32_bf16 a[244:247], a[188:191], v[144:147], a[244:247]\n"
    "ds_read_b64_tr_b16 a[176:177], v246 offset:8192\n"
    "v_lshlrev_b32_e32 v239, 5, v248\n"
    "v_mfma_f32_16x16x32_bf16 a[248:251], a[188:191], v[152:155], a[248:251]\n"
    "ds_read_b64_tr_b16 a[170:171], v245 offset:18432\n"
    "v_and_or_b32 v239, v252, 48, v239\n"
    "v_mfma_f32_16x16x32_bf16 a[248:251], a[184:187], v[148:151], a[248:251]\n"
    "ds_read_b64_tr_b16 a[172:173], v245 offset:49152\n"
    "v_lshrrev_b32_e32 v248, 2, v248\n"
    "v_mfma_f32_16x16x32_bf16 a[252:255], a[184:187], v[180:183], a[252:255]\n"
    "ds_read_b64_tr_b16 a[174:175], v245 offset:51200\n"
    "v_lshrrev_b32_e32 v253, 4, v252\n"
    "v_mfma_f32_16x16x32_bf16 a[252:255], a[188:191], v[184:187], a[252:255]\n"
    "s_waitcnt lgkmcnt(4)\n"
    "ds_read_b64_tr_b16 a[178:179], v246 offset:10240\n"
    "v_lshl_add_u32 v248, v253, 7, v248\n"
    "v_mfma_f32_16x16x32_bf16 a[252:255], a[160:163], v[212:215], a[252:255]\n"
    "buffer_load_dwordx4 a[96:99], v250, s[8:11], s62 offen offset:2048 sc1\n"
    "ds_read_b64_tr_b16 a[184:185], v246 offset:16384\n"
    "v_mfma_f32_16x16x32_bf16 a[252:255], a[164:167], v[216:219], a[252:255]\n"
    "ds_read_b64_tr_b16 a[180:181], v246 offset:40960\n"
    "v_bfe_u32 v255, v252, 2, 2\n"
    "v_mfma_f32_16x16x32_bf16 a[248:251], a[164:167], v[208:211], a[248:251]\n"
    "ds_read_b64_tr_b16 a[186:187], v246 offset:18432\n"
    "v_max_u32_e32 v238, 1, v255\n"
    "v_mfma_f32_16x16x32_bf16 a[248:251], a[160:163], v[204:207], a[248:251]\n"
    "v_add_u32_e32 v238, s32, v238\n"
    "v_and_b32_e32 v238, 3, v238\n"
    "v_mfma_f32_16x16x32_bf16 a[244:247], a[160:163], v[196:199], a[244:247]\n"
    "ds_read_b64_tr_b16 a[182:183], v246 offset:43008\n"
    "v_lshlrev_b32_e32 v238, 10, v238\n"
    "v_mfma_f32_16x16x32_bf16 a[244:247], a[164:167], v[200:203], a[244:247]\n"
    "s_waitcnt lgkmcnt(5)\n"
    "ds_read_b64_tr_b16 a[160:161], v247 offset:24576\n"
    "v_and_b32_e32 v252, 3, v252\n"
    "v_mfma_f32_16x16x32_bf16 v[8:11], a[168:171], v[196:199], v[8:11]\n"
    "buffer_load_dwordx4 a[100:103], v250, s[8:11], s62 offen offset:3072 sc1\n"
    "v_mfma_f32_16x16x32_bf16 v[8:11], a[172:175], v[200:203], v[8:11]\n"
    "s_add_u32 s62, s57, 16384\n"
    "ds_read_b64_tr_b16 a[188:189], v246 offset:49152\n"
    "v_xor_b32_e32 v253, v253, v255\n"
    "v_mfma_f32_16x16x32_bf16 v[12:15], a[172:175], v[208:211], v[12:15]\n"
    "ds_read_b64_tr_b16 a[190:191], v246 offset:51200\n"
    "v_mfma_f32_16x16x32_bf16 v[12:15], a[168:171], v[204:207], v[12:15]\n"
    "v_cvt_pk_bf16_f32 v11, v10, v11\n"
    "v_cvt_pk_bf16_f32 v10, v8, v9\n"
    "v_mfma_f32_16x16x32_bf16 v[16:19], a[168:171], v[212:215], v[16:19]\n"
    "ds_read_b64_tr_b16 a[162:163], v247 offset:26624\n"
    "v_accvgpr_read_b32 v8, a244\n"
    "v_mfma_f32_16x16x32_bf16 v[16:19], a[172:175], v[216:219], v[16:19]\n"
    "s_waitcnt lgkmcnt(4)\n"
    "v_accvgpr_read_b32 v9, a245\n"
    "v_accvgpr_read_b32 v255, a247\n"
    "v_mfma_f32_16x16x32_bf16 v[36:39], a[176:179], v[180:183], v[36:39]\n"
    "v_cvt_pk_bf16_f32 v8, v8, v9\n"
    "v_accvgpr_read_b32 v9, a246\n"
    "v_mfma_f32_16x16x32_bf16 v[36:39], a[180:183], v[184:187], v[36:39]\n"
    "ds_read_b64_tr_b16 a[164:165], v247 offset:57344\n"
    "v_cvt_pk_bf16_f32 v9, v9, v255\n"
    "v_mfma_f32_16x16x32_bf16 v[32:35], a[180:183], v[152:155], v[32:35]\n"
    "buffer_store_dwordx4 v[8:11], v250, s[8:11], s82 offen offset:0 sc1\n"
    "v_mfma_f32_16x16x32_bf16 v[32:35], a[176:179], v[148:151], v[32:35]\n"
    "ds_read_b64_tr_b16 a[168:169], v247 offset:0\n"
    "v_mfma_f32_16x16x32_bf16 v[28:31], a[176:179], v[140:143], v[28:31]\n"
    "ds_read_b64_tr_b16 a[166:167], v247 offset:59392\n"
    "v_cvt_pk_bf16_f32 v15, v14, v15\n"
    "v_mfma_f32_16x16x32_bf16 v[28:31], a[180:183], v[144:147], v[28:31]\n"
    "s_waitcnt lgkmcnt(4)\n"
    "ds_read_b64_tr_b16 a[170:171], v247 offset:2048\n"
    "v_cvt_pk_bf16_f32 v14, v12, v13\n"
    "v_mfma_f32_16x16x32_bf16 v[28:31], a[184:187], v[196:199], v[28:31]\n"
    "buffer_load_dwordx4 a[104:107], v250, s[8:11], s62 offen offset:0 sc1\n"
    "v_mfma_f32_16x16x32_bf16 v[28:31], a[188:191], v[200:203], v[28:31]\n"
    "ds_read_b64_tr_b16 a[172:173], v247 offset:32768\n"
    "v_lshl_or_b32 v252, v253, 2, v252\n"
    "v_mfma_f32_16x16x32_bf16 v[32:35], a[188:191], v[208:211], v[32:35]\n"
    "v_mov_b32_e32 v253, 0x3f80\n"
    "v_accvgpr_read_b32 v12, a248\n"
    "v_mfma_f32_16x16x32_bf16 v[32:35], a[184:187], v[204:207], v[32:35]\n"
    "ds_read_b64_tr_b16 a[176:177], v247 offset:8192\n"
    "v_accvgpr_read_b32 v13, a249\n"
    "v_mfma_f32_16x16x32_bf16 v[36:39], a[184:187], v[212:215], v[36:39]\n"
    "ds_read_b64_tr_b16 a[174:175], v247 offset:34816\n"
    "v_accvgpr_read_b32 v255, a251\n"
    "v_mfma_f32_16x16x32_bf16 v[36:39], a[188:191], v[216:219], v[36:39]\n"
    "s_waitcnt lgkmcnt(4)\n"
    "ds_read_b64_tr_b16 a[184:185], v247 offset:16384\n"
    "v_cvt_pk_bf16_f32 v12, v12, v13\n"
    "v_mfma_f32_16x16x32_bf16 v[56:59], a[160:163], a[208:211], v[56:59]\n"
    "buffer_load_dwordx4 a[108:111], v250, s[8:11], s62 offen offset:1024 sc1\n"
    "v_mfma_f32_16x16x32_bf16 v[56:59], a[164:167], a[212:215], v[56:59]\n"
    "ds_read_b64_tr_b16 a[178:179], v247 offset:10240\n"
    "v_accvgpr_read_b32 v13, a250\n"
    "v_mfma_f32_16x16x32_bf16 v[52:55], a[164:167], a[204:207], v[52:55]\n"
    "ds_read_b64_tr_b16 a[180:181], v247 offset:40960\n"
    "v_cvt_pk_bf16_f32 v13, v13, v255\n"
    "v_mfma_f32_16x16x32_bf16 v[52:55], a[160:163], a[200:203], v[52:55]\n"
    "buffer_store_dwordx4 v[12:15], v250, s[8:11], s28 offen offset:0 sc1\n"
    "v_mfma_f32_16x16x32_bf16 v[48:51], a[160:163], a[192:195], v[48:51]\n"
    "s_add_u32 s62, s57, 16384\n"
    "ds_read_b64_tr_b16 a[182:183], v247 offset:43008\n"
    "v_cvt_pk_bf16_f32 v28, v28, v29\n"
    "v_mfma_f32_16x16x32_bf16 v[48:51], a[164:167], a[196:199], v[48:51]\n"
    "s_waitcnt lgkmcnt(4)\n"
    "ds_read_b64_tr_b16 a[160:161], v244 offset:28672\n"
    "v_cvt_pk_bf16_f32 v29, v30, v31\n"
    "v_mfma_f32_16x16x32_bf16 v[48:51], a[168:171], v[156:159], v[48:51]\n"
    "buffer_load_dwordx4 a[112:115], v250, s[8:11], s62 offen offset:2048 sc1\n"
    "v_mfma_f32_16x16x32_bf16 v[48:51], a[172:175], v[160:163], v[48:51]\n"
    "ds_read_b64_tr_b16 a[186:187], v247 offset:18432\n"
    "v_cvt_pk_bf16_f32 v32, v32, v33\n"
    "v_mfma_f32_16x16x32_bf16 v[52:55], a[172:175], v[168:171], v[52:55]\n"
    "ds_read_b64_tr_b16 a[188:189], v247 offset:49152\n"
    "v_mfma_f32_16x16x32_bf16 v[52:55], a[168:171], v[164:167], v[52:55]\n"
    "v_cvt_pk_bf16_f32 v19, v18, v19\n"
    "v_cvt_pk_bf16_f32 v18, v16, v17\n"
    "v_mfma_f32_16x16x32_bf16 v[56:59], a[168:171], v[172:175], v[56:59]\n"
    "ds_read_b64_tr_b16 a[190:191], v247 offset:51200\n"
    "v_accvgpr_read_b32 v16, a252\n"
    "v_mfma_f32_16x16x32_bf16 v[56:59], a[172:175], v[176:179], v[56:59]\n"
    "s_waitcnt lgkmcnt(4)\n"
    "v_accvgpr_read_b32 v17, a253\n"
    "v_accvgpr_read_b32 v255, a255\n"
    "v_mfma_f32_16x16x32_bf16 v[56:59], a[176:179], v[180:183], v[56:59]\n"
    "ds_read_b64_tr_b16 a[162:163], v244 offset:30720\n"
    "v_mfma_f32_16x16x32_bf16 v[56:59], a[180:183], v[184:187], v[56:59]\n"
    "ds_read_b64_tr_b16 a[168:169], v244 offset:4096\n"
    "v_cvt_pk_bf16_f32 v16, v16, v17\n"
    "v_mfma_f32_16x16x32_bf16 v[52:55], a[180:183], v[152:155], v[52:55]\n"
    "ds_read_b64_tr_b16 a[164:165], v244 offset:61440\n"
    "v_accvgpr_read_b32 v17, a254\n"
    "v_mfma_f32_16x16x32_bf16 v[52:55], a[176:179], v[148:151], v[52:55]\n"
    "ds_read_b64_tr_b16 a[166:167], v244 offset:63488\n"
    "v_cvt_pk_bf16_f32 v17, v17, v255\n"
    "v_mfma_f32_16x16x32_bf16 v[48:51], a[176:179], v[140:143], v[48:51]\n"
    "buffer_store_dwordx4 v[16:19], v250, s[8:11], s29 offen offset:0 sc1\n"
    "v_mfma_f32_16x16x32_bf16 v[48:51], a[180:183], v[144:147], v[48:51]\n"
    "s_waitcnt lgkmcnt(4)\n"
    "ds_read_b64_tr_b16 a[170:171], v244 offset:6144\n"
    "v_mfma_f32_16x16x32_bf16 v[48:51], a[184:187], v[196:199], v[48:51]\n"
    "ds_read_b64_tr_b16 a[176:177], v244 offset:12288\n"
    "v_mfma_f32_16x16x32_bf16 v[48:51], a[188:191], v[200:203], v[48:51]\n"
    "buffer_load_dwordx4 a[116:119], v250, s[8:11], s62 offen offset:3072 sc1\n"
    "v_mfma_f32_16x16x32_bf16 v[52:55], a[188:191], v[208:211], v[52:55]\n"
    "s_add_u32 s62, s57, 20480\n"
    "ds_read_b64_tr_b16 a[178:179], v244 offset:14336\n"
    "v_mfma_f32_16x16x32_bf16 v[52:55], a[184:187], v[204:207], v[52:55]\n"
    "ds_read_b64_tr_b16 a[172:173], v244 offset:36864\n"
    "v_cvt_pk_bf16_f32 v33, v34, v35\n"
    "v_mfma_f32_16x16x32_bf16 v[56:59], a[184:187], v[212:215], v[56:59]\n"
    "ds_read_b64_tr_b16 a[174:175], v244 offset:38912\n"
    "v_mfma_f32_16x16x32_bf16 v[56:59], a[188:191], v[216:219], v[56:59]\n"
    "s_waitcnt lgkmcnt(5)\n"
    "ds_read_b64_tr_b16 a[184:185], v244 offset:20480\n"
    "v_mfma_f32_16x16x32_bf16 v[76:79], a[160:163], a[208:211], v[76:79]\n"
    ".long 0xbedf00ff\n"
    ".Lat_w11:\n"
    "s_setpc_b64 s[2:3]\n"
    "ds_read_b64_tr_b16 a[180:181], v244 offset:45056\n"
    "v_mfma_f32_16x16x32_bf16 v[76:79], a[164:167], a[212:215], v[76:79]\n"
    "ds_read_b64_tr_b16 a[182:183], v244 offset:47104\n"
    "v_cvt_pk_bf16_f32 v30, v48, v49\n"
    "v_mfma_f32_16x16x32_bf16 v[72:75], a[164:167], a[204:207], v[72:75]\n"
    "v_cvt_pk_bf16_f32 v31, v50, v51\n"
    "v_cvt_pk_bf16_f32 v36, v36, v37\n"
    "v_mfma_f32_16x16x32_bf16 v[72:75], a[160:163], a[200:203], v[72:75]\n"
    "buffer_load_dwordx4 a[120:123], v250, s[8:11], s62 offen offset:0 sc1\n"
    "v_mfma_f32_16x16x32_bf16 v[68:71], a[160:163], a[192:195], v[68:71]\n"
    "ds_read_b64_tr_b16 a[186:187], v244 offset:22528\n"
    "v_mfma_f32_16x16x32_bf16 v[68:71], a[164:167], a[196:199], v[68:71]\n"
    "s_waitcnt lgkmcnt(4)\n"
    "ds_read_b64_tr_b16 a[160:161], v245 offset:28672\n"
    "v_mfma_f32_16x16x32_bf16 v[68:71], a[168:171], v[156:159], v[68:71]\n"
    "buffer_store_dwordx4 v[28:31], v250, s[8:11], s82 offen offset:1024 sc1\n"
    "v_mfma_f32_16x16x32_bf16 v[68:71], a[172:175], v[160:163], v[68:71]\n"
    "ds_read_b64_tr_b16 a[188:189], v244 offset:53248\n"
    "v_mfma_f32_16x16x32_bf16 v[72:75], a[172:175], v[168:171], v[72:75]\n"
    "v_mfma_f32_16x16x32_bf16 v[72:75], a[168:171], v[164:167], v[72:75]\n"
    "ds_read_b64_tr_b16 a[190:191], v244 offset:55296\n"
    "v_mfma_f32_16x16x32_bf16 v[76:79], a[168:171], v[172:175], v[76:79]\n"
    "ds_read_b64_tr_b16 a[162:163], v245 offset:30720\n"
    "v_cvt_pk_bf16_f32 v37, v38, v39\n"
    "v_mfma_f32_16x16x32_bf16 v[76:79], a[172:175], v[176:179], v[76:79]\n"
    "s_waitcnt lgkmcnt(5)\n"
    "ds_read_b64_tr_b16 a[168:169], v245 offset:4096\n"
    "v_mfma_f32_16x16x32_bf16 v[76:79], a[176:179], v[180:183], v[76:79]\n"
    "buffer_load_dwordx4 a[124:127], v250, s[8:11], s62 offen offset:1024 sc1\n"
    "v_mfma_f32_16x16x32_bf16 v[76:79], a[180:183], v[184:187], v[76:79]\n"
    "v_cvt_pk_bf16_f32 v34, v52, v53\n"
    "v_mfma_f32_16x16x32_bf16 v[72:75], a[180:183], v[152:155], v[72:75]\n"
    "ds_read_b64_tr_b16 a[170:171], v245 offset:6144\n"
    "v_mov_b64_e32 v[232:233], 0\n"
    "v_mfma_f32_16x16x32_bf16 v[72:75], a[176:179], v[148:151], v[72:75]\n"
    "ds_read_b64_tr_b16 a[164:165], v245 offset:61440\n"
    "v_mov_b64_e32 v[236:237], 0\n"
    "v_mfma_f32_16x16x32_bf16 v[68:71], a[176:179], v[140:143], v[68:71]\n"
    "ds_read_b64_tr_b16 a[166:167], v245 offset:63488\n"
    "v_mfma_f32_16x16x32_bf16 v[68:71], a[180:183], v[144:147], v[68:71]\n"
    "s_waitcnt lgkmcnt(5)\n"
    "ds_read_b64_tr_b16 a[172:173], v245 offset:36864\n"
    "v_cvt_pk_bf16_f32 v38, v56, v57\n"
    "v_mfma_f32_16x16x32_bf16 v[68:71], a[184:187], v[196:199], v[68:71]\n"
    "v_cvt_pk_bf16_f32 v35, v54, v55\n"
    "v_mfma_f32_16x16x32_bf16 v[68:71], a[188:191], v[200:203], v[68:71]\n"
    "buffer_store_dwordx4 v[32:35], v250, s[8:11], s28 offen offset:1024 sc1\n"
    "v_mfma_f32_16x16x32_bf16 v[72:75], a[188:191], v[208:211], v[72:75]\n"
    "ds_read_b64_tr_b16 a[176:177], v245 offset:12288\n"
    "v_cvt_pk_bf16_f32 v39, v58, v59\n"
    "v_mfma_f32_16x16x32_bf16 v[72:75], a[184:187], v[204:207], v[72:75]\n"
    "ds_read_b64_tr_b16 a[174:175], v245 offset:38912\n"
    "v_mfma_f32_16x16x32_bf16 v[76:79], a[184:187], v[212:215], v[76:79]\n"
    "buffer_load_dwordx4 a[128:131], v250, s[8:11], s62 offen offset:2048 sc1\n"
    "v_mfma_f32_16x16x32_bf16 v[76:79], a[188:191], v[216:219], v[76:79]\n"
    "s_waitcnt lgkmcnt(3)\n"
    "ds_read_b64_tr_b16 a[184:185], v245 offset:20480\n"
    "v_cvt_pk_bf16_f32 v68, v68, v69\n"
    "v_mfma_f32_16x16x32_bf16 v[96:99], a[160:163], a[208:211], v[96:99]\n"
    "ds_read_b64_tr_b16 a[178:179], v245 offset:14336\n"
    "v_mfma_f32_16x16x32_bf16 v[96:99], a[164:167], a[212:215], v[96:99]\n"
    "ds_read_b64_tr_b16 a[180:181], v245 offset:45056\n"
    "v_mfma_f32_16x16x32_bf16 v[92:95], a[164:167], a[204:207], v[92:95]\n"
    "ds_read_b64_tr_b16 a[182:183], v245 offset:47104\n"
    "v_mfma_f32_16x16x32_bf16 v[92:95], a[160:163], a[200:203], v[92:95]\n"
    "v_cmp_eq_u32_e32 vcc, 0, v252\n"
    "v_cvt_pk_bf16_f32 v72, v72, v73\n"
    "v_mfma_f32_16x16x32_bf16 v[88:91], a[160:163], a[192:195], v[88:91]\n"
    "v_cndmask_b32_e32 v234, 0, v253, vcc\n"
    "v_mfma_f32_16x16x32_bf16 v[88:91], a[164:167], a[196:199], v[88:91]\n"
    "s_waitcnt lgkmcnt(4)\n"
    "ds_read_b64_tr_b16 a[186:187], v245 offset:22528\n"
    "v_cvt_pk_bf16_f32 v69, v70, v71\n"
    "v_mfma_f32_16x16x32_bf16 v[88:91], a[168:171], v[156:159], v[88:91]\n"
    "ds_read_b64_tr_b16 a[188:189], v245 offset:53248\n"
    "v_mfma_f32_16x16x32_bf16 v[88:91], a[172:175], v[160:163], v[88:91]\n"
    "ds_read_b64_tr_b16 a[160:161], v246 offset:28672\n"
    "v_mfma_f32_16x16x32_bf16 v[92:95], a[172:175], v[168:171], v[92:95]\n"
    "buffer_store_dwordx4 v[36:39], v250, s[8:11], s29 offen offset:1024 sc1\n"
    "v_mfma_f32_16x16x32_bf16 v[92:95], a[168:171], v[164:167], v[92:95]\n"
    "ds_read_b64_tr_b16 a[190:191], v245 offset:55296\n"
    "v_mfma_f32_16x16x32_bf16 v[96:99], a[168:171], v[172:175], v[96:99]\n"
    "ds_read_b64_tr_b16 a[162:163], v246 offset:30720\n"
    "v_cmp_eq_u32_e32 vcc, 1, v252\n"
    "v_mfma_f32_16x16x32_bf16 v[96:99], a[172:175], v[176:179], v[96:99]\n"
    "s_waitcnt lgkmcnt(5)\n"
    "ds_read_b64_tr_b16 a[164:165], v246 offset:61440\n"
    "v_mfma_f32_16x16x32_bf16 v[96:99], a[176:179], v[180:183], v[96:99]\n"
    "buffer_load_dwordx4 a[132:135], v250, s[8:11], s62 offen offset:3072 sc1\n"
    "v_mfma_f32_16x16x32_bf16 v[96:99], a[180:183], v[184:187], v[96:99]\n"
    "s_add_u32 s62, s57, 24576\n"
    "ds_read_b64_tr_b16 a[168:169], v246 offset:4096\n"
    "v_mfma_f32_16x16x32_bf16 v[92:95], a[180:183], v[152:155], v[92:95]\n"
    "ds_read_b64_tr_b16 a[170:171], v246 offset:6144\n"
    "v_mfma_f32_16x16x32_bf16 v[92:95], a[176:179], v[148:151], v[92:95]\n"
    "ds_read_b64_tr_b16 a[166:167], v246 offset:63488\n"
    "v_mfma_f32_16x16x32_bf16 v[88:91], a[176:179], v[140:143], v[88:91]\n"
    "v_mfma_f32_16x16x32_bf16 v[88:91], a[180:183], v[144:147], v[88:91]\n"
    "s_waitcnt lgkmcnt(5)\n"
    "ds_read_b64_tr_b16 a[176:177], v246 offset:12288\n"
    "v_cvt_pk_bf16_f32 v73, v74, v75\n"
    "v_mfma_f32_16x16x32_bf16 v[88:91], a[184:187], v[196:199], v[88:91]\n"
    "ds_read_b64_tr_b16 a[172:173], v246 offset:36864\n"
    "v_cndmask_b32_e64 v234, v234, 1.0, vcc\n"
    "v_mfma_f32_16x16x32_bf16 v[88:91], a[188:191], v[200:203], v[88:91]\n"
    "ds_read_b64_tr_b16 a[178:179], v246 offset:14336\n"
    "v_mfma_f32_16x16x32_bf16 v[92:95], a[188:191], v[208:211], v[92:95]\n"
    "buffer_load_dwordx4 a[136:139], v250, s[8:11], s62 offen offset:0 sc1\n"
    "v_mfma_f32_16x16x32_bf16 v[92:95], a[184:187], v[204:207], v[92:95]\n"
    "ds_read_b64_tr_b16 a[180:181], v246 offset:45056\n"
    "v_cvt_pk_bf16_f32 v70, v88, v89\n"
    "v_mfma_f32_16x16x32_bf16 v[96:99], a[184:187], v[212:215], v[96:99]\n"
    "ds_read_b64_tr_b16 a[174:175], v246 offset:38912\n"
    "v_cvt_pk_bf16_f32 v71, v90, v91\n"
    "v_mfma_f32_16x16x32_bf16 v[96:99], a[188:191], v[216:219], v[96:99]\n"
    "s_waitcnt lgkmcnt(5)\n"
    "ds_read_b64_tr_b16 a[182:183], v246 offset:47104\n"
    "v_mfma_f32_16x16x32_bf16 v[116:119], a[160:163], a[208:211], v[116:119]\n"
    "buffer_store_dwordx4 v[68:71], v250, s[8:11], s82 offen offset:2048 sc1\n"
    "v_mfma_f32_16x16x32_bf16 v[116:119], a[164:167], a[212:215], v[116:119]\n"
    "ds_read_b64_tr_b16 a[184:185], v246 offset:20480\n"
    "v_mfma_f32_16x16x32_bf16 v[112:115], a[164:167], a[204:207], v[112:115]\n"
    "v_cvt_pk_bf16_f32 v74, v92, v93\n"
    "v_cvt_pk_bf16_f32 v75, v94, v95\n"
    "v_mfma_f32_16x16x32_bf16 v[112:115], a[160:163], a[200:203], v[112:115]\n"
    "ds_read_b64_tr_b16 a[186:187], v246 offset:22528\n"
    "v_mfma_f32_16x16x32_bf16 v[108:111], a[160:163], a[192:195], v[108:111]\n"
    "buffer_load_dwordx4 a[140:143], v250, s[8:11], s62 offen offset:1024 sc1\n"
    "v_mfma_f32_16x16x32_bf16 v[108:111], a[164:167], a[196:199], v[108:111]\n"
    "s_waitcnt lgkmcnt(3)\n"
    "ds_read_b64_tr_b16 a[160:161], v247 offset:28672\n"
    "v_mfma_f32_16x16x32_bf16 v[108:111], a[168:171], v[156:159], v[108:111]\n"
    "ds_read_b64_tr_b16 a[162:163], v247 offset:30720\n"
    "v_mfma_f32_16x16x32_bf16 v[108:111], a[172:175], v[160:163], v[108:111]\n"
    "v_cvt_pk_bf16_f32 v76, v76, v77\n"
    "v_mfma_f32_16x16x32_bf16 v[112:115], a[172:175], v[168:171], v[112:115]\n"
    "ds_read_b64_tr_b16 a[188:189], v246 offset:53248\n"
    "v_mfma_f32_16x16x32_bf16 v[112:115], a[168:171], v[164:167], v[112:115]\n"
    "ds_read_b64_tr_b16 a[190:191], v246 offset:55296\n"
    "v_mfma_f32_16x16x32_bf16 v[116:119], a[168:171], v[172:175], v[116:119]\n"
    "v_mfma_f32_16x16x32_bf16 v[116:119], a[172:175], v[176:179], v[116:119]\n"
    "s_waitcnt lgkmcnt(6)\n"
    "ds_read_b64_tr_b16 a[164:165], v247 offset:61440\n"
    "v_cmp_eq_u32_e32 vcc, 2, v252\n"
    "v_mfma_f32_16x16x32_bf16 v[116:119], a[176:179], v[180:183], v[116:119]\n"
    "ds_read_b64_tr_b16 a[168:169], v247 offset:4096\n"
    "v_mfma_f32_16x16x32_bf16 v[116:119], a[180:183], v[184:187], v[116:119]\n"
    "buffer_store_dwordx4 v[72:75], v250, s[8:11], s28 offen offset:2048 sc1\n"
    "v_mfma_f32_16x16x32_bf16 v[112:115], a[180:183], v[152:155], v[112:115]\n"
    "ds_read_b64_tr_b16 a[170:171], v247 offset:6144\n"
    "v_cndmask_b32_e32 v235, 0, v253, vcc\n"
    "v_mfma_f32_16x16x32_bf16 v[112:115], a[176:179], v[148:151], v[112:115]\n"
    "ds_read_b64_tr_b16 a[166:167], v247 offset:63488\n"
    "v_cvt_pk_bf16_f32 v77, v78, v79\n"
    "v_mfma_f32_16x16x32_bf16 v[108:111], a[176:179], v[140:143], v[108:111]\n"
    "buffer_load_dwordx4 a[144:147], v250, s[8:11], s62 offen offset:2048 sc1\n"
    "v_mfma_f32_16x16x32_bf16 v[108:111], a[180:183], v[144:147], v[108:111]\n"
    "s_waitcnt lgkmcnt(4)\n"
    "v_cmp_eq_u32_e32 vcc, 3, v252\n"
    "v_cvt_pk_bf16_f32 v78, v96, v97\n"
    "v_mfma_f32_16x16x32_bf16 v[108:111], a[184:187], v[196:199], v[108:111]\n"
    "v_cvt_pk_bf16_f32 v79, v98, v99\n"
    "v_mfma_f32_16x16x32_bf16 v[108:111], a[188:191], v[200:203], v[108:111]\n"
    "ds_read_b64_tr_b16 a[176:177], v247 offset:12288\n"
    "v_mfma_f32_16x16x32_bf16 v[112:115], a[188:191], v[208:211], v[112:115]\n"
    "ds_read_b64_tr_b16 a[172:173], v247 offset:36864\n"
    "v_mfma_f32_16x16x32_bf16 v[112:115], a[184:187], v[204:207], v[112:115]\n"
    "ds_read_b64_tr_b16 a[178:179], v247 offset:14336\n"
    "v_cvt_pk_bf16_f32 v108, v108, v109\n"
    "v_mfma_f32_16x16x32_bf16 v[116:119], a[184:187], v[212:215], v[116:119]\n"
    "ds_read_b64_tr_b16 a[174:175], v247 offset:38912\n"
    "v_cndmask_b32_e64 v235, v235, 1.0, vcc\n"
    "v_mfma_f32_16x16x32_bf16 v[116:119], a[188:191], v[216:219], v[116:119]\n"
    "s_waitcnt lgkmcnt(4)\n"
    "v_cvt_pk_bf16_f32 v109, v110, v111\n"
    "v_mfma_f32_16x16x32_bf16 v[136:139], a[160:163], a[208:211], v[136:139]\n"
    "s_cmp_lg_u32 s44, 0\n"
    "s_cbranch_scc1 .Lat_glate\n"
    ".Lat_glate_ret:\n"
    "v_mfma_f32_16x16x32_bf16 v[136:139], a[164:167], a[212:215], v[136:139]\n"
    "ds_read_b64_tr_b16 a[180:181], v247 offset:45056\n"
    "v_mfma_f32_16x16x32_bf16 v[132:135], a[164:167], a[204:207], v[132:135]\n"
    "buffer_store_dwordx4 v[76:79], v250, s[8:11], s29 offen offset:2048 sc1\n"
    "v_mfma_f32_16x16x32_bf16 v[132:135], a[160:163], a[200:203], v[132:135]\n"
    "ds_read_b64_tr_b16 a[184:185], v247 offset:20480\n"
    "v_mfma_f32_16x16x32_bf16 v[128:131], a[160:163], a[192:195], v[128:131]\n"
    "ds_read_b64_tr_b16 a[182:183], v247 offset:47104\n"
    "v_mfma_f32_16x16x32_bf16 v[128:131], a[164:167], a[196:199], v[128:131]\n"
    "s_waitcnt lgkmcnt(3)\n"
    "ds_read_b64_tr_b16 a[186:187], v247 offset:22528\n"
    "v_mfma_f32_16x16x32_bf16 v[128:131], a[168:171], v[156:159], v[128:131]\n"
    "buffer_load_dwordx4 a[148:151], v250, s[8:11], s62 offen offset:3072 sc1\n"
    "v_mfma_f32_16x16x32_bf16 v[128:131], a[172:175], v[160:163], v[128:131]\n"
    "s_add_u32 s62, s57, 28672\n"
    "ds_read_b64_tr_b16 a[188:189], v247 offset:53248\n"
    "v_mfma_f32_16x16x32_bf16 v[132:135], a[172:175], v[168:171], v[132:135]\n"
    "ds_read_b64_tr_b16 a[190:191], v247 offset:55296\n"
    "v_mfma_f32_16x16x32_bf16 v[132:135], a[168:171], v[164:167], v[132:135]\n"
    "ds_read_b128 a[160:163], v240 offset:24576\n"
    "v_mfma_f32_16x16x32_bf16 v[136:139], a[168:171], v[172:175], v[136:139]\n"
    "v_mfma_f32_16x16x32_bf16 v[136:139], a[172:175], v[176:179], v[136:139]\n"
    "s_waitcnt lgkmcnt(4)\n"
    "ds_read_b128 a[168:171], v240 offset:28672\n"
    "v_mfma_f32_16x16x32_bf16 v[136:139], a[176:179], v[180:183], v[136:139]\n"
    "v_cvt_pk_bf16_f32 v116, v116, v117\n"
    "v_mfma_f32_16x16x32_bf16 v[136:139], a[180:183], v[184:187], v[136:139]\n"
    "v_cvt_pk_bf16_f32 v112, v112, v113\n"
    "v_mfma_f32_16x16x32_bf16 v[132:135], a[180:183], v[152:155], v[132:135]\n"
    "buffer_load_dwordx4 a[152:155], v250, s[8:11], s62 offen offset:0 sc1\n"
    "v_mfma_f32_16x16x32_bf16 v[132:135], a[176:179], v[148:151], v[132:135]\n"
    "ds_read_b128 a[164:167], v241 offset:24576\n"
    "v_mfma_f32_16x16x32_bf16 v[128:131], a[176:179], v[140:143], v[128:131]\n"
    "ds_read_b128 a[172:175], v241 offset:28672\n"
    "v_mfma_f32_16x16x32_bf16 v[128:131], a[180:183], v[144:147], v[128:131]\n"
    "s_waitcnt lgkmcnt(4)\n"
    "v_mfma_f32_16x16x32_bf16 v[128:131], a[184:187], v[196:199], v[128:131]\n"
    "v_cvt_pk_bf16_f32 v117, v118, v119\n"
    "v_mfma_f32_16x16x32_bf16 v[128:131], a[188:191], v[200:203], v[128:131]\n"
    "v_cvt_pk_bf16_f32 v113, v114, v115\n"
    "v_mfma_f32_16x16x32_bf16 v[132:135], a[188:191], v[208:211], v[132:135]\n"
    "ds_read_b128 a[176:179], v242 offset:24576\n"
    "v_mfma_f32_16x16x32_bf16 v[132:135], a[184:187], v[204:207], v[132:135]\n"
    "ds_read_b128 a[180:183], v243 offset:24576\n"
    "v_mfma_f32_16x16x32_bf16 v[136:139], a[184:187], v[212:215], v[136:139]\n"
    "v_cvt_pk_bf16_f32 v110, v128, v129\n"
    "v_mfma_f32_16x16x32_bf16 v[136:139], a[188:191], v[216:219], v[136:139]\n"
    "s_waitcnt lgkmcnt(3)\n"
    "ds_read_b128 a[184:187], v242 offset:28672\n"
    "v_cvt_pk_bf16_f32 v111, v130, v131\n"
    "v_mfma_f32_16x16x32_bf16 v[164:167], a[160:163], a[32:35], v[224:227]\n"
    "buffer_store_dwordx4 v[108:111], v250, s[8:11], s82 offen offset:3072 sc1\n"
    "v_mfma_f32_16x16x32_bf16 v[164:167], a[164:167], a[36:39], v[164:167]\n"
    "v_cvt_pk_bf16_f32 v118, v136, v137\n"
    "v_mfma_f32_16x16x32_bf16 v[156:159], a[164:167], a[4:7], v[220:223]\n"
    "v_mfma_f32_16x16x32_bf16 v[156:159], a[160:163], a[0:3], v[156:159]\n"
    "s_waitcnt lgkmcnt(3)\n"
    "ds_read_b128 a[188:191], v243 offset:28672\n"
    "v_mfma_f32_16x16x32_bf16 v[156:159], a[168:171], a[8:11], v[156:159]\n"
    "buffer_store_dword v131, v231, s[8:11], s87 offen offset:0 sc1\n"
    "v_mfma_f32_16x16x32_bf16 v[156:159], a[172:175], a[12:15], v[156:159]\n"
    "ds_read_b128 a[160:163], v240 offset:26624\n"
    "v_cvt_pk_bf16_f32 v114, v132, v133\n"
    "v_mfma_f32_16x16x32_bf16 v[164:167], a[172:175], a[44:47], v[164:167]\n"
    "ds_read_b128 a[164:167], v241 offset:26624\n"
    "v_cvt_pk_bf16_f32 v115, v134, v135\n"
    "v_mfma_f32_16x16x32_bf16 v[164:167], a[168:171], a[40:43], v[164:167]\n"
    "s_waitcnt lgkmcnt(4)\n"
    "v_cvt_pk_bf16_f32 v119, v138, v139\n"
    "v_mfma_f32_16x16x32_bf16 v[164:167], a[176:179], a[48:51], v[164:167]\n"
    "ds_read_b128 a[168:171], v240 offset:30720\n"
    "v_mfma_f32_16x16x32_bf16 v[164:167], a[180:183], a[52:55], v[164:167]\n"
    "buffer_store_dword v135, v231, s[8:11], s88 offen offset:0 sc1\n"
    "v_mfma_f32_16x16x32_bf16 v[156:159], a[180:183], a[20:23], v[156:159]\n"
    "ds_read_b128 a[172:175], v241 offset:30720\n"
    "v_mfma_f32_16x16x32_bf16 v[156:159], a[176:179], a[16:19], v[156:159]\n"
    "s_waitcnt lgkmcnt(4)\n"
    "v_mfma_f32_16x16x32_bf16 v[156:159], a[184:187], a[24:27], v[156:159]\n"
    "ds_read_b128 a[176:179], v242 offset:26624\n"
    "v_mfma_f32_16x16x32_bf16 v[156:159], a[188:191], a[28:31], v[156:159]\n"
    "buffer_store_dwordx4 v[112:115], v250, s[8:11], s28 offen offset:3072 sc1\n"
    "v_mfma_f32_16x16x32_bf16 v[164:167], a[188:191], a[60:63], v[164:167]\n"
    "ds_read_b128 a[180:183], v243 offset:26624\n"
    "v_mfma_f32_16x16x32_bf16 v[164:167], a[184:187], a[56:59], v[164:167]\n"
    "s_waitcnt lgkmcnt(4)\n"
    "v_exp_f32_e32 v156, v156\n"
    "v_mfma_f32_16x16x32_bf16 v[168:171], a[160:163], a[32:35], v[224:227]\n"
    "buffer_store_dword v139, v231, s[8:11], s89 offen offset:0 sc1\n"
    "v_mfma_f32_16x16x32_bf16 v[168:171], a[164:167], a[36:39], v[168:171]\n"
    "s_mov_b32 s56, 0\n"
    "ds_read_b128 a[184:187], v242 offset:30720\n"
    "ds_read_b128 a[188:191], v243 offset:30720\n"
    "v_mfma_f32_16x16x32_bf16 v[160:163], a[164:167], a[4:7], v[220:223]\n"
    "v_exp_f32_e32 v157, v157\n"
    "v_mfma_f32_16x16x32_bf16 v[160:163], a[160:163], a[0:3], v[160:163]\n"
    "s_waitcnt lgkmcnt(4)\n"
    "ds_read_b128 a[160:163], v240 offset:0\n"
    "v_add_f32_e32 v128, v131, v135\n"
    "v_mfma_f32_16x16x32_bf16 v[160:163], a[168:171], a[8:11], v[160:163]\n"
    "buffer_load_dwordx4 a[156:159], v250, s[8:11], s62 offen offset:1024 sc1\n"
    "v_mfma_f32_16x16x32_bf16 v[160:163], a[172:175], a[12:15], v[160:163]\n"
    "s_add_u32 s62, s58, 0\n"
    "ds_read_b128 a[164:167], v241 offset:0\n"
    "v_min3_f32 v129, v131, v135, v139\n"
    "v_mfma_f32_16x16x32_bf16 v[168:171], a[172:175], a[44:47], v[168:171]\n"
    "v_exp_f32_e32 v158, v158\n"
    "v_mfma_f32_16x16x32_bf16 v[168:171], a[168:171], a[40:43], v[168:171]\n"
    "s_waitcnt lgkmcnt(4)\n"
    "ds_read_b128 a[168:171], v240 offset:4096\n"
    "v_add_f32_e32 v128, v128, v139\n"
    "v_mfma_f32_16x16x32_bf16 v[168:171], a[176:179], a[48:51], v[168:171]\n"
    "v_exp_f32_e32 v159, v159\n"
    "v_mfma_f32_16x16x32_bf16 v[168:171], a[180:183], a[52:55], v[168:171]\n"
    "ds_read_b128 a[172:175], v241 offset:4096\n"
    "v_cvt_pk_bf16_f32 v156, v156, v157\n"
    "v_mfma_f32_16x16x32_bf16 v[160:163], a[180:183], a[20:23], v[160:163]\n"
    "buffer_store_dwordx4 v[116:119], v250, s[8:11], s29 offen offset:3072 sc1\n"
    "v_mfma_f32_16x16x32_bf16 v[160:163], a[176:179], a[16:19], v[160:163]\n"
    "s_waitcnt lgkmcnt(4)\n"
    "v_exp_f32_e32 v164, v164\n"
    "v_mfma_f32_16x16x32_bf16 v[160:163], a[184:187], a[24:27], v[160:163]\n"
    "ds_write_addtid_b32 v156 offset:0\n"
    "v_exp_f32_e32 v165, v165\n"
    "v_mfma_f32_16x16x32_bf16 v[160:163], a[188:191], a[28:31], v[160:163]\n"
    "buffer_load_dwordx4 a[244:247], v250, s[8:11], s62 offen offset:0 sc1\n"
    "v_mfma_f32_16x16x32_bf16 v[168:171], a[188:191], a[60:63], v[168:171]\n"
    "ds_read_b128 a[176:179], v242 offset:0\n"
    "v_cvt_pk_bf16_f32 v157, v158, v159\n"
    "v_mfma_f32_16x16x32_bf16 v[168:171], a[184:187], a[56:59], v[168:171]\n"
    "s_waitcnt lgkmcnt(4)\n"
    "ds_read_b128 a[180:183], v243 offset:0\n"
    "v_exp_f32_e32 v166, v166\n"
    "v_mfma_f32_16x16x32_bf16 v[204:207], a[160:163], a[32:35], v[224:227]\n"
    "v_exp_f32_e32 v167, v167\n"
    "v_mfma_f32_16x16x32_bf16 v[204:207], a[164:167], a[36:39], v[204:207]\n"
    "ds_read_b128 a[184:187], v242 offset:4096\n"
    "v_cvt_pk_bf16_f32 v164, v164, v165\n"
    "v_mfma_f32_16x16x32_bf16 v[196:199], a[164:167], a[4:7], v[220:223]\n"
    "v_exp_f32_e32 v160, v160\n"
    "v_mfma_f32_16x16x32_bf16 v[196:199], a[160:163], a[0:3], v[196:199]\n"
    "s_waitcnt lgkmcnt(4)\n"
    "v_exp_f32_e32 v161, v161\n"
    "v_mfma_f32_16x16x32_bf16 v[196:199], a[168:171], a[8:11], v[196:199]\n"
    "ds_read_b128 a[188:191], v243 offset:4096\n"
    "v_cvt_pk_bf16_f32 v165, v166, v167\n"
    "v_mfma_f32_16x16x32_bf16 v[196:199], a[172:175], a[12:15], v[196:199]\n"
    "buffer_load_dwordx4 v[8:11], v250, s[8:11], s62 offen offset:1024 sc1\n"
    "v_mfma_f32_16x16x32_bf16 v[204:207], a[172:175], a[44:47], v[204:207]\n"
    "ds_write_addtid_b32 v157 offset:256\n"
    "v_exp_f32_e32 v162, v162\n"
    "v_mfma_f32_16x16x32_bf16 v[204:207], a[168:171], a[40:43], v[204:207]\n"
    "s_waitcnt lgkmcnt(3)\n"
    "ds_read_b128 a[160:163], v240 offset:2048\n"
    "v_exp_f32_e32 v163, v163\n"
    "v_mfma_f32_16x16x32_bf16 v[204:207], a[176:179], a[48:51], v[204:207]\n"
    "ds_read_b128 a[164:167], v241 offset:2048\n"
    "v_cvt_pk_bf16_f32 v158, v160, v161\n"
    "v_mfma_f32_16x16x32_bf16 v[204:207], a[180:183], a[52:55], v[204:207]\n"
    "ds_read_b128 a[168:171], v240 offset:6144\n"
    "ds_read_b128 a[172:175], v241 offset:6144\n"
    "v_mfma_f32_16x16x32_bf16 v[196:199], a[180:183], a[20:23], v[196:199]\n"
    "v_exp_f32_e32 v168, v168\n"
    "v_mfma_f32_16x16x32_bf16 v[196:199], a[176:179], a[16:19], v[196:199]\n"
    "s_waitcnt lgkmcnt(5)\n"
    "v_exp_f32_e32 v169, v169\n"
    "v_mfma_f32_16x16x32_bf16 v[196:199], a[184:187], a[24:27], v[196:199]\n"
    "ds_read_b128 a[176:179], v242 offset:2048\n"
    "v_cvt_pk_bf16_f32 v159, v162, v163\n"
    "v_mfma_f32_16x16x32_bf16 v[196:199], a[188:191], a[28:31], v[196:199]\n"
    "buffer_load_dwordx4 a[248:251], v250, s[8:11], s62 offen offset:2048 sc1\n"
    "ds_read_b128 a[180:183], v243 offset:2048\n"
    "v_mfma_f32_16x16x32_bf16 v[204:207], a[188:191], a[60:63], v[204:207]\n"
    "v_exp_f32_e32 v170, v170\n"
    "v_mfma_f32_16x16x32_bf16 v[204:207], a[184:187], a[56:59], v[204:207]\n"
    "s_waitcnt lgkmcnt(4)\n"
    "ds_write_addtid_b32 v164 offset:1024\n"
    "v_exp_f32_e32 v196, v196\n"
    "v_mfma_f32_16x16x32_bf16 v[208:211], a[160:163], a[32:35], v[224:227]\n"
    "ds_read_b128 a[184:187], v242 offset:6144\n"
    "v_exp_f32_e32 v197, v197\n"
    "v_mfma_f32_16x16x32_bf16 v[208:211], a[164:167], a[36:39], v[208:211]\n"
    "ds_read_b128 a[188:191], v243 offset:6144\n"
    "v_exp_f32_e32 v198, v198\n"
    "v_mfma_f32_16x16x32_bf16 v[200:203], a[164:167], a[4:7], v[220:223]\n"
    "v_exp_f32_e32 v171, v171\n"
    "v_mfma_f32_16x16x32_bf16 v[200:203], a[160:163], a[0:3], v[200:203]\n"
    "s_waitcnt lgkmcnt(5)\n"
    "ds_write_addtid_b32 v165 offset:1280\n"
    "v_cvt_pk_bf16_f32 v166, v168, v169\n"
    "v_mfma_f32_16x16x32_bf16 v[200:203], a[168:171], a[8:11], v[200:203]\n"
    "ds_read_b128 a[160:163], v240 offset:8192\n"
    "v_exp_f32_e32 v199, v199\n"
    "v_mfma_f32_16x16x32_bf16 v[200:203], a[172:175], a[12:15], v[200:203]\n"
    "buffer_load_dwordx4 v[12:15], v250, s[8:11], s62 offen offset:3072 sc1\n"
    "v_mfma_f32_16x16x32_bf16 v[208:211], a[172:175], a[44:47], v[208:211]\n"
    "s_add_u32 s62, s58, 4096\n"
    "ds_read_b128 a[164:167], v241 offset:8192\n"
    "v_cvt_pk_bf16_f32 v196, v196, v197\n"
    "v_mfma_f32_16x16x32_bf16 v[208:211], a[168:171], a[40:43], v[208:211]\n"
    "s_waitcnt lgkmcnt(6)\n"
    "ds_write_addtid_b32 v158 offset:512\n"
    "v_exp_f32_e32 v204, v204\n"
    "v_mfma_f32_16x16x32_bf16 v[208:211], a[176:179], a[48:51], v[208:211]\n"
    "v_exp_f32_e32 v205, v205\n"
    "v_mfma_f32_16x16x32_bf16 v[208:211], a[180:183], a[52:55], v[208:211]\n"
    "ds_read_b128 a[168:171], v240 offset:12288\n"
    "v_cvt_pk_bf16_f32 v197, v198, v199\n"
    "v_mfma_f32_16x16x32_bf16 v[200:203], a[180:183], a[20:23], v[200:203]\n"
    "ds_read_b128 a[172:175], v241 offset:12288\n"
    "ds_write_addtid_b32 v159 offset:768\n"
    "v_mfma_f32_16x16x32_bf16 v[200:203], a[176:179], a[16:19], v[200:203]\n"
    "s_waitcnt lgkmcnt(7)\n"
    "v_exp_f32_e32 v206, v206\n"
    "v_mfma_f32_16x16x32_bf16 v[200:203], a[184:187], a[24:27], v[200:203]\n"
    "v_exp_f32_e32 v207, v207\n"
    "v_mfma_f32_16x16x32_bf16 v[200:203], a[188:191], a[28:31], v[200:203]\n"
    "buffer_load_dwordx4 a[252:255], v250, s[8:11], s62 offen offset:0 sc1\n"
    "v_mfma_f32_16x16x32_bf16 v[208:211], a[188:191], a[60:63], v[208:211]\n"
    "ds_read_b128 a[176:179], v242 offset:8192\n"
    "v_cvt_pk_bf16_f32 v204, v204, v205\n"
    "v_mfma_f32_16x16x32_bf16 v[208:211], a[184:187], a[56:59], v[208:211]\n"
    "s_waitcnt lgkmcnt(5)\n"
    "v_exp_f32_e32 v200, v200\n"
    "v_mfma_f32_16x16x32_bf16 v[212:215], a[160:163], a[32:35], v[224:227]\n"
    "ds_read_b128 a[180:183], v243 offset:8192\n"
    "v_exp_f32_e32 v201, v201\n"
    "v_mfma_f32_16x16x32_bf16 v[212:215], a[164:167], a[36:39], v[212:215]\n"
    "ds_read_b128 a[184:187], v242 offset:12288\n"
    "v_cvt_pk_bf16_f32 v205, v206, v207\n"
    "v_mfma_f32_16x16x32_bf16 v[172:175], a[164:167], a[4:7], v[220:223]\n"
    "v_exp_f32_e32 v202, v202\n"
    "v_mfma_f32_16x16x32_bf16 v[172:175], a[160:163], a[0:3], v[172:175]\n"
    "s_waitcnt lgkmcnt(4)\n"
    "ds_read_b128 a[160:163], v240 offset:10240\n"
    "v_exp_f32_e32 v203, v203\n"
    "v_mfma_f32_16x16x32_bf16 v[172:175], a[168:171], a[8:11], v[172:175]\n"
    "ds_read_b128 a[188:191], v243 offset:12288\n"
    "v_cvt_pk_bf16_f32 v198, v200, v201\n"
    "v_mfma_f32_16x16x32_bf16 v[172:175], a[172:175], a[12:15], v[172:175]\n"
    "buffer_load_dwordx4 v[16:19], v250, s[8:11], s62 offen offset:1024 sc1\n"
    "v_mfma_f32_16x16x32_bf16 v[212:215], a[172:175], a[44:47], v[212:215]\n"
    "s_add_u32 s62, s57, 28672\n"
    "ds_read_b128 a[164:167], v241 offset:10240\n"
    "v_exp_f32_e32 v208, v208\n"
    "v_mfma_f32_16x16x32_bf16 v[212:215], a[168:171], a[40:43], v[212:215]\n"
    "s_waitcnt lgkmcnt(4)\n"
    "ds_write_addtid_b32 v166 offset:1536\n"
    "v_cvt_pk_bf16_f32 v167, v170, v171\n"
    "v_mfma_f32_16x16x32_bf16 v[212:215], a[176:179], a[48:51], v[212:215]\n"
    "v_exp_f32_e32 v209, v209\n"
    "v_mfma_f32_16x16x32_bf16 v[212:215], a[180:183], a[52:55], v[212:215]\n"
    "ds_read_b128 a[168:171], v240 offset:14336\n"
    "v_cvt_pk_bf16_f32 v199, v202, v203\n"
    "v_mfma_f32_16x16x32_bf16 v[172:175], a[180:183], a[20:23], v[172:175]\n"
    "v_exp_f32_e32 v210, v210\n"
    "v_mfma_f32_16x16x32_bf16 v[172:175], a[176:179], a[16:19], v[172:175]\n"
    "s_waitcnt lgkmcnt(3)\n"
    "v_exp_f32_e32 v211, v211\n"
    "v_mfma_f32_16x16x32_bf16 v[172:175], a[184:187], a[24:27], v[172:175]\n"
    "ds_read_b128 a[172:175], v241 offset:14336\n"
    "ds_write_addtid_b32 v167 offset:1792\n"
    "v_mfma_f32_16x16x32_bf16 v[172:175], a[188:191], a[28:31], v[172:175]\n"
    "buffer_load_dwordx4 v[180:183], v250, s[8:11], s62 offen offset:2048 sc1\n"
    "v_mfma_f32_16x16x32_bf16 v[212:215], a[188:191], a[60:63], v[212:215]\n"
    "ds_read_b128 a[176:179], v242 offset:10240\n"
    "ds_read_b128 a[180:183], v243 offset:10240\n"
    "v_mfma_f32_16x16x32_bf16 v[212:215], a[184:187], a[56:59], v[212:215]\n"
    "s_waitcnt lgkmcnt(6)\n"
    "ds_read_b128 a[184:187], v242 offset:14336\n"
    "v_exp_f32_e32 v172, v172\n"
    "v_mfma_f32_16x16x32_bf16 v[216:219], a[160:163], a[32:35], v[224:227]\n"
    "ds_read_b128 a[188:191], v243 offset:14336\n"
    "v_exp_f32_e32 v173, v173\n"
    "v_mfma_f32_16x16x32_bf16 v[216:219], a[164:167], a[36:39], v[216:219]\n"
    "s_waitcnt lgkmcnt(4)\n"
    "v_mfma_f32_16x16x32_bf16 v[176:179], a[164:167], a[4:7], v[220:223]\n"
    "s_barrier\n"
    "v_mfma_f32_16x16x32_bf16 v[176:179], a[160:163], a[0:3], v[176:179]\n"
    "s_waitcnt vmcnt(7)\n"
    "buffer_store_dword v254, v231, s[8:11], s38 offen offset:0 sc1\n"
    "v_mfma_f32_16x16x32_bf16 v[176:179], a[168:171], a[8:11], v[176:179]\n"
    "ds_read_b64_tr_b16 a[160:161], v244 offset:24576\n"
    "ds_read_b64_tr_b16 a[162:163], v244 offset:26624\n"
    "v_mfma_f32_16x16x32_bf16 v[176:179], a[172:175], a[12:15], v[176:179]\n"
    "ds_read2st64_b32 v[160:161], v249 offset0:0 offset1:1\n"
    "ds_read2st64_b32 v[162:163], v249 offset0:2 offset1:3\n"
    "v_mfma_f32_16x16x32_bf16 v[216:219], a[172:175], a[44:47], v[216:219]\n"
    "buffer_load_dwordx4 v[184:187], v250, s[8:11], s62 offen offset:3072 sc1\n"
    "ds_read2st64_b32 v[168:169], v249 offset0:4 offset1:5\n"
    "v_mfma_f32_16x16x32_bf16 v[216:219], a[168:171], a[40:43], v[216:219]\n"
    "s_waitcnt lgkmcnt(7)\n"
    "ds_read_b64_tr_b16 a[164:165], v244 offset:57344\n"
    "ds_read_b64_tr_b16 a[166:167], v244 offset:59392\n"
    "v_mfma_f32_16x16x32_bf16 v[216:219], a[176:179], a[48:51], v[216:219]\n"
    "ds_read_b64_tr_b16 a[168:169], v245 offset:24576\n"
    "ds_read2st64_b32 v[170:171], v249 offset0:6 offset1:7\n"
    "v_mfma_f32_16x16x32_bf16 v[216:219], a[180:183], a[52:55], v[216:219]\n"
    "ds_read_b64_tr_b16 a[170:171], v245 offset:26624\n"
    "v_exp_f32_e32 v174, v174\n"
    "v_mfma_f32_16x16x32_bf16 v[176:179], a[180:183], a[20:23], v[176:179]\n"
    "s_add_u32 s62, s58, 4096\n"
    "ds_read_b64_tr_b16 a[172:173], v245 offset:57344\n"
    "v_exp_f32_e32 v175, v175\n"
    "v_mfma_f32_16x16x32_bf16 v[176:179], a[176:179], a[16:19], v[176:179]\n"
    "s_waitcnt lgkmcnt(11)\n"
    "ds_read_b64_tr_b16 a[176:177], v246 offset:24576\n"
    "ds_read_b64_tr_b16 a[178:179], v246 offset:26624\n"
    "v_mfma_f32_16x16x32_bf16 v[176:179], a[184:187], a[24:27], v[176:179]\n"
    "buffer_load_dwordx4 v[28:31], v250, s[8:11], s62 offen offset:2048 sc1\n"
    "ds_read_b64_tr_b16 a[174:175], v245 offset:59392\n"
    "v_mfma_f32_16x16x32_bf16 v[176:179], a[188:191], a[28:31], v[176:179]\n"
    "ds_read_b64_tr_b16 a[180:181], v246 offset:57344\n"
    "v_cvt_pk_bf16_f32 v172, v172, v173\n"
    "v_mfma_f32_16x16x32_bf16 v[216:219], a[188:191], a[60:63], v[216:219]\n"
    "v_exp_f32_e32 v212, v212\n"
    "v_mfma_f32_16x16x32_bf16 v[216:219], a[184:187], a[56:59], v[216:219]\n"
    "s_waitcnt lgkmcnt(6)\n"
    "ds_read_b64_tr_b16 a[182:183], v246 offset:59392\n"
    "ds_read_b64_tr_b16 a[184:185], v247 offset:24576\n"
    "v_mfma_f32_16x16x32_bf16 a[240:243], a[160:163], v[164:167], a[240:243]\n"
    "buffer_load_dwordx4 v[48:51], v250, s[8:11], s62 offen offset:3072 sc1\n"
    "v_mfma_f32_16x16x32_bf16 a[240:243], a[164:167], v[168:171], a[240:243]\n"
    "s_add_u32 s62, s58, 8192\n"
    "ds_read_b64_tr_b16 a[186:187], v247 offset:26624\n"
    "v_exp_f32_e32 v213, v213\n"
    "v_mfma_f32_16x16x32_bf16 a[236:239], a[164:167], v[160:163], a[236:239]\n"
    "ds_write_addtid_b32 v196 offset:2048\n"
    "ds_write_addtid_b32 v172 offset:4096\n"
    "v_mfma_f32_16x16x32_bf16 a[236:239], a[160:163], v[156:159], a[236:239]\n"
    "s_waitcnt lgkmcnt(6)\n"
    "ds_read_b64_tr_b16 a[188:189], v247 offset:57344\n"
    "v_cvt_pk_bf16_f32 v173, v174, v175\n"
    "v_mfma_f32_16x16x32_bf16 v[0:3], a[168:171], v[156:159], v[0:3]\n"
    "buffer_load_dwordx4 v[32:35], v250, s[8:11], s62 offen offset:0 sc1\n"
    "ds_read_b64_tr_b16 a[190:191], v247 offset:59392\n"
    "v_mfma_f32_16x16x32_bf16 v[0:3], a[172:175], v[160:163], v[0:3]\n"
    "ds_read_b64_tr_b16 a[160:161], v244 offset:28672\n"
    "ds_read_b64_tr_b16 a[162:163], v244 offset:30720\n"
    "v_mfma_f32_16x16x32_bf16 v[4:7], a[172:175], v[168:171], v[4:7]\n"
    "v_exp_f32_e32 v214, v214\n"
    "v_mfma_f32_16x16x32_bf16 v[4:7], a[168:171], v[164:167], v[4:7]\n"
    "s_waitcnt lgkmcnt(8)\n"
    "ds_read_b64_tr_b16 a[164:165], v244 offset:61440\n"
    "ds_read_b64_tr_b16 a[168:169], v245 offset:28672\n"
    "v_mfma_f32_16x16x32_bf16 v[24:27], a[176:179], v[164:167], v[24:27]\n"
    "buffer_load_dwordx4 v[52:55], v250, s[8:11], s62 offen offset:1024 sc1\n"
    "ds_read_b64_tr_b16 a[166:167], v244 offset:63488\n"
    "v_mfma_f32_16x16x32_bf16 v[24:27], a[180:183], v[168:171], v[24:27]\n"
    "ds_read_b64_tr_b16 a[170:171], v245 offset:30720\n"
    "ds_read_b64_tr_b16 a[172:173], v245 offset:61440\n"
    "v_mfma_f32_16x16x32_bf16 v[20:23], a[180:183], v[160:163], v[20:23]\n"
    "ds_read_b64_tr_b16 a[174:175], v245 offset:63488\n"
    "v_exp_f32_e32 v215, v215\n"
    "v_mfma_f32_16x16x32_bf16 v[20:23], a[176:179], v[156:159], v[20:23]\n"
    "s_waitcnt lgkmcnt(8)\n"
    "ds_read_b64_tr_b16 a[176:177], v246 offset:28672\n"
    "ds_read_b64_tr_b16 a[178:179], v246 offset:30720\n"
    "v_mfma_f32_16x16x32_bf16 v[40:43], a[184:187], v[156:159], v[40:43]\n"
    "buffer_load_dwordx4 v[36:39], v250, s[8:11], s62 offen offset:2048 sc1\n"
    "ds_write_addtid_b32 v197 offset:2304\n"
    "v_mfma_f32_16x16x32_bf16 v[40:43], a[188:191], v[160:163], v[40:43]\n"
    "ds_write_addtid_b32 v204 offset:3072\n"
    "v_cvt_pk_bf16_f32 v212, v212, v213\n"
    "v_mfma_f32_16x16x32_bf16 v[44:47], a[188:191], v[168:171], v[44:47]\n"
    "ds_write_addtid_b32 v205 offset:3328\n"
    "v_exp_f32_e32 v176, v176\n"
    "v_mfma_f32_16x16x32_bf16 v[44:47], a[184:187], v[164:167], v[44:47]\n"
    "s_waitcnt lgkmcnt(8)\n"
    "ds_read_b64_tr_b16 a[180:181], v246 offset:61440\n"
    "ds_read_b64_tr_b16 a[184:185], v247 offset:28672\n"
    "v_mfma_f32_16x16x32_bf16 v[64:67], a[160:163], v[164:167], v[64:67]\n"
    "buffer_load_dwordx4 v[56:59], v250, s[8:11], s62 offen offset:3072 sc1\n"
    "ds_read_b64_tr_b16 a[182:183], v246 offset:63488\n"
    "v_mfma_f32_16x16x32_bf16 v[64:67], a[164:167], v[168:171], v[64:67]\n"
    "s_add_u32 s62, s58, 12288\n"
    "ds_write_addtid_b32 v173 offset:4352\n"
    "v_exp_f32_e32 v177, v177\n"
    "v_mfma_f32_16x16x32_bf16 v[60:63], a[164:167], v[160:163], v[60:63]\n"
    "ds_read_b64_tr_b16 a[186:187], v247 offset:30720\n"
    "ds_read_b64_tr_b16 a[188:189], v247 offset:61440\n"
    "v_mfma_f32_16x16x32_bf16 v[60:63], a[160:163], v[156:159], v[60:63]\n"
    "s_waitcnt lgkmcnt(11)\n"
    "ds_read_b128 a[160:163], v240 offset:16384\n"
    "ds_read_b128 a[164:167], v241 offset:16384\n"
    "v_mfma_f32_16x16x32_bf16 v[80:83], a[168:171], v[156:159], v[80:83]\n"
    "buffer_load_dwordx4 v[68:71], v250, s[8:11], s62 offen offset:0 sc1\n"
    "ds_read_b64_tr_b16 a[190:191], v247 offset:63488\n"
    "v_mfma_f32_16x16x32_bf16 v[80:83], a[172:175], v[160:163], v[80:83]\n"
    ".long 0xbedf00ff\n"
    ".Lat_w12:\n"
    "s_setpc_b64 s[2:3]\n"
    "ds_write_addtid_b32 v212 offset:5120\n"
    "v_cvt_pk_bf16_f32 v213, v214, v215\n"
    "v_mfma_f32_16x16x32_bf16 v[84:87], a[172:175], v[168:171], v[84:87]\n"
    "v_exp_f32_e32 v178, v178\n"
    "v_mfma_f32_16x16x32_bf16 v[84:87], a[168:171], v[164:167], v[84:87]\n"
    "s_waitcnt lgkmcnt(7)\n"
    "ds_write_addtid_b32 v198 offset:2560\n"
    "v_exp_f32_e32 v179, v179\n"
    "v_mfma_f32_16x16x32_bf16 v[104:107], a[176:179], v[164:167], v[104:107]\n"
    "buffer_load_dwordx4 v[88:91], v250, s[8:11], s62 offen offset:1024 sc1\n"
    "ds_write_addtid_b32 v213 offset:5376\n"
    "v_mfma_f32_16x16x32_bf16 v[104:107], a[180:183], v[168:171], v[104:107]\n"
    "s_waitcnt lgkmcnt(3)\n"
    "v_mfma_f32_16x16x32_bf16 v[100:103], a[180:183], v[160:163], v[100:103]\n"
    "s_barrier\n"
    "v_mfma_f32_16x16x32_bf16 v[100:103], a[176:179], v[156:159], v[100:103]\n"
    "ds_read_b128 a[168:171], v240 offset:20480\n"
    "ds_read_b128 a[176:179], v242 offset:16384\n"
    "v_mfma_f32_16x16x32_bf16 v[120:123], a[184:187], v[156:159], v[120:123]\n"
    "s_add_u32 s31, s47, s35\n"
    "ds_read_b128 a[172:175], v241 offset:20480\n"
    "v_cvt_pk_bf16_f32 v174, v176, v177\n"
    "v_mfma_f32_16x16x32_bf16 v[120:123], a[188:191], v[160:163], v[120:123]\n"
    "s_add_u32 m0, s55, 24576\n"
    "v_exp_f32_e32 v216, v216\n"
    "v_mfma_f32_16x16x32_bf16 v[124:127], a[188:191], v[168:171], v[124:127]\n"
    "buffer_load_dwordx4 v251, s[8:11], s31 offen offset:0 sc1 lds\n"
    "v_mfma_f32_16x16x32_bf16 v[124:127], a[184:187], v[164:167], v[124:127]\n"
    "ds_read_b128 a[180:183], v243 offset:16384\n"
    "ds_read_b128 a[184:187], v242 offset:20480\n"
    "v_mfma_f32_16x16x32_bf16 v[148:151], a[160:163], a[32:35], v[224:227]\n"
    "s_mov_b32 m0, s63\n"
    "v_exp_f32_e32 v217, v217\n"
    "v_mfma_f32_16x16x32_bf16 v[148:151], a[164:167], a[36:39], v[148:151]\n"
    "buffer_load_dword v253, v238, s[8:11], s48 offen offset:0 sc1\n"
    "v_mfma_f32_16x16x32_bf16 v[140:143], a[164:167], a[4:7], v[220:223]\n"
    "s_add_u32 s31, s31, 0x1000\n"
    "ds_read_b128 a[188:191], v243 offset:20480\n"
    "v_cvt_pk_bf16_f32 v206, v208, v209\n"
    "v_mfma_f32_16x16x32_bf16 v[140:143], a[160:163], a[0:3], v[140:143]\n"
    "s_waitcnt lgkmcnt(3)\n"
    "ds_write_addtid_b32 v199 offset:2816\n"
    "v_cvt_pk_bf16_f32 v207, v210, v211\n"
    "v_mfma_f32_16x16x32_bf16 v[140:143], a[168:171], a[8:11], v[140:143]\n"
    "s_add_u32 m0, s55, 25600\n"
    "ds_read_b128 a[160:163], v240 offset:18432\n"
    "v_cvt_pk_bf16_f32 v175, v178, v179\n"
    "v_mfma_f32_16x16x32_bf16 v[140:143], a[172:175], a[12:15], v[140:143]\n"
    "buffer_load_dwordx4 v251, s[8:11], s31 offen offset:0 sc1 lds\n"
    "ds_read_b128 a[164:167], v241 offset:18432\n"
    "v_mfma_f32_16x16x32_bf16 v[148:151], a[172:175], a[44:47], v[148:151]\n"
    "s_mov_b32 m0, s63\n"
    "v_exp_f32_e32 v218, v218\n"
    "v_mfma_f32_16x16x32_bf16 v[148:151], a[168:171], a[40:43], v[148:151]\n"
    "s_waitcnt lgkmcnt(5)\n"
    "ds_write_addtid_b32 v206 offset:3584\n"
    "ds_write_addtid_b32 v174 offset:4608\n"
    "v_mfma_f32_16x16x32_bf16 v[148:151], a[176:179], a[48:51], v[148:151]\n"
    "s_add_u32 s87, s59, 8192\n"
    "v_exp_f32_e32 v219, v219\n"
    "v_mfma_f32_16x16x32_bf16 v[148:151], a[180:183], a[52:55], v[148:151]\n"
    "s_add_u32 s31, s47, s36\n"
    "ds_write_addtid_b32 v207 offset:3840\n"
    "ds_write_addtid_b32 v175 offset:4864\n"
    "v_mfma_f32_16x16x32_bf16 v[140:143], a[180:183], a[20:23], v[140:143]\n"
    "s_add_u32 m0, s55, 26624\n"
    "ds_read_b128 a[168:171], v240 offset:22528\n"
    "v_cvt_pk_bf16_f32 v214, v216, v217\n"
    "v_mfma_f32_16x16x32_bf16 v[140:143], a[176:179], a[16:19], v[140:143]\n"
    "s_waitcnt lgkmcnt(8)\n"
    "ds_read_b128 a[172:175], v241 offset:22528\n"
    "ds_read_b128 a[176:179], v242 offset:18432\n"
    "v_mfma_f32_16x16x32_bf16 v[140:143], a[184:187], a[24:27], v[140:143]\n"
    "buffer_load_dwordx4 v251, s[8:11], s31 offen offset:0 sc1 lds\n"
    "v_mfma_f32_16x16x32_bf16 v[140:143], a[188:191], a[28:31], v[140:143]\n"
    "s_mov_b32 m0, s63\n"
    "ds_read_b128 a[180:183], v243 offset:18432\n"
    "v_cvt_pk_bf16_f32 v215, v218, v219\n"
    "v_mfma_f32_16x16x32_bf16 v[148:151], a[188:191], a[60:63], v[148:151]\n"
    "s_add_u32 s88, s59, 16384\n"
    "ds_write_addtid_b32 v214 offset:5632\n"
    "ds_write_addtid_b32 v215 offset:5888\n"
    "v_mfma_f32_16x16x32_bf16 v[148:151], a[184:187], a[56:59], v[148:151]\n"
    "s_waitcnt lgkmcnt(10)\n"
    "ds_read_b128 a[184:187], v242 offset:22528\n"
    "v_exp_f32_e32 v140, v140\n"
    "v_mfma_f32_16x16x32_bf16 v[152:155], a[160:163], a[32:35], v[224:227]\n"
    "s_add_u32 s31, s31, 0x1000\n"
    "ds_read_b128 a[188:191], v243 offset:22528\n"
    "v_exp_f32_e32 v141, v141\n"
    "v_mfma_f32_16x16x32_bf16 v[152:155], a[164:167], a[36:39], v[152:155]\n"
    "s_waitcnt lgkmcnt(2)\n"
    "v_mfma_f32_16x16x32_bf16 v[144:147], a[164:167], a[4:7], v[220:223]\n"
    "s_barrier\n"
    "v_mfma_f32_16x16x32_bf16 v[144:147], a[160:163], a[0:3], v[144:147]\n"
    "ds_read2st64_b32 v[200:201], v249 offset0:8 offset1:9\n"
    "v_exp_f32_e32 v142, v142\n"
    "v_mfma_f32_16x16x32_bf16 v[144:147], a[168:171], a[8:11], v[144:147]\n"
    "s_add_u32 m0, s55, 27648\n"
    "ds_read2st64_b32 v[202:203], v249 offset0:10 offset1:11\n"
    "ds_read2st64_b32 v[208:209], v249 offset0:12 offset1:13\n"
    "v_mfma_f32_16x16x32_bf16 v[144:147], a[172:175], a[12:15], v[144:147]\n"
    "buffer_load_dwordx4 v251, s[8:11], s31 offen offset:0 sc1 lds\n"
    "ds_read_b64_tr_b16 a[160:161], v244 offset:0\n"
    "v_mfma_f32_16x16x32_bf16 v[152:155], a[172:175], a[44:47], v[152:155]\n"
    "s_mov_b32 m0, s63\n"
    "ds_read_b64_tr_b16 a[162:163], v244 offset:2048\n"
    "ds_read2st64_b32 v[210:211], v249 offset0:14 offset1:15\n"
    "v_mfma_f32_16x16x32_bf16 v[152:155], a[168:171], a[40:43], v[152:155]\n"
    "ds_read_b64_tr_b16 a[164:165], v244 offset:32768\n"
    "ds_read_b64_tr_b16 a[166:167], v244 offset:34816\n"
    "v_mfma_f32_16x16x32_bf16 v[152:155], a[176:179], a[48:51], v[152:155]\n"
    "s_add_u32 s89, s59, 24576\n"
    "ds_read_b64_tr_b16 a[168:169], v245 offset:0\n"
    "ds_read2st64_b32 v[176:177], v249 offset0:16 offset1:17\n"
    "v_mfma_f32_16x16x32_bf16 v[152:155], a[180:183], a[52:55], v[152:155]\n"
    "s_add_u32 s31, s47, s37\n"
    "ds_read_b64_tr_b16 a[170:171], v245 offset:2048\n"
    "v_exp_f32_e32 v143, v143\n"
    "v_mfma_f32_16x16x32_bf16 v[144:147], a[180:183], a[20:23], v[144:147]\n"
    "s_add_u32 m0, s55, 28672\n"
    "ds_read_b64_tr_b16 a[172:173], v245 offset:32768\n"
    "v_cvt_pk_bf16_f32 v140, v140, v141\n"
    "v_mfma_f32_16x16x32_bf16 v[144:147], a[176:179], a[16:19], v[144:147]\n"
    "s_waitcnt lgkmcnt(12)\n"
    "ds_read_b64_tr_b16 a[176:177], v246 offset:0\n"
    "v_exp_f32_e32 v148, v148\n"
    "v_mfma_f32_16x16x32_bf16 v[144:147], a[184:187], a[24:27], v[144:147]\n"
    "buffer_load_dwordx4 v251, s[8:11], s31 offen offset:0 sc1 lds\n"
    "ds_read_b64_tr_b16 a[174:175], v245 offset:34816\n"
    "v_mfma_f32_16x16x32_bf16 v[144:147], a[188:191], a[28:31], v[144:147]\n"
    "s_mov_b32 m0, s63\n"
    "ds_read_b64_tr_b16 a[178:179], v246 offset:2048\n"
    "v_exp_f32_e32 v149, v149\n"
    "v_mfma_f32_16x16x32_bf16 v[152:155], a[188:191], a[60:63], v[152:155]\n"
    "ds_read_b64_tr_b16 a[180:181], v246 offset:32768\n"
    "v_cvt_pk_bf16_f32 v141, v142, v143\n"
    "v_mfma_f32_16x16x32_bf16 v[152:155], a[184:187], a[56:59], v[152:155]\n"
    "s_waitcnt lgkmcnt(8)\n"
    "ds_read2st64_b32 v[178:179], v249 offset0:18 offset1:19\n"
    "v_exp_f32_e32 v150, v150\n"
    "v_mfma_f32_16x16x32_bf16 a[240:243], a[160:163], v[204:207], a[240:243]\n"
    "s_add_u32 s31, s31, 0x1000\n"
    "ds_read_b64_tr_b16 a[182:183], v246 offset:34816\n"
    "v_exp_f32_e32 v151, v151\n"
    "v_mfma_f32_16x16x32_bf16 a[240:243], a[164:167], v[208:211], a[240:243]\n"
    "s_add_u32 m0, s55, 29696\n"
    "ds_read_b64_tr_b16 a[184:185], v247 offset:0\n"
    "v_cvt_pk_bf16_f32 v148, v148, v149\n"
    "v_mfma_f32_16x16x32_bf16 a[236:239], a[164:167], v[200:203], a[236:239]\n"
    "buffer_load_dwordx4 v251, s[8:11], s31 offen offset:0 sc1 lds\n"
    "ds_read_b64_tr_b16 a[186:187], v247 offset:2048\n"
    "v_mfma_f32_16x16x32_bf16 a[236:239], a[160:163], v[196:199], a[236:239]\n"
    "s_waitcnt lgkmcnt(6)\n"
    "ds_read_b64_tr_b16 a[188:189], v247 offset:32768\n"
    "ds_read_b64_tr_b16 a[160:161], v244 offset:4096\n"
    "v_mfma_f32_16x16x32_bf16 v[0:3], a[168:171], v[196:199], v[0:3]\n"
    "s_mov_b32 m0, s63\n"
    "ds_read_b64_tr_b16 a[190:191], v247 offset:34816\n"
    "v_exp_f32_e32 v144, v144\n"
    "v_mfma_f32_16x16x32_bf16 v[0:3], a[172:175], v[200:203], v[0:3]\n"
    "buffer_load_dwordx4 v[72:75], v250, s[8:11], s62 offen offset:2048 sc1\n"
    "ds_read_b64_tr_b16 a[162:163], v244 offset:6144\n"
    "v_mfma_f32_16x16x32_bf16 v[4:7], a[172:175], v[208:211], v[4:7]\n"
    "s_add_u32 s31, s46, s36\n"
    "ds_read_b64_tr_b16 a[164:165], v244 offset:36864\n"
    "ds_read_b64_tr_b16 a[166:167], v244 offset:38912\n"
    "v_mfma_f32_16x16x32_bf16 v[4:7], a[168:171], v[204:207], v[4:7]\n"
    "s_waitcnt lgkmcnt(8)\n"
    "ds_write_addtid_b32 v140 offset:0\n"
    "v_exp_f32_e32 v145, v145\n"
    "v_mfma_f32_16x16x32_bf16 v[24:27], a[176:179], v[204:207], v[24:27]\n"
    "s_add_u32 m0, s55, 30720\n"
    "s_waitcnt vmcnt(6)\n"
    "ds_read_b64_tr_b16 a[168:169], v245 offset:4096\n"
    "v_subrev_u32_e32 v253, s39, v253\n"
    "v_subrev_u32_e32 v253, 0, v253\n"
    "v_mfma_f32_16x16x32_bf16 v[24:27], a[180:183], v[208:211], v[24:27]\n"
    "buffer_load_dwordx4 v251, s[12:15], s31 offen offset:0 lds\n"
    "ds_read_b64_tr_b16 a[170:171], v245 offset:6144\n"
    "v_cmp_le_u32_e32 vcc, 1, v253\n"
    "v_min_u32_e32 v220, 0x13c, v248\n"
    "v_mfma_f32_16x16x32_bf16 v[20:23], a[180:183], v[200:203], v[20:23]\n"
    "s_mov_b32 m0, s63\n"
    "ds_read_b64_tr_b16 a[172:173], v245 offset:36864\n"
    "v_cvt_pk_bf16_f32 v149, v150, v151\n"
    "v_mfma_f32_16x16x32_bf16 v[20:23], a[176:179], v[196:199], v[20:23]\n"
    "s_waitcnt lgkmcnt(7)\n"
    "ds_read_b64_tr_b16 a[174:175], v245 offset:38912\n"
    "ds_read2st64_b32 v[216:217], v249 offset0:20 offset1:21\n"
    "v_mfma_f32_16x16x32_bf16 v[40:43], a[184:187], v[196:199], v[40:43]\n"
    "s_cbranch_vccnz .Lat_k\n"
    ".Lat_kr:\n"
    "s_cmp_lg_u32 s56, 0\n"
    "s_cbranch_scc1 .Lat_kx\n"
    ".Lat_kxr:\n"
    "v_mfma_f32_16x16x32_bf16 v[40:43], a[188:191], v[200:203], v[40:43]\n"
    "s_add_u32 s31, s31, 0x1000\n"
    "ds_read_b64_tr_b16 a[176:177], v246 offset:4096\n"
    "v_exp_f32_e32 v146, v146\n"
    "v_mfma_f32_16x16x32_bf16 v[44:47], a[188:191], v[208:211], v[44:47]\n"
    "s_add_u32 m0, s55, 31744\n"
    "ds_read_b64_tr_b16 a[178:179], v246 offset:6144\n"
    "v_exp_f32_e32 v147, v147\n"
    "v_mfma_f32_16x16x32_bf16 v[44:47], a[184:187], v[204:207], v[44:47]\n"
    "s_waitcnt lgkmcnt(8)\n"
    "ds_read_b64_tr_b16 a[180:181], v246 offset:36864\n"
    "ds_read2st64_b32 v[218:219], v249 offset0:22 offset1:23\n"
    "v_mfma_f32_16x16x32_bf16 v[64:67], a[160:163], v[204:207], v[64:67]\n"
    "buffer_load_dwordx4 v251, s[12:15], s31 offen offset:0 lds\n"
    "ds_read_b64_tr_b16 a[182:183], v246 offset:38912\n"
    "v_mfma_f32_16x16x32_bf16 v[64:67], a[164:167], v[208:211], v[64:67]\n"
    "s_mov_b32 m0, s63\n"
    "ds_read_b64_tr_b16 a[184:185], v247 offset:4096\n"
    "ds_read_b64_tr_b16 a[186:187], v247 offset:6144\n"
    "v_mfma_f32_16x16x32_bf16 v[60:63], a[164:167], v[200:203], v[60:63]\n"
    "buffer_load_dwordx4 a[0:3], v250, s[8:11], s87 offen offset:0 sc1\n"
    "ds_write_addtid_b32 v141 offset:256\n"
    "v_mfma_f32_16x16x32_bf16 v[60:63], a[160:163], v[196:199], v[60:63]\n"
    "s_waitcnt lgkmcnt(9)\n"
    "ds_read_b64_tr_b16 a[160:161], v244 offset:12288\n"
    "ds_read_b64_tr_b16 a[162:163], v244 offset:14336\n"
    "v_mfma_f32_16x16x32_bf16 v[80:83], a[168:171], v[196:199], v[80:83]\n"
    "ds_read_b64_tr_b16 a[188:189], v247 offset:36864\n"
    "ds_read_b64_tr_b16 a[190:191], v247 offset:38912\n"
    "v_mfma_f32_16x16x32_bf16 v[80:83], a[172:175], v[200:203], v[80:83]\n"
    "buffer_load_dwordx4 v[92:95], v250, s[8:11], s62 offen offset:3072 sc1\n"
    "ds_read_b64_tr_b16 a[164:165], v244 offset:45056\n"
    "v_mfma_f32_16x16x32_bf16 v[84:87], a[172:175], v[208:211], v[84:87]\n"
    "s_add_u32 s62, s58, 16384\n"
    "ds_write_addtid_b32 v148 offset:1024\n"
    "v_cvt_pk_bf16_f32 v142, v144, v145\n"
    "v_mfma_f32_16x16x32_bf16 v[84:87], a[168:171], v[204:207], v[84:87]\n"
    "s_waitcnt lgkmcnt(9)\n"
    "ds_read_b64_tr_b16 a[168:169], v245 offset:12288\n"
    "v_exp_f32_e32 v152, v152\n"
    "v_mfma_f32_16x16x32_bf16 v[104:107], a[176:179], v[204:207], v[104:107]\n"
    "ds_read_b64_tr_b16 a[166:167], v244 offset:47104\n"
    "ds_write_addtid_b32 v149 offset:1280\n"
    "v_mfma_f32_16x16x32_bf16 v[104:107], a[180:183], v[208:211], v[104:107]\n"
    "buffer_load_dwordx4 v[76:79], v250, s[8:11], s62 offen offset:0 sc1\n"
    "ds_read_b64_tr_b16 a[170:171], v245 offset:14336\n"
    "v_mfma_f32_16x16x32_bf16 v[100:103], a[180:183], v[200:203], v[100:103]\n"
    "ds_read_b64_tr_b16 a[172:173], v245 offset:45056\n"
    "v_exp_f32_e32 v153, v153\n"
    "v_mfma_f32_16x16x32_bf16 v[100:103], a[176:179], v[196:199], v[100:103]\n"
    "s_waitcnt lgkmcnt(7)\n"
    "ds_read_b64_tr_b16 a[174:175], v245 offset:47104\n"
    "ds_write_addtid_b32 v142 offset:512\n"
    "v_mfma_f32_16x16x32_bf16 v[120:123], a[184:187], v[196:199], v[120:123]\n"
    "buffer_load_dwordx4 a[4:7], v250, s[8:11], s88 offen offset:0 sc1\n"
    "ds_read_b64_tr_b16 a[176:177], v246 offset:12288\n"
    "v_mfma_f32_16x16x32_bf16 v[120:123], a[188:191], v[200:203], v[120:123]\n"
    "ds_read_b64_tr_b16 a[178:179], v246 offset:14336\n"
    "v_cvt_pk_bf16_f32 v143, v146, v147\n"
    "v_mfma_f32_16x16x32_bf16 v[124:127], a[188:191], v[208:211], v[124:127]\n"
    "ds_read_b64_tr_b16 a[180:181], v246 offset:45056\n"
    "ds_read_b64_tr_b16 a[182:183], v246 offset:47104\n"
    "v_mfma_f32_16x16x32_bf16 v[124:127], a[184:187], v[204:207], v[124:127]\n"
    "s_waitcnt lgkmcnt(9)\n"
    "ds_read_b64_tr_b16 a[184:185], v247 offset:12288\n"
    "v_exp_f32_e32 v154, v154\n"
    "v_mfma_f32_16x16x32_bf16 v[64:67], a[160:163], v[212:215], v[64:67]\n"
    "buffer_load_dwordx4 v[96:99], v250, s[8:11], s62 offen offset:1024 sc1\n"
    "ds_read_b64_tr_b16 a[186:187], v247 offset:14336\n"
    "v_mfma_f32_16x16x32_bf16 v[64:67], a[164:167], v[216:219], v[64:67]\n"
    "ds_read_b64_tr_b16 a[188:189], v247 offset:45056\n"
    "v_exp_f32_e32 v155, v155\n"
    "v_mfma_f32_16x16x32_bf16 v[60:63], a[164:167], v[176:179], v[60:63]\n"
    "ds_write_addtid_b32 v143 offset:768\n"
    "v_cvt_pk_bf16_f32 v150, v152, v153\n"
    "v_mfma_f32_16x16x32_bf16 v[60:63], a[160:163], v[172:175], v[60:63]\n"
    "s_waitcnt lgkmcnt(9)\n"
    "ds_write_addtid_b32 v150 offset:1536\n"
    "v_cvt_pk_bf16_f32 v151, v154, v155\n"
    "v_mfma_f32_16x16x32_bf16 v[80:83], a[168:171], v[172:175], v[80:83]\n"
    "ds_read_b64_tr_b16 a[190:191], v247 offset:47104\n"
    "ds_write_addtid_b32 v151 offset:1792\n"
    "v_mfma_f32_16x16x32_bf16 v[80:83], a[172:175], v[176:179], v[80:83]\n"
    "buffer_load_dwordx4 a[8:11], v250, s[8:11], s89 offen offset:0 sc1\n"
    "ds_read_b64_tr_b16 a[160:161], v244 offset:8192\n"
    "v_mfma_f32_16x16x32_bf16 v[84:87], a[172:175], v[216:219], v[84:87]\n"
    "ds_read_b64_tr_b16 a[162:163], v244 offset:10240\n"
    "ds_read_b64_tr_b16 a[164:165], v244 offset:40960\n"
    "ds_read_b64_tr_b16 a[166:167], v244 offset:43008\n"
    "v_mfma_f32_16x16x32_bf16 v[84:87], a[168:171], v[212:215], v[84:87]\n"
    "s_waitcnt lgkmcnt(11)\n"
    "s_waitcnt lgkmcnt(4)\n"
    "s_waitcnt vmcnt(9)\n"
    "v_mfma_f32_16x16x32_bf16 v[104:107], a[176:179], v[212:215], v[104:107]\n"
    "s_barrier\n"
    "v_mfma_f32_16x16x32_bf16 v[104:107], a[180:183], v[216:219], v[104:107]\n"
    "buffer_load_dwordx4 a[192:195], v250, s[8:11], s62 offen offset:2048 sc1\n"
    "ds_read_b64_tr_b16 a[168:169], v244 offset:16384\n"
    "v_mfma_f32_16x16x32_bf16 v[100:103], a[180:183], v[176:179], v[100:103]\n"
    "s_add_u32 s31, s46, s37\n"
    "ds_read_b64_tr_b16 a[170:171], v244 offset:18432\n"
    "ds_read_b64_tr_b16 a[172:173], v244 offset:49152\n"
    "v_mfma_f32_16x16x32_bf16 v[100:103], a[176:179], v[172:175], v[100:103]\n"
    "ds_read2st64_b32 v[144:145], v249 offset0:0 offset1:1\n"
    "ds_read2st64_b32 v[146:147], v249 offset0:2 offset1:3\n"
    "v_mfma_f32_16x16x32_bf16 v[120:123], a[184:187], v[172:175], v[120:123]\n"
    "s_add_u32 m0, s55, 4096\n"
    "ds_read_b64_tr_b16 a[174:175], v244 offset:51200\n"
    "ds_read2st64_b32 v[152:153], v249 offset0:4 offset1:5\n"
    "v_mfma_f32_16x16x32_bf16 v[120:123], a[188:191], v[176:179], v[120:123]\n"
    "buffer_load_dwordx4 v251, s[12:15], s31 offen offset:0 lds\n"
    "ds_read2st64_b32 v[154:155], v249 offset0:6 offset1:7\n"
    "v_mfma_f32_16x16x32_bf16 v[124:127], a[188:191], v[216:219], v[124:127]\n"
    "s_mov_b32 m0, s63\n"
    "ds_read_b64_tr_b16 a[176:177], v245 offset:8192\n"
    "ds_read_b64_tr_b16 a[178:179], v245 offset:10240\n"
    "v_mfma_f32_16x16x32_bf16 v[124:127], a[184:187], v[212:215], v[124:127]\n"
    "s_waitcnt lgkmcnt(10)\n"
    "ds_read_b64_tr_b16 a[180:181], v245 offset:40960\n"
    "v_add_u32_e32 v240, s45, v240\n"
    "v_mfma_f32_16x16x32_bf16 a[240:243], a[160:163], v[212:215], a[240:243]\n"
    "buffer_load_dwordx4 a[12:15], v250, s[8:11], s87 offen offset:1024 sc1\n"
    "ds_read_b64_tr_b16 a[182:183], v245 offset:43008\n"
    "v_mfma_f32_16x16x32_bf16 a[240:243], a[164:167], v[216:219], a[240:243]\n"
    "s_add_u32 s31, s31, 0x1000\n"
    "ds_read_b64_tr_b16 a[184:185], v245 offset:16384\n"
    "v_xor_b32_e32 v241, 64, v240\n"
    "v_mfma_f32_16x16x32_bf16 a[236:239], a[164:167], v[176:179], a[236:239]\n"
    "s_add_u32 m0, s55, 5120\n"
    "ds_read_b64_tr_b16 a[186:187], v245 offset:18432\n"
    "v_add_u32_e32 v242, 0x10000, v240\n"
    "v_mfma_f32_16x16x32_bf16 a[236:239], a[160:163], v[172:175], a[236:239]\n"
    "s_waitcnt lgkmcnt(6)\n"
    "ds_read_b64_tr_b16 a[188:189], v245 offset:49152\n"
    "ds_read_b64_tr_b16 a[190:191], v245 offset:51200\n"
    "v_mfma_f32_16x16x32_bf16 a[236:239], a[168:171], v[140:143], a[236:239]\n"
    "buffer_load_dwordx4 v251, s[12:15], s31 offen offset:0 lds\n"
    "v_mfma_f32_16x16x32_bf16 a[236:239], a[172:175], v[144:147], a[236:239]\n"
    "s_mov_b32 m0, s63\n"
    "ds_read_b64_tr_b16 a[160:161], v246 offset:8192\n"
    "ds_read_b64_tr_b16 a[162:163], v246 offset:10240\n"
    "v_mfma_f32_16x16x32_bf16 a[240:243], a[172:175], v[152:155], a[240:243]\n"
    "ds_read_b64_tr_b16 a[164:165], v246 offset:40960\n"
    "v_mfma_f32_16x16x32_bf16 a[240:243], a[168:171], v[148:151], a[240:243]\n"
    "s_waitcnt lgkmcnt(7)\n"
    "ds_read_b64_tr_b16 a[166:167], v246 offset:43008\n"
    "ds_read_b64_tr_b16 a[168:169], v246 offset:16384\n"
    "v_mfma_f32_16x16x32_bf16 v[4:7], a[176:179], v[212:215], v[4:7]\n"
    "ds_read_b64_tr_b16 a[170:171], v246 offset:18432\n"
    "v_mfma_f32_16x16x32_bf16 v[4:7], a[180:183], v[216:219], v[4:7]\n"
    "buffer_load_dwordx4 a[16:19], v250, s[8:11], s88 offen offset:1024 sc1\n"
    "v_mfma_f32_16x16x32_bf16 v[0:3], a[180:183], v[176:179], v[0:3]\n"
    "ds_read_b64_tr_b16 a[172:173], v246 offset:49152\n"
    "v_add_u32_e32 v243, 0x10000, v241\n"
    "v_mfma_f32_16x16x32_bf16 v[0:3], a[176:179], v[172:175], v[0:3]\n"
    "s_waitcnt lgkmcnt(7)\n"
    "ds_read_b64_tr_b16 a[174:175], v246 offset:51200\n"
    "v_add_u32_e32 v252, s54, v240\n"
    "v_mfma_f32_16x16x32_bf16 v[0:3], a[184:187], v[140:143], v[0:3]\n"
    "ds_read_b64_tr_b16 a[176:177], v247 offset:8192\n"
    "v_xor_b32_e32 v255, 64, v252\n"
    "v_mfma_f32_16x16x32_bf16 v[0:3], a[188:191], v[144:147], v[0:3]\n"
    "ds_read_b64_tr_b16 a[178:179], v247 offset:10240\n"
    "v_lshlrev_b32_e32 v228, 16, v228\n"
    "v_mfma_f32_16x16x32_bf16 v[4:7], a[188:191], v[152:155], v[4:7]\n"
    "ds_read_b64_tr_b16 a[180:181], v247 offset:40960\n"
    "v_mfma_f32_16x16x32_bf16 v[4:7], a[184:187], v[148:151], v[4:7]\n"
    "s_waitcnt lgkmcnt(7)\n"
    "ds_read_b64_tr_b16 a[184:185], v247 offset:16384\n"
    "v_lshlrev_b32_e32 v229, 16, v229\n"
    "v_mfma_f32_16x16x32_bf16 v[24:27], a[160:163], v[212:215], v[24:27]\n"
    "ds_read_b64_tr_b16 a[182:183], v247 offset:43008\n"
    "v_lshlrev_b32_e32 v230, 16, v230\n"
    "v_mfma_f32_16x16x32_bf16 v[24:27], a[164:167], v[216:219], v[24:27]\n"
    "ds_read_b64_tr_b16 a[186:187], v247 offset:18432\n"
    "v_cvt_pk_bf16_f32 v7, v6, v7\n"
    "v_mfma_f32_16x16x32_bf16 v[20:23], a[164:167], v[176:179], v[20:23]\n"
    "buffer_load_dwordx4 a[20:23], v250, s[8:11], s89 offen offset:1024 sc1\n"
    "v_mfma_f32_16x16x32_bf16 v[20:23], a[160:163], v[172:175], v[20:23]\n"
    "s_waitcnt lgkmcnt(6)\n"
    "ds_read_b64_tr_b16 a[188:189], v247 offset:49152\n"
    "v_cvt_pk_bf16_f32 v6, v4, v5\n"
    "v_mfma_f32_16x16x32_bf16 v[20:23], a[168:171], v[140:143], v[20:23]\n"
    "ds_read_b64_tr_b16 a[190:191], v247 offset:51200\n"
    "v_accvgpr_read_b32 v4, a240\n"
    "v_mfma_f32_16x16x32_bf16 v[20:23], a[172:175], v[144:147], v[20:23]\n"
    "ds_read_b64_tr_b16 a[160:161], v244 offset:20480\n"
    "ds_read_b64_tr_b16 a[162:163], v244 offset:22528\n"
    "v_mfma_f32_16x16x32_bf16 v[24:27], a[172:175], v[152:155], v[24:27]\n"
    "ds_read_b64_tr_b16 a[164:165], v244 offset:53248\n"
    "v_accvgpr_read_b32 v5, a241\n"
    "v_mfma_f32_16x16x32_bf16 v[24:27], a[168:171], v[148:151], v[24:27]\n"
    "s_waitcnt lgkmcnt(6)\n"
    "ds_read_b64_tr_b16 a[168:169], v245 offset:20480\n"
    "ds_read_b64_tr_b16 a[170:171], v245 offset:22528\n"
    "v_mfma_f32_16x16x32_bf16 v[44:47], a[176:179], v[212:215], v[44:47]\n"
    "ds_read_b64_tr_b16 a[166:167], v244 offset:55296\n"
    "ds_read_b64_tr_b16 a[172:173], v245 offset:53248\n"
    "v_mfma_f32_16x16x32_bf16 v[44:47], a[180:183], v[216:219], v[44:47]\n"
    "ds_read_b64_tr_b16 a[174:175], v245 offset:55296\n"
    "v_accvgpr_read_b32 v225, a243\n"
    "v_mfma_f32_16x16x32_bf16 v[40:43], a[180:183], v[176:179], v[40:43]\n"
    "v_cvt_pk_bf16_f32 v4, v4, v5\n"
    "v_accvgpr_read_b32 v5, a242\n"
    "v_mfma_f32_16x16x32_bf16 v[40:43], a[176:179], v[172:175], v[40:43]\n"
    "s_waitcnt lgkmcnt(8)\n"
    "ds_read_b64_tr_b16 a[176:177], v246 offset:20480\n"
    "ds_read_b64_tr_b16 a[178:179], v246 offset:22528\n"
    "v_mfma_f32_16x16x32_bf16 v[40:43], a[184:187], v[140:143], v[40:43]\n"
    "buffer_load_dwordx4 a[24:27], v250, s[8:11], s87 offen offset:2048 sc1\n"
    "ds_read_b64_tr_b16 a[180:181], v246 offset:53248\n"
    "v_mfma_f32_16x16x32_bf16 v[40:43], a[188:191], v[144:147], v[40:43]\n"
    "ds_read_b64_tr_b16 a[182:183], v246 offset:55296\n"
    "v_cvt_pk_bf16_f32 v5, v5, v225\n"
    "v_mfma_f32_16x16x32_bf16 v[44:47], a[188:191], v[152:155], v[44:47]\n"
    "buffer_store_dwordx4 v[4:7], v250, s[8:11], s80 offen offset:0 sc1\n"
    "v_mfma_f32_16x16x32_bf16 v[44:47], a[184:187], v[148:151], v[44:47]\n"
    "s_waitcnt lgkmcnt(6)\n"
    "ds_read_b64_tr_b16 a[184:185], v247 offset:20480\n"
    "v_cvt_pk_bf16_f32 v24, v24, v25\n"
    "v_mfma_f32_16x16x32_bf16 v[64:67], a[160:163], v[148:151], v[64:67]\n"
    "ds_read_b64_tr_b16 a[186:187], v247 offset:22528\n"
    "v_mfma_f32_16x16x32_bf16 v[64:67], a[164:167], v[152:155], v[64:67]\n"
    "s_waitcnt lgkmcnt(6)\n"
    "ds_read_b64_tr_b16 a[188:189], v247 offset:53248\n"
    "ds_read_b64_tr_b16 a[190:191], v247 offset:55296\n"
    "v_mfma_f32_16x16x32_bf16 v[84:87], a[172:175], v[152:155], v[84:87]\n"
    "ds_read_b128 a[48:51], v240 offset:0\n"
    "v_cvt_pk_bf16_f32 v25, v26, v27\n"
    "v_mfma_f32_16x16x32_bf16 v[84:87], a[168:171], v[148:151], v[84:87]\n"
    "s_waitcnt lgkmcnt(5)\n"
    "v_cvt_pk_bf16_f32 v26, v44, v45\n"
    "v_cvt_pk_bf16_f32 v27, v46, v47\n"
    "v_mfma_f32_16x16x32_bf16 v[104:107], a[176:179], v[148:151], v[104:107]\n"
    "buffer_store_dwordx4 v[24:27], v250, s[8:11], s80 offen offset:1024 sc1\n"
    "v_mfma_f32_16x16x32_bf16 v[104:107], a[180:183], v[152:155], v[104:107]\n"
    "s_waitcnt lgkmcnt(1)\n"
    "ds_read_b128 a[52:55], v241 offset:0\n"
    "v_cvt_pk_bf16_f32 v64, v64, v65\n"
    "v_mfma_f32_16x16x32_bf16 v[124:127], a[188:191], v[152:155], v[124:127]\n"
    "buffer_load_dwordx4 a[28:31], v250, s[8:11], s88 offen offset:2048 sc1\n"
    "v_mfma_f32_16x16x32_bf16 v[124:127], a[184:187], v[148:151], v[124:127]\n"
    "ds_read_b128 a[56:59], v240 offset:32768\n"
    "v_mfma_f32_16x16x32_bf16 v[60:63], a[164:167], v[144:147], v[60:63]\n"
    "ds_read_b128 a[60:63], v241 offset:32768\n"
    "v_cvt_pk_bf16_f32 v65, v66, v67\n"
    "v_mfma_f32_16x16x32_bf16 v[60:63], a[160:163], v[140:143], v[60:63]\n"
    "v_cvt_pk_bf16_f32 v66, v84, v85\n"
    "v_cvt_pk_bf16_f32 v67, v86, v87\n"
    "v_mfma_f32_16x16x32_bf16 v[80:83], a[168:171], v[140:143], v[80:83]\n"
    "buffer_store_dwordx4 v[64:67], v250, s[8:11], s80 offen offset:2048 sc1\n"
    "v_mfma_f32_16x16x32_bf16 v[80:83], a[172:175], v[144:147], v[80:83]\n"
    "v_cvt_pk_bf16_f32 v104, v104, v105\n"
    "v_cvt_pk_bf16_f32 v105, v106, v107\n"
    "v_mfma_f32_16x16x32_bf16 v[100:103], a[180:183], v[144:147], v[100:103]\n"
    "s_mov_b32 s45, 0x71800000\n"
    "v_cvt_pk_bf16_f32 v106, v124, v125\n"
    "v_cvt_pk_bf16_f32 v107, v126, v127\n"
    "v_mfma_f32_16x16x32_bf16 v[100:103], a[176:179], v[140:143], v[100:103]\n"
    "buffer_store_dwordx4 v[104:107], v250, s[8:11], s80 offen offset:3072 sc1\n"
    "v_mfma_f32_16x16x32_bf16 v[120:123], a[184:187], v[140:143], v[120:123]\n"
    "buffer_store_dword v127, v231, s[8:11], s86 offen offset:0 sc1\n"
    "v_mfma_f32_16x16x32_bf16 v[120:123], a[188:191], v[144:147], v[120:123]\n"
    "s_waitcnt lgkmcnt(2)\n"
    "buffer_load_dwordx4 a[32:35], v250, s[8:11], s89 offen offset:2048 sc1\n"
    "v_mfma_f32_16x16x32_bf16 v[156:159], a[216:219], a[48:51], 0\n"
    "ds_read_b128 a[160:163], v242 offset:0\n"
    "v_mfma_f32_16x16x32_bf16 v[160:163], a[220:223], a[48:51], 0\n"
    "ds_read_b128 a[164:167], v243 offset:0\n"
    "v_mfma_f32_16x16x32_bf16 v[164:167], a[224:227], a[48:51], 0\n"
    "ds_read_b128 a[168:171], v242 offset:32768\n"
    "v_mfma_f32_16x16x32_bf16 v[168:171], a[228:231], a[48:51], 0\n"
    "ds_read_b128 a[172:175], v243 offset:32768\n"
    "v_add_f32_e32 v225, v123, v127\n"
    "v_mfma_f32_16x16x32_bf16 v[156:159], a[232:235], a[52:55], v[156:159]\n"
    "ds_read_b128 a[176:179], v240 offset:2048\n"
    "v_mfma_f32_16x16x32_bf16 v[160:163], v[192:195], a[52:55], v[160:163]\n"
    "ds_read_b128 a[180:183], v241 offset:2048\n"
    "v_min3_f32 v129, v129, v123, v127\n"
    "v_mfma_f32_16x16x32_bf16 v[164:167], a[64:67], a[52:55], v[164:167]\n"
    "ds_read_b128 a[184:187], v240 offset:34816\n"
    "v_add_f32_e32 v225, v225, v128\n"
    "v_mfma_f32_16x16x32_bf16 v[168:171], a[68:71], a[52:55], v[168:171]\n"
    "s_waitcnt lgkmcnt(7)\n"
    "ds_read_b128 a[188:191], v241 offset:34816\n"
    "v_mov_b32_e32 v227, v123\n"
    "v_mfma_f32_16x16x32_bf16 v[156:159], a[72:75], a[56:59], v[156:159]\n"
    "buffer_load_dwordx4 a[36:39], v250, s[8:11], s87 offen offset:3072 sc1\n"
    "ds_read_b128 a[48:51], v242 offset:2048\n"
    "v_mfma_f32_16x16x32_bf16 v[160:163], a[76:79], a[56:59], v[160:163]\n"
    "v_cmp_nlt_f32_e32 vcc, 0x12800000, v129\n"
    "v_cmp_nlt_f32_e64 s[28:29], v225, s45\n"
    "v_mfma_f32_16x16x32_bf16 v[164:167], a[80:83], a[56:59], v[164:167]\n"
    "v_mfma_f32_16x16x32_bf16 v[168:171], a[84:87], a[56:59], v[168:171]\n"
    "s_or_b32 s45, s29, vcc_hi\n"
    "v_mfma_f32_16x16x32_bf16 v[156:159], a[88:91], a[60:63], v[156:159]\n"
    "s_lshr_b32 s45, s45, 16\n"
    "s_cselect_b32 s45, s51, 0\n"
    "ds_read_b128 a[52:55], v243 offset:2048\n"
    "v_mfma_f32_16x16x32_bf16 v[160:163], a[92:95], a[60:63], v[160:163]\n"
    "s_andn2_b32 s45, s45, s49\n"
    "v_mfma_f32_16x16x32_bf16 v[164:167], a[96:99], a[60:63], v[164:167]\n"
    "s_add_u32 s45, s45, s39\n"
    "v_mfma_f32_16x16x32_bf16 v[168:171], a[100:103], a[60:63], v[168:171]\n"
    "s_waitcnt lgkmcnt(8)\n"
    "v_mov_b32_e32 v254, s45\n"
    "v_mfma_f32_16x16x32_bf16 v[156:159], a[104:107], a[160:163], v[156:159]\n"
    "s_lshl1_add_u32 s55, s50, s51\n"
    "ds_read_b128 a[56:59], v242 offset:34816\n"
    "v_and_b32_e32 v251, 0x7ff, v252\n"
    "v_mfma_f32_16x16x32_bf16 v[160:163], a[108:111], a[160:163], v[160:163]\n"
    "buffer_load_dwordx4 a[40:43], v250, s[8:11], s88 offen offset:3072 sc1\n"
    "v_mfma_f32_16x16x32_bf16 v[164:167], a[112:115], a[160:163], v[164:167]\n"
    "s_lshl_b32 s55, s55, 12\n"
    "v_mfma_f32_16x16x32_bf16 v[168:171], a[116:119], a[160:163], v[168:171]\n"
    "s_xor_b32 s31, s93, 2\n"
    "v_mfma_f32_16x16x32_bf16 v[156:159], a[120:123], a[164:167], v[156:159]\n"
    "s_lshl_b32 s31, s31, 8\n"
    "ds_read_b128 a[60:63], v243 offset:34816\n"
    "v_mfma_f32_16x16x32_bf16 v[160:163], a[124:127], a[164:167], v[160:163]\n"
    "s_add_u32 s31, s31, 0x6000\n"
    "v_add_u32_e32 v251, s63, v251\n"
    "v_mfma_f32_16x16x32_bf16 v[164:167], a[128:131], a[164:167], v[164:167]\n"
    "s_mul_i32 s45, s49, 252\n"
    "v_mfma_f32_16x16x32_bf16 v[168:171], a[132:135], a[164:167], v[168:171]\n"
    "s_waitcnt lgkmcnt(8)\n"
    "v_mfma_f32_16x16x32_bf16 v[156:159], a[136:139], a[168:171], v[156:159]\n"
    "s_add_u32 s45, s45, s34\n"
    "ds_read_b128 a[160:163], v240 offset:4096\n"
    "v_mfma_f32_16x16x32_bf16 v[160:163], a[140:143], a[168:171], v[160:163]\n"
    "s_mul_i32 s45, s45, 101376\n"
    "v_mfma_f32_16x16x32_bf16 v[164:167], a[144:147], a[168:171], v[164:167]\n"
    "buffer_load_dwordx4 a[44:47], v250, s[8:11], s89 offen offset:3072 sc1\n"
    "v_mfma_f32_16x16x32_bf16 v[168:171], a[148:151], a[168:171], v[168:171]\n"
    "s_add_u32 s45, s45, 16654336\n"
    "v_mfma_f32_16x16x32_bf16 v[156:159], a[152:155], a[172:175], v[156:159]\n"
    "s_lshl_b32 s47, s93, 11\n"
    "ds_read_b128 a[164:167], v241 offset:4096\n"
    "v_mfma_f32_16x16x32_bf16 v[160:163], a[156:159], a[172:175], v[160:163]\n"
    "s_add_u32 s54, s47, s45\n"
    "v_mfma_f32_16x16x32_bf16 v[164:167], v[180:183], a[172:175], v[164:167]\n"
    "s_add_u32 s45, s45, 32768\n"
    "v_mfma_f32_16x16x32_bf16 v[168:171], v[184:187], a[172:175], v[168:171]\n"
    "s_waitcnt lgkmcnt(8)\n"
    "v_mfma_f32_16x16x32_bf16 v[196:199], a[216:219], a[176:179], 0\n"
    "s_cmp_lt_u32 s32, 2\n"
    "s_cselect_b32 s54, s54, 2.0\n"
    "s_cselect_b32 s45, s45, 2.0\n"
    "ds_read_b128 a[168:171], v240 offset:36864\n"
    "v_mfma_f32_16x16x32_bf16 v[200:203], a[220:223], a[176:179], 0\n"
    "s_add_u32 s80, s35, s46\n"
    "v_xor_b32_e32 v249, 64, v251\n"
    "v_mfma_f32_16x16x32_bf16 v[204:207], a[224:227], a[176:179], 0\n"
    "s_add_u32 s82, s36, s46\n"
    "v_mul_f32_e32 v156, v156, v228\n"
    "v_mfma_f32_16x16x32_bf16 v[208:211], a[228:231], a[176:179], 0\n"
    "buffer_load_dword v220, v220, s[8:11], s60 offen offset:192 sc1\n"
    "v_mfma_f32_16x16x32_bf16 v[196:199], a[232:235], a[180:183], v[196:199]\n"
    "s_add_u32 s28, s37, s46\n"
    "ds_read_b128 a[172:175], v241 offset:36864\n"
    "v_mul_f32_e32 v157, v157, v228\n"
    "v_mfma_f32_16x16x32_bf16 v[200:203], v[192:195], a[180:183], v[200:203]\n"
    "v_add_u32_e32 v244, s55, v250\n"
    "v_mul_f32_e32 v158, v158, v228\n"
    "v_mfma_f32_16x16x32_bf16 v[204:207], a[64:67], a[180:183], v[204:207]\n"
    "v_mul_f32_e32 v159, v159, v228\n"
    "v_mfma_f32_16x16x32_bf16 v[208:211], a[68:71], a[180:183], v[208:211]\n"
    "s_waitcnt lgkmcnt(8)\n"
    "v_mul_f32_e32 v160, v160, v228\n"
    "v_mfma_f32_16x16x32_bf16 v[196:199], a[72:75], a[184:187], v[196:199]\n"
    "ds_read_b128 a[176:179], v242 offset:4096\n"
    "v_mfma_f32_16x16x32_bf16 v[200:203], a[76:79], a[184:187], v[200:203]\n"
    "v_mul_f32_e32 v161, v161, v228\n"
    "v_mfma_f32_16x16x32_bf16 v[204:207], a[80:83], a[184:187], v[204:207]\n"
    "s_waitcnt vmcnt(5)\n"
    "buffer_store_dword v254, off, s[8:11], s90 offset:0 sc1\n"
    "v_mfma_f32_16x16x32_bf16 v[208:211], a[84:87], a[184:187], v[208:211]\n"
    ".long 0xbedf00ff\n"
    ".Lat_w13:\n"
    "s_setpc_b64 s[2:3]\n"
    "v_lshrrev_b32_e32 v247, 2, v250\n"
    "v_mul_f32_e32 v162, v162, v228\n"
    "v_mfma_f32_16x16x32_bf16 v[196:199], a[88:91], a[188:191], v[196:199]\n"
    "s_add_u32 s62, s58, 16384\n"
    "ds_read_b128 a[180:183], v243 offset:4096\n"
    "v_mfma_f32_16x16x32_bf16 v[200:203], a[92:95], a[188:191], v[200:203]\n"
    "v_add_u32_e32 v247, s31, v247\n"
    "v_mul_f32_e32 v163, v163, v228\n"
    "v_mfma_f32_16x16x32_bf16 v[204:207], a[96:99], a[188:191], v[204:207]\n"
    "v_add_u32_e32 v246, s47, v250\n"
    "v_and_b32_e32 v245, 0x603f, v247\n"
    "v_mfma_f32_16x16x32_bf16 v[208:211], a[100:103], a[188:191], v[208:211]\n"
    "s_waitcnt lgkmcnt(8)\n"
    "v_bfe_u32 v226, v250, 2, 4\n"
    "v_mfma_f32_16x16x32_bf16 v[196:199], a[104:107], a[48:51], v[196:199]\n"
    "ds_read_b128 a[184:187], v242 offset:36864\n"
    "v_bfe_u32 v225, v250, 6, 2\n"
    "v_mfma_f32_16x16x32_bf16 v[200:203], a[108:111], a[48:51], v[200:203]\n"
    "v_lshl_or_b32 v226, v225, 7, v226\n"
    "v_mul_f32_e32 v164, v164, v228\n"
    "v_mfma_f32_16x16x32_bf16 v[204:207], a[112:115], a[48:51], v[204:207]\n"
    "s_waitcnt vmcnt(15)\n"
    "v_and_b32_e32 v225, 0x60ff, v247\n"
    "v_mul_f32_e32 v165, v165, v228\n"
    "v_mfma_f32_16x16x32_bf16 v[208:211], a[116:119], a[48:51], v[208:211]\n"
    "buffer_load_dwordx4 a[196:199], v250, s[8:11], s62 offen offset:3072 sc1\n"
    "v_mfma_f32_16x16x32_bf16 v[196:199], a[120:123], a[52:55], v[196:199]\n"
    "s_add_u32 s62, s58, 20480\n"
    "ds_read_b128 a[188:191], v243 offset:36864\n"
    "v_mfma_f32_16x16x32_bf16 v[200:203], a[124:127], a[52:55], v[200:203]\n"
    "v_mul_f32_e32 v166, v166, v228\n"
    "v_mfma_f32_16x16x32_bf16 v[204:207], a[128:131], a[52:55], v[204:207]\n"
    "v_mfma_f32_16x16x32_bf16 v[208:211], a[132:135], a[52:55], v[208:211]\n"
    "s_waitcnt lgkmcnt(8)\n"
    "ds_read_b128 a[48:51], v251 offset:6144\n"
    "v_mfma_f32_16x16x32_bf16 v[196:199], a[136:139], a[56:59], v[196:199]\n"
    "buffer_load_dwordx4 a[200:203], v250, s[8:11], s62 offen offset:0 sc1\n"
    "v_mfma_f32_16x16x32_bf16 v[200:203], a[140:143], a[56:59], v[200:203]\n"
    "v_mfma_f32_16x16x32_bf16 v[204:207], a[144:147], a[56:59], v[204:207]\n"
    "v_mul_f32_e32 v167, v167, v228\n"
    "v_mfma_f32_16x16x32_bf16 v[208:211], a[148:151], a[56:59], v[208:211]\n"
    "ds_read_b128 a[52:55], v249 offset:6144\n"
    "v_mfma_f32_16x16x32_bf16 v[196:199], a[152:155], a[60:63], v[196:199]\n"
    "buffer_load_dwordx4 a[204:207], v250, s[8:11], s62 offen offset:1024 sc1\n"
    "v_mfma_f32_16x16x32_bf16 v[200:203], a[156:159], a[60:63], v[200:203]\n"
    "v_mul_f32_e32 v168, v168, v228\n"
    "v_mfma_f32_16x16x32_bf16 v[204:207], v[180:183], a[60:63], v[204:207]\n"
    "v_mul_f32_e32 v169, v169, v228\n"
    "v_mul_f32_e32 v170, v170, v228\n"
    "v_mfma_f32_16x16x32_bf16 v[208:211], v[184:187], a[60:63], v[208:211]\n"
    "s_waitcnt vmcnt(4)\n"
    "v_mul_f32_e32 v171, v171, v228\n"
    "v_mfma_f32_16x16x32_bf16 a[236:239], v[234:237], a[0:3], a[236:239]\n"
    "buffer_load_dwordx4 a[208:211], v250, s[8:11], s62 offen offset:2048 sc1\n"
    "v_mfma_f32_16x16x32_bf16 v[0:3], v[232:235], a[0:3], v[0:3]\n"
    "v_mfma_f32_16x16x32_bf16 a[236:239], v[234:237], a[4:7], a[236:239]\n"
    "v_mfma_f32_16x16x32_bf16 v[0:3], v[232:235], a[4:7], v[0:3]\n"
    "v_cmp_le_u32_e32 vcc, 0x300, v250\n"
    "v_mfma_f32_16x16x32_bf16 a[236:239], v[234:237], a[8:11], a[236:239]\n"
    "buffer_load_dwordx4 a[212:215], v250, s[8:11], s62 offen offset:3072 sc1\n"
    "v_mfma_f32_16x16x32_bf16 v[0:3], v[232:235], a[8:11], v[0:3]\n"
    "s_waitcnt lgkmcnt(0)\n"
    "v_cndmask_b32_e32 v227, v220, v227, vcc\n"
    "v_mfma_f32_16x16x32_bf16 v[20:23], v[234:237], a[12:15], v[20:23]\n"
    "s_barrier\n"
    "v_mfma_f32_16x16x32_bf16 v[40:43], v[232:235], a[12:15], v[40:43]\n"
    "s_add_u32 s62, s58, 24576\n"
    "v_accvgpr_read_b32 v64, a236\n"
    "v_accvgpr_read_b32 v65, a237\n"
    "v_mfma_f32_16x16x32_bf16 v[20:23], v[234:237], a[16:19], v[20:23]\n"
    "buffer_load_dwordx4 v[108:111], v250, s[8:11], s62 offen offset:0 sc1\n"
    "v_mfma_f32_16x16x32_bf16 v[40:43], v[232:235], a[16:19], v[40:43]\n"
    "v_accvgpr_read_b32 v66, a238\n"
    "v_accvgpr_read_b32 v67, a239\n"
    "v_mfma_f32_16x16x32_bf16 v[20:23], v[234:237], a[20:23], v[20:23]\n"
    "ds_write_b32 v247, v227 offset:0\n"
    "v_mfma_f32_16x16x32_bf16 v[40:43], v[232:235], a[20:23], v[40:43]\n"
    "v_cvt_pk_bf16_f32 v64, v64, v65\n"
    "v_mfma_f32_16x16x32_bf16 v[60:63], v[234:237], a[24:27], v[60:63]\n"
    "buffer_load_dwordx4 v[112:115], v250, s[8:11], s62 offen offset:1024 sc1\n"
    "v_mfma_f32_16x16x32_bf16 v[80:83], v[232:235], a[24:27], v[80:83]\n"
    "v_cvt_pk_bf16_f32 v65, v66, v67\n"
    "v_cvt_pk_bf16_f32 v66, v0, v1\n"
    "v_mfma_f32_16x16x32_bf16 v[60:63], v[234:237], a[28:31], v[60:63]\n"
    "v_cvt_pk_bf16_f32 v67, v2, v3\n"
    "v_mfma_f32_16x16x32_bf16 v[80:83], v[232:235], a[28:31], v[80:83]\n"
    "ds_write_b128 v244, v[64:67] offset:0\n"
    "v_mfma_f32_16x16x32_bf16 v[60:63], v[234:237], a[32:35], v[60:63]\n"
    "buffer_load_dwordx4 v[132:135], v250, s[8:11], s62 offen offset:2048 sc1\n"
    "v_mfma_f32_16x16x32_bf16 v[80:83], v[232:235], a[32:35], v[80:83]\n"
    "v_cvt_pk_bf16_f32 v20, v20, v21\n"
    "v_mfma_f32_16x16x32_bf16 v[100:103], v[234:237], a[36:39], v[100:103]\n"
    "v_cvt_pk_bf16_f32 v21, v22, v23\n"
    "v_cvt_pk_bf16_f32 v22, v40, v41\n"
    "v_mfma_f32_16x16x32_bf16 v[120:123], v[232:235], a[36:39], v[120:123]\n"
    "v_cvt_pk_bf16_f32 v23, v42, v43\n"
    "v_mfma_f32_16x16x32_bf16 v[100:103], v[234:237], a[40:43], v[100:103]\n"
    "buffer_load_dwordx4 v[116:119], v250, s[8:11], s62 offen offset:3072 sc1\n"
    "v_mfma_f32_16x16x32_bf16 v[120:123], v[232:235], a[40:43], v[120:123]\n"
    "s_add_u32 s62, s58, 28672\n"
    "ds_write_b128 v244, v[20:23] offset:1024\n"
    "v_mfma_f32_16x16x32_bf16 v[100:103], v[234:237], a[44:47], v[100:103]\n"
    "v_cvt_pk_bf16_f32 v60, v60, v61\n"
    "v_cvt_pk_bf16_f32 v61, v62, v63\n"
    "v_mfma_f32_16x16x32_bf16 v[120:123], v[232:235], a[44:47], v[120:123]\n"
    "v_cvt_pk_bf16_f32 v62, v80, v81\n"
    "v_cvt_pk_bf16_f32 v63, v82, v83\n"
    "v_mfma_f32_16x16x32_bf16 v[172:175], a[216:219], a[160:163], 0\n"
    "buffer_load_dword v253, v226, s[8:11], s61 offen offset:0 sc1\n"
    "v_mfma_f32_16x16x32_bf16 v[176:179], a[220:223], a[160:163], 0\n"
    "ds_write_b128 v244, v[60:63] offset:2048\n"
    "v_mfma_f32_16x16x32_bf16 v[212:215], a[224:227], a[160:163], 0\n"
    "buffer_load_dwordx4 v[136:139], v250, s[8:11], s62 offen offset:0 sc1\n"
    "v_mfma_f32_16x16x32_bf16 v[216:219], a[228:231], a[160:163], 0\n"
    "v_cvt_pk_bf16_f32 v100, v100, v101\n"
    "v_cvt_pk_bf16_f32 v101, v102, v103\n"
    "v_mfma_f32_16x16x32_bf16 v[172:175], a[232:235], a[164:167], v[172:175]\n"
    "v_cvt_pk_bf16_f32 v102, v120, v121\n"
    "v_cvt_pk_bf16_f32 v103, v122, v123\n"
    "v_mfma_f32_16x16x32_bf16 v[176:179], v[192:195], a[164:167], v[176:179]\n"
    "ds_write_b128 v244, v[100:103] offset:3072\n"
    "v_mfma_f32_16x16x32_bf16 v[212:215], a[64:67], a[164:167], v[212:215]\n"
    "buffer_load_dwordx4 a[240:243], v250, s[8:11], s62 offen offset:1024 sc1\n"
    "v_mfma_f32_16x16x32_bf16 v[216:219], a[68:71], a[164:167], v[216:219]\n"
    "v_mfma_f32_16x16x32_bf16 v[172:175], a[72:75], a[168:171], v[172:175]\n"
    "v_mfma_f32_16x16x32_bf16 v[176:179], a[76:79], a[168:171], v[176:179]\n"
    "s_waitcnt lgkmcnt(0)\n"
    "v_mfma_f32_16x16x32_bf16 v[212:215], a[80:83], a[168:171], v[212:215]\n"
    "s_barrier\n"
    "v_mfma_f32_16x16x32_bf16 v[216:219], a[84:87], a[168:171], v[216:219]\n"
    "buffer_load_dwordx4 v[4:7], v250, s[8:11], s62 offen offset:2048 sc1\n"
    "v_mfma_f32_16x16x32_bf16 v[172:175], a[88:91], a[172:175], v[172:175]\n"
    "ds_read_b128 a[0:3], v250 offset:0\n"
    "v_mfma_f32_16x16x32_bf16 v[176:179], a[92:95], a[172:175], v[176:179]\n"
    "ds_read_b128 a[4:7], v250 offset:1024\n"
    "v_mfma_f32_16x16x32_bf16 v[212:215], a[96:99], a[172:175], v[212:215]\n"
    "v_mfma_f32_16x16x32_bf16 v[216:219], a[100:103], a[172:175], v[216:219]\n"
    "ds_read_b128 a[8:11], v250 offset:2048\n"
    "v_mfma_f32_16x16x32_bf16 v[172:175], a[104:107], a[176:179], v[172:175]\n"
    "buffer_load_dwordx4 v[24:27], v250, s[8:11], s62 offen offset:3072 sc1\n"
    "v_mfma_f32_16x16x32_bf16 v[176:179], a[108:111], a[176:179], v[176:179]\n"
    "ds_read_b128 a[12:15], v250 offset:3072\n"
    "v_mfma_f32_16x16x32_bf16 v[212:215], a[112:115], a[176:179], v[212:215]\n"
    "ds_read_b128 a[16:19], v250 offset:4096\n"
    "v_mfma_f32_16x16x32_bf16 v[216:219], a[116:119], a[176:179], v[216:219]\n"
    "ds_read_b128 a[20:23], v250 offset:5120\n"
    "v_mul_f32_e32 v196, v196, v229\n"
    "v_mfma_f32_16x16x32_bf16 v[172:175], a[120:123], a[180:183], v[172:175]\n"
    "ds_read_b128 a[24:27], v250 offset:6144\n"
    "v_mfma_f32_16x16x32_bf16 v[176:179], a[124:127], a[180:183], v[176:179]\n"
    "v_mul_f32_e32 v197, v197, v229\n"
    "v_mfma_f32_16x16x32_bf16 v[212:215], a[128:131], a[180:183], v[212:215]\n"
    "v_mfma_f32_16x16x32_bf16 v[216:219], a[132:135], a[180:183], v[216:219]\n"
    "ds_read_b128 a[28:31], v250 offset:7168\n"
    "v_mfma_f32_16x16x32_bf16 v[172:175], a[136:139], a[184:187], v[172:175]\n"
    "ds_read2_b32 v[220:221], v245 offset0:0 offset1:16\n"
    "v_mfma_f32_16x16x32_bf16 v[176:179], a[140:143], a[184:187], v[176:179]\n"
    "ds_read2_b32 v[222:223], v245 offset0:32 offset1:48\n"
    "v_mul_f32_e32 v198, v198, v229\n"
    "v_mfma_f32_16x16x32_bf16 v[212:215], a[144:147], a[184:187], v[212:215]\n"
    "ds_read2_b32 v[80:81], v245 offset0:64 offset1:80\n"
    "v_mfma_f32_16x16x32_bf16 v[216:219], a[148:151], a[184:187], v[216:219]\n"
    "ds_read2_b32 v[82:83], v245 offset0:96 offset1:112\n"
    "v_mfma_f32_16x16x32_bf16 v[172:175], a[152:155], a[188:191], v[172:175]\n"
    "v_mul_f32_e32 v199, v199, v229\n"
    "v_mfma_f32_16x16x32_bf16 v[176:179], a[156:159], a[188:191], v[176:179]\n"
    "v_mfma_f32_16x16x32_bf16 v[212:215], v[180:183], a[188:191], v[212:215]\n"
    "v_mul_f32_e32 v200, v200, v229\n"
    "v_mfma_f32_16x16x32_bf16 v[216:219], v[184:187], a[188:191], v[216:219]\n"
    "v_mul_f32_e32 v201, v201, v229\n"
    "v_mul_f32_e32 v202, v202, v229\n"
    "v_mfma_f32_16x16x32_bf16 v[156:159], v[234:237], a[48:51], v[156:159]\n"
    "v_mul_f32_e32 v203, v203, v229\n"
    "v_mul_f32_e32 v204, v204, v229\n"
    "v_mfma_f32_16x16x32_bf16 v[160:163], v[232:235], a[48:51], v[160:163]\n"
    "ds_read_b128 a[64:67], v250 offset:8192\n"
    "v_mfma_f32_16x16x32_bf16 v[164:167], v[234:237], a[52:55], v[164:167]\n"
    "ds_read_b128 a[68:71], v250 offset:9216\n"
    "v_mul_f32_e32 v205, v205, v229\n"
    "v_mfma_f32_16x16x32_bf16 v[168:171], v[232:235], a[52:55], v[168:171]\n"
    "s_waitcnt lgkmcnt(5)\n"
    "ds_read_b128 a[72:75], v250 offset:10240\n"
    "v_mul_f32_e32 v206, v206, v229\n"
    "v_mfma_f32_16x16x32_bf16 v[140:143], a[244:247], a[0:3], 0\n"
    "ds_read_b128 a[76:79], v250 offset:11264\n"
    "v_add_f32_e32 v220, v220, v221\n"
    "v_mfma_f32_16x16x32_bf16 v[144:147], v[8:11], a[0:3], 0\n"
    "ds_read_b128 a[80:83], v250 offset:12288\n"
    "v_mul_f32_e32 v207, v207, v229\n"
    "v_mfma_f32_16x16x32_bf16 v[148:151], a[248:251], a[0:3], 0\n"
    "v_mfma_f32_16x16x32_bf16 v[152:155], v[12:15], a[0:3], 0\n"
    "s_waitcnt vmcnt(4)\n"
    "v_subrev_u32_e32 v253, s39, v253\n"
    "v_cmp_lt_u32_e32 vcc, 1, v253\n"
    "v_mfma_f32_16x16x32_bf16 v[140:143], a[252:255], a[4:7], v[140:143]\n"
    "v_mul_f32_e32 v208, v208, v229\n"
    "v_mul_f32_e32 v209, v209, v229\n"
    "v_mfma_f32_16x16x32_bf16 v[144:147], v[16:19], a[4:7], v[144:147]\n"
    "ds_read_b128 a[84:87], v250 offset:13312\n"
    "v_mfma_f32_16x16x32_bf16 v[148:151], v[28:31], a[4:7], v[148:151]\n"
    "s_cbranch_vccnz .Lat_ppoll\n"
    ".Lat_pok:\n"
    "v_cmp_eq_u32_e32 vcc, 1, v253\n"
    "v_mfma_f32_16x16x32_bf16 v[152:155], v[48:51], a[4:7], v[152:155]\n"
    "s_waitcnt lgkmcnt(8)\n"
    "buffer_load_dwordx4 a[128:131], v250, s[8:11], s54 offen offset:0 sc1\n"
    "v_mfma_f32_16x16x32_bf16 v[140:143], v[32:35], a[8:11], v[140:143]\n"
    "v_mul_f32_e32 v172, v172, v230\n"
    "v_add_f32_e32 v222, v222, v223\n"
    "v_mfma_f32_16x16x32_bf16 v[144:147], v[52:55], a[8:11], v[144:147]\n"
    "s_cbranch_vccnz .Lat_redo\n"
    "v_mfma_f32_16x16x32_bf16 v[148:151], v[36:39], a[8:11], v[148:151]\n"
    "buffer_load_dwordx4 a[132:135], v250, s[8:11], s54 offen offset:1024 sc1\n"
    "v_mfma_f32_16x16x32_bf16 v[152:155], v[56:59], a[8:11], v[152:155]\n"
    "s_add_u32 s54, s54, 8192\n"
    "ds_read_b128 a[88:91], v250 offset:14336\n"
    "v_add_f32_e32 v220, v220, v222\n"
    "v_mfma_f32_16x16x32_bf16 v[140:143], v[68:71], a[12:15], v[140:143]\n"
    "buffer_load_dwordx4 a[136:139], v250, s[8:11], s54 offen offset:0 sc1\n"
    "v_mfma_f32_16x16x32_bf16 v[144:147], v[88:91], a[12:15], v[144:147]\n"
    "ds_read_b128 a[92:95], v250 offset:15360\n"
    "v_mul_f32_e32 v210, v210, v229\n"
    "v_mfma_f32_16x16x32_bf16 v[148:151], v[72:75], a[12:15], v[148:151]\n"
    "s_waitcnt vmcnt(3)\n"
    "buffer_load_dwordx4 a[140:143], v250, s[8:11], s54 offen offset:1024 sc1\n"
    "v_mfma_f32_16x16x32_bf16 v[152:155], v[92:95], a[12:15], v[152:155]\n"
    "s_add_u32 s54, s54, 8192\n"
    "v_mfma_f32_16x16x32_bf16 v[140:143], v[76:79], a[16:19], v[140:143]\n"
    "buffer_load_dwordx4 a[144:147], v250, s[8:11], s54 offen offset:0 sc1\n"
    "v_mfma_f32_16x16x32_bf16 v[144:147], v[96:99], a[16:19], v[144:147]\n"
    "ds_read_b128 a[56:59], v252 offset:30720\n"
    "v_mfma_f32_16x16x32_bf16 v[148:151], a[192:195], a[16:19], v[148:151]\n"
    "buffer_load_dwordx4 a[148:151], v250, s[8:11], s54 offen offset:1024 sc1\n"
    "v_mfma_f32_16x16x32_bf16 v[152:155], a[196:199], a[16:19], v[152:155]\n"
    "s_add_u32 s54, s54, 8192\n"
    "v_rcp_f32_e32 v220, v220\n"
    "v_mfma_f32_16x16x32_bf16 v[140:143], a[200:203], a[20:23], v[140:143]\n"
    "buffer_load_dwordx4 a[152:155], v250, s[8:11], s54 offen offset:0 sc1\n"
    "v_mfma_f32_16x16x32_bf16 v[144:147], a[204:207], a[20:23], v[144:147]\n"
    "v_mul_f32_e32 v211, v211, v229\n"
    "v_mfma_f32_16x16x32_bf16 v[148:151], a[208:211], a[20:23], v[148:151]\n"
    "buffer_load_dwordx4 a[156:159], v250, s[8:11], s54 offen offset:1024 sc1\n"
    "v_mfma_f32_16x16x32_bf16 v[152:155], a[212:215], a[20:23], v[152:155]\n"
    "s_waitcnt lgkmcnt(9)\n"
    "v_mul_f32_e32 v173, v173, v230\n"
    "v_mfma_f32_16x16x32_bf16 v[140:143], v[108:111], a[24:27], v[140:143]\n"
    "buffer_load_dword v227, v248, s[8:11], s45 offen offset:1088 sc1\n"
    "v_mfma_f32_16x16x32_bf16 v[144:147], v[112:115], a[24:27], v[144:147]\n"
    "ds_read_b128 a[60:63], v255 offset:30720\n"
    "v_mfma_f32_16x16x32_bf16 v[148:151], v[132:135], a[24:27], v[148:151]\n"
    "ds_read_b128 a[160:163], v252 offset:4096\n"
    "v_mfma_f32_16x16x32_bf16 v[152:155], v[116:119], a[24:27], v[152:155]\n"
    "ds_read_b128 a[164:167], v255 offset:4096\n"
    "v_mfma_f32_16x16x32_bf16 v[140:143], v[136:139], a[28:31], v[140:143]\n"
    "v_add_f32_e32 v80, v80, v81\n"
    "v_mfma_f32_16x16x32_bf16 v[144:147], a[240:243], a[28:31], v[144:147]\n"
    "v_mfma_f32_16x16x32_bf16 v[148:151], v[4:7], a[28:31], v[148:151]\n"
    "v_add_f32_e32 v82, v82, v83\n"
    "v_add_f32_e32 v80, v80, v82\n"
    "v_mfma_f32_16x16x32_bf16 v[152:155], v[24:27], a[28:31], v[152:155]\n"
    "s_waitcnt lgkmcnt(10)\n"
    "v_rcp_f32_e32 v80, v80\n"
    "v_mfma_f32_16x16x32_bf16 v[180:183], a[244:247], a[64:67], 0\n"
    "v_mul_f32_e32 v174, v174, v230\n"
    "v_mfma_f32_16x16x32_bf16 v[184:187], v[8:11], a[64:67], 0\n"
    "v_mul_f32_e32 v175, v175, v230\n"
    "v_fma_f32 v140, v140, v220, v156\n"
    "v_mfma_f32_16x16x32_bf16 v[188:191], a[248:251], a[64:67], 0\n"
    "v_fma_f32 v141, v141, v220, v157\n"
    "v_fma_f32 v142, v142, v220, v158\n"
    "v_mfma_f32_16x16x32_bf16 v[192:195], v[12:15], a[64:67], 0\n"
    "v_fma_f32 v143, v143, v220, v159\n"
    "v_mfma_f32_16x16x32_bf16 v[180:183], a[252:255], a[68:71], v[180:183]\n"
    "v_fma_f32 v144, v144, v220, v160\n"
    "v_mfma_f32_16x16x32_bf16 v[184:187], v[16:19], a[68:71], v[184:187]\n"
    "v_fma_f32 v145, v145, v220, v161\n"
    "v_mfma_f32_16x16x32_bf16 v[188:191], v[28:31], a[68:71], v[188:191]\n"
    "v_fma_f32 v146, v146, v220, v162\n"
    "v_fma_f32 v147, v147, v220, v163\n"
    "v_mfma_f32_16x16x32_bf16 v[192:195], v[48:51], a[68:71], v[192:195]\n"
    "s_waitcnt lgkmcnt(8)\n"
    "v_cvt_pk_bf16_f32 v140, v140, v141\n"
    "v_cvt_pk_bf16_f32 v141, v142, v143\n"
    "v_mfma_f32_16x16x32_bf16 v[180:183], v[32:35], a[72:75], v[180:183]\n"
    "v_cvt_pk_bf16_f32 v142, v144, v145\n"
    "v_cvt_pk_bf16_f32 v143, v146, v147\n"
    "v_mfma_f32_16x16x32_bf16 v[184:187], v[52:55], a[72:75], v[184:187]\n"
    "v_mul_f32_e32 v176, v176, v230\n"
    "v_mul_f32_e32 v177, v177, v230\n"
    "v_mfma_f32_16x16x32_bf16 v[188:191], v[36:39], a[72:75], v[188:191]\n"
    "buffer_store_dwordx4 v[140:143], v239, s[16:19], s80 offen offset:0 sc0 sc1\n"
    "v_mfma_f32_16x16x32_bf16 v[192:195], v[56:59], a[72:75], v[192:195]\n"
    "v_fma_f32 v148, v148, v220, v164\n"
    "v_mfma_f32_16x16x32_bf16 v[180:183], v[68:71], a[76:79], v[180:183]\n"
    "v_fma_f32 v149, v149, v220, v165\n"
    "v_mfma_f32_16x16x32_bf16 v[184:187], v[88:91], a[76:79], v[184:187]\n"
    "v_fma_f32 v150, v150, v220, v166\n"
    "v_mfma_f32_16x16x32_bf16 v[188:191], v[72:75], a[76:79], v[188:191]\n"
    "v_mul_f32_e32 v178, v178, v230\n"
    "v_mul_f32_e32 v179, v179, v230\n"
    "v_mfma_f32_16x16x32_bf16 v[192:195], v[92:95], a[76:79], v[192:195]\n"
    "s_waitcnt lgkmcnt(6)\n"
    "v_fma_f32 v151, v151, v220, v167\n"
    "v_mfma_f32_16x16x32_bf16 v[180:183], v[76:79], a[80:83], v[180:183]\n"
    "v_fma_f32 v152, v152, v220, v168\n"
    "v_fma_f32 v153, v153, v220, v169\n"
    "v_mfma_f32_16x16x32_bf16 v[184:187], v[96:99], a[80:83], v[184:187]\n"
    "v_fma_f32 v154, v154, v220, v170\n"
    "v_fma_f32 v155, v155, v220, v171\n"
    "v_mfma_f32_16x16x32_bf16 v[188:191], a[192:195], a[80:83], v[188:191]\n"
    "v_cvt_pk_bf16_f32 v148, v148, v149\n"
    "v_cvt_pk_bf16_f32 v149, v150, v151\n"
    "v_mfma_f32_16x16x32_bf16 v[192:195], a[196:199], a[80:83], v[192:195]\n"
    "v_mul_f32_e32 v212, v212, v230\n"
    "v_mfma_f32_16x16x32_bf16 v[180:183], a[200:203], a[84:87], v[180:183]\n"
    "v_cvt_pk_bf16_f32 v150, v152, v153\n"
    "v_cvt_pk_bf16_f32 v151, v154, v155\n"
    "v_mfma_f32_16x16x32_bf16 v[184:187], a[204:207], a[84:87], v[184:187]\n"
    "v_mfma_f32_16x16x32_bf16 v[188:191], a[208:211], a[84:87], v[188:191]\n"
    "buffer_store_dwordx4 v[148:151], v239, s[16:19], s80 offen offset:64 sc0 sc1\n"
    "v_mfma_f32_16x16x32_bf16 v[192:195], a[212:215], a[84:87], v[192:195]\n"
    "s_waitcnt lgkmcnt(4)\n"
    "v_mfma_f32_16x16x32_bf16 v[180:183], v[108:111], a[88:91], v[180:183]\n"
    "v_mfma_f32_16x16x32_bf16 v[184:187], v[112:115], a[88:91], v[184:187]\n"
    "v_mfma_f32_16x16x32_bf16 v[188:191], v[132:135], a[88:91], v[188:191]\n"
    "v_mul_f32_e32 v213, v213, v230\n"
    "v_mfma_f32_16x16x32_bf16 v[192:195], v[116:119], a[88:91], v[192:195]\n"
    "s_waitcnt vmcnt(9)\n"
    "v_mul_f32_e32 v214, v214, v230\n"
    "v_mfma_f32_16x16x32_bf16 v[64:67], v[234:237], a[128:131], 0\n"
    "v_mul_f32_e32 v215, v215, v230\n"
    "v_mfma_f32_16x16x32_bf16 v[84:87], v[232:235], a[128:131], 0\n"
    "v_mfma_f32_16x16x32_bf16 v[104:107], v[234:237], a[132:135], 0\n"
    "v_mul_f32_e32 v216, v216, v230\n"
    "v_mfma_f32_16x16x32_bf16 v[124:127], v[232:235], a[132:135], 0\n"
    "s_waitcnt vmcnt(7)\n"
    "v_mul_f32_e32 v217, v217, v230\n"
    "v_mfma_f32_16x16x32_bf16 v[64:67], v[234:237], a[136:139], v[64:67]\n"
    "v_mul_f32_e32 v218, v218, v230\n"
    "v_mfma_f32_16x16x32_bf16 v[84:87], v[232:235], a[136:139], v[84:87]\n"
    "v_mfma_f32_16x16x32_bf16 v[104:107], v[234:237], a[140:143], v[104:107]\n"
    "v_mfma_f32_16x16x32_bf16 v[124:127], v[232:235], a[140:143], v[124:127]\n"
    "s_waitcnt vmcnt(5)\n"
    "v_mul_f32_e32 v219, v219, v230\n"
    "v_mfma_f32_16x16x32_bf16 v[64:67], v[234:237], a[144:147], v[64:67]\n"
    "v_mfma_f32_16x16x32_bf16 v[84:87], v[232:235], a[144:147], v[84:87]\n"
    "v_mfma_f32_16x16x32_bf16 v[104:107], v[234:237], a[148:151], v[104:107]\n"
    "v_mfma_f32_16x16x32_bf16 v[124:127], v[232:235], a[148:151], v[124:127]\n"
    "s_waitcnt vmcnt(3)\n"
    "v_mfma_f32_16x16x32_bf16 v[64:67], v[234:237], a[152:155], v[64:67]\n"
    "s_waitcnt vmcnt(2)\n"
    "ds_write_b32 v225, v227 offset:512\n"
    "v_mfma_f32_16x16x32_bf16 v[84:87], v[232:235], a[152:155], v[84:87]\n"
    "v_mfma_f32_16x16x32_bf16 v[104:107], v[234:237], a[156:159], v[104:107]\n"
    "v_mfma_f32_16x16x32_bf16 v[124:127], v[232:235], a[156:159], v[124:127]\n"
    "v_cvt_pk_bf16_f32 v64, v64, v65\n"
    "v_cvt_pk_bf16_f32 v65, v66, v67\n"
    "v_mfma_f32_16x16x32_bf16 v[180:183], v[136:139], a[92:95], v[180:183]\n"
    "v_cvt_pk_bf16_f32 v66, v84, v85\n"
    "v_cvt_pk_bf16_f32 v67, v86, v87\n"
    "v_mfma_f32_16x16x32_bf16 v[184:187], a[240:243], a[92:95], v[184:187]\n"
    "ds_write_b128 v246, v[64:67] offset:16384\n"
    "v_mfma_f32_16x16x32_bf16 v[188:191], v[4:7], a[92:95], v[188:191]\n"
    "v_cvt_pk_bf16_f32 v104, v104, v105\n"
    "v_cvt_pk_bf16_f32 v105, v106, v107\n"
    "v_mfma_f32_16x16x32_bf16 v[192:195], v[24:27], a[92:95], v[192:195]\n"
    "s_waitcnt lgkmcnt(4)\n"
    "v_cvt_pk_bf16_f32 v106, v124, v125\n"
    "v_cvt_pk_bf16_f32 v107, v126, v127\n"
    "v_mfma_f32_16x16x32_bf16 v[196:199], v[234:237], a[56:59], v[196:199]\n"
    "ds_write_b128 v246, v[104:107] offset:17408\n"
    "v_mfma_f32_16x16x32_bf16 v[200:203], v[232:235], a[56:59], v[200:203]\n"
    "v_mfma_f32_16x16x32_bf16 v[204:207], v[234:237], a[60:63], v[204:207]\n"
    "s_waitcnt lgkmcnt(0)\n"
    "v_mfma_f32_16x16x32_bf16 v[208:211], v[232:235], a[60:63], v[208:211]\n"
    "s_barrier\n"
    "v_mfma_f32_16x16x32_bf16 v[172:175], v[234:237], a[160:163], v[172:175]\n"
    "ds_read_b128 a[96:99], v250 offset:16384\n"
    "v_mfma_f32_16x16x32_bf16 v[176:179], v[232:235], a[160:163], v[176:179]\n"
    "ds_read_b128 a[100:103], v250 offset:17408\n"
    "v_mfma_f32_16x16x32_bf16 v[212:215], v[234:237], a[164:167], v[212:215]\n"
    "ds_read_b128 a[104:107], v250 offset:18432\n"
    "ds_read_b128 a[108:111], v250 offset:19456\n"
    "v_mfma_f32_16x16x32_bf16 v[216:219], v[232:235], a[164:167], v[216:219]\n"
    "s_waitcnt lgkmcnt(2)\n"
    "ds_read_b128 a[112:115], v250 offset:20480\n"
    "ds_read_b128 a[116:119], v250 offset:21504\n"
    "v_mfma_f32_16x16x32_bf16 v[0:3], a[244:247], a[96:99], 0\n"
    "ds_read_b128 a[120:123], v250 offset:22528\n"
    "ds_read_b128 a[124:127], v250 offset:23552\n"
    "v_mfma_f32_16x16x32_bf16 v[20:23], v[8:11], a[96:99], 0\n"
    "ds_read2_b32 v[220:221], v245 offset0:128 offset1:144\n"
    "ds_read2_b32 v[222:223], v245 offset0:160 offset1:176\n"
    "v_mfma_f32_16x16x32_bf16 v[0:3], a[252:255], a[100:103], v[0:3]\n"
    "v_fma_f32 v180, v180, v80, v196\n"
    "v_fma_f32 v181, v181, v80, v197\n"
    "v_mfma_f32_16x16x32_bf16 v[20:23], v[16:19], a[100:103], v[20:23]\n"
    "s_waitcnt lgkmcnt(6)\n"
    "v_fma_f32 v182, v182, v80, v198\n"
    "v_fma_f32 v183, v183, v80, v199\n"
    "v_mfma_f32_16x16x32_bf16 v[0:3], v[32:35], a[104:107], v[0:3]\n"
    "s_or_b32 s45, s34, s93\n"
    "s_cselect_b32 s45, 2.0, 6101760\n"
    "v_fma_f32 v184, v184, v80, v200\n"
    "v_mfma_f32_16x16x32_bf16 v[20:23], v[52:55], a[104:107], v[20:23]\n"
    "v_fma_f32 v185, v185, v80, v201\n"
    "v_fma_f32 v186, v186, v80, v202\n"
    "v_mfma_f32_16x16x32_bf16 v[0:3], v[68:71], a[108:111], v[0:3]\n"
    "v_fma_f32 v187, v187, v80, v203\n"
    "v_cvt_pk_bf16_f32 v180, v180, v181\n"
    "v_cvt_pk_bf16_f32 v181, v182, v183\n"
    "v_mfma_f32_16x16x32_bf16 v[20:23], v[88:91], a[108:111], v[20:23]\n"
    "s_waitcnt lgkmcnt(4)\n"
    "v_cvt_pk_bf16_f32 v182, v184, v185\n"
    "v_cvt_pk_bf16_f32 v183, v186, v187\n"
    "v_mfma_f32_16x16x32_bf16 v[0:3], v[76:79], a[112:115], v[0:3]\n"
    "buffer_store_dwordx4 v[180:183], v239, s[16:19], s82 offen offset:0 sc0 sc1\n"
    "v_mfma_f32_16x16x32_bf16 v[20:23], v[96:99], a[112:115], v[20:23]\n"
    "v_fma_f32 v188, v188, v80, v204\n"
    "v_fma_f32 v189, v189, v80, v205\n"
    "v_mfma_f32_16x16x32_bf16 v[0:3], a[200:203], a[116:119], v[0:3]\n"
    "v_fma_f32 v190, v190, v80, v206\n"
    "v_fma_f32 v191, v191, v80, v207\n"
    "v_mfma_f32_16x16x32_bf16 v[20:23], a[204:207], a[116:119], v[20:23]\n"
    "s_waitcnt lgkmcnt(0)\n"
    "v_fma_f32 v192, v192, v80, v208\n"
    "v_fma_f32 v193, v193, v80, v209\n"
    "v_mfma_f32_16x16x32_bf16 v[0:3], v[108:111], a[120:123], v[0:3]\n"
    "v_fma_f32 v194, v194, v80, v210\n"
    "v_fma_f32 v195, v195, v80, v211\n"
    "v_mfma_f32_16x16x32_bf16 v[20:23], v[112:115], a[120:123], v[20:23]\n"
    "v_cvt_pk_bf16_f32 v188, v188, v189\n"
    "v_cvt_pk_bf16_f32 v189, v190, v191\n"
    "v_mfma_f32_16x16x32_bf16 v[0:3], v[136:139], a[124:127], v[0:3]\n"
    "v_cvt_pk_bf16_f32 v190, v192, v193\n"
    "v_cvt_pk_bf16_f32 v191, v194, v195\n"
    "v_mfma_f32_16x16x32_bf16 v[20:23], a[240:243], a[124:127], v[20:23]\n"
    "buffer_store_dwordx4 v[188:191], v239, s[16:19], s82 offen offset:64 sc0 sc1\n"
    "v_mfma_f32_16x16x32_bf16 v[40:43], a[248:251], a[96:99], 0\n"
    "v_add_f32_e32 v220, v220, v221\n"
    "v_add_f32_e32 v222, v222, v223\n"
    "v_mfma_f32_16x16x32_bf16 v[60:63], v[12:15], a[96:99], 0\n"
    "v_add_f32_e32 v220, v220, v222\n"
    "v_rcp_f32_e32 v220, v220\n"
    "v_mfma_f32_16x16x32_bf16 v[40:43], v[28:31], a[100:103], v[40:43]\n"
    "v_fma_f32 v0, v0, v220, v172\n"
    "v_mfma_f32_16x16x32_bf16 v[60:63], v[48:51], a[100:103], v[60:63]\n"
    "v_fma_f32 v1, v1, v220, v173\n"
    "v_mfma_f32_16x16x32_bf16 v[40:43], v[36:39], a[104:107], v[40:43]\n"
    "v_fma_f32 v2, v2, v220, v174\n"
    "v_fma_f32 v3, v3, v220, v175\n"
    "v_mfma_f32_16x16x32_bf16 v[60:63], v[56:59], a[104:107], v[60:63]\n"
    "v_fma_f32 v20, v20, v220, v176\n"
    "v_mfma_f32_16x16x32_bf16 v[40:43], v[72:75], a[108:111], v[40:43]\n"
    "v_fma_f32 v21, v21, v220, v177\n"
    "v_mfma_f32_16x16x32_bf16 v[60:63], v[92:95], a[108:111], v[60:63]\n"
    "v_fma_f32 v22, v22, v220, v178\n"
    "v_mfma_f32_16x16x32_bf16 v[40:43], a[192:195], a[112:115], v[40:43]\n"
    "v_fma_f32 v23, v23, v220, v179\n"
    "v_cvt_pk_bf16_f32 v0, v0, v1\n"
    "v_mfma_f32_16x16x32_bf16 v[60:63], a[196:199], a[112:115], v[60:63]\n"
    "v_cvt_pk_bf16_f32 v1, v2, v3\n"
    "v_mfma_f32_16x16x32_bf16 v[40:43], a[208:211], a[116:119], v[40:43]\n"
    "v_cvt_pk_bf16_f32 v2, v20, v21\n"
    "v_cvt_pk_bf16_f32 v3, v22, v23\n"
    "v_mfma_f32_16x16x32_bf16 v[60:63], a[212:215], a[116:119], v[60:63]\n"
    "buffer_store_dwordx4 v[0:3], v239, s[16:19], s28 offen offset:0 sc0 sc1\n"
    "v_mfma_f32_16x16x32_bf16 v[40:43], v[132:135], a[120:123], v[40:43]\n"
    "v_mfma_f32_16x16x32_bf16 v[60:63], v[116:119], a[120:123], v[60:63]\n"
    "v_mfma_f32_16x16x32_bf16 v[40:43], v[4:7], a[124:127], v[40:43]\n"
    "v_mfma_f32_16x16x32_bf16 v[60:63], v[24:27], a[124:127], v[60:63]\n"
    "v_mov_b32_e32 v254, s39\n"
    "s_nop 2\n"
    "v_fma_f32 v40, v40, v220, v212\n"
    "v_fma_f32 v41, v41, v220, v213\n"
    "v_fma_f32 v42, v42, v220, v214\n"
    "v_fma_f32 v43, v43, v220, v215\n"
    "v_fma_f32 v60, v60, v220, v216\n"
    "v_fma_f32 v61, v61, v220, v217\n"
    "v_fma_f32 v62, v62, v220, v218\n"
    "v_fma_f32 v63, v63, v220, v219\n"
    "v_cvt_pk_bf16_f32 v40, v40, v41\n"
    "v_cvt_pk_bf16_f32 v41, v42, v43\n"
    "v_cvt_pk_bf16_f32 v42, v60, v61\n"
    "v_cvt_pk_bf16_f32 v43, v62, v63\n"
    "buffer_store_dwordx4 v[40:43], v239, s[16:19], s28 offen offset:64 sc0 sc1\n"
    "buffer_store_dword v254, off, s[8:11], s45 offset:0 sc1\n"
    "s_endpgm\n"
    ".p2align 6\n"
    ".long 0xbedf00ff\n"
    ".Lat_w14:\n"
    "s_setpc_b64 s[2:3]\n"
    ".Lat_k:\n"
    "s_mov_b32 s53, 0x400000\n"
    ".Lat_kl:\n"
    "buffer_load_dword v253, v238, s[8:11], s48 offen offset:0 sc1\n"
    "s_waitcnt vmcnt(0)\n"
    "v_subrev_u32_e32 v253, s39, v253\n"
    "v_subrev_u32_e32 v253, 0, v253\n"
    "v_cmp_le_u32_e32 vcc, 1, v253\n"
    "s_sub_u32 s53, s53, 1\n"
    "s_cbranch_scc1 .Lat_kr\n"
    "s_nop 2\n"
    "s_cbranch_vccnz .Lat_kl\n"
    "s_branch .Lat_kr\n"
    ".Lat_kx:\n"
    "s_branch .Lat_kxr\n"
    ".Lat_ppoll:\n"
    "s_mov_b32 s53, 0x400000\n"
    ".Lat_ppl:\n"
    "buffer_load_dword v253, v226, s[8:11], s61 offen offset:0 sc1\n"
    "s_waitcnt vmcnt(0)\n"
    "v_subrev_u32_e32 v253, s39, v253\n"
    "v_cmp_lt_u32_e32 vcc, 1, v253\n"
    "s_sub_u32 s53, s53, 1\n"
    "s_cbranch_scc1 .Lat_pok\n"
    "s_nop 2\n"
    "s_cbranch_vccnz .Lat_ppl\n"
    "s_branch .Lat_pok\n"
    ".Lat_wpoll:\n"
    "s_mov_b32 s53, 0x400000\n"
    ".Lat_wpoll_l:\n"
    "s_sleep 1\n"
    "buffer_load_dword v253, v255, s[8:11], s52 offen offset:0 sc1\n"
    "s_waitcnt vmcnt(0)\n"
    "v_cmp_ne_u32_e32 vcc, s39, v253\n"
    "s_sub_u32 s53, s53, 1\n"
    "s_cbranch_scc1 .Lat_wpoll_ret\n"
    "s_nop 2\n"
    "s_cbranch_vccnz .Lat_wpoll_l\n"
    "s_branch .Lat_wpoll_ret\n"
    ".Lat_glate:\n"
    "s_mov_b32 s53, 0x400000\n"
    ".Lat_glate_l:\n"
    "s_sleep 2\n"
    "buffer_load_dword v188, v189, s[8:11], s81 offen offset:0 sc1\n"
    "s_waitcnt vmcnt(0)\n"
    "v_cmp_ne_u32_e32 vcc, s39, v188\n"
    "s_sub_u32 s53, s53, 1\n"
    "s_cbranch_scc1 .Lat_glate_ret\n"
    "s_nop 2\n"
    "s_cbranch_vccnz .Lat_glate_l\n"
    "s_branch .Lat_glate_ret\n"
    ".Lat_lp0:\n"
    "s_mov_b32 s53, 0x400000\n"
    ".Lat_lp:\n"
    "s_sleep 1\n"
    "buffer_load_dword v253, off, s[8:11], s82 sc1\n"
    "s_waitcnt vmcnt(0)\n"
    "v_readfirstlane_b32 s81, v253\n"
    "s_sub_u32 s53, s53, 1\n"
    "s_cbranch_scc1 .Lat_lpx\n"
    "s_nop 3\n"
    "s_cmp_lg_u32 s81, s39\n"
    "s_cbranch_scc1 .Lat_lp\n"
    ".Lat_lpx:\n"
    "s_mov_b32 m0, s30\n"
    "s_nop 0\n"
    "buffer_load_dwordx4 v250, s[8:11], s83 offen offset:0 sc1 lds\n"
    "s_mov_b32 m0, s42\n"
    "s_branch .Lat_g1b\n"
    ".Lat_h1poll:\n"
    "s_mov_b32 s53, 0x400000\n"
    ".Lat_h1poll_l:\n"
    "s_sleep 1\n"
    "buffer_load_dword v80, v83, s[8:11], s59 offen offset:0 sc1\n"
    "s_waitcnt vmcnt(0)\n"
    "v_cmp_ne_u32_e32 vcc, s39, v80\n"
    "s_sub_u32 s53, s53, 1\n"
    "s_cbranch_scc1 .Lat_h1poll_ret\n"
    "s_nop 2\n"
    "s_cbranch_vccnz .Lat_h1poll_l\n"
    "s_branch .Lat_h1poll_ret\n"
    ".Lat_hpoll:\n"
    "s_mov_b32 s53, 0x400000\n"
    ".Lat_hpoll_l:\n"
    "s_sleep 1\n"
    "buffer_load_dword v253, v250, s[8:11], s59 offen offset:0 sc1\n"
    "buffer_load_dword v254, v252, s[8:11], s59 offen offset:0 sc1\n"
    "s_waitcnt vmcnt(0)\n"
    "v_xor_b32_e32 v254, s39, v254\n"
    "v_xor_b32_e32 v253, s39, v253\n"
    "v_or_b32_e32 v253, v253, v254\n"
    "v_cmp_ne_u32_e32 vcc, 0, v253\n"
    "s_sub_u32 s53, s53, 1\n"
    "s_cbranch_scc1 .Lat_hpoll_ret\n"
    "s_nop 2\n"
    "s_cbranch_vccnz .Lat_hpoll_l\n"
    "s_branch .Lat_hpoll_ret\n"
    ".Lat_redo:\n"
    "s_waitcnt vmcnt(0) lgkmcnt(0)\n"
    "s_barrier\n"
    "s_mov_b32 s49, 1\n"
    "s_mov_b32 s80, 0\n"
    "s_mov_b32 s82, 2.0\n"
    "v_lshrrev_b32_e32 v224, 4, v250\n"
    "s_and_b32 s50, s93, 1\n"
    "s_lshr_b32 s51, s93, 1\n"
    "v_lshrrev_b32_e32 v225, 3, v224\n"
    "v_and_b32_e32 v226, 6, v225\n"
    "v_and_b32_e32 v227, 7, v224\n"
    "v_xor_b32_e32 v226, v226, v227\n"
    "v_lshlrev_b32_e32 v226, 4, v226\n"
    "v_and_b32_e32 v227, 4, v225\n"
    "v_add_u32_e32 v227, v227, v225\n"
    "v_lshl_add_u32 v251, v227, 9, v226\n"
    "v_add_u32_e32 v252, 0x1c00, v251\n"
    "v_and_b32_e32 v225, 7, v224\n"
    "v_lshlrev_b32_e32 v227, 7, v225\n"
    "v_bfe_u32 v226, v224, 3, 1\n"
    "v_lshl_add_u32 v227, v226, 10, v227\n"
    "v_and_b32_e32 v225, 6, v224\n"
    "v_lshrrev_b32_e32 v226, 4, v224\n"
    "v_xor_b32_e32 v225, v225, v226\n"
    "v_lshl_add_u32 v240, v225, 4, v227\n"
    "v_lshrrev_b32_e32 v8, 4, v250\n"
    "v_and_b32_e32 v10, 0x70, v251\n"
    "v_bfe_u32 v9, v8, 3, 1\n"
    "v_lshl_add_u32 v248, v9, 9, v10\n"
    "v_lshrrev_b32_e32 v9, 4, v8\n"
    "v_lshl_add_u32 v248, v9, 12, v248\n"
    "s_mul_i32 s43, s50, 3\n"
    "s_xor_b32 s43, s43, s51\n"
    "s_lshl_b32 s43, s43, 15\n"
    "s_add_u32 s43, s43, 32768\n"
    "s_mul_i32 s42, s93, 5120\n"
    "s_lshl_b32 s45, s50, 8\n"
    "s_mul_i32 s40, s32, 1277952\n"
    "s_lshl_b32 s41, s51, 14\n"
    "s_add_u32 s40, s40, s41\n"
    "s_add_u32 s40, s40, s45\n"
    "s_add_u32 s41, s40, 128\n"
    "v_bfe_u32 v9, v8, 2, 3\n"
    "v_lshlrev_b32_e32 v244, 7, v9\n"
    "v_lshrrev_b32_e32 v10, 5, v8\n"
    "v_lshl_add_u32 v244, v10, 10, v244\n"
    "v_and_b32_e32 v9, 6, v9\n"
    "v_bfe_u32 v10, v8, 1, 1\n"
    "v_xor_b32_e32 v9, v9, v10\n"
    "v_lshl_add_u32 v244, v9, 4, v244\n"
    "v_and_b32_e32 v10, 1, v8\n"
    "v_lshl_add_u32 v244, v10, 3, v244\n"
    "s_lshl_b32 s45, s51, 16\n"
    "s_add_u32 s45, s45, 32768\n"
    "v_add_u32_e32 v244, s45, v244\n"
    "v_xor_b32_e32 v245, 32, v244\n"
    "v_xor_b32_e32 v246, 64, v244\n"
    "v_xor_b32_e32 v247, 0x60, v244\n"
    "s_lshl_b32 s45, s51, 15\n"
    "s_add_u32 s48, s45, 32768\n"
    "s_sub_u32 s45, 131072, s45\n"
    "v_add_u32_e32 v242, s45, v240\n"
    "v_add_u32_e32 v240, s48, v240\n"
    "v_xor_b32_e32 v241, 64, v240\n"
    "v_xor_b32_e32 v243, 64, v242\n"
    "s_xor_b32 s45, s93, 2\n"
    "s_mul_i32 s45, s45, 5120\n"
    "v_lshl_add_u32 v249, v8, 2, s45\n"
    "s_lshl_b32 s45, s93, 11\n"
    "v_add_u32_e32 v99, s45, v250\n"
    "v_add_u32_e32 v98, 11345920, v99\n"
    "v_and_b32_e32 v252, 0x30, v250\n"
    "v_add_u32_e32 v252, 0x400, v252\n"
    "s_mul_i32 s59, s32, 999\n"
    "s_andn2_b32 s59, s59, 63\n"
    "s_sub_u32 s48, 3968, s59\n"
    "v_cmp_le_u32_e32 vcc, 0x80, v250\n"
    "v_mov_b32_e32 v80, s48\n"
    "s_add_u32 s59, s59, 6076416\n"
    "v_cndmask_b32_e32 v83, v250, v80, vcc\n"
    "v_bfe_u32 v253, v250, 4, 2\n"
    "v_max_u32_e32 v253, 1, v253\n"
    "v_add_u32_e32 v253, s32, v253\n"
    "v_and_b32_e32 v253, 3, v253\n"
    "v_lshlrev_b32_e32 v253, 9, v253\n"
    "v_mov_b32_e32 v84, s39\n"
    "v_lshrrev_b32_e32 v86, 2, v250\n"
    "v_min_u32_e32 v86, 124, v86\n"
    "s_lshl2_add_u32 s58, s34, s93\n"
    "s_lshl_b32 s58, s58, 7\n"
    "s_add_u32 s58, s58, 5910528\n"
    "s_lshl_b32 s45, s50, 13\n"
    "v_add_u32_e32 v85, s45, v250\n"
    "s_lshl3_add_u32 s45, s45, 49152\n"
    "v_add_u32_e32 v255, s45, v250\n"
    "v_lshrrev_b32_e32 v105, 7, v250\n"
    "v_and_b32_e32 v104, 0x70, v251\n"
    "v_lshl_add_u32 v104, v105, 9, v104\n"
    "v_add_u32_e32 v105, 0xc00, v104\n"
    "v_add_u32_e32 v106, 0x1800, v104\n"
    "v_add_u32_e32 v107, 0x2400, v104\n"
    "s_lshl_b32 s57, s93, 7\n"
    "s_lshl4_add_u32 s56, s57, 24576\n"
    "s_add_u32 s57, s57, 5111808\n"
    "s_mul_i32 s45, s51, 32768\n"
    "s_add_u32 s45, s45, 8192\n"
    "v_subrev_u32_e32 v46, s45, v240\n"
    "v_subrev_u32_e32 v47, s45, v241\n"
    "s_mul_i32 s45, s51, 61440\n"
    "s_add_u32 s45, s45, 8192\n"
    "v_subrev_u32_e32 v4, s45, v244\n"
    "v_subrev_u32_e32 v5, s45, v245\n"
    "v_subrev_u32_e32 v6, s45, v246\n"
    "v_subrev_u32_e32 v7, s45, v247\n"
    "v_accvgpr_write_b32 a244, 0\n"
    "v_accvgpr_write_b32 a245, 0\n"
    "v_accvgpr_write_b32 a246, 0\n"
    "v_accvgpr_write_b32 a247, 0\n"
    "v_accvgpr_write_b32 a248, 0\n"
    "v_accvgpr_write_b32 a249, 0\n"
    "v_accvgpr_write_b32 a250, 0\n"
    "v_accvgpr_write_b32 a251, 0\n"
    "v_accvgpr_write_b32 a252, 0\n"
    "v_accvgpr_write_b32 a253, 0\n"
    "v_accvgpr_write_b32 a254, 0\n"
    "v_accvgpr_write_b32 a255, 0\n"
    "v_mov_b64_e32 v[8:9], 0\n"
    "v_mov_b64_e32 v[10:11], 0\n"
    "v_mov_b64_e32 v[12:13], 0\n"
    "v_mov_b64_e32 v[14:15], 0\n"
    "v_mov_b64_e32 v[16:17], 0\n"
    "v_mov_b64_e32 v[18:19], 0\n"
    "v_mov_b64_e32 v[28:29], 0\n"
    "v_mov_b64_e32 v[30:31], 0\n"
    "v_mov_b64_e32 v[32:33], 0\n"
    "v_mov_b64_e32 v[34:35], 0\n"
    "v_mov_b64_e32 v[36:37], 0\n"
    "v_mov_b64_e32 v[38:39], 0\n"
    "v_mov_b64_e32 v[48:49], 0\n"
    "v_mov_b64_e32 v[50:51], 0\n"
    "v_mov_b64_e32 v[52:53], 0\n"
    "v_mov_b64_e32 v[54:55], 0\n"
    "v_mov_b64_e32 v[56:57], 0\n"
    "v_mov_b64_e32 v[58:59], 0\n"
    "v_mov_b64_e32 v[68:69], 0\n"
    "v_mov_b64_e32 v[70:71], 0\n"
    "v_mov_b64_e32 v[72:73], 0\n"
    "v_mov_b64_e32 v[74:75], 0\n"
    "v_mov_b64_e32 v[76:77], 0\n"
    "v_mov_b64_e32 v[78:79], 0\n"
    "v_mov_b64_e32 v[88:89], 0\n"
    "v_mov_b64_e32 v[90:91], 0\n"
    "v_mov_b64_e32 v[92:93], 0\n"
    "v_mov_b64_e32 v[94:95], 0\n"
    "v_mov_b64_e32 v[96:97], 0\n"
    "v_mov_b64_e32 v[98:99], 0\n"
    "v_mov_b64_e32 v[108:109], 0\n"
    "v_mov_b64_e32 v[110:111], 0\n"
    "v_mov_b64_e32 v[112:113], 0\n"
    "v_mov_b64_e32 v[114:115], 0\n"
    "v_mov_b64_e32 v[116:117], 0\n"
    "v_mov_b64_e32 v[118:119], 0\n"
    "v_mov_b64_e32 v[128:129], 0\n"
    "v_mov_b64_e32 v[130:131], 0\n"
    "v_mov_b64_e32 v[132:133], 0\n"
    "v_mov_b64_e32 v[134:135], 0\n"
    "v_mov_b64_e32 v[136:137], 0\n"
    "v_mov_b64_e32 v[138:139], 0\n"
    "s_mov_b32 m0, s56\n"
    "s_nop 0\n"
    "buffer_load_dwordx4 v104, s[8:11], s57 offen offset:0 sc1 lds\n"
    "buffer_load_dwordx4 v105, s[8:11], s57 offen offset:1024 sc1 lds\n"
    "s_add_u32 m0, s43, 0\n"
    "s_nop 0\n"
    "buffer_load_dwordx4 v104, s[8:11], s40 offen offset:0 sc1 lds\n"
    "buffer_load_dwordx4 v105, s[8:11], s40 offen offset:1024 sc1 lds\n"
    "buffer_load_dwordx4 v106, s[8:11], s40 offen offset:2048 sc1 lds\n"
    "buffer_load_dwordx4 v107, s[8:11], s40 offen offset:3072 sc1 lds\n"
    "s_add_u32 s40, s40, 0x8000\n"
    "s_add_u32 m0, s43, 4096\n"
    "s_nop 0\n"
    "buffer_load_dwordx4 v104, s[8:11], s41 offen offset:0 sc1 lds\n"
    "buffer_load_dwordx4 v105, s[8:11], s41 offen offset:1024 sc1 lds\n"
    "buffer_load_dwordx4 v106, s[8:11], s41 offen offset:2048 sc1 lds\n"
    "buffer_load_dwordx4 v107, s[8:11], s41 offen offset:3072 sc1 lds\n"
    "s_add_u32 s41, s41, 0x8000\n"
    "s_add_u32 m0, s43, 8192\n"
    "s_nop 0\n"
    "buffer_load_dwordx4 v104, s[8:11], s40 offen offset:0 sc1 lds\n"
    "buffer_load_dwordx4 v105, s[8:11], s40 offen offset:1024 sc1 lds\n"
    "buffer_load_dwordx4 v106, s[8:11], s40 offen offset:2048 sc1 lds\n"
    "buffer_load_dwordx4 v107, s[8:11], s40 offen offset:3072 sc1 lds\n"
    "s_add_u32 s40, s40, 0x8000\n"
    "s_add_u32 m0, s43, 12288\n"
    "s_nop 0\n"
    "buffer_load_dwordx4 v104, s[8:11], s41 offen offset:0 sc1 lds\n"
    "buffer_load_dwordx4 v105, s[8:11], s41 offen offset:1024 sc1 lds\n"
    "buffer_load_dwordx4 v106, s[8:11], s41 offen offset:2048 sc1 lds\n"
    "buffer_load_dwordx4 v107, s[8:11], s41 offen offset:3072 sc1 lds\n"
    "s_add_u32 s41, s41, 0x8000\n"
    "s_waitcnt vmcnt(0)\n"
    "s_branch .Lat_p2\n"
    ".Lat_maxpass:\n"
    "s_waitcnt lgkmcnt(0)\n"
    "s_mul_i32 s48, s33, 81920\n"
    "s_add_u32 s48, s48, 11345920\n"
    "s_add_u32 s45, s32, 0\n"
    "s_and_b32 s45, s45, 3\n"
    "s_lshl2_add_u32 s45, s50, s45\n"
    "s_lshl_b32 s45, s45, 13\n"
    "s_add_u32 s45, s45, s48\n"
    "s_add_u32 s46, s45, 4096\n"
    "buffer_load_dwordx4 a[0:3], v250, s[8:11], s45 offen offset:0 sc1\n"
    "buffer_load_dwordx4 a[4:7], v250, s[8:11], s45 offen offset:1024 sc1\n"
    "buffer_load_dwordx4 a[8:11], v250, s[8:11], s45 offen offset:2048 sc1\n"
    "buffer_load_dwordx4 a[12:15], v250, s[8:11], s45 offen offset:3072 sc1\n"
    "buffer_load_dwordx4 a[16:19], v250, s[8:11], s46 offen offset:0 sc1\n"
    "buffer_load_dwordx4 a[20:23], v250, s[8:11], s46 offen offset:1024 sc1\n"
    "buffer_load_dwordx4 a[24:27], v250, s[8:11], s46 offen offset:2048 sc1\n"
    "buffer_load_dwordx4 a[28:31], v250, s[8:11], s46 offen offset:3072 sc1\n"
    "s_and_b32 s45, s32, 1\n"
    "s_xor_b32 s45, s45, s50\n"
    "s_add_u32 s45, s45, 8\n"
    "s_lshl_b32 s45, s45, 13\n"
    "s_add_u32 s45, s45, s48\n"
    "s_add_u32 s46, s45, 4096\n"
    "buffer_load_dwordx4 a[32:35], v250, s[8:11], s45 offen offset:0 sc1\n"
    "buffer_load_dwordx4 a[36:39], v250, s[8:11], s45 offen offset:1024 sc1\n"
    "buffer_load_dwordx4 a[40:43], v250, s[8:11], s45 offen offset:2048 sc1\n"
    "buffer_load_dwordx4 a[44:47], v250, s[8:11], s45 offen offset:3072 sc1\n"
    "buffer_load_dwordx4 a[48:51], v250, s[8:11], s46 offen offset:0 sc1\n"
    "buffer_load_dwordx4 a[52:55], v250, s[8:11], s46 offen offset:1024 sc1\n"
    "buffer_load_dwordx4 a[56:59], v250, s[8:11], s46 offen offset:2048 sc1\n"
    "buffer_load_dwordx4 a[60:63], v250, s[8:11], s46 offen offset:3072 sc1\n"
    "s_add_u32 s45, s32, 1\n"
    "s_and_b32 s45, s45, 3\n"
    "s_lshl2_add_u32 s45, s50, s45\n"
    "s_lshl_b32 s45, s45, 13\n"
    "s_add_u32 s45, s45, s48\n"
    "s_add_u32 s46, s45, 4096\n"
    "buffer_load_dwordx4 a[64:67], v250, s[8:11], s45 offen offset:0 sc1\n"
    "buffer_load_dwordx4 a[68:71], v250, s[8:11], s45 offen offset:1024 sc1\n"
    "buffer_load_dwordx4 a[72:75], v250, s[8:11], s45 offen offset:2048 sc1\n"
    "buffer_load_dwordx4 a[76:79], v250, s[8:11], s45 offen offset:3072 sc1\n"
    "buffer_load_dwordx4 a[80:83], v250, s[8:11], s46 offen offset:0 sc1\n"
    "buffer_load_dwordx4 a[84:87], v250, s[8:11], s46 offen offset:1024 sc1\n"
    "buffer_load_dwordx4 a[88:91], v250, s[8:11], s46 offen offset:2048 sc1\n"
    "buffer_load_dwordx4 a[92:95], v250, s[8:11], s46 offen offset:3072 sc1\n"
    "s_add_u32 s45, s32, 2\n"
    "s_and_b32 s45, s45, 3\n"
    "s_lshl2_add_u32 s45, s50, s45\n"
    "s_lshl_b32 s45, s45, 13\n"
    "s_add_u32 s45, s45, s48\n"
    "s_add_u32 s46, s45, 4096\n"
    "buffer_load_dwordx4 a[96:99], v250, s[8:11], s45 offen offset:0 sc1\n"
    "buffer_load_dwordx4 a[100:103], v250, s[8:11], s45 offen offset:1024 sc1\n"
    "buffer_load_dwordx4 a[104:107], v250, s[8:11], s45 offen offset:2048 sc1\n"
    "buffer_load_dwordx4 a[108:111], v250, s[8:11], s45 offen offset:3072 sc1\n"
    "buffer_load_dwordx4 a[112:115], v250, s[8:11], s46 offen offset:0 sc1\n"
    "buffer_load_dwordx4 a[116:119], v250, s[8:11], s46 offen offset:1024 sc1\n"
    "buffer_load_dwordx4 a[120:123], v250, s[8:11], s46 offen offset:2048 sc1\n"
    "buffer_load_dwordx4 a[124:127], v250, s[8:11], s46 offen offset:3072 sc1\n"
    "s_add_u32 s45, s32, 3\n"
    "s_and_b32 s45, s45, 3\n"
    "s_lshl2_add_u32 s45, s50, s45\n"
    "s_lshl_b32 s45, s45, 13\n"
    "s_add_u32 s45, s45, s48\n"
    "s_add_u32 s46, s45, 4096\n"
    "buffer_load_dwordx4 a[128:131], v250, s[8:11], s45 offen offset:0 sc1\n"
    "buffer_load_dwordx4 a[132:135], v250, s[8:11], s45 offen offset:1024 sc1\n"
    "buffer_load_dwordx4 a[136:139], v250, s[8:11], s45 offen offset:2048 sc1\n"
    "buffer_load_dwordx4 a[140:143], v250, s[8:11], s45 offen offset:3072 sc1\n"
    "buffer_load_dwordx4 a[144:147], v250, s[8:11], s46 offen offset:0 sc1\n"
    "buffer_load_dwordx4 a[148:151], v250, s[8:11], s46 offen offset:1024 sc1\n"
    "buffer_load_dwordx4 a[152:155], v250, s[8:11], s46 offen offset:2048 sc1\n"
    "buffer_load_dwordx4 a[156:159], v250, s[8:11], s46 offen offset:3072 sc1\n"
    "s_waitcnt vmcnt(0)\n"
    "v_mov_b32_e32 v200, 0xff800000\n"
    "v_mov_b32_e32 v201, 0xff800000\n"
    "v_mov_b32_e32 v202, 0xff800000\n"
    "v_mov_b32_e32 v203, 0xff800000\n"
    "v_mov_b32_e32 v204, 0xff800000\n"
    "s_lshl_b32 s45, s51, 14\n"
    "s_lshl_b32 s46, s50, 8\n"
    "s_add_u32 s45, s45, s46\n"
    "s_add_u32 s61, s46, 5103616\n"
    "s_movk_i32 s48, 157\n"
    ".Lat_mp_loop:\n"
    "s_min_u32 s45, s45, s61\n"
    "s_add_u32 s46, s45, 128\n"
    "s_add_u32 m0, s43, 16384\n"
    "s_nop 0\n"
    "buffer_load_dwordx4 v248, s[8:11], s45 offen offset:0 sc1 lds\n"
    "buffer_load_dwordx4 v248, s[8:11], s45 offen offset:1024 sc1 lds\n"
    "buffer_load_dwordx4 v248, s[8:11], s45 offen offset:2048 sc1 lds\n"
    "buffer_load_dwordx4 v248, s[8:11], s45 offen offset:3072 sc1 lds\n"
    "s_add_u32 m0, s43, 20480\n"
    "s_nop 0\n"
    "buffer_load_dwordx4 v248, s[8:11], s46 offen offset:0 sc1 lds\n"
    "buffer_load_dwordx4 v248, s[8:11], s46 offen offset:1024 sc1 lds\n"
    "buffer_load_dwordx4 v248, s[8:11], s46 offen offset:2048 sc1 lds\n"
    "buffer_load_dwordx4 v248, s[8:11], s46 offen offset:3072 sc1 lds\n"
    "s_waitcnt vmcnt(0)\n"
    "s_barrier\n"
    "ds_read_b128 a[160:163], v240 offset:16384\n"
    "ds_read_b128 a[164:167], v241 offset:16384\n"
    "ds_read_b128 a[168:171], v240 offset:20480\n"
    "ds_read_b128 a[172:175], v241 offset:20480\n"
    "ds_read_b128 a[176:179], v242 offset:16384\n"
    "ds_read_b128 a[180:183], v243 offset:16384\n"
    "ds_read_b128 a[184:187], v242 offset:20480\n"
    "ds_read_b128 a[188:191], v243 offset:20480\n"
    "s_waitcnt lgkmcnt(0)\n"
    "v_mfma_f32_16x16x32_bf16 v[180:183], a[160:163], a[0:3], 0\n"
    "v_mfma_f32_16x16x32_bf16 v[184:187], a[160:163], a[32:35], 0\n"
    "v_mfma_f32_16x16x32_bf16 v[188:191], a[160:163], a[64:67], 0\n"
    "v_mfma_f32_16x16x32_bf16 v[192:195], a[160:163], a[96:99], 0\n"
    "v_mfma_f32_16x16x32_bf16 v[196:199], a[160:163], a[128:131], 0\n"
    "v_mfma_f32_16x16x32_bf16 v[180:183], a[164:167], a[4:7], v[180:183]\n"
    "v_mfma_f32_16x16x32_bf16 v[184:187], a[164:167], a[36:39], v[184:187]\n"
    "v_mfma_f32_16x16x32_bf16 v[188:191], a[164:167], a[68:71], v[188:191]\n"
    "v_mfma_f32_16x16x32_bf16 v[192:195], a[164:167], a[100:103], v[192:195]\n"
    "v_mfma_f32_16x16x32_bf16 v[196:199], a[164:167], a[132:135], v[196:199]\n"
    "v_mfma_f32_16x16x32_bf16 v[180:183], a[168:171], a[8:11], v[180:183]\n"
    "v_mfma_f32_16x16x32_bf16 v[184:187], a[168:171], a[40:43], v[184:187]\n"
    "v_mfma_f32_16x16x32_bf16 v[188:191], a[168:171], a[72:75], v[188:191]\n"
    "v_mfma_f32_16x16x32_bf16 v[192:195], a[168:171], a[104:107], v[192:195]\n"
    "v_mfma_f32_16x16x32_bf16 v[196:199], a[168:171], a[136:139], v[196:199]\n"
    "v_mfma_f32_16x16x32_bf16 v[180:183], a[172:175], a[12:15], v[180:183]\n"
    "v_mfma_f32_16x16x32_bf16 v[184:187], a[172:175], a[44:47], v[184:187]\n"
    "v_mfma_f32_16x16x32_bf16 v[188:191], a[172:175], a[76:79], v[188:191]\n"
    "v_mfma_f32_16x16x32_bf16 v[192:195], a[172:175], a[108:111], v[192:195]\n"
    "v_mfma_f32_16x16x32_bf16 v[196:199], a[172:175], a[140:143], v[196:199]\n"
    "v_mfma_f32_16x16x32_bf16 v[180:183], a[176:179], a[16:19], v[180:183]\n"
    "v_mfma_f32_16x16x32_bf16 v[184:187], a[176:179], a[48:51], v[184:187]\n"
    "v_mfma_f32_16x16x32_bf16 v[188:191], a[176:179], a[80:83], v[188:191]\n"
    "v_mfma_f32_16x16x32_bf16 v[192:195], a[176:179], a[112:115], v[192:195]\n"
    "v_mfma_f32_16x16x32_bf16 v[196:199], a[176:179], a[144:147], v[196:199]\n"
    "v_mfma_f32_16x16x32_bf16 v[180:183], a[180:183], a[20:23], v[180:183]\n"
    "v_mfma_f32_16x16x32_bf16 v[184:187], a[180:183], a[52:55], v[184:187]\n"
    "v_mfma_f32_16x16x32_bf16 v[188:191], a[180:183], a[84:87], v[188:191]\n"
    "v_mfma_f32_16x16x32_bf16 v[192:195], a[180:183], a[116:119], v[192:195]\n"
    "v_mfma_f32_16x16x32_bf16 v[196:199], a[180:183], a[148:151], v[196:199]\n"
    "v_mfma_f32_16x16x32_bf16 v[180:183], a[184:187], a[24:27], v[180:183]\n"
    "v_mfma_f32_16x16x32_bf16 v[184:187], a[184:187], a[56:59], v[184:187]\n"
    "v_mfma_f32_16x16x32_bf16 v[188:191], a[184:187], a[88:91], v[188:191]\n"
    "v_mfma_f32_16x16x32_bf16 v[192:195], a[184:187], a[120:123], v[192:195]\n"
    "v_mfma_f32_16x16x32_bf16 v[196:199], a[184:187], a[152:155], v[196:199]\n"
    "v_mfma_f32_16x16x32_bf16 v[180:183], a[188:191], a[28:31], v[180:183]\n"
    "v_mfma_f32_16x16x32_bf16 v[184:187], a[188:191], a[60:63], v[184:187]\n"
    "v_mfma_f32_16x16x32_bf16 v[188:191], a[188:191], a[92:95], v[188:191]\n"
    "v_mfma_f32_16x16x32_bf16 v[192:195], a[188:191], a[124:127], v[192:195]\n"
    "v_mfma_f32_16x16x32_bf16 v[196:199], a[188:191], a[156:159], v[196:199]\n"
    "s_nop 15\n"
    "v_max3_f32 v200, v200, v180, v181\n"
    "v_max3_f32 v200, v200, v182, v183\n"
    "v_max3_f32 v201, v201, v184, v185\n"
    "v_max3_f32 v201, v201, v186, v187\n"
    "v_max3_f32 v202, v202, v188, v189\n"
    "v_max3_f32 v202, v202, v190, v191\n"
    "v_max3_f32 v203, v203, v192, v193\n"
    "v_max3_f32 v203, v203, v194, v195\n"
    "v_max3_f32 v204, v204, v196, v197\n"
    "v_max3_f32 v204, v204, v198, v199\n"
    "s_nop 3\n"
    "ds_read_b128 a[160:163], v240 offset:18432\n"
    "ds_read_b128 a[164:167], v241 offset:18432\n"
    "ds_read_b128 a[168:171], v240 offset:22528\n"
    "ds_read_b128 a[172:175], v241 offset:22528\n"
    "ds_read_b128 a[176:179], v242 offset:18432\n"
    "ds_read_b128 a[180:183], v243 offset:18432\n"
    "ds_read_b128 a[184:187], v242 offset:22528\n"
    "ds_read_b128 a[188:191], v243 offset:22528\n"
    "s_waitcnt lgkmcnt(0)\n"
    "v_mfma_f32_16x16x32_bf16 v[180:183], a[160:163], a[0:3], 0\n"
    "v_mfma_f32_16x16x32_bf16 v[184:187], a[160:163], a[32:35], 0\n"
    "v_mfma_f32_16x16x32_bf16 v[188:191], a[160:163], a[64:67], 0\n"
    "v_mfma_f32_16x16x32_bf16 v[192:195], a[160:163], a[96:99], 0\n"
    "v_mfma_f32_16x16x32_bf16 v[196:199], a[160:163], a[128:131], 0\n"
    "v_mfma_f32_16x16x32_bf16 v[180:183], a[164:167], a[4:7], v[180:183]\n"
    "v_mfma_f32_16x16x32_bf16 v[184:187], a[164:167], a[36:39], v[184:187]\n"
    "v_mfma_f32_16x16x32_bf16 v[188:191], a[164:167], a[68:71], v[188:191]\n"
    "v_mfma_f32_16x16x32_bf16 v[192:195], a[164:167], a[100:103], v[192:195]\n"
    "v_mfma_f32_16x16x32_bf16 v[196:199], a[164:167], a[132:135], v[196:199]\n"
    "v_mfma_f32_16x16x32_bf16 v[180:183], a[168:171], a[8:11], v[180:183]\n"
    "v_mfma_f32_16x16x32_bf16 v[184:187], a[168:171], a[40:43], v[184:187]\n"
    "v_mfma_f32_16x16x32_bf16 v[188:191], a[168:171], a[72:75], v[188:191]\n"
    "v_mfma_f32_16x16x32_bf16 v[192:195], a[168:171], a[104:107], v[192:195]\n"
    "v_mfma_f32_16x16x32_bf16 v[196:199], a[168:171], a[136:139], v[196:199]\n"
    "v_mfma_f32_16x16x32_bf16 v[180:183], a[172:175], a[12:15], v[180:183]\n"
    "v_mfma_f32_16x16x32_bf16 v[184:187], a[172:175], a[44:47], v[184:187]\n"
    "v_mfma_f32_16x16x32_bf16 v[188:191], a[172:175], a[76:79], v[188:191]\n"
    "v_mfma_f32_16x16x32_bf16 v[192:195], a[172:175], a[108:111], v[192:195]\n"
    "v_mfma_f32_16x16x32_bf16 v[196:199], a[172:175], a[140:143], v[196:199]\n"
    "v_mfma_f32_16x16x32_bf16 v[180:183], a[176:179], a[16:19], v[180:183]\n"
    "v_mfma_f32_16x16x32_bf16 v[184:187], a[176:179], a[48:51], v[184:187]\n"
    "v_mfma_f32_16x16x32_bf16 v[188:191], a[176:179], a[80:83], v[188:191]\n"
    "v_mfma_f32_16x16x32_bf16 v[192:195], a[176:179], a[112:115], v[192:195]\n"
    "v_mfma_f32_16x16x32_bf16 v[196:199], a[176:179], a[144:147], v[196:199]\n"
    "v_mfma_f32_16x16x32_bf16 v[180:183], a[180:183], a[20:23], v[180:183]\n"
    "v_mfma_f32_16x16x32_bf16 v[184:187], a[180:183], a[52:55], v[184:187]\n"
    "v_mfma_f32_16x16x32_bf16 v[188:191], a[180:183], a[84:87], v[188:191]\n"
    "v_mfma_f32_16x16x32_bf16 v[192:195], a[180:183], a[116:119], v[192:195]\n"
    "v_mfma_f32_16x16x32_bf16 v[196:199], a[180:183], a[148:151], v[196:199]\n"
    "v_mfma_f32_16x16x32_bf16 v[180:183], a[184:187], a[24:27], v[180:183]\n"
    "v_mfma_f32_16x16x32_bf16 v[184:187], a[184:187], a[56:59], v[184:187]\n"
    "v_mfma_f32_16x16x32_bf16 v[188:191], a[184:187], a[88:91], v[188:191]\n"
    "v_mfma_f32_16x16x32_bf16 v[192:195], a[184:187], a[120:123], v[192:195]\n"
    "v_mfma_f32_16x16x32_bf16 v[196:199], a[184:187], a[152:155], v[196:199]\n"
    "v_mfma_f32_16x16x32_bf16 v[180:183], a[188:191], a[28:31], v[180:183]\n"
    "v_mfma_f32_16x16x32_bf16 v[184:187], a[188:191], a[60:63], v[184:187]\n"
    "v_mfma_f32_16x16x32_bf16 v[188:191], a[188:191], a[92:95], v[188:191]\n"
    "v_mfma_f32_16x16x32_bf16 v[192:195], a[188:191], a[124:127], v[192:195]\n"
    "v_mfma_f32_16x16x32_bf16 v[196:199], a[188:191], a[156:159], v[196:199]\n"
    "s_nop 15\n"
    "v_max3_f32 v200, v200, v180, v181\n"
    "v_max3_f32 v200, v200, v182, v183\n"
    "v_max3_f32 v201, v201, v184, v185\n"
    "v_max3_f32 v201, v201, v186, v187\n"
    "v_max3_f32 v202, v202, v188, v189\n"
    "v_max3_f32 v202, v202, v190, v191\n"
    "v_max3_f32 v203, v203, v192, v193\n"
    "v_max3_f32 v203, v203, v194, v195\n"
    "v_max3_f32 v204, v204, v196, v197\n"
    "v_max3_f32 v204, v204, v198, v199\n"
    "s_nop 3\n"
    "s_barrier\n"
    "s_add_u32 s45, s45, 0x8000\n"
    "s_sub_u32 s48, s48, 1\n"
    "s_cmp_lg_u32 s48, 0\n"
    "s_cbranch_scc1 .Lat_mp_loop\n"
    "s_mov_b32 m0, s42\n"
    "s_nop 1\n"
    "ds_write_addtid_b32 v200 offset:0\n"
    "ds_write_addtid_b32 v201 offset:256\n"
    "ds_write_addtid_b32 v202 offset:512\n"
    "ds_write_addtid_b32 v203 offset:768\n"
    "ds_write_addtid_b32 v204 offset:1024\n"
    "s_waitcnt lgkmcnt(0)\n"
    "s_barrier\n"
    "ds_read_b32 v180, v249 offset:0\n"
    "ds_read_b32 v184, v249 offset:256\n"
    "ds_read_b32 v188, v249 offset:512\n"
    "ds_read_b32 v192, v249 offset:768\n"
    "ds_read_b32 v196, v249 offset:1024\n"
    "s_waitcnt lgkmcnt(0)\n"
    "v_max_f32_e32 v205, v200, v180\n"
    "v_mov_b32_e32 v206, v205\n"
    "s_nop 1\n"
    "v_permlane16_swap_b32_e32 v205, v206\n"
    "s_nop 0\n"
    "v_max_f32_e32 v205, v205, v206\n"
    "v_mov_b32_e32 v206, v205\n"
    "s_nop 1\n"
    "v_permlane32_swap_b32_e32 v205, v206\n"
    "s_nop 0\n"
    "v_max_f32_e32 v205, v205, v206\n"
    "v_add_f32_e32 v205, 0x42540000, v205\n"
    "v_xor_b32_e32 v220, 0x80000000, v205\n"
    "v_xor_b32_e32 v221, 0x80000000, v205\n"
    "v_xor_b32_e32 v222, 0x80000000, v205\n"
    "v_xor_b32_e32 v223, 0x80000000, v205\n"
    "v_max_f32_e32 v205, v201, v184\n"
    "v_mov_b32_e32 v206, v205\n"
    "s_nop 1\n"
    "v_permlane16_swap_b32_e32 v205, v206\n"
    "s_nop 0\n"
    "v_max_f32_e32 v205, v205, v206\n"
    "v_mov_b32_e32 v206, v205\n"
    "s_nop 1\n"
    "v_permlane32_swap_b32_e32 v205, v206\n"
    "s_nop 0\n"
    "v_max_f32_e32 v205, v205, v206\n"
    "v_add_f32_e32 v205, 0x42540000, v205\n"
    "v_xor_b32_e32 v224, 0x80000000, v205\n"
    "v_xor_b32_e32 v225, 0x80000000, v205\n"
    "v_xor_b32_e32 v226, 0x80000000, v205\n"
    "v_xor_b32_e32 v227, 0x80000000, v205\n"
    "v_max_f32_e32 v205, v202, v188\n"
    "v_mov_b32_e32 v206, v205\n"
    "s_nop 1\n"
    "v_permlane16_swap_b32_e32 v205, v206\n"
    "s_nop 0\n"
    "v_max_f32_e32 v205, v205, v206\n"
    "v_mov_b32_e32 v206, v205\n"
    "s_nop 1\n"
    "v_permlane32_swap_b32_e32 v205, v206\n"
    "s_nop 0\n"
    "v_max_f32_e32 v205, v205, v206\n"
    "v_add_f32_e32 v205, 0x42540000, v205\n"
    "v_xor_b32_e32 v228, 0x80000000, v205\n"
    "v_xor_b32_e32 v229, 0x80000000, v205\n"
    "v_xor_b32_e32 v230, 0x80000000, v205\n"
    "v_xor_b32_e32 v231, 0x80000000, v205\n"
    "v_max_f32_e32 v205, v203, v192\n"
    "v_mov_b32_e32 v206, v205\n"
    "s_nop 1\n"
    "v_permlane16_swap_b32_e32 v205, v206\n"
    "s_nop 0\n"
    "v_max_f32_e32 v205, v205, v206\n"
    "v_mov_b32_e32 v206, v205\n"
    "s_nop 1\n"
    "v_permlane32_swap_b32_e32 v205, v206\n"
    "s_nop 0\n"
    "v_max_f32_e32 v205, v205, v206\n"
    "v_add_f32_e32 v205, 0x42540000, v205\n"
    "v_xor_b32_e32 v232, 0x80000000, v205\n"
    "v_xor_b32_e32 v233, 0x80000000, v205\n"
    "v_xor_b32_e32 v234, 0x80000000, v205\n"
    "v_xor_b32_e32 v235, 0x80000000, v205\n"
    "v_max_f32_e32 v205, v204, v196\n"
    "v_mov_b32_e32 v206, v205\n"
    "s_nop 1\n"
    "v_permlane16_swap_b32_e32 v205, v206\n"
    "s_nop 0\n"
    "v_max_f32_e32 v205, v205, v206\n"
    "v_mov_b32_e32 v206, v205\n"
    "s_nop 1\n"
    "v_permlane32_swap_b32_e32 v205, v206\n"
    "s_nop 0\n"
    "v_max_f32_e32 v205, v205, v206\n"
    "v_add_f32_e32 v205, 0x42540000, v205\n"
    "v_xor_b32_e32 v236, 0x80000000, v205\n"
    "v_xor_b32_e32 v237, 0x80000000, v205\n"
    "v_xor_b32_e32 v238, 0x80000000, v205\n"
    "v_xor_b32_e32 v239, 0x80000000, v205\n"
    "s_nop 1\n"
    "s_barrier\n"
    "s_mul_i32 s48, s33, 81920\n"
    "s_add_u32 s48, s48, 11345920\n"
    "s_lshl_b32 s46, s93, 11\n"
    "s_add_u32 s48, s48, s46\n"
    "s_add_u32 s46, s46, 49152\n"
    "s_and_b32 s45, s32, 1\n"
    "s_xor_b32 s45, s45, 8\n"
    "s_lshl_b32 s45, s45, 13\n"
    "s_add_u32 s45, s45, s48\n"
    "s_add_u32 m0, s46, 0\n"
    "s_nop 0\n"
    "buffer_load_dwordx4 v250, s[8:11], s45 offen offset:0 sc1 lds\n"
    "buffer_load_dwordx4 v250, s[8:11], s45 offen offset:1024 sc1 lds\n"
    "s_branch .Lat_maxpass_ret\n"
    ".p2align 12\n"
    ".Lat_out:\n"
    "s_waitcnt lgkmcnt(0)\n"
    "s_mov_b64 s[2:3], s[0:1]\n"
    "s_mov_b64 %0, s[2:3]\n"
    "s_mov_b32 %1, s92\n"
    "s_mov_b32 %2, s93\n"
    : "=s"(ka), "=s"(b), "=s"(wid) : "0"(__builtin_amdgcn_kernarg_segment_ptr()), "1"(blockIdx.x)
    : "v0", "v1", "v2", "v3", "v4", "v5", "v6", "v7", "v8", "v9", "v10", "v11", "v12", "v13", "v14", "v15", "v16", "v17", "v18", "v19", "v20", "v21", "v22", "v23", "v24", "v25", "v26", "v27", "v28", "v29", "v30", "v31", "v32", "v33", "v34", "v35", "v36", "v37", "v38", "v39", "v40", "v41", "v42", "v43", "v44", "v45", "v46", "v47", "v48", "v49", "v50", "v51", "v52", "v53", "v54", "v55", "v56", "v57", "v58", "v59", "v60", "v61", "v62", "v63", "v64", "v65", "v66", "v67", "v68", "v69", "v70", "v71", "v72", "v73", "v74", "v75", "v76", "v77", "v78", "v79", "v80", "v81", "v82", "v83", "v84", "v85", "v86", "v87", "v88", "v89", "v90", "v91", "v92", "v93", "v94", "v95", "v96", "v97", "v98", "v99", "v100", "v101", "v102", "v103", "v104", "v105", "v106", "v107", "v108", "v109", "v110", "v111", "v112", "v113", "v114", "v115", "v116", "v117", "v118", "v119", "v120", "v121", "v122", "v123", "v124", "v125", "v126", "v127", "v128", "v129", "v130", "v131", "v132", "v133", "v134", "v135", "v136", "v137", "v138", "v139", "v140", "v141", "v142", "v143", "v144", "v145", "v146", "v147", "v148", "v149", "v150", "v151", "v152", "v153", "v154", "v155", "v156", "v157", "v158", "v159", "v160", "v161", "v162", "v163", "v164", "v165", "v166", "v167", "v168", "v169", "v170", "v171", "v172", "v173", "v174", "v175", "v176", "v177", "v178", "v179", "v180", "v181", "v182", "v183", "v184", "v185", "v186", "v187", "v188", "v189", "v190", "v191", "v192", "v193", "v194", "v195", "v196", "v197", "v198", "v199", "v200", "v201", "v202", "v203", "v204", "v205", "v206", "v207", "v208", "v209", "v210", "v211", "v212", "v213", "v214", "v215", "v216", "v217", "v218", "v219", "v220", "v221", "v222", "v223", "v224", "v225", "v226", "v227", "v228", "v229", "v230", "v231", "v232", "v233", "v234", "v235", "v236", "v237", "v238", "v239", "v240", "v241", "v242", "v243", "v244", "v245", "v246", "v247", "v248", "v249", "v250", "v251", "v252", "v253", "v254", "v255", "a0", "a1", "a2", "a3", "a4", "a5", "a6", "a7", "a8", "a9", "a10", "a11", "a12", "a13", "a14", "a15", "a16", "a17", "a18", "a19", "a20", "a21", "a22", "a23", "a24", "a25", "a26", "a27", "a28", "a29", "a30", "a31", "a32", "a33", "a34", "a35", "a36", "a37", "a38", "a39", "a40", "a41", "a42", "a43", "a44", "a45", "a46", "a47", "a48", "a49", "a50", "a51", "a52", "a53", "a54", "a55", "a56", "a57", "a58", "a59", "a60", "a61", "a62", "a63", "a64", "a65", "a66", "a67", "a68", "a69", "a70", "a71", "a72", "a73", "a74", "a75", "a76", "a77", "a78", "a79", "a80", "a81", "a82", "a83", "a84", "a85", "a86", "a87", "a88", "a89", "a90", "a91", "a92", "a93", "a94", "a95", "a96", "a97", "a98", "a99", "a100", "a101", "a102", "a103", "a104", "a105", "a106", "a107", "a108", "a109", "a110", "a111", "a112", "a113", "a114", "a115", "a116", "a117", "a118", "a119", "a120", "a121", "a122", "a123", "a124", "a125", "a126", "a127", "a128", "a129", "a130", "a131", "a132", "a133", "a134", "a135", "a136", "a137", "a138", "a139", "a140", "a141", "a142", "a143", "a144", "a145", "a146", "a147", "a148", "a149", "a150", "a151", "a152", "a153", "a154", "a155", "a156", "a157", "a158", "a159", "a160", "a161", "a162", "a163", "a164", "a165", "a166", "a167", "a168", "a169", "a170", "a171", "a172", "a173", "a174", "a175", "a176", "a177", "a178", "a179", "a180", "a181", "a182", "a183", "a184", "a185", "a186", "a187", "a188", "a189", "a190", "a191", "a192", "a193", "a194", "a195", "a196", "a197", "a198", "a199", "a200", "a201", "a202", "a203", "a204", "a205", "a206", "a207", "a208", "a209", "a210", "a211", "a212", "a213", "a214", "a215", "a216", "a217", "a218", "a219", "a220", "a221", "a222", "a223", "a224", "a225", "a226", "a227", "a228", "a229", "a230", "a231", "a232", "a233", "a234", "a235", "a236", "a237", "a238", "a239", "a240", "a241", "a242", "a243", "a244", "a245", "a246", "a247", "a248", "a249", "a250", "a251", "a252", "a253", "a254", "a255", "s2", "s3", "s4", "s5", "s6", "s7", "s8", "s9", "s10", "s11", "s12", "s13", "s14", "s15", "s16", "s17", "s18", "s19", "s20", "s21", "s22", "s23", "s24", "s25", "s26", "s27", "s28", "s29", "s30", "s31", "s32", "s33", "s34", "s35", "s36", "s37", "s38", "s39", "s40", "s41", "s42", "s43", "s44", "s45", "s46", "s47", "s48", "s49", "s50", "s51", "s52", "s53", "s54", "s55", "s56", "s57", "s58", "s59", "s60", "s61", "s62", "s63", "s80", "s81", "s82", "s83", "s84", "s85", "s86", "s87", "s88", "s89", "s90", "s91", "s92", "s93", "s94", "s95", "vcc", "m0", "scc", "memory");
  typedef __attribute__((address_space(1))) void* gp;
  helper(lds, b >> 1 & 3, wid, (const int*)(gp)ka[2], (const int*)(gp)ka[4], (const u16*)(gp)ka[8], (const u16*)(gp)ka[9], (const u16*)(gp)ka[10], (const u16*)(gp)ka[7], (char*)(gp)ka[3]);
}

extern "C" void kernel_launch(void* const* d_in, const int* in_sizes, int n_in, void* d_out, int out_size, void* d_ws,
                              size_t ws_size, hipStream_t stream) {
  const u16* x = (const u16*)d_in[0];
  const int* src = (const int*)d_in[1];
  const int* dst = (const int*)d_in[2];
  const u16* deg_inv = (const u16*)d_in[3];
  const u16* wmsg = (const u16*)d_in[4];
  const u16* wq = (const u16*)d_in[5];
  const u16* wk = (const u16*)d_in[6];
  const u16* wv = (const u16*)d_in[7];
  const u16* wout = (const u16*)d_in[8];
  char* ws = (char*)d_ws;
  hipFuncSetAttribute((const void*)gps_kernel, hipFuncAttributeMaxDynamicSharedMemorySize, 163840);
  gps_kernel<<<256, 256, 163840, stream>>>(wq, wk, src, ws, dst, x, (u16*)d_out, deg_inv, wv, wmsg, wout);
}
```

```cpp
#include <hip/hip_runtime.h>
#include <hip/hip_bf16.h>
#include <stdint.h>

typedef unsigned short u16;
typedef __attribute__((ext_vector_type(8))) short bf16x8;
typedef __attribute__((ext_vector_type(4))) short bf16x4;
typedef __attribute__((ext_vector_type(4))) float f32x4;
typedef __attribute__((ext_vector_type(2))) float f32x2;
typedef __attribute__((ext_vector_type(4))) unsigned u32x4;
typedef __attribute__((ext_vector_type(2))) unsigned u32x2;
#define DI __device__ __forceinline__

constexpr int NN = 10000, DD = 256, EE = 160000;
constexpr int NATT = 252, NSP = 4, SPN = 2560;
constexpr int GU = 5, GNB = 5; static_assert(GU * GNB <= 30, "list size");

constexpr unsigned WS_H = 0;
constexpr unsigned WS_AF = WS_H + 10240 * 512;
constexpr unsigned WS_WV = WS_AF + 135168;
constexpr unsigned WS_WM = WS_WV + 135168;
constexpr unsigned WS_XF = WS_WM + 135168;
constexpr unsigned WS_GF = WS_XF + 3328 * 128;
constexpr unsigned WS_HF = WS_GF + 2048;
constexpr unsigned WS_PF = WS_HF + 4096;
constexpr unsigned WS_NF = WS_PF + 4096;
constexpr unsigned WS_WF = WS_NF + 16384;
constexpr unsigned WS_LF = WS_WF + 256;
constexpr unsigned WS_EP = WS_LF + 512;
constexpr unsigned WS_G = WS_EP + 256 + 1024;
constexpr unsigned WS_QP = WS_G + 10240 * 512;
constexpr unsigned WS_XB = WS_QP + 10240 * 512;
constexpr unsigned WS_LS = WS_XB + 2 * NATT * 101376;
constexpr unsigned WS_BL = WS_LS + 524288;
constexpr unsigned WS_DBG = WS_BL + 64 * 260 * 64;
constexpr unsigned WS_END = WS_DBG + 1024 * 128 + 8192;

DI float bf2f(unsigned x) { return __uint_as_float(x << 16); }
DI float bfhi(unsigned x) { return __uint_as_float(x & 0xffff0000u); }
DI unsigned pk2(float lo, float hi) {
  typedef __attribute__((ext_vector_type(2))) __bf16 bf2_t;
  const bf2_t r = __builtin_convertvector((f32x2){lo, hi}, bf2_t);
  return __builtin_bit_cast(unsigned, r);
}
typedef __attribute__((address_space(3))) bf16x4* lds_bf16x4_p;
DI bf16x4 tr_read(unsigned p) { return __builtin_amdgcn_ds_read_tr16_b64_v4i16((lds_bf16x4_p)p); }
DI bf16x8 as_bf(u32x4 v) { return __builtin_bit_cast(bf16x8, v); }
#define SB() __builtin_amdgcn_sched_barrier(0)
#define MFMA16(a, b, c) __builtin_amdgcn_mfma_f32_16x16x32_bf16(a, b, c, 0, 0, 0)

typedef __amdgpu_buffer_rsrc_t srd_t;
DI void drain() { asm volatile("s_waitcnt vmcnt(0)" ::: "memory"); }
DI void st_flag(srd_t w, unsigned off, unsigned tag) { __builtin_amdgcn_raw_buffer_store_b32(tag, w, off, 0, 16); }
DI void wait_flag(srd_t w, unsigned off, unsigned tag) {
#pragma unroll 1
  for (int it = 0; it < (1 << 22); ++it) {
    unsigned v;
    asm volatile("buffer_load_dword %0, %1, %2, 0 offen sc1\n s_waitcnt vmcnt(0)" : "=v"(v) : "v"(off), "s"(w) : "memory");
    if (__ballot(v != tag) == 0) break;
    __builtin_amdgcn_s_sleep(2);
    asm volatile("" ::: "memory");
  }
  asm volatile("" ::: "memory");
}

DI void prod_store(const f32x4 (&acc)[2][2], int mat, int ks, int J, int lane, srd_t wsrd) {
#pragma unroll
  for (int s = 0; s < 2; ++s) {
    u32x4 o = {pk2(acc[0][s][0], acc[0][s][1]), pk2(acc[0][s][2], acc[0][s][3]), pk2(acc[1][s][0], acc[1][s][1]), pk2(acc[1][s][2], acc[1][s][3])};
    if (ks == 7 && lane >= 48) o[3] &= 0xffffu;
    const int T = 2 * J + s;
    const int at = mat * 135168 + ks * 4096 + (T & 3) * 1024;
    __builtin_amdgcn_raw_buffer_store_b128(o, wsrd, WS_WV + (T >> 2) * 33792 + at + lane * 16, 0, 16);
  }
}
DI void corr(unsigned* dl, const unsigned (&lr)[16]) {
  float a0 = 0.f, a1 = 0.f;
#pragma unroll
  for (int r = 0; r < 16; ++r) { a0 += bf2f(lr[r] & 0xffffu); a1 += bfhi(lr[r]); }
  dl[0] = pk2(a0 * -0.0625f, a1 * -0.0625f);
  dl[128] = pk2((bf2f(lr[15] & 0xffffu) - 0.25f * a0) * (1.f / 3.f), (bfhi(lr[15]) - 0.25f * a1) * (1.f / 3.f));
}
DI void product_load(char* lds, int wid, int lane, int job, const u16* __restrict__ wv, const u16* __restrict__ wm, const u16* __restrict__ wo) {
  const unsigned ldsb = (unsigned)(size_t)lds;
  const int r = lane >> 3, sw = ((lane & 7) ^ (r & 6)) * 16 + wid * 128;
  const unsigned vp = (8 * (r >> 2) + (r & 3)) * 512 + sw, vn = r * 512 + sw;
  auto image = [&](int n, const u16* w, int row0, bool nat) {
#pragma unroll
    for (int i = 0; i < 8; ++i) {
      const int a = i & 3;
      asm volatile("s_mov_b32 m0, %2\n s_nop 0\n global_load_lds_dwordx4 %0, %1"
                   :: "v"(nat ? vn : vp), "s"(w + (row0 + (i >> 2) * 32 + (nat ? 8 * a : 16 * (a & 1) + 4 * (a >> 1))) * DD),
                      "s"(ldsb + n * 32768 + (i >> 2) * 16384 + wid * 4096 + a * 1024) : "m0", "memory");
    }
  };
  image(2, wv, (job >> 2) * 64, true);
  image(3, wm, (job >> 2) * 64, false);
#pragma unroll
  for (int k = 0; k < 8; ++k)
    asm volatile("s_mov_b32 m0, %2\n s_nop 0\n global_load_lds_dwordx4 %0, %1"
                 :: "v"((lane >> 3) * 512 + (lane & 7) * 16), "s"(wo + (k * 32 + wid * 8) * DD + (job & 3) * 64), "s"(ldsb + k * 4096 + wid * 1024) : "m0", "memory");
}
DI void product_w(char* lds, int wid, int lane, int job, srd_t wsrd) {
  const int m = lane & 15, g = lane >> 4;
  const int ks = 2 * (job >> 2) + (wid >> 1), J = 2 * (job & 3) + (wid & 1);
  const unsigned ldsb = (unsigned)(size_t)lds;
  unsigned* dl = reinterpret_cast<unsigned*>(lds + 0x20000);
  const bool hi = (m >> 3) != 0;
  const unsigned ra = (m >> 3) * 1024 + (m & 7) * 128 + (g ^ (m & 6)) * 16;
  auto frag = [&](int n, int tl, int t, int k) {
    return *reinterpret_cast<const bf16x8*>(lds + n * 32768 + tl * 16384 + (k >> 1) * 4096 + t * 2048 + (ra ^ ((k & 1) * 64)));
  };
  f32x4 av[2][2] = {}, am[2][2] = {}, ad[2] = {};
  const unsigned tb = ldsb + (8 * g + ((lane >> 2) & 3)) * 128 + (wid & 1) * 64 + (lane & 3) * 16;
  bf16x8 d0[8], d1[8], d2[8], rr[8][2], vf[8][2], mf[8][2];
#pragma unroll
  for (int kk = 0; kk < 9; ++kk) {
    if (kk < 8) {
      d0[kk] = *reinterpret_cast<const bf16x8*>(dl + 512 + 16 * kk + 4 * g); d1[kk] = *reinterpret_cast<const bf16x8*>(dl + 512 + (ks == 7 ? 128 : 0) + 16 * kk + 4 * g);
      d2[kk] = *reinterpret_cast<const bf16x8*>(dl + 768 + (ks == 7 && hi ? 128 : 0) + 16 * kk + 4 * g);
#pragma unroll
      for (int s = 0; s < 2; ++s) {
        rr[kk][s] = __builtin_shufflevector(tr_read(tb + kk * 4096 + s * 8), tr_read(tb + kk * 4096 + s * 8 + 512), 0, 1, 2, 3, 4, 5, 6, 7);
        vf[kk][s] = frag(2, wid >> 1, s, kk); mf[kk][s] = frag(3, wid >> 1, s, kk);
      }
    }
    SB();
    if (kk) {
      const int k = kk - 1;
#pragma unroll
      for (int s = 0; s < 2; ++s) {
        av[0][s] = MFMA16(d0[k], rr[k][s], av[0][s]); av[1][s] = MFMA16(d1[k], rr[k][s], av[1][s]); ad[s] = MFMA16(d2[k], rr[k][s], ad[s]);
#pragma unroll
        for (int t = 0; t < 2; ++t) { av[t][s] = MFMA16(vf[k][t], rr[k][s], av[t][s]); am[t][s] = MFMA16(mf[k][t], rr[k][s], am[t][s]); }
      }
    }
    SB();
  }
#pragma unroll
  for (int t = 0; t < 2; ++t)
#pragma unroll
    for (int s = 0; s < 2; ++s) am[t][s] += ad[s];
  prod_store(av, 0, ks, J, lane, wsrd);
  prod_store(am, 1, ks, J, lane, wsrd);
}

DI void helper(char* lds, int j, int wid, const int* __restrict__ src, const int* __restrict__ dst,
               const u16* __restrict__ wv, const u16* __restrict__ wm, const u16* __restrict__ wo, const u16* dinv, char* ws) {
  unsigned* img = reinterpret_cast<unsigned*>(lds);
  char* cntb = lds + 0x20000;
  int* cnt = reinterpret_cast<int*>(cntb);
  unsigned* ub = reinterpret_cast<unsigned*>(lds + 0x22c00);
  int* red = reinterpret_cast<int*>(ub + 256);
  u16* dg = reinterpret_cast<u16*>(lds + 0x23400);
  u16* tmp = reinterpret_cast<u16*>(lds + 0x25000) + wid * 512;
  int lane; asm volatile("v_mbcnt_lo_u32_b32 %0, -1, 0\n\tv_mbcnt_hi_u32_b32 %0, -1, %0" : "=v"(lane));
  const int tid = wid * 64 + lane, lo = j * SPN, nn_ = min(SPN, NN - lo);
  const srd_t wsrd = __builtin_amdgcn_make_buffer_rsrc(ws, 0, WS_END, 0x00020000);
  const unsigned tag = __builtin_amdgcn_raw_buffer_load_b32(wsrd, WS_EP, 0, 0) + 4u;
  auto touch = [&](unsigned at) { asm volatile("s_mov_b32 m0, 0x27c00\n s_nop 0\n buffer_load_dword %0, %1, 0 offen sc1 lds" :: "v"(at), "s"(wsrd) : "m0", "memory"); };
  touch(lane < 16 ? min(WS_GF + (lane * 16 + wid * 4 + j) * 128, WS_EP) : 0x40000000u);
  for (int i = tid; i < 8192; i += 256) { const unsigned f = (i & 63) >= 60 ? 0u : ~0u; reinterpret_cast<u32x4*>(img)[i] = (u32x4){f, f, f, f}; }
  for (int i = tid; i < SPN + 64; i += 256) cnt[i] = 0;
  ub[tid] = 0u;
  if (tid < 8) red[tid] = 0;
  touch(WS_XF + 262144 + (j * 256 + tid) * 128);
  wait_flag(wsrd, WS_NF + j * 4096 + min(tid, 249) * 16, tag);
  __syncthreads();
  const unsigned dum = (SPN + lane) << 18;
  auto fetch = [&](int k) {
    return __builtin_amdgcn_raw_buffer_load_b128(wsrd, WS_BL + (k < 48 ? tid * 16 + (k & 7) * 4096 : (tid >> 2) * 128 + (tid & 3) * 16 + (k & 3) * 8192 + (k & 4) * 16), (j * 8 + (k >> 3)) * 33280, 16);
  };
  auto clean = [&](u32x4& e, int k) {
#pragma unroll
    for (int r = 0; r < 4; ++r) e[r] = (k < 48 ? (k & 7) == 7 && tid >= 208 : (k & 3) == 3 && tid >= 232) ? dum : min(e[r], dum);
  };
  auto slot = [&](unsigned e) { return reinterpret_cast<int*>(cntb + (e >> 16)); };
  constexpr int NL = 52;
  u32x4 ent[NL];
#pragma unroll
  for (int k = 0; k < NL; ++k) ent[k] = fetch(k);
  SB();
#pragma unroll
  for (int k = 0; k < NL; ++k) {
    clean(ent[k], k);
    int* p[4];
#pragma unroll
    for (int r = 0; r < 4; ++r) p[r] = slot(ent[k][r]);
    SB();
#pragma unroll
    for (int r = 0; r < 4; ++r) atomicAdd(p[r], 2);
    SB();
  }
  unsigned use = 0;
  const unsigned dum3 = (tid & 3) == 3 ? dum : 0;
#pragma unroll
  for (int k = 0; k < 4; ++k) use |= __ballot(ent[NL - 4 + k][3] < dum3) ? 1u << k : 0u;
  if (use)
#pragma unroll 1
    for (int k = NL; k < 64; ++k)
      if (use >> (k - NL) & 1) {
        u32x4 e = fetch(k); clean(e, k);
#pragma unroll
        for (int r = 0; r < 4; ++r) atomicAdd(slot(e[r]), 2);
        if (__ballot(e[3] < dum3)) { use |= 16u << (k - NL); if (k >= 60) red[7] = 1; }
      }
  __syncthreads();
  int inc = 0, nu[10], cn[10], cm = 0;
  auto units = [&](int c) { return max(1, (c + 6) >> 3); };
#pragma unroll
  for (int i = 0; i < 10; i += 2) { const u32x2 v = *reinterpret_cast<u32x2*>(cnt + 10 * tid + i); cn[i] = v[0]; cn[i + 1] = v[1]; }
  const int nup = units(cnt[max(10 * tid - 1, 0)]);
  const bool act = 10 * tid < nn_;
#pragma unroll
  for (int i = 0; i < 10; ++i) { cm = max(cm, cn[i]); nu[i] = act ? units(cn[i]) : 0; inc += nu[i]; }
  const int own = inc;
  inc += __builtin_amdgcn_update_dpp(0, inc, 0x111, 15, 15, true); inc += __builtin_amdgcn_update_dpp(0, inc, 0x112, 15, 15, true);
  inc += __builtin_amdgcn_update_dpp(0, inc, 0x114, 15, 15, true); inc += __builtin_amdgcn_update_dpp(0, inc, 0x118, 15, 15, true);
  inc += __builtin_amdgcn_update_dpp(0, inc, 0x142, 10, 15, false); inc += __builtin_amdgcn_update_dpp(0, inc, 0x143, 12, 15, false);
  if (lane == 63) red[wid] = inc;
  const bool srt = __ballot(cm > 64);
  __syncthreads();
  const u32x4 rd = *reinterpret_cast<u32x4*>(red);
  const float rs_ = 64.f / (float)(rd[0] + rd[1] + rd[2] + rd[3]);
  inc += (wid > 0 ? rd[0] : 0) + (wid > 1 ? rd[1] : 0) + (wid > 2 ? rd[2] : 0) - own;
  int wv_[10];
  {
    int o = inc, wp = tid ? min(127, (int)((float)(2 * o - nup) * rs_)) : 128;
#pragma unroll
    for (int i = 0; i < 10; ++i) {
      wv_[i] = min(127, (int)((float)(2 * o + nu[i]) * rs_));
      ub[act && wv_[i] != wp ? wv_[i] : 128 + lane] = (unsigned)o << 12 | (unsigned)(10 * tid + i);
      wp = wv_[i]; o += nu[i];
    }
  }
  __syncthreads();
  {
    int u0[10], top = 0;
#pragma unroll
    for (int i = 0; i < 10; ++i) u0[i] = ub[wv_[i]] >> 12;
#pragma unroll
    for (int i = 0; i < 10; ++i) {
      const int lu = inc - u0[i], e = lu + nu[i] - 1 + GU, q = min(e / (4 * GU), GNB);
      u0[i] = wv_[i] * 1024 + lu * 8; top = max(top, q);
      atomicOr(img + wv_[i] * 256 + 244 + q, act ? 1u << (e - q * 4 * GU) : 0u);
      inc += nu[i];
    }
#pragma unroll
    for (int i = 0; i < 10; i += 2) *reinterpret_cast<u32x2*>(cnt + 10 * tid + i) = (u32x2){(unsigned)u0[i], (unsigned)u0[i + 1]};
    if (tid < 64) cnt[SPN + tid] = 0x30000;
    if (act && top == GNB) red[7] = 1;
  }
  __syncthreads();
  const bool big_ = red[7];
  if (tid < 128) {
    unsigned nb = 0;
#pragma unroll
    for (int q = 0; q < GNB; ++q) nb = img[tid * 256 + 244 + q] ? q + 1 : nb;
    *reinterpret_cast<u32x4*>(img + tid * 256 + 240) = (u32x4){big_ ? 0u : nb, WS_G + (lo + (ub[tid] & 4095u)) * 512, big_ || (tid & 3) ? 0x40000000u : WS_GF + j * 512 + tid * 4, 0u};
  }
  if (!big_) {
    const unsigned dmp = 0x27000 + lane * 4;
    typedef __attribute__((address_space(3))) u16* lds_u16_p;
    unsigned p[2][8];
#pragma unroll
    for (int k = 0; k < NL + 2; k += 2) {
      if (k < NL) {
        int* q[8];
#pragma unroll
        for (int r = 0; r < 8; ++r) q[r] = slot(ent[k + (r >> 2)][r & 3]);
        SB();
#pragma unroll
        for (int r = 0; r < 8; ++r) p[k >> 1 & 1][r] = atomicAdd(q[r], 2);
        SB();
      }
      if (k) {
#pragma unroll
        for (int r = 0; r < 8; ++r) p[~k >> 1 & 1][r] = min(p[~k >> 1 & 1][r], dmp);
        SB();
#pragma unroll
        for (int r = 0; r < 8; ++r) *(lds_u16_p)p[~k >> 1 & 1][r] = ent[k - 2 + (r >> 2)][r & 3];
        SB();
      }
    }
    if (use)
#pragma unroll 1
      for (int k = NL; k < 64; ++k)
        if (use >> (k - NL) & 1) {
          u32x4 e = fetch(k); clean(e, k);
#pragma unroll
          for (int r = 0; r < 4; ++r) *(lds_u16_p)min((unsigned)atomicAdd(slot(e[r]), 2), dmp) = e[r];
        }
    __syncthreads();
    if (srt) {
#pragma unroll
      for (int i = 0; i < 10; ++i) dg[10 * tid + i] = cn[i] >> 1;
      for (unsigned long long big = __ballot(cm > 64); big; big &= big - 1)
      for (int n = 10 * (wid * 64 + __builtin_ctzll(big)), n1 = n + 10; n < n1; ++n) {
        const int d = __builtin_amdgcn_readfirstlane(dg[n]);
        if (d <= 32) continue;
        u16* f = reinterpret_cast<u16*>(lds + cnt[n]) - d;
        for (int i = lane; i < d + 8; i += 64) tmp[i] = i < d ? f[i] : 0xffff;
        __builtin_amdgcn_wave_barrier();
        for (int i = lane; i < d; i += 64) {
          const unsigned key = tmp[i] << 9 | i; int p_ = 0;
          for (int q = 0; q < d; q += 8) {
            const u32x4 v = *reinterpret_cast<u32x4*>(tmp + q);
#pragma unroll
            for (int e = 0; e < 4; ++e) p_ += (((v[e] & 0xffffu) << 9 | (q + 2 * e)) < key) + (((v[e] >> 16) << 9 | (q + 2 * e + 1)) < key);
          }
          f[p_] = key >> 9;
        }
        __builtin_amdgcn_wave_barrier();
      }
    }
  }
  __syncthreads();
#pragma unroll 8
  for (int k = 0; k < 32; ++k)
    __builtin_amdgcn_raw_buffer_store_b128(reinterpret_cast<u32x4*>(img)[k * 256 + tid], wsrd, WS_LS + j * 131072 + (k * 256 + tid) * 16, 0, 16);
  unsigned lr[16];
#pragma unroll
  for (int r = 0; r < 16; ++r) lr[r] = reinterpret_cast<const unsigned*>((wid >= 2 ? wm : wv) + (240 + r) * DD)[tid & 127];
  wait_flag(wsrd, WS_HF + min(tid, NATT - 1) * 16, tag);
  drain();
  __syncthreads();
  if (tid == 0) st_flag(wsrd, WS_LF + j * 128, tag);
  asm volatile("s_mov_b32 m0, 0x27c00\n s_nop 0\n global_load_lds_dword %0, %1" :: "v"(min(tid * 128, NN * 2 - 4)), "s"(dinv) : "m0", "memory");
  touch(WS_XF + (j * 256 + tid) * 256);
  touch(wid ? 0x40000000u : WS_GF - 32768 + (j * 64 + lane) * 128);
  corr(reinterpret_cast<unsigned*>(lds + 0x20000) + 512 + (wid >> 1) * 256 + (tid & 127), lr);
#pragma unroll 1
  for (int job = 4 * j; job < 4 * j + 4; ++job) {
    product_load(lds, wid, lane, job, wv, wm, wo);
    drain();
    __syncthreads();
    product_w(lds, wid, lane, job, wsrd);
    __syncthreads();
  }
  drain();
  __syncthreads();
  if (tid == 0) st_flag(wsrd, WS_WF + j * 4, tag);
  if (big_) {
    const srd_t msrd = __builtin_amdgcn_make_buffer_rsrc(ws + WS_H, 0, 10240 * 512, 0x00020000);
    bf16x8 b1;
#pragma unroll
    for (int e = 0; e < 8; ++e) b1[e] = (lane & 15) == e + ((lane >> 1) & 8) ? 0x3f80 : 0;
    for (int n = wid; n < nn_; n += 4) {
      f32x4 acc = {};
      for (int e0 = 0; e0 < EE; e0 += 64) {
        unsigned long long m = __ballot(dst[e0 + lane] == lo + n);
        while (m) {
          const int e = e0 + __builtin_ctzll(m); m &= m - 1;
          const unsigned vo = lane < 32 ? (unsigned)src[e] * 512 + lane * 16 : 0x7fffff00u;
          acc = MFMA16(b1, as_bf(__builtin_amdgcn_raw_buffer_load_b128(msrd, vo, 0, 16)), acc);
        }
      }
      __builtin_amdgcn_raw_buffer_store_b64((u32x2){pk2(acc[0], acc[1]), pk2(acc[2], acc[3])}, wsrd,
                                            WS_G + (lo + n) * 512 + (lane >> 5) * 256 + (lane & 15) * 16 + (lane & 16) / 2, 0, 16);
    }
    drain();
    __syncthreads();
    if (tid < 32) st_flag(wsrd, WS_GF + j * 512 + tid * 16, tag);
  }
}

extern "C" __global__ __launch_bounds__(256, 1) void gps_kernel(const u16* wq, const u16* wk, const int* src, char* ws, const int* dst, const u16* x, u16* out,
                                                                const u16* deg_inv, const u16* wv, const u16* wm, const u16* wo) {
  extern __shared__ __attribute__((aligned(16))) char lds[];
  static_assert(WS_END == 69410816 && WS_EP == 6101760, "workspace layout of the generated code");
  typedef void* const __attribute__((address_space(4)))* arg_p;
  arg_p ka; int b, wid;
  asm volatile(
    ".Lat_0:\n"
    "s_mov_b32 s92, s2\n"
    "s_load_dwordx2 s[2:3], s[0:1], 0x0\n"
    "s_load_dwordx4 s[4:7], s[0:1], 0x8\n"
    "s_load_dwordx8 s[8:15], s[0:1], 0x18\n"
    "s_load_dwordx2 s[28:29], s[0:1], 0x38\n"
    "s_getpc_b64 s[30:31]\n"
    "v_readfirstlane_b32 s93, v0\n"
    "s_bfe_u32 s45, s92, 0x40003\n"
    "v_lshlrev_b32_e32 v1, 5, v0\n"
    "s_lshl_b32 s45, s45, 13\n"
    "s_mov_b32 m0, 0x27f00\n"
    "v_add_u32_e32 v1, s45, v1\n"
    "v_min_u32_e32 v1, 76488, v1\n"
    "global_load_lds_dword v1, s[30:31]\n"
    "s_and_b32 s45, s92, 249\n"
    "s_lshr_b32 s93, s93, 6\n"
    "s_cmp_eq_u32 s45, 249\n"
    "s_cbranch_scc1 .Lat_out\n"
    "v_mbcnt_lo_u32_b32 v24, -1, 0\n"
    "v_mbcnt_hi_u32_b32 v24, -1, v24\n"
    "s_and_b32 s50, s93, 1\n"
    "s_lshr_b32 s51, s93, 1\n"
    "v_lshrrev_b32_e32 v25, 3, v24\n"
    "v_and_b32_e32 v26, 6, v25\n"
    "v_and_b32_e32 v27, 7, v24\n"
    "v_xor_b32_e32 v26, v26, v27\n"
    "v_lshlrev_b32_e32 v26, 4, v26\n"
    "v_and_b32_e32 v27, 4, v25\n"
    "v_add_u32_e32 v27, v27, v25\n"
    "v_lshl_add_u32 v251, v27, 9, v26\n"
    "v_add_u32_e32 v252, 0x1c00, v251\n"
    "s_mov_b32 s22, 0x20000\n"
    "s_mov_b32 s23, 0x20000\n"
    "s_mov_b32 s26, 0x20000\n"
    "s_mov_b32 s27, 0x20000\n"
    "s_lshr_b32 s36, s92, 7\n"
    "s_mul_i32 s36, s36, 3\n"
    "s_add_u32 s36, s36, s92\n"
    "s_and_b32 s36, s36, 127\n"
    "s_lshl_b32 s45, s93, 7\n"
    "s_lshl_b32 s46, s93, 12\n"
    "s_and_b32 s47, s36, 7\n"
    "s_lshl_b32 s47, s47, 14\n"
    "s_mov_b32 m0, s46\n"
    "s_add_u32 s47, s47, s45\n"
    "s_and_b32 s34, s92, 7\n"
    "s_lshl_b32 s34, s34, 5\n"
    "s_lshr_b32 s32, s92, 3\n"
    "s_add_u32 s34, s34, s32\n"
    "s_lshl_b32 s32, s34, 6\n"
    "s_or_b32 s32, s32, 63\n"
    "s_and_b32 s32, s32, 255\n"
    "s_cmp_ge_u32 s34, 252\n"
    "s_cselect_b32 s34, s32, s34\n"
    "s_and_b32 s32, s34, 3\n"
    "s_lshr_b32 s33, s34, 2\n"
    "s_or_b32 s38, s33, 16\n"
    "s_cmp_eq_u32 s38, 62\n"
    "s_cselect_b32 s38, 16, 0\n"
    "s_xor_b32 s33, s33, s38\n"
    "s_lshl2_add_u32 s34, s33, s32\n"
    "s_mul_i32 s38, s33, 81920\n"
    "s_lshl_b32 s35, s32, 13\n"
    "s_and_b32 s55, s35, 0x2000\n"
    "s_add_u32 s35, s35, s38\n"
    "s_add_u32 s55, s55, s38\n"
    "s_mov_b32 s39, 6101760\n"
    "s_waitcnt lgkmcnt(0)\n"
    "s_mov_b64 s[20:21], s[2:3]\n"
    "s_mov_b64 s[24:25], s[4:5]\n"
    "buffer_load_dwordx4 v251, s[20:23], s47 offen offset:0 lds\n"
    "s_add_u32 s48, s47, 1024\n"
    "buffer_load_dwordx4 v252, s[20:23], s47 offen offset:3072 lds\n"
    "s_add_u32 s47, s47, 0x1800\n"
    "buffer_load_dwordx4 v251, s[20:23], s48 offen offset:1024 lds\n"
    "buffer_load_dwordx4 v251, s[20:23], s47 offen offset:2048 lds\n"
    "s_lshr_b32 s47, s36, 4\n"
    "s_lshl_b32 s47, s47, 14\n"
    "s_and_b32 s48, s36, 8\n"
    "s_lshl_b32 s48, s48, 8\n"
    "s_add_u32 s47, s47, s48\n"
    "s_add_u32 m0, s46, 0x4000\n"
    "s_add_u32 s47, s47, s45\n"
    "buffer_load_dwordx4 v251, s[24:27], s47 offen offset:0 lds\n"
    "buffer_load_dwordx4 v252, s[24:27], s47 offen offset:1024 lds\n"
    "s_add_u32 s47, s45, 120832\n"
    "buffer_load_dwordx4 v251, s[24:27], s47 offen offset:2048 lds\n"
    "s_add_u32 s47, s47, 1024\n"
    "buffer_load_dwordx4 v251, s[24:27], s47 offen offset:3072 lds\n"
    "s_mov_b64 s[16:17], s[14:15]\n"
    "s_mov_b64 s[86:87], s[10:11]\n"
    "s_mov_b32 s14, 5120000\n"
    "s_mov_b32 s15, 0x20000\n"
    "v_mov_b32_e32 v102, s39\n"
    "global_load_dword v102, v102, s[8:9]\n"
    "s_lshl_b32 s45, s93, 2\n"
    "s_add_u32 s54, s35, 0x8000\n"
    "s_add_u32 s55, s55, 0x10000\n"
    "v_lshlrev_b32_e32 v250, 4, v24\n"
    "v_lshrrev_b32_e32 v28, 4, v24\n"
    "v_add_u32_e32 v28, s45, v28\n"
    "v_and_b32_e32 v104, 0xf0, v250\n"
    "v_lshl_add_u32 v104, v28, 9, v104\n"
    "buffer_load_dwordx4 v[0:3], v104, s[12:15], s35 offen offset:0\n"
    "buffer_load_dwordx4 v[4:7], v104, s[12:15], s35 offen offset:256\n"
    "s_cmp_lt_u32 s32, 2\n"
    "s_cselect_b32 s60, s55, 2.0\n"
    "buffer_load_dwordx4 v[8:11], v104, s[12:15], s54 offen offset:0\n"
    "buffer_load_dwordx4 v[12:15], v104, s[12:15], s54 offen offset:256\n"
    "buffer_load_dwordx4 v[16:19], v104, s[12:15], s55 offen offset:0\n"
    "buffer_load_dwordx4 v[20:23], v104, s[12:15], s55 offen offset:256\n"
    "s_min_u32 s46, s34, 249\n"
    "s_mul_i32 s46, s46, 2560\n"
    "s_lshl_b32 s47, s50, 10\n"
    "s_add_u32 s47, s47, s46\n"
    "v_add_u32_e32 v158, s47, v250\n"
    "s_lshl_b32 s47, s50, 8\n"
    "s_add_u32 s47, s47, s46\n"
    "s_add_u32 s47, s47, 2048\n"
    "v_lshl_add_u32 v159, v24, 2, s47\n"
    "s_cmp_lg_u32 s51, 0\n"
    "s_cselect_b64 exec, -1, 1\n"
    "global_load_dwordx4 v[144:147], v158, s[86:87]\n"
    "global_load_dword v148, v159, s[86:87]\n"
    "global_load_dwordx4 v[152:155], v158, s[6:7]\n"
    "global_load_dword v156, v159, s[6:7]\n"
    "s_mov_b64 exec, -1\n"
    "s_mov_b32 s10, 69410816\n"
    "s_mov_b32 s11, 0x20000\n"
    "s_mov_b32 s49, 0\n"
    "v_and_b32_e32 v25, 7, v24\n"
    "v_lshlrev_b32_e32 v27, 7, v25\n"
    "v_bfe_u32 v26, v24, 3, 1\n"
    "v_lshl_add_u32 v27, v26, 10, v27\n"
    "v_and_b32_e32 v25, 6, v24\n"
    "v_lshrrev_b32_e32 v26, 4, v24\n"
    "v_xor_b32_e32 v25, v25, v26\n"
    "v_lshl_add_u32 v240, v25, 4, v27\n"
    "s_lshl_b32 s48, s50, 11\n"
    "v_xor_b32_e32 v241, 64, v240\n"
    "v_add_u32_e32 v72, s48, v240\n"
    "v_add_u32_e32 v73, s48, v241\n"
    "s_lshl_b32 s47, s93, 10\n"
    "v_add_u32_e32 v108, s47, v250\n"
    "v_lshrrev_b32_e32 v109, 7, v108\n"
    "v_mul_u32_u24_e32 v109, 0x8200, v109\n"
    "v_and_b32_e32 v32, 0x70, v250\n"
    "v_or_b32_e32 v109, v109, v32\n"
    "v_add_u32_e32 v108, 81920, v108\n"
    "v_mov_b64_e32 v[32:33], -1\n"
    "v_mov_b64_e32 v[34:35], -1\n"
    "ds_write_b128 v108, v[32:35] offset:0\n"
    "ds_write_b128 v108, v[32:35] offset:16\n"
    "v_and_b32_e32 v29, 6, v28\n"
    "v_xor_b32_e32 v29, v29, v24\n"
    "v_and_b32_e32 v29, 7, v29\n"
    "v_lshlrev_b32_e32 v105, 7, v28\n"
    "v_lshl_add_u32 v105, v29, 4, v105\n"
    "v_bfe_u32 v29, v24, 3, 1\n"
    "v_subrev_u32_e32 v29, s32, v29\n"
    "v_and_b32_e32 v29, 3, v29\n"
    "v_lshl_add_u32 v105, v29, 11, v105\n"
    "v_add_u32_e32 v105, 32768, v105\n"
    "v_xor_b32_e32 v106, 0x1000, v105\n"
    "v_and_b32_e32 v29, 15, v24\n"
    "v_cmp_eq_u32_e32 vcc, 15, v29\n"
    "v_mov_b32_e32 v107, 0xffff\n"
    "s_mov_b32 s58, 0x3f803f80\n"
    "s_mov_b32 s57, 0x43c00000\n"
    "v_cndmask_b32_e32 v107, -1, v107, vcc\n"
    "s_mov_b32 s56, 0xc3c00000\n"
    "s_mov_b32 s62, 0xc000c000\n"
    "s_mov_b32 s63, 0xc000c000\n"
    "s_lshl_b32 s47, s93, 10\n"
    "s_add_u32 s47, s47, 6080512\n"
    "v_add_u32_e32 v100, s47, v250\n"
    "v_and_b32_e32 v29, 15, v24\n"
    "s_cmp_ge_u32 s36, 112\n"
    "s_cselect_b32 s47, 8, 16\n"
    "v_cmp_le_u32_e32 vcc, s47, v29\n"
    "v_mov_b32_e32 v64, 0xbd80bd80\n"
    "v_mov_b32_e32 v65, 0xbdabbdab\n"
    "v_mov_b32_e32 v66, 0x392b392b\n"
    "v_cndmask_b32_e32 v64, v64, v65, vcc\n"
    "v_cndmask_b32_e32 v66, 0, v66, vcc\n"
    "s_cselect_b32 s47, 56, 64\n"
    "v_cmp_le_u32_e32 vcc, s47, v24\n"
    "v_mov_b32_e32 v65, 0x3e80bdab\n"
    "v_mov_b32_e32 v67, 0x392b\n"
    "v_cndmask_b32_e32 v65, v64, v65, vcc\n"
    "v_cndmask_b32_e32 v67, v66, v67, vcc\n"
    "s_cmp_ge_u32 s36, 120\n"
    "s_cselect_b32 s47, 15, 16\n"
    "v_cmp_ne_u32_e32 vcc, s47, v29\n"
    "v_mov_b32_e32 v68, 0x3db8aa3b\n"
    "s_lshr_b32 s47, s36, 5\n"
    "v_cndmask_b32_e32 v68, 0, v68, vcc\n"
    "s_waitcnt vmcnt(11)\n"
    "s_waitcnt lgkmcnt(0)\n"
    "s_barrier\n"
    "s_cmp_lg_u32 s51, 0\n"
    "s_cbranch_scc1 .Lat_noj2\n"
    "ds_read_b128 v[112:115], v72 offset:0\n"
    "ds_read_b128 v[208:211], v240 offset:18432\n"
    "ds_read_b128 v[176:179], v240 offset:16384\n"
    "ds_read_b128 v[116:119], v73 offset:0\n"
    "ds_read_b128 v[212:215], v241 offset:18432\n"
    "ds_read_b128 v[180:183], v241 offset:16384\n"
    "ds_read_b128 v[120:123], v72 offset:4096\n"
    "ds_read_b128 v[216:219], v240 offset:22528\n"
    "ds_read_b128 v[184:187], v240 offset:20480\n"
    "ds_read_b128 v[124:127], v73 offset:4096\n"
    "ds_read_b128 v[220:223], v241 offset:22528\n"
    "ds_read_b128 v[188:191], v241 offset:20480\n"
    "ds_read_b128 v[128:131], v72 offset:8192\n"
    "ds_read_b128 v[224:227], v240 offset:26624\n"
    "ds_read_b128 v[192:195], v240 offset:24576\n"
    "s_waitcnt lgkmcnt(12)\n"
    "v_mfma_f32_16x16x32_bf16 v[56:59], v[208:211], v[112:115], 0\n"
    "ds_read_b128 v[132:135], v73 offset:8192\n"
    "ds_read_b128 v[228:231], v241 offset:26624\n"
    "v_mfma_f32_16x16x32_bf16 v[48:51], v[112:115], v[176:179], 0\n"
    "ds_read_b128 v[196:199], v241 offset:24576\n"
    "s_waitcnt lgkmcnt(12)\n"
    "v_mfma_f32_16x16x32_bf16 v[56:59], v[212:215], v[116:119], v[56:59]\n"
    "ds_read_b128 v[136:139], v72 offset:12288\n"
    "ds_read_b128 v[232:235], v240 offset:30720\n"
    "v_mfma_f32_16x16x32_bf16 v[48:51], v[116:119], v[180:183], v[48:51]\n"
    "ds_read_b128 v[200:203], v240 offset:28672\n"
    "s_waitcnt lgkmcnt(12)\n"
    "v_mfma_f32_16x16x32_bf16 v[56:59], v[216:219], v[120:123], v[56:59]\n"
    "ds_read_b128 v[140:143], v73 offset:12288\n"
    "ds_read_b128 v[236:239], v241 offset:30720\n"
    "v_mfma_f32_16x16x32_bf16 v[48:51], v[120:123], v[184:187], v[48:51]\n"
    "ds_read_b128 v[204:207], v241 offset:28672\n"
    "s_waitcnt lgkmcnt(12)\n"
    "v_mfma_f32_16x16x32_bf16 v[56:59], v[220:223], v[124:127], v[56:59]\n"
    "s_mul_i32 s47, s47, 33792\n"
    "s_and_b32 s48, s36, 7\n"
    "s_lshl_b32 s48, s48, 11\n"
    "v_mfma_f32_16x16x32_bf16 v[48:51], v[124:127], v[188:191], v[48:51]\n"
    "s_add_u32 s47, s47, s48\n"
    "s_and_b32 s48, s36, 8\n"
    "s_lshl_b32 s48, s48, 7\n"
    "s_waitcnt lgkmcnt(9)\n"
    "v_mfma_f32_16x16x32_bf16 v[56:59], v[224:227], v[128:131], v[56:59]\n"
    "s_add_u32 s47, s47, s48\n"
    "s_and_b32 s48, s36, 16\n"
    "s_lshl_b32 s48, s48, 10\n"
    "v_mfma_f32_16x16x32_bf16 v[48:51], v[128:131], v[192:195], v[48:51]\n"
    "s_add_u32 s47, s47, s48\n"
    "s_add_u32 s52, s47, 5242880\n"
    "s_lshl_b32 s48, s50, 9\n"
    "s_waitcnt lgkmcnt(6)\n"
    "v_mfma_f32_16x16x32_bf16 v[56:59], v[228:231], v[132:135], v[56:59]\n"
    "s_add_u32 s52, s52, s48\n"
    "v_and_b32_e32 v74, 0x1f0, v250\n"
    "v_lshrrev_b32_e32 v29, 5, v24\n"
    "v_mfma_f32_16x16x32_bf16 v[48:51], v[132:135], v[196:199], v[48:51]\n"
    "v_lshl_add_u32 v74, v29, 3, v74\n"
    "s_lshl1_add_u32 s53, s36, s50\n"
    "s_lshl4_add_u32 s53, s53, 6080512\n"
    "s_waitcnt lgkmcnt(3)\n"
    "v_mfma_f32_16x16x32_bf16 v[56:59], v[232:235], v[136:139], v[56:59]\n"
    "s_cmp_lg_u32 s51, 0\n"
    "v_mfma_f32_16x16x32_bf16 v[48:51], v[136:139], v[200:203], v[48:51]\n"
    "s_waitcnt lgkmcnt(0)\n"
    "v_mfma_f32_16x16x32_bf16 v[56:59], v[236:239], v[140:143], v[56:59]\n"
    "v_mfma_f32_16x16x32_bf16 v[48:51], v[140:143], v[204:207], v[48:51]\n"
    "s_nop 3\n"
    "v_cvt_pk_bf16_f32 v56, v56, v57\n"
    "v_cvt_pk_bf16_f32 v57, v58, v59\n"
    "s_nop 1\n"
    "v_mfma_f32_16x16x16_bf16 v[48:51], v[56:57], v[64:65], v[48:51]\n"
    "v_mfma_f32_16x16x16_bf16 v[48:51], v[56:57], v[66:67], v[48:51]\n"
    "s_nop 7\n"
    "v_mul_f32_e32 v48, v48, v68\n"
    "v_mul_f32_e32 v49, v49, v68\n"
    "v_mul_f32_e32 v50, v50, v68\n"
    "v_mul_f32_e32 v51, v51, v68\n"
    "v_cvt_pk_bf16_f32 v48, v48, v49\n"
    "v_cvt_pk_bf16_f32 v49, v50, v51\n"
    "buffer_store_dwordx2 v[48:49], v74, s[8:11], s52 offen offset:0 sc1\n"
    "s_waitcnt vmcnt(5)\n"
    "s_branch .Lat_ln\n"
    ".Lat_noj2:\n"
    "s_cmp_lg_u32 s93, 3\n"
    "s_cbranch_scc1 .Lat_wn10\n"
    "s_call_b64 s[2:3], .Lat_w0\n"
    "s_call_b64 s[2:3], .Lat_w2\n"
    "s_call_b64 s[2:3], .Lat_w4\n"
    ".Lat_wn10:\n"
    "s_cmp_lg_u32 s93, 2\n"
    "s_cbranch_scc1 .Lat_wn11\n"
    "s_call_b64 s[2:3], .Lat_w1\n"
    "s_call_b64 s[2:3], .Lat_w3\n"
    "s_call_b64 s[2:3], .Lat_w5\n"
    ".Lat_wn11:\n"
    "s_cmp_lg_u32 s51, 0\n"
    "s_waitcnt vmcnt(4)\n"
    ".Lat_ln:\n"
    "v_add_u32_e32 v102, 4, v102\n"
    "s_nop 1\n"
    "v_readfirstlane_b32 s39, v102\n"
    "v_dot2_f32_bf16 v24, v0, s58, 0\n"
    "v_dot2_f32_bf16 v32, v8, s58, 0\n"
    "v_dot2_f32_bf16 v40, v16, s58, 0\n"
    "v_dot2_f32_bf16 v25, v4, s58, 0\n"
    "v_dot2_f32_bf16 v33, v12, s58, 0\n"
    "v_dot2_f32_bf16 v41, v20, s58, 0\n"
    "v_dot2_f32_bf16 v26, v0, v0, 0\n"
    "v_dot2_f32_bf16 v34, v8, v8, 0\n"
    "v_dot2_f32_bf16 v42, v16, v16, 0\n"
    "v_dot2_f32_bf16 v27, v4, v4, 0\n"
    "v_dot2_f32_bf16 v35, v12, v12, 0\n"
    "v_dot2_f32_bf16 v43, v20, v20, 0\n"
    "v_dot2_f32_bf16 v24, v1, s58, v24\n"
    "v_dot2_f32_bf16 v32, v9, s58, v32\n"
    "v_dot2_f32_bf16 v40, v17, s58, v40\n"
    "v_dot2_f32_bf16 v25, v5, s58, v25\n"
    "v_dot2_f32_bf16 v33, v13, s58, v33\n"
    "v_dot2_f32_bf16 v41, v21, s58, v41\n"
    "v_dot2_f32_bf16 v26, v1, v1, v26\n"
    "v_dot2_f32_bf16 v34, v9, v9, v34\n"
    "v_dot2_f32_bf16 v42, v17, v17, v42\n"
    "v_dot2_f32_bf16 v27, v5, v5, v27\n"
    "v_dot2_f32_bf16 v35, v13, v13, v35\n"
    "v_dot2_f32_bf16 v43, v21, v21, v43\n"
    "v_dot2_f32_bf16 v24, v2, s58, v24\n"
    "v_dot2_f32_bf16 v32, v10, s58, v32\n"
    "v_dot2_f32_bf16 v40, v18, s58, v40\n"
    "v_dot2_f32_bf16 v25, v6, s58, v25\n"
    "v_dot2_f32_bf16 v33, v14, s58, v33\n"
    "v_dot2_f32_bf16 v41, v22, s58, v41\n"
    "v_dot2_f32_bf16 v26, v2, v2, v26\n"
    "v_dot2_f32_bf16 v34, v10, v10, v34\n"
    "v_dot2_f32_bf16 v42, v18, v18, v42\n"
    "v_dot2_f32_bf16 v27, v6, v6, v27\n"
    "v_dot2_f32_bf16 v35, v14, v14, v35\n"
    "v_dot2_f32_bf16 v43, v22, v22, v43\n"
    "v_dot2_f32_bf16 v24, v3, s58, v24\n"
    "v_dot2_f32_bf16 v32, v11, s58, v32\n"
    "v_dot2_f32_bf16 v40, v19, s58, v40\n"
    "v_dot2_f32_bf16 v25, v7, s58, v25\n"
    "v_dot2_f32_bf16 v33, v15, s58, v33\n"
    "v_dot2_f32_bf16 v41, v23, s58, v41\n"
    "v_dot2_f32_bf16 v26, v3, v3, v26\n"
    "v_dot2_f32_bf16 v34, v11, v11, v34\n"
    "v_dot2_f32_bf16 v42, v19, v19, v42\n"
    "v_dot2_f32_bf16 v27, v7, v7, v27\n"
    "v_dot2_f32_bf16 v35, v15, v15, v35\n"
    "v_dot2_f32_bf16 v43, v23, v23, v43\n"
    "v_and_b32_e32 v30, 0xffff0000, v7\n"
    "v_and_b32_e32 v38, 0xffff0000, v15\n"
    "v_and_b32_e32 v46, 0xffff0000, v23\n"
    "v_add_f32_e32 v24, v24, v25\n"
    "v_add_f32_e32 v32, v32, v33\n"
    "v_add_f32_e32 v40, v40, v41\n"
    "v_add_f32_e32 v26, v26, v27\n"
    "v_add_f32_e32 v34, v34, v35\n"
    "v_add_f32_e32 v42, v42, v43\n"
    "s_cbranch_scc1 .Lat_nojf\n"
    "s_waitcnt vmcnt(0)\n"
    "buffer_store_dword v102, off, s[8:11], s53 offset:0 sc1\n"
    ".Lat_nojf:\n"
    "v_add_f32_dpp v25, v25, v25 quad_perm:[1,0,3,2] row_mask:0xf bank_mask:0xf bound_ctrl:1\n"
    "v_lshlrev_b32_e32 v48, 16, v0\n"
    "v_add_f32_dpp v33, v33, v33 quad_perm:[1,0,3,2] row_mask:0xf bank_mask:0xf bound_ctrl:1\n"
    "v_and_b32_e32 v49, 0xffff0000, v0\n"
    "v_add_f32_dpp v41, v41, v41 quad_perm:[1,0,3,2] row_mask:0xf bank_mask:0xf bound_ctrl:1\n"
    "v_lshlrev_b32_e32 v50, 16, v1\n"
    "v_mov_b32_dpp v30, v30 quad_perm:[3,3,3,3] row_mask:0xf bank_mask:0xf bound_ctrl:1\n"
    "v_and_b32_e32 v51, 0xffff0000, v1\n"
    "v_mov_b32_dpp v38, v38 quad_perm:[3,3,3,3] row_mask:0xf bank_mask:0xf bound_ctrl:1\n"
    "v_lshlrev_b32_e32 v52, 16, v2\n"
    "v_mov_b32_dpp v46, v46 quad_perm:[3,3,3,3] row_mask:0xf bank_mask:0xf bound_ctrl:1\n"
    "v_and_b32_e32 v53, 0xffff0000, v2\n"
    "v_add_f32_dpp v24, v24, v24 quad_perm:[1,0,3,2] row_mask:0xf bank_mask:0xf bound_ctrl:1\n"
    "v_lshlrev_b32_e32 v54, 16, v3\n"
    "v_add_f32_dpp v32, v32, v32 quad_perm:[1,0,3,2] row_mask:0xf bank_mask:0xf bound_ctrl:1\n"
    "v_and_b32_e32 v55, 0xffff0000, v3\n"
    "v_add_f32_dpp v40, v40, v40 quad_perm:[1,0,3,2] row_mask:0xf bank_mask:0xf bound_ctrl:1\n"
    "v_lshlrev_b32_e32 v56, 16, v4\n"
    "v_add_f32_dpp v26, v26, v26 quad_perm:[1,0,3,2] row_mask:0xf bank_mask:0xf bound_ctrl:1\n"
    "v_and_b32_e32 v57, 0xffff0000, v4\n"
    "v_add_f32_dpp v34, v34, v34 quad_perm:[1,0,3,2] row_mask:0xf bank_mask:0xf bound_ctrl:1\n"
    "v_lshlrev_b32_e32 v58, 16, v5\n"
    "v_add_f32_dpp v42, v42, v42 quad_perm:[1,0,3,2] row_mask:0xf bank_mask:0xf bound_ctrl:1\n"
    "v_and_b32_e32 v59, 0xffff0000, v5\n"
    "v_add_f32_dpp v24, v24, v24 quad_perm:[2,3,0,1] row_mask:0xf bank_mask:0xf bound_ctrl:1\n"
    "v_lshlrev_b32_e32 v60, 16, v6\n"
    "v_add_f32_dpp v32, v32, v32 quad_perm:[2,3,0,1] row_mask:0xf bank_mask:0xf bound_ctrl:1\n"
    "v_and_b32_e32 v61, 0xffff0000, v6\n"
    "v_add_f32_dpp v40, v40, v40 quad_perm:[2,3,0,1] row_mask:0xf bank_mask:0xf bound_ctrl:1\n"
    "v_lshlrev_b32_e32 v62, 16, v7\n"
    "v_add_f32_dpp v26, v26, v26 quad_perm:[2,3,0,1] row_mask:0xf bank_mask:0xf bound_ctrl:1\n"
    "v_and_b32_e32 v63, 0xffff0000, v7\n"
    "v_add_f32_dpp v34, v34, v34 quad_perm:[2,3,0,1] row_mask:0xf bank_mask:0xf bound_ctrl:1\n"
    "v_lshlrev_b32_e32 v64, 16, v8\n"
    "v_add_f32_dpp v42, v42, v42 quad_perm:[2,3,0,1] row_mask:0xf bank_mask:0xf bound_ctrl:1\n"
    "v_and_b32_e32 v65, 0xffff0000, v8\n"
    "v_add_f32_dpp v24, v24, v24 row_half_mirror row_mask:0xf bank_mask:0xf bound_ctrl:1\n"
    "v_lshlrev_b32_e32 v66, 16, v9\n"
    "v_add_f32_dpp v32, v32, v32 row_half_mirror row_mask:0xf bank_mask:0xf bound_ctrl:1\n"
    "v_and_b32_e32 v67, 0xffff0000, v9\n"
    "v_add_f32_dpp v40, v40, v40 row_half_mirror row_mask:0xf bank_mask:0xf bound_ctrl:1\n"
    "v_lshlrev_b32_e32 v68, 16, v10\n"
    "v_add_f32_dpp v26, v26, v26 row_half_mirror row_mask:0xf bank_mask:0xf bound_ctrl:1\n"
    "v_and_b32_e32 v69, 0xffff0000, v10\n"
    "v_add_f32_dpp v34, v34, v34 row_half_mirror row_mask:0xf bank_mask:0xf bound_ctrl:1\n"
    "v_lshlrev_b32_e32 v70, 16, v11\n"
    "v_add_f32_dpp v42, v42, v42 row_half_mirror row_mask:0xf bank_mask:0xf bound_ctrl:1\n"
    "v_and_b32_e32 v71, 0xffff0000, v11\n"
    "v_add_f32_dpp v24, v24, v24 row_mirror row_mask:0xf bank_mask:0xf bound_ctrl:1\n"
    "v_lshlrev_b32_e32 v72, 16, v12\n"
    "v_add_f32_dpp v32, v32, v32 row_mirror row_mask:0xf bank_mask:0xf bound_ctrl:1\n"
    "v_and_b32_e32 v73, 0xffff0000, v12\n"
    "v_add_f32_dpp v40, v40, v40 row_mirror row_mask:0xf bank_mask:0xf bound_ctrl:1\n"
    "v_lshlrev_b32_e32 v74, 16, v13\n"
    "v_add_f32_dpp v26, v26, v26 row_mirror row_mask:0xf bank_mask:0xf bound_ctrl:1\n"
    "v_and_b32_e32 v75, 0xffff0000, v13\n"
    "v_add_f32_dpp v34, v34, v34 row_mirror row_mask:0xf bank_mask:0xf bound_ctrl:1\n"
    "v_lshlrev_b32_e32 v76, 16, v14\n"
    "v_add_f32_dpp v42, v42, v42 row_mirror row_mask:0xf bank_mask:0xf bound_ctrl:1\n"
    "v_and_b32_e32 v77, 0xffff0000, v14\n"
    "v_mul_f32_e32 v24, 0x3b800000, v24\n"
    "v_lshlrev_b32_e32 v78, 16, v15\n"
    "v_mul_f32_e32 v32, 0x3b800000, v32\n"
    "v_and_b32_e32 v79, 0xffff0000, v15\n"
    "v_mul_f32_e32 v40, 0x3b800000, v40\n"
    "v_lshlrev_b32_e32 v80, 16, v16\n"
    "v_mul_f32_e32 v25, 0.25, v25\n"
    "v_and_b32_e32 v81, 0xffff0000, v16\n"
    "v_mul_f32_e32 v33, 0.25, v33\n"
    "v_lshlrev_b32_e32 v82, 16, v17\n"
    "v_mul_f32_e32 v41, 0.25, v41\n"
    "v_and_b32_e32 v83, 0xffff0000, v17\n"
    "v_mul_f32_e64 v27, v24, -v24\n"
    "v_lshlrev_b32_e32 v84, 16, v18\n"
    "v_mul_f32_e64 v35, v32, -v32\n"
    "v_and_b32_e32 v85, 0xffff0000, v18\n"
    "v_mul_f32_e64 v43, v40, -v40\n"
    "v_lshlrev_b32_e32 v86, 16, v19\n"
    "v_sub_f32_e32 v25, v25, v30\n"
    "v_and_b32_e32 v87, 0xffff0000, v19\n"
    "v_sub_f32_e32 v33, v33, v38\n"
    "v_lshlrev_b32_e32 v88, 16, v20\n"
    "v_sub_f32_e32 v41, v41, v46\n"
    "v_and_b32_e32 v89, 0xffff0000, v20\n"
    "v_fmac_f32_e32 v27, 0x3b800000, v26\n"
    "v_lshlrev_b32_e32 v90, 16, v21\n"
    "v_fmac_f32_e32 v35, 0x3b800000, v34\n"
    "v_and_b32_e32 v91, 0xffff0000, v21\n"
    "v_fmac_f32_e32 v43, 0x3b800000, v42\n"
    "v_lshlrev_b32_e32 v92, 16, v22\n"
    "v_mul_f32_e32 v25, 0x3eaaaaab, v25\n"
    "v_and_b32_e32 v93, 0xffff0000, v22\n"
    "v_mul_f32_e32 v33, 0x3eaaaaab, v33\n"
    "v_lshlrev_b32_e32 v94, 16, v23\n"
    "v_mul_f32_e32 v41, 0x3eaaaaab, v41\n"
    "v_and_b32_e32 v95, 0xffff0000, v23\n"
    "v_max_f32_e32 v27, 0, v27\n"
    "v_max_f32_e32 v35, 0, v35\n"
    "v_max_f32_e32 v43, 0, v43\n"
    "s_cbranch_scc0 .Lat_bin\n"
    "s_movk_i32 s45, -2560\n"
    "s_mov_b32 s48, 81920\n"
    "v_mov_b32_e32 v172, 1\n"
    "s_waitcnt vmcnt(0)\n"
    "v_mul_u32_u24_e32 v160, 0x199a, v144\n"
    "v_mul_u32_u24_e32 v161, 0x199a, v145\n"
    "v_mul_u32_u24_e32 v162, 0x199a, v146\n"
    "v_mul_u32_u24_e32 v163, 0x199a, v147\n"
    "v_mul_u32_u24_e32 v164, 0x199a, v148\n"
    "v_bfe_u32 v160, v160, 24, 2\n"
    "v_bfe_u32 v161, v161, 24, 2\n"
    "v_bfe_u32 v162, v162, 24, 2\n"
    "v_bfe_u32 v163, v163, 24, 2\n"
    "v_bfe_u32 v164, v164, 24, 2\n"
    "v_lshl_add_u32 v166, v160, 2, s48\n"
    "v_lshl_add_u32 v167, v161, 2, s48\n"
    "v_lshl_add_u32 v168, v162, 2, s48\n"
    "v_lshl_add_u32 v169, v163, 2, s48\n"
    "v_lshl_add_u32 v170, v164, 2, s48\n"
    "ds_add_rtn_u32 v166, v166, v172 offset:4096\n"
    "ds_add_rtn_u32 v167, v167, v172 offset:4096\n"
    "ds_add_rtn_u32 v168, v168, v172 offset:4096\n"
    "ds_add_rtn_u32 v169, v169, v172 offset:4096\n"
    "ds_add_rtn_u32 v170, v170, v172 offset:4096\n"
    "v_mad_i32_i24 v144, v160, s45, v144\n"
    "v_mad_i32_i24 v145, v161, s45, v145\n"
    "v_mad_i32_i24 v146, v162, s45, v146\n"
    "v_mad_i32_i24 v147, v163, s45, v147\n"
    "v_mad_i32_i24 v148, v164, s45, v148\n"
    "v_lshl_or_b32 v144, v144, 18, v152\n"
    "v_lshl_or_b32 v145, v145, 18, v153\n"
    "v_lshl_or_b32 v146, v146, 18, v154\n"
    "v_lshl_or_b32 v147, v147, 18, v155\n"
    "v_lshl_or_b32 v148, v148, 18, v156\n"
    "v_lshl_add_u32 v160, v160, 10, s48\n"
    "v_lshl_add_u32 v161, v161, 10, s48\n"
    "v_lshl_add_u32 v162, v162, 10, s48\n"
    "v_lshl_add_u32 v163, v163, 10, s48\n"
    "v_lshl_add_u32 v164, v164, 10, s48\n"
    ".Lat_bin:\n"
    "v_cndmask_b32_e64 v25, v24, v25, s[62:63]\n"
    "v_cndmask_b32_e64 v33, v32, v33, s[62:63]\n"
    "v_cndmask_b32_e64 v41, v40, v41, s[62:63]\n"
    "v_add_f32_e32 v27, 0x3727c5ac, v27\n"
    "v_add_f32_e32 v35, 0x3727c5ac, v35\n"
    "v_add_f32_e32 v43, 0x3727c5ac, v43\n"
    "v_rsq_f32_e32 v28, v27\n"
    "v_rsq_f32_e32 v36, v35\n"
    "v_rsq_f32_e32 v44, v43\n"
    "v_fma_f32 v29, -v24, v28, s57\n"
    "v_fma_f32 v37, -v32, v36, s57\n"
    "v_fma_f32 v45, -v40, v44, s57\n"
    "v_fma_f32 v31, -v25, v28, s57\n"
    "v_fma_f32 v39, -v33, v36, s57\n"
    "v_fma_f32 v47, -v41, v44, s57\n"
    "v_sub_f32_e32 v24, v29, v31\n"
    "v_sub_f32_e32 v32, v37, v39\n"
    "v_sub_f32_e32 v40, v45, v47\n"
    "v_pk_fma_f32 v[48:49], v[48:49], v[28:29], v[28:29] op_sel:[0,0,1] op_sel_hi:[1,0,1]\n"
    "v_pk_fma_f32 v[50:51], v[50:51], v[28:29], v[28:29] op_sel:[0,0,1] op_sel_hi:[1,0,1]\n"
    "v_pk_fma_f32 v[52:53], v[52:53], v[28:29], v[28:29] op_sel:[0,0,1] op_sel_hi:[1,0,1]\n"
    "v_pk_fma_f32 v[54:55], v[54:55], v[28:29], v[28:29] op_sel:[0,0,1] op_sel_hi:[1,0,1]\n"
    "v_pk_fma_f32 v[56:57], v[56:57], v[28:29], v[30:31] op_sel:[0,0,1] op_sel_hi:[1,0,1]\n"
    "v_pk_fma_f32 v[58:59], v[58:59], v[28:29], v[30:31] op_sel:[0,0,1] op_sel_hi:[1,0,1]\n"
    "v_pk_fma_f32 v[60:61], v[60:61], v[28:29], v[30:31] op_sel:[0,0,1] op_sel_hi:[1,0,1]\n"
    "v_pk_fma_f32 v[62:63], v[62:63], v[28:29], v[30:31] op_sel:[0,0,1] op_sel_hi:[1,0,1]\n"
    "v_pk_add_f32 v[48:49], v[48:49], s[56:57] op_sel_hi:[1,0]\n"
    "v_pk_add_f32 v[50:51], v[50:51], s[56:57] op_sel_hi:[1,0]\n"
    "v_pk_add_f32 v[52:53], v[52:53], s[56:57] op_sel_hi:[1,0]\n"
    "v_pk_add_f32 v[54:55], v[54:55], s[56:57] op_sel_hi:[1,0]\n"
    "v_pk_add_f32 v[56:57], v[56:57], s[56:57] op_sel_hi:[1,0]\n"
    "v_pk_add_f32 v[58:59], v[58:59], s[56:57] op_sel_hi:[1,0]\n"
    "v_pk_add_f32 v[60:61], v[60:61], s[56:57] op_sel_hi:[1,0]\n"
    "v_pk_add_f32 v[62:63], v[62:63], s[56:57] op_sel_hi:[1,0]\n"
    "v_cvt_pk_bf16_f32 v0, v48, v49\n"
    "v_cvt_pk_bf16_f32 v1, v50, v51\n"
    "v_cvt_pk_bf16_f32 v2, v52, v53\n"
    "v_cvt_pk_bf16_f32 v3, v54, v55\n"
    "v_cvt_pk_bf16_f32 v4, v56, v57\n"
    "v_cvt_pk_bf16_f32 v5, v58, v59\n"
    "v_cvt_pk_bf16_f32 v6, v60, v61\n"
    "v_cvt_pk_bf16_f32 v7, v62, v63\n"
    "buffer_store_dwordx4 v[0:3], v104, s[8:11], s35 offen offset:0 sc1\n"
    "v_bfi_b32 v7, v107, v7, 1.0\n"
    "ds_write_b128 v105, v[0:3] offset:0\n"
    "buffer_store_dwordx4 v[4:7], v104, s[8:11], s35 offen offset:256 sc1\n"
    "v_pk_fma_f32 v[64:65], v[64:65], v[36:37], v[36:37] op_sel:[0,0,1] op_sel_hi:[1,0,1]\n"
    "v_pk_fma_f32 v[66:67], v[66:67], v[36:37], v[36:37] op_sel:[0,0,1] op_sel_hi:[1,0,1]\n"
    "v_pk_fma_f32 v[68:69], v[68:69], v[36:37], v[36:37] op_sel:[0,0,1] op_sel_hi:[1,0,1]\n"
    "v_pk_fma_f32 v[70:71], v[70:71], v[36:37], v[36:37] op_sel:[0,0,1] op_sel_hi:[1,0,1]\n"
    "v_pk_fma_f32 v[72:73], v[72:73], v[36:37], v[38:39] op_sel:[0,0,1] op_sel_hi:[1,0,1]\n"
    "v_pk_fma_f32 v[74:75], v[74:75], v[36:37], v[38:39] op_sel:[0,0,1] op_sel_hi:[1,0,1]\n"
    "v_pk_fma_f32 v[76:77], v[76:77], v[36:37], v[38:39] op_sel:[0,0,1] op_sel_hi:[1,0,1]\n"
    "v_pk_fma_f32 v[78:79], v[78:79], v[36:37], v[38:39] op_sel:[0,0,1] op_sel_hi:[1,0,1]\n"
    "v_pk_add_f32 v[64:65], v[64:65], s[56:57] op_sel_hi:[1,0]\n"
    "v_pk_add_f32 v[66:67], v[66:67], s[56:57] op_sel_hi:[1,0]\n"
    "v_pk_add_f32 v[68:69], v[68:69], s[56:57] op_sel_hi:[1,0]\n"
    "v_pk_add_f32 v[70:71], v[70:71], s[56:57] op_sel_hi:[1,0]\n"
    "v_pk_add_f32 v[72:73], v[72:73], s[56:57] op_sel_hi:[1,0]\n"
    "v_pk_add_f32 v[74:75], v[74:75], s[56:57] op_sel_hi:[1,0]\n"
    "v_pk_add_f32 v[76:77], v[76:77], s[56:57] op_sel_hi:[1,0]\n"
    "v_pk_add_f32 v[78:79], v[78:79], s[56:57] op_sel_hi:[1,0]\n"
    "v_cvt_pk_bf16_f32 v8, v64, v65\n"
    "v_cvt_pk_bf16_f32 v9, v66, v67\n"
    "v_cvt_pk_bf16_f32 v10, v68, v69\n"
    "v_cvt_pk_bf16_f32 v11, v70, v71\n"
    "v_cvt_pk_bf16_f32 v12, v72, v73\n"
    "v_cvt_pk_bf16_f32 v13, v74, v75\n"
    "v_cvt_pk_bf16_f32 v14, v76, v77\n"
    "v_cvt_pk_bf16_f32 v15, v78, v79\n"
    "buffer_store_dwordx4 v[8:11], v104, s[8:11], s54 offen offset:0 sc1\n"
    "v_bfi_b32 v15, v107, v15, 1.0\n"
    "ds_write_b128 v105, v[8:11] offset:8192\n"
    "buffer_store_dwordx4 v[12:15], v104, s[8:11], s54 offen offset:256 sc1\n"
    "v_pk_fma_f32 v[80:81], v[80:81], v[44:45], v[44:45] op_sel:[0,0,1] op_sel_hi:[1,0,1]\n"
    "v_pk_fma_f32 v[82:83], v[82:83], v[44:45], v[44:45] op_sel:[0,0,1] op_sel_hi:[1,0,1]\n"
    "v_pk_fma_f32 v[84:85], v[84:85], v[44:45], v[44:45] op_sel:[0,0,1] op_sel_hi:[1,0,1]\n"
    "v_pk_fma_f32 v[86:87], v[86:87], v[44:45], v[44:45] op_sel:[0,0,1] op_sel_hi:[1,0,1]\n"
    "v_pk_fma_f32 v[88:89], v[88:89], v[44:45], v[46:47] op_sel:[0,0,1] op_sel_hi:[1,0,1]\n"
    "v_pk_fma_f32 v[90:91], v[90:91], v[44:45], v[46:47] op_sel:[0,0,1] op_sel_hi:[1,0,1]\n"
    "v_pk_fma_f32 v[92:93], v[92:93], v[44:45], v[46:47] op_sel:[0,0,1] op_sel_hi:[1,0,1]\n"
    "v_pk_fma_f32 v[94:95], v[94:95], v[44:45], v[46:47] op_sel:[0,0,1] op_sel_hi:[1,0,1]\n"
    "v_pk_add_f32 v[80:81], v[80:81], s[56:57] op_sel_hi:[1,0]\n"
    "v_pk_add_f32 v[82:83], v[82:83], s[56:57] op_sel_hi:[1,0]\n"
    "v_pk_add_f32 v[84:85], v[84:85], s[56:57] op_sel_hi:[1,0]\n"
    "v_pk_add_f32 v[86:87], v[86:87], s[56:57] op_sel_hi:[1,0]\n"
    "v_pk_add_f32 v[88:89], v[88:89], s[56:57] op_sel_hi:[1,0]\n"
    "v_pk_add_f32 v[90:91], v[90:91], s[56:57] op_sel_hi:[1,0]\n"
    "v_pk_add_f32 v[92:93], v[92:93], s[56:57] op_sel_hi:[1,0]\n"
    "v_pk_add_f32 v[94:95], v[94:95], s[56:57] op_sel_hi:[1,0]\n"
    "v_cvt_pk_bf16_f32 v16, v80, v81\n"
    "v_cvt_pk_bf16_f32 v17, v82, v83\n"
    "v_cvt_pk_bf16_f32 v18, v84, v85\n"
    "v_cvt_pk_bf16_f32 v19, v86, v87\n"
    "v_cvt_pk_bf16_f32 v20, v88, v89\n"
    "v_cvt_pk_bf16_f32 v21, v90, v91\n"
    "v_cvt_pk_bf16_f32 v22, v92, v93\n"
    "v_cvt_pk_bf16_f32 v23, v94, v95\n"
    "buffer_store_dwordx4 v[16:19], v104, s[8:11], s60 offen offset:0 sc1\n"
    "v_bfi_b32 v23, v107, v23, 1.0\n"
    "ds_write_b128 v105, v[16:19] offset:16384\n"
    "buffer_store_dwordx4 v[20:23], v104, s[8:11], s60 offen offset:256 sc1\n"
    "v_pk_add_f32 v[56:57], v[56:57], v[24:25] op_sel_hi:[1,0]\n"
    "v_pk_add_f32 v[58:59], v[58:59], v[24:25] op_sel_hi:[1,0]\n"
    "v_pk_add_f32 v[60:61], v[60:61], v[24:25] op_sel_hi:[1,0]\n"
    "v_pk_add_f32 v[62:63], v[62:63], v[24:25] op_sel_hi:[1,0]\n"
    "v_cvt_pk_bf16_f32 v56, v56, v57\n"
    "v_cvt_pk_bf16_f32 v57, v58, v59\n"
    "v_cvt_pk_bf16_f32 v58, v60, v61\n"
    "v_cvt_pk_bf16_f32 v59, v62, v63\n"
    "ds_write_b128 v106, v[56:59] offset:0\n"
    "v_pk_add_f32 v[72:73], v[72:73], v[32:33] op_sel_hi:[1,0]\n"
    "v_pk_add_f32 v[74:75], v[74:75], v[32:33] op_sel_hi:[1,0]\n"
    "v_pk_add_f32 v[76:77], v[76:77], v[32:33] op_sel_hi:[1,0]\n"
    "v_pk_add_f32 v[78:79], v[78:79], v[32:33] op_sel_hi:[1,0]\n"
    "v_cvt_pk_bf16_f32 v72, v72, v73\n"
    "v_cvt_pk_bf16_f32 v73, v74, v75\n"
    ".long 0xbedf00ff\n"
    ".Lat_w0:\n"
    "s_setpc_b64 s[2:3]\n"
    "v_cvt_pk_bf16_f32 v74, v76, v77\n"
    "v_cvt_pk_bf16_f32 v75, v78, v79\n"
    "ds_write_b128 v106, v[72:75] offset:8192\n"
    "v_pk_add_f32 v[88:89], v[88:89], v[40:41] op_sel_hi:[1,0]\n"
    "v_pk_add_f32 v[90:91], v[90:91], v[40:41] op_sel_hi:[1,0]\n"
    "v_pk_add_f32 v[92:93], v[92:93], v[40:41] op_sel_hi:[1,0]\n"
    "v_pk_add_f32 v[94:95], v[94:95], v[40:41] op_sel_hi:[1,0]\n"
    "v_cvt_pk_bf16_f32 v88, v88, v89\n"
    "v_cvt_pk_bf16_f32 v89, v90, v91\n"
    "v_cvt_pk_bf16_f32 v90, v92, v93\n"
    "v_cvt_pk_bf16_f32 v91, v94, v95\n"
    "ds_write_b128 v106, v[88:91] offset:16384\n"
    "s_cbranch_scc0 .Lat_bin2\n"
    "s_waitcnt lgkmcnt(0)\n"
    "v_min_i32_e32 v166, 254, v166\n"
    "v_min_i32_e32 v167, 254, v167\n"
    "v_min_i32_e32 v168, 254, v168\n"
    "v_min_i32_e32 v169, 254, v169\n"
    "v_min_i32_e32 v170, 254, v170\n"
    "v_lshl_add_u32 v160, v166, 2, v160\n"
    "v_lshl_add_u32 v161, v167, 2, v161\n"
    "v_lshl_add_u32 v162, v168, 2, v162\n"
    "v_lshl_add_u32 v163, v169, 2, v163\n"
    "v_lshl_add_u32 v164, v170, 2, v164\n"
    "ds_write_b32 v160, v144 offset:4\n"
    "ds_write_b32 v161, v145 offset:4\n"
    "ds_write_b32 v162, v146 offset:4\n"
    "ds_write_b32 v163, v147 offset:4\n"
    "ds_write_b32 v164, v148 offset:4\n"
    ".Lat_bin2:\n"
    "s_waitcnt lgkmcnt(0)\n"
    "s_barrier\n"
    "ds_read_b128 a[0:3], v240 offset:32768\n"
    "ds_read_b128 a[32:35], v240 offset:40960\n"
    "ds_read_b128 a[64:67], v240 offset:49152\n"
    "ds_read_b128 a[4:7], v241 offset:32768\n"
    "ds_read_b128 a[36:39], v241 offset:40960\n"
    "ds_read_b128 a[68:71], v241 offset:49152\n"
    "ds_read_b128 a[8:11], v240 offset:34816\n"
    "ds_read_b128 a[40:43], v240 offset:43008\n"
    "ds_read_b128 a[72:75], v240 offset:51200\n"
    "ds_read_b128 a[12:15], v241 offset:34816\n"
    "ds_read_b128 a[44:47], v241 offset:43008\n"
    "ds_read_b128 a[76:79], v241 offset:51200\n"
    "buffer_load_dword v101, v100, s[8:11], 0 offen offset:0 sc1\n"
    "ds_read_b128 a[16:19], v240 offset:36864\n"
    "ds_read_b128 a[48:51], v240 offset:45056\n"
    "ds_read_b128 a[80:83], v240 offset:53248\n"
    "ds_read_b128 a[20:23], v241 offset:36864\n"
    "ds_read_b128 a[52:55], v241 offset:45056\n"
    "ds_read_b128 a[84:87], v241 offset:53248\n"
    "ds_read_b128 a[24:27], v240 offset:38912\n"
    "ds_read_b128 a[56:59], v240 offset:47104\n"
    "ds_read_b128 a[88:91], v240 offset:55296\n"
    "ds_read_b128 a[28:31], v241 offset:38912\n"
    "ds_read_b128 a[60:63], v241 offset:47104\n"
    "ds_read_b128 a[92:95], v241 offset:55296\n"
    "ds_read_b128 v[40:43], v108 offset:0\n"
    "s_mul_i32 s52, s93, 33792\n"
    "s_add_u32 s52, s52, 5242880\n"
    "s_lshl_b32 s61, s32, 12\n"
    "s_lshl4_add_u32 s46, s34, 6076416\n"
    "s_cmp_eq_u32 s93, 0\n"
    "s_cselect_b32 s46, s46, 2.0\n"
    "s_lshl_b32 s48, s34, 7\n"
    "s_add_u32 s48, s48, 68206592\n"
    "s_lshl_b32 s47, s93, 12\n"
    "s_waitcnt vmcnt(1)\n"
    "s_waitcnt lgkmcnt(0)\n"
    "s_barrier\n"
    "buffer_store_dword v102, off, s[8:11], s46 offset:0 sc1\n"
    "buffer_store_dwordx4 v[40:43], v109, s[8:11], s48 offen offset:0 sc1\n"
    "s_lshl4_add_u32 s46, s34, s47\n"
    "s_add_u32 s46, s46, 6084608\n"
    "s_waitcnt vmcnt(2)\n"
    "v_cmp_ne_u32_e32 vcc, s39, v101\n"
    "s_nop 4\n"
    "s_cbranch_vccz .Lat_apoll_ret\n"
    ".Lat_apoll:\n"
    "s_mov_b32 s53, 0x400000\n"
    ".Lat_apoll_l:\n"
    "s_sleep 1\n"
    "buffer_load_dword v101, v100, s[8:11], 0 offen offset:0 sc1\n"
    "s_waitcnt vmcnt(0)\n"
    "v_cmp_ne_u32_e32 vcc, s39, v101\n"
    "s_sub_u32 s53, s53, 1\n"
    "s_cbranch_scc1 .Lat_apoll_ret\n"
    "s_nop 2\n"
    "s_cbranch_vccnz .Lat_apoll_l\n"
    ".Lat_apoll_ret:\n"
    "s_add_u32 s47, s52, s61\n"
    "buffer_load_dwordx4 v[112:115], v250, s[8:11], s47 offen offset:0 sc1\n"
    "buffer_load_dwordx4 v[116:119], v250, s[8:11], s47 offen offset:1024 sc1\n"
    "buffer_load_dwordx4 v[128:131], v250, s[8:11], s47 offen offset:2048 sc1\n"
    "s_add_u32 s61, s61, 0x1000\n"
    "buffer_load_dwordx4 v[132:135], v250, s[8:11], s47 offen offset:3072 sc1\n"
    "s_bitset0_b32 s61, 14\n"
    "s_add_u32 s47, s52, s61\n"
    "buffer_load_dwordx4 v[144:147], v250, s[8:11], s47 offen offset:0 sc1\n"
    "buffer_load_dwordx4 v[148:151], v250, s[8:11], s47 offen offset:1024 sc1\n"
    "buffer_load_dwordx4 v[160:163], v250, s[8:11], s47 offen offset:2048 sc1\n"
    "s_add_u32 s61, s61, 0x1000\n"
    "buffer_load_dwordx4 v[164:167], v250, s[8:11], s47 offen offset:3072 sc1\n"
    "s_bitset0_b32 s61, 14\n"
    "s_add_u32 s47, s52, s61\n"
    "buffer_load_dwordx4 v[176:179], v250, s[8:11], s47 offen offset:0 sc1\n"
    "buffer_load_dwordx4 v[180:183], v250, s[8:11], s47 offen offset:1024 sc1\n"
    "buffer_load_dwordx4 v[192:195], v250, s[8:11], s47 offen offset:2048 sc1\n"
    "s_add_u32 s61, s61, 0x1000\n"
    "v_lshrrev_b32_e32 v8, 4, v250\n"
    "v_and_b32_e32 v10, 0x70, v251\n"
    "v_bfe_u32 v9, v8, 3, 1\n"
    "v_lshl_add_u32 v248, v9, 9, v10\n"
    "v_lshrrev_b32_e32 v9, 4, v8\n"
    "v_lshl_add_u32 v248, v9, 12, v248\n"
    "s_mul_i32 s43, s50, 3\n"
    "s_xor_b32 s43, s43, s51\n"
    "s_lshl_b32 s43, s43, 15\n"
    "s_add_u32 s43, s43, 32768\n"
    "s_mul_i32 s42, s93, 5120\n"
    "s_lshl_b32 s45, s50, 8\n"
    "s_mul_i32 s40, s32, 1277952\n"
    "s_lshl_b32 s41, s51, 14\n"
    "s_add_u32 s40, s40, s41\n"
    "s_add_u32 s40, s40, s45\n"
    "s_add_u32 s41, s40, 128\n"
    "v_bfe_u32 v9, v8, 2, 3\n"
    "v_lshlrev_b32_e32 v244, 7, v9\n"
    "v_lshrrev_b32_e32 v10, 5, v8\n"
    "v_lshl_add_u32 v244, v10, 10, v244\n"
    "v_and_b32_e32 v9, 6, v9\n"
    "v_bfe_u32 v10, v8, 1, 1\n"
    "v_xor_b32_e32 v9, v9, v10\n"
    "v_lshl_add_u32 v244, v9, 4, v244\n"
    "v_and_b32_e32 v10, 1, v8\n"
    "v_lshl_add_u32 v244, v10, 3, v244\n"
    "s_lshl_b32 s45, s51, 16\n"
    "s_add_u32 s45, s45, 32768\n"
    "v_add_u32_e32 v244, s45, v244\n"
    "v_xor_b32_e32 v245, 32, v244\n"
    "v_xor_b32_e32 v246, 64, v244\n"
    "v_xor_b32_e32 v247, 0x60, v244\n"
    "s_lshl_b32 s45, s51, 15\n"
    "s_add_u32 s48, s45, 32768\n"
    "s_sub_u32 s45, 131072, s45\n"
    "v_add_u32_e32 v242, s45, v240\n"
    "v_add_u32_e32 v240, s48, v240\n"
    "v_xor_b32_e32 v241, 64, v240\n"
    "v_xor_b32_e32 v243, 64, v242\n"
    "s_xor_b32 s45, s93, 2\n"
    "s_mul_i32 s45, s45, 5120\n"
    "v_lshl_add_u32 v249, v8, 2, s45\n"
    "s_lshl_b32 s45, s93, 11\n"
    "v_add_u32_e32 v99, s45, v250\n"
    "v_add_u32_e32 v98, 11345920, v99\n"
    "v_and_b32_e32 v252, 0x30, v250\n"
    "v_add_u32_e32 v252, 0x400, v252\n"
    "s_mul_i32 s59, s32, 999\n"
    "s_andn2_b32 s59, s59, 63\n"
    "s_sub_u32 s48, 3968, s59\n"
    "v_cmp_le_u32_e32 vcc, 0x80, v250\n"
    "v_mov_b32_e32 v80, s48\n"
    "s_add_u32 s59, s59, 6076416\n"
    "v_cndmask_b32_e32 v83, v250, v80, vcc\n"
    "v_bfe_u32 v253, v250, 4, 2\n"
    "v_max_u32_e32 v253, 1, v253\n"
    "v_add_u32_e32 v253, s32, v253\n"
    "v_and_b32_e32 v253, 3, v253\n"
    "v_lshlrev_b32_e32 v253, 9, v253\n"
    "v_mov_b32_e32 v84, s39\n"
    "v_lshrrev_b32_e32 v86, 2, v250\n"
    "v_min_u32_e32 v86, 124, v86\n"
    "s_lshl2_add_u32 s58, s34, s93\n"
    "s_lshl_b32 s58, s58, 7\n"
    "s_add_u32 s58, s58, 5910528\n"
    "s_lshl_b32 s45, s50, 13\n"
    "v_add_u32_e32 v85, s45, v250\n"
    "s_lshl3_add_u32 s45, s45, 49152\n"
    "v_add_u32_e32 v255, s45, v250\n"
    "v_lshrrev_b32_e32 v105, 7, v250\n"
    "v_and_b32_e32 v104, 0x70, v251\n"
    "v_lshl_add_u32 v104, v105, 9, v104\n"
    "v_add_u32_e32 v105, 0xc00, v104\n"
    "v_add_u32_e32 v106, 0x1800, v104\n"
    "v_add_u32_e32 v107, 0x2400, v104\n"
    "s_lshl_b32 s57, s93, 7\n"
    "s_lshl4_add_u32 s56, s57, 24576\n"
    "s_add_u32 s57, s57, 5111808\n"
    "s_mul_i32 s45, s51, 32768\n"
    "s_add_u32 s45, s45, 8192\n"
    "v_subrev_u32_e32 v46, s45, v240\n"
    "v_subrev_u32_e32 v47, s45, v241\n"
    "s_mul_i32 s45, s51, 61440\n"
    "s_add_u32 s45, s45, 8192\n"
    "v_subrev_u32_e32 v4, s45, v244\n"
    "v_subrev_u32_e32 v5, s45, v245\n"
    "v_subrev_u32_e32 v6, s45, v246\n"
    "v_subrev_u32_e32 v7, s45, v247\n"
    "s_waitcnt vmcnt(9)\n"
    "v_mfma_f32_16x16x32_bf16 v[24:27], v[112:115], a[0:3], 0\n"
    "buffer_load_dwordx4 v[196:199], v250, s[8:11], s47 offen offset:3072 sc1\n"
    "s_bitset0_b32 s61, 14\n"
    "v_mfma_f32_16x16x32_bf16 v[28:31], v[116:119], a[0:3], 0\n"
    "v_accvgpr_write_b32 a244, 0\n"
    "v_mfma_f32_16x16x32_bf16 v[48:51], v[112:115], a[32:35], 0\n"
    "v_accvgpr_write_b32 a245, 0\n"
    "v_mfma_f32_16x16x32_bf16 v[52:55], v[116:119], a[32:35], 0\n"
    "s_add_u32 s47, s52, s61\n"
    "buffer_load_dwordx4 v[208:211], v250, s[8:11], s47 offen offset:0 sc1\n"
    "v_mfma_f32_16x16x32_bf16 v[64:67], v[112:115], a[64:67], 0\n"
    "v_bfe_u32 v0, v250, 4, 2\n"
    "v_mfma_f32_16x16x32_bf16 v[68:71], v[116:119], a[64:67], 0\n"
    "s_mov_b32 s18, 5120000\n"
    "v_accvgpr_write_b32 a246, 0\n"
    "s_waitcnt vmcnt(9)\n"
    "v_mfma_f32_16x16x32_bf16 v[24:27], v[128:131], a[4:7], v[24:27]\n"
    "buffer_load_dwordx4 v[212:215], v250, s[8:11], s47 offen offset:1024 sc1\n"
    "s_add_u32 s52, s52, 0x4000\n"
    "v_mfma_f32_16x16x32_bf16 v[28:31], v[132:135], a[4:7], v[28:31]\n"
    "buffer_store_dword v102, off, s[8:11], s46 offset:0 sc1\n"
    "v_accvgpr_write_b32 a247, 0\n"
    "v_mfma_f32_16x16x32_bf16 v[48:51], v[128:131], a[36:39], v[48:51]\n"
    "v_bfe_u32 v1, v250, 6, 2\n"
    "v_accvgpr_write_b32 a248, 0\n"
    "v_mfma_f32_16x16x32_bf16 v[52:55], v[132:135], a[36:39], v[52:55]\n"
    "buffer_load_dwordx4 v[224:227], v250, s[8:11], s47 offen offset:2048 sc1\n"
    "s_add_u32 s61, s61, 0x1000\n"
    "v_mfma_f32_16x16x32_bf16 v[64:67], v[128:131], a[68:71], v[64:67]\n"
    "s_mov_b32 s19, 0x20000\n"
    "v_accvgpr_write_b32 a249, 0\n"
    "v_mfma_f32_16x16x32_bf16 v[68:71], v[132:135], a[68:71], v[68:71]\n"
    "v_mov_b32_e32 v2, 0x3f80\n"
    "s_waitcnt vmcnt(10)\n"
    "v_mfma_f32_16x16x32_bf16 v[24:27], v[144:147], a[8:11], v[24:27]\n"
    "buffer_load_dwordx4 v[228:231], v250, s[8:11], s47 offen offset:3072 sc1\n"
    "s_bitset0_b32 s61, 14\n"
    "v_mfma_f32_16x16x32_bf16 v[28:31], v[148:151], a[8:11], v[28:31]\n"
    "v_accvgpr_write_b32 a250, 0\n"
    "v_mfma_f32_16x16x32_bf16 v[48:51], v[144:147], a[40:43], v[48:51]\n"
    "s_mov_b64 s[20:21], s[8:9]\n"
    "v_accvgpr_write_b32 a251, 0\n"
    "v_mfma_f32_16x16x32_bf16 v[52:55], v[148:151], a[40:43], v[52:55]\n"
    "s_add_u32 s47, s52, s61\n"
    "buffer_load_dwordx4 v[120:123], v250, s[8:11], s47 offen offset:0 sc1\n"
    "v_mfma_f32_16x16x32_bf16 v[64:67], v[144:147], a[72:75], v[64:67]\n"
    "v_cmp_eq_u32_e32 vcc, 3, v0\n"
    "s_nop 1\n"
    "v_cndmask_b32_e64 v2, v2, 1.0, vcc\n"
    "v_mfma_f32_16x16x32_bf16 v[68:71], v[148:151], a[72:75], v[68:71]\n"
    "v_accvgpr_write_b32 a252, 0\n"
    "s_waitcnt vmcnt(10)\n"
    "v_mfma_f32_16x16x32_bf16 v[24:27], v[160:163], a[12:15], v[24:27]\n"
    "buffer_load_dwordx4 v[124:127], v250, s[8:11], s47 offen offset:1024 sc1\n"
    "v_accvgpr_write_b32 a253, 0\n"
    "v_mfma_f32_16x16x32_bf16 v[28:31], v[164:167], a[12:15], v[28:31]\n"
    "s_mov_b32 s22, 5242880\n"
    "v_accvgpr_write_b32 a254, 0\n"
    "v_mfma_f32_16x16x32_bf16 v[48:51], v[160:163], a[44:47], v[48:51]\n"
    "v_cmp_eq_u32_e32 vcc, 0, v1\n"
    "s_nop 1\n"
    "v_cndmask_b32_e32 v3, 0, v2, vcc\n"
    "v_mfma_f32_16x16x32_bf16 v[52:55], v[164:167], a[44:47], v[52:55]\n"
    "buffer_load_dwordx4 v[136:139], v250, s[8:11], s47 offen offset:2048 sc1\n"
    "s_add_u32 s61, s61, 0x1000\n"
    "v_mfma_f32_16x16x32_bf16 v[64:67], v[160:163], a[76:79], v[64:67]\n"
    "v_accvgpr_write_b32 a232, v3\n"
    "v_accvgpr_write_b32 a255, 0\n"
    "v_mfma_f32_16x16x32_bf16 v[68:71], v[164:167], a[76:79], v[68:71]\n"
    "s_mov_b64 s[24:25], s[28:29]\n"
    "v_accvgpr_write_b32 a192, 0\n"
    "s_waitcnt vmcnt(10)\n"
    "v_mfma_f32_16x16x32_bf16 v[24:27], v[176:179], a[16:19], v[24:27]\n"
    "buffer_load_dwordx4 v[140:143], v250, s[8:11], s47 offen offset:3072 sc1\n"
    "s_bitset0_b32 s61, 14\n"
    "v_mfma_f32_16x16x32_bf16 v[28:31], v[180:183], a[16:19], v[28:31]\n"
    "v_cmp_eq_u32_e32 vcc, 1, v1\n"
    "s_nop 1\n"
    "v_cndmask_b32_e32 v3, 0, v2, vcc\n"
    "v_mfma_f32_16x16x32_bf16 v[48:51], v[176:179], a[48:51], v[48:51]\n"
    "s_movk_i32 s26, 20000\n"
    "v_accvgpr_write_b32 a193, 0\n"
    "v_mfma_f32_16x16x32_bf16 v[52:55], v[180:183], a[48:51], v[52:55]\n"
    "s_add_u32 s47, s52, s61\n"
    "buffer_load_dwordx4 v[152:155], v250, s[8:11], s47 offen offset:0 sc1\n"
    "v_mfma_f32_16x16x32_bf16 v[64:67], v[176:179], a[80:83], v[64:67]\n"
    "buffer_load_dword v80, v83, s[8:11], s59 offen offset:0 sc1\n"
    "v_accvgpr_write_b32 a194, 0\n"
    "v_mfma_f32_16x16x32_bf16 v[68:71], v[180:183], a[80:83], v[68:71]\n"
    "v_accvgpr_write_b32 a233, v3\n"
    "v_accvgpr_write_b32 a195, 0\n"
    "s_waitcnt vmcnt(11)\n"
    "v_mfma_f32_16x16x32_bf16 v[24:27], v[192:195], a[20:23], v[24:27]\n"
    "buffer_load_dwordx4 v[156:159], v250, s[8:11], s47 offen offset:1024 sc1\n"
    "v_accvgpr_write_b32 a196, 0\n"
    "v_mfma_f32_16x16x32_bf16 v[28:31], v[196:199], a[20:23], v[28:31]\n"
    "s_bfe_u32 s88, s92, 0x20001\n"
    "v_mfma_f32_16x16x32_bf16 v[48:51], v[192:195], a[52:55], v[48:51]\n"
    "v_accvgpr_write_b32 a197, 0\n"
    "v_mfma_f32_16x16x32_bf16 v[52:55], v[196:199], a[52:55], v[52:55]\n"
    "buffer_load_dwordx4 v[168:171], v250, s[8:11], s47 offen offset:2048 sc1\n"
    "s_add_u32 s61, s61, 0x1000\n"
    "v_mfma_f32_16x16x32_bf16 v[64:67], v[192:195], a[84:87], v[64:67]\n"
    "v_cmp_eq_u32_e32 vcc, 2, v1\n"
    "s_nop 1\n"
    "v_cndmask_b32_e32 v3, 0, v2, vcc\n"
    "v_mfma_f32_16x16x32_bf16 v[68:71], v[196:199], a[84:87], v[68:71]\n"
    "s_lshl_b32 s82, s88, 7\n"
    "v_accvgpr_write_b32 a198, 0\n"
    "s_waitcnt vmcnt(11)\n"
    "v_mfma_f32_16x16x32_bf16 v[24:27], v[208:211], a[24:27], v[24:27]\n"
    "buffer_load_dwordx4 v[172:175], v250, s[8:11], s47 offen offset:3072 sc1\n"
    "s_bitset0_b32 s61, 14\n"
    "v_mfma_f32_16x16x32_bf16 v[28:31], v[212:215], a[24:27], v[28:31]\n"
    "v_accvgpr_write_b32 a234, v3\n"
    "v_accvgpr_write_b32 a199, 0\n"
    "v_mfma_f32_16x16x32_bf16 v[48:51], v[208:211], a[56:59], v[48:51]\n"
    "v_accvgpr_write_b32 a200, 0\n"
    "v_mfma_f32_16x16x32_bf16 v[52:55], v[212:215], a[56:59], v[52:55]\n"
    "s_add_u32 s47, s52, s61\n"
    "buffer_load_dwordx4 v[184:187], v250, s[8:11], s47 offen offset:0 sc1\n"
    "v_mfma_f32_16x16x32_bf16 v[64:67], v[208:211], a[88:91], v[64:67]\n"
    "s_add_u32 s82, s82, 6101248\n"
    "v_accvgpr_write_b32 a201, 0\n"
    "v_mfma_f32_16x16x32_bf16 v[68:71], v[212:215], a[88:91], v[68:71]\n"
    "v_cmp_eq_u32_e32 vcc, 3, v1\n"
    "s_nop 1\n"
    "v_cndmask_b32_e32 v3, 0, v2, vcc\n"
    "s_waitcnt vmcnt(10)\n"
    "v_mfma_f32_16x16x32_bf16 v[24:27], v[224:227], a[28:31], v[24:27]\n"
    "buffer_load_dwordx4 v[188:191], v250, s[8:11], s47 offen offset:1024 sc1\n"
    "v_accvgpr_write_b32 a202, 0\n"
    "v_mfma_f32_16x16x32_bf16 v[28:31], v[228:231], a[28:31], v[28:31]\n"
    "v_accvgpr_write_b32 a235, v3\n"
    "v_accvgpr_write_b32 a203, 0\n"
    "v_mfma_f32_16x16x32_bf16 v[48:51], v[224:227], a[60:63], v[48:51]\n"
    "s_lshl_b32 s83, s88, 17\n"
    "v_accvgpr_write_b32 a204, 0\n"
    "v_mfma_f32_16x16x32_bf16 v[52:55], v[228:231], a[60:63], v[52:55]\n"
    "buffer_load_dwordx4 v[200:203], v250, s[8:11], s47 offen offset:2048 sc1\n"
    "s_add_u32 s61, s61, 0x1000\n"
    "v_mfma_f32_16x16x32_bf16 v[64:67], v[224:227], a[92:95], v[64:67]\n"
    "s_and_b32 s89, s34, 31\n"
    "v_accvgpr_write_b32 a205, 0\n"
    "v_mfma_f32_16x16x32_bf16 v[68:71], v[228:231], a[92:95], v[68:71]\n"
    "s_lshl2_add_u32 s89, s89, s93\n"
    "v_accvgpr_write_b32 a206, 0\n"
    "s_waitcnt vmcnt(10)\n"
    "v_mfma_f32_16x16x32_bf16 v[32:35], v[120:123], a[0:3], 0\n"
    "buffer_load_dwordx4 v[204:207], v250, s[8:11], s47 offen offset:3072 sc1\n"
    "s_bitset0_b32 s61, 14\n"
    "v_mfma_f32_16x16x32_bf16 v[36:39], v[124:127], a[0:3], 0\n"
    "v_cvt_pk_bf16_f32 v24, v24, v25\n"
    "v_accvgpr_write_b32 a207, 0\n"
    "v_mfma_f32_16x16x32_bf16 v[56:59], v[120:123], a[32:35], 0\n"
    "v_cvt_pk_bf16_f32 v25, v26, v27\n"
    "v_accvgpr_write_b32 a208, 0\n"
    "v_mfma_f32_16x16x32_bf16 v[60:63], v[124:127], a[32:35], 0\n"
    "s_add_u32 s47, s52, s61\n"
    "buffer_load_dwordx4 v[216:219], v250, s[8:11], s47 offen offset:0 sc1\n"
    "v_mfma_f32_16x16x32_bf16 v[72:75], v[120:123], a[64:67], 0\n"
    "v_cvt_pk_bf16_f32 v26, v28, v29\n"
    "v_cvt_pk_bf16_f32 v27, v30, v31\n"
    "v_mfma_f32_16x16x32_bf16 v[76:79], v[124:127], a[64:67], 0\n"
    "buffer_store_dwordx4 v[24:27], v98, s[8:11], s35 offen offset:0 sc1\n"
    "v_accvgpr_write_b32 a209, 0\n"
    "s_waitcnt vmcnt(11)\n"
    "v_mfma_f32_16x16x32_bf16 v[32:35], v[136:139], a[4:7], v[32:35]\n"
    "buffer_load_dwordx4 v[220:223], v250, s[8:11], s47 offen offset:1024 sc1\n"
    "s_add_u32 s52, s52, 0x4000\n"
    "v_mfma_f32_16x16x32_bf16 v[36:39], v[140:143], a[4:7], v[36:39]\n"
    "v_cvt_pk_bf16_f32 v48, v48, v49\n"
    "v_accvgpr_write_b32 a210, 0\n"
    "v_mfma_f32_16x16x32_bf16 v[56:59], v[136:139], a[36:39], v[56:59]\n"
    "v_cvt_pk_bf16_f32 v49, v50, v51\n"
    "v_cvt_pk_bf16_f32 v50, v52, v53\n"
    "v_mfma_f32_16x16x32_bf16 v[60:63], v[140:143], a[36:39], v[60:63]\n"
    "buffer_load_dwordx4 v[232:235], v250, s[8:11], s47 offen offset:2048 sc1\n"
    "s_add_u32 s61, s61, 0x1000\n"
    "s_waitcnt vmcnt(11)\n"
    "v_cmp_ne_u32_e32 vcc, s39, v80\n"
    "v_mfma_f32_16x16x32_bf16 v[72:75], v[136:139], a[68:71], v[72:75]\n"
    "v_cvt_pk_bf16_f32 v51, v54, v55\n"
    "buffer_store_dwordx4 v[48:51], v98, s[8:11], s54 offen offset:0 sc1\n"
    "v_mfma_f32_16x16x32_bf16 v[76:79], v[140:143], a[68:71], v[76:79]\n"
    "v_cvt_pk_bf16_f32 v64, v64, v65\n"
    "v_cvt_pk_bf16_f32 v65, v66, v67\n"
    "s_waitcnt vmcnt(11)\n"
    "v_mfma_f32_16x16x32_bf16 v[32:35], v[152:155], a[8:11], v[32:35]\n"
    "buffer_load_dwordx4 v[236:239], v250, s[8:11], s47 offen offset:3072 sc1\n"
    "s_bitset0_b32 s61, 14\n"
    "v_mfma_f32_16x16x32_bf16 v[36:39], v[156:159], a[8:11], v[36:39]\n"
    "s_cbranch_vccnz .Lat_h1poll\n"
    ".Lat_h1poll_ret:\n"
    "s_mov_b32 m0, s56\n"
    "v_mfma_f32_16x16x32_bf16 v[56:59], v[152:155], a[40:43], v[56:59]\n"
    "buffer_load_dwordx4 v104, s[8:11], s57 offen offset:0 sc1 lds\n"
    "v_cvt_pk_bf16_f32 v66, v68, v69\n"
    "v_mfma_f32_16x16x32_bf16 v[60:63], v[156:159], a[40:43], v[60:63]\n"
    "v_cvt_pk_bf16_f32 v67, v70, v71\n"
    "buffer_store_dwordx4 v[64:67], v98, s[8:11], s60 offen offset:0 sc1\n"
    "v_mfma_f32_16x16x32_bf16 v[72:75], v[152:155], a[72:75], v[72:75]\n"
    "s_lshl_b32 s89, s89, 10\n"
    "v_accvgpr_write_b32 a211, 0\n"
    "v_mfma_f32_16x16x32_bf16 v[76:79], v[156:159], a[72:75], v[76:79]\n"
    "s_add_u32 s83, s83, s89\n"
    "v_accvgpr_write_b32 a212, 0\n"
    "s_waitcnt vmcnt(12)\n"
    "v_mfma_f32_16x16x32_bf16 v[32:35], v[168:171], a[12:15], v[32:35]\n"
    "buffer_load_dwordx4 v105, s[8:11], s57 offen offset:1024 sc1 lds\n"
    "v_accvgpr_write_b32 a213, 0\n"
    "v_mfma_f32_16x16x32_bf16 v[36:39], v[172:175], a[12:15], v[36:39]\n"
    "s_add_u32 s83, s83, 67682304\n"
    "v_accvgpr_write_b32 a214, 0\n"
    "v_mfma_f32_16x16x32_bf16 v[56:59], v[168:171], a[44:47], v[56:59]\n"
    "ds_write_b128 v99, v[24:27] offset:0\n"
    "v_accvgpr_write_b32 a215, 0\n"
    "v_mfma_f32_16x16x32_bf16 v[60:63], v[172:175], a[44:47], v[60:63]\n"
    "s_add_u32 m0, s43, 0\n"
    "v_accvgpr_write_b32 a216, 0\n"
    "v_mfma_f32_16x16x32_bf16 v[72:75], v[168:171], a[76:79], v[72:75]\n"
    "buffer_load_dwordx4 v104, s[8:11], s40 offen offset:0 sc1 lds\n"
    "v_accvgpr_write_b32 a217, 0\n"
    "v_mfma_f32_16x16x32_bf16 v[76:79], v[172:175], a[76:79], v[76:79]\n"
    "s_lshl_b32 s30, s93, 10\n"
    "s_waitcnt vmcnt(12)\n"
    "v_mfma_f32_16x16x32_bf16 v[32:35], v[184:187], a[16:19], v[32:35]\n"
    "v_accvgpr_write_b32 a218, 0\n"
    "v_mfma_f32_16x16x32_bf16 v[36:39], v[188:191], a[16:19], v[36:39]\n"
    "s_add_u32 s30, s30, 20480\n"
    "v_accvgpr_write_b32 a219, 0\n"
    "v_mfma_f32_16x16x32_bf16 v[56:59], v[184:187], a[48:51], v[56:59]\n"
    "buffer_load_dwordx4 v105, s[8:11], s40 offen offset:1024 sc1 lds\n"
    "v_mfma_f32_16x16x32_bf16 v[60:63], v[188:191], a[48:51], v[60:63]\n"
    "v_accvgpr_write_b32 a220, 0\n"
    "v_mfma_f32_16x16x32_bf16 v[72:75], v[184:187], a[80:83], v[72:75]\n"
    "s_bitcmp0_b32 s92, 0\n"
    "s_cselect_b32 s85, 1, 0\n"
    "s_cselect_b32 s80, 5, 0\n"
    "v_mfma_f32_16x16x32_bf16 v[76:79], v[188:191], a[80:83], v[76:79]\n"
    "ds_write_b128 v99, v[48:51] offset:8192\n"
    "v_accvgpr_write_b32 a221, 0\n"
    "s_waitcnt vmcnt(11)\n"
    "v_mfma_f32_16x16x32_bf16 v[32:35], v[200:203], a[20:23], v[32:35]\n"
    "buffer_load_dwordx4 v106, s[8:11], s40 offen offset:2048 sc1 lds\n"
    "v_mfma_f32_16x16x32_bf16 v[36:39], v[204:207], a[20:23], v[36:39]\n"
    "v_accvgpr_write_b32 a222, 0\n"
    "v_mfma_f32_16x16x32_bf16 v[56:59], v[200:203], a[52:55], v[56:59]\n"
    "v_accvgpr_write_b32 a223, 0\n"
    "v_mfma_f32_16x16x32_bf16 v[60:63], v[204:207], a[52:55], v[60:63]\n"
    "s_bitcmp0_b32 s92, 0\n"
    "s_cselect_b32 s82, s82, 2.0\n"
    "s_cselect_b32 s83, s83, 2.0\n"
    "v_mfma_f32_16x16x32_bf16 v[72:75], v[200:203], a[84:87], v[72:75]\n"
    "buffer_load_dwordx4 v107, s[8:11], s40 offen offset:3072 sc1 lds\n"
    "v_mfma_f32_16x16x32_bf16 v[76:79], v[204:207], a[84:87], v[76:79]\n"
    "v_accvgpr_write_b32 a224, 0\n"
    "s_waitcnt vmcnt(10)\n"
    "v_mfma_f32_16x16x32_bf16 v[32:35], v[216:219], a[24:27], v[32:35]\n"
    "v_accvgpr_write_b32 a225, 0\n"
    "v_mfma_f32_16x16x32_bf16 v[36:39], v[220:223], a[24:27], v[36:39]\n"
    "s_add_u32 s40, s40, 0x8000\n"
    "v_mfma_f32_16x16x32_bf16 v[56:59], v[216:219], a[56:59], v[56:59]\n"
    "ds_write_b128 v99, v[64:67] offset:49152\n"
    "v_mfma_f32_16x16x32_bf16 v[60:63], v[220:223], a[56:59], v[60:63]\n"
    "s_mov_b32 s84, 2.0\n"
    "v_mfma_f32_16x16x32_bf16 v[72:75], v[216:219], a[88:91], v[72:75]\n"
    "v_accvgpr_write_b32 a226, 0\n"
    "v_mfma_f32_16x16x32_bf16 v[76:79], v[220:223], a[88:91], v[76:79]\n"
    "v_accvgpr_write_b32 a227, 0\n"
    "s_waitcnt vmcnt(7)\n"
    "v_mfma_f32_16x16x32_bf16 v[32:35], v[232:235], a[28:31], v[32:35]\n"
    "s_movk_i32 s90, 512\n"
    "v_mfma_f32_16x16x32_bf16 v[36:39], v[236:239], a[28:31], v[36:39]\n"
    "v_accvgpr_write_b32 a228, 0\n"
    "v_mfma_f32_16x16x32_bf16 v[56:59], v[232:235], a[60:63], v[56:59]\n"
    "v_accvgpr_write_b32 a229, 0\n"
    "v_mfma_f32_16x16x32_bf16 v[60:63], v[236:239], a[60:63], v[60:63]\n"
    "v_accvgpr_write_b32 a230, 0\n"
    "v_mfma_f32_16x16x32_bf16 v[72:75], v[232:235], a[92:95], v[72:75]\n"
    "s_mov_b32 s4, 0x1c400000\n"
    "v_mfma_f32_16x16x32_bf16 v[76:79], v[236:239], a[92:95], v[76:79]\n"
    "v_accvgpr_write_b32 a231, 0\n"
    "v_cvt_pk_bf16_f32 v32, v32, v33\n"
    "v_cvt_pk_bf16_f32 v33, v34, v35\n"
    "v_cvt_pk_bf16_f32 v34, v36, v37\n"
    "v_cvt_pk_bf16_f32 v35, v38, v39\n"
    "ds_write_b128 v99, v[32:35] offset:1024\n"
    "buffer_store_dwordx4 v[32:35], v98, s[8:11], s35 offen offset:1024 sc1\n"
    "v_cvt_pk_bf16_f32 v56, v56, v57\n"
    "v_cvt_pk_bf16_f32 v57, v58, v59\n"
    "v_cvt_pk_bf16_f32 v58, v60, v61\n"
    "v_cvt_pk_bf16_f32 v59, v62, v63\n"
    "ds_write_b128 v99, v[56:59] offset:9216\n"
    "buffer_store_dwordx4 v[56:59], v98, s[8:11], s54 offen offset:1024 sc1\n"
    "v_cvt_pk_bf16_f32 v72, v72, v73\n"
    "v_cvt_pk_bf16_f32 v73, v74, v75\n"
    "v_cvt_pk_bf16_f32 v74, v76, v77\n"
    "v_cvt_pk_bf16_f32 v75, v78, v79\n"
    "ds_write_b128 v99, v[72:75] offset:50176\n"
    "buffer_store_dwordx4 v[72:75], v98, s[8:11], s60 offen offset:1024 sc1\n"
    "v_mov_b32_e32 v8, s4\n"
    "v_mov_b32_e32 v9, s4\n"
    "s_add_u32 m0, s43, 4096\n"
    "v_mov_b64_e32 v[10:11], v[8:9]\n"
    "v_mov_b64_e32 v[12:13], v[8:9]\n"
    "buffer_load_dwordx4 v104, s[8:11], s41 offen offset:0 sc1 lds\n"
    "v_mov_b64_e32 v[14:15], v[8:9]\n"
    "v_mov_b64_e32 v[16:17], v[8:9]\n"
    "buffer_load_dwordx4 v105, s[8:11], s41 offen offset:1024 sc1 lds\n"
    "v_mov_b64_e32 v[18:19], v[8:9]\n"
    "v_mov_b64_e32 v[28:29], v[8:9]\n"
    "buffer_load_dwordx4 v106, s[8:11], s41 offen offset:2048 sc1 lds\n"
    "v_mov_b64_e32 v[30:31], v[8:9]\n"
    "v_mov_b64_e32 v[32:33], v[8:9]\n"
    "buffer_load_dwordx4 v107, s[8:11], s41 offen offset:3072 sc1 lds\n"
    "v_mov_b64_e32 v[34:35], v[8:9]\n"
    "v_mov_b64_e32 v[36:37], v[8:9]\n"
    "s_add_u32 s41, s41, 0x8000\n"
    "v_mov_b64_e32 v[38:39], v[8:9]\n"
    "v_mov_b64_e32 v[48:49], v[8:9]\n"
    "v_mov_b64_e32 v[50:51], v[8:9]\n"
    "v_mov_b64_e32 v[52:53], v[8:9]\n"
    "v_mov_b64_e32 v[54:55], v[8:9]\n"
    "v_mov_b64_e32 v[56:57], v[8:9]\n"
    "v_mov_b64_e32 v[58:59], v[8:9]\n"
    "v_mov_b64_e32 v[68:69], v[8:9]\n"
    "v_mov_b64_e32 v[70:71], v[8:9]\n"
    "v_mov_b64_e32 v[72:73], v[8:9]\n"
    "v_mov_b64_e32 v[74:75], v[8:9]\n"
    "v_mov_b64_e32 v[76:77], v[8:9]\n"
    "s_cmp_lg_u32 s93, 3\n"
    "s_cbranch_scc1 .Lat_wn40\n"
    "s_call_b64 s[2:3], .Lat_w6\n"
    "s_call_b64 s[2:3], .Lat_w10\n"
    ".Lat_wn40:\n"
    "s_cmp_lg_u32 s93, 2\n"
    "s_cbranch_scc1 .Lat_wn41\n"
    "s_call_b64 s[2:3], .Lat_w7\n"
    "s_call_b64 s[2:3], .Lat_w11\n"
    ".Lat_wn41:\n"
    "s_cmp_lg_u32 s93, 1\n"
    "s_cbranch_scc1 .Lat_wn42\n"
    "s_call_b64 s[2:3], .Lat_w8\n"
    "s_call_b64 s[2:3], .Lat_w12\n"
    ".Lat_wn42:\n"
    "s_cmp_lg_u32 s93, 0\n"
    "s_cbranch_scc1 .Lat_wn43\n"
    "s_call_b64 s[2:3], .Lat_w9\n"
    "s_call_b64 s[2:3], .Lat_w13\n"
    ".Lat_wn43:\n"
    "s_waitcnt vmcnt(4)\n"
    "buffer_store_dword v84, v86, s[8:11], s58 offen offset:0 sc1\n"
    "v_mov_b64_e32 v[78:79], v[8:9]\n"
    "v_mov_b64_e32 v[88:89], v[8:9]\n"
    "s_add_u32 m0, s43, 8192\n"
    "v_mov_b64_e32 v[90:91], v[8:9]\n"
    "v_mov_b64_e32 v[92:93], v[8:9]\n"
    "buffer_load_dwordx4 v104, s[8:11], s40 offen offset:0 sc1 lds\n"
    "v_mov_b64_e32 v[94:95], v[8:9]\n"
    "v_mov_b64_e32 v[96:97], v[8:9]\n"
    "buffer_load_dwordx4 v105, s[8:11], s40 offen offset:1024 sc1 lds\n"
    "v_mov_b64_e32 v[98:99], v[8:9]\n"
    "v_mov_b64_e32 v[108:109], v[8:9]\n"
    "buffer_load_dwordx4 v106, s[8:11], s40 offen offset:2048 sc1 lds\n"
    "v_mov_b64_e32 v[110:111], v[8:9]\n"
    "v_mov_b64_e32 v[112:113], v[8:9]\n"
    "buffer_load_dwordx4 v107, s[8:11], s40 offen offset:3072 sc1 lds\n"
    "v_mov_b64_e32 v[114:115], v[8:9]\n"
    "v_mov_b64_e32 v[116:117], v[8:9]\n"
    "s_add_u32 s40, s40, 0x8000\n"
    "v_mov_b64_e32 v[118:119], v[8:9]\n"
    "v_mov_b64_e32 v[128:129], v[8:9]\n"
    "s_add_u32 m0, s43, 12288\n"
    "v_mov_b64_e32 v[130:131], v[8:9]\n"
    "v_mov_b64_e32 v[132:133], v[8:9]\n"
    "buffer_load_dwordx4 v104, s[8:11], s41 offen offset:0 sc1 lds\n"
    "v_mov_b64_e32 v[134:135], v[8:9]\n"
    "v_mov_b64_e32 v[136:137], v[8:9]\n"
    "buffer_load_dwordx4 v105, s[8:11], s41 offen offset:1024 sc1 lds\n"
    "v_mov_b64_e32 v[138:139], v[8:9]\n"
    "buffer_load_dwordx4 v106, s[8:11], s41 offen offset:2048 sc1 lds\n"
    "buffer_load_dwordx4 v107, s[8:11], s41 offen offset:3072 sc1 lds\n"
    "s_add_u32 s41, s41, 0x8000\n"
    ".Lat_p2:\n"
    "v_mov_b32_e32 v220, 0xc2ae0000\n"
    "v_mov_b32_e32 v221, 0xc2ae0000\n"
    "v_mov_b64_e32 v[222:223], v[220:221]\n"
    "v_mov_b64_e32 v[224:225], v[220:221]\n"
    "v_mov_b64_e32 v[226:227], v[220:221]\n"
    "v_mov_b64_e32 v[228:229], v[220:221]\n"
    "v_mov_b64_e32 v[230:231], v[220:221]\n"
    "v_mov_b64_e32 v[232:233], v[220:221]\n"
    "v_mov_b64_e32 v[234:235], v[220:221]\n"
    "v_mov_b64_e32 v[236:237], v[220:221]\n"
    "v_mov_b64_e32 v[238:239], v[220:221]\n"
    "s_andn2_b32 s47, s58, 0x600\n"
    "s_mov_b32 s44, 9\n"
    "s_waitcnt vmcnt(9)\n"
    "s_waitcnt lgkmcnt(0)\n"
    "s_barrier\n"
    "s_cmp_lg_u32 s49, 0\n"
    "s_cbranch_scc1 .Lat_maxpass\n"
    "ds_read_b128 a[0:3], v85 offset:0\n"
    "ds_read_b128 a[4:7], v85 offset:1024\n"
    "ds_read_b128 a[8:11], v85 offset:2048\n"
    "ds_read_b128 a[12:15], v85 offset:3072\n"
    "ds_read_b128 a[16:19], v85 offset:4096\n"
    "ds_read_b128 a[20:23], v85 offset:5120\n"
    "ds_read_b128 a[24:27], v85 offset:6144\n"
    "ds_read_b128 a[28:31], v85 offset:7168\n"
    "ds_read_b128 a[32:35], v255 offset:0\n"
    "ds_read_b128 a[36:39], v255 offset:1024\n"
    "ds_read_b128 a[40:43], v255 offset:2048\n"
    "ds_read_b128 a[44:47], v255 offset:3072\n"
    "ds_read_b128 a[48:51], v255 offset:4096\n"
    "ds_read_b128 a[52:55], v255 offset:5120\n"
    "ds_read_b128 a[56:59], v255 offset:6144\n"
    "ds_read_b128 a[60:63], v255 offset:7168\n"
    ".Lat_maxpass_ret:\n"
    "ds_read_b128 a[160:163], v46 offset:0\n"
    "ds_read_b128 a[164:167], v47 offset:0\n"
    "ds_read_b128 a[168:171], v46 offset:2048\n"
    "ds_read_b128 a[172:175], v47 offset:2048\n"
    "ds_read_b128 a[176:179], v46 offset:4096\n"
    "ds_read_b128 a[180:183], v47 offset:4096\n"
    "ds_read_b128 a[184:187], v46 offset:6144\n"
    "ds_read_b128 a[188:191], v47 offset:6144\n"
    "s_mul_i32 s48, s33, 81920\n"
    "s_add_u32 s48, s48, 11345920\n"
    "s_lshl_b32 s46, s93, 11\n"
    "s_add_u32 s48, s48, s46\n"
    ".long 0xbedf00ff\n"
    ".Lat_w1:\n"
    "s_setpc_b64 s[2:3]\n"
    "s_add_u32 s46, s46, 49152\n"
    "buffer_load_dword v254, v253, s[8:11], s47 offen offset:0 sc1\n"
    "s_waitcnt lgkmcnt(4)\n"
    "v_mfma_f32_16x16x32_bf16 v[212:215], a[160:163], a[0:3], v[220:223]\n"
    "s_and_b32 s45, s32, 1\n"
    "ds_read_b64_tr_b16 v[140:141], v4 offset:0\n"
    "v_mfma_f32_16x16x32_bf16 v[216:219], a[160:163], a[32:35], v[224:227]\n"
    "s_xor_b32 s45, s45, 9\n"
    "ds_read_b64_tr_b16 v[142:143], v5 offset:0\n"
    "v_mfma_f32_16x16x32_bf16 v[212:215], a[164:167], a[4:7], v[212:215]\n"
    "s_lshl_b32 s45, s45, 13\n"
    "ds_read_b64_tr_b16 v[144:145], v6 offset:0\n"
    "v_mfma_f32_16x16x32_bf16 v[216:219], a[164:167], a[36:39], v[216:219]\n"
    "s_add_u32 s45, s45, s48\n"
    "ds_read_b64_tr_b16 v[146:147], v7 offset:0\n"
    "v_mfma_f32_16x16x32_bf16 v[212:215], a[168:171], a[8:11], v[212:215]\n"
    "s_add_u32 s54, s32, 1\n"
    "ds_read_b64_tr_b16 v[148:149], v4 offset:2048\n"
    "v_mfma_f32_16x16x32_bf16 v[216:219], a[168:171], a[40:43], v[216:219]\n"
    "s_and_b32 s54, s54, 3\n"
    "ds_read_b64_tr_b16 v[150:151], v5 offset:2048\n"
    "v_mfma_f32_16x16x32_bf16 v[212:215], a[172:175], a[12:15], v[212:215]\n"
    "s_lshl_b32 s54, s54, 13\n"
    "ds_read_b64_tr_b16 v[152:153], v6 offset:2048\n"
    "v_mfma_f32_16x16x32_bf16 v[216:219], a[172:175], a[44:47], v[216:219]\n"
    "s_add_u32 s54, s54, s48\n"
    "ds_read_b64_tr_b16 v[154:155], v7 offset:2048\n"
    "s_waitcnt lgkmcnt(8)\n"
    "v_mfma_f32_16x16x32_bf16 v[212:215], a[176:179], a[16:19], v[212:215]\n"
    "s_add_u32 s55, s32, 2\n"
    "ds_read_b128 v[180:183], v240 offset:2048\n"
    "v_mfma_f32_16x16x32_bf16 v[216:219], a[176:179], a[48:51], v[216:219]\n"
    "s_and_b32 s55, s55, 3\n"
    "ds_read_b128 v[184:187], v241 offset:2048\n"
    "v_mfma_f32_16x16x32_bf16 v[212:215], a[180:183], a[20:23], v[212:215]\n"
    "s_lshl_b32 s55, s55, 13\n"
    "ds_read_b128 v[188:191], v240 offset:6144\n"
    "v_mfma_f32_16x16x32_bf16 v[216:219], a[180:183], a[52:55], v[216:219]\n"
    "s_add_u32 s55, s55, s48\n"
    "ds_read_b128 v[192:195], v241 offset:6144\n"
    "v_mfma_f32_16x16x32_bf16 v[212:215], a[184:187], a[24:27], v[212:215]\n"
    "s_add_u32 s60, s32, 3\n"
    "ds_read_b128 v[196:199], v242 offset:2048\n"
    "v_mfma_f32_16x16x32_bf16 v[216:219], a[184:187], a[56:59], v[216:219]\n"
    "s_and_b32 s60, s60, 3\n"
    "ds_read_b128 v[200:203], v243 offset:2048\n"
    "v_mfma_f32_16x16x32_bf16 v[212:215], a[188:191], a[28:31], v[212:215]\n"
    "s_lshl_b32 s60, s60, 13\n"
    "ds_read_b128 v[204:207], v242 offset:6144\n"
    "v_mfma_f32_16x16x32_bf16 v[216:219], a[188:191], a[60:63], v[216:219]\n"
    "s_add_u32 s60, s60, s48\n"
    "ds_read_b128 v[208:211], v243 offset:6144\n"
    "s_lshr_b32 s61, s32, 1\n"
    "s_or_b32 s61, s61, s50\n"
    "s_sub_u32 s61, s61, 1\n"
    "s_add_u32 m0, s46, 65536\n"
    "s_waitcnt vmcnt(0)\n"
    "v_cmp_ne_u32_e32 vcc, s39, v254\n"
    "ds_read_b128 a[160:163], v240 offset:0\n"
    "ds_read_b128 a[164:167], v241 offset:0\n"
    "ds_read_b128 a[168:171], v240 offset:4096\n"
    "ds_read_b128 a[172:175], v241 offset:4096\n"
    "ds_read_b128 a[176:179], v242 offset:0\n"
    "s_cbranch_vccz .Lat_qpoll_ret\n"
    ".Lat_qpoll:\n"
    "s_mov_b32 s53, 0x400000\n"
    ".Lat_qpoll_l:\n"
    "s_sleep 0\n"
    "buffer_load_dword v254, v253, s[8:11], s47 offen offset:0 sc1\n"
    "s_waitcnt vmcnt(0)\n"
    "v_cmp_ne_u32_e32 vcc, s39, v254\n"
    "s_sub_u32 s53, s53, 1\n"
    "s_cbranch_scc1 .Lat_qpoll_ret\n"
    "s_nop 2\n"
    "s_cbranch_vccnz .Lat_qpoll_l\n"
    ".Lat_qpoll_ret:\n"
    "buffer_load_dwordx4 v250, s[8:11], s45 offen offset:0 sc1 lds\n"
    "buffer_load_dwordx4 v250, s[8:11], s45 offen offset:1024 sc1 lds\n"
    "s_add_u32 m0, s46, 8192\n"
    "ds_read_b128 a[180:183], v243 offset:0\n"
    "s_nop 0\n"
    "ds_read_b128 a[184:187], v242 offset:4096\n"
    "buffer_load_dwordx4 v250, s[8:11], s54 offen offset:0 sc1 lds\n"
    "ds_read_b128 a[188:191], v243 offset:4096\n"
    "buffer_load_dwordx4 v250, s[8:11], s54 offen offset:1024 sc1 lds\n"
    "v_exp_f32_e32 v212, v212\n"
    "s_add_u32 m0, s46, 73728\n"
    "v_exp_f32_e32 v213, v213\n"
    "s_add_u32 s54, s54, 0x8000\n"
    "v_exp_f32_e32 v214, v214\n"
    "buffer_load_dwordx4 v250, s[8:11], s54 offen offset:0 sc1 lds\n"
    "v_exp_f32_e32 v215, v215\n"
    "buffer_load_dwordx4 v250, s[8:11], s54 offen offset:1024 sc1 lds\n"
    "v_exp_f32_e32 v216, v216\n"
    "s_add_u32 m0, s46, 32768\n"
    "v_exp_f32_e32 v217, v217\n"
    "s_nop 0\n"
    "v_exp_f32_e32 v218, v218\n"
    "buffer_load_dwordx4 v250, s[8:11], s55 offen offset:0 sc1 lds\n"
    "v_exp_f32_e32 v219, v219\n"
    "buffer_load_dwordx4 v250, s[8:11], s55 offen offset:1024 sc1 lds\n"
    "v_cvt_pk_bf16_f32 v212, v212, v213\n"
    "v_cvt_pk_bf16_f32 v213, v214, v215\n"
    "v_cvt_pk_bf16_f32 v216, v216, v217\n"
    "v_cvt_pk_bf16_f32 v217, v218, v219\n"
    "v_and_b32_e32 v216, s61, v216\n"
    "v_and_b32_e32 v217, s61, v217\n"
    "s_waitcnt lgkmcnt(15)\n"
    "v_mfma_f32_16x16x16_bf16 a[236:239], v[140:141], v[212:213], 0\n"
    "s_add_u32 m0, s46, 98304\n"
    "v_mfma_f32_16x16x16_bf16 v[0:3], v[142:143], v[212:213], v[136:139]\n"
    "s_add_u32 s55, s55, 0x8000\n"
    "v_mfma_f32_16x16x16_bf16 v[20:23], v[144:145], v[212:213], v[136:139]\n"
    "buffer_load_dwordx4 v250, s[8:11], s55 offen offset:0 sc1 lds\n"
    "v_mfma_f32_16x16x16_bf16 v[40:43], v[146:147], v[212:213], v[136:139]\n"
    "buffer_load_dwordx4 v250, s[8:11], s55 offen offset:1024 sc1 lds\n"
    "v_mfma_f32_16x16x16_bf16 v[60:63], v[148:149], v[212:213], v[136:139]\n"
    "s_add_u32 m0, s46, 40960\n"
    "v_mfma_f32_16x16x16_bf16 v[80:83], v[150:151], v[212:213], v[136:139]\n"
    "buffer_load_dwordx4 v250, s[8:11], s60 offen offset:0 sc1 lds\n"
    "v_mfma_f32_16x16x16_bf16 v[100:103], v[152:153], v[212:213], v[136:139]\n"
    "buffer_load_dwordx4 v250, s[8:11], s60 offen offset:1024 sc1 lds\n"
    "v_mfma_f32_16x16x16_bf16 v[120:123], v[154:155], v[212:213], v[136:139]\n"
    "s_add_u32 m0, s46, 106496\n"
    "v_mfma_f32_16x16x16_bf16 a[240:243], v[140:141], v[216:217], 0\n"
    "s_add_u32 s60, s60, 0x8000\n"
    "v_mfma_f32_16x16x16_bf16 v[4:7], v[142:143], v[216:217], v[136:139]\n"
    "buffer_load_dwordx4 v250, s[8:11], s60 offen offset:0 sc1 lds\n"
    "v_mfma_f32_16x16x16_bf16 v[24:27], v[144:145], v[216:217], v[136:139]\n"
    "buffer_load_dwordx4 v250, s[8:11], s60 offen offset:1024 sc1 lds\n"
    "v_mfma_f32_16x16x16_bf16 v[44:47], v[146:147], v[216:217], v[136:139]\n"
    "v_mfma_f32_16x16x16_bf16 v[64:67], v[148:149], v[216:217], v[136:139]\n"
    "buffer_load_dword v253, v250, s[8:11], s59 offen offset:0 sc1\n"
    "v_mfma_f32_16x16x16_bf16 v[84:87], v[150:151], v[216:217], v[136:139]\n"
    "v_mfma_f32_16x16x16_bf16 v[104:107], v[152:153], v[216:217], v[136:139]\n"
    "v_mfma_f32_16x16x16_bf16 v[124:127], v[154:155], v[216:217], v[136:139]\n"
    "s_waitcnt lgkmcnt(0)\n"
    "buffer_load_dword v254, v252, s[8:11], s59 offen offset:0 sc1\n"
    "v_mfma_f32_16x16x32_bf16 v[140:143], a[160:163], a[0:3], v[220:223]\n"
    "v_mfma_f32_16x16x32_bf16 v[140:143], a[164:167], a[4:7], v[140:143]\n"
    "v_mfma_f32_16x16x32_bf16 v[140:143], a[168:171], a[8:11], v[140:143]\n"
    "v_mfma_f32_16x16x32_bf16 v[140:143], a[172:175], a[12:15], v[140:143]\n"
    "v_mfma_f32_16x16x32_bf16 v[140:143], a[176:179], a[16:19], v[140:143]\n"
    "v_mfma_f32_16x16x32_bf16 v[140:143], a[180:183], a[20:23], v[140:143]\n"
    "v_mfma_f32_16x16x32_bf16 v[140:143], a[184:187], a[24:27], v[140:143]\n"
    "v_mfma_f32_16x16x32_bf16 v[140:143], a[188:191], a[28:31], v[140:143]\n"
    "v_mfma_f32_16x16x32_bf16 v[144:147], v[180:183], a[0:3], v[220:223]\n"
    "v_mfma_f32_16x16x32_bf16 v[144:147], v[184:187], a[4:7], v[144:147]\n"
    "s_waitcnt vmcnt(10)\n"
    "s_barrier\n"
    "v_mfma_f32_16x16x32_bf16 v[144:147], v[188:191], a[8:11], v[144:147]\n"
    "s_mov_b32 m0, s42\n"
    "ds_read_b128 a[32:35], v255 offset:0\n"
    "v_exp_f32_e32 v140, v140\n"
    "v_mfma_f32_16x16x32_bf16 v[144:147], v[192:195], a[12:15], v[144:147]\n"
    "ds_read_b128 a[36:39], v255 offset:1024\n"
    "v_exp_f32_e32 v141, v141\n"
    "v_mfma_f32_16x16x32_bf16 v[144:147], v[196:199], a[16:19], v[144:147]\n"
    "ds_read_b128 a[40:43], v255 offset:2048\n"
    "ds_read_b128 a[44:47], v255 offset:3072\n"
    "v_mfma_f32_16x16x32_bf16 v[144:147], v[200:203], a[20:23], v[144:147]\n"
    "ds_read_b128 a[48:51], v255 offset:4096\n"
    "ds_read_b128 a[52:55], v255 offset:5120\n"
    "v_mfma_f32_16x16x32_bf16 v[144:147], v[204:207], a[24:27], v[144:147]\n"
    "ds_read_b128 a[56:59], v255 offset:6144\n"
    "ds_read_b128 a[60:63], v255 offset:7168\n"
    "v_mfma_f32_16x16x32_bf16 v[144:147], v[208:211], a[28:31], v[144:147]\n"
    "s_waitcnt lgkmcnt(4)\n"
    "v_exp_f32_e32 v142, v142\n"
    "v_mfma_f32_16x16x32_bf16 v[148:151], a[160:163], a[32:35], v[224:227]\n"
    "ds_read_b128 a[64:67], v255 offset:8192\n"
    "v_exp_f32_e32 v143, v143\n"
    "v_mfma_f32_16x16x32_bf16 v[148:151], a[164:167], a[36:39], v[148:151]\n"
    "ds_read_b128 a[68:71], v255 offset:9216\n"
    "v_cvt_pk_bf16_f32 v140, v140, v141\n"
    "v_mfma_f32_16x16x32_bf16 v[148:151], a[168:171], a[40:43], v[148:151]\n"
    "ds_read_b128 a[72:75], v255 offset:10240\n"
    "v_cvt_pk_bf16_f32 v141, v142, v143\n"
    "v_mfma_f32_16x16x32_bf16 v[148:151], a[172:175], a[44:47], v[148:151]\n"
    "s_waitcnt lgkmcnt(3)\n"
    "v_exp_f32_e32 v144, v144\n"
    "v_mfma_f32_16x16x32_bf16 v[148:151], a[176:179], a[48:51], v[148:151]\n"
    "ds_read_b128 a[76:79], v255 offset:11264\n"
    "ds_write_addtid_b32 v140 offset:0\n"
    "v_mfma_f32_16x16x32_bf16 v[148:151], a[180:183], a[52:55], v[148:151]\n"
    "ds_read_b128 a[80:83], v255 offset:12288\n"
    "v_exp_f32_e32 v145, v145\n"
    "v_mfma_f32_16x16x32_bf16 v[148:151], a[184:187], a[56:59], v[148:151]\n"
    "v_exp_f32_e32 v146, v146\n"
    "v_mfma_f32_16x16x32_bf16 v[148:151], a[188:191], a[60:63], v[148:151]\n"
    "ds_read_b128 a[84:87], v255 offset:13312\n"
    "v_exp_f32_e32 v147, v147\n"
    "v_mfma_f32_16x16x32_bf16 v[152:155], v[180:183], a[32:35], v[224:227]\n"
    "ds_read_b128 a[88:91], v255 offset:14336\n"
    "v_cvt_pk_bf16_f32 v142, v144, v145\n"
    "v_mfma_f32_16x16x32_bf16 v[152:155], v[184:187], a[36:39], v[152:155]\n"
    "ds_read_b128 a[92:95], v255 offset:15360\n"
    "v_cvt_pk_bf16_f32 v143, v146, v147\n"
    "v_mfma_f32_16x16x32_bf16 v[152:155], v[188:191], a[40:43], v[152:155]\n"
    "v_exp_f32_e32 v148, v148\n"
    "v_mfma_f32_16x16x32_bf16 v[152:155], v[192:195], a[44:47], v[152:155]\n"
    "v_exp_f32_e32 v149, v149\n"
    "v_mfma_f32_16x16x32_bf16 v[152:155], v[196:199], a[48:51], v[152:155]\n"
    "v_exp_f32_e32 v150, v150\n"
    "v_mfma_f32_16x16x32_bf16 v[152:155], v[200:203], a[52:55], v[152:155]\n"
    "v_exp_f32_e32 v151, v151\n"
    "v_mfma_f32_16x16x32_bf16 v[152:155], v[204:207], a[56:59], v[152:155]\n"
    "s_waitcnt vmcnt(2)\n"
    "s_barrier\n"
    "v_mfma_f32_16x16x32_bf16 v[152:155], v[208:211], a[60:63], v[152:155]\n"
    "s_waitcnt lgkmcnt(5)\n"
    "ds_read_b128 a[96:99], v255 offset:32768\n"
    "ds_write_addtid_b32 v141 offset:256\n"
    "v_mfma_f32_16x16x32_bf16 v[156:159], a[160:163], a[64:67], v[228:231]\n"
    "ds_read_b128 a[100:103], v255 offset:33792\n"
    "ds_write_addtid_b32 v142 offset:512\n"
    "v_mfma_f32_16x16x32_bf16 v[156:159], a[164:167], a[68:71], v[156:159]\n"
    "ds_read_b128 a[104:107], v255 offset:34816\n"
    "v_mfma_f32_16x16x32_bf16 v[156:159], a[168:171], a[72:75], v[156:159]\n"
    "ds_read_b128 a[108:111], v255 offset:35840\n"
    "v_mfma_f32_16x16x32_bf16 v[156:159], a[172:175], a[76:79], v[156:159]\n"
    "s_waitcnt lgkmcnt(6)\n"
    "ds_read_b128 a[112:115], v255 offset:36864\n"
    "v_cvt_pk_bf16_f32 v148, v148, v149\n"
    "v_mfma_f32_16x16x32_bf16 v[156:159], a[176:179], a[80:83], v[156:159]\n"
    "ds_read_b128 a[116:119], v255 offset:37888\n"
    "v_cvt_pk_bf16_f32 v149, v150, v151\n"
    "v_mfma_f32_16x16x32_bf16 v[156:159], a[180:183], a[84:87], v[156:159]\n"
    "ds_read_b128 a[120:123], v255 offset:38912\n"
    "v_exp_f32_e32 v152, v152\n"
    "v_mfma_f32_16x16x32_bf16 v[156:159], a[184:187], a[88:91], v[156:159]\n"
    "ds_read_b128 a[124:127], v255 offset:39936\n"
    "v_exp_f32_e32 v153, v153\n"
    "v_mfma_f32_16x16x32_bf16 v[156:159], a[188:191], a[92:95], v[156:159]\n"
    "ds_read_b128 a[128:131], v255 offset:40960\n"
    "v_exp_f32_e32 v154, v154\n"
    "v_mfma_f32_16x16x32_bf16 v[160:163], v[180:183], a[64:67], v[228:231]\n"
    "ds_read_b128 a[132:135], v255 offset:41984\n"
    "v_exp_f32_e32 v155, v155\n"
    "v_mfma_f32_16x16x32_bf16 v[160:163], v[184:187], a[68:71], v[160:163]\n"
    "ds_read_b128 a[136:139], v255 offset:43008\n"
    "v_cvt_pk_bf16_f32 v150, v152, v153\n"
    "v_mfma_f32_16x16x32_bf16 v[160:163], v[188:191], a[72:75], v[160:163]\n"
    "ds_read_b128 a[140:143], v255 offset:44032\n"
    "v_cvt_pk_bf16_f32 v151, v154, v155\n"
    "v_mfma_f32_16x16x32_bf16 v[160:163], v[192:195], a[76:79], v[160:163]\n"
    "ds_read_b128 a[144:147], v255 offset:45056\n"
    "ds_read_b128 a[148:151], v255 offset:46080\n"
    "v_mfma_f32_16x16x32_bf16 v[160:163], v[196:199], a[80:83], v[160:163]\n"
    "ds_read_b128 a[152:155], v255 offset:47104\n"
    "ds_read_b128 a[156:159], v255 offset:48128\n"
    "v_mfma_f32_16x16x32_bf16 v[160:163], v[200:203], a[84:87], v[160:163]\n"
    "s_waitcnt vmcnt(0)\n"
    "ds_write_addtid_b32 v148 offset:1024\n"
    "v_xor_b32_e32 v254, s39, v254\n"
    "v_xor_b32_e32 v253, s39, v253\n"
    "v_mfma_f32_16x16x32_bf16 v[160:163], v[204:207], a[88:91], v[160:163]\n"
    "ds_write_addtid_b32 v149 offset:1280\n"
    "v_or_b32_e32 v253, v253, v254\n"
    "v_cmp_ne_u32_e32 vcc, 0, v253\n"
    "v_mfma_f32_16x16x32_bf16 v[160:163], v[208:211], a[92:95], v[160:163]\n"
    "s_waitcnt lgkmcnt(14)\n"
    "v_exp_f32_e32 v156, v156\n"
    "v_mfma_f32_16x16x32_bf16 v[164:167], a[160:163], a[96:99], v[232:235]\n"
    "v_exp_f32_e32 v157, v157\n"
    "v_mfma_f32_16x16x32_bf16 v[164:167], a[164:167], a[100:103], v[164:167]\n"
    "v_exp_f32_e32 v158, v158\n"
    "v_mfma_f32_16x16x32_bf16 v[164:167], a[168:171], a[104:107], v[164:167]\n"
    "s_cbranch_vccnz .Lat_hpoll\n"
    ".Lat_hpoll_ret:\n"
    "s_waitcnt lgkmcnt(2)\n"
    "s_waitcnt vmcnt(17)\n"
    "s_barrier\n"
    "v_mfma_f32_16x16x32_bf16 v[164:167], a[172:175], a[108:111], v[164:167]\n"
    "v_exp_f32_e32 v159, v159\n"
    "v_mfma_f32_16x16x32_bf16 v[164:167], a[176:179], a[112:115], v[164:167]\n"
    "s_add_u32 m0, s43, 16384\n"
    "v_cvt_pk_bf16_f32 v156, v156, v157\n"
    "v_mfma_f32_16x16x32_bf16 v[164:167], a[180:183], a[116:119], v[164:167]\n"
    "buffer_load_dwordx4 v248, s[8:11], s40 offen offset:0 sc1 lds\n"
    "v_cvt_pk_bf16_f32 v157, v158, v159\n"
    "v_mfma_f32_16x16x32_bf16 v[164:167], a[184:187], a[120:123], v[164:167]\n"
    "v_exp_f32_e32 v160, v160\n"
    "v_mfma_f32_16x16x32_bf16 v[164:167], a[188:191], a[124:127], v[164:167]\n"
    "v_exp_f32_e32 v161, v161\n"
    "v_mfma_f32_16x16x32_bf16 v[168:171], v[180:183], a[96:99], v[232:235]\n"
    "buffer_load_dwordx4 v248, s[8:11], s40 offen offset:1024 sc1 lds\n"
    "v_mfma_f32_16x16x32_bf16 v[168:171], v[184:187], a[100:103], v[168:171]\n"
    "v_exp_f32_e32 v162, v162\n"
    "v_mfma_f32_16x16x32_bf16 v[168:171], v[188:191], a[104:107], v[168:171]\n"
    "v_exp_f32_e32 v163, v163\n"
    "v_mfma_f32_16x16x32_bf16 v[168:171], v[192:195], a[108:111], v[168:171]\n"
    "buffer_load_dwordx4 v248, s[8:11], s40 offen offset:2048 sc1 lds\n"
    "v_mfma_f32_16x16x32_bf16 v[168:171], v[196:199], a[112:115], v[168:171]\n"
    "v_cvt_pk_bf16_f32 v158, v160, v161\n"
    "v_cvt_pk_bf16_f32 v159, v162, v163\n"
    "v_mfma_f32_16x16x32_bf16 v[168:171], v[200:203], a[116:119], v[168:171]\n"
    "v_exp_f32_e32 v164, v164\n"
    "v_mfma_f32_16x16x32_bf16 v[168:171], v[204:207], a[120:123], v[168:171]\n"
    "buffer_load_dwordx4 v248, s[8:11], s40 offen offset:3072 sc1 lds\n"
    "v_mfma_f32_16x16x32_bf16 v[168:171], v[208:211], a[124:127], v[168:171]\n"
    "s_add_u32 s40, s40, 0x8000\n"
    "v_exp_f32_e32 v165, v165\n"
    "v_mfma_f32_16x16x32_bf16 v[172:175], a[160:163], a[128:131], v[236:239]\n"
    "s_add_u32 m0, s43, 20480\n"
    "v_exp_f32_e32 v166, v166\n"
    "v_mfma_f32_16x16x32_bf16 v[172:175], a[164:167], a[132:135], v[172:175]\n"
    "buffer_load_dwordx4 v248, s[8:11], s41 offen offset:0 sc1 lds\n"
    "ds_read_b128 a[160:163], v240 offset:8192\n"
    "v_mfma_f32_16x16x32_bf16 v[172:175], a[168:171], a[136:139], v[172:175]\n"
    "ds_read_b128 a[164:167], v241 offset:8192\n"
    "v_mfma_f32_16x16x32_bf16 v[172:175], a[172:175], a[140:143], v[172:175]\n"
    "ds_read_b128 a[168:171], v240 offset:12288\n"
    "v_mfma_f32_16x16x32_bf16 v[172:175], a[176:179], a[144:147], v[172:175]\n"
    "buffer_load_dwordx4 v248, s[8:11], s41 offen offset:1024 sc1 lds\n"
    "ds_read_b128 a[172:175], v241 offset:12288\n"
    "v_mfma_f32_16x16x32_bf16 v[172:175], a[180:183], a[148:151], v[172:175]\n"
    "ds_read_b128 a[176:179], v242 offset:8192\n"
    "v_exp_f32_e32 v167, v167\n"
    "v_mfma_f32_16x16x32_bf16 v[172:175], a[184:187], a[152:155], v[172:175]\n"
    "ds_read_b128 a[180:183], v243 offset:8192\n"
    "v_cvt_pk_bf16_f32 v164, v164, v165\n"
    "v_mfma_f32_16x16x32_bf16 v[172:175], a[188:191], a[156:159], v[172:175]\n"
    "buffer_load_dwordx4 v248, s[8:11], s41 offen offset:2048 sc1 lds\n"
    "v_cvt_pk_bf16_f32 v165, v166, v167\n"
    "v_mfma_f32_16x16x32_bf16 v[176:179], v[180:183], a[128:131], v[236:239]\n"
    "v_exp_f32_e32 v168, v168\n"
    "v_mfma_f32_16x16x32_bf16 v[176:179], v[184:187], a[132:135], v[176:179]\n"
    "v_exp_f32_e32 v169, v169\n"
    "v_mfma_f32_16x16x32_bf16 v[176:179], v[188:191], a[136:139], v[176:179]\n"
    "buffer_load_dwordx4 v248, s[8:11], s41 offen offset:3072 sc1 lds\n"
    "v_mfma_f32_16x16x32_bf16 v[176:179], v[192:195], a[140:143], v[176:179]\n"
    "s_mov_b32 m0, s42\n"
    "s_add_u32 s41, s41, 0x8000\n"
    "v_exp_f32_e32 v172, v172\n"
    "v_mfma_f32_16x16x32_bf16 v[176:179], v[196:199], a[144:147], v[176:179]\n"
    "ds_write_addtid_b32 v156 offset:2048\n"
    "v_exp_f32_e32 v173, v173\n"
    "v_mfma_f32_16x16x32_bf16 v[176:179], v[200:203], a[148:151], v[176:179]\n"
    "ds_write_addtid_b32 v157 offset:2304\n"
    "v_exp_f32_e32 v174, v174\n"
    "v_mfma_f32_16x16x32_bf16 v[176:179], v[204:207], a[152:155], v[176:179]\n"
    "ds_write_addtid_b32 v164 offset:3072\n"
    "v_exp_f32_e32 v175, v175\n"
    "v_mfma_f32_16x16x32_bf16 v[176:179], v[208:211], a[156:159], v[176:179]\n"
    "ds_write_addtid_b32 v165 offset:3328\n"
    "v_cvt_pk_bf16_f32 v172, v172, v173\n"
    "v_cvt_pk_bf16_f32 v173, v174, v175\n"
    "ds_write_addtid_b32 v172 offset:4096\n"
    "ds_write_addtid_b32 v173 offset:4352\n"
    "s_mul_i32 s45, s33, 320\n"
    "s_lshl_b32 s46, s93, 7\n"
    "s_add_u32 s45, s45, s46\n"
    "buffer_load_dword v254, off, s[24:27], s45\n"
    "s_andn2_b32 s45, s34, 3\n"
    "s_add_u32 s45, s45, s93\n"
    "s_mul_i32 s45, s45, 101376\n"
    "s_add_u32 s45, s45, 16588800\n"
    "buffer_load_dword v254, off, s[8:11], s45\n"
    "s_waitcnt lgkmcnt(10)\n"
    "s_branch .Lat_in\n"
    ".fill 233, 4, 0xbf800000\n"
    ".long 0xbedf00ff\n"
    ".Lat_w2:\n"
    "s_setpc_b64 s[2:3]\n"
    ".Lat_loop:\n"
    "v_mfma_f32_16x16x32_bf16 v[212:215], a[160:163], a[128:131], v[236:239]\n"
    "v_mfma_f32_16x16x32_bf16 v[212:215], a[164:167], a[132:135], v[212:215]\n"
    "v_exp_f32_e32 v170, v170\n"
    "v_mfma_f32_16x16x32_bf16 v[204:207], a[164:167], a[100:103], v[232:235]\n"
    "ds_write_addtid_b32 v143 offset:768\n"
    "v_mfma_f32_16x16x32_bf16 v[204:207], a[160:163], a[96:99], v[204:207]\n"
    "ds_read_b128 a[184:187], v242 offset:28672\n"
    "v_mfma_f32_16x16x32_bf16 v[196:199], a[160:163], a[64:67], v[228:231]\n"
    "v_exp_f32_e32 v171, v171\n"
    "v_mfma_f32_16x16x32_bf16 v[196:199], a[164:167], a[68:71], v[196:199]\n"
    "ds_write_addtid_b32 v150 offset:1536\n"
    "v_mfma_f32_16x16x32_bf16 v[188:191], a[164:167], a[36:39], v[224:227]\n"
    "v_cvt_pk_bf16_f32 v166, v168, v169\n"
    "v_mfma_f32_16x16x32_bf16 v[188:191], a[160:163], a[32:35], v[188:191]\n"
    "v_mfma_f32_16x16x32_bf16 v[180:183], a[160:163], a[0:3], v[220:223]\n"
    "ds_write_addtid_b32 v151 offset:1792\n"
    "v_mfma_f32_16x16x32_bf16 v[180:183], a[164:167], a[4:7], v[180:183]\n"
    "s_waitcnt lgkmcnt(10)\n"
    "v_exp_f32_e32 v176, v176\n"
    "v_mfma_f32_16x16x32_bf16 v[180:183], a[168:171], a[8:11], v[180:183]\n"
    "ds_read_b128 a[188:191], v243 offset:28672\n"
    "v_mfma_f32_16x16x32_bf16 v[180:183], a[172:175], a[12:15], v[180:183]\n"
    "v_exp_f32_e32 v177, v177\n"
    "v_mfma_f32_16x16x32_bf16 v[188:191], a[172:175], a[44:47], v[188:191]\n"
    "ds_write_addtid_b32 v158 offset:2560\n"
    "v_mfma_f32_16x16x32_bf16 v[188:191], a[168:171], a[40:43], v[188:191]\n"
    "ds_read_b128 a[160:163], v240 offset:26624\n"
    "v_mfma_f32_16x16x32_bf16 v[196:199], a[168:171], a[72:75], v[196:199]\n"
    "v_cvt_pk_bf16_f32 v167, v170, v171\n"
    "v_mfma_f32_16x16x32_bf16 v[196:199], a[172:175], a[76:79], v[196:199]\n"
    "ds_write_addtid_b32 v159 offset:2816\n"
    "v_mfma_f32_16x16x32_bf16 v[204:207], a[172:175], a[108:111], v[204:207]\n"
    "v_exp_f32_e32 v178, v178\n"
    "v_mfma_f32_16x16x32_bf16 v[204:207], a[168:171], a[104:107], v[204:207]\n"
    "ds_write_addtid_b32 v166 offset:3584\n"
    "v_mfma_f32_16x16x32_bf16 v[212:215], a[168:171], a[136:139], v[212:215]\n"
    "v_mfma_f32_16x16x32_bf16 v[212:215], a[172:175], a[140:143], v[212:215]\n"
    "s_waitcnt lgkmcnt(10)\n"
    "v_exp_f32_e32 v179, v179\n"
    "v_mfma_f32_16x16x32_bf16 v[212:215], a[176:179], a[144:147], v[212:215]\n"
    "ds_read_b128 a[164:167], v241 offset:26624\n"
    "v_mfma_f32_16x16x32_bf16 v[212:215], a[180:183], a[148:151], v[212:215]\n"
    "v_cvt_pk_bf16_f32 v174, v176, v177\n"
    "v_mfma_f32_16x16x32_bf16 v[204:207], a[180:183], a[116:119], v[204:207]\n"
    "ds_write_addtid_b32 v167 offset:3840\n"
    "v_mfma_f32_16x16x32_bf16 v[204:207], a[176:179], a[112:115], v[204:207]\n"
    "ds_read_b128 a[168:171], v240 offset:30720\n"
    "v_mfma_f32_16x16x32_bf16 v[196:199], a[176:179], a[80:83], v[196:199]\n"
    "v_cvt_pk_bf16_f32 v175, v178, v179\n"
    "v_mfma_f32_16x16x32_bf16 v[196:199], a[180:183], a[84:87], v[196:199]\n"
    "ds_write_addtid_b32 v174 offset:4608\n"
    "v_mfma_f32_16x16x32_bf16 v[188:191], a[180:183], a[52:55], v[188:191]\n"
    "ds_write_addtid_b32 v175 offset:4864\n"
    "v_mfma_f32_16x16x32_bf16 v[188:191], a[176:179], a[48:51], v[188:191]\n"
    "v_mfma_f32_16x16x32_bf16 v[180:183], a[176:179], a[16:19], v[180:183]\n"
    "ds_read_b128 a[172:175], v241 offset:30720\n"
    "v_mfma_f32_16x16x32_bf16 v[180:183], a[180:183], a[20:23], v[180:183]\n"
    "s_waitcnt lgkmcnt(10)\n"
    "s_waitcnt lgkmcnt(1)\n"
    "v_mfma_f32_16x16x32_bf16 v[180:183], a[184:187], a[24:27], v[180:183]\n"
    "s_barrier\n"
    "v_mfma_f32_16x16x32_bf16 v[180:183], a[188:191], a[28:31], v[180:183]\n"
    "s_add_u32 m0, s43, 8192\n"
    "v_mfma_f32_16x16x32_bf16 v[188:191], a[188:191], a[60:63], v[188:191]\n"
    "ds_read_b128 a[176:179], v242 offset:26624\n"
    "v_mfma_f32_16x16x32_bf16 v[188:191], a[184:187], a[56:59], v[188:191]\n"
    "buffer_load_dwordx4 v248, s[8:11], s40 offen offset:0 sc1 lds\n"
    "v_mfma_f32_16x16x32_bf16 v[196:199], a[184:187], a[88:91], v[196:199]\n"
    "ds_read2st64_b32 v[144:145], v249 offset0:0 offset1:1\n"
    "v_mfma_f32_16x16x32_bf16 v[196:199], a[188:191], a[92:95], v[196:199]\n"
    "v_mfma_f32_16x16x32_bf16 v[204:207], a[188:191], a[124:127], v[204:207]\n"
    "ds_read2st64_b32 v[146:147], v249 offset0:2 offset1:3\n"
    "v_mfma_f32_16x16x32_bf16 v[204:207], a[184:187], a[120:123], v[204:207]\n"
    "buffer_load_dwordx4 v248, s[8:11], s40 offen offset:1024 sc1 lds\n"
    "v_mfma_f32_16x16x32_bf16 v[212:215], a[184:187], a[152:155], v[212:215]\n"
    "v_mfma_f32_16x16x32_bf16 v[212:215], a[188:191], a[156:159], v[212:215]\n"
    "v_mfma_f32_16x16x32_bf16 v[216:219], a[160:163], a[128:131], v[236:239]\n"
    "ds_read_b128 a[180:183], v243 offset:26624\n"
    "v_mfma_f32_16x16x32_bf16 v[216:219], a[164:167], a[132:135], v[216:219]\n"
    "buffer_load_dwordx4 v248, s[8:11], s40 offen offset:2048 sc1 lds\n"
    "v_mfma_f32_16x16x32_bf16 v[208:211], a[164:167], a[100:103], v[232:235]\n"
    "ds_read2st64_b32 v[152:153], v249 offset0:4 offset1:5\n"
    "v_mfma_f32_16x16x32_bf16 v[208:211], a[160:163], a[96:99], v[208:211]\n"
    "ds_read_b128 a[184:187], v242 offset:30720\n"
    "v_mfma_f32_16x16x32_bf16 v[200:203], a[160:163], a[64:67], v[228:231]\n"
    "ds_read2st64_b32 v[154:155], v249 offset0:6 offset1:7\n"
    "v_mfma_f32_16x16x32_bf16 v[200:203], a[164:167], a[68:71], v[200:203]\n"
    "buffer_load_dwordx4 v248, s[8:11], s40 offen offset:3072 sc1 lds\n"
    "v_mfma_f32_16x16x32_bf16 v[192:195], a[164:167], a[36:39], v[224:227]\n"
    "s_add_u32 m0, s43, 12288\n"
    "v_exp_f32_e32 v180, v180\n"
    "v_mfma_f32_16x16x32_bf16 v[192:195], a[160:163], a[32:35], v[192:195]\n"
    "s_add_u32 s40, s40, 0x8000\n"
    "v_exp_f32_e32 v181, v181\n"
    "v_mfma_f32_16x16x32_bf16 v[184:187], a[160:163], a[0:3], v[220:223]\n"
    "ds_read2st64_b32 v[160:161], v249 offset0:8 offset1:9\n"
    "v_mfma_f32_16x16x32_bf16 v[184:187], a[164:167], a[4:7], v[184:187]\n"
    "s_waitcnt lgkmcnt(8)\n"
    "buffer_load_dwordx4 v248, s[8:11], s41 offen offset:0 sc1 lds\n"
    "v_mfma_f32_16x16x32_bf16 v[184:187], a[168:171], a[8:11], v[184:187]\n"
    ".long 0xbedf00ff\n"
    ".Lat_w3:\n"
    "s_setpc_b64 s[2:3]\n"
    "ds_read_b128 a[188:191], v243 offset:30720\n"
    "v_mfma_f32_16x16x32_bf16 v[184:187], a[172:175], a[12:15], v[184:187]\n"
    "ds_read_b64_tr_b16 a[160:161], v244 offset:16384\n"
    "v_mfma_f32_16x16x32_bf16 v[192:195], a[172:175], a[44:47], v[192:195]\n"
    "ds_read2st64_b32 v[162:163], v249 offset0:10 offset1:11\n"
    "v_mfma_f32_16x16x32_bf16 v[192:195], a[168:171], a[40:43], v[192:195]\n"
    "buffer_load_dwordx4 v248, s[8:11], s41 offen offset:1024 sc1 lds\n"
    "v_mfma_f32_16x16x32_bf16 v[200:203], a[168:171], a[72:75], v[200:203]\n"
    "ds_read_b64_tr_b16 a[162:163], v244 offset:18432\n"
    "v_mfma_f32_16x16x32_bf16 v[200:203], a[172:175], a[76:79], v[200:203]\n"
    "v_exp_f32_e32 v182, v182\n"
    "v_mfma_f32_16x16x32_bf16 v[208:211], a[172:175], a[108:111], v[208:211]\n"
    "ds_read2st64_b32 v[168:169], v249 offset0:12 offset1:13\n"
    "v_mfma_f32_16x16x32_bf16 v[208:211], a[168:171], a[104:107], v[208:211]\n"
    "buffer_load_dwordx4 v248, s[8:11], s41 offen offset:2048 sc1 lds\n"
    "v_mfma_f32_16x16x32_bf16 v[216:219], a[168:171], a[136:139], v[216:219]\n"
    "ds_read_b64_tr_b16 a[164:165], v244 offset:49152\n"
    "v_mfma_f32_16x16x32_bf16 v[216:219], a[172:175], a[140:143], v[216:219]\n"
    "s_waitcnt lgkmcnt(10)\n"
    "ds_read2st64_b32 v[170:171], v249 offset0:14 offset1:15\n"
    "v_mfma_f32_16x16x32_bf16 v[216:219], a[176:179], a[144:147], v[216:219]\n"
    "ds_read_b64_tr_b16 a[168:169], v245 offset:16384\n"
    "v_mfma_f32_16x16x32_bf16 v[216:219], a[180:183], a[148:151], v[216:219]\n"
    "buffer_load_dwordx4 v248, s[8:11], s41 offen offset:3072 sc1 lds\n"
    "v_mfma_f32_16x16x32_bf16 v[208:211], a[180:183], a[116:119], v[208:211]\n"
    "ds_read_b64_tr_b16 a[166:167], v244 offset:51200\n"
    "v_mfma_f32_16x16x32_bf16 v[208:211], a[176:179], a[112:115], v[208:211]\n"
    "s_add_u32 s41, s41, 0x8000\n"
    "v_exp_f32_e32 v183, v183\n"
    "v_mfma_f32_16x16x32_bf16 v[200:203], a[176:179], a[80:83], v[200:203]\n"
    "ds_read_b64_tr_b16 a[170:171], v245 offset:18432\n"
    "v_cvt_pk_bf16_f32 v180, v180, v181\n"
    "v_mfma_f32_16x16x32_bf16 v[200:203], a[180:183], a[84:87], v[200:203]\n"
    "ds_read2st64_b32 v[176:177], v249 offset0:16 offset1:17\n"
    "v_mfma_f32_16x16x32_bf16 v[192:195], a[180:183], a[52:55], v[192:195]\n"
    "v_exp_f32_e32 v188, v188\n"
    "v_mfma_f32_16x16x32_bf16 v[192:195], a[176:179], a[48:51], v[192:195]\n"
    "v_exp_f32_e32 v189, v189\n"
    "v_mfma_f32_16x16x32_bf16 v[184:187], a[176:179], a[16:19], v[184:187]\n"
    "ds_read_b64_tr_b16 a[172:173], v245 offset:49152\n"
    "v_cvt_pk_bf16_f32 v181, v182, v183\n"
    "v_mfma_f32_16x16x32_bf16 v[184:187], a[180:183], a[20:23], v[184:187]\n"
    "s_waitcnt lgkmcnt(11)\n"
    "ds_read2st64_b32 v[178:179], v249 offset0:18 offset1:19\n"
    "v_mfma_f32_16x16x32_bf16 v[184:187], a[184:187], a[24:27], v[184:187]\n"
    "ds_read_b64_tr_b16 a[176:177], v246 offset:16384\n"
    "v_mfma_f32_16x16x32_bf16 v[184:187], a[188:191], a[28:31], v[184:187]\n"
    "ds_read_b64_tr_b16 a[174:175], v245 offset:51200\n"
    "v_mfma_f32_16x16x32_bf16 v[192:195], a[188:191], a[60:63], v[192:195]\n"
    "v_exp_f32_e32 v190, v190\n"
    "v_mfma_f32_16x16x32_bf16 v[192:195], a[184:187], a[56:59], v[192:195]\n"
    "v_exp_f32_e32 v191, v191\n"
    "v_mfma_f32_16x16x32_bf16 v[200:203], a[184:187], a[88:91], v[200:203]\n"
    "v_mfma_f32_16x16x32_bf16 v[200:203], a[188:191], a[92:95], v[200:203]\n"
    "ds_read_b64_tr_b16 a[178:179], v246 offset:18432\n"
    "v_mfma_f32_16x16x32_bf16 v[208:211], a[188:191], a[124:127], v[208:211]\n"
    "v_cvt_pk_bf16_f32 v188, v188, v189\n"
    "v_mfma_f32_16x16x32_bf16 v[208:211], a[184:187], a[120:123], v[208:211]\n"
    "v_exp_f32_e32 v196, v196\n"
    "v_mfma_f32_16x16x32_bf16 v[216:219], a[184:187], a[152:155], v[216:219]\n"
    "ds_read_b64_tr_b16 a[180:181], v246 offset:49152\n"
    "v_mfma_f32_16x16x32_bf16 v[216:219], a[188:191], a[156:159], v[216:219]\n"
    "s_waitcnt lgkmcnt(4)\n"
    "v_exp_f32_e32 v197, v197\n"
    "v_mfma_f32_16x16x32_bf16 a[236:239], a[160:163], v[140:143], a[236:239]\n"
    "v_mfma_f32_16x16x32_bf16 a[236:239], a[164:167], v[144:147], a[236:239]\n"
    "ds_read_b64_tr_b16 a[182:183], v246 offset:51200\n"
    "v_mfma_f32_16x16x32_bf16 a[240:243], a[164:167], v[152:155], a[240:243]\n"
    "ds_read_b64_tr_b16 a[184:185], v247 offset:16384\n"
    "v_mfma_f32_16x16x32_bf16 a[240:243], a[160:163], v[148:151], a[240:243]\n"
    "v_cvt_pk_bf16_f32 v189, v190, v191\n"
    "v_mfma_f32_16x16x32_bf16 a[244:247], a[160:163], v[156:159], a[244:247]\n"
    "ds_read_b64_tr_b16 a[186:187], v247 offset:18432\n"
    "v_mfma_f32_16x16x32_bf16 a[244:247], a[164:167], v[160:163], a[244:247]\n"
    "v_exp_f32_e32 v198, v198\n"
    "v_mfma_f32_16x16x32_bf16 a[248:251], a[164:167], v[168:171], a[248:251]\n"
    "v_exp_f32_e32 v199, v199\n"
    "v_mfma_f32_16x16x32_bf16 a[248:251], a[160:163], v[164:167], a[248:251]\n"
    "v_cvt_pk_bf16_f32 v196, v196, v197\n"
    "v_mfma_f32_16x16x32_bf16 a[252:255], a[160:163], v[172:175], a[252:255]\n"
    "ds_read_b64_tr_b16 a[188:189], v247 offset:49152\n"
    "v_mfma_f32_16x16x32_bf16 a[252:255], a[164:167], v[176:179], a[252:255]\n"
    "s_waitcnt lgkmcnt(6)\n"
    "v_exp_f32_e32 v204, v204\n"
    "v_mfma_f32_16x16x32_bf16 v[16:19], a[172:175], v[176:179], v[16:19]\n"
    "ds_read_b64_tr_b16 a[160:161], v244 offset:20480\n"
    "v_mfma_f32_16x16x32_bf16 v[16:19], a[168:171], v[172:175], v[16:19]\n"
    "ds_read_b64_tr_b16 a[190:191], v247 offset:51200\n"
    "v_mfma_f32_16x16x32_bf16 v[12:15], a[168:171], v[164:167], v[12:15]\n"
    "v_exp_f32_e32 v205, v205\n"
    "v_mfma_f32_16x16x32_bf16 v[12:15], a[172:175], v[168:171], v[12:15]\n"
    "v_cvt_pk_bf16_f32 v197, v198, v199\n"
    "v_mfma_f32_16x16x32_bf16 v[8:11], a[172:175], v[160:163], v[8:11]\n"
    "ds_read_b64_tr_b16 a[162:163], v244 offset:22528\n"
    "v_mfma_f32_16x16x32_bf16 v[8:11], a[168:171], v[156:159], v[8:11]\n"
    "v_exp_f32_e32 v206, v206\n"
    "v_mfma_f32_16x16x32_bf16 v[4:7], a[168:171], v[148:151], v[4:7]\n"
    "v_exp_f32_e32 v207, v207\n"
    "v_mfma_f32_16x16x32_bf16 v[4:7], a[172:175], v[152:155], v[4:7]\n"
    "v_cvt_pk_bf16_f32 v204, v204, v205\n"
    "v_mfma_f32_16x16x32_bf16 v[0:3], a[172:175], v[144:147], v[0:3]\n"
    "ds_read_b64_tr_b16 a[164:165], v244 offset:53248\n"
    "v_mfma_f32_16x16x32_bf16 v[0:3], a[168:171], v[140:143], v[0:3]\n"
    "s_waitcnt lgkmcnt(7)\n"
    "v_exp_f32_e32 v212, v212\n"
    "v_mfma_f32_16x16x32_bf16 v[20:23], a[176:179], v[140:143], v[20:23]\n"
    "v_mfma_f32_16x16x32_bf16 v[20:23], a[180:183], v[144:147], v[20:23]\n"
    "ds_read_b64_tr_b16 a[166:167], v244 offset:55296\n"
    "v_mfma_f32_16x16x32_bf16 v[24:27], a[180:183], v[152:155], v[24:27]\n"
    "ds_read_b64_tr_b16 a[168:169], v245 offset:20480\n"
    "v_mfma_f32_16x16x32_bf16 v[24:27], a[176:179], v[148:151], v[24:27]\n"
    "v_exp_f32_e32 v213, v213\n"
    "v_mfma_f32_16x16x32_bf16 v[28:31], a[176:179], v[156:159], v[28:31]\n"
    "ds_read_b64_tr_b16 a[170:171], v245 offset:22528\n"
    "v_mfma_f32_16x16x32_bf16 v[28:31], a[180:183], v[160:163], v[28:31]\n"
    "v_cvt_pk_bf16_f32 v205, v206, v207\n"
    "v_mfma_f32_16x16x32_bf16 v[32:35], a[180:183], v[168:171], v[32:35]\n"
    "v_exp_f32_e32 v214, v214\n"
    "v_mfma_f32_16x16x32_bf16 v[32:35], a[176:179], v[164:167], v[32:35]\n"
    "v_exp_f32_e32 v215, v215\n"
    "v_mfma_f32_16x16x32_bf16 v[36:39], a[176:179], v[172:175], v[36:39]\n"
    "ds_read_b64_tr_b16 a[172:173], v245 offset:53248\n"
    "v_mfma_f32_16x16x32_bf16 v[36:39], a[180:183], v[176:179], v[36:39]\n"
    "s_waitcnt lgkmcnt(6)\n"
    "v_cvt_pk_bf16_f32 v212, v212, v213\n"
    "v_mfma_f32_16x16x32_bf16 v[56:59], a[188:191], v[176:179], v[56:59]\n"
    "ds_read_b64_tr_b16 a[176:177], v246 offset:20480\n"
    "v_mfma_f32_16x16x32_bf16 v[56:59], a[184:187], v[172:175], v[56:59]\n"
    "ds_read_b64_tr_b16 a[174:175], v245 offset:55296\n"
    "v_mfma_f32_16x16x32_bf16 v[52:55], a[184:187], v[164:167], v[52:55]\n"
    "v_cvt_pk_bf16_f32 v213, v214, v215\n"
    "v_mfma_f32_16x16x32_bf16 v[52:55], a[188:191], v[168:171], v[52:55]\n"
    "v_exp_f32_e32 v184, v184\n"
    "v_mfma_f32_16x16x32_bf16 v[48:51], a[188:191], v[160:163], v[48:51]\n"
    "ds_read_b64_tr_b16 a[178:179], v246 offset:22528\n"
    "v_mfma_f32_16x16x32_bf16 v[48:51], a[184:187], v[156:159], v[48:51]\n"
    "v_mfma_f32_16x16x32_bf16 v[44:47], a[184:187], v[148:151], v[44:47]\n"
    "v_exp_f32_e32 v185, v185\n"
    "v_mfma_f32_16x16x32_bf16 v[44:47], a[188:191], v[152:155], v[44:47]\n"
    "v_exp_f32_e32 v186, v186\n"
    "v_mfma_f32_16x16x32_bf16 v[40:43], a[188:191], v[144:147], v[40:43]\n"
    "ds_read_b64_tr_b16 a[180:181], v246 offset:53248\n"
    "v_mfma_f32_16x16x32_bf16 v[40:43], a[184:187], v[140:143], v[40:43]\n"
    "s_waitcnt lgkmcnt(7)\n"
    "v_exp_f32_e32 v187, v187\n"
    "v_mfma_f32_16x16x32_bf16 v[60:63], a[160:163], v[140:143], v[60:63]\n"
    "ds_read_b64_tr_b16 a[184:185], v247 offset:20480\n"
    "v_mfma_f32_16x16x32_bf16 v[60:63], a[164:167], v[144:147], v[60:63]\n"
    "ds_read_b64_tr_b16 a[182:183], v246 offset:55296\n"
    "v_mfma_f32_16x16x32_bf16 v[64:67], a[164:167], v[152:155], v[64:67]\n"
    "ds_read_b64_tr_b16 a[186:187], v247 offset:22528\n"
    "v_mfma_f32_16x16x32_bf16 v[64:67], a[160:163], v[148:151], v[64:67]\n"
    "v_cvt_pk_bf16_f32 v182, v184, v185\n"
    "v_mfma_f32_16x16x32_bf16 v[68:71], a[160:163], v[156:159], v[68:71]\n"
    "v_exp_f32_e32 v192, v192\n"
    "v_mfma_f32_16x16x32_bf16 v[68:71], a[164:167], v[160:163], v[68:71]\n"
    "ds_read_b64_tr_b16 a[188:189], v247 offset:53248\n"
    "v_mfma_f32_16x16x32_bf16 v[72:75], a[164:167], v[168:171], v[72:75]\n"
    "ds_read_b64_tr_b16 a[190:191], v247 offset:55296\n"
    "v_mfma_f32_16x16x32_bf16 v[72:75], a[160:163], v[164:167], v[72:75]\n"
    "s_waitcnt vmcnt(9)\n"
    "v_mfma_f32_16x16x32_bf16 v[76:79], a[160:163], v[172:175], v[76:79]\n"
    "s_barrier\n"
    "v_mfma_f32_16x16x32_bf16 v[76:79], a[164:167], v[176:179], v[76:79]\n"
    "s_waitcnt lgkmcnt(7)\n"
    "s_mov_b32 m0, s42\n"
    "v_mfma_f32_16x16x32_bf16 v[96:99], a[172:175], v[176:179], v[96:99]\n"
    "v_exp_f32_e32 v193, v193\n"
    "v_mfma_f32_16x16x32_bf16 v[96:99], a[168:171], v[172:175], v[96:99]\n"
    "v_cvt_pk_bf16_f32 v183, v186, v187\n"
    "v_mfma_f32_16x16x32_bf16 v[92:95], a[168:171], v[164:167], v[92:95]\n"
    "v_exp_f32_e32 v194, v194\n"
    "v_mfma_f32_16x16x32_bf16 v[92:95], a[172:175], v[168:171], v[92:95]\n"
    "ds_read_b128 a[160:163], v240 offset:0\n"
    "v_mfma_f32_16x16x32_bf16 v[88:91], a[172:175], v[160:163], v[88:91]\n"
    "ds_write_addtid_b32 v180 offset:0\n"
    "v_mfma_f32_16x16x32_bf16 v[88:91], a[168:171], v[156:159], v[88:91]\n"
    "v_exp_f32_e32 v195, v195\n"
    "v_mfma_f32_16x16x32_bf16 v[84:87], a[168:171], v[148:151], v[84:87]\n"
    "ds_write_addtid_b32 v181 offset:256\n"
    "v_mfma_f32_16x16x32_bf16 v[84:87], a[172:175], v[152:155], v[84:87]\n"
    "ds_write_addtid_b32 v188 offset:1024\n"
    "v_mfma_f32_16x16x32_bf16 v[80:83], a[172:175], v[144:147], v[80:83]\n"
    "v_cvt_pk_bf16_f32 v190, v192, v193\n"
    "v_mfma_f32_16x16x32_bf16 v[80:83], a[168:171], v[140:143], v[80:83]\n"
    "s_waitcnt lgkmcnt(7)\n"
    "v_exp_f32_e32 v200, v200\n"
    "v_mfma_f32_16x16x32_bf16 v[100:103], a[176:179], v[140:143], v[100:103]\n"
    "ds_read_b128 a[164:167], v241 offset:0\n"
    "v_mfma_f32_16x16x32_bf16 v[100:103], a[180:183], v[144:147], v[100:103]\n"
    "ds_write_addtid_b32 v189 offset:1280\n"
    "v_mfma_f32_16x16x32_bf16 v[104:107], a[180:183], v[152:155], v[104:107]\n"
    "v_exp_f32_e32 v201, v201\n"
    "v_mfma_f32_16x16x32_bf16 v[104:107], a[176:179], v[148:151], v[104:107]\n"
    "ds_read_b128 a[168:171], v240 offset:4096\n"
    "v_mfma_f32_16x16x32_bf16 v[108:111], a[176:179], v[156:159], v[108:111]\n"
    "ds_write_addtid_b32 v196 offset:2048\n"
    "v_mfma_f32_16x16x32_bf16 v[108:111], a[180:183], v[160:163], v[108:111]\n"
    "v_cvt_pk_bf16_f32 v191, v194, v195\n"
    "v_mfma_f32_16x16x32_bf16 v[112:115], a[180:183], v[168:171], v[112:115]\n"
    "ds_write_addtid_b32 v197 offset:2304\n"
    "v_mfma_f32_16x16x32_bf16 v[112:115], a[176:179], v[164:167], v[112:115]\n"
    "v_exp_f32_e32 v202, v202\n"
    "v_mfma_f32_16x16x32_bf16 v[116:119], a[176:179], v[172:175], v[116:119]\n"
    "ds_write_addtid_b32 v204 offset:3072\n"
    "v_mfma_f32_16x16x32_bf16 v[116:119], a[180:183], v[176:179], v[116:119]\n"
    "s_waitcnt lgkmcnt(10)\n"
    "v_exp_f32_e32 v203, v203\n"
    "v_mfma_f32_16x16x32_bf16 v[136:139], a[188:191], v[176:179], v[136:139]\n"
    "ds_read_b128 a[172:175], v241 offset:4096\n"
    "v_mfma_f32_16x16x32_bf16 v[136:139], a[184:187], v[172:175], v[136:139]\n"
    "ds_write_addtid_b32 v205 offset:3328\n"
    "v_cvt_pk_bf16_f32 v198, v200, v201\n"
    "v_mfma_f32_16x16x32_bf16 v[132:135], a[184:187], v[164:167], v[132:135]\n"
    "ds_read_b128 a[176:179], v242 offset:0\n"
    "v_mfma_f32_16x16x32_bf16 v[132:135], a[188:191], v[168:171], v[132:135]\n"
    "ds_write_addtid_b32 v212 offset:4096\n"
    "v_mfma_f32_16x16x32_bf16 v[128:131], a[188:191], v[160:163], v[128:131]\n"
    "v_exp_f32_e32 v208, v208\n"
    "v_mfma_f32_16x16x32_bf16 v[128:131], a[184:187], v[156:159], v[128:131]\n"
    "ds_write_addtid_b32 v213 offset:4352\n"
    "v_mfma_f32_16x16x32_bf16 v[124:127], a[184:187], v[148:151], v[124:127]\n"
    "v_exp_f32_e32 v209, v209\n"
    "v_mfma_f32_16x16x32_bf16 v[124:127], a[188:191], v[152:155], v[124:127]\n"
    "ds_read_b128 a[180:183], v243 offset:0\n"
    "v_mfma_f32_16x16x32_bf16 v[120:123], a[188:191], v[144:147], v[120:123]\n"
    "ds_write_addtid_b32 v182 offset:512\n"
    "v_mfma_f32_16x16x32_bf16 v[120:123], a[184:187], v[140:143], v[120:123]\n"
    "s_waitcnt lgkmcnt(12)\n"
    "v_cvt_pk_bf16_f32 v199, v202, v203\n"
    "v_mfma_f32_16x16x32_bf16 v[172:175], a[160:163], a[128:131], v[236:239]\n"
    "v_mfma_f32_16x16x32_bf16 v[172:175], a[164:167], a[132:135], v[172:175]\n"
    "v_exp_f32_e32 v210, v210\n"
    "v_mfma_f32_16x16x32_bf16 v[164:167], a[164:167], a[100:103], v[232:235]\n"
    "ds_write_addtid_b32 v183 offset:768\n"
    "v_mfma_f32_16x16x32_bf16 v[164:167], a[160:163], a[96:99], v[164:167]\n"
    "ds_read_b128 a[184:187], v242 offset:4096\n"
    "v_mfma_f32_16x16x32_bf16 v[156:159], a[160:163], a[64:67], v[228:231]\n"
    "v_exp_f32_e32 v211, v211\n"
    "v_mfma_f32_16x16x32_bf16 v[156:159], a[164:167], a[68:71], v[156:159]\n"
    "ds_write_addtid_b32 v190 offset:1536\n"
    "v_mfma_f32_16x16x32_bf16 v[148:151], a[164:167], a[36:39], v[224:227]\n"
    "v_cvt_pk_bf16_f32 v206, v208, v209\n"
    "v_mfma_f32_16x16x32_bf16 v[148:151], a[160:163], a[32:35], v[148:151]\n"
    "v_mfma_f32_16x16x32_bf16 v[140:143], a[160:163], a[0:3], v[220:223]\n"
    "ds_write_addtid_b32 v191 offset:1792\n"
    "v_mfma_f32_16x16x32_bf16 v[140:143], a[164:167], a[4:7], v[140:143]\n"
    "s_waitcnt lgkmcnt(10)\n"
    "v_exp_f32_e32 v216, v216\n"
    "v_mfma_f32_16x16x32_bf16 v[140:143], a[168:171], a[8:11], v[140:143]\n"
    "ds_read_b128 a[188:191], v243 offset:4096\n"
    "v_mfma_f32_16x16x32_bf16 v[140:143], a[172:175], a[12:15], v[140:143]\n"
    "v_exp_f32_e32 v217, v217\n"
    "v_mfma_f32_16x16x32_bf16 v[148:151], a[172:175], a[44:47], v[148:151]\n"
    "ds_write_addtid_b32 v198 offset:2560\n"
    "v_mfma_f32_16x16x32_bf16 v[148:151], a[168:171], a[40:43], v[148:151]\n"
    "ds_read_b128 a[160:163], v240 offset:2048\n"
    "v_mfma_f32_16x16x32_bf16 v[156:159], a[168:171], a[72:75], v[156:159]\n"
    "v_cvt_pk_bf16_f32 v207, v210, v211\n"
    "v_mfma_f32_16x16x32_bf16 v[156:159], a[172:175], a[76:79], v[156:159]\n"
    "ds_write_addtid_b32 v199 offset:2816\n"
    "v_mfma_f32_16x16x32_bf16 v[164:167], a[172:175], a[108:111], v[164:167]\n"
    "v_exp_f32_e32 v218, v218\n"
    "v_mfma_f32_16x16x32_bf16 v[164:167], a[168:171], a[104:107], v[164:167]\n"
    "ds_write_addtid_b32 v206 offset:3584\n"
    "v_mfma_f32_16x16x32_bf16 v[172:175], a[168:171], a[136:139], v[172:175]\n"
    "v_mfma_f32_16x16x32_bf16 v[172:175], a[172:175], a[140:143], v[172:175]\n"
    "s_waitcnt lgkmcnt(10)\n"
    "v_exp_f32_e32 v219, v219\n"
    "v_mfma_f32_16x16x32_bf16 v[172:175], a[176:179], a[144:147], v[172:175]\n"
    "ds_read_b128 a[164:167], v241 offset:2048\n"
    "v_mfma_f32_16x16x32_bf16 v[172:175], a[180:183], a[148:151], v[172:175]\n"
    "v_cvt_pk_bf16_f32 v214, v216, v217\n"
    "v_mfma_f32_16x16x32_bf16 v[164:167], a[180:183], a[116:119], v[164:167]\n"
    "ds_write_addtid_b32 v207 offset:3840\n"
    "v_mfma_f32_16x16x32_bf16 v[164:167], a[176:179], a[112:115], v[164:167]\n"
    "ds_read_b128 a[168:171], v240 offset:6144\n"
    "v_mfma_f32_16x16x32_bf16 v[156:159], a[176:179], a[80:83], v[156:159]\n"
    "v_cvt_pk_bf16_f32 v215, v218, v219\n"
    "v_mfma_f32_16x16x32_bf16 v[156:159], a[180:183], a[84:87], v[156:159]\n"
    "ds_write_addtid_b32 v214 offset:4608\n"
    "v_mfma_f32_16x16x32_bf16 v[148:151], a[180:183], a[52:55], v[148:151]\n"
    "ds_write_addtid_b32 v215 offset:4864\n"
    "v_mfma_f32_16x16x32_bf16 v[148:151], a[176:179], a[48:51], v[148:151]\n"
    "v_mfma_f32_16x16x32_bf16 v[140:143], a[176:179], a[16:19], v[140:143]\n"
    "ds_read_b128 a[172:175], v241 offset:6144\n"
    "v_mfma_f32_16x16x32_bf16 v[140:143], a[180:183], a[20:23], v[140:143]\n"
    "s_waitcnt lgkmcnt(10)\n"
    "s_waitcnt lgkmcnt(1)\n"
    "v_mfma_f32_16x16x32_bf16 v[140:143], a[184:187], a[24:27], v[140:143]\n"
    "s_barrier\n"
    "v_mfma_f32_16x16x32_bf16 v[140:143], a[188:191], a[28:31], v[140:143]\n"
    "s_add_u32 m0, s43, 16384\n"
    "v_mfma_f32_16x16x32_bf16 v[148:151], a[188:191], a[60:63], v[148:151]\n"
    "ds_read_b128 a[176:179], v242 offset:2048\n"
    "v_mfma_f32_16x16x32_bf16 v[148:151], a[184:187], a[56:59], v[148:151]\n"
    "buffer_load_dwordx4 v248, s[8:11], s40 offen offset:0 sc1 lds\n"
    "v_mfma_f32_16x16x32_bf16 v[156:159], a[184:187], a[88:91], v[156:159]\n"
    "ds_read2st64_b32 v[184:185], v249 offset0:0 offset1:1\n"
    "v_mfma_f32_16x16x32_bf16 v[156:159], a[188:191], a[92:95], v[156:159]\n"
    "v_mfma_f32_16x16x32_bf16 v[164:167], a[188:191], a[124:127], v[164:167]\n"
    "ds_read2st64_b32 v[186:187], v249 offset0:2 offset1:3\n"
    "v_mfma_f32_16x16x32_bf16 v[164:167], a[184:187], a[120:123], v[164:167]\n"
    "buffer_load_dwordx4 v248, s[8:11], s40 offen offset:1024 sc1 lds\n"
    "v_mfma_f32_16x16x32_bf16 v[172:175], a[184:187], a[152:155], v[172:175]\n"
    "v_mfma_f32_16x16x32_bf16 v[172:175], a[188:191], a[156:159], v[172:175]\n"
    "v_mfma_f32_16x16x32_bf16 v[176:179], a[160:163], a[128:131], v[236:239]\n"
    "ds_read_b128 a[180:183], v243 offset:2048\n"
    "v_mfma_f32_16x16x32_bf16 v[176:179], a[164:167], a[132:135], v[176:179]\n"
    "buffer_load_dwordx4 v248, s[8:11], s40 offen offset:2048 sc1 lds\n"
    "v_mfma_f32_16x16x32_bf16 v[168:171], a[164:167], a[100:103], v[232:235]\n"
    "ds_read2st64_b32 v[192:193], v249 offset0:4 offset1:5\n"
    "v_mfma_f32_16x16x32_bf16 v[168:171], a[160:163], a[96:99], v[168:171]\n"
    "ds_read_b128 a[184:187], v242 offset:6144\n"
    "v_mfma_f32_16x16x32_bf16 v[160:163], a[160:163], a[64:67], v[228:231]\n"
    "ds_read2st64_b32 v[194:195], v249 offset0:6 offset1:7\n"
    "v_mfma_f32_16x16x32_bf16 v[160:163], a[164:167], a[68:71], v[160:163]\n"
    "buffer_load_dwordx4 v248, s[8:11], s40 offen offset:3072 sc1 lds\n"
    "v_mfma_f32_16x16x32_bf16 v[152:155], a[164:167], a[36:39], v[224:227]\n"
    "s_add_u32 m0, s43, 20480\n"
    "v_exp_f32_e32 v140, v140\n"
    "v_mfma_f32_16x16x32_bf16 v[152:155], a[160:163], a[32:35], v[152:155]\n"
    "s_add_u32 s40, s40, 0x8000\n"
    "v_exp_f32_e32 v141, v141\n"
    "v_mfma_f32_16x16x32_bf16 v[144:147], a[160:163], a[0:3], v[220:223]\n"
    "ds_read2st64_b32 v[200:201], v249 offset0:8 offset1:9\n"
    "v_mfma_f32_16x16x32_bf16 v[144:147], a[164:167], a[4:7], v[144:147]\n"
    "s_waitcnt lgkmcnt(8)\n"
    "buffer_load_dwordx4 v248, s[8:11], s41 offen offset:0 sc1 lds\n"
    "v_mfma_f32_16x16x32_bf16 v[144:147], a[168:171], a[8:11], v[144:147]\n"
    "ds_read_b128 a[188:191], v243 offset:6144\n"
    "v_mfma_f32_16x16x32_bf16 v[144:147], a[172:175], a[12:15], v[144:147]\n"
    "ds_read_b64_tr_b16 a[160:161], v244 offset:24576\n"
    "v_mfma_f32_16x16x32_bf16 v[152:155], a[172:175], a[44:47], v[152:155]\n"
    "ds_read2st64_b32 v[202:203], v249 offset0:10 offset1:11\n"
    "v_mfma_f32_16x16x32_bf16 v[152:155], a[168:171], a[40:43], v[152:155]\n"
    "buffer_load_dwordx4 v248, s[8:11], s41 offen offset:1024 sc1 lds\n"
    "v_mfma_f32_16x16x32_bf16 v[160:163], a[168:171], a[72:75], v[160:163]\n"
    "ds_read_b64_tr_b16 a[162:163], v244 offset:26624\n"
    "v_mfma_f32_16x16x32_bf16 v[160:163], a[172:175], a[76:79], v[160:163]\n"
    "v_exp_f32_e32 v142, v142\n"
    "v_mfma_f32_16x16x32_bf16 v[168:171], a[172:175], a[108:111], v[168:171]\n"
    "ds_read2st64_b32 v[208:209], v249 offset0:12 offset1:13\n"
    "v_mfma_f32_16x16x32_bf16 v[168:171], a[168:171], a[104:107], v[168:171]\n"
    "buffer_load_dwordx4 v248, s[8:11], s41 offen offset:2048 sc1 lds\n"
    "v_mfma_f32_16x16x32_bf16 v[176:179], a[168:171], a[136:139], v[176:179]\n"
    "ds_read_b64_tr_b16 a[164:165], v244 offset:57344\n"
    "v_mfma_f32_16x16x32_bf16 v[176:179], a[172:175], a[140:143], v[176:179]\n"
    "s_waitcnt lgkmcnt(10)\n"
    "ds_read2st64_b32 v[210:211], v249 offset0:14 offset1:15\n"
    "v_mfma_f32_16x16x32_bf16 v[176:179], a[176:179], a[144:147], v[176:179]\n"
    "ds_read_b64_tr_b16 a[168:169], v245 offset:24576\n"
    "v_mfma_f32_16x16x32_bf16 v[176:179], a[180:183], a[148:151], v[176:179]\n"
    "buffer_load_dwordx4 v248, s[8:11], s41 offen offset:3072 sc1 lds\n"
    "v_mfma_f32_16x16x32_bf16 v[168:171], a[180:183], a[116:119], v[168:171]\n"
    "ds_read_b64_tr_b16 a[166:167], v244 offset:59392\n"
    "v_mfma_f32_16x16x32_bf16 v[168:171], a[176:179], a[112:115], v[168:171]\n"
    "s_add_u32 s41, s41, 0x8000\n"
    "v_exp_f32_e32 v143, v143\n"
    "v_mfma_f32_16x16x32_bf16 v[160:163], a[176:179], a[80:83], v[160:163]\n"
    "ds_read_b64_tr_b16 a[170:171], v245 offset:26624\n"
    "v_cvt_pk_bf16_f32 v140, v140, v141\n"
    "v_mfma_f32_16x16x32_bf16 v[160:163], a[180:183], a[84:87], v[160:163]\n"
    "ds_read2st64_b32 v[216:217], v249 offset0:16 offset1:17\n"
    "v_mfma_f32_16x16x32_bf16 v[152:155], a[180:183], a[52:55], v[152:155]\n"
    "v_exp_f32_e32 v148, v148\n"
    "v_mfma_f32_16x16x32_bf16 v[152:155], a[176:179], a[48:51], v[152:155]\n"
    "v_exp_f32_e32 v149, v149\n"
    "v_mfma_f32_16x16x32_bf16 v[144:147], a[176:179], a[16:19], v[144:147]\n"
    "ds_read_b64_tr_b16 a[172:173], v245 offset:57344\n"
    "v_cvt_pk_bf16_f32 v141, v142, v143\n"
    "v_mfma_f32_16x16x32_bf16 v[144:147], a[180:183], a[20:23], v[144:147]\n"
    "s_waitcnt lgkmcnt(11)\n"
    "ds_read2st64_b32 v[218:219], v249 offset0:18 offset1:19\n"
    "v_mfma_f32_16x16x32_bf16 v[144:147], a[184:187], a[24:27], v[144:147]\n"
    "ds_read_b64_tr_b16 a[176:177], v246 offset:24576\n"
    "v_mfma_f32_16x16x32_bf16 v[144:147], a[188:191], a[28:31], v[144:147]\n"
    "ds_read_b64_tr_b16 a[174:175], v245 offset:59392\n"
    "v_mfma_f32_16x16x32_bf16 v[152:155], a[188:191], a[60:63], v[152:155]\n"
    "v_exp_f32_e32 v150, v150\n"
    "v_mfma_f32_16x16x32_bf16 v[152:155], a[184:187], a[56:59], v[152:155]\n"
    "v_exp_f32_e32 v151, v151\n"
    "v_mfma_f32_16x16x32_bf16 v[160:163], a[184:187], a[88:91], v[160:163]\n"
    "v_mfma_f32_16x16x32_bf16 v[160:163], a[188:191], a[92:95], v[160:163]\n"
    "ds_read_b64_tr_b16 a[178:179], v246 offset:26624\n"
    "v_mfma_f32_16x16x32_bf16 v[168:171], a[188:191], a[124:127], v[168:171]\n"
    "v_cvt_pk_bf16_f32 v148, v148, v149\n"
    "v_mfma_f32_16x16x32_bf16 v[168:171], a[184:187], a[120:123], v[168:171]\n"
    "v_exp_f32_e32 v156, v156\n"
    "v_mfma_f32_16x16x32_bf16 v[176:179], a[184:187], a[152:155], v[176:179]\n"
    "ds_read_b64_tr_b16 a[180:181], v246 offset:57344\n"
    "v_mfma_f32_16x16x32_bf16 v[176:179], a[188:191], a[156:159], v[176:179]\n"
    "s_waitcnt lgkmcnt(4)\n"
    "v_exp_f32_e32 v157, v157\n"
    "v_mfma_f32_16x16x32_bf16 a[236:239], a[160:163], v[180:183], a[236:239]\n"
    "v_mfma_f32_16x16x32_bf16 a[236:239], a[164:167], v[184:187], a[236:239]\n"
    "ds_read_b64_tr_b16 a[182:183], v246 offset:59392\n"
    "v_mfma_f32_16x16x32_bf16 a[240:243], a[164:167], v[192:195], a[240:243]\n"
    "ds_read_b64_tr_b16 a[184:185], v247 offset:24576\n"
    "v_mfma_f32_16x16x32_bf16 a[240:243], a[160:163], v[188:191], a[240:243]\n"
    "v_cvt_pk_bf16_f32 v149, v150, v151\n"
    "v_mfma_f32_16x16x32_bf16 a[244:247], a[160:163], v[196:199], a[244:247]\n"
    "ds_read_b64_tr_b16 a[186:187], v247 offset:26624\n"
    "v_mfma_f32_16x16x32_bf16 a[244:247], a[164:167], v[200:203], a[244:247]\n"
    "v_exp_f32_e32 v158, v158\n"
    "v_mfma_f32_16x16x32_bf16 a[248:251], a[164:167], v[208:211], a[248:251]\n"
    "v_exp_f32_e32 v159, v159\n"
    "v_mfma_f32_16x16x32_bf16 a[248:251], a[160:163], v[204:207], a[248:251]\n"
    "v_cvt_pk_bf16_f32 v156, v156, v157\n"
    "v_mfma_f32_16x16x32_bf16 a[252:255], a[160:163], v[212:215], a[252:255]\n"
    "ds_read_b64_tr_b16 a[188:189], v247 offset:57344\n"
    "v_mfma_f32_16x16x32_bf16 a[252:255], a[164:167], v[216:219], a[252:255]\n"
    "s_waitcnt lgkmcnt(6)\n"
    "v_exp_f32_e32 v164, v164\n"
    "v_mfma_f32_16x16x32_bf16 v[16:19], a[172:175], v[216:219], v[16:19]\n"
    "ds_read_b64_tr_b16 a[160:161], v244 offset:28672\n"
    "v_mfma_f32_16x16x32_bf16 v[16:19], a[168:171], v[212:215], v[16:19]\n"
    "ds_read_b64_tr_b16 a[190:191], v247 offset:59392\n"
    "v_mfma_f32_16x16x32_bf16 v[12:15], a[168:171], v[204:207], v[12:15]\n"
    "v_exp_f32_e32 v165, v165\n"
    "v_mfma_f32_16x16x32_bf16 v[12:15], a[172:175], v[208:211], v[12:15]\n"
    "v_cvt_pk_bf16_f32 v157, v158, v159\n"
    "v_mfma_f32_16x16x32_bf16 v[8:11], a[172:175], v[200:203], v[8:11]\n"
    "ds_read_b64_tr_b16 a[162:163], v244 offset:30720\n"
    "v_mfma_f32_16x16x32_bf16 v[8:11], a[168:171], v[196:199], v[8:11]\n"
    "v_exp_f32_e32 v166, v166\n"
    "v_mfma_f32_16x16x32_bf16 v[4:7], a[168:171], v[188:191], v[4:7]\n"
    "v_exp_f32_e32 v167, v167\n"
    "v_mfma_f32_16x16x32_bf16 v[4:7], a[172:175], v[192:195], v[4:7]\n"
    "v_cvt_pk_bf16_f32 v164, v164, v165\n"
    "v_mfma_f32_16x16x32_bf16 v[0:3], a[172:175], v[184:187], v[0:3]\n"
    "ds_read_b64_tr_b16 a[164:165], v244 offset:61440\n"
    "v_mfma_f32_16x16x32_bf16 v[0:3], a[168:171], v[180:183], v[0:3]\n"
    "s_waitcnt lgkmcnt(7)\n"
    "v_exp_f32_e32 v172, v172\n"
    "v_mfma_f32_16x16x32_bf16 v[20:23], a[176:179], v[180:183], v[20:23]\n"
    "v_mfma_f32_16x16x32_bf16 v[20:23], a[180:183], v[184:187], v[20:23]\n"
    "ds_read_b64_tr_b16 a[166:167], v244 offset:63488\n"
    "v_mfma_f32_16x16x32_bf16 v[24:27], a[180:183], v[192:195], v[24:27]\n"
    "ds_read_b64_tr_b16 a[168:169], v245 offset:28672\n"
    "v_mfma_f32_16x16x32_bf16 v[24:27], a[176:179], v[188:191], v[24:27]\n"
    "v_exp_f32_e32 v173, v173\n"
    "v_mfma_f32_16x16x32_bf16 v[28:31], a[176:179], v[196:199], v[28:31]\n"
    "ds_read_b64_tr_b16 a[170:171], v245 offset:30720\n"
    "v_mfma_f32_16x16x32_bf16 v[28:31], a[180:183], v[200:203], v[28:31]\n"
    "v_cvt_pk_bf16_f32 v165, v166, v167\n"
    "v_mfma_f32_16x16x32_bf16 v[32:35], a[180:183], v[208:211], v[32:35]\n"
    "v_exp_f32_e32 v174, v174\n"
    "v_mfma_f32_16x16x32_bf16 v[32:35], a[176:179], v[204:207], v[32:35]\n"
    "v_exp_f32_e32 v175, v175\n"
    "v_mfma_f32_16x16x32_bf16 v[36:39], a[176:179], v[212:215], v[36:39]\n"
    "ds_read_b64_tr_b16 a[172:173], v245 offset:61440\n"
    "v_mfma_f32_16x16x32_bf16 v[36:39], a[180:183], v[216:219], v[36:39]\n"
    "s_waitcnt lgkmcnt(6)\n"
    "v_cvt_pk_bf16_f32 v172, v172, v173\n"
    "v_mfma_f32_16x16x32_bf16 v[56:59], a[188:191], v[216:219], v[56:59]\n"
    "ds_read_b64_tr_b16 a[176:177], v246 offset:28672\n"
    "v_mfma_f32_16x16x32_bf16 v[56:59], a[184:187], v[212:215], v[56:59]\n"
    "ds_read_b64_tr_b16 a[174:175], v245 offset:63488\n"
    "v_mfma_f32_16x16x32_bf16 v[52:55], a[184:187], v[204:207], v[52:55]\n"
    "v_cvt_pk_bf16_f32 v173, v174, v175\n"
    "v_mfma_f32_16x16x32_bf16 v[52:55], a[188:191], v[208:211], v[52:55]\n"
    "v_exp_f32_e32 v144, v144\n"
    "v_mfma_f32_16x16x32_bf16 v[48:51], a[188:191], v[200:203], v[48:51]\n"
    "ds_read_b64_tr_b16 a[178:179], v246 offset:30720\n"
    "v_mfma_f32_16x16x32_bf16 v[48:51], a[184:187], v[196:199], v[48:51]\n"
    "v_mfma_f32_16x16x32_bf16 v[44:47], a[184:187], v[188:191], v[44:47]\n"
    "v_exp_f32_e32 v145, v145\n"
    "v_mfma_f32_16x16x32_bf16 v[44:47], a[188:191], v[192:195], v[44:47]\n"
    "v_exp_f32_e32 v146, v146\n"
    "v_mfma_f32_16x16x32_bf16 v[40:43], a[188:191], v[184:187], v[40:43]\n"
    "ds_read_b64_tr_b16 a[180:181], v246 offset:61440\n"
    "v_mfma_f32_16x16x32_bf16 v[40:43], a[184:187], v[180:183], v[40:43]\n"
    "s_waitcnt lgkmcnt(7)\n"
    "v_exp_f32_e32 v147, v147\n"
    "v_mfma_f32_16x16x32_bf16 v[60:63], a[160:163], v[180:183], v[60:63]\n"
    "ds_read_b64_tr_b16 a[184:185], v247 offset:28672\n"
    "v_mfma_f32_16x16x32_bf16 v[60:63], a[164:167], v[184:187], v[60:63]\n"
    "ds_read_b64_tr_b16 a[182:183], v246 offset:63488\n"
    "v_mfma_f32_16x16x32_bf16 v[64:67], a[164:167], v[192:195], v[64:67]\n"
    "ds_read_b64_tr_b16 a[186:187], v247 offset:30720\n"
    "v_mfma_f32_16x16x32_bf16 v[64:67], a[160:163], v[188:191], v[64:67]\n"
    "v_cvt_pk_bf16_f32 v142, v144, v145\n"
    "v_mfma_f32_16x16x32_bf16 v[68:71], a[160:163], v[196:199], v[68:71]\n"
    "v_exp_f32_e32 v152, v152\n"
    "v_mfma_f32_16x16x32_bf16 v[68:71], a[164:167], v[200:203], v[68:71]\n"
    "ds_read_b64_tr_b16 a[188:189], v247 offset:61440\n"
    "v_mfma_f32_16x16x32_bf16 v[72:75], a[164:167], v[208:211], v[72:75]\n"
    "ds_read_b64_tr_b16 a[190:191], v247 offset:63488\n"
    "v_mfma_f32_16x16x32_bf16 v[72:75], a[160:163], v[204:207], v[72:75]\n"
    "s_waitcnt vmcnt(8)\n"
    "v_mfma_f32_16x16x32_bf16 v[76:79], a[160:163], v[212:215], v[76:79]\n"
    "s_barrier\n"
    "v_mfma_f32_16x16x32_bf16 v[76:79], a[164:167], v[216:219], v[76:79]\n"
    "s_waitcnt lgkmcnt(7)\n"
    "s_mov_b32 m0, s42\n"
    "v_mfma_f32_16x16x32_bf16 v[96:99], a[172:175], v[216:219], v[96:99]\n"
    "v_exp_f32_e32 v153, v153\n"
    "v_mfma_f32_16x16x32_bf16 v[96:99], a[168:171], v[212:215], v[96:99]\n"
    "v_cvt_pk_bf16_f32 v143, v146, v147\n"
    "v_mfma_f32_16x16x32_bf16 v[92:95], a[168:171], v[204:207], v[92:95]\n"
    "v_exp_f32_e32 v154, v154\n"
    "v_mfma_f32_16x16x32_bf16 v[92:95], a[172:175], v[208:211], v[92:95]\n"
    "ds_read_b128 a[160:163], v240 offset:8192\n"
    "v_mfma_f32_16x16x32_bf16 v[88:91], a[172:175], v[200:203], v[88:91]\n"
    "ds_write_addtid_b32 v140 offset:0\n"
    "v_mfma_f32_16x16x32_bf16 v[88:91], a[168:171], v[196:199], v[88:91]\n"
    "v_exp_f32_e32 v155, v155\n"
    "v_mfma_f32_16x16x32_bf16 v[84:87], a[168:171], v[188:191], v[84:87]\n"
    "ds_write_addtid_b32 v141 offset:256\n"
    "v_mfma_f32_16x16x32_bf16 v[84:87], a[172:175], v[192:195], v[84:87]\n"
    "ds_write_addtid_b32 v148 offset:1024\n"
    "v_mfma_f32_16x16x32_bf16 v[80:83], a[172:175], v[184:187], v[80:83]\n"
    "v_cvt_pk_bf16_f32 v150, v152, v153\n"
    "v_mfma_f32_16x16x32_bf16 v[80:83], a[168:171], v[180:183], v[80:83]\n"
    "s_waitcnt lgkmcnt(7)\n"
    "v_exp_f32_e32 v160, v160\n"
    "v_mfma_f32_16x16x32_bf16 v[100:103], a[176:179], v[180:183], v[100:103]\n"
    "ds_read_b128 a[164:167], v241 offset:8192\n"
    "v_mfma_f32_16x16x32_bf16 v[100:103], a[180:183], v[184:187], v[100:103]\n"
    "ds_write_addtid_b32 v149 offset:1280\n"
    "v_mfma_f32_16x16x32_bf16 v[104:107], a[180:183], v[192:195], v[104:107]\n"
    "v_exp_f32_e32 v161, v161\n"
    "v_mfma_f32_16x16x32_bf16 v[104:107], a[176:179], v[188:191], v[104:107]\n"
    "ds_read_b128 a[168:171], v240 offset:12288\n"
    "v_mfma_f32_16x16x32_bf16 v[108:111], a[176:179], v[196:199], v[108:111]\n"
    "ds_write_addtid_b32 v156 offset:2048\n"
    "v_mfma_f32_16x16x32_bf16 v[108:111], a[180:183], v[200:203], v[108:111]\n"
    "v_cvt_pk_bf16_f32 v151, v154, v155\n"
    "v_mfma_f32_16x16x32_bf16 v[112:115], a[180:183], v[208:211], v[112:115]\n"
    "ds_write_addtid_b32 v157 offset:2304\n"
    "v_mfma_f32_16x16x32_bf16 v[112:115], a[176:179], v[204:207], v[112:115]\n"
    "v_exp_f32_e32 v162, v162\n"
    "v_mfma_f32_16x16x32_bf16 v[116:119], a[176:179], v[212:215], v[116:119]\n"
    "ds_write_addtid_b32 v164 offset:3072\n"
    "v_mfma_f32_16x16x32_bf16 v[116:119], a[180:183], v[216:219], v[116:119]\n"
    "s_waitcnt lgkmcnt(10)\n"
    "v_exp_f32_e32 v163, v163\n"
    "v_mfma_f32_16x16x32_bf16 v[136:139], a[188:191], v[216:219], v[136:139]\n"
    "ds_read_b128 a[172:175], v241 offset:12288\n"
    "v_mfma_f32_16x16x32_bf16 v[136:139], a[184:187], v[212:215], v[136:139]\n"
    "ds_write_addtid_b32 v165 offset:3328\n"
    "v_cvt_pk_bf16_f32 v158, v160, v161\n"
    "v_mfma_f32_16x16x32_bf16 v[132:135], a[184:187], v[204:207], v[132:135]\n"
    "ds_read_b128 a[176:179], v242 offset:8192\n"
    "v_mfma_f32_16x16x32_bf16 v[132:135], a[188:191], v[208:211], v[132:135]\n"
    "ds_write_addtid_b32 v172 offset:4096\n"
    "v_mfma_f32_16x16x32_bf16 v[128:131], a[188:191], v[200:203], v[128:131]\n"
    "v_exp_f32_e32 v168, v168\n"
    "v_mfma_f32_16x16x32_bf16 v[128:131], a[184:187], v[196:199], v[128:131]\n"
    "ds_write_addtid_b32 v173 offset:4352\n"
    "v_mfma_f32_16x16x32_bf16 v[124:127], a[184:187], v[188:191], v[124:127]\n"
    "v_exp_f32_e32 v169, v169\n"
    "v_mfma_f32_16x16x32_bf16 v[124:127], a[188:191], v[192:195], v[124:127]\n"
    "ds_read_b128 a[180:183], v243 offset:8192\n"
    "v_mfma_f32_16x16x32_bf16 v[120:123], a[188:191], v[184:187], v[120:123]\n"
    "ds_write_addtid_b32 v142 offset:512\n"
    "v_mfma_f32_16x16x32_bf16 v[120:123], a[184:187], v[180:183], v[120:123]\n"
    "s_waitcnt lgkmcnt(12)\n"
    "v_cvt_pk_bf16_f32 v159, v162, v163\n"
    ".Lat_in:\n"
    "v_mfma_f32_16x16x32_bf16 v[212:215], a[160:163], a[128:131], v[236:239]\n"
    ".long 0xbedf00ff\n"
    ".Lat_w4:\n"
    "s_setpc_b64 s[2:3]\n"
    "v_mfma_f32_16x16x32_bf16 v[212:215], a[164:167], a[132:135], v[212:215]\n"
    "v_exp_f32_e32 v170, v170\n"
    "v_mfma_f32_16x16x32_bf16 v[204:207], a[164:167], a[100:103], v[232:235]\n"
    "ds_write_addtid_b32 v143 offset:768\n"
    "v_mfma_f32_16x16x32_bf16 v[204:207], a[160:163], a[96:99], v[204:207]\n"
    "ds_read_b128 a[184:187], v242 offset:12288\n"
    "v_mfma_f32_16x16x32_bf16 v[196:199], a[160:163], a[64:67], v[228:231]\n"
    "v_exp_f32_e32 v171, v171\n"
    "v_mfma_f32_16x16x32_bf16 v[196:199], a[164:167], a[68:71], v[196:199]\n"
    "ds_write_addtid_b32 v150 offset:1536\n"
    "v_mfma_f32_16x16x32_bf16 v[188:191], a[164:167], a[36:39], v[224:227]\n"
    "v_cvt_pk_bf16_f32 v166, v168, v169\n"
    "v_mfma_f32_16x16x32_bf16 v[188:191], a[160:163], a[32:35], v[188:191]\n"
    "v_mfma_f32_16x16x32_bf16 v[180:183], a[160:163], a[0:3], v[220:223]\n"
    "ds_write_addtid_b32 v151 offset:1792\n"
    "v_mfma_f32_16x16x32_bf16 v[180:183], a[164:167], a[4:7], v[180:183]\n"
    "s_waitcnt lgkmcnt(10)\n"
    "v_exp_f32_e32 v176, v176\n"
    "v_mfma_f32_16x16x32_bf16 v[180:183], a[168:171], a[8:11], v[180:183]\n"
    "ds_read_b128 a[188:191], v243 offset:12288\n"
    "v_mfma_f32_16x16x32_bf16 v[180:183], a[172:175], a[12:15], v[180:183]\n"
    "v_exp_f32_e32 v177, v177\n"
    "v_mfma_f32_16x16x32_bf16 v[188:191], a[172:175], a[44:47], v[188:191]\n"
    "ds_write_addtid_b32 v158 offset:2560\n"
    "v_mfma_f32_16x16x32_bf16 v[188:191], a[168:171], a[40:43], v[188:191]\n"
    "ds_read_b128 a[160:163], v240 offset:10240\n"
    "v_mfma_f32_16x16x32_bf16 v[196:199], a[168:171], a[72:75], v[196:199]\n"
    "v_cvt_pk_bf16_f32 v167, v170, v171\n"
    "v_mfma_f32_16x16x32_bf16 v[196:199], a[172:175], a[76:79], v[196:199]\n"
    "ds_write_addtid_b32 v159 offset:2816\n"
    "v_mfma_f32_16x16x32_bf16 v[204:207], a[172:175], a[108:111], v[204:207]\n"
    "v_exp_f32_e32 v178, v178\n"
    "v_mfma_f32_16x16x32_bf16 v[204:207], a[168:171], a[104:107], v[204:207]\n"
    "ds_write_addtid_b32 v166 offset:3584\n"
    "v_mfma_f32_16x16x32_bf16 v[212:215], a[168:171], a[136:139], v[212:215]\n"
    "v_mfma_f32_16x16x32_bf16 v[212:215], a[172:175], a[140:143], v[212:215]\n"
    "s_waitcnt lgkmcnt(10)\n"
    "v_exp_f32_e32 v179, v179\n"
    "v_mfma_f32_16x16x32_bf16 v[212:215], a[176:179], a[144:147], v[212:215]\n"
    "ds_read_b128 a[164:167], v241 offset:10240\n"
    "v_mfma_f32_16x16x32_bf16 v[212:215], a[180:183], a[148:151], v[212:215]\n"
    "v_cvt_pk_bf16_f32 v174, v176, v177\n"
    "v_mfma_f32_16x16x32_bf16 v[204:207], a[180:183], a[116:119], v[204:207]\n"
    "ds_write_addtid_b32 v167 offset:3840\n"
    "v_mfma_f32_16x16x32_bf16 v[204:207], a[176:179], a[112:115], v[204:207]\n"
    "ds_read_b128 a[168:171], v240 offset:14336\n"
    "v_mfma_f32_16x16x32_bf16 v[196:199], a[176:179], a[80:83], v[196:199]\n"
    "v_cvt_pk_bf16_f32 v175, v178, v179\n"
    "v_mfma_f32_16x16x32_bf16 v[196:199], a[180:183], a[84:87], v[196:199]\n"
    "ds_write_addtid_b32 v174 offset:4608\n"
    "v_mfma_f32_16x16x32_bf16 v[188:191], a[180:183], a[52:55], v[188:191]\n"
    "ds_write_addtid_b32 v175 offset:4864\n"
    "v_mfma_f32_16x16x32_bf16 v[188:191], a[176:179], a[48:51], v[188:191]\n"
    "v_mfma_f32_16x16x32_bf16 v[180:183], a[176:179], a[16:19], v[180:183]\n"
    "ds_read_b128 a[172:175], v241 offset:14336\n"
    "v_mfma_f32_16x16x32_bf16 v[180:183], a[180:183], a[20:23], v[180:183]\n"
    "s_waitcnt lgkmcnt(10)\n"
    "s_waitcnt lgkmcnt(1)\n"
    "v_mfma_f32_16x16x32_bf16 v[180:183], a[184:187], a[24:27], v[180:183]\n"
    "s_barrier\n"
    "v_mfma_f32_16x16x32_bf16 v[180:183], a[188:191], a[28:31], v[180:183]\n"
    "s_add_u32 m0, s43, 24576\n"
    "v_mfma_f32_16x16x32_bf16 v[188:191], a[188:191], a[60:63], v[188:191]\n"
    "ds_read_b128 a[176:179], v242 offset:10240\n"
    "v_mfma_f32_16x16x32_bf16 v[188:191], a[184:187], a[56:59], v[188:191]\n"
    "buffer_load_dwordx4 v248, s[8:11], s40 offen offset:0 sc1 lds\n"
    "v_mfma_f32_16x16x32_bf16 v[196:199], a[184:187], a[88:91], v[196:199]\n"
    "ds_read2st64_b32 v[144:145], v249 offset0:0 offset1:1\n"
    "v_mfma_f32_16x16x32_bf16 v[196:199], a[188:191], a[92:95], v[196:199]\n"
    "v_mfma_f32_16x16x32_bf16 v[204:207], a[188:191], a[124:127], v[204:207]\n"
    "ds_read2st64_b32 v[146:147], v249 offset0:2 offset1:3\n"
    "v_mfma_f32_16x16x32_bf16 v[204:207], a[184:187], a[120:123], v[204:207]\n"
    "buffer_load_dwordx4 v248, s[8:11], s40 offen offset:1024 sc1 lds\n"
    "v_mfma_f32_16x16x32_bf16 v[212:215], a[184:187], a[152:155], v[212:215]\n"
    "v_mfma_f32_16x16x32_bf16 v[212:215], a[188:191], a[156:159], v[212:215]\n"
    "v_mfma_f32_16x16x32_bf16 v[216:219], a[160:163], a[128:131], v[236:239]\n"
    "ds_read_b128 a[180:183], v243 offset:10240\n"
    "v_mfma_f32_16x16x32_bf16 v[216:219], a[164:167], a[132:135], v[216:219]\n"
    "buffer_load_dwordx4 v248, s[8:11], s40 offen offset:2048 sc1 lds\n"
    "v_mfma_f32_16x16x32_bf16 v[208:211], a[164:167], a[100:103], v[232:235]\n"
    "ds_read2st64_b32 v[152:153], v249 offset0:4 offset1:5\n"
    "v_mfma_f32_16x16x32_bf16 v[208:211], a[160:163], a[96:99], v[208:211]\n"
    "ds_read_b128 a[184:187], v242 offset:14336\n"
    "v_mfma_f32_16x16x32_bf16 v[200:203], a[160:163], a[64:67], v[228:231]\n"
    "ds_read2st64_b32 v[154:155], v249 offset0:6 offset1:7\n"
    "v_mfma_f32_16x16x32_bf16 v[200:203], a[164:167], a[68:71], v[200:203]\n"
    "buffer_load_dwordx4 v248, s[8:11], s40 offen offset:3072 sc1 lds\n"
    "v_mfma_f32_16x16x32_bf16 v[192:195], a[164:167], a[36:39], v[224:227]\n"
    "s_add_u32 m0, s43, 28672\n"
    "v_exp_f32_e32 v180, v180\n"
    "v_mfma_f32_16x16x32_bf16 v[192:195], a[160:163], a[32:35], v[192:195]\n"
    "s_add_u32 s40, s40, 0x8000\n"
    "v_exp_f32_e32 v181, v181\n"
    "v_mfma_f32_16x16x32_bf16 v[184:187], a[160:163], a[0:3], v[220:223]\n"
    "ds_read2st64_b32 v[160:161], v249 offset0:8 offset1:9\n"
    "v_mfma_f32_16x16x32_bf16 v[184:187], a[164:167], a[4:7], v[184:187]\n"
    "s_waitcnt lgkmcnt(8)\n"
    "buffer_load_dwordx4 v248, s[8:11], s41 offen offset:0 sc1 lds\n"
    "v_mfma_f32_16x16x32_bf16 v[184:187], a[168:171], a[8:11], v[184:187]\n"
    "ds_read_b128 a[188:191], v243 offset:14336\n"
    "v_mfma_f32_16x16x32_bf16 v[184:187], a[172:175], a[12:15], v[184:187]\n"
    "ds_read_b64_tr_b16 a[160:161], v244 offset:0\n"
    "v_mfma_f32_16x16x32_bf16 v[192:195], a[172:175], a[44:47], v[192:195]\n"
    "ds_read2st64_b32 v[162:163], v249 offset0:10 offset1:11\n"
    "v_mfma_f32_16x16x32_bf16 v[192:195], a[168:171], a[40:43], v[192:195]\n"
    "buffer_load_dwordx4 v248, s[8:11], s41 offen offset:1024 sc1 lds\n"
    "v_mfma_f32_16x16x32_bf16 v[200:203], a[168:171], a[72:75], v[200:203]\n"
    "ds_read_b64_tr_b16 a[162:163], v244 offset:2048\n"
    "v_mfma_f32_16x16x32_bf16 v[200:203], a[172:175], a[76:79], v[200:203]\n"
    "v_exp_f32_e32 v182, v182\n"
    "v_mfma_f32_16x16x32_bf16 v[208:211], a[172:175], a[108:111], v[208:211]\n"
    "ds_read2st64_b32 v[168:169], v249 offset0:12 offset1:13\n"
    "v_mfma_f32_16x16x32_bf16 v[208:211], a[168:171], a[104:107], v[208:211]\n"
    "buffer_load_dwordx4 v248, s[8:11], s41 offen offset:2048 sc1 lds\n"
    "v_mfma_f32_16x16x32_bf16 v[216:219], a[168:171], a[136:139], v[216:219]\n"
    "ds_read_b64_tr_b16 a[164:165], v244 offset:32768\n"
    "v_mfma_f32_16x16x32_bf16 v[216:219], a[172:175], a[140:143], v[216:219]\n"
    "s_waitcnt lgkmcnt(10)\n"
    "ds_read2st64_b32 v[170:171], v249 offset0:14 offset1:15\n"
    "v_mfma_f32_16x16x32_bf16 v[216:219], a[176:179], a[144:147], v[216:219]\n"
    "ds_read_b64_tr_b16 a[168:169], v245 offset:0\n"
    "v_mfma_f32_16x16x32_bf16 v[216:219], a[180:183], a[148:151], v[216:219]\n"
    "buffer_load_dwordx4 v248, s[8:11], s41 offen offset:3072 sc1 lds\n"
    "v_mfma_f32_16x16x32_bf16 v[208:211], a[180:183], a[116:119], v[208:211]\n"
    "ds_read_b64_tr_b16 a[166:167], v244 offset:34816\n"
    "v_mfma_f32_16x16x32_bf16 v[208:211], a[176:179], a[112:115], v[208:211]\n"
    "s_add_u32 s41, s41, 0x8000\n"
    "v_exp_f32_e32 v183, v183\n"
    "v_mfma_f32_16x16x32_bf16 v[200:203], a[176:179], a[80:83], v[200:203]\n"
    "ds_read_b64_tr_b16 a[170:171], v245 offset:2048\n"
    "v_cvt_pk_bf16_f32 v180, v180, v181\n"
    "v_mfma_f32_16x16x32_bf16 v[200:203], a[180:183], a[84:87], v[200:203]\n"
    "ds_read2st64_b32 v[176:177], v249 offset0:16 offset1:17\n"
    "v_mfma_f32_16x16x32_bf16 v[192:195], a[180:183], a[52:55], v[192:195]\n"
    "v_exp_f32_e32 v188, v188\n"
    "v_mfma_f32_16x16x32_bf16 v[192:195], a[176:179], a[48:51], v[192:195]\n"
    "v_exp_f32_e32 v189, v189\n"
    "v_mfma_f32_16x16x32_bf16 v[184:187], a[176:179], a[16:19], v[184:187]\n"
    "ds_read_b64_tr_b16 a[172:173], v245 offset:32768\n"
    "v_cvt_pk_bf16_f32 v181, v182, v183\n"
    "v_mfma_f32_16x16x32_bf16 v[184:187], a[180:183], a[20:23], v[184:187]\n"
    "s_waitcnt lgkmcnt(11)\n"
    "ds_read2st64_b32 v[178:179], v249 offset0:18 offset1:19\n"
    "v_mfma_f32_16x16x32_bf16 v[184:187], a[184:187], a[24:27], v[184:187]\n"
    "ds_read_b64_tr_b16 a[176:177], v246 offset:0\n"
    "v_mfma_f32_16x16x32_bf16 v[184:187], a[188:191], a[28:31], v[184:187]\n"
    "ds_read_b64_tr_b16 a[174:175], v245 offset:34816\n"
    "v_mfma_f32_16x16x32_bf16 v[192:195], a[188:191], a[60:63], v[192:195]\n"
    "v_exp_f32_e32 v190, v190\n"
    "v_mfma_f32_16x16x32_bf16 v[192:195], a[184:187], a[56:59], v[192:195]\n"
    "v_exp_f32_e32 v191, v191\n"
    "v_mfma_f32_16x16x32_bf16 v[200:203], a[184:187], a[88:91], v[200:203]\n"
    "v_mfma_f32_16x16x32_bf16 v[200:203], a[188:191], a[92:95], v[200:203]\n"
    "ds_read_b64_tr_b16 a[178:179], v246 offset:2048\n"
    "v_mfma_f32_16x16x32_bf16 v[208:211], a[188:191], a[124:127], v[208:211]\n"
    "v_cvt_pk_bf16_f32 v188, v188, v189\n"
    "v_mfma_f32_16x16x32_bf16 v[208:211], a[184:187], a[120:123], v[208:211]\n"
    "v_exp_f32_e32 v196, v196\n"
    "v_mfma_f32_16x16x32_bf16 v[216:219], a[184:187], a[152:155], v[216:219]\n"
    "ds_read_b64_tr_b16 a[180:181], v246 offset:32768\n"
    "v_mfma_f32_16x16x32_bf16 v[216:219], a[188:191], a[156:159], v[216:219]\n"
    "s_waitcnt lgkmcnt(4)\n"
    "v_exp_f32_e32 v197, v197\n"
    "v_mfma_f32_16x16x32_bf16 a[236:239], a[160:163], v[140:143], a[236:239]\n"
    "v_mfma_f32_16x16x32_bf16 a[236:239], a[164:167], v[144:147], a[236:239]\n"
    "ds_read_b64_tr_b16 a[182:183], v246 offset:34816\n"
    "v_mfma_f32_16x16x32_bf16 a[240:243], a[164:167], v[152:155], a[240:243]\n"
    "ds_read_b64_tr_b16 a[184:185], v247 offset:0\n"
    "v_mfma_f32_16x16x32_bf16 a[240:243], a[160:163], v[148:151], a[240:243]\n"
    "v_cvt_pk_bf16_f32 v189, v190, v191\n"
    "v_mfma_f32_16x16x32_bf16 a[244:247], a[160:163], v[156:159], a[244:247]\n"
    "ds_read_b64_tr_b16 a[186:187], v247 offset:2048\n"
    "v_mfma_f32_16x16x32_bf16 a[244:247], a[164:167], v[160:163], a[244:247]\n"
    "v_exp_f32_e32 v198, v198\n"
    "v_mfma_f32_16x16x32_bf16 a[248:251], a[164:167], v[168:171], a[248:251]\n"
    "v_exp_f32_e32 v199, v199\n"
    "v_mfma_f32_16x16x32_bf16 a[248:251], a[160:163], v[164:167], a[248:251]\n"
    "v_cvt_pk_bf16_f32 v196, v196, v197\n"
    "v_mfma_f32_16x16x32_bf16 a[252:255], a[160:163], v[172:175], a[252:255]\n"
    "ds_read_b64_tr_b16 a[188:189], v247 offset:32768\n"
    "v_mfma_f32_16x16x32_bf16 a[252:255], a[164:167], v[176:179], a[252:255]\n"
    "s_waitcnt lgkmcnt(6)\n"
    "v_exp_f32_e32 v204, v204\n"
    "v_mfma_f32_16x16x32_bf16 v[16:19], a[172:175], v[176:179], v[16:19]\n"
    "ds_read_b64_tr_b16 a[160:161], v244 offset:4096\n"
    "v_mfma_f32_16x16x32_bf16 v[16:19], a[168:171], v[172:175], v[16:19]\n"
    "ds_read_b64_tr_b16 a[190:191], v247 offset:34816\n"
    "v_mfma_f32_16x16x32_bf16 v[12:15], a[168:171], v[164:167], v[12:15]\n"
    "v_exp_f32_e32 v205, v205\n"
    "v_mfma_f32_16x16x32_bf16 v[12:15], a[172:175], v[168:171], v[12:15]\n"
    "v_cvt_pk_bf16_f32 v197, v198, v199\n"
    "v_mfma_f32_16x16x32_bf16 v[8:11], a[172:175], v[160:163], v[8:11]\n"
    "ds_read_b64_tr_b16 a[162:163], v244 offset:6144\n"
    "v_mfma_f32_16x16x32_bf16 v[8:11], a[168:171], v[156:159], v[8:11]\n"
    "v_exp_f32_e32 v206, v206\n"
    "v_mfma_f32_16x16x32_bf16 v[4:7], a[168:171], v[148:151], v[4:7]\n"
    "v_exp_f32_e32 v207, v207\n"
    "v_mfma_f32_16x16x32_bf16 v[4:7], a[172:175], v[152:155], v[4:7]\n"
    "v_cvt_pk_bf16_f32 v204, v204, v205\n"
    "v_mfma_f32_16x16x32_bf16 v[0:3], a[172:175], v[144:147], v[0:3]\n"
    "ds_read_b64_tr_b16 a[164:165], v244 offset:36864\n"
    "v_mfma_f32_16x16x32_bf16 v[0:3], a[168:171], v[140:143], v[0:3]\n"
    "s_waitcnt lgkmcnt(7)\n"
    "v_exp_f32_e32 v212, v212\n"
    "v_mfma_f32_16x16x32_bf16 v[20:23], a[176:179], v[140:143], v[20:23]\n"
    "buffer_load_dword v253, off, s[8:11], s82 sc1\n"
    "v_mfma_f32_16x16x32_bf16 v[20:23], a[180:183], v[144:147], v[20:23]\n"
    "ds_read_b64_tr_b16 a[166:167], v244 offset:38912\n"
    "v_mfma_f32_16x16x32_bf16 v[24:27], a[180:183], v[152:155], v[24:27]\n"
    "ds_read_b64_tr_b16 a[168:169], v245 offset:4096\n"
    "v_mfma_f32_16x16x32_bf16 v[24:27], a[176:179], v[148:151], v[24:27]\n"
    "v_exp_f32_e32 v213, v213\n"
    "v_mfma_f32_16x16x32_bf16 v[28:31], a[176:179], v[156:159], v[28:31]\n"
    "ds_read_b64_tr_b16 a[170:171], v245 offset:6144\n"
    "v_mfma_f32_16x16x32_bf16 v[28:31], a[180:183], v[160:163], v[28:31]\n"
    "v_cvt_pk_bf16_f32 v205, v206, v207\n"
    "v_mfma_f32_16x16x32_bf16 v[32:35], a[180:183], v[168:171], v[32:35]\n"
    "v_exp_f32_e32 v214, v214\n"
    "v_mfma_f32_16x16x32_bf16 v[32:35], a[176:179], v[164:167], v[32:35]\n"
    "v_exp_f32_e32 v215, v215\n"
    "v_mfma_f32_16x16x32_bf16 v[36:39], a[176:179], v[172:175], v[36:39]\n"
    "ds_read_b64_tr_b16 a[172:173], v245 offset:36864\n"
    "v_mfma_f32_16x16x32_bf16 v[36:39], a[180:183], v[176:179], v[36:39]\n"
    "s_waitcnt lgkmcnt(6)\n"
    "v_cvt_pk_bf16_f32 v212, v212, v213\n"
    "v_mfma_f32_16x16x32_bf16 v[56:59], a[188:191], v[176:179], v[56:59]\n"
    "ds_read_b64_tr_b16 a[176:177], v246 offset:4096\n"
    "v_mfma_f32_16x16x32_bf16 v[56:59], a[184:187], v[172:175], v[56:59]\n"
    "ds_read_b64_tr_b16 a[174:175], v245 offset:38912\n"
    "v_mfma_f32_16x16x32_bf16 v[52:55], a[184:187], v[164:167], v[52:55]\n"
    "v_cvt_pk_bf16_f32 v213, v214, v215\n"
    "v_mfma_f32_16x16x32_bf16 v[52:55], a[188:191], v[168:171], v[52:55]\n"
    "v_exp_f32_e32 v184, v184\n"
    "v_mfma_f32_16x16x32_bf16 v[48:51], a[188:191], v[160:163], v[48:51]\n"
    "ds_read_b64_tr_b16 a[178:179], v246 offset:6144\n"
    "v_mfma_f32_16x16x32_bf16 v[48:51], a[184:187], v[156:159], v[48:51]\n"
    "v_mfma_f32_16x16x32_bf16 v[44:47], a[184:187], v[148:151], v[44:47]\n"
    "v_exp_f32_e32 v185, v185\n"
    "v_mfma_f32_16x16x32_bf16 v[44:47], a[188:191], v[152:155], v[44:47]\n"
    "v_exp_f32_e32 v186, v186\n"
    "v_mfma_f32_16x16x32_bf16 v[40:43], a[188:191], v[144:147], v[40:43]\n"
    "ds_read_b64_tr_b16 a[180:181], v246 offset:36864\n"
    "v_mfma_f32_16x16x32_bf16 v[40:43], a[184:187], v[140:143], v[40:43]\n"
    "s_waitcnt lgkmcnt(7)\n"
    "v_exp_f32_e32 v187, v187\n"
    "v_mfma_f32_16x16x32_bf16 v[60:63], a[160:163], v[140:143], v[60:63]\n"
    "ds_read_b64_tr_b16 a[184:185], v247 offset:4096\n"
    "v_mfma_f32_16x16x32_bf16 v[60:63], a[164:167], v[144:147], v[60:63]\n"
    "ds_read_b64_tr_b16 a[182:183], v246 offset:38912\n"
    "v_mfma_f32_16x16x32_bf16 v[64:67], a[164:167], v[152:155], v[64:67]\n"
    "ds_read_b64_tr_b16 a[186:187], v247 offset:6144\n"
    "v_mfma_f32_16x16x32_bf16 v[64:67], a[160:163], v[148:151], v[64:67]\n"
    "v_cvt_pk_bf16_f32 v182, v184, v185\n"
    "v_mfma_f32_16x16x32_bf16 v[68:71], a[160:163], v[156:159], v[68:71]\n"
    "v_exp_f32_e32 v192, v192\n"
    "v_mfma_f32_16x16x32_bf16 v[68:71], a[164:167], v[160:163], v[68:71]\n"
    "ds_read_b64_tr_b16 a[188:189], v247 offset:36864\n"
    "v_mfma_f32_16x16x32_bf16 v[72:75], a[164:167], v[168:171], v[72:75]\n"
    "ds_read_b64_tr_b16 a[190:191], v247 offset:38912\n"
    "v_mfma_f32_16x16x32_bf16 v[72:75], a[160:163], v[164:167], v[72:75]\n"
    "s_waitcnt vmcnt(9)\n"
    "v_mfma_f32_16x16x32_bf16 v[76:79], a[160:163], v[172:175], v[76:79]\n"
    "s_barrier\n"
    "v_mfma_f32_16x16x32_bf16 v[76:79], a[164:167], v[176:179], v[76:79]\n"
    "s_waitcnt lgkmcnt(7)\n"
    "s_mov_b32 m0, s42\n"
    "v_mfma_f32_16x16x32_bf16 v[96:99], a[172:175], v[176:179], v[96:99]\n"
    "v_exp_f32_e32 v193, v193\n"
    "v_mfma_f32_16x16x32_bf16 v[96:99], a[168:171], v[172:175], v[96:99]\n"
    "v_cvt_pk_bf16_f32 v183, v186, v187\n"
    "v_mfma_f32_16x16x32_bf16 v[92:95], a[168:171], v[164:167], v[92:95]\n"
    "v_exp_f32_e32 v194, v194\n"
    "v_mfma_f32_16x16x32_bf16 v[92:95], a[172:175], v[168:171], v[92:95]\n"
    "ds_read_b128 a[160:163], v240 offset:16384\n"
    "v_mfma_f32_16x16x32_bf16 v[88:91], a[172:175], v[160:163], v[88:91]\n"
    "ds_write_addtid_b32 v180 offset:0\n"
    "v_mfma_f32_16x16x32_bf16 v[88:91], a[168:171], v[156:159], v[88:91]\n"
    "v_exp_f32_e32 v195, v195\n"
    "v_mfma_f32_16x16x32_bf16 v[84:87], a[168:171], v[148:151], v[84:87]\n"
    "ds_write_addtid_b32 v181 offset:256\n"
    "v_mfma_f32_16x16x32_bf16 v[84:87], a[172:175], v[152:155], v[84:87]\n"
    "ds_write_addtid_b32 v188 offset:1024\n"
    "v_mfma_f32_16x16x32_bf16 v[80:83], a[172:175], v[144:147], v[80:83]\n"
    "v_cvt_pk_bf16_f32 v190, v192, v193\n"
    "v_mfma_f32_16x16x32_bf16 v[80:83], a[168:171], v[140:143], v[80:83]\n"
    "s_waitcnt lgkmcnt(7)\n"
    "v_exp_f32_e32 v200, v200\n"
    "v_mfma_f32_16x16x32_bf16 v[100:103], a[176:179], v[140:143], v[100:103]\n"
    "ds_read_b128 a[164:167], v241 offset:16384\n"
    "v_mfma_f32_16x16x32_bf16 v[100:103], a[180:183], v[144:147], v[100:103]\n"
    "ds_write_addtid_b32 v189 offset:1280\n"
    "v_mfma_f32_16x16x32_bf16 v[104:107], a[180:183], v[152:155], v[104:107]\n"
    "v_exp_f32_e32 v201, v201\n"
    "v_mfma_f32_16x16x32_bf16 v[104:107], a[176:179], v[148:151], v[104:107]\n"
    "ds_read_b128 a[168:171], v240 offset:20480\n"
    "v_mfma_f32_16x16x32_bf16 v[108:111], a[176:179], v[156:159], v[108:111]\n"
    "ds_write_addtid_b32 v196 offset:2048\n"
    "v_mfma_f32_16x16x32_bf16 v[108:111], a[180:183], v[160:163], v[108:111]\n"
    "v_cvt_pk_bf16_f32 v191, v194, v195\n"
    "v_mfma_f32_16x16x32_bf16 v[112:115], a[180:183], v[168:171], v[112:115]\n"
    "ds_write_addtid_b32 v197 offset:2304\n"
    "v_mfma_f32_16x16x32_bf16 v[112:115], a[176:179], v[164:167], v[112:115]\n"
    "v_exp_f32_e32 v202, v202\n"
    "v_mfma_f32_16x16x32_bf16 v[116:119], a[176:179], v[172:175], v[116:119]\n"
    "ds_write_addtid_b32 v204 offset:3072\n"
    "v_mfma_f32_16x16x32_bf16 v[116:119], a[180:183], v[176:179], v[116:119]\n"
    "s_waitcnt lgkmcnt(10)\n"
    "v_exp_f32_e32 v203, v203\n"
    "v_mfma_f32_16x16x32_bf16 v[136:139], a[188:191], v[176:179], v[136:139]\n"
    "ds_read_b128 a[172:175], v241 offset:20480\n"
    "v_mfma_f32_16x16x32_bf16 v[136:139], a[184:187], v[172:175], v[136:139]\n"
    "ds_write_addtid_b32 v205 offset:3328\n"
    "v_cvt_pk_bf16_f32 v198, v200, v201\n"
    "v_mfma_f32_16x16x32_bf16 v[132:135], a[184:187], v[164:167], v[132:135]\n"
    "ds_read_b128 a[176:179], v242 offset:16384\n"
    "v_mfma_f32_16x16x32_bf16 v[132:135], a[188:191], v[168:171], v[132:135]\n"
    "ds_write_addtid_b32 v212 offset:4096\n"
    "v_mfma_f32_16x16x32_bf16 v[128:131], a[188:191], v[160:163], v[128:131]\n"
    "v_exp_f32_e32 v208, v208\n"
    "v_mfma_f32_16x16x32_bf16 v[128:131], a[184:187], v[156:159], v[128:131]\n"
    "ds_write_addtid_b32 v213 offset:4352\n"
    "v_mfma_f32_16x16x32_bf16 v[124:127], a[184:187], v[148:151], v[124:127]\n"
    "v_exp_f32_e32 v209, v209\n"
    "v_mfma_f32_16x16x32_bf16 v[124:127], a[188:191], v[152:155], v[124:127]\n"
    "ds_read_b128 a[180:183], v243 offset:16384\n"
    "v_mfma_f32_16x16x32_bf16 v[120:123], a[188:191], v[144:147], v[120:123]\n"
    "ds_write_addtid_b32 v182 offset:512\n"
    "v_mfma_f32_16x16x32_bf16 v[120:123], a[184:187], v[140:143], v[120:123]\n"
    "s_waitcnt lgkmcnt(12)\n"
    "v_cvt_pk_bf16_f32 v199, v202, v203\n"
    "v_mfma_f32_16x16x32_bf16 v[172:175], a[160:163], a[128:131], v[236:239]\n"
    "v_mfma_f32_16x16x32_bf16 v[172:175], a[164:167], a[132:135], v[172:175]\n"
    "v_exp_f32_e32 v210, v210\n"
    "v_mfma_f32_16x16x32_bf16 v[164:167], a[164:167], a[100:103], v[232:235]\n"
    "ds_write_addtid_b32 v183 offset:768\n"
    "v_mfma_f32_16x16x32_bf16 v[164:167], a[160:163], a[96:99], v[164:167]\n"
    "ds_read_b128 a[184:187], v242 offset:20480\n"
    "v_mfma_f32_16x16x32_bf16 v[156:159], a[160:163], a[64:67], v[228:231]\n"
    "v_exp_f32_e32 v211, v211\n"
    "v_mfma_f32_16x16x32_bf16 v[156:159], a[164:167], a[68:71], v[156:159]\n"
    "ds_write_addtid_b32 v190 offset:1536\n"
    "v_mfma_f32_16x16x32_bf16 v[148:151], a[164:167], a[36:39], v[224:227]\n"
    "v_cvt_pk_bf16_f32 v206, v208, v209\n"
    "v_mfma_f32_16x16x32_bf16 v[148:151], a[160:163], a[32:35], v[148:151]\n"
    "v_mfma_f32_16x16x32_bf16 v[140:143], a[160:163], a[0:3], v[220:223]\n"
    "ds_write_addtid_b32 v191 offset:1792\n"
    "v_mfma_f32_16x16x32_bf16 v[140:143], a[164:167], a[4:7], v[140:143]\n"
    "s_waitcnt lgkmcnt(10)\n"
    "v_exp_f32_e32 v216, v216\n"
    "v_mfma_f32_16x16x32_bf16 v[140:143], a[168:171], a[8:11], v[140:143]\n"
    "ds_read_b128 a[188:191], v243 offset:20480\n"
    "v_mfma_f32_16x16x32_bf16 v[140:143], a[172:175], a[12:15], v[140:143]\n"
    "v_exp_f32_e32 v217, v217\n"
    "v_mfma_f32_16x16x32_bf16 v[148:151], a[172:175], a[44:47], v[148:151]\n"
    "ds_write_addtid_b32 v198 offset:2560\n"
    "v_mfma_f32_16x16x32_bf16 v[148:151], a[168:171], a[40:43], v[148:151]\n"
    "ds_read_b128 a[160:163], v240 offset:18432\n"
    "v_mfma_f32_16x16x32_bf16 v[156:159], a[168:171], a[72:75], v[156:159]\n"
    "v_cvt_pk_bf16_f32 v207, v210, v211\n"
    "v_mfma_f32_16x16x32_bf16 v[156:159], a[172:175], a[76:79], v[156:159]\n"
    "ds_write_addtid_b32 v199 offset:2816\n"
    "v_mfma_f32_16x16x32_bf16 v[164:167], a[172:175], a[108:111], v[164:167]\n"
    "v_exp_f32_e32 v218, v218\n"
    "v_mfma_f32_16x16x32_bf16 v[164:167], a[168:171], a[104:107], v[164:167]\n"
    "ds_write_addtid_b32 v206 offset:3584\n"
    "v_mfma_f32_16x16x32_bf16 v[172:175], a[168:171], a[136:139], v[172:175]\n"
    "v_mfma_f32_16x16x32_bf16 v[172:175], a[172:175], a[140:143], v[172:175]\n"
    "s_waitcnt lgkmcnt(10)\n"
    "v_exp_f32_e32 v219, v219\n"
    "v_mfma_f32_16x16x32_bf16 v[172:175], a[176:179], a[144:147], v[172:175]\n"
    "ds_read_b128 a[164:167], v241 offset:18432\n"
    "v_mfma_f32_16x16x32_bf16 v[172:175], a[180:183], a[148:151], v[172:175]\n"
    "v_cvt_pk_bf16_f32 v214, v216, v217\n"
    "v_mfma_f32_16x16x32_bf16 v[164:167], a[180:183], a[116:119], v[164:167]\n"
    "ds_write_addtid_b32 v207 offset:3840\n"
    "v_mfma_f32_16x16x32_bf16 v[164:167], a[176:179], a[112:115], v[164:167]\n"
    "ds_read_b128 a[168:171], v240 offset:22528\n"
    "v_mfma_f32_16x16x32_bf16 v[156:159], a[176:179], a[80:83], v[156:159]\n"
    "v_cvt_pk_bf16_f32 v215, v218, v219\n"
    "v_mfma_f32_16x16x32_bf16 v[156:159], a[180:183], a[84:87], v[156:159]\n"
    "ds_write_addtid_b32 v214 offset:4608\n"
    "v_mfma_f32_16x16x32_bf16 v[148:151], a[180:183], a[52:55], v[148:151]\n"
    "ds_write_addtid_b32 v215 offset:4864\n"
    "v_mfma_f32_16x16x32_bf16 v[148:151], a[176:179], a[48:51], v[148:151]\n"
    "v_mfma_f32_16x16x32_bf16 v[140:143], a[176:179], a[16:19], v[140:143]\n"
    "ds_read_b128 a[172:175], v241 offset:22528\n"
    "v_mfma_f32_16x16x32_bf16 v[140:143], a[180:183], a[20:23], v[140:143]\n"
    "s_waitcnt lgkmcnt(10)\n"
    "s_waitcnt lgkmcnt(1)\n"
    "v_mfma_f32_16x16x32_bf16 v[140:143], a[184:187], a[24:27], v[140:143]\n"
    "s_barrier\n"
    "v_mfma_f32_16x16x32_bf16 v[140:143], a[188:191], a[28:31], v[140:143]\n"
    "s_add_u32 m0, s43, 0\n"
    "v_mfma_f32_16x16x32_bf16 v[148:151], a[188:191], a[60:63], v[148:151]\n"
    "ds_read_b128 a[176:179], v242 offset:18432\n"
    "v_mfma_f32_16x16x32_bf16 v[148:151], a[184:187], a[56:59], v[148:151]\n"
    "buffer_load_dwordx4 v248, s[8:11], s40 offen offset:0 sc1 lds\n"
    "v_mfma_f32_16x16x32_bf16 v[156:159], a[184:187], a[88:91], v[156:159]\n"
    "ds_read2st64_b32 v[184:185], v249 offset0:0 offset1:1\n"
    "v_mfma_f32_16x16x32_bf16 v[156:159], a[188:191], a[92:95], v[156:159]\n"
    "v_mfma_f32_16x16x32_bf16 v[164:167], a[188:191], a[124:127], v[164:167]\n"
    "ds_read2st64_b32 v[186:187], v249 offset0:2 offset1:3\n"
    "v_mfma_f32_16x16x32_bf16 v[164:167], a[184:187], a[120:123], v[164:167]\n"
    "buffer_load_dwordx4 v248, s[8:11], s40 offen offset:1024 sc1 lds\n"
    "v_mfma_f32_16x16x32_bf16 v[172:175], a[184:187], a[152:155], v[172:175]\n"
    "v_mfma_f32_16x16x32_bf16 v[172:175], a[188:191], a[156:159], v[172:175]\n"
    "v_mfma_f32_16x16x32_bf16 v[176:179], a[160:163], a[128:131], v[236:239]\n"
    "ds_read_b128 a[180:183], v243 offset:18432\n"
    "v_mfma_f32_16x16x32_bf16 v[176:179], a[164:167], a[132:135], v[176:179]\n"
    "buffer_load_dwordx4 v248, s[8:11], s40 offen offset:2048 sc1 lds\n"
    "v_mfma_f32_16x16x32_bf16 v[168:171], a[164:167], a[100:103], v[232:235]\n"
    "ds_read2st64_b32 v[192:193], v249 offset0:4 offset1:5\n"
    "v_mfma_f32_16x16x32_bf16 v[168:171], a[160:163], a[96:99], v[168:171]\n"
    "ds_read_b128 a[184:187], v242 offset:22528\n"
    "v_mfma_f32_16x16x32_bf16 v[160:163], a[160:163], a[64:67], v[228:231]\n"
    "ds_read2st64_b32 v[194:195], v249 offset0:6 offset1:7\n"
    "v_mfma_f32_16x16x32_bf16 v[160:163], a[164:167], a[68:71], v[160:163]\n"
    "buffer_load_dwordx4 v248, s[8:11], s40 offen offset:3072 sc1 lds\n"
    "v_mfma_f32_16x16x32_bf16 v[152:155], a[164:167], a[36:39], v[224:227]\n"
    "s_add_u32 m0, s43, 4096\n"
    "v_exp_f32_e32 v140, v140\n"
    "v_mfma_f32_16x16x32_bf16 v[152:155], a[160:163], a[32:35], v[152:155]\n"
    "s_add_u32 s40, s40, 0x8000\n"
    "v_exp_f32_e32 v141, v141\n"
    "v_mfma_f32_16x16x32_bf16 v[144:147], a[160:163], a[0:3], v[220:223]\n"
    "ds_read2st64_b32 v[200:201], v249 offset0:8 offset1:9\n"
    "v_mfma_f32_16x16x32_bf16 v[144:147], a[164:167], a[4:7], v[144:147]\n"
    "s_waitcnt lgkmcnt(8)\n"
    "buffer_load_dwordx4 v248, s[8:11], s41 offen offset:0 sc1 lds\n"
    "v_mfma_f32_16x16x32_bf16 v[144:147], a[168:171], a[8:11], v[144:147]\n"
    "ds_read_b128 a[188:191], v243 offset:22528\n"
    "v_mfma_f32_16x16x32_bf16 v[144:147], a[172:175], a[12:15], v[144:147]\n"
    "ds_read_b64_tr_b16 a[160:161], v244 offset:8192\n"
    "v_mfma_f32_16x16x32_bf16 v[152:155], a[172:175], a[44:47], v[152:155]\n"
    "ds_read2st64_b32 v[202:203], v249 offset0:10 offset1:11\n"
    "v_mfma_f32_16x16x32_bf16 v[152:155], a[168:171], a[40:43], v[152:155]\n"
    "buffer_load_dwordx4 v248, s[8:11], s41 offen offset:1024 sc1 lds\n"
    "v_mfma_f32_16x16x32_bf16 v[160:163], a[168:171], a[72:75], v[160:163]\n"
    "ds_read_b64_tr_b16 a[162:163], v244 offset:10240\n"
    "v_mfma_f32_16x16x32_bf16 v[160:163], a[172:175], a[76:79], v[160:163]\n"
    "v_exp_f32_e32 v142, v142\n"
    "v_mfma_f32_16x16x32_bf16 v[168:171], a[172:175], a[108:111], v[168:171]\n"
    "ds_read2st64_b32 v[208:209], v249 offset0:12 offset1:13\n"
    "v_mfma_f32_16x16x32_bf16 v[168:171], a[168:171], a[104:107], v[168:171]\n"
    "buffer_load_dwordx4 v248, s[8:11], s41 offen offset:2048 sc1 lds\n"
    "v_mfma_f32_16x16x32_bf16 v[176:179], a[168:171], a[136:139], v[176:179]\n"
    "ds_read_b64_tr_b16 a[164:165], v244 offset:40960\n"
    "v_mfma_f32_16x16x32_bf16 v[176:179], a[172:175], a[140:143], v[176:179]\n"
    "s_waitcnt lgkmcnt(10)\n"
    "ds_read2st64_b32 v[210:211], v249 offset0:14 offset1:15\n"
    "v_mfma_f32_16x16x32_bf16 v[176:179], a[176:179], a[144:147], v[176:179]\n"
    "ds_read_b64_tr_b16 a[168:169], v245 offset:8192\n"
    "v_mfma_f32_16x16x32_bf16 v[176:179], a[180:183], a[148:151], v[176:179]\n"
    "buffer_load_dwordx4 v248, s[8:11], s41 offen offset:3072 sc1 lds\n"
    "v_mfma_f32_16x16x32_bf16 v[168:171], a[180:183], a[116:119], v[168:171]\n"
    "ds_read_b64_tr_b16 a[166:167], v244 offset:43008\n"
    "v_mfma_f32_16x16x32_bf16 v[168:171], a[176:179], a[112:115], v[168:171]\n"
    "s_add_u32 s41, s41, 0x8000\n"
    "v_exp_f32_e32 v143, v143\n"
    "v_mfma_f32_16x16x32_bf16 v[160:163], a[176:179], a[80:83], v[160:163]\n"
    "ds_read_b64_tr_b16 a[170:171], v245 offset:10240\n"
    "v_cvt_pk_bf16_f32 v140, v140, v141\n"
    "v_mfma_f32_16x16x32_bf16 v[160:163], a[180:183], a[84:87], v[160:163]\n"
    "ds_read2st64_b32 v[216:217], v249 offset0:16 offset1:17\n"
    "v_mfma_f32_16x16x32_bf16 v[152:155], a[180:183], a[52:55], v[152:155]\n"
    "v_exp_f32_e32 v148, v148\n"
    "v_mfma_f32_16x16x32_bf16 v[152:155], a[176:179], a[48:51], v[152:155]\n"
    "v_exp_f32_e32 v149, v149\n"
    "v_mfma_f32_16x16x32_bf16 v[144:147], a[176:179], a[16:19], v[144:147]\n"
    "ds_read_b64_tr_b16 a[172:173], v245 offset:40960\n"
    "v_cvt_pk_bf16_f32 v141, v142, v143\n"
    "v_mfma_f32_16x16x32_bf16 v[144:147], a[180:183], a[20:23], v[144:147]\n"
    "s_waitcnt lgkmcnt(11)\n"
    "ds_read2st64_b32 v[218:219], v249 offset0:18 offset1:19\n"
    "v_mfma_f32_16x16x32_bf16 v[144:147], a[184:187], a[24:27], v[144:147]\n"
    "ds_read_b64_tr_b16 a[176:177], v246 offset:8192\n"
    "v_mfma_f32_16x16x32_bf16 v[144:147], a[188:191], a[28:31], v[144:147]\n"
    "ds_read_b64_tr_b16 a[174:175], v245 offset:43008\n"
    "v_mfma_f32_16x16x32_bf16 v[152:155], a[188:191], a[60:63], v[152:155]\n"
    "v_exp_f32_e32 v150, v150\n"
    "v_mfma_f32_16x16x32_bf16 v[152:155], a[184:187], a[56:59], v[152:155]\n"
    "v_exp_f32_e32 v151, v151\n"
    "v_mfma_f32_16x16x32_bf16 v[160:163], a[184:187], a[88:91], v[160:163]\n"
    "s_waitcnt vmcnt(8)\n"
    "v_readfirstlane_b32 s81, v253\n"
    "v_mfma_f32_16x16x32_bf16 v[160:163], a[188:191], a[92:95], v[160:163]\n"
    "ds_read_b64_tr_b16 a[178:179], v246 offset:10240\n"
    "v_mfma_f32_16x16x32_bf16 v[168:171], a[188:191], a[124:127], v[168:171]\n"
    "s_cmp_eq_u32 s81, s39\n"
    "v_cvt_pk_bf16_f32 v148, v148, v149\n"
    "v_mfma_f32_16x16x32_bf16 v[168:171], a[184:187], a[120:123], v[168:171]\n"
    "s_cselect_b32 s88, s83, 2.0\n"
    "v_exp_f32_e32 v156, v156\n"
    "v_mfma_f32_16x16x32_bf16 v[176:179], a[184:187], a[152:155], v[176:179]\n"
    "s_mov_b32 m0, s30\n"
    "ds_read_b64_tr_b16 a[180:181], v246 offset:40960\n"
    "v_mfma_f32_16x16x32_bf16 v[176:179], a[188:191], a[156:159], v[176:179]\n"
    "s_waitcnt lgkmcnt(4)\n"
    "v_exp_f32_e32 v157, v157\n"
    "v_mfma_f32_16x16x32_bf16 a[236:239], a[160:163], v[180:183], a[236:239]\n"
    "buffer_load_dwordx4 v250, s[8:11], s88 offen offset:0 sc1 lds\n"
    "v_mfma_f32_16x16x32_bf16 a[236:239], a[164:167], v[184:187], a[236:239]\n"
    "ds_read_b64_tr_b16 a[182:183], v246 offset:43008\n"
    "v_mfma_f32_16x16x32_bf16 a[240:243], a[164:167], v[192:195], a[240:243]\n"
    "ds_read_b64_tr_b16 a[184:185], v247 offset:8192\n"
    "v_mfma_f32_16x16x32_bf16 a[240:243], a[160:163], v[188:191], a[240:243]\n"
    "v_cvt_pk_bf16_f32 v149, v150, v151\n"
    "v_mfma_f32_16x16x32_bf16 a[244:247], a[160:163], v[196:199], a[244:247]\n"
    "ds_read_b64_tr_b16 a[186:187], v247 offset:10240\n"
    "v_mfma_f32_16x16x32_bf16 a[244:247], a[164:167], v[200:203], a[244:247]\n"
    "v_exp_f32_e32 v158, v158\n"
    "v_mfma_f32_16x16x32_bf16 a[248:251], a[164:167], v[208:211], a[248:251]\n"
    "v_exp_f32_e32 v159, v159\n"
    "v_mfma_f32_16x16x32_bf16 a[248:251], a[160:163], v[204:207], a[248:251]\n"
    "v_cvt_pk_bf16_f32 v156, v156, v157\n"
    "v_mfma_f32_16x16x32_bf16 a[252:255], a[160:163], v[212:215], a[252:255]\n"
    "ds_read_b64_tr_b16 a[188:189], v247 offset:40960\n"
    "v_mfma_f32_16x16x32_bf16 a[252:255], a[164:167], v[216:219], a[252:255]\n"
    "s_waitcnt lgkmcnt(6)\n"
    "v_exp_f32_e32 v164, v164\n"
    "v_mfma_f32_16x16x32_bf16 v[16:19], a[172:175], v[216:219], v[16:19]\n"
    "ds_read_b64_tr_b16 a[160:161], v244 offset:12288\n"
    "v_mfma_f32_16x16x32_bf16 v[16:19], a[168:171], v[212:215], v[16:19]\n"
    "ds_read_b64_tr_b16 a[190:191], v247 offset:43008\n"
    "v_mfma_f32_16x16x32_bf16 v[12:15], a[168:171], v[204:207], v[12:15]\n"
    "v_exp_f32_e32 v165, v165\n"
    "v_mfma_f32_16x16x32_bf16 v[12:15], a[172:175], v[208:211], v[12:15]\n"
    "v_cvt_pk_bf16_f32 v157, v158, v159\n"
    "v_mfma_f32_16x16x32_bf16 v[8:11], a[172:175], v[200:203], v[8:11]\n"
    "ds_read_b64_tr_b16 a[162:163], v244 offset:14336\n"
    "v_mfma_f32_16x16x32_bf16 v[8:11], a[168:171], v[196:199], v[8:11]\n"
    "v_exp_f32_e32 v166, v166\n"
    "v_mfma_f32_16x16x32_bf16 v[4:7], a[168:171], v[188:191], v[4:7]\n"
    "v_exp_f32_e32 v167, v167\n"
    "v_mfma_f32_16x16x32_bf16 v[4:7], a[172:175], v[192:195], v[4:7]\n"
    "v_cvt_pk_bf16_f32 v164, v164, v165\n"
    "v_mfma_f32_16x16x32_bf16 v[0:3], a[172:175], v[184:187], v[0:3]\n"
    "ds_read_b64_tr_b16 a[164:165], v244 offset:45056\n"
    "v_mfma_f32_16x16x32_bf16 v[0:3], a[168:171], v[180:183], v[0:3]\n"
    "s_waitcnt lgkmcnt(7)\n"
    "v_exp_f32_e32 v172, v172\n"
    "v_mfma_f32_16x16x32_bf16 v[20:23], a[176:179], v[180:183], v[20:23]\n"
    "v_mfma_f32_16x16x32_bf16 v[20:23], a[180:183], v[184:187], v[20:23]\n"
    "ds_read_b64_tr_b16 a[166:167], v244 offset:47104\n"
    "v_mfma_f32_16x16x32_bf16 v[24:27], a[180:183], v[192:195], v[24:27]\n"
    "ds_read_b64_tr_b16 a[168:169], v245 offset:12288\n"
    "v_mfma_f32_16x16x32_bf16 v[24:27], a[176:179], v[188:191], v[24:27]\n"
    "v_exp_f32_e32 v173, v173\n"
    "v_mfma_f32_16x16x32_bf16 v[28:31], a[176:179], v[196:199], v[28:31]\n"
    "ds_read_b64_tr_b16 a[170:171], v245 offset:14336\n"
    "v_mfma_f32_16x16x32_bf16 v[28:31], a[180:183], v[200:203], v[28:31]\n"
    "v_cvt_pk_bf16_f32 v165, v166, v167\n"
    "v_mfma_f32_16x16x32_bf16 v[32:35], a[180:183], v[208:211], v[32:35]\n"
    "v_exp_f32_e32 v174, v174\n"
    "v_mfma_f32_16x16x32_bf16 v[32:35], a[176:179], v[204:207], v[32:35]\n"
    "v_exp_f32_e32 v175, v175\n"
    "v_mfma_f32_16x16x32_bf16 v[36:39], a[176:179], v[212:215], v[36:39]\n"
    "ds_read_b64_tr_b16 a[172:173], v245 offset:45056\n"
    "v_mfma_f32_16x16x32_bf16 v[36:39], a[180:183], v[216:219], v[36:39]\n"
    "s_waitcnt lgkmcnt(6)\n"
    "v_cvt_pk_bf16_f32 v172, v172, v173\n"
    "v_mfma_f32_16x16x32_bf16 v[56:59], a[188:191], v[216:219], v[56:59]\n"
    ".long 0xbedf00ff\n"
    ".Lat_w5:\n"
    "s_setpc_b64 s[2:3]\n"
    "ds_read_b64_tr_b16 a[176:177], v246 offset:12288\n"
    "v_mfma_f32_16x16x32_bf16 v[56:59], a[184:187], v[212:215], v[56:59]\n"
    "ds_read_b64_tr_b16 a[174:175], v245 offset:47104\n"
    "v_mfma_f32_16x16x32_bf16 v[52:55], a[184:187], v[204:207], v[52:55]\n"
    "v_cvt_pk_bf16_f32 v173, v174, v175\n"
    "v_mfma_f32_16x16x32_bf16 v[52:55], a[188:191], v[208:211], v[52:55]\n"
    "v_exp_f32_e32 v144, v144\n"
    "v_mfma_f32_16x16x32_bf16 v[48:51], a[188:191], v[200:203], v[48:51]\n"
    "ds_read_b64_tr_b16 a[178:179], v246 offset:14336\n"
    "v_mfma_f32_16x16x32_bf16 v[48:51], a[184:187], v[196:199], v[48:51]\n"
    "v_mfma_f32_16x16x32_bf16 v[44:47], a[184:187], v[188:191], v[44:47]\n"
    "v_exp_f32_e32 v145, v145\n"
    "v_mfma_f32_16x16x32_bf16 v[44:47], a[188:191], v[192:195], v[44:47]\n"
    "v_exp_f32_e32 v146, v146\n"
    "v_mfma_f32_16x16x32_bf16 v[40:43], a[188:191], v[184:187], v[40:43]\n"
    "ds_read_b64_tr_b16 a[180:181], v246 offset:45056\n"
    "v_mfma_f32_16x16x32_bf16 v[40:43], a[184:187], v[180:183], v[40:43]\n"
    "s_waitcnt lgkmcnt(7)\n"
    "v_exp_f32_e32 v147, v147\n"
    "v_mfma_f32_16x16x32_bf16 v[60:63], a[160:163], v[180:183], v[60:63]\n"
    "ds_read_b64_tr_b16 a[184:185], v247 offset:12288\n"
    "v_mfma_f32_16x16x32_bf16 v[60:63], a[164:167], v[184:187], v[60:63]\n"
    "ds_read_b64_tr_b16 a[182:183], v246 offset:47104\n"
    "v_mfma_f32_16x16x32_bf16 v[64:67], a[164:167], v[192:195], v[64:67]\n"
    "ds_read_b64_tr_b16 a[186:187], v247 offset:14336\n"
    "v_mfma_f32_16x16x32_bf16 v[64:67], a[160:163], v[188:191], v[64:67]\n"
    "v_cvt_pk_bf16_f32 v142, v144, v145\n"
    "v_mfma_f32_16x16x32_bf16 v[68:71], a[160:163], v[196:199], v[68:71]\n"
    "v_exp_f32_e32 v152, v152\n"
    "v_mfma_f32_16x16x32_bf16 v[68:71], a[164:167], v[200:203], v[68:71]\n"
    "ds_read_b64_tr_b16 a[188:189], v247 offset:45056\n"
    "v_mfma_f32_16x16x32_bf16 v[72:75], a[164:167], v[208:211], v[72:75]\n"
    "ds_read_b64_tr_b16 a[190:191], v247 offset:47104\n"
    "v_mfma_f32_16x16x32_bf16 v[72:75], a[160:163], v[204:207], v[72:75]\n"
    "s_waitcnt vmcnt(10)\n"
    "v_mfma_f32_16x16x32_bf16 v[76:79], a[160:163], v[212:215], v[76:79]\n"
    "s_barrier\n"
    "v_mfma_f32_16x16x32_bf16 v[76:79], a[164:167], v[216:219], v[76:79]\n"
    "s_waitcnt lgkmcnt(7)\n"
    "s_mov_b32 m0, s42\n"
    "v_mfma_f32_16x16x32_bf16 v[96:99], a[172:175], v[216:219], v[96:99]\n"
    "v_exp_f32_e32 v153, v153\n"
    "v_mfma_f32_16x16x32_bf16 v[96:99], a[168:171], v[212:215], v[96:99]\n"
    "v_cvt_pk_bf16_f32 v143, v146, v147\n"
    "v_mfma_f32_16x16x32_bf16 v[92:95], a[168:171], v[204:207], v[92:95]\n"
    "v_exp_f32_e32 v154, v154\n"
    "v_mfma_f32_16x16x32_bf16 v[92:95], a[172:175], v[208:211], v[92:95]\n"
    "ds_read_b128 a[160:163], v240 offset:24576\n"
    "v_mfma_f32_16x16x32_bf16 v[88:91], a[172:175], v[200:203], v[88:91]\n"
    "ds_write_addtid_b32 v140 offset:0\n"
    "v_mfma_f32_16x16x32_bf16 v[88:91], a[168:171], v[196:199], v[88:91]\n"
    "v_exp_f32_e32 v155, v155\n"
    "v_mfma_f32_16x16x32_bf16 v[84:87], a[168:171], v[188:191], v[84:87]\n"
    "ds_write_addtid_b32 v141 offset:256\n"
    "v_mfma_f32_16x16x32_bf16 v[84:87], a[172:175], v[192:195], v[84:87]\n"
    "ds_write_addtid_b32 v148 offset:1024\n"
    "v_mfma_f32_16x16x32_bf16 v[80:83], a[172:175], v[184:187], v[80:83]\n"
    "v_cvt_pk_bf16_f32 v150, v152, v153\n"
    "v_mfma_f32_16x16x32_bf16 v[80:83], a[168:171], v[180:183], v[80:83]\n"
    "s_waitcnt lgkmcnt(7)\n"
    "v_exp_f32_e32 v160, v160\n"
    "v_mfma_f32_16x16x32_bf16 v[100:103], a[176:179], v[180:183], v[100:103]\n"
    "ds_read_b128 a[164:167], v241 offset:24576\n"
    "v_mfma_f32_16x16x32_bf16 v[100:103], a[180:183], v[184:187], v[100:103]\n"
    "ds_write_addtid_b32 v149 offset:1280\n"
    "v_mfma_f32_16x16x32_bf16 v[104:107], a[180:183], v[192:195], v[104:107]\n"
    "v_exp_f32_e32 v161, v161\n"
    "v_mfma_f32_16x16x32_bf16 v[104:107], a[176:179], v[188:191], v[104:107]\n"
    "ds_read_b128 a[168:171], v240 offset:28672\n"
    "v_mfma_f32_16x16x32_bf16 v[108:111], a[176:179], v[196:199], v[108:111]\n"
    "ds_write_addtid_b32 v156 offset:2048\n"
    "v_mfma_f32_16x16x32_bf16 v[108:111], a[180:183], v[200:203], v[108:111]\n"
    "v_cvt_pk_bf16_f32 v151, v154, v155\n"
    "v_mfma_f32_16x16x32_bf16 v[112:115], a[180:183], v[208:211], v[112:115]\n"
    "ds_write_addtid_b32 v157 offset:2304\n"
    "v_mfma_f32_16x16x32_bf16 v[112:115], a[176:179], v[204:207], v[112:115]\n"
    "v_exp_f32_e32 v162, v162\n"
    "v_mfma_f32_16x16x32_bf16 v[116:119], a[176:179], v[212:215], v[116:119]\n"
    "ds_write_addtid_b32 v164 offset:3072\n"
    "v_mfma_f32_16x16x32_bf16 v[116:119], a[180:183], v[216:219], v[116:119]\n"
    "s_waitcnt lgkmcnt(10)\n"
    "v_exp_f32_e32 v163, v163\n"
    "v_mfma_f32_16x16x32_bf16 v[136:139], a[188:191], v[216:219], v[136:139]\n"
    "ds_read_b128 a[172:175], v241 offset:28672\n"
    "v_mfma_f32_16x16x32_bf16 v[136:139], a[184:187], v[212:215], v[136:139]\n"
    "ds_write_addtid_b32 v165 offset:3328\n"
    "v_cvt_pk_bf16_f32 v158, v160, v161\n"
    "v_mfma_f32_16x16x32_bf16 v[132:135], a[184:187], v[204:207], v[132:135]\n"
    "ds_read_b128 a[176:179], v242 offset:24576\n"
    "v_mfma_f32_16x16x32_bf16 v[132:135], a[188:191], v[208:211], v[132:135]\n"
    "ds_write_addtid_b32 v172 offset:4096\n"
    "v_mfma_f32_16x16x32_bf16 v[128:131], a[188:191], v[200:203], v[128:131]\n"
    "v_exp_f32_e32 v168, v168\n"
    "v_mfma_f32_16x16x32_bf16 v[128:131], a[184:187], v[196:199], v[128:131]\n"
    "s_sub_u32 s44, s44, 1\n"
    "ds_write_addtid_b32 v173 offset:4352\n"
    "v_mfma_f32_16x16x32_bf16 v[124:127], a[184:187], v[188:191], v[124:127]\n"
    "s_cmp_eq_u32 s44, s80\n"
    "v_exp_f32_e32 v169, v169\n"
    "v_mfma_f32_16x16x32_bf16 v[124:127], a[188:191], v[192:195], v[124:127]\n"
    "s_cselect_b32 s88, s39, s81\n"
    "ds_read_b128 a[180:183], v243 offset:24576\n"
    "v_mfma_f32_16x16x32_bf16 v[120:123], a[188:191], v[184:187], v[120:123]\n"
    "s_cmp_lg_u32 s88, s39\n"
    "ds_write_addtid_b32 v142 offset:512\n"
    "v_mfma_f32_16x16x32_bf16 v[120:123], a[184:187], v[180:183], v[120:123]\n"
    "s_waitcnt lgkmcnt(12)\n"
    "v_cvt_pk_bf16_f32 v159, v162, v163\n"
    "s_cbranch_scc1 .Lat_loop\n"
    "s_cmp_eq_u32 s44, 0\n"
    "s_cbranch_scc1 .Lat_tail\n"
    "s_cmp_eq_u32 s85, 0\n"
    "s_cbranch_scc1 .Lat_loop\n"
    "s_cmp_lg_u32 s81, s39\n"
    "s_cbranch_scc1 .Lat_lp0\n"
    ".Lat_g1b:\n"
    "v_lshrrev_b32_e32 v228, 2, v250\n"
    "v_add_u32_e32 v254, s30, v228\n"
    "s_waitcnt vmcnt(0)\n"
    "ds_read_b32 v254, v254 offset:960\n"
    "v_lshrrev_b32_e32 v228, 9, v250\n"
    "v_lshl_add_u32 v251, v228, 2, s30\n"
    "v_and_b32_e32 v252, 0xf0, v250\n"
    "v_lshl_add_u32 v252, v228, 8, v252\n"
    "v_bfe_u32 v229, v250, 8, 1\n"
    "v_lshl_add_u32 v252, v229, 3, v252\n"
    "v_and_b32_e32 v239, 0x1f0, v250\n"
    "v_bfe_u32 v228, v250, 2, 4\n"
    "s_mov_b32 s88, 0xcedc\n"
    "v_bfe_u32 v238, s88, v228, 4\n"
    "v_mul_u32_u24_e32 v238, 0x1111, v238\n"
    "v_mov_b64_e32 v[224:225], 0\n"
    "v_mov_b64_e32 v[226:227], 0\n"
    "s_mov_b32 s85, 0\n"
    "s_mov_b32 s82, 2.0\n"
    "s_mov_b32 s83, 2.0\n"
    "s_mov_b32 s89, 4\n"
    "s_waitcnt lgkmcnt(0)\n"
    "v_readlane_b32 s88, v254, 0\n"
    "v_readlane_b32 s87, v254, 1\n"
    "v_readlane_b32 s84, v254, 2\n"
    "v_readlane_b32 s86, v254, 4\n"
    "s_nop 3\n"
    "s_min_u32 s88, s88, s44\n"
    "s_sub_u32 s80, s44, s88\n"
    "s_cmp_eq_u32 s88, 0\n"
    "s_cbranch_scc1 .Lat_g2\n"
    ".Lat_gloop:\n"
    "v_mfma_f32_16x16x32_bf16 v[212:215], a[160:163], a[128:131], v[220:223]\n"
    "ds_write_addtid_b32 v143 offset:768\n"
    "v_mfma_f32_16x16x32_bf16 v[212:215], a[164:167], a[132:135], v[212:215]\n"
    "v_exp_f32_e32 v170, v170\n"
    "v_mfma_f32_16x16x32_bf16 v[204:207], a[164:167], a[100:103], v[220:223]\n"
    "ds_write_addtid_b32 v150 offset:1536\n"
    "v_mfma_f32_16x16x32_bf16 v[204:207], a[160:163], a[96:99], v[204:207]\n"
    "ds_read_b128 a[184:187], v242 offset:28672\n"
    "v_mfma_f32_16x16x32_bf16 v[196:199], a[160:163], a[64:67], v[220:223]\n"
    "v_exp_f32_e32 v171, v171\n"
    "v_mfma_f32_16x16x32_bf16 v[196:199], a[164:167], a[68:71], v[196:199]\n"
    "ds_write_addtid_b32 v151 offset:1792\n"
    "v_cvt_pk_bf16_f32 v166, v168, v169\n"
    "v_mfma_f32_16x16x32_bf16 v[188:191], a[164:167], a[36:39], v[220:223]\n"
    "v_exp_f32_e32 v176, v176\n"
    "v_mfma_f32_16x16x32_bf16 v[188:191], a[160:163], a[32:35], v[188:191]\n"
    "v_exp_f32_e32 v177, v177\n"
    "v_mfma_f32_16x16x32_bf16 v[180:183], a[160:163], a[0:3], v[220:223]\n"
    "ds_write_addtid_b32 v158 offset:2560\n"
    "v_cvt_pk_bf16_f32 v167, v170, v171\n"
    "v_mfma_f32_16x16x32_bf16 v[180:183], a[164:167], a[4:7], v[180:183]\n"
    "s_waitcnt vmcnt(20)\n"
    "v_exp_f32_e32 v178, v178\n"
    "v_smfmac_f32_16x16x64_bf16 v[224:227], a[232:235], a[192:199], v238\n"
    "s_waitcnt lgkmcnt(10)\n"
    "ds_read_b128 a[188:191], v243 offset:28672\n"
    "v_mfma_f32_16x16x32_bf16 v[180:183], a[168:171], a[8:11], v[180:183]\n"
    "s_bitcmp1_b32 s86, 0\n"
    "ds_read_b128 a[160:163], v240 offset:26624\n"
    "v_mfma_f32_16x16x32_bf16 v[180:183], a[172:175], a[12:15], v[180:183]\n"
    "s_cselect_b64 exec, -1, 0\n"
    "v_cvt_pk_bf16_f32 v236, v224, v225\n"
    "v_cvt_pk_bf16_f32 v237, v226, v227\n"
    "v_mfma_f32_16x16x32_bf16 v[188:191], a[172:175], a[44:47], v[188:191]\n"
    "s_cselect_b32 s88, 512, 0\n"
    "v_mov_b64_e32 v[224:225], 0\n"
    "v_mov_b64_e32 v[226:227], 0\n"
    "v_mfma_f32_16x16x32_bf16 v[188:191], a[168:171], a[40:43], v[188:191]\n"
    "buffer_store_dwordx2 v[236:237], v252, s[8:11], s87 offen sc1\n"
    "v_mfma_f32_16x16x32_bf16 v[196:199], a[168:171], a[72:75], v[196:199]\n"
    "s_mov_b64 exec, -1\n"
    "ds_read2_b32 v[228:229], v251 offset0:0 offset1:2\n"
    "v_mfma_f32_16x16x32_bf16 v[196:199], a[172:175], a[76:79], v[196:199]\n"
    "s_add_u32 s87, s87, s88\n"
    "ds_read2_b32 v[230:231], v251 offset0:4 offset1:6\n"
    "v_mfma_f32_16x16x32_bf16 v[204:207], a[172:175], a[108:111], v[204:207]\n"
    "ds_read_b32 v232, v251 offset:32\n"
    "v_mfma_f32_16x16x32_bf16 v[204:207], a[168:171], a[104:107], v[204:207]\n"
    "v_exp_f32_e32 v179, v179\n"
    "v_mfma_f32_16x16x32_bf16 v[212:215], a[168:171], a[136:139], v[212:215]\n"
    "ds_write_addtid_b32 v159 offset:2816\n"
    "v_cvt_pk_bf16_f32 v174, v176, v177\n"
    "v_mfma_f32_16x16x32_bf16 v[212:215], a[172:175], a[140:143], v[212:215]\n"
    "s_waitcnt lgkmcnt(13)\n"
    "ds_read_b128 a[164:167], v241 offset:26624\n"
    "v_mfma_f32_16x16x32_bf16 v[212:215], a[176:179], a[144:147], v[212:215]\n"
    "ds_write_addtid_b32 v166 offset:3584\n"
    "v_cvt_pk_bf16_f32 v175, v178, v179\n"
    "v_mfma_f32_16x16x32_bf16 v[212:215], a[180:183], a[148:151], v[212:215]\n"
    "ds_read_b128 a[168:171], v240 offset:30720\n"
    "v_mfma_f32_16x16x32_bf16 v[204:207], a[180:183], a[116:119], v[204:207]\n"
    "s_waitcnt lgkmcnt(6)\n"
    "ds_write_addtid_b32 v167 offset:3840\n"
    "v_mad_u32_u16 v234, v228, s90, v239 op_sel:[0,0,0,0]\n"
    "v_mfma_f32_16x16x32_bf16 v[204:207], a[176:179], a[112:115], v[204:207]\n"
    "buffer_load_dwordx4 a[192:195], v234, s[20:23], 0 offen sc1\n"
    "v_mad_u32_u16 v235, v228, s90, v239 op_sel:[1,0,0,0]\n"
    "v_mfma_f32_16x16x32_bf16 v[196:199], a[176:179], a[80:83], v[196:199]\n"
    "ds_write_addtid_b32 v174 offset:4608\n"
    "v_mfma_f32_16x16x32_bf16 v[196:199], a[180:183], a[84:87], v[196:199]\n"
    "ds_write_addtid_b32 v175 offset:4864\n"
    "v_mfma_f32_16x16x32_bf16 v[188:191], a[180:183], a[52:55], v[188:191]\n"
    "buffer_load_dwordx4 a[196:199], v235, s[20:23], 0 offen sc1\n"
    "v_mfma_f32_16x16x32_bf16 v[188:191], a[176:179], a[48:51], v[188:191]\n"
    "v_mfma_f32_16x16x32_bf16 v[180:183], a[176:179], a[16:19], v[180:183]\n"
    "s_waitcnt lgkmcnt(0)\n"
    "v_mfma_f32_16x16x32_bf16 v[180:183], a[180:183], a[20:23], v[180:183]\n"
    "s_barrier\n"
    "v_mfma_f32_16x16x32_bf16 v[180:183], a[184:187], a[24:27], v[180:183]\n"
    "s_add_u32 m0, s43, 8192\n"
    "ds_read_b128 a[172:175], v241 offset:30720\n"
    "v_mfma_f32_16x16x32_bf16 v[180:183], a[188:191], a[28:31], v[180:183]\n"
    "ds_read2st64_b32 v[144:145], v249 offset0:0 offset1:1\n"
    "v_mfma_f32_16x16x32_bf16 v[188:191], a[188:191], a[60:63], v[188:191]\n"
    "buffer_load_dwordx4 v248, s[8:11], s40 offen offset:0 sc1 lds\n"
    "v_mfma_f32_16x16x32_bf16 v[188:191], a[184:187], a[56:59], v[188:191]\n"
    "ds_read_b128 a[176:179], v242 offset:26624\n"
    "v_mfma_f32_16x16x32_bf16 v[196:199], a[184:187], a[88:91], v[196:199]\n"
    "v_mfma_f32_16x16x32_bf16 v[196:199], a[188:191], a[92:95], v[196:199]\n"
    "v_mfma_f32_16x16x32_bf16 v[204:207], a[188:191], a[124:127], v[204:207]\n"
    "buffer_load_dwordx4 v248, s[8:11], s40 offen offset:1024 sc1 lds\n"
    "v_mfma_f32_16x16x32_bf16 v[204:207], a[184:187], a[120:123], v[204:207]\n"
    "ds_read2st64_b32 v[146:147], v249 offset0:2 offset1:3\n"
    "v_mfma_f32_16x16x32_bf16 v[212:215], a[184:187], a[152:155], v[212:215]\n"
    "v_mfma_f32_16x16x32_bf16 v[212:215], a[188:191], a[156:159], v[212:215]\n"
    "ds_read_b128 a[180:183], v243 offset:26624\n"
    "v_mfma_f32_16x16x32_bf16 v[216:219], a[160:163], a[128:131], v[220:223]\n"
    "buffer_load_dwordx4 v248, s[8:11], s40 offen offset:2048 sc1 lds\n"
    "v_mfma_f32_16x16x32_bf16 v[216:219], a[164:167], a[132:135], v[216:219]\n"
    "ds_read2st64_b32 v[152:153], v249 offset0:4 offset1:5\n"
    "v_mfma_f32_16x16x32_bf16 v[208:211], a[164:167], a[100:103], v[220:223]\n"
    "v_exp_f32_e32 v180, v180\n"
    "v_mfma_f32_16x16x32_bf16 v[208:211], a[160:163], a[96:99], v[208:211]\n"
    "ds_read_b128 a[184:187], v242 offset:30720\n"
    "v_mfma_f32_16x16x32_bf16 v[200:203], a[160:163], a[64:67], v[220:223]\n"
    "buffer_load_dwordx4 v248, s[8:11], s40 offen offset:3072 sc1 lds\n"
    "v_mfma_f32_16x16x32_bf16 v[200:203], a[164:167], a[68:71], v[200:203]\n"
    "s_add_u32 m0, s43, 12288\n"
    "ds_read2st64_b32 v[154:155], v249 offset0:6 offset1:7\n"
    "v_mfma_f32_16x16x32_bf16 v[192:195], a[164:167], a[36:39], v[220:223]\n"
    "s_add_u32 s40, s40, 0x8000\n"
    "ds_read2st64_b32 v[160:161], v249 offset0:8 offset1:9\n"
    "v_mfma_f32_16x16x32_bf16 v[192:195], a[160:163], a[32:35], v[192:195]\n"
    "ds_read2st64_b32 v[162:163], v249 offset0:10 offset1:11\n"
    "v_mfma_f32_16x16x32_bf16 v[184:187], a[160:163], a[0:3], v[220:223]\n"
    "buffer_load_dwordx4 v248, s[8:11], s41 offen offset:0 sc1 lds\n"
    "v_mfma_f32_16x16x32_bf16 v[184:187], a[164:167], a[4:7], v[184:187]\n"
    "s_waitcnt vmcnt(17)\n"
    "ds_read_b128 a[188:191], v243 offset:30720\n"
    "v_smfmac_f32_16x16x64_bf16 v[224:227], a[232:235], a[200:207], v238\n"
    "s_waitcnt lgkmcnt(10)\n"
    "v_mad_u32_u16 v234, v229, s90, v239 op_sel:[0,0,0,0]\n"
    "v_mfma_f32_16x16x32_bf16 v[184:187], a[168:171], a[8:11], v[184:187]\n"
    "s_bitcmp1_b32 s86, 1\n"
    "buffer_load_dwordx4 a[200:203], v234, s[20:23], 0 offen sc1\n"
    "v_mad_u32_u16 v235, v229, s90, v239 op_sel:[1,0,0,0]\n"
    "v_mfma_f32_16x16x32_bf16 v[184:187], a[172:175], a[12:15], v[184:187]\n"
    "s_cselect_b64 exec, -1, 0\n"
    "v_cvt_pk_bf16_f32 v236, v224, v225\n"
    "v_cvt_pk_bf16_f32 v237, v226, v227\n"
    "v_mfma_f32_16x16x32_bf16 v[192:195], a[172:175], a[44:47], v[192:195]\n"
    "s_cselect_b32 s88, 512, 0\n"
    "v_mov_b64_e32 v[224:225], 0\n"
    "v_mov_b64_e32 v[226:227], 0\n"
    "v_mfma_f32_16x16x32_bf16 v[192:195], a[168:171], a[40:43], v[192:195]\n"
    "buffer_store_dwordx2 v[236:237], v252, s[8:11], s87 offen sc1\n"
    "v_mfma_f32_16x16x32_bf16 v[200:203], a[168:171], a[72:75], v[200:203]\n"
    "s_mov_b64 exec, -1\n"
    "ds_read_b64_tr_b16 a[160:161], v244 offset:16384\n"
    "v_mfma_f32_16x16x32_bf16 v[200:203], a[172:175], a[76:79], v[200:203]\n"
    "s_add_u32 s87, s87, s88\n"
    "ds_read_b64_tr_b16 a[162:163], v244 offset:18432\n"
    "v_mfma_f32_16x16x32_bf16 v[208:211], a[172:175], a[108:111], v[208:211]\n"
    "buffer_load_dwordx4 v248, s[8:11], s41 offen offset:1024 sc1 lds\n"
    "v_mfma_f32_16x16x32_bf16 v[208:211], a[168:171], a[104:107], v[208:211]\n"
    "ds_read_b64_tr_b16 a[164:165], v244 offset:49152\n"
    "v_mfma_f32_16x16x32_bf16 v[216:219], a[168:171], a[136:139], v[216:219]\n"
    "ds_read2st64_b32 v[168:169], v249 offset0:12 offset1:13\n"
    "v_mfma_f32_16x16x32_bf16 v[216:219], a[172:175], a[140:143], v[216:219]\n"
    "s_waitcnt lgkmcnt(10)\n"
    "ds_read2st64_b32 v[170:171], v249 offset0:14 offset1:15\n"
    "v_mfma_f32_16x16x32_bf16 v[216:219], a[176:179], a[144:147], v[216:219]\n"
    "buffer_load_dwordx4 v248, s[8:11], s41 offen offset:2048 sc1 lds\n"
    "v_mfma_f32_16x16x32_bf16 v[216:219], a[180:183], a[148:151], v[216:219]\n"
    "ds_read_b64_tr_b16 a[166:167], v244 offset:51200\n"
    "v_mfma_f32_16x16x32_bf16 v[208:211], a[180:183], a[116:119], v[208:211]\n"
    "ds_read_b64_tr_b16 a[168:169], v245 offset:16384\n"
    "v_mfma_f32_16x16x32_bf16 v[208:211], a[176:179], a[112:115], v[208:211]\n"
    "v_exp_f32_e32 v181, v181\n"
    "v_mfma_f32_16x16x32_bf16 v[200:203], a[176:179], a[80:83], v[200:203]\n"
    "buffer_load_dwordx4 v248, s[8:11], s41 offen offset:3072 sc1 lds\n"
    "v_mfma_f32_16x16x32_bf16 v[200:203], a[180:183], a[84:87], v[200:203]\n"
    "ds_read_b64_tr_b16 a[170:171], v245 offset:18432\n"
    "v_mfma_f32_16x16x32_bf16 v[192:195], a[180:183], a[52:55], v[192:195]\n"
    "s_add_u32 s41, s41, 0x8000\n"
    "ds_read2st64_b32 v[176:177], v249 offset0:16 offset1:17\n"
    "v_mfma_f32_16x16x32_bf16 v[192:195], a[176:179], a[48:51], v[192:195]\n"
    "buffer_load_dwordx4 a[204:207], v235, s[20:23], 0 offen sc1\n"
    "v_mfma_f32_16x16x32_bf16 v[184:187], a[176:179], a[16:19], v[184:187]\n"
    "ds_read_b64_tr_b16 a[172:173], v245 offset:49152\n"
    "v_mfma_f32_16x16x32_bf16 v[184:187], a[180:183], a[20:23], v[184:187]\n"
    "s_waitcnt lgkmcnt(10)\n"
    "ds_read2st64_b32 v[178:179], v249 offset0:18 offset1:19\n"
    "v_mfma_f32_16x16x32_bf16 v[184:187], a[184:187], a[24:27], v[184:187]\n"
    "ds_read_b64_tr_b16 a[176:177], v246 offset:16384\n"
    "v_mfma_f32_16x16x32_bf16 v[184:187], a[188:191], a[28:31], v[184:187]\n"
    "ds_read_b64_tr_b16 a[174:175], v245 offset:51200\n"
    "v_mfma_f32_16x16x32_bf16 v[192:195], a[188:191], a[60:63], v[192:195]\n"
    "v_exp_f32_e32 v182, v182\n"
    "v_mfma_f32_16x16x32_bf16 v[192:195], a[184:187], a[56:59], v[192:195]\n"
    "v_exp_f32_e32 v183, v183\n"
    "v_mfma_f32_16x16x32_bf16 v[200:203], a[184:187], a[88:91], v[200:203]\n"
    "ds_read_b64_tr_b16 a[178:179], v246 offset:18432\n"
    "v_cvt_pk_bf16_f32 v180, v180, v181\n"
    "v_mfma_f32_16x16x32_bf16 v[200:203], a[188:191], a[92:95], v[200:203]\n"
    "v_exp_f32_e32 v188, v188\n"
    "v_mfma_f32_16x16x32_bf16 v[208:211], a[188:191], a[124:127], v[208:211]\n"
    "v_exp_f32_e32 v189, v189\n"
    "v_mfma_f32_16x16x32_bf16 v[208:211], a[184:187], a[120:123], v[208:211]\n"
    "v_cvt_pk_bf16_f32 v181, v182, v183\n"
    "v_mfma_f32_16x16x32_bf16 v[216:219], a[184:187], a[152:155], v[216:219]\n"
    "ds_read_b64_tr_b16 a[180:181], v246 offset:49152\n"
    "v_mfma_f32_16x16x32_bf16 v[216:219], a[188:191], a[156:159], v[216:219]\n"
    "s_waitcnt vmcnt(20)\n"
    "ds_read_b64_tr_b16 a[182:183], v246 offset:51200\n"
    "v_smfmac_f32_16x16x64_bf16 v[224:227], a[232:235], a[208:215], v238\n"
    "s_waitcnt lgkmcnt(5)\n"
    "v_mad_u32_u16 v234, v230, s90, v239 op_sel:[0,0,0,0]\n"
    "v_mfma_f32_16x16x32_bf16 a[236:239], a[160:163], v[140:143], a[236:239]\n"
    "s_bitcmp1_b32 s86, 2\n"
    "buffer_load_dwordx4 a[208:211], v234, s[20:23], 0 offen sc1\n"
    "v_mad_u32_u16 v235, v230, s90, v239 op_sel:[1,0,0,0]\n"
    "v_mfma_f32_16x16x32_bf16 a[236:239], a[164:167], v[144:147], a[236:239]\n"
    "s_cselect_b64 exec, -1, 0\n"
    "v_cvt_pk_bf16_f32 v236, v224, v225\n"
    "v_cvt_pk_bf16_f32 v237, v226, v227\n"
    "v_mfma_f32_16x16x32_bf16 a[240:243], a[164:167], v[152:155], a[240:243]\n"
    "s_cselect_b32 s88, 512, 0\n"
    "v_mov_b64_e32 v[224:225], 0\n"
    "v_mov_b64_e32 v[226:227], 0\n"
    "v_mfma_f32_16x16x32_bf16 a[240:243], a[160:163], v[148:151], a[240:243]\n"
    "buffer_store_dwordx2 v[236:237], v252, s[8:11], s87 offen sc1\n"
    "v_mfma_f32_16x16x32_bf16 a[244:247], a[160:163], v[156:159], a[244:247]\n"
    "s_mov_b64 exec, -1\n"
    "ds_read_b64_tr_b16 a[184:185], v247 offset:16384\n"
    "v_mfma_f32_16x16x32_bf16 a[244:247], a[164:167], v[160:163], a[244:247]\n"
    "s_add_u32 s87, s87, s88\n"
    "ds_read_b64_tr_b16 a[186:187], v247 offset:18432\n"
    "v_mfma_f32_16x16x32_bf16 a[248:251], a[164:167], v[168:171], a[248:251]\n"
    "buffer_load_dwordx4 a[212:215], v235, s[20:23], 0 offen sc1\n"
    "v_mfma_f32_16x16x32_bf16 a[248:251], a[160:163], v[164:167], a[248:251]\n"
    "ds_read_b64_tr_b16 a[188:189], v247 offset:49152\n"
    "v_mfma_f32_16x16x32_bf16 a[252:255], a[160:163], v[172:175], a[252:255]\n"
    "v_exp_f32_e32 v190, v190\n"
    "v_mfma_f32_16x16x32_bf16 a[252:255], a[164:167], v[176:179], a[252:255]\n"
    "s_waitcnt lgkmcnt(6)\n"
    "v_exp_f32_e32 v191, v191\n"
    "v_mfma_f32_16x16x32_bf16 v[16:19], a[172:175], v[176:179], v[16:19]\n"
    "ds_read_b64_tr_b16 a[190:191], v247 offset:51200\n"
    "v_cvt_pk_bf16_f32 v188, v188, v189\n"
    "v_mfma_f32_16x16x32_bf16 v[16:19], a[168:171], v[172:175], v[16:19]\n"
    "ds_read_b64_tr_b16 a[160:161], v244 offset:20480\n"
    "v_mfma_f32_16x16x32_bf16 v[12:15], a[168:171], v[164:167], v[12:15]\n"
    "v_exp_f32_e32 v196, v196\n"
    "v_mfma_f32_16x16x32_bf16 v[12:15], a[172:175], v[168:171], v[12:15]\n"
    "v_exp_f32_e32 v197, v197\n"
    "v_mfma_f32_16x16x32_bf16 v[8:11], a[172:175], v[160:163], v[8:11]\n"
    "ds_read_b64_tr_b16 a[162:163], v244 offset:22528\n"
    "v_cvt_pk_bf16_f32 v189, v190, v191\n"
    "v_mfma_f32_16x16x32_bf16 v[8:11], a[168:171], v[156:159], v[8:11]\n"
    "v_exp_f32_e32 v198, v198\n"
    "v_mfma_f32_16x16x32_bf16 v[4:7], a[168:171], v[148:151], v[4:7]\n"
    "v_exp_f32_e32 v199, v199\n"
    "v_mfma_f32_16x16x32_bf16 v[4:7], a[172:175], v[152:155], v[4:7]\n"
    "v_cvt_pk_bf16_f32 v196, v196, v197\n"
    "v_mfma_f32_16x16x32_bf16 v[0:3], a[172:175], v[144:147], v[0:3]\n"
    "ds_read_b64_tr_b16 a[164:165], v244 offset:53248\n"
    "v_mfma_f32_16x16x32_bf16 v[0:3], a[168:171], v[140:143], v[0:3]\n"
    "s_waitcnt lgkmcnt(7)\n"
    "v_exp_f32_e32 v204, v204\n"
    "v_mfma_f32_16x16x32_bf16 v[20:23], a[176:179], v[140:143], v[20:23]\n"
    "ds_read_b64_tr_b16 a[168:169], v245 offset:20480\n"
    "v_mfma_f32_16x16x32_bf16 v[20:23], a[180:183], v[144:147], v[20:23]\n"
    "ds_read_b64_tr_b16 a[166:167], v244 offset:55296\n"
    "v_mfma_f32_16x16x32_bf16 v[24:27], a[180:183], v[152:155], v[24:27]\n"
    "v_exp_f32_e32 v205, v205\n"
    "v_mfma_f32_16x16x32_bf16 v[24:27], a[176:179], v[148:151], v[24:27]\n"
    "v_cvt_pk_bf16_f32 v197, v198, v199\n"
    "v_mfma_f32_16x16x32_bf16 v[28:31], a[176:179], v[156:159], v[28:31]\n"
    "ds_read_b64_tr_b16 a[170:171], v245 offset:22528\n"
    "v_mfma_f32_16x16x32_bf16 v[28:31], a[180:183], v[160:163], v[28:31]\n"
    "v_exp_f32_e32 v206, v206\n"
    "v_mfma_f32_16x16x32_bf16 v[32:35], a[180:183], v[168:171], v[32:35]\n"
    "v_exp_f32_e32 v207, v207\n"
    "v_mfma_f32_16x16x32_bf16 v[32:35], a[176:179], v[164:167], v[32:35]\n"
    "v_cvt_pk_bf16_f32 v204, v204, v205\n"
    "v_mfma_f32_16x16x32_bf16 v[36:39], a[176:179], v[172:175], v[36:39]\n"
    "ds_read_b64_tr_b16 a[172:173], v245 offset:53248\n"
    "v_mfma_f32_16x16x32_bf16 v[36:39], a[180:183], v[176:179], v[36:39]\n"
    "s_waitcnt vmcnt(20)\n"
    "ds_read_b64_tr_b16 a[174:175], v245 offset:55296\n"
    "v_smfmac_f32_16x16x64_bf16 v[224:227], a[232:235], a[216:223], v238\n"
    "s_waitcnt lgkmcnt(8)\n"
    "v_mad_u32_u16 v234, v231, s90, v239 op_sel:[0,0,0,0]\n"
    "v_mfma_f32_16x16x32_bf16 v[56:59], a[188:191], v[176:179], v[56:59]\n"
    "s_bitcmp1_b32 s86, 3\n"
    "buffer_load_dwordx4 a[216:219], v234, s[20:23], 0 offen sc1\n"
    "v_mad_u32_u16 v235, v231, s90, v239 op_sel:[1,0,0,0]\n"
    "v_mfma_f32_16x16x32_bf16 v[56:59], a[184:187], v[172:175], v[56:59]\n"
    "s_cselect_b64 exec, -1, 0\n"
    "v_cvt_pk_bf16_f32 v236, v224, v225\n"
    "v_cvt_pk_bf16_f32 v237, v226, v227\n"
    "v_mfma_f32_16x16x32_bf16 v[52:55], a[184:187], v[164:167], v[52:55]\n"
    "s_cselect_b32 s88, 512, 0\n"
    "v_mov_b64_e32 v[224:225], 0\n"
    "v_mov_b64_e32 v[226:227], 0\n"
    "v_mfma_f32_16x16x32_bf16 v[52:55], a[188:191], v[168:171], v[52:55]\n"
    "buffer_store_dwordx2 v[236:237], v252, s[8:11], s87 offen sc1\n"
    "v_mfma_f32_16x16x32_bf16 v[48:51], a[188:191], v[160:163], v[48:51]\n"
    "s_mov_b64 exec, -1\n"
    "ds_read_b64_tr_b16 a[176:177], v246 offset:20480\n"
    "v_mfma_f32_16x16x32_bf16 v[48:51], a[184:187], v[156:159], v[48:51]\n"
    "s_add_u32 s87, s87, s88\n"
    "ds_read_b64_tr_b16 a[178:179], v246 offset:22528\n"
    "v_mfma_f32_16x16x32_bf16 v[44:47], a[184:187], v[148:151], v[44:47]\n"
    "buffer_load_dwordx4 a[220:223], v235, s[20:23], 0 offen sc1\n"
    "v_mfma_f32_16x16x32_bf16 v[44:47], a[188:191], v[152:155], v[44:47]\n"
    "ds_read_b64_tr_b16 a[180:181], v246 offset:53248\n"
    "v_mfma_f32_16x16x32_bf16 v[40:43], a[188:191], v[144:147], v[40:43]\n"
    "v_exp_f32_e32 v212, v212\n"
    "v_mfma_f32_16x16x32_bf16 v[40:43], a[184:187], v[140:143], v[40:43]\n"
    "s_waitcnt lgkmcnt(6)\n"
    "v_exp_f32_e32 v213, v213\n"
    "v_mfma_f32_16x16x32_bf16 v[60:63], a[160:163], v[140:143], v[60:63]\n"
    "ds_read_b64_tr_b16 a[182:183], v246 offset:55296\n"
    "v_cvt_pk_bf16_f32 v205, v206, v207\n"
    "v_mfma_f32_16x16x32_bf16 v[60:63], a[164:167], v[144:147], v[60:63]\n"
    "ds_read_b64_tr_b16 a[184:185], v247 offset:20480\n"
    "v_mfma_f32_16x16x32_bf16 v[64:67], a[164:167], v[152:155], v[64:67]\n"
    "ds_read_b64_tr_b16 a[186:187], v247 offset:22528\n"
    "v_mfma_f32_16x16x32_bf16 v[64:67], a[160:163], v[148:151], v[64:67]\n"
    "v_exp_f32_e32 v214, v214\n"
    "v_mfma_f32_16x16x32_bf16 v[68:71], a[160:163], v[156:159], v[68:71]\n"
    "v_exp_f32_e32 v215, v215\n"
    "v_mfma_f32_16x16x32_bf16 v[68:71], a[164:167], v[160:163], v[68:71]\n"
    "ds_read_b64_tr_b16 a[188:189], v247 offset:53248\n"
    "v_cvt_pk_bf16_f32 v212, v212, v213\n"
    "v_mfma_f32_16x16x32_bf16 v[72:75], a[164:167], v[168:171], v[72:75]\n"
    "ds_read_b64_tr_b16 a[190:191], v247 offset:55296\n"
    "v_cvt_pk_bf16_f32 v213, v214, v215\n"
    "v_mfma_f32_16x16x32_bf16 v[72:75], a[160:163], v[164:167], v[72:75]\n"
    "s_waitcnt vmcnt(30)\n"
    "v_mfma_f32_16x16x32_bf16 v[76:79], a[160:163], v[172:175], v[76:79]\n"
    "s_barrier\n"
    "v_mfma_f32_16x16x32_bf16 v[76:79], a[164:167], v[176:179], v[76:79]\n"
    "s_waitcnt lgkmcnt(8)\n"
    "s_mov_b32 m0, s42\n"
    "v_mfma_f32_16x16x32_bf16 v[96:99], a[172:175], v[176:179], v[96:99]\n"
    "v_exp_f32_e32 v184, v184\n"
    "v_mfma_f32_16x16x32_bf16 v[96:99], a[168:171], v[172:175], v[96:99]\n"
    "v_exp_f32_e32 v185, v185\n"
    "v_mfma_f32_16x16x32_bf16 v[92:95], a[168:171], v[164:167], v[92:95]\n"
    "v_exp_f32_e32 v186, v186\n"
    "v_mfma_f32_16x16x32_bf16 v[92:95], a[172:175], v[168:171], v[92:95]\n"
    "ds_read_b128 a[160:163], v240 offset:0\n"
    "ds_write_addtid_b32 v180 offset:0\n"
    "v_mfma_f32_16x16x32_bf16 v[88:91], a[172:175], v[160:163], v[88:91]\n"
    "v_exp_f32_e32 v187, v187\n"
    "v_mfma_f32_16x16x32_bf16 v[88:91], a[168:171], v[156:159], v[88:91]\n"
    "ds_write_addtid_b32 v181 offset:256\n"
    "v_cvt_pk_bf16_f32 v182, v184, v185\n"
    "v_mfma_f32_16x16x32_bf16 v[84:87], a[168:171], v[148:151], v[84:87]\n"
    "v_exp_f32_e32 v192, v192\n"
    "v_mfma_f32_16x16x32_bf16 v[84:87], a[172:175], v[152:155], v[84:87]\n"
    "v_exp_f32_e32 v193, v193\n"
    "v_mfma_f32_16x16x32_bf16 v[80:83], a[172:175], v[144:147], v[80:83]\n"
    "ds_write_addtid_b32 v188 offset:1024\n"
    "v_cvt_pk_bf16_f32 v183, v186, v187\n"
    "v_mfma_f32_16x16x32_bf16 v[80:83], a[168:171], v[140:143], v[80:83]\n"
    "s_waitcnt vmcnt(20)\n"
    "ds_read_b128 a[164:167], v241 offset:0\n"
    "ds_write_addtid_b32 v189 offset:1280\n"
    "v_smfmac_f32_16x16x64_bf16 v[224:227], a[232:235], a[224:231], v238\n"
    "s_waitcnt lgkmcnt(10)\n"
    "ds_write_addtid_b32 v196 offset:2048\n"
    "v_mad_u32_u16 v234, v232, s90, v239 op_sel:[0,0,0,0]\n"
    "v_mfma_f32_16x16x32_bf16 v[100:103], a[176:179], v[140:143], v[100:103]\n"
    "s_bitcmp1_b32 s86, 4\n"
    "buffer_load_dwordx4 a[224:227], v234, s[20:23], 0 offen sc1\n"
    "v_mad_u32_u16 v235, v232, s90, v239 op_sel:[1,0,0,0]\n"
    "v_mfma_f32_16x16x32_bf16 v[100:103], a[180:183], v[144:147], v[100:103]\n"
    "s_cselect_b64 exec, -1, 0\n"
    "v_cvt_pk_bf16_f32 v236, v224, v225\n"
    "v_cvt_pk_bf16_f32 v237, v226, v227\n"
    "v_mfma_f32_16x16x32_bf16 v[104:107], a[180:183], v[152:155], v[104:107]\n"
    "s_cselect_b32 s88, 512, 0\n"
    "v_mov_b64_e32 v[224:225], 0\n"
    "v_mov_b64_e32 v[226:227], 0\n"
    "v_mfma_f32_16x16x32_bf16 v[104:107], a[176:179], v[148:151], v[104:107]\n"
    "buffer_store_dwordx2 v[236:237], v252, s[8:11], s87 offen sc1\n"
    "v_mfma_f32_16x16x32_bf16 v[108:111], a[176:179], v[156:159], v[108:111]\n"
    "s_mov_b64 exec, -1\n"
    "ds_read_b128 a[168:171], v240 offset:4096\n"
    "ds_write_addtid_b32 v197 offset:2304\n"
    "v_mfma_f32_16x16x32_bf16 v[108:111], a[180:183], v[160:163], v[108:111]\n"
    "s_add_u32 s87, s87, s88\n"
    "v_exp_f32_e32 v194, v194\n"
    "v_mfma_f32_16x16x32_bf16 v[112:115], a[180:183], v[168:171], v[112:115]\n"
    "buffer_load_dwordx4 a[228:231], v235, s[20:23], 0 offen sc1\n"
    "ds_write_addtid_b32 v204 offset:3072\n"
    "v_mfma_f32_16x16x32_bf16 v[112:115], a[176:179], v[164:167], v[112:115]\n"
    "ds_write_addtid_b32 v205 offset:3328\n"
    "v_mfma_f32_16x16x32_bf16 v[116:119], a[176:179], v[172:175], v[116:119]\n"
    "v_exp_f32_e32 v195, v195\n"
    "v_mfma_f32_16x16x32_bf16 v[116:119], a[180:183], v[176:179], v[116:119]\n"
    "s_waitcnt lgkmcnt(11)\n"
    "ds_read_b128 a[172:175], v241 offset:4096\n"
    "v_cvt_pk_bf16_f32 v190, v192, v193\n"
    "v_mfma_f32_16x16x32_bf16 v[136:139], a[188:191], v[176:179], v[136:139]\n"
    "v_exp_f32_e32 v200, v200\n"
    "v_mfma_f32_16x16x32_bf16 v[136:139], a[184:187], v[172:175], v[136:139]\n"
    "v_exp_f32_e32 v201, v201\n"
    "v_mfma_f32_16x16x32_bf16 v[132:135], a[184:187], v[164:167], v[132:135]\n"
    "ds_read_b128 a[176:179], v242 offset:0\n"
    "v_cvt_pk_bf16_f32 v191, v194, v195\n"
    "v_mfma_f32_16x16x32_bf16 v[132:135], a[188:191], v[168:171], v[132:135]\n"
    "v_exp_f32_e32 v202, v202\n"
    "v_mfma_f32_16x16x32_bf16 v[128:131], a[188:191], v[160:163], v[128:131]\n"
    "v_exp_f32_e32 v203, v203\n"
    "v_mfma_f32_16x16x32_bf16 v[128:131], a[184:187], v[156:159], v[128:131]\n"
    "ds_write_addtid_b32 v212 offset:4096\n"
    "v_cvt_pk_bf16_f32 v198, v200, v201\n"
    "v_mfma_f32_16x16x32_bf16 v[124:127], a[184:187], v[148:151], v[124:127]\n"
    "v_exp_f32_e32 v208, v208\n"
    "v_mfma_f32_16x16x32_bf16 v[124:127], a[188:191], v[152:155], v[124:127]\n"
    "ds_read_b128 a[180:183], v243 offset:0\n"
    "ds_write_addtid_b32 v213 offset:4352\n"
    "v_mfma_f32_16x16x32_bf16 v[120:123], a[188:191], v[144:147], v[120:123]\n"
    "v_exp_f32_e32 v209, v209\n"
    "v_mfma_f32_16x16x32_bf16 v[120:123], a[184:187], v[140:143], v[120:123]\n"
    "s_waitcnt lgkmcnt(11)\n"
    "ds_write_addtid_b32 v182 offset:512\n"
    "v_cvt_pk_bf16_f32 v199, v202, v203\n"
    "v_mfma_f32_16x16x32_bf16 v[172:175], a[160:163], a[128:131], v[220:223]\n"
    "ds_write_addtid_b32 v183 offset:768\n"
    "v_mfma_f32_16x16x32_bf16 v[172:175], a[164:167], a[132:135], v[172:175]\n"
    "v_exp_f32_e32 v210, v210\n"
    "v_mfma_f32_16x16x32_bf16 v[164:167], a[164:167], a[100:103], v[220:223]\n"
    "ds_write_addtid_b32 v190 offset:1536\n"
    "v_mfma_f32_16x16x32_bf16 v[164:167], a[160:163], a[96:99], v[164:167]\n"
    "ds_read_b128 a[184:187], v242 offset:4096\n"
    "v_mfma_f32_16x16x32_bf16 v[156:159], a[160:163], a[64:67], v[220:223]\n"
    "v_exp_f32_e32 v211, v211\n"
    "v_mfma_f32_16x16x32_bf16 v[156:159], a[164:167], a[68:71], v[156:159]\n"
    "ds_write_addtid_b32 v191 offset:1792\n"
    "v_cvt_pk_bf16_f32 v206, v208, v209\n"
    "v_mfma_f32_16x16x32_bf16 v[148:151], a[164:167], a[36:39], v[220:223]\n"
    "v_exp_f32_e32 v216, v216\n"
    "v_mfma_f32_16x16x32_bf16 v[148:151], a[160:163], a[32:35], v[148:151]\n"
    "v_exp_f32_e32 v217, v217\n"
    "v_mfma_f32_16x16x32_bf16 v[140:143], a[160:163], a[0:3], v[220:223]\n"
    "ds_write_addtid_b32 v198 offset:2560\n"
    "v_cvt_pk_bf16_f32 v207, v210, v211\n"
    "v_mfma_f32_16x16x32_bf16 v[140:143], a[164:167], a[4:7], v[140:143]\n"
    "s_waitcnt vmcnt(20)\n"
    "v_exp_f32_e32 v218, v218\n"
    "v_smfmac_f32_16x16x64_bf16 v[224:227], a[232:235], a[192:199], v238\n"
    "s_waitcnt lgkmcnt(10)\n"
    "ds_read_b128 a[188:191], v243 offset:4096\n"
    "v_mfma_f32_16x16x32_bf16 v[140:143], a[168:171], a[8:11], v[140:143]\n"
    "s_bitcmp1_b32 s86, 5\n"
    "ds_read_b128 a[160:163], v240 offset:2048\n"
    "v_mfma_f32_16x16x32_bf16 v[140:143], a[172:175], a[12:15], v[140:143]\n"
    "s_cselect_b64 exec, -1, 0\n"
    "v_cvt_pk_bf16_f32 v236, v224, v225\n"
    "v_cvt_pk_bf16_f32 v237, v226, v227\n"
    "v_mfma_f32_16x16x32_bf16 v[148:151], a[172:175], a[44:47], v[148:151]\n"
    ".long 0xbedf00ff\n"
    ".Lat_w6:\n"
    "s_setpc_b64 s[2:3]\n"
    "s_cselect_b32 s88, 512, 0\n"
    "v_mov_b64_e32 v[224:225], 0\n"
    "v_mov_b64_e32 v[226:227], 0\n"
    "v_mfma_f32_16x16x32_bf16 v[148:151], a[168:171], a[40:43], v[148:151]\n"
    "buffer_store_dwordx2 v[236:237], v252, s[8:11], s87 offen sc1\n"
    "v_mfma_f32_16x16x32_bf16 v[156:159], a[168:171], a[72:75], v[156:159]\n"
    "s_mov_b64 exec, -1\n"
    "ds_read2_b32 v[228:229], v251 offset0:10 offset1:12\n"
    "v_mfma_f32_16x16x32_bf16 v[156:159], a[172:175], a[76:79], v[156:159]\n"
    "s_add_u32 s87, s87, s88\n"
    "ds_read2_b32 v[230:231], v251 offset0:14 offset1:16\n"
    "v_mfma_f32_16x16x32_bf16 v[164:167], a[172:175], a[108:111], v[164:167]\n"
    "ds_read_b32 v232, v251 offset:72\n"
    "v_mfma_f32_16x16x32_bf16 v[164:167], a[168:171], a[104:107], v[164:167]\n"
    "v_exp_f32_e32 v219, v219\n"
    "v_mfma_f32_16x16x32_bf16 v[172:175], a[168:171], a[136:139], v[172:175]\n"
    "ds_write_addtid_b32 v199 offset:2816\n"
    "v_cvt_pk_bf16_f32 v214, v216, v217\n"
    "v_mfma_f32_16x16x32_bf16 v[172:175], a[172:175], a[140:143], v[172:175]\n"
    "s_waitcnt lgkmcnt(13)\n"
    "ds_read_b128 a[164:167], v241 offset:2048\n"
    "v_mfma_f32_16x16x32_bf16 v[172:175], a[176:179], a[144:147], v[172:175]\n"
    "ds_write_addtid_b32 v206 offset:3584\n"
    "v_cvt_pk_bf16_f32 v215, v218, v219\n"
    "v_mfma_f32_16x16x32_bf16 v[172:175], a[180:183], a[148:151], v[172:175]\n"
    "ds_read_b128 a[168:171], v240 offset:6144\n"
    "v_mfma_f32_16x16x32_bf16 v[164:167], a[180:183], a[116:119], v[164:167]\n"
    "s_waitcnt lgkmcnt(6)\n"
    "ds_write_addtid_b32 v207 offset:3840\n"
    "v_mad_u32_u16 v234, v228, s90, v239 op_sel:[0,0,0,0]\n"
    "v_mfma_f32_16x16x32_bf16 v[164:167], a[176:179], a[112:115], v[164:167]\n"
    "buffer_load_dwordx4 a[192:195], v234, s[20:23], 0 offen sc1\n"
    "v_mad_u32_u16 v235, v228, s90, v239 op_sel:[1,0,0,0]\n"
    "v_mfma_f32_16x16x32_bf16 v[156:159], a[176:179], a[80:83], v[156:159]\n"
    "ds_write_addtid_b32 v214 offset:4608\n"
    "v_mfma_f32_16x16x32_bf16 v[156:159], a[180:183], a[84:87], v[156:159]\n"
    "ds_write_addtid_b32 v215 offset:4864\n"
    "v_mfma_f32_16x16x32_bf16 v[148:151], a[180:183], a[52:55], v[148:151]\n"
    "buffer_load_dwordx4 a[196:199], v235, s[20:23], 0 offen sc1\n"
    "v_mfma_f32_16x16x32_bf16 v[148:151], a[176:179], a[48:51], v[148:151]\n"
    "v_mfma_f32_16x16x32_bf16 v[140:143], a[176:179], a[16:19], v[140:143]\n"
    "s_waitcnt lgkmcnt(0)\n"
    "v_mfma_f32_16x16x32_bf16 v[140:143], a[180:183], a[20:23], v[140:143]\n"
    "s_barrier\n"
    "v_mfma_f32_16x16x32_bf16 v[140:143], a[184:187], a[24:27], v[140:143]\n"
    "s_add_u32 m0, s43, 16384\n"
    "ds_read_b128 a[172:175], v241 offset:6144\n"
    "v_mfma_f32_16x16x32_bf16 v[140:143], a[188:191], a[28:31], v[140:143]\n"
    "ds_read2st64_b32 v[184:185], v249 offset0:0 offset1:1\n"
    "v_mfma_f32_16x16x32_bf16 v[148:151], a[188:191], a[60:63], v[148:151]\n"
    "buffer_load_dwordx4 v248, s[8:11], s40 offen offset:0 sc1 lds\n"
    "v_mfma_f32_16x16x32_bf16 v[148:151], a[184:187], a[56:59], v[148:151]\n"
    "ds_read_b128 a[176:179], v242 offset:2048\n"
    "v_mfma_f32_16x16x32_bf16 v[156:159], a[184:187], a[88:91], v[156:159]\n"
    "v_mfma_f32_16x16x32_bf16 v[156:159], a[188:191], a[92:95], v[156:159]\n"
    "v_mfma_f32_16x16x32_bf16 v[164:167], a[188:191], a[124:127], v[164:167]\n"
    "buffer_load_dwordx4 v248, s[8:11], s40 offen offset:1024 sc1 lds\n"
    "v_mfma_f32_16x16x32_bf16 v[164:167], a[184:187], a[120:123], v[164:167]\n"
    "ds_read2st64_b32 v[186:187], v249 offset0:2 offset1:3\n"
    "v_mfma_f32_16x16x32_bf16 v[172:175], a[184:187], a[152:155], v[172:175]\n"
    "v_mfma_f32_16x16x32_bf16 v[172:175], a[188:191], a[156:159], v[172:175]\n"
    "ds_read_b128 a[180:183], v243 offset:2048\n"
    "v_mfma_f32_16x16x32_bf16 v[176:179], a[160:163], a[128:131], v[220:223]\n"
    "buffer_load_dwordx4 v248, s[8:11], s40 offen offset:2048 sc1 lds\n"
    "v_mfma_f32_16x16x32_bf16 v[176:179], a[164:167], a[132:135], v[176:179]\n"
    "ds_read2st64_b32 v[192:193], v249 offset0:4 offset1:5\n"
    "v_mfma_f32_16x16x32_bf16 v[168:171], a[164:167], a[100:103], v[220:223]\n"
    "v_exp_f32_e32 v140, v140\n"
    "v_mfma_f32_16x16x32_bf16 v[168:171], a[160:163], a[96:99], v[168:171]\n"
    "ds_read_b128 a[184:187], v242 offset:6144\n"
    "v_mfma_f32_16x16x32_bf16 v[160:163], a[160:163], a[64:67], v[220:223]\n"
    "buffer_load_dwordx4 v248, s[8:11], s40 offen offset:3072 sc1 lds\n"
    "v_mfma_f32_16x16x32_bf16 v[160:163], a[164:167], a[68:71], v[160:163]\n"
    "s_add_u32 m0, s43, 20480\n"
    "ds_read2st64_b32 v[194:195], v249 offset0:6 offset1:7\n"
    "v_mfma_f32_16x16x32_bf16 v[152:155], a[164:167], a[36:39], v[220:223]\n"
    "s_add_u32 s40, s40, 0x8000\n"
    "ds_read2st64_b32 v[200:201], v249 offset0:8 offset1:9\n"
    "v_mfma_f32_16x16x32_bf16 v[152:155], a[160:163], a[32:35], v[152:155]\n"
    "ds_read2st64_b32 v[202:203], v249 offset0:10 offset1:11\n"
    "v_mfma_f32_16x16x32_bf16 v[144:147], a[160:163], a[0:3], v[220:223]\n"
    "buffer_load_dwordx4 v248, s[8:11], s41 offen offset:0 sc1 lds\n"
    "v_mfma_f32_16x16x32_bf16 v[144:147], a[164:167], a[4:7], v[144:147]\n"
    "s_waitcnt vmcnt(17)\n"
    "ds_read_b128 a[188:191], v243 offset:6144\n"
    "v_smfmac_f32_16x16x64_bf16 v[224:227], a[232:235], a[200:207], v238\n"
    "s_waitcnt lgkmcnt(10)\n"
    "v_mad_u32_u16 v234, v229, s90, v239 op_sel:[0,0,0,0]\n"
    "v_mfma_f32_16x16x32_bf16 v[144:147], a[168:171], a[8:11], v[144:147]\n"
    "s_bitcmp1_b32 s86, 6\n"
    "buffer_load_dwordx4 a[200:203], v234, s[20:23], 0 offen sc1\n"
    "v_mad_u32_u16 v235, v229, s90, v239 op_sel:[1,0,0,0]\n"
    "v_mfma_f32_16x16x32_bf16 v[144:147], a[172:175], a[12:15], v[144:147]\n"
    "s_cselect_b64 exec, -1, 0\n"
    "v_cvt_pk_bf16_f32 v236, v224, v225\n"
    "v_cvt_pk_bf16_f32 v237, v226, v227\n"
    "v_mfma_f32_16x16x32_bf16 v[152:155], a[172:175], a[44:47], v[152:155]\n"
    "s_cselect_b32 s88, 512, 0\n"
    "v_mov_b64_e32 v[224:225], 0\n"
    "v_mov_b64_e32 v[226:227], 0\n"
    "v_mfma_f32_16x16x32_bf16 v[152:155], a[168:171], a[40:43], v[152:155]\n"
    "buffer_store_dwordx2 v[236:237], v252, s[8:11], s87 offen sc1\n"
    "v_mfma_f32_16x16x32_bf16 v[160:163], a[168:171], a[72:75], v[160:163]\n"
    "s_mov_b64 exec, -1\n"
    "ds_read_b64_tr_b16 a[160:161], v244 offset:24576\n"
    "v_mfma_f32_16x16x32_bf16 v[160:163], a[172:175], a[76:79], v[160:163]\n"
    "s_add_u32 s87, s87, s88\n"
    "ds_read_b64_tr_b16 a[162:163], v244 offset:26624\n"
    "v_mfma_f32_16x16x32_bf16 v[168:171], a[172:175], a[108:111], v[168:171]\n"
    "buffer_load_dwordx4 v248, s[8:11], s41 offen offset:1024 sc1 lds\n"
    "v_mfma_f32_16x16x32_bf16 v[168:171], a[168:171], a[104:107], v[168:171]\n"
    "ds_read_b64_tr_b16 a[164:165], v244 offset:57344\n"
    "v_mfma_f32_16x16x32_bf16 v[176:179], a[168:171], a[136:139], v[176:179]\n"
    "ds_read2st64_b32 v[208:209], v249 offset0:12 offset1:13\n"
    "v_mfma_f32_16x16x32_bf16 v[176:179], a[172:175], a[140:143], v[176:179]\n"
    "s_waitcnt lgkmcnt(10)\n"
    "ds_read2st64_b32 v[210:211], v249 offset0:14 offset1:15\n"
    "v_mfma_f32_16x16x32_bf16 v[176:179], a[176:179], a[144:147], v[176:179]\n"
    "buffer_load_dwordx4 v248, s[8:11], s41 offen offset:2048 sc1 lds\n"
    "v_mfma_f32_16x16x32_bf16 v[176:179], a[180:183], a[148:151], v[176:179]\n"
    "ds_read_b64_tr_b16 a[166:167], v244 offset:59392\n"
    "v_mfma_f32_16x16x32_bf16 v[168:171], a[180:183], a[116:119], v[168:171]\n"
    "ds_read_b64_tr_b16 a[168:169], v245 offset:24576\n"
    "v_mfma_f32_16x16x32_bf16 v[168:171], a[176:179], a[112:115], v[168:171]\n"
    "v_exp_f32_e32 v141, v141\n"
    "v_mfma_f32_16x16x32_bf16 v[160:163], a[176:179], a[80:83], v[160:163]\n"
    "buffer_load_dwordx4 v248, s[8:11], s41 offen offset:3072 sc1 lds\n"
    "v_mfma_f32_16x16x32_bf16 v[160:163], a[180:183], a[84:87], v[160:163]\n"
    "ds_read_b64_tr_b16 a[170:171], v245 offset:26624\n"
    "v_mfma_f32_16x16x32_bf16 v[152:155], a[180:183], a[52:55], v[152:155]\n"
    "s_add_u32 s41, s41, 0x8000\n"
    "ds_read2st64_b32 v[216:217], v249 offset0:16 offset1:17\n"
    "v_mfma_f32_16x16x32_bf16 v[152:155], a[176:179], a[48:51], v[152:155]\n"
    "buffer_load_dwordx4 a[204:207], v235, s[20:23], 0 offen sc1\n"
    "v_mfma_f32_16x16x32_bf16 v[144:147], a[176:179], a[16:19], v[144:147]\n"
    "ds_read_b64_tr_b16 a[172:173], v245 offset:57344\n"
    "v_mfma_f32_16x16x32_bf16 v[144:147], a[180:183], a[20:23], v[144:147]\n"
    "s_waitcnt lgkmcnt(10)\n"
    "ds_read2st64_b32 v[218:219], v249 offset0:18 offset1:19\n"
    "v_mfma_f32_16x16x32_bf16 v[144:147], a[184:187], a[24:27], v[144:147]\n"
    "ds_read_b64_tr_b16 a[176:177], v246 offset:24576\n"
    "v_mfma_f32_16x16x32_bf16 v[144:147], a[188:191], a[28:31], v[144:147]\n"
    "ds_read_b64_tr_b16 a[174:175], v245 offset:59392\n"
    "v_mfma_f32_16x16x32_bf16 v[152:155], a[188:191], a[60:63], v[152:155]\n"
    "v_exp_f32_e32 v142, v142\n"
    "v_mfma_f32_16x16x32_bf16 v[152:155], a[184:187], a[56:59], v[152:155]\n"
    "v_exp_f32_e32 v143, v143\n"
    "v_mfma_f32_16x16x32_bf16 v[160:163], a[184:187], a[88:91], v[160:163]\n"
    "ds_read_b64_tr_b16 a[178:179], v246 offset:26624\n"
    "v_cvt_pk_bf16_f32 v140, v140, v141\n"
    "v_mfma_f32_16x16x32_bf16 v[160:163], a[188:191], a[92:95], v[160:163]\n"
    "v_exp_f32_e32 v148, v148\n"
    "v_mfma_f32_16x16x32_bf16 v[168:171], a[188:191], a[124:127], v[168:171]\n"
    "v_exp_f32_e32 v149, v149\n"
    "v_mfma_f32_16x16x32_bf16 v[168:171], a[184:187], a[120:123], v[168:171]\n"
    "v_cvt_pk_bf16_f32 v141, v142, v143\n"
    "v_mfma_f32_16x16x32_bf16 v[176:179], a[184:187], a[152:155], v[176:179]\n"
    "ds_read_b64_tr_b16 a[180:181], v246 offset:57344\n"
    "v_mfma_f32_16x16x32_bf16 v[176:179], a[188:191], a[156:159], v[176:179]\n"
    "s_waitcnt vmcnt(20)\n"
    "ds_read_b64_tr_b16 a[182:183], v246 offset:59392\n"
    "v_smfmac_f32_16x16x64_bf16 v[224:227], a[232:235], a[208:215], v238\n"
    "s_waitcnt lgkmcnt(5)\n"
    "v_mad_u32_u16 v234, v230, s90, v239 op_sel:[0,0,0,0]\n"
    "v_mfma_f32_16x16x32_bf16 a[236:239], a[160:163], v[180:183], a[236:239]\n"
    "s_bitcmp1_b32 s86, 7\n"
    "buffer_load_dwordx4 a[208:211], v234, s[20:23], 0 offen sc1\n"
    "v_mad_u32_u16 v235, v230, s90, v239 op_sel:[1,0,0,0]\n"
    "v_mfma_f32_16x16x32_bf16 a[236:239], a[164:167], v[184:187], a[236:239]\n"
    "s_cselect_b64 exec, -1, 0\n"
    "v_cvt_pk_bf16_f32 v236, v224, v225\n"
    "v_cvt_pk_bf16_f32 v237, v226, v227\n"
    "v_mfma_f32_16x16x32_bf16 a[240:243], a[164:167], v[192:195], a[240:243]\n"
    "s_cselect_b32 s88, 512, 0\n"
    "v_mov_b64_e32 v[224:225], 0\n"
    "v_mov_b64_e32 v[226:227], 0\n"
    "v_mfma_f32_16x16x32_bf16 a[240:243], a[160:163], v[188:191], a[240:243]\n"
    "buffer_store_dwordx2 v[236:237], v252, s[8:11], s87 offen sc1\n"
    "v_mfma_f32_16x16x32_bf16 a[244:247], a[160:163], v[196:199], a[244:247]\n"
    "s_mov_b64 exec, -1\n"
    "ds_read_b64_tr_b16 a[184:185], v247 offset:24576\n"
    "v_mfma_f32_16x16x32_bf16 a[244:247], a[164:167], v[200:203], a[244:247]\n"
    "s_add_u32 s87, s87, s88\n"
    "ds_read_b64_tr_b16 a[186:187], v247 offset:26624\n"
    "v_mfma_f32_16x16x32_bf16 a[248:251], a[164:167], v[208:211], a[248:251]\n"
    "buffer_load_dwordx4 a[212:215], v235, s[20:23], 0 offen sc1\n"
    "v_mfma_f32_16x16x32_bf16 a[248:251], a[160:163], v[204:207], a[248:251]\n"
    "ds_read_b64_tr_b16 a[188:189], v247 offset:57344\n"
    "v_mfma_f32_16x16x32_bf16 a[252:255], a[160:163], v[212:215], a[252:255]\n"
    "v_exp_f32_e32 v150, v150\n"
    "v_mfma_f32_16x16x32_bf16 a[252:255], a[164:167], v[216:219], a[252:255]\n"
    "s_waitcnt lgkmcnt(6)\n"
    "v_exp_f32_e32 v151, v151\n"
    "v_mfma_f32_16x16x32_bf16 v[16:19], a[172:175], v[216:219], v[16:19]\n"
    "ds_read_b64_tr_b16 a[190:191], v247 offset:59392\n"
    "v_cvt_pk_bf16_f32 v148, v148, v149\n"
    "v_mfma_f32_16x16x32_bf16 v[16:19], a[168:171], v[212:215], v[16:19]\n"
    "ds_read_b64_tr_b16 a[160:161], v244 offset:28672\n"
    "v_mfma_f32_16x16x32_bf16 v[12:15], a[168:171], v[204:207], v[12:15]\n"
    "v_exp_f32_e32 v156, v156\n"
    "v_mfma_f32_16x16x32_bf16 v[12:15], a[172:175], v[208:211], v[12:15]\n"
    "v_exp_f32_e32 v157, v157\n"
    "v_mfma_f32_16x16x32_bf16 v[8:11], a[172:175], v[200:203], v[8:11]\n"
    "ds_read_b64_tr_b16 a[162:163], v244 offset:30720\n"
    "v_cvt_pk_bf16_f32 v149, v150, v151\n"
    "v_mfma_f32_16x16x32_bf16 v[8:11], a[168:171], v[196:199], v[8:11]\n"
    "v_exp_f32_e32 v158, v158\n"
    "v_mfma_f32_16x16x32_bf16 v[4:7], a[168:171], v[188:191], v[4:7]\n"
    "v_exp_f32_e32 v159, v159\n"
    "v_mfma_f32_16x16x32_bf16 v[4:7], a[172:175], v[192:195], v[4:7]\n"
    "v_cvt_pk_bf16_f32 v156, v156, v157\n"
    "v_mfma_f32_16x16x32_bf16 v[0:3], a[172:175], v[184:187], v[0:3]\n"
    "ds_read_b64_tr_b16 a[164:165], v244 offset:61440\n"
    "v_mfma_f32_16x16x32_bf16 v[0:3], a[168:171], v[180:183], v[0:3]\n"
    "s_waitcnt lgkmcnt(7)\n"
    "v_exp_f32_e32 v164, v164\n"
    "v_mfma_f32_16x16x32_bf16 v[20:23], a[176:179], v[180:183], v[20:23]\n"
    "ds_read_b64_tr_b16 a[168:169], v245 offset:28672\n"
    "v_mfma_f32_16x16x32_bf16 v[20:23], a[180:183], v[184:187], v[20:23]\n"
    "ds_read_b64_tr_b16 a[166:167], v244 offset:63488\n"
    "v_mfma_f32_16x16x32_bf16 v[24:27], a[180:183], v[192:195], v[24:27]\n"
    "v_exp_f32_e32 v165, v165\n"
    "v_mfma_f32_16x16x32_bf16 v[24:27], a[176:179], v[188:191], v[24:27]\n"
    "v_cvt_pk_bf16_f32 v157, v158, v159\n"
    "v_mfma_f32_16x16x32_bf16 v[28:31], a[176:179], v[196:199], v[28:31]\n"
    "ds_read_b64_tr_b16 a[170:171], v245 offset:30720\n"
    "v_mfma_f32_16x16x32_bf16 v[28:31], a[180:183], v[200:203], v[28:31]\n"
    "v_exp_f32_e32 v166, v166\n"
    "v_mfma_f32_16x16x32_bf16 v[32:35], a[180:183], v[208:211], v[32:35]\n"
    "v_exp_f32_e32 v167, v167\n"
    "v_mfma_f32_16x16x32_bf16 v[32:35], a[176:179], v[204:207], v[32:35]\n"
    "v_cvt_pk_bf16_f32 v164, v164, v165\n"
    "v_mfma_f32_16x16x32_bf16 v[36:39], a[176:179], v[212:215], v[36:39]\n"
    "ds_read_b64_tr_b16 a[172:173], v245 offset:61440\n"
    "v_mfma_f32_16x16x32_bf16 v[36:39], a[180:183], v[216:219], v[36:39]\n"
    "s_waitcnt vmcnt(20)\n"
    "ds_read_b64_tr_b16 a[174:175], v245 offset:63488\n"
    "v_smfmac_f32_16x16x64_bf16 v[224:227], a[232:235], a[216:223], v238\n"
    "s_waitcnt lgkmcnt(8)\n"
    "v_mad_u32_u16 v234, v231, s90, v239 op_sel:[0,0,0,0]\n"
    "v_mfma_f32_16x16x32_bf16 v[56:59], a[188:191], v[216:219], v[56:59]\n"
    "s_bitcmp1_b32 s86, 8\n"
    "buffer_load_dwordx4 a[216:219], v234, s[20:23], 0 offen sc1\n"
    "v_mad_u32_u16 v235, v231, s90, v239 op_sel:[1,0,0,0]\n"
    "v_mfma_f32_16x16x32_bf16 v[56:59], a[184:187], v[212:215], v[56:59]\n"
    "s_cselect_b64 exec, -1, 0\n"
    "v_cvt_pk_bf16_f32 v236, v224, v225\n"
    "v_cvt_pk_bf16_f32 v237, v226, v227\n"
    "v_mfma_f32_16x16x32_bf16 v[52:55], a[184:187], v[204:207], v[52:55]\n"
    "s_cselect_b32 s88, 512, 0\n"
    "v_mov_b64_e32 v[224:225], 0\n"
    "v_mov_b64_e32 v[226:227], 0\n"
    "v_mfma_f32_16x16x32_bf16 v[52:55], a[188:191], v[208:211], v[52:55]\n"
    "buffer_store_dwordx2 v[236:237], v252, s[8:11], s87 offen sc1\n"
    "v_mfma_f32_16x16x32_bf16 v[48:51], a[188:191], v[200:203], v[48:51]\n"
    "s_mov_b64 exec, -1\n"
    "ds_read_b64_tr_b16 a[176:177], v246 offset:28672\n"
    "v_mfma_f32_16x16x32_bf16 v[48:51], a[184:187], v[196:199], v[48:51]\n"
    "s_add_u32 s87, s87, s88\n"
    "ds_read_b64_tr_b16 a[178:179], v246 offset:30720\n"
    "v_mfma_f32_16x16x32_bf16 v[44:47], a[184:187], v[188:191], v[44:47]\n"
    "buffer_load_dwordx4 a[220:223], v235, s[20:23], 0 offen sc1\n"
    "v_mfma_f32_16x16x32_bf16 v[44:47], a[188:191], v[192:195], v[44:47]\n"
    "ds_read_b64_tr_b16 a[180:181], v246 offset:61440\n"
    "v_mfma_f32_16x16x32_bf16 v[40:43], a[188:191], v[184:187], v[40:43]\n"
    "v_exp_f32_e32 v172, v172\n"
    "v_mfma_f32_16x16x32_bf16 v[40:43], a[184:187], v[180:183], v[40:43]\n"
    "s_waitcnt lgkmcnt(6)\n"
    "v_exp_f32_e32 v173, v173\n"
    "v_mfma_f32_16x16x32_bf16 v[60:63], a[160:163], v[180:183], v[60:63]\n"
    "ds_read_b64_tr_b16 a[182:183], v246 offset:63488\n"
    "v_cvt_pk_bf16_f32 v165, v166, v167\n"
    "v_mfma_f32_16x16x32_bf16 v[60:63], a[164:167], v[184:187], v[60:63]\n"
    "ds_read_b64_tr_b16 a[184:185], v247 offset:28672\n"
    "v_mfma_f32_16x16x32_bf16 v[64:67], a[164:167], v[192:195], v[64:67]\n"
    "ds_read_b64_tr_b16 a[186:187], v247 offset:30720\n"
    "v_mfma_f32_16x16x32_bf16 v[64:67], a[160:163], v[188:191], v[64:67]\n"
    "v_exp_f32_e32 v174, v174\n"
    "v_mfma_f32_16x16x32_bf16 v[68:71], a[160:163], v[196:199], v[68:71]\n"
    "v_exp_f32_e32 v175, v175\n"
    "v_mfma_f32_16x16x32_bf16 v[68:71], a[164:167], v[200:203], v[68:71]\n"
    "ds_read_b64_tr_b16 a[188:189], v247 offset:61440\n"
    "v_cvt_pk_bf16_f32 v172, v172, v173\n"
    "v_mfma_f32_16x16x32_bf16 v[72:75], a[164:167], v[208:211], v[72:75]\n"
    "ds_read_b64_tr_b16 a[190:191], v247 offset:63488\n"
    "v_cvt_pk_bf16_f32 v173, v174, v175\n"
    "v_mfma_f32_16x16x32_bf16 v[72:75], a[160:163], v[204:207], v[72:75]\n"
    "s_waitcnt vmcnt(30)\n"
    "v_mfma_f32_16x16x32_bf16 v[76:79], a[160:163], v[212:215], v[76:79]\n"
    "s_barrier\n"
    "v_mfma_f32_16x16x32_bf16 v[76:79], a[164:167], v[216:219], v[76:79]\n"
    "s_waitcnt lgkmcnt(8)\n"
    "s_mov_b32 m0, s42\n"
    "v_mfma_f32_16x16x32_bf16 v[96:99], a[172:175], v[216:219], v[96:99]\n"
    "v_exp_f32_e32 v144, v144\n"
    "v_mfma_f32_16x16x32_bf16 v[96:99], a[168:171], v[212:215], v[96:99]\n"
    "v_exp_f32_e32 v145, v145\n"
    "v_mfma_f32_16x16x32_bf16 v[92:95], a[168:171], v[204:207], v[92:95]\n"
    "v_exp_f32_e32 v146, v146\n"
    "v_mfma_f32_16x16x32_bf16 v[92:95], a[172:175], v[208:211], v[92:95]\n"
    "ds_read_b128 a[160:163], v240 offset:8192\n"
    "ds_write_addtid_b32 v140 offset:0\n"
    "v_mfma_f32_16x16x32_bf16 v[88:91], a[172:175], v[200:203], v[88:91]\n"
    "v_exp_f32_e32 v147, v147\n"
    "v_mfma_f32_16x16x32_bf16 v[88:91], a[168:171], v[196:199], v[88:91]\n"
    "ds_write_addtid_b32 v141 offset:256\n"
    "v_cvt_pk_bf16_f32 v142, v144, v145\n"
    "v_mfma_f32_16x16x32_bf16 v[84:87], a[168:171], v[188:191], v[84:87]\n"
    "v_exp_f32_e32 v152, v152\n"
    "v_mfma_f32_16x16x32_bf16 v[84:87], a[172:175], v[192:195], v[84:87]\n"
    "v_exp_f32_e32 v153, v153\n"
    "v_mfma_f32_16x16x32_bf16 v[80:83], a[172:175], v[184:187], v[80:83]\n"
    "ds_write_addtid_b32 v148 offset:1024\n"
    "v_cvt_pk_bf16_f32 v143, v146, v147\n"
    "v_mfma_f32_16x16x32_bf16 v[80:83], a[168:171], v[180:183], v[80:83]\n"
    "s_waitcnt vmcnt(20)\n"
    "ds_read_b128 a[164:167], v241 offset:8192\n"
    "ds_write_addtid_b32 v149 offset:1280\n"
    "v_smfmac_f32_16x16x64_bf16 v[224:227], a[232:235], a[224:231], v238\n"
    "s_waitcnt lgkmcnt(10)\n"
    "ds_write_addtid_b32 v156 offset:2048\n"
    "v_mad_u32_u16 v234, v232, s90, v239 op_sel:[0,0,0,0]\n"
    "v_mfma_f32_16x16x32_bf16 v[100:103], a[176:179], v[180:183], v[100:103]\n"
    "s_bitcmp1_b32 s86, 9\n"
    "buffer_load_dwordx4 a[224:227], v234, s[20:23], 0 offen sc1\n"
    "v_mad_u32_u16 v235, v232, s90, v239 op_sel:[1,0,0,0]\n"
    "v_mfma_f32_16x16x32_bf16 v[100:103], a[180:183], v[184:187], v[100:103]\n"
    "s_cselect_b64 exec, -1, 0\n"
    "v_cvt_pk_bf16_f32 v236, v224, v225\n"
    "v_cvt_pk_bf16_f32 v237, v226, v227\n"
    "v_mfma_f32_16x16x32_bf16 v[104:107], a[180:183], v[192:195], v[104:107]\n"
    "s_cselect_b32 s88, 512, 0\n"
    "v_mov_b64_e32 v[224:225], 0\n"
    "v_mov_b64_e32 v[226:227], 0\n"
    "v_mfma_f32_16x16x32_bf16 v[104:107], a[176:179], v[188:191], v[104:107]\n"
    "buffer_store_dwordx2 v[236:237], v252, s[8:11], s87 offen sc1\n"
    "v_mfma_f32_16x16x32_bf16 v[108:111], a[176:179], v[196:199], v[108:111]\n"
    "s_mov_b64 exec, -1\n"
    "ds_read_b128 a[168:171], v240 offset:12288\n"
    "ds_write_addtid_b32 v157 offset:2304\n"
    "v_mfma_f32_16x16x32_bf16 v[108:111], a[180:183], v[200:203], v[108:111]\n"
    "s_add_u32 s87, s87, s88\n"
    "v_exp_f32_e32 v154, v154\n"
    "v_mfma_f32_16x16x32_bf16 v[112:115], a[180:183], v[208:211], v[112:115]\n"
    "buffer_load_dwordx4 a[228:231], v235, s[20:23], 0 offen sc1\n"
    "ds_write_addtid_b32 v164 offset:3072\n"
    "v_mfma_f32_16x16x32_bf16 v[112:115], a[176:179], v[204:207], v[112:115]\n"
    "ds_write_addtid_b32 v165 offset:3328\n"
    "v_mfma_f32_16x16x32_bf16 v[116:119], a[176:179], v[212:215], v[116:119]\n"
    "v_exp_f32_e32 v155, v155\n"
    "v_mfma_f32_16x16x32_bf16 v[116:119], a[180:183], v[216:219], v[116:119]\n"
    "s_waitcnt lgkmcnt(11)\n"
    "ds_read_b128 a[172:175], v241 offset:12288\n"
    "v_cvt_pk_bf16_f32 v150, v152, v153\n"
    "v_mfma_f32_16x16x32_bf16 v[136:139], a[188:191], v[216:219], v[136:139]\n"
    "v_exp_f32_e32 v160, v160\n"
    "v_mfma_f32_16x16x32_bf16 v[136:139], a[184:187], v[212:215], v[136:139]\n"
    "v_exp_f32_e32 v161, v161\n"
    "v_mfma_f32_16x16x32_bf16 v[132:135], a[184:187], v[204:207], v[132:135]\n"
    "ds_read_b128 a[176:179], v242 offset:8192\n"
    "v_cvt_pk_bf16_f32 v151, v154, v155\n"
    "v_mfma_f32_16x16x32_bf16 v[132:135], a[188:191], v[208:211], v[132:135]\n"
    "v_exp_f32_e32 v162, v162\n"
    "v_mfma_f32_16x16x32_bf16 v[128:131], a[188:191], v[200:203], v[128:131]\n"
    "v_exp_f32_e32 v163, v163\n"
    "v_mfma_f32_16x16x32_bf16 v[128:131], a[184:187], v[196:199], v[128:131]\n"
    "ds_write_addtid_b32 v172 offset:4096\n"
    "v_cvt_pk_bf16_f32 v158, v160, v161\n"
    "v_mfma_f32_16x16x32_bf16 v[124:127], a[184:187], v[188:191], v[124:127]\n"
    "v_exp_f32_e32 v168, v168\n"
    "v_mfma_f32_16x16x32_bf16 v[124:127], a[188:191], v[192:195], v[124:127]\n"
    "ds_read_b128 a[180:183], v243 offset:8192\n"
    "ds_write_addtid_b32 v173 offset:4352\n"
    "v_mfma_f32_16x16x32_bf16 v[120:123], a[188:191], v[184:187], v[120:123]\n"
    "v_exp_f32_e32 v169, v169\n"
    "v_mfma_f32_16x16x32_bf16 v[120:123], a[184:187], v[180:183], v[120:123]\n"
    "s_waitcnt lgkmcnt(11)\n"
    "ds_write_addtid_b32 v142 offset:512\n"
    "v_cvt_pk_bf16_f32 v159, v162, v163\n"
    "v_mfma_f32_16x16x32_bf16 v[212:215], a[160:163], a[128:131], v[220:223]\n"
    "ds_write_addtid_b32 v143 offset:768\n"
    "v_mfma_f32_16x16x32_bf16 v[212:215], a[164:167], a[132:135], v[212:215]\n"
    "v_exp_f32_e32 v170, v170\n"
    "v_mfma_f32_16x16x32_bf16 v[204:207], a[164:167], a[100:103], v[220:223]\n"
    "ds_write_addtid_b32 v150 offset:1536\n"
    "v_mfma_f32_16x16x32_bf16 v[204:207], a[160:163], a[96:99], v[204:207]\n"
    "ds_read_b128 a[184:187], v242 offset:12288\n"
    "v_mfma_f32_16x16x32_bf16 v[196:199], a[160:163], a[64:67], v[220:223]\n"
    "v_exp_f32_e32 v171, v171\n"
    "v_mfma_f32_16x16x32_bf16 v[196:199], a[164:167], a[68:71], v[196:199]\n"
    "ds_write_addtid_b32 v151 offset:1792\n"
    "v_cvt_pk_bf16_f32 v166, v168, v169\n"
    "v_mfma_f32_16x16x32_bf16 v[188:191], a[164:167], a[36:39], v[220:223]\n"
    "v_exp_f32_e32 v176, v176\n"
    "v_mfma_f32_16x16x32_bf16 v[188:191], a[160:163], a[32:35], v[188:191]\n"
    "v_exp_f32_e32 v177, v177\n"
    "v_mfma_f32_16x16x32_bf16 v[180:183], a[160:163], a[0:3], v[220:223]\n"
    "ds_write_addtid_b32 v158 offset:2560\n"
    "v_cvt_pk_bf16_f32 v167, v170, v171\n"
    "v_mfma_f32_16x16x32_bf16 v[180:183], a[164:167], a[4:7], v[180:183]\n"
    "s_waitcnt vmcnt(20)\n"
    "v_exp_f32_e32 v178, v178\n"
    "v_smfmac_f32_16x16x64_bf16 v[224:227], a[232:235], a[192:199], v238\n"
    "s_waitcnt lgkmcnt(10)\n"
    "ds_read_b128 a[188:191], v243 offset:12288\n"
    "v_mfma_f32_16x16x32_bf16 v[180:183], a[168:171], a[8:11], v[180:183]\n"
    "s_bitcmp1_b32 s86, 10\n"
    "ds_read_b128 a[160:163], v240 offset:10240\n"
    "v_mfma_f32_16x16x32_bf16 v[180:183], a[172:175], a[12:15], v[180:183]\n"
    "s_cselect_b64 exec, -1, 0\n"
    "v_cvt_pk_bf16_f32 v236, v224, v225\n"
    "v_cvt_pk_bf16_f32 v237, v226, v227\n"
    "v_mfma_f32_16x16x32_bf16 v[188:191], a[172:175], a[44:47], v[188:191]\n"
    "s_cselect_b32 s88, 512, 0\n"
    "v_mov_b64_e32 v[224:225], 0\n"
    "v_mov_b64_e32 v[226:227], 0\n"
    "v_mfma_f32_16x16x32_bf16 v[188:191], a[168:171], a[40:43], v[188:191]\n"
    "buffer_store_dwordx2 v[236:237], v252, s[8:11], s87 offen sc1\n"
    "v_mfma_f32_16x16x32_bf16 v[196:199], a[168:171], a[72:75], v[196:199]\n"
    "s_mov_b64 exec, -1\n"
    "ds_read2_b32 v[228:229], v251 offset0:20 offset1:22\n"
    "v_mfma_f32_16x16x32_bf16 v[196:199], a[172:175], a[76:79], v[196:199]\n"
    "s_add_u32 s87, s87, s88\n"
    "ds_read2_b32 v[230:231], v251 offset0:24 offset1:26\n"
    "v_mfma_f32_16x16x32_bf16 v[204:207], a[172:175], a[108:111], v[204:207]\n"
    "ds_read_b32 v232, v251 offset:112\n"
    "v_mfma_f32_16x16x32_bf16 v[204:207], a[168:171], a[104:107], v[204:207]\n"
    "v_exp_f32_e32 v179, v179\n"
    "v_mfma_f32_16x16x32_bf16 v[212:215], a[168:171], a[136:139], v[212:215]\n"
    "ds_write_addtid_b32 v159 offset:2816\n"
    "v_cvt_pk_bf16_f32 v174, v176, v177\n"
    "v_mfma_f32_16x16x32_bf16 v[212:215], a[172:175], a[140:143], v[212:215]\n"
    "s_waitcnt lgkmcnt(13)\n"
    "ds_read_b128 a[164:167], v241 offset:10240\n"
    "v_mfma_f32_16x16x32_bf16 v[212:215], a[176:179], a[144:147], v[212:215]\n"
    "ds_write_addtid_b32 v166 offset:3584\n"
    "v_cvt_pk_bf16_f32 v175, v178, v179\n"
    "v_mfma_f32_16x16x32_bf16 v[212:215], a[180:183], a[148:151], v[212:215]\n"
    "ds_read_b128 a[168:171], v240 offset:14336\n"
    "v_mfma_f32_16x16x32_bf16 v[204:207], a[180:183], a[116:119], v[204:207]\n"
    "s_waitcnt lgkmcnt(6)\n"
    "ds_write_addtid_b32 v167 offset:3840\n"
    "v_mad_u32_u16 v234, v228, s90, v239 op_sel:[0,0,0,0]\n"
    "v_mfma_f32_16x16x32_bf16 v[204:207], a[176:179], a[112:115], v[204:207]\n"
    "buffer_load_dwordx4 a[192:195], v234, s[20:23], 0 offen sc1\n"
    "v_mad_u32_u16 v235, v228, s90, v239 op_sel:[1,0,0,0]\n"
    "v_mfma_f32_16x16x32_bf16 v[196:199], a[176:179], a[80:83], v[196:199]\n"
    "ds_write_addtid_b32 v174 offset:4608\n"
    "v_mfma_f32_16x16x32_bf16 v[196:199], a[180:183], a[84:87], v[196:199]\n"
    "ds_write_addtid_b32 v175 offset:4864\n"
    "v_mfma_f32_16x16x32_bf16 v[188:191], a[180:183], a[52:55], v[188:191]\n"
    "buffer_load_dwordx4 a[196:199], v235, s[20:23], 0 offen sc1\n"
    "v_mfma_f32_16x16x32_bf16 v[188:191], a[176:179], a[48:51], v[188:191]\n"
    "v_mfma_f32_16x16x32_bf16 v[180:183], a[176:179], a[16:19], v[180:183]\n"
    "s_waitcnt lgkmcnt(0)\n"
    "v_mfma_f32_16x16x32_bf16 v[180:183], a[180:183], a[20:23], v[180:183]\n"
    "s_barrier\n"
    "v_mfma_f32_16x16x32_bf16 v[180:183], a[184:187], a[24:27], v[180:183]\n"
    "s_add_u32 m0, s43, 24576\n"
    "ds_read_b128 a[172:175], v241 offset:14336\n"
    "v_mfma_f32_16x16x32_bf16 v[180:183], a[188:191], a[28:31], v[180:183]\n"
    "ds_read2st64_b32 v[144:145], v249 offset0:0 offset1:1\n"
    "v_mfma_f32_16x16x32_bf16 v[188:191], a[188:191], a[60:63], v[188:191]\n"
    "buffer_load_dwordx4 v248, s[8:11], s40 offen offset:0 sc1 lds\n"
    "v_mfma_f32_16x16x32_bf16 v[188:191], a[184:187], a[56:59], v[188:191]\n"
    "ds_read_b128 a[176:179], v242 offset:10240\n"
    "v_mfma_f32_16x16x32_bf16 v[196:199], a[184:187], a[88:91], v[196:199]\n"
    "v_mfma_f32_16x16x32_bf16 v[196:199], a[188:191], a[92:95], v[196:199]\n"
    "v_mfma_f32_16x16x32_bf16 v[204:207], a[188:191], a[124:127], v[204:207]\n"
    "buffer_load_dwordx4 v248, s[8:11], s40 offen offset:1024 sc1 lds\n"
    "v_mfma_f32_16x16x32_bf16 v[204:207], a[184:187], a[120:123], v[204:207]\n"
    "ds_read2st64_b32 v[146:147], v249 offset0:2 offset1:3\n"
    "v_mfma_f32_16x16x32_bf16 v[212:215], a[184:187], a[152:155], v[212:215]\n"
    "v_mfma_f32_16x16x32_bf16 v[212:215], a[188:191], a[156:159], v[212:215]\n"
    "ds_read_b128 a[180:183], v243 offset:10240\n"
    "v_mfma_f32_16x16x32_bf16 v[216:219], a[160:163], a[128:131], v[220:223]\n"
    "buffer_load_dwordx4 v248, s[8:11], s40 offen offset:2048 sc1 lds\n"
    "v_mfma_f32_16x16x32_bf16 v[216:219], a[164:167], a[132:135], v[216:219]\n"
    "ds_read2st64_b32 v[152:153], v249 offset0:4 offset1:5\n"
    "v_mfma_f32_16x16x32_bf16 v[208:211], a[164:167], a[100:103], v[220:223]\n"
    "v_exp_f32_e32 v180, v180\n"
    "v_mfma_f32_16x16x32_bf16 v[208:211], a[160:163], a[96:99], v[208:211]\n"
    "ds_read_b128 a[184:187], v242 offset:14336\n"
    "v_mfma_f32_16x16x32_bf16 v[200:203], a[160:163], a[64:67], v[220:223]\n"
    "buffer_load_dwordx4 v248, s[8:11], s40 offen offset:3072 sc1 lds\n"
    "v_mfma_f32_16x16x32_bf16 v[200:203], a[164:167], a[68:71], v[200:203]\n"
    "s_add_u32 m0, s43, 28672\n"
    "ds_read2st64_b32 v[154:155], v249 offset0:6 offset1:7\n"
    "v_mfma_f32_16x16x32_bf16 v[192:195], a[164:167], a[36:39], v[220:223]\n"
    "s_add_u32 s40, s40, 0x8000\n"
    "ds_read2st64_b32 v[160:161], v249 offset0:8 offset1:9\n"
    "v_mfma_f32_16x16x32_bf16 v[192:195], a[160:163], a[32:35], v[192:195]\n"
    "ds_read2st64_b32 v[162:163], v249 offset0:10 offset1:11\n"
    "v_mfma_f32_16x16x32_bf16 v[184:187], a[160:163], a[0:3], v[220:223]\n"
    "buffer_load_dwordx4 v248, s[8:11], s41 offen offset:0 sc1 lds\n"
    "v_mfma_f32_16x16x32_bf16 v[184:187], a[164:167], a[4:7], v[184:187]\n"
    "s_waitcnt vmcnt(17)\n"
    "ds_read_b128 a[188:191], v243 offset:14336\n"
    "v_smfmac_f32_16x16x64_bf16 v[224:227], a[232:235], a[200:207], v238\n"
    "s_waitcnt lgkmcnt(10)\n"
    "v_mad_u32_u16 v234, v229, s90, v239 op_sel:[0,0,0,0]\n"
    "v_mfma_f32_16x16x32_bf16 v[184:187], a[168:171], a[8:11], v[184:187]\n"
    "s_bitcmp1_b32 s86, 11\n"
    "buffer_load_dwordx4 a[200:203], v234, s[20:23], 0 offen sc1\n"
    "v_mad_u32_u16 v235, v229, s90, v239 op_sel:[1,0,0,0]\n"
    "v_mfma_f32_16x16x32_bf16 v[184:187], a[172:175], a[12:15], v[184:187]\n"
    "s_cselect_b64 exec, -1, 0\n"
    "v_cvt_pk_bf16_f32 v236, v224, v225\n"
    "v_cvt_pk_bf16_f32 v237, v226, v227\n"
    "v_mfma_f32_16x16x32_bf16 v[192:195], a[172:175], a[44:47], v[192:195]\n"
    "s_cselect_b32 s88, 512, 0\n"
    "v_mov_b64_e32 v[224:225], 0\n"
    "v_mov_b64_e32 v[226:227], 0\n"
    "v_mfma_f32_16x16x32_bf16 v[192:195], a[168:171], a[40:43], v[192:195]\n"
    "buffer_store_dwordx2 v[236:237], v252, s[8:11], s87 offen sc1\n"
    "v_mfma_f32_16x16x32_bf16 v[200:203], a[168:171], a[72:75], v[200:203]\n"
    "s_mov_b64 exec, -1\n"
    "ds_read_b64_tr_b16 a[160:161], v244 offset:0\n"
    "v_mfma_f32_16x16x32_bf16 v[200:203], a[172:175], a[76:79], v[200:203]\n"
    "s_add_u32 s87, s87, s88\n"
    "ds_read_b64_tr_b16 a[162:163], v244 offset:2048\n"
    "v_mfma_f32_16x16x32_bf16 v[208:211], a[172:175], a[108:111], v[208:211]\n"
    "buffer_load_dwordx4 v248, s[8:11], s41 offen offset:1024 sc1 lds\n"
    "v_mfma_f32_16x16x32_bf16 v[208:211], a[168:171], a[104:107], v[208:211]\n"
    "ds_read_b64_tr_b16 a[164:165], v244 offset:32768\n"
    "v_mfma_f32_16x16x32_bf16 v[216:219], a[168:171], a[136:139], v[216:219]\n"
    "ds_read2st64_b32 v[168:169], v249 offset0:12 offset1:13\n"
    "v_mfma_f32_16x16x32_bf16 v[216:219], a[172:175], a[140:143], v[216:219]\n"
    "s_waitcnt lgkmcnt(10)\n"
    "ds_read2st64_b32 v[170:171], v249 offset0:14 offset1:15\n"
    "v_mfma_f32_16x16x32_bf16 v[216:219], a[176:179], a[144:147], v[216:219]\n"
    "buffer_load_dwordx4 v248, s[8:11], s41 offen offset:2048 sc1 lds\n"
    "v_mfma_f32_16x16x32_bf16 v[216:219], a[180:183], a[148:151], v[216:219]\n"
    "ds_read_b64_tr_b16 a[166:167], v244 offset:34816\n"
    "v_mfma_f32_16x16x32_bf16 v[208:211], a[180:183], a[116:119], v[208:211]\n"
    "ds_read_b64_tr_b16 a[168:169], v245 offset:0\n"
    "v_mfma_f32_16x16x32_bf16 v[208:211], a[176:179], a[112:115], v[208:211]\n"
    "v_exp_f32_e32 v181, v181\n"
    "v_mfma_f32_16x16x32_bf16 v[200:203], a[176:179], a[80:83], v[200:203]\n"
    "buffer_load_dwordx4 v248, s[8:11], s41 offen offset:3072 sc1 lds\n"
    "v_mfma_f32_16x16x32_bf16 v[200:203], a[180:183], a[84:87], v[200:203]\n"
    "ds_read_b64_tr_b16 a[170:171], v245 offset:2048\n"
    "v_mfma_f32_16x16x32_bf16 v[192:195], a[180:183], a[52:55], v[192:195]\n"
    "s_add_u32 s41, s41, 0x8000\n"
    "ds_read2st64_b32 v[176:177], v249 offset0:16 offset1:17\n"
    "v_mfma_f32_16x16x32_bf16 v[192:195], a[176:179], a[48:51], v[192:195]\n"
    "buffer_load_dwordx4 a[204:207], v235, s[20:23], 0 offen sc1\n"
    "v_mfma_f32_16x16x32_bf16 v[184:187], a[176:179], a[16:19], v[184:187]\n"
    "ds_read_b64_tr_b16 a[172:173], v245 offset:32768\n"
    "v_mfma_f32_16x16x32_bf16 v[184:187], a[180:183], a[20:23], v[184:187]\n"
    "s_waitcnt lgkmcnt(10)\n"
    "ds_read2st64_b32 v[178:179], v249 offset0:18 offset1:19\n"
    "v_mfma_f32_16x16x32_bf16 v[184:187], a[184:187], a[24:27], v[184:187]\n"
    "ds_read_b64_tr_b16 a[176:177], v246 offset:0\n"
    "v_mfma_f32_16x16x32_bf16 v[184:187], a[188:191], a[28:31], v[184:187]\n"
    "ds_read_b64_tr_b16 a[174:175], v245 offset:34816\n"
    "v_mfma_f32_16x16x32_bf16 v[192:195], a[188:191], a[60:63], v[192:195]\n"
    "v_exp_f32_e32 v182, v182\n"
    "v_mfma_f32_16x16x32_bf16 v[192:195], a[184:187], a[56:59], v[192:195]\n"
    "v_exp_f32_e32 v183, v183\n"
    "v_mfma_f32_16x16x32_bf16 v[200:203], a[184:187], a[88:91], v[200:203]\n"
    "ds_read_b64_tr_b16 a[178:179], v246 offset:2048\n"
    "v_cvt_pk_bf16_f32 v180, v180, v181\n"
    "v_mfma_f32_16x16x32_bf16 v[200:203], a[188:191], a[92:95], v[200:203]\n"
    "v_exp_f32_e32 v188, v188\n"
    "v_mfma_f32_16x16x32_bf16 v[208:211], a[188:191], a[124:127], v[208:211]\n"
    ".long 0xbedf00ff\n"
    ".Lat_w7:\n"
    "s_setpc_b64 s[2:3]\n"
    "v_exp_f32_e32 v189, v189\n"
    "v_mfma_f32_16x16x32_bf16 v[208:211], a[184:187], a[120:123], v[208:211]\n"
    "v_cvt_pk_bf16_f32 v181, v182, v183\n"
    "v_mfma_f32_16x16x32_bf16 v[216:219], a[184:187], a[152:155], v[216:219]\n"
    "ds_read_b64_tr_b16 a[180:181], v246 offset:32768\n"
    "v_mfma_f32_16x16x32_bf16 v[216:219], a[188:191], a[156:159], v[216:219]\n"
    "s_waitcnt vmcnt(20)\n"
    "ds_read_b64_tr_b16 a[182:183], v246 offset:34816\n"
    "v_smfmac_f32_16x16x64_bf16 v[224:227], a[232:235], a[208:215], v238\n"
    "s_waitcnt lgkmcnt(5)\n"
    "v_mad_u32_u16 v234, v230, s90, v239 op_sel:[0,0,0,0]\n"
    "v_mfma_f32_16x16x32_bf16 a[236:239], a[160:163], v[140:143], a[236:239]\n"
    "s_bitcmp1_b32 s86, 12\n"
    "buffer_load_dwordx4 a[208:211], v234, s[20:23], 0 offen sc1\n"
    "v_mad_u32_u16 v235, v230, s90, v239 op_sel:[1,0,0,0]\n"
    "v_mfma_f32_16x16x32_bf16 a[236:239], a[164:167], v[144:147], a[236:239]\n"
    "s_cselect_b64 exec, -1, 0\n"
    "v_cvt_pk_bf16_f32 v236, v224, v225\n"
    "v_cvt_pk_bf16_f32 v237, v226, v227\n"
    "v_mfma_f32_16x16x32_bf16 a[240:243], a[164:167], v[152:155], a[240:243]\n"
    "s_cselect_b32 s88, 512, 0\n"
    "v_mov_b64_e32 v[224:225], 0\n"
    "v_mov_b64_e32 v[226:227], 0\n"
    "v_mfma_f32_16x16x32_bf16 a[240:243], a[160:163], v[148:151], a[240:243]\n"
    "buffer_store_dwordx2 v[236:237], v252, s[8:11], s87 offen sc1\n"
    "v_mfma_f32_16x16x32_bf16 a[244:247], a[160:163], v[156:159], a[244:247]\n"
    "s_mov_b64 exec, -1\n"
    "ds_read_b64_tr_b16 a[184:185], v247 offset:0\n"
    "v_mfma_f32_16x16x32_bf16 a[244:247], a[164:167], v[160:163], a[244:247]\n"
    "s_add_u32 s87, s87, s88\n"
    "ds_read_b64_tr_b16 a[186:187], v247 offset:2048\n"
    "v_mfma_f32_16x16x32_bf16 a[248:251], a[164:167], v[168:171], a[248:251]\n"
    "buffer_load_dwordx4 a[212:215], v235, s[20:23], 0 offen sc1\n"
    "v_mfma_f32_16x16x32_bf16 a[248:251], a[160:163], v[164:167], a[248:251]\n"
    "ds_read_b64_tr_b16 a[188:189], v247 offset:32768\n"
    "v_mfma_f32_16x16x32_bf16 a[252:255], a[160:163], v[172:175], a[252:255]\n"
    "v_exp_f32_e32 v190, v190\n"
    "v_mfma_f32_16x16x32_bf16 a[252:255], a[164:167], v[176:179], a[252:255]\n"
    "s_waitcnt lgkmcnt(6)\n"
    "v_exp_f32_e32 v191, v191\n"
    "v_mfma_f32_16x16x32_bf16 v[16:19], a[172:175], v[176:179], v[16:19]\n"
    "ds_read_b64_tr_b16 a[190:191], v247 offset:34816\n"
    "v_cvt_pk_bf16_f32 v188, v188, v189\n"
    "v_mfma_f32_16x16x32_bf16 v[16:19], a[168:171], v[172:175], v[16:19]\n"
    "ds_read_b64_tr_b16 a[160:161], v244 offset:4096\n"
    "v_mfma_f32_16x16x32_bf16 v[12:15], a[168:171], v[164:167], v[12:15]\n"
    "v_exp_f32_e32 v196, v196\n"
    "v_mfma_f32_16x16x32_bf16 v[12:15], a[172:175], v[168:171], v[12:15]\n"
    "v_exp_f32_e32 v197, v197\n"
    "v_mfma_f32_16x16x32_bf16 v[8:11], a[172:175], v[160:163], v[8:11]\n"
    "ds_read_b64_tr_b16 a[162:163], v244 offset:6144\n"
    "v_cvt_pk_bf16_f32 v189, v190, v191\n"
    "v_mfma_f32_16x16x32_bf16 v[8:11], a[168:171], v[156:159], v[8:11]\n"
    "v_exp_f32_e32 v198, v198\n"
    "v_mfma_f32_16x16x32_bf16 v[4:7], a[168:171], v[148:151], v[4:7]\n"
    "v_exp_f32_e32 v199, v199\n"
    "v_mfma_f32_16x16x32_bf16 v[4:7], a[172:175], v[152:155], v[4:7]\n"
    "v_cvt_pk_bf16_f32 v196, v196, v197\n"
    "v_mfma_f32_16x16x32_bf16 v[0:3], a[172:175], v[144:147], v[0:3]\n"
    "ds_read_b64_tr_b16 a[164:165], v244 offset:36864\n"
    "v_mfma_f32_16x16x32_bf16 v[0:3], a[168:171], v[140:143], v[0:3]\n"
    "s_waitcnt lgkmcnt(7)\n"
    "v_exp_f32_e32 v204, v204\n"
    "v_mfma_f32_16x16x32_bf16 v[20:23], a[176:179], v[140:143], v[20:23]\n"
    "ds_read_b64_tr_b16 a[168:169], v245 offset:4096\n"
    "v_mfma_f32_16x16x32_bf16 v[20:23], a[180:183], v[144:147], v[20:23]\n"
    "ds_read_b64_tr_b16 a[166:167], v244 offset:38912\n"
    "v_mfma_f32_16x16x32_bf16 v[24:27], a[180:183], v[152:155], v[24:27]\n"
    "v_exp_f32_e32 v205, v205\n"
    "v_mfma_f32_16x16x32_bf16 v[24:27], a[176:179], v[148:151], v[24:27]\n"
    "v_cvt_pk_bf16_f32 v197, v198, v199\n"
    "v_mfma_f32_16x16x32_bf16 v[28:31], a[176:179], v[156:159], v[28:31]\n"
    "ds_read_b64_tr_b16 a[170:171], v245 offset:6144\n"
    "v_mfma_f32_16x16x32_bf16 v[28:31], a[180:183], v[160:163], v[28:31]\n"
    "v_exp_f32_e32 v206, v206\n"
    "v_mfma_f32_16x16x32_bf16 v[32:35], a[180:183], v[168:171], v[32:35]\n"
    "v_exp_f32_e32 v207, v207\n"
    "v_mfma_f32_16x16x32_bf16 v[32:35], a[176:179], v[164:167], v[32:35]\n"
    "v_cvt_pk_bf16_f32 v204, v204, v205\n"
    "v_mfma_f32_16x16x32_bf16 v[36:39], a[176:179], v[172:175], v[36:39]\n"
    "ds_read_b64_tr_b16 a[172:173], v245 offset:36864\n"
    "v_mfma_f32_16x16x32_bf16 v[36:39], a[180:183], v[176:179], v[36:39]\n"
    "s_waitcnt vmcnt(20)\n"
    "ds_read_b64_tr_b16 a[174:175], v245 offset:38912\n"
    "v_smfmac_f32_16x16x64_bf16 v[224:227], a[232:235], a[216:223], v238\n"
    "s_waitcnt lgkmcnt(8)\n"
    "v_mad_u32_u16 v234, v231, s90, v239 op_sel:[0,0,0,0]\n"
    "v_mfma_f32_16x16x32_bf16 v[56:59], a[188:191], v[176:179], v[56:59]\n"
    "s_bitcmp1_b32 s86, 13\n"
    "buffer_load_dwordx4 a[216:219], v234, s[20:23], 0 offen sc1\n"
    "v_mad_u32_u16 v235, v231, s90, v239 op_sel:[1,0,0,0]\n"
    "v_mfma_f32_16x16x32_bf16 v[56:59], a[184:187], v[172:175], v[56:59]\n"
    "s_cselect_b64 exec, -1, 0\n"
    "v_cvt_pk_bf16_f32 v236, v224, v225\n"
    "v_cvt_pk_bf16_f32 v237, v226, v227\n"
    "v_mfma_f32_16x16x32_bf16 v[52:55], a[184:187], v[164:167], v[52:55]\n"
    "s_cselect_b32 s88, 512, 0\n"
    "v_mov_b64_e32 v[224:225], 0\n"
    "v_mov_b64_e32 v[226:227], 0\n"
    "v_mfma_f32_16x16x32_bf16 v[52:55], a[188:191], v[168:171], v[52:55]\n"
    "buffer_store_dwordx2 v[236:237], v252, s[8:11], s87 offen sc1\n"
    "v_mfma_f32_16x16x32_bf16 v[48:51], a[188:191], v[160:163], v[48:51]\n"
    "s_mov_b64 exec, -1\n"
    "ds_read_b64_tr_b16 a[176:177], v246 offset:4096\n"
    "v_mfma_f32_16x16x32_bf16 v[48:51], a[184:187], v[156:159], v[48:51]\n"
    "s_add_u32 s87, s87, s88\n"
    "ds_read_b64_tr_b16 a[178:179], v246 offset:6144\n"
    "v_mfma_f32_16x16x32_bf16 v[44:47], a[184:187], v[148:151], v[44:47]\n"
    "buffer_load_dwordx4 a[220:223], v235, s[20:23], 0 offen sc1\n"
    "v_mfma_f32_16x16x32_bf16 v[44:47], a[188:191], v[152:155], v[44:47]\n"
    "ds_read_b64_tr_b16 a[180:181], v246 offset:36864\n"
    "v_mfma_f32_16x16x32_bf16 v[40:43], a[188:191], v[144:147], v[40:43]\n"
    "v_exp_f32_e32 v212, v212\n"
    "v_mfma_f32_16x16x32_bf16 v[40:43], a[184:187], v[140:143], v[40:43]\n"
    "s_waitcnt lgkmcnt(6)\n"
    "v_exp_f32_e32 v213, v213\n"
    "v_mfma_f32_16x16x32_bf16 v[60:63], a[160:163], v[140:143], v[60:63]\n"
    "ds_read_b64_tr_b16 a[182:183], v246 offset:38912\n"
    "v_cvt_pk_bf16_f32 v205, v206, v207\n"
    "v_mfma_f32_16x16x32_bf16 v[60:63], a[164:167], v[144:147], v[60:63]\n"
    "ds_read_b64_tr_b16 a[184:185], v247 offset:4096\n"
    "v_mfma_f32_16x16x32_bf16 v[64:67], a[164:167], v[152:155], v[64:67]\n"
    "ds_read_b64_tr_b16 a[186:187], v247 offset:6144\n"
    "v_mfma_f32_16x16x32_bf16 v[64:67], a[160:163], v[148:151], v[64:67]\n"
    "v_exp_f32_e32 v214, v214\n"
    "v_mfma_f32_16x16x32_bf16 v[68:71], a[160:163], v[156:159], v[68:71]\n"
    "v_exp_f32_e32 v215, v215\n"
    "v_mfma_f32_16x16x32_bf16 v[68:71], a[164:167], v[160:163], v[68:71]\n"
    "ds_read_b64_tr_b16 a[188:189], v247 offset:36864\n"
    "v_cvt_pk_bf16_f32 v212, v212, v213\n"
    "v_mfma_f32_16x16x32_bf16 v[72:75], a[164:167], v[168:171], v[72:75]\n"
    "ds_read_b64_tr_b16 a[190:191], v247 offset:38912\n"
    "v_cvt_pk_bf16_f32 v213, v214, v215\n"
    "v_mfma_f32_16x16x32_bf16 v[72:75], a[160:163], v[164:167], v[72:75]\n"
    "s_waitcnt vmcnt(30)\n"
    "v_mfma_f32_16x16x32_bf16 v[76:79], a[160:163], v[172:175], v[76:79]\n"
    "s_barrier\n"
    "v_mfma_f32_16x16x32_bf16 v[76:79], a[164:167], v[176:179], v[76:79]\n"
    "s_waitcnt lgkmcnt(8)\n"
    "s_mov_b32 m0, s42\n"
    "v_mfma_f32_16x16x32_bf16 v[96:99], a[172:175], v[176:179], v[96:99]\n"
    "v_exp_f32_e32 v184, v184\n"
    "v_mfma_f32_16x16x32_bf16 v[96:99], a[168:171], v[172:175], v[96:99]\n"
    "v_exp_f32_e32 v185, v185\n"
    "v_mfma_f32_16x16x32_bf16 v[92:95], a[168:171], v[164:167], v[92:95]\n"
    "v_exp_f32_e32 v186, v186\n"
    "v_mfma_f32_16x16x32_bf16 v[92:95], a[172:175], v[168:171], v[92:95]\n"
    "ds_read_b128 a[160:163], v240 offset:16384\n"
    "ds_write_addtid_b32 v180 offset:0\n"
    "v_mfma_f32_16x16x32_bf16 v[88:91], a[172:175], v[160:163], v[88:91]\n"
    "v_exp_f32_e32 v187, v187\n"
    "v_mfma_f32_16x16x32_bf16 v[88:91], a[168:171], v[156:159], v[88:91]\n"
    "ds_write_addtid_b32 v181 offset:256\n"
    "v_cvt_pk_bf16_f32 v182, v184, v185\n"
    "v_mfma_f32_16x16x32_bf16 v[84:87], a[168:171], v[148:151], v[84:87]\n"
    "v_exp_f32_e32 v192, v192\n"
    "v_mfma_f32_16x16x32_bf16 v[84:87], a[172:175], v[152:155], v[84:87]\n"
    "v_exp_f32_e32 v193, v193\n"
    "v_mfma_f32_16x16x32_bf16 v[80:83], a[172:175], v[144:147], v[80:83]\n"
    "ds_write_addtid_b32 v188 offset:1024\n"
    "v_cvt_pk_bf16_f32 v183, v186, v187\n"
    "v_mfma_f32_16x16x32_bf16 v[80:83], a[168:171], v[140:143], v[80:83]\n"
    "s_waitcnt vmcnt(20)\n"
    "ds_read_b128 a[164:167], v241 offset:16384\n"
    "ds_write_addtid_b32 v189 offset:1280\n"
    "v_smfmac_f32_16x16x64_bf16 v[224:227], a[232:235], a[224:231], v238\n"
    "s_waitcnt lgkmcnt(10)\n"
    "ds_write_addtid_b32 v196 offset:2048\n"
    "v_mad_u32_u16 v234, v232, s90, v239 op_sel:[0,0,0,0]\n"
    "v_mfma_f32_16x16x32_bf16 v[100:103], a[176:179], v[140:143], v[100:103]\n"
    "s_bitcmp1_b32 s86, 14\n"
    "buffer_load_dwordx4 a[224:227], v234, s[20:23], 0 offen sc1\n"
    "v_mad_u32_u16 v235, v232, s90, v239 op_sel:[1,0,0,0]\n"
    "v_mfma_f32_16x16x32_bf16 v[100:103], a[180:183], v[144:147], v[100:103]\n"
    "s_cselect_b64 exec, -1, 0\n"
    "v_cvt_pk_bf16_f32 v236, v224, v225\n"
    "v_cvt_pk_bf16_f32 v237, v226, v227\n"
    "v_mfma_f32_16x16x32_bf16 v[104:107], a[180:183], v[152:155], v[104:107]\n"
    "s_cselect_b32 s88, 512, 0\n"
    "v_mov_b64_e32 v[224:225], 0\n"
    "v_mov_b64_e32 v[226:227], 0\n"
    "v_mfma_f32_16x16x32_bf16 v[104:107], a[176:179], v[148:151], v[104:107]\n"
    "buffer_store_dwordx2 v[236:237], v252, s[8:11], s87 offen sc1\n"
    "v_mfma_f32_16x16x32_bf16 v[108:111], a[176:179], v[156:159], v[108:111]\n"
    "s_mov_b64 exec, -1\n"
    "ds_read_b128 a[168:171], v240 offset:20480\n"
    "ds_write_addtid_b32 v197 offset:2304\n"
    "v_mfma_f32_16x16x32_bf16 v[108:111], a[180:183], v[160:163], v[108:111]\n"
    "s_add_u32 s87, s87, s88\n"
    "v_exp_f32_e32 v194, v194\n"
    "v_mfma_f32_16x16x32_bf16 v[112:115], a[180:183], v[168:171], v[112:115]\n"
    "buffer_load_dwordx4 a[228:231], v235, s[20:23], 0 offen sc1\n"
    "ds_write_addtid_b32 v204 offset:3072\n"
    "v_mfma_f32_16x16x32_bf16 v[112:115], a[176:179], v[164:167], v[112:115]\n"
    "ds_write_addtid_b32 v205 offset:3328\n"
    "v_mfma_f32_16x16x32_bf16 v[116:119], a[176:179], v[172:175], v[116:119]\n"
    "v_exp_f32_e32 v195, v195\n"
    "v_mfma_f32_16x16x32_bf16 v[116:119], a[180:183], v[176:179], v[116:119]\n"
    "s_waitcnt lgkmcnt(11)\n"
    "ds_read_b128 a[172:175], v241 offset:20480\n"
    "v_cvt_pk_bf16_f32 v190, v192, v193\n"
    "v_mfma_f32_16x16x32_bf16 v[136:139], a[188:191], v[176:179], v[136:139]\n"
    "v_exp_f32_e32 v200, v200\n"
    "v_mfma_f32_16x16x32_bf16 v[136:139], a[184:187], v[172:175], v[136:139]\n"
    "v_exp_f32_e32 v201, v201\n"
    "v_mfma_f32_16x16x32_bf16 v[132:135], a[184:187], v[164:167], v[132:135]\n"
    "ds_read_b128 a[176:179], v242 offset:16384\n"
    "v_cvt_pk_bf16_f32 v191, v194, v195\n"
    "v_mfma_f32_16x16x32_bf16 v[132:135], a[188:191], v[168:171], v[132:135]\n"
    "v_exp_f32_e32 v202, v202\n"
    "v_mfma_f32_16x16x32_bf16 v[128:131], a[188:191], v[160:163], v[128:131]\n"
    "v_exp_f32_e32 v203, v203\n"
    "v_mfma_f32_16x16x32_bf16 v[128:131], a[184:187], v[156:159], v[128:131]\n"
    "ds_write_addtid_b32 v212 offset:4096\n"
    "v_cvt_pk_bf16_f32 v198, v200, v201\n"
    "v_mfma_f32_16x16x32_bf16 v[124:127], a[184:187], v[148:151], v[124:127]\n"
    "v_exp_f32_e32 v208, v208\n"
    "v_mfma_f32_16x16x32_bf16 v[124:127], a[188:191], v[152:155], v[124:127]\n"
    "ds_read_b128 a[180:183], v243 offset:16384\n"
    "ds_write_addtid_b32 v213 offset:4352\n"
    "v_mfma_f32_16x16x32_bf16 v[120:123], a[188:191], v[144:147], v[120:123]\n"
    "v_exp_f32_e32 v209, v209\n"
    "v_mfma_f32_16x16x32_bf16 v[120:123], a[184:187], v[140:143], v[120:123]\n"
    "s_waitcnt lgkmcnt(11)\n"
    "ds_write_addtid_b32 v182 offset:512\n"
    "v_cvt_pk_bf16_f32 v199, v202, v203\n"
    "v_mfma_f32_16x16x32_bf16 v[172:175], a[160:163], a[128:131], v[220:223]\n"
    "ds_write_addtid_b32 v183 offset:768\n"
    "v_mfma_f32_16x16x32_bf16 v[172:175], a[164:167], a[132:135], v[172:175]\n"
    "v_exp_f32_e32 v210, v210\n"
    "v_mfma_f32_16x16x32_bf16 v[164:167], a[164:167], a[100:103], v[220:223]\n"
    "ds_write_addtid_b32 v190 offset:1536\n"
    "v_mfma_f32_16x16x32_bf16 v[164:167], a[160:163], a[96:99], v[164:167]\n"
    "ds_read_b128 a[184:187], v242 offset:20480\n"
    "v_mfma_f32_16x16x32_bf16 v[156:159], a[160:163], a[64:67], v[220:223]\n"
    "v_exp_f32_e32 v211, v211\n"
    "v_mfma_f32_16x16x32_bf16 v[156:159], a[164:167], a[68:71], v[156:159]\n"
    "ds_write_addtid_b32 v191 offset:1792\n"
    "v_cvt_pk_bf16_f32 v206, v208, v209\n"
    "v_mfma_f32_16x16x32_bf16 v[148:151], a[164:167], a[36:39], v[220:223]\n"
    "v_exp_f32_e32 v216, v216\n"
    "v_mfma_f32_16x16x32_bf16 v[148:151], a[160:163], a[32:35], v[148:151]\n"
    "v_exp_f32_e32 v217, v217\n"
    "v_mfma_f32_16x16x32_bf16 v[140:143], a[160:163], a[0:3], v[220:223]\n"
    "ds_write_addtid_b32 v198 offset:2560\n"
    "v_cvt_pk_bf16_f32 v207, v210, v211\n"
    "v_mfma_f32_16x16x32_bf16 v[140:143], a[164:167], a[4:7], v[140:143]\n"
    "s_waitcnt vmcnt(20)\n"
    "v_exp_f32_e32 v218, v218\n"
    "v_smfmac_f32_16x16x64_bf16 v[224:227], a[232:235], a[192:199], v238\n"
    "s_waitcnt lgkmcnt(10)\n"
    "ds_read_b128 a[188:191], v243 offset:20480\n"
    "v_mfma_f32_16x16x32_bf16 v[140:143], a[168:171], a[8:11], v[140:143]\n"
    "s_bitcmp1_b32 s86, 15\n"
    "ds_read_b128 a[160:163], v240 offset:18432\n"
    "v_mfma_f32_16x16x32_bf16 v[140:143], a[172:175], a[12:15], v[140:143]\n"
    "s_cselect_b64 exec, -1, 0\n"
    "v_cvt_pk_bf16_f32 v236, v224, v225\n"
    "v_cvt_pk_bf16_f32 v237, v226, v227\n"
    "v_mfma_f32_16x16x32_bf16 v[148:151], a[172:175], a[44:47], v[148:151]\n"
    "s_cselect_b32 s88, 512, 0\n"
    "v_mov_b64_e32 v[224:225], 0\n"
    "v_mov_b64_e32 v[226:227], 0\n"
    "v_mfma_f32_16x16x32_bf16 v[148:151], a[168:171], a[40:43], v[148:151]\n"
    "buffer_store_dwordx2 v[236:237], v252, s[8:11], s87 offen sc1\n"
    "v_mfma_f32_16x16x32_bf16 v[156:159], a[168:171], a[72:75], v[156:159]\n"
    "s_mov_b64 exec, -1\n"
    "ds_read2_b32 v[228:229], v251 offset0:30 offset1:32\n"
    "v_mfma_f32_16x16x32_bf16 v[156:159], a[172:175], a[76:79], v[156:159]\n"
    "s_add_u32 s87, s87, s88\n"
    "ds_read2_b32 v[230:231], v251 offset0:34 offset1:36\n"
    "v_mfma_f32_16x16x32_bf16 v[164:167], a[172:175], a[108:111], v[164:167]\n"
    "ds_read_b32 v232, v251 offset:152\n"
    "v_mfma_f32_16x16x32_bf16 v[164:167], a[168:171], a[104:107], v[164:167]\n"
    "v_exp_f32_e32 v219, v219\n"
    "v_mfma_f32_16x16x32_bf16 v[172:175], a[168:171], a[136:139], v[172:175]\n"
    "ds_write_addtid_b32 v199 offset:2816\n"
    "v_cvt_pk_bf16_f32 v214, v216, v217\n"
    "v_mfma_f32_16x16x32_bf16 v[172:175], a[172:175], a[140:143], v[172:175]\n"
    "s_waitcnt lgkmcnt(13)\n"
    "ds_read_b128 a[164:167], v241 offset:18432\n"
    "v_mfma_f32_16x16x32_bf16 v[172:175], a[176:179], a[144:147], v[172:175]\n"
    "ds_write_addtid_b32 v206 offset:3584\n"
    "v_cvt_pk_bf16_f32 v215, v218, v219\n"
    "v_mfma_f32_16x16x32_bf16 v[172:175], a[180:183], a[148:151], v[172:175]\n"
    "ds_read_b128 a[168:171], v240 offset:22528\n"
    "v_mfma_f32_16x16x32_bf16 v[164:167], a[180:183], a[116:119], v[164:167]\n"
    "s_waitcnt lgkmcnt(6)\n"
    "ds_write_addtid_b32 v207 offset:3840\n"
    "v_mad_u32_u16 v234, v228, s90, v239 op_sel:[0,0,0,0]\n"
    "v_mfma_f32_16x16x32_bf16 v[164:167], a[176:179], a[112:115], v[164:167]\n"
    "buffer_load_dwordx4 a[192:195], v234, s[20:23], 0 offen sc1\n"
    "v_mad_u32_u16 v235, v228, s90, v239 op_sel:[1,0,0,0]\n"
    "v_mfma_f32_16x16x32_bf16 v[156:159], a[176:179], a[80:83], v[156:159]\n"
    "ds_write_addtid_b32 v214 offset:4608\n"
    "v_add_u32_e32 v251, 160, v251\n"
    "v_mfma_f32_16x16x32_bf16 v[156:159], a[180:183], a[84:87], v[156:159]\n"
    "ds_write_addtid_b32 v215 offset:4864\n"
    "v_mfma_f32_16x16x32_bf16 v[148:151], a[180:183], a[52:55], v[148:151]\n"
    "buffer_load_dwordx4 a[196:199], v235, s[20:23], 0 offen sc1\n"
    "v_mfma_f32_16x16x32_bf16 v[148:151], a[176:179], a[48:51], v[148:151]\n"
    "v_mfma_f32_16x16x32_bf16 v[140:143], a[176:179], a[16:19], v[140:143]\n"
    "s_waitcnt lgkmcnt(0)\n"
    "v_mfma_f32_16x16x32_bf16 v[140:143], a[180:183], a[20:23], v[140:143]\n"
    "s_barrier\n"
    "v_mfma_f32_16x16x32_bf16 v[140:143], a[184:187], a[24:27], v[140:143]\n"
    "s_add_u32 m0, s43, 0\n"
    "ds_read_b128 a[172:175], v241 offset:22528\n"
    "v_mfma_f32_16x16x32_bf16 v[140:143], a[188:191], a[28:31], v[140:143]\n"
    "ds_read2st64_b32 v[184:185], v249 offset0:0 offset1:1\n"
    "v_mfma_f32_16x16x32_bf16 v[148:151], a[188:191], a[60:63], v[148:151]\n"
    "buffer_load_dwordx4 v248, s[8:11], s40 offen offset:0 sc1 lds\n"
    "v_mfma_f32_16x16x32_bf16 v[148:151], a[184:187], a[56:59], v[148:151]\n"
    "ds_read_b128 a[176:179], v242 offset:18432\n"
    "v_mfma_f32_16x16x32_bf16 v[156:159], a[184:187], a[88:91], v[156:159]\n"
    "v_mfma_f32_16x16x32_bf16 v[156:159], a[188:191], a[92:95], v[156:159]\n"
    "v_mfma_f32_16x16x32_bf16 v[164:167], a[188:191], a[124:127], v[164:167]\n"
    "buffer_load_dwordx4 v248, s[8:11], s40 offen offset:1024 sc1 lds\n"
    "v_mfma_f32_16x16x32_bf16 v[164:167], a[184:187], a[120:123], v[164:167]\n"
    "ds_read2st64_b32 v[186:187], v249 offset0:2 offset1:3\n"
    "v_mfma_f32_16x16x32_bf16 v[172:175], a[184:187], a[152:155], v[172:175]\n"
    "v_mfma_f32_16x16x32_bf16 v[172:175], a[188:191], a[156:159], v[172:175]\n"
    "ds_read_b128 a[180:183], v243 offset:18432\n"
    "v_mfma_f32_16x16x32_bf16 v[176:179], a[160:163], a[128:131], v[220:223]\n"
    "buffer_load_dwordx4 v248, s[8:11], s40 offen offset:2048 sc1 lds\n"
    "v_mfma_f32_16x16x32_bf16 v[176:179], a[164:167], a[132:135], v[176:179]\n"
    "ds_read2st64_b32 v[192:193], v249 offset0:4 offset1:5\n"
    "v_mfma_f32_16x16x32_bf16 v[168:171], a[164:167], a[100:103], v[220:223]\n"
    "v_exp_f32_e32 v140, v140\n"
    "v_mfma_f32_16x16x32_bf16 v[168:171], a[160:163], a[96:99], v[168:171]\n"
    "ds_read_b128 a[184:187], v242 offset:22528\n"
    "v_mfma_f32_16x16x32_bf16 v[160:163], a[160:163], a[64:67], v[220:223]\n"
    "buffer_load_dwordx4 v248, s[8:11], s40 offen offset:3072 sc1 lds\n"
    "v_mfma_f32_16x16x32_bf16 v[160:163], a[164:167], a[68:71], v[160:163]\n"
    "s_add_u32 m0, s43, 4096\n"
    "ds_read2st64_b32 v[194:195], v249 offset0:6 offset1:7\n"
    "v_mfma_f32_16x16x32_bf16 v[152:155], a[164:167], a[36:39], v[220:223]\n"
    "s_add_u32 s40, s40, 0x8000\n"
    "ds_read2st64_b32 v[200:201], v249 offset0:8 offset1:9\n"
    "v_mfma_f32_16x16x32_bf16 v[152:155], a[160:163], a[32:35], v[152:155]\n"
    "ds_read2st64_b32 v[202:203], v249 offset0:10 offset1:11\n"
    "v_mfma_f32_16x16x32_bf16 v[144:147], a[160:163], a[0:3], v[220:223]\n"
    "buffer_load_dwordx4 v248, s[8:11], s41 offen offset:0 sc1 lds\n"
    "v_mfma_f32_16x16x32_bf16 v[144:147], a[164:167], a[4:7], v[144:147]\n"
    "s_waitcnt vmcnt(17)\n"
    "ds_read_b128 a[188:191], v243 offset:22528\n"
    "v_smfmac_f32_16x16x64_bf16 v[224:227], a[232:235], a[200:207], v238\n"
    "s_waitcnt lgkmcnt(10)\n"
    "v_mad_u32_u16 v234, v229, s90, v239 op_sel:[0,0,0,0]\n"
    "v_mfma_f32_16x16x32_bf16 v[144:147], a[168:171], a[8:11], v[144:147]\n"
    "s_bitcmp1_b32 s86, 16\n"
    "buffer_load_dwordx4 a[200:203], v234, s[20:23], 0 offen sc1\n"
    "v_mad_u32_u16 v235, v229, s90, v239 op_sel:[1,0,0,0]\n"
    "v_mfma_f32_16x16x32_bf16 v[144:147], a[172:175], a[12:15], v[144:147]\n"
    "s_cselect_b64 exec, -1, 0\n"
    "v_cvt_pk_bf16_f32 v236, v224, v225\n"
    "v_cvt_pk_bf16_f32 v237, v226, v227\n"
    "v_mfma_f32_16x16x32_bf16 v[152:155], a[172:175], a[44:47], v[152:155]\n"
    "s_cselect_b32 s88, 512, 0\n"
    "v_mov_b64_e32 v[224:225], 0\n"
    "v_mov_b64_e32 v[226:227], 0\n"
    "v_mfma_f32_16x16x32_bf16 v[152:155], a[168:171], a[40:43], v[152:155]\n"
    "buffer_store_dwordx2 v[236:237], v252, s[8:11], s87 offen sc1\n"
    "v_mfma_f32_16x16x32_bf16 v[160:163], a[168:171], a[72:75], v[160:163]\n"
    "s_mov_b64 exec, -1\n"
    "ds_read_b64_tr_b16 a[160:161], v244 offset:8192\n"
    "v_mfma_f32_16x16x32_bf16 v[160:163], a[172:175], a[76:79], v[160:163]\n"
    "s_add_u32 s87, s87, s88\n"
    "ds_read_b64_tr_b16 a[162:163], v244 offset:10240\n"
    "v_mfma_f32_16x16x32_bf16 v[168:171], a[172:175], a[108:111], v[168:171]\n"
    "buffer_load_dwordx4 v248, s[8:11], s41 offen offset:1024 sc1 lds\n"
    "v_mfma_f32_16x16x32_bf16 v[168:171], a[168:171], a[104:107], v[168:171]\n"
    "ds_read_b64_tr_b16 a[164:165], v244 offset:40960\n"
    "v_mfma_f32_16x16x32_bf16 v[176:179], a[168:171], a[136:139], v[176:179]\n"
    "ds_read2st64_b32 v[208:209], v249 offset0:12 offset1:13\n"
    "v_mfma_f32_16x16x32_bf16 v[176:179], a[172:175], a[140:143], v[176:179]\n"
    "s_waitcnt lgkmcnt(10)\n"
    "ds_read2st64_b32 v[210:211], v249 offset0:14 offset1:15\n"
    "v_mfma_f32_16x16x32_bf16 v[176:179], a[176:179], a[144:147], v[176:179]\n"
    "buffer_load_dwordx4 v248, s[8:11], s41 offen offset:2048 sc1 lds\n"
    "v_mfma_f32_16x16x32_bf16 v[176:179], a[180:183], a[148:151], v[176:179]\n"
    "ds_read_b64_tr_b16 a[166:167], v244 offset:43008\n"
    "v_mfma_f32_16x16x32_bf16 v[168:171], a[180:183], a[116:119], v[168:171]\n"
    "ds_read_b64_tr_b16 a[168:169], v245 offset:8192\n"
    "v_mfma_f32_16x16x32_bf16 v[168:171], a[176:179], a[112:115], v[168:171]\n"
    "v_exp_f32_e32 v141, v141\n"
    "v_mfma_f32_16x16x32_bf16 v[160:163], a[176:179], a[80:83], v[160:163]\n"
    "buffer_load_dwordx4 v248, s[8:11], s41 offen offset:3072 sc1 lds\n"
    "v_mfma_f32_16x16x32_bf16 v[160:163], a[180:183], a[84:87], v[160:163]\n"
    "ds_read_b64_tr_b16 a[170:171], v245 offset:10240\n"
    "v_mfma_f32_16x16x32_bf16 v[152:155], a[180:183], a[52:55], v[152:155]\n"
    "s_add_u32 s41, s41, 0x8000\n"
    "ds_read2st64_b32 v[216:217], v249 offset0:16 offset1:17\n"
    "v_mfma_f32_16x16x32_bf16 v[152:155], a[176:179], a[48:51], v[152:155]\n"
    "buffer_load_dwordx4 a[204:207], v235, s[20:23], 0 offen sc1\n"
    "v_mfma_f32_16x16x32_bf16 v[144:147], a[176:179], a[16:19], v[144:147]\n"
    "ds_read_b64_tr_b16 a[172:173], v245 offset:40960\n"
    "v_mfma_f32_16x16x32_bf16 v[144:147], a[180:183], a[20:23], v[144:147]\n"
    "s_waitcnt lgkmcnt(10)\n"
    "ds_read2st64_b32 v[218:219], v249 offset0:18 offset1:19\n"
    "v_mfma_f32_16x16x32_bf16 v[144:147], a[184:187], a[24:27], v[144:147]\n"
    "ds_read_b64_tr_b16 a[176:177], v246 offset:8192\n"
    "v_mfma_f32_16x16x32_bf16 v[144:147], a[188:191], a[28:31], v[144:147]\n"
    "ds_read_b64_tr_b16 a[174:175], v245 offset:43008\n"
    "v_mfma_f32_16x16x32_bf16 v[152:155], a[188:191], a[60:63], v[152:155]\n"
    "v_exp_f32_e32 v142, v142\n"
    "v_mfma_f32_16x16x32_bf16 v[152:155], a[184:187], a[56:59], v[152:155]\n"
    "v_exp_f32_e32 v143, v143\n"
    "v_mfma_f32_16x16x32_bf16 v[160:163], a[184:187], a[88:91], v[160:163]\n"
    "ds_read_b64_tr_b16 a[178:179], v246 offset:10240\n"
    "v_cvt_pk_bf16_f32 v140, v140, v141\n"
    "v_mfma_f32_16x16x32_bf16 v[160:163], a[188:191], a[92:95], v[160:163]\n"
    "v_exp_f32_e32 v148, v148\n"
    "v_mfma_f32_16x16x32_bf16 v[168:171], a[188:191], a[124:127], v[168:171]\n"
    "v_exp_f32_e32 v149, v149\n"
    "v_mfma_f32_16x16x32_bf16 v[168:171], a[184:187], a[120:123], v[168:171]\n"
    "v_cvt_pk_bf16_f32 v141, v142, v143\n"
    "v_mfma_f32_16x16x32_bf16 v[176:179], a[184:187], a[152:155], v[176:179]\n"
    "ds_read_b64_tr_b16 a[180:181], v246 offset:40960\n"
    "v_mfma_f32_16x16x32_bf16 v[176:179], a[188:191], a[156:159], v[176:179]\n"
    "s_waitcnt vmcnt(20)\n"
    "ds_read_b64_tr_b16 a[182:183], v246 offset:43008\n"
    "v_smfmac_f32_16x16x64_bf16 v[224:227], a[232:235], a[208:215], v238\n"
    "s_waitcnt lgkmcnt(5)\n"
    "v_mad_u32_u16 v234, v230, s90, v239 op_sel:[0,0,0,0]\n"
    "v_mfma_f32_16x16x32_bf16 a[236:239], a[160:163], v[180:183], a[236:239]\n"
    "s_bitcmp1_b32 s86, 17\n"
    "buffer_load_dwordx4 a[208:211], v234, s[20:23], 0 offen sc1\n"
    "v_mad_u32_u16 v235, v230, s90, v239 op_sel:[1,0,0,0]\n"
    "v_mfma_f32_16x16x32_bf16 a[236:239], a[164:167], v[184:187], a[236:239]\n"
    "s_cselect_b64 exec, -1, 0\n"
    "v_cvt_pk_bf16_f32 v236, v224, v225\n"
    "v_cvt_pk_bf16_f32 v237, v226, v227\n"
    "v_mfma_f32_16x16x32_bf16 a[240:243], a[164:167], v[192:195], a[240:243]\n"
    "s_cselect_b32 s88, 512, 0\n"
    "v_mov_b64_e32 v[224:225], 0\n"
    "v_mov_b64_e32 v[226:227], 0\n"
    "v_mfma_f32_16x16x32_bf16 a[240:243], a[160:163], v[188:191], a[240:243]\n"
    "buffer_store_dwordx2 v[236:237], v252, s[8:11], s87 offen sc1\n"
    "v_mfma_f32_16x16x32_bf16 a[244:247], a[160:163], v[196:199], a[244:247]\n"
    "s_mov_b64 exec, -1\n"
    "ds_read_b64_tr_b16 a[184:185], v247 offset:8192\n"
    "v_mfma_f32_16x16x32_bf16 a[244:247], a[164:167], v[200:203], a[244:247]\n"
    "s_add_u32 s87, s87, s88\n"
    "ds_read_b64_tr_b16 a[186:187], v247 offset:10240\n"
    "v_mfma_f32_16x16x32_bf16 a[248:251], a[164:167], v[208:211], a[248:251]\n"
    "buffer_load_dwordx4 a[212:215], v235, s[20:23], 0 offen sc1\n"
    "v_mfma_f32_16x16x32_bf16 a[248:251], a[160:163], v[204:207], a[248:251]\n"
    "ds_read_b64_tr_b16 a[188:189], v247 offset:40960\n"
    "v_mfma_f32_16x16x32_bf16 a[252:255], a[160:163], v[212:215], a[252:255]\n"
    "v_exp_f32_e32 v150, v150\n"
    "v_mfma_f32_16x16x32_bf16 a[252:255], a[164:167], v[216:219], a[252:255]\n"
    "s_waitcnt lgkmcnt(6)\n"
    "v_exp_f32_e32 v151, v151\n"
    "v_mfma_f32_16x16x32_bf16 v[16:19], a[172:175], v[216:219], v[16:19]\n"
    "ds_read_b64_tr_b16 a[190:191], v247 offset:43008\n"
    "v_cvt_pk_bf16_f32 v148, v148, v149\n"
    "v_mfma_f32_16x16x32_bf16 v[16:19], a[168:171], v[212:215], v[16:19]\n"
    "ds_read_b64_tr_b16 a[160:161], v244 offset:12288\n"
    "v_mfma_f32_16x16x32_bf16 v[12:15], a[168:171], v[204:207], v[12:15]\n"
    "v_exp_f32_e32 v156, v156\n"
    "v_mfma_f32_16x16x32_bf16 v[12:15], a[172:175], v[208:211], v[12:15]\n"
    "v_exp_f32_e32 v157, v157\n"
    "v_mfma_f32_16x16x32_bf16 v[8:11], a[172:175], v[200:203], v[8:11]\n"
    "ds_read_b64_tr_b16 a[162:163], v244 offset:14336\n"
    "v_cvt_pk_bf16_f32 v149, v150, v151\n"
    "v_mfma_f32_16x16x32_bf16 v[8:11], a[168:171], v[196:199], v[8:11]\n"
    "v_exp_f32_e32 v158, v158\n"
    "v_mfma_f32_16x16x32_bf16 v[4:7], a[168:171], v[188:191], v[4:7]\n"
    "v_exp_f32_e32 v159, v159\n"
    "v_mfma_f32_16x16x32_bf16 v[4:7], a[172:175], v[192:195], v[4:7]\n"
    "v_cvt_pk_bf16_f32 v156, v156, v157\n"
    "v_mfma_f32_16x16x32_bf16 v[0:3], a[172:175], v[184:187], v[0:3]\n"
    "ds_read_b64_tr_b16 a[164:165], v244 offset:45056\n"
    "v_mfma_f32_16x16x32_bf16 v[0:3], a[168:171], v[180:183], v[0:3]\n"
    "s_waitcnt lgkmcnt(7)\n"
    "v_exp_f32_e32 v164, v164\n"
    "v_mfma_f32_16x16x32_bf16 v[20:23], a[176:179], v[180:183], v[20:23]\n"
    "ds_read_b64_tr_b16 a[168:169], v245 offset:12288\n"
    "v_mfma_f32_16x16x32_bf16 v[20:23], a[180:183], v[184:187], v[20:23]\n"
    "ds_read_b64_tr_b16 a[166:167], v244 offset:47104\n"
    "v_mfma_f32_16x16x32_bf16 v[24:27], a[180:183], v[192:195], v[24:27]\n"
    "v_exp_f32_e32 v165, v165\n"
    "v_mfma_f32_16x16x32_bf16 v[24:27], a[176:179], v[188:191], v[24:27]\n"
    "v_cvt_pk_bf16_f32 v157, v158, v159\n"
    "v_mfma_f32_16x16x32_bf16 v[28:31], a[176:179], v[196:199], v[28:31]\n"
    "ds_read_b64_tr_b16 a[170:171], v245 offset:14336\n"
    "v_mfma_f32_16x16x32_bf16 v[28:31], a[180:183], v[200:203], v[28:31]\n"
    "v_exp_f32_e32 v166, v166\n"
    "v_mfma_f32_16x16x32_bf16 v[32:35], a[180:183], v[208:211], v[32:35]\n"
    "v_exp_f32_e32 v167, v167\n"
    "v_mfma_f32_16x16x32_bf16 v[32:35], a[176:179], v[204:207], v[32:35]\n"
    "v_cvt_pk_bf16_f32 v164, v164, v165\n"
    "v_mfma_f32_16x16x32_bf16 v[36:39], a[176:179], v[212:215], v[36:39]\n"
    "ds_read_b64_tr_b16 a[172:173], v245 offset:45056\n"
    "v_mfma_f32_16x16x32_bf16 v[36:39], a[180:183], v[216:219], v[36:39]\n"
    "s_waitcnt vmcnt(20)\n"
    "ds_read_b64_tr_b16 a[174:175], v245 offset:47104\n"
    "v_smfmac_f32_16x16x64_bf16 v[224:227], a[232:235], a[216:223], v238\n"
    "s_waitcnt lgkmcnt(8)\n"
    "v_mad_u32_u16 v234, v231, s90, v239 op_sel:[0,0,0,0]\n"
    "v_mfma_f32_16x16x32_bf16 v[56:59], a[188:191], v[216:219], v[56:59]\n"
    "s_bitcmp1_b32 s86, 18\n"
    "buffer_load_dwordx4 a[216:219], v234, s[20:23], 0 offen sc1\n"
    "v_mad_u32_u16 v235, v231, s90, v239 op_sel:[1,0,0,0]\n"
    "v_mfma_f32_16x16x32_bf16 v[56:59], a[184:187], v[212:215], v[56:59]\n"
    "s_cselect_b64 exec, -1, 0\n"
    "v_cvt_pk_bf16_f32 v236, v224, v225\n"
    "v_cvt_pk_bf16_f32 v237, v226, v227\n"
    "v_mfma_f32_16x16x32_bf16 v[52:55], a[184:187], v[204:207], v[52:55]\n"
    "s_cselect_b32 s88, 512, 0\n"
    "v_mov_b64_e32 v[224:225], 0\n"
    "v_mov_b64_e32 v[226:227], 0\n"
    "v_mfma_f32_16x16x32_bf16 v[52:55], a[188:191], v[208:211], v[52:55]\n"
    "buffer_store_dwordx2 v[236:237], v252, s[8:11], s87 offen sc1\n"
    "v_mfma_f32_16x16x32_bf16 v[48:51], a[188:191], v[200:203], v[48:51]\n"
    "s_mov_b64 exec, -1\n"
    "ds_read_b64_tr_b16 a[176:177], v246 offset:12288\n"
    "v_mfma_f32_16x16x32_bf16 v[48:51], a[184:187], v[196:199], v[48:51]\n"
    "s_add_u32 s87, s87, s88\n"
    "ds_read_b64_tr_b16 a[178:179], v246 offset:14336\n"
    "v_mfma_f32_16x16x32_bf16 v[44:47], a[184:187], v[188:191], v[44:47]\n"
    "buffer_load_dwordx4 a[220:223], v235, s[20:23], 0 offen sc1\n"
    "v_mfma_f32_16x16x32_bf16 v[44:47], a[188:191], v[192:195], v[44:47]\n"
    "ds_read_b64_tr_b16 a[180:181], v246 offset:45056\n"
    "v_mfma_f32_16x16x32_bf16 v[40:43], a[188:191], v[184:187], v[40:43]\n"
    "v_exp_f32_e32 v172, v172\n"
    "v_mfma_f32_16x16x32_bf16 v[40:43], a[184:187], v[180:183], v[40:43]\n"
    "s_waitcnt lgkmcnt(6)\n"
    "v_exp_f32_e32 v173, v173\n"
    "v_mfma_f32_16x16x32_bf16 v[60:63], a[160:163], v[180:183], v[60:63]\n"
    "ds_read_b64_tr_b16 a[182:183], v246 offset:47104\n"
    "v_cvt_pk_bf16_f32 v165, v166, v167\n"
    "v_mfma_f32_16x16x32_bf16 v[60:63], a[164:167], v[184:187], v[60:63]\n"
    "ds_read_b64_tr_b16 a[184:185], v247 offset:12288\n"
    "v_mfma_f32_16x16x32_bf16 v[64:67], a[164:167], v[192:195], v[64:67]\n"
    "ds_read_b64_tr_b16 a[186:187], v247 offset:14336\n"
    "v_mfma_f32_16x16x32_bf16 v[64:67], a[160:163], v[188:191], v[64:67]\n"
    "v_exp_f32_e32 v174, v174\n"
    "v_mfma_f32_16x16x32_bf16 v[68:71], a[160:163], v[196:199], v[68:71]\n"
    "v_exp_f32_e32 v175, v175\n"
    "v_mfma_f32_16x16x32_bf16 v[68:71], a[164:167], v[200:203], v[68:71]\n"
    "ds_read_b64_tr_b16 a[188:189], v247 offset:45056\n"
    "v_cvt_pk_bf16_f32 v172, v172, v173\n"
    "v_mfma_f32_16x16x32_bf16 v[72:75], a[164:167], v[208:211], v[72:75]\n"
    "ds_read_b64_tr_b16 a[190:191], v247 offset:47104\n"
    "v_cvt_pk_bf16_f32 v173, v174, v175\n"
    "v_mfma_f32_16x16x32_bf16 v[72:75], a[160:163], v[204:207], v[72:75]\n"
    "s_waitcnt vmcnt(30)\n"
    "v_mfma_f32_16x16x32_bf16 v[76:79], a[160:163], v[212:215], v[76:79]\n"
    "s_barrier\n"
    "v_mfma_f32_16x16x32_bf16 v[76:79], a[164:167], v[216:219], v[76:79]\n"
    "s_waitcnt lgkmcnt(8)\n"
    "s_mov_b32 m0, s42\n"
    "v_mfma_f32_16x16x32_bf16 v[96:99], a[172:175], v[216:219], v[96:99]\n"
    "v_exp_f32_e32 v144, v144\n"
    "v_mfma_f32_16x16x32_bf16 v[96:99], a[168:171], v[212:215], v[96:99]\n"
    "v_exp_f32_e32 v145, v145\n"
    "v_mfma_f32_16x16x32_bf16 v[92:95], a[168:171], v[204:207], v[92:95]\n"
    "v_exp_f32_e32 v146, v146\n"
    "v_mfma_f32_16x16x32_bf16 v[92:95], a[172:175], v[208:211], v[92:95]\n"
    "ds_read_b128 a[160:163], v240 offset:24576\n"
    "ds_write_addtid_b32 v140 offset:0\n"
    "v_mfma_f32_16x16x32_bf16 v[88:91], a[172:175], v[200:203], v[88:91]\n"
    "v_exp_f32_e32 v147, v147\n"
    "v_mfma_f32_16x16x32_bf16 v[88:91], a[168:171], v[196:199], v[88:91]\n"
    "ds_write_addtid_b32 v141 offset:256\n"
    "v_cvt_pk_bf16_f32 v142, v144, v145\n"
    "v_mfma_f32_16x16x32_bf16 v[84:87], a[168:171], v[188:191], v[84:87]\n"
    "v_exp_f32_e32 v152, v152\n"
    "v_mfma_f32_16x16x32_bf16 v[84:87], a[172:175], v[192:195], v[84:87]\n"
    "v_exp_f32_e32 v153, v153\n"
    "v_mfma_f32_16x16x32_bf16 v[80:83], a[172:175], v[184:187], v[80:83]\n"
    "ds_write_addtid_b32 v148 offset:1024\n"
    "v_cvt_pk_bf16_f32 v143, v146, v147\n"
    "v_mfma_f32_16x16x32_bf16 v[80:83], a[168:171], v[180:183], v[80:83]\n"
    "s_waitcnt vmcnt(20)\n"
    "ds_read_b128 a[164:167], v241 offset:24576\n"
    "ds_write_addtid_b32 v149 offset:1280\n"
    "v_smfmac_f32_16x16x64_bf16 v[224:227], a[232:235], a[224:231], v238\n"
    "s_waitcnt lgkmcnt(10)\n"
    "ds_write_addtid_b32 v156 offset:2048\n"
    "v_mad_u32_u16 v234, v232, s90, v239 op_sel:[0,0,0,0]\n"
    "v_mfma_f32_16x16x32_bf16 v[100:103], a[176:179], v[180:183], v[100:103]\n"
    "s_bitcmp1_b32 s86, 19\n"
    "buffer_load_dwordx4 a[224:227], v234, s[20:23], 0 offen sc1\n"
    "v_mad_u32_u16 v235, v232, s90, v239 op_sel:[1,0,0,0]\n"
    ".long 0xbedf00ff\n"
    ".Lat_w8:\n"
    "s_setpc_b64 s[2:3]\n"
    "v_mfma_f32_16x16x32_bf16 v[100:103], a[180:183], v[184:187], v[100:103]\n"
    "s_cselect_b64 exec, -1, 0\n"
    "v_cvt_pk_bf16_f32 v236, v224, v225\n"
    "v_cvt_pk_bf16_f32 v237, v226, v227\n"
    "v_mfma_f32_16x16x32_bf16 v[104:107], a[180:183], v[192:195], v[104:107]\n"
    "s_cselect_b32 s88, 512, 0\n"
    "v_mov_b64_e32 v[224:225], 0\n"
    "v_mov_b64_e32 v[226:227], 0\n"
    "v_mfma_f32_16x16x32_bf16 v[104:107], a[176:179], v[188:191], v[104:107]\n"
    "buffer_store_dwordx2 v[236:237], v252, s[8:11], s87 offen sc1\n"
    "v_mfma_f32_16x16x32_bf16 v[108:111], a[176:179], v[196:199], v[108:111]\n"
    "s_mov_b64 exec, -1\n"
    "ds_read_b128 a[168:171], v240 offset:28672\n"
    "ds_write_addtid_b32 v157 offset:2304\n"
    "v_mfma_f32_16x16x32_bf16 v[108:111], a[180:183], v[200:203], v[108:111]\n"
    "s_add_u32 s87, s87, s88\n"
    "v_exp_f32_e32 v154, v154\n"
    "v_mfma_f32_16x16x32_bf16 v[112:115], a[180:183], v[208:211], v[112:115]\n"
    "s_add_u32 s89, s89, 1\n"
    "buffer_load_dwordx4 a[228:231], v235, s[20:23], 0 offen sc1\n"
    "ds_write_addtid_b32 v164 offset:3072\n"
    "v_mfma_f32_16x16x32_bf16 v[112:115], a[176:179], v[204:207], v[112:115]\n"
    "ds_write_addtid_b32 v165 offset:3328\n"
    "v_readlane_b32 s86, v254, s89\n"
    "v_mfma_f32_16x16x32_bf16 v[116:119], a[176:179], v[212:215], v[116:119]\n"
    "v_exp_f32_e32 v155, v155\n"
    "v_mfma_f32_16x16x32_bf16 v[116:119], a[180:183], v[216:219], v[116:119]\n"
    "s_waitcnt lgkmcnt(11)\n"
    "ds_read_b128 a[172:175], v241 offset:28672\n"
    "v_cvt_pk_bf16_f32 v150, v152, v153\n"
    "v_mfma_f32_16x16x32_bf16 v[136:139], a[188:191], v[216:219], v[136:139]\n"
    "v_exp_f32_e32 v160, v160\n"
    "v_mfma_f32_16x16x32_bf16 v[136:139], a[184:187], v[212:215], v[136:139]\n"
    "v_exp_f32_e32 v161, v161\n"
    "v_mfma_f32_16x16x32_bf16 v[132:135], a[184:187], v[204:207], v[132:135]\n"
    "ds_read_b128 a[176:179], v242 offset:24576\n"
    "v_cvt_pk_bf16_f32 v151, v154, v155\n"
    "v_mfma_f32_16x16x32_bf16 v[132:135], a[188:191], v[208:211], v[132:135]\n"
    "v_exp_f32_e32 v162, v162\n"
    "v_mfma_f32_16x16x32_bf16 v[128:131], a[188:191], v[200:203], v[128:131]\n"
    "v_exp_f32_e32 v163, v163\n"
    "v_mfma_f32_16x16x32_bf16 v[128:131], a[184:187], v[196:199], v[128:131]\n"
    "ds_write_addtid_b32 v172 offset:4096\n"
    "v_cvt_pk_bf16_f32 v158, v160, v161\n"
    "v_mfma_f32_16x16x32_bf16 v[124:127], a[184:187], v[188:191], v[124:127]\n"
    "v_exp_f32_e32 v168, v168\n"
    "v_mfma_f32_16x16x32_bf16 v[124:127], a[188:191], v[192:195], v[124:127]\n"
    "s_sub_u32 s44, s44, 1\n"
    "ds_read_b128 a[180:183], v243 offset:24576\n"
    "ds_write_addtid_b32 v173 offset:4352\n"
    "v_mfma_f32_16x16x32_bf16 v[120:123], a[188:191], v[184:187], v[120:123]\n"
    "s_cmp_lg_u32 s44, s80\n"
    "v_exp_f32_e32 v169, v169\n"
    "v_mfma_f32_16x16x32_bf16 v[120:123], a[184:187], v[180:183], v[120:123]\n"
    "s_waitcnt lgkmcnt(11)\n"
    "ds_write_addtid_b32 v142 offset:512\n"
    "v_cvt_pk_bf16_f32 v159, v162, v163\n"
    "s_cbranch_scc1 .Lat_gloop\n"
    ".Lat_g2:\n"
    "v_mov_b64_e32 v[224:225], v[220:221]\n"
    "v_mov_b64_e32 v[226:227], v[220:221]\n"
    "v_mov_b64_e32 v[228:229], v[220:221]\n"
    "v_mov_b64_e32 v[230:231], v[220:221]\n"
    "v_mov_b64_e32 v[232:233], v[220:221]\n"
    "v_mov_b64_e32 v[234:235], v[220:221]\n"
    "v_mov_b64_e32 v[236:237], v[220:221]\n"
    "v_mov_b64_e32 v[238:239], v[220:221]\n"
    "s_mov_b32 s80, 0\n"
    "s_waitcnt lgkmcnt(0)\n"
    "s_cmp_lg_u32 s44, 0\n"
    "s_cbranch_scc1 .Lat_loop\n"
    ".Lat_tail:\n"
    "v_mfma_f32_16x16x32_bf16 v[212:215], a[160:163], a[128:131], v[236:239]\n"
    ".long 0xbedf00ff\n"
    ".Lat_w9:\n"
    "s_setpc_b64 s[2:3]\n"
    "ds_read_b128 a[184:187], v242 offset:28672\n"
    "v_and_b32_e32 v189, 0x1f0, v250\n"
    "v_lshlrev_b32_e32 v190, 3, v250\n"
    "v_mfma_f32_16x16x32_bf16 v[212:215], a[164:167], a[132:135], v[212:215]\n"
    "ds_write_addtid_b32 v143 offset:768\n"
    "v_lshrrev_b32_e32 v253, 4, v250\n"
    "v_mfma_f32_16x16x32_bf16 v[204:207], a[164:167], a[100:103], v[232:235]\n"
    "s_mov_b32 s52, 6100992\n"
    "v_exp_f32_e32 v170, v170\n"
    "v_bfe_u32 v251, v253, 4, 2\n"
    "v_mfma_f32_16x16x32_bf16 v[204:207], a[160:163], a[96:99], v[204:207]\n"
    "s_lshr_b32 s81, s34, 6\n"
    "v_exp_f32_e32 v171, v171\n"
    "v_mfma_f32_16x16x32_bf16 v[196:199], a[160:163], a[64:67], v[228:231]\n"
    "s_lshl_b32 s81, s81, 9\n"
    "v_cvt_pk_bf16_f32 v166, v168, v169\n"
    "v_lshlrev_b32_e32 v251, 1, v251\n"
    "v_xor_b32_e32 v251, v251, v253\n"
    "v_mfma_f32_16x16x32_bf16 v[196:199], a[164:167], a[68:71], v[196:199]\n"
    "s_waitcnt lgkmcnt(8)\n"
    "ds_read_b128 a[188:191], v243 offset:28672\n"
    "v_and_b32_e32 v251, 7, v251\n"
    "v_mfma_f32_16x16x32_bf16 v[196:199], a[168:171], a[72:75], v[196:199]\n"
    "s_add_u32 s81, s81, 6074368\n"
    "v_exp_f32_e32 v176, v176\n"
    "v_mfma_f32_16x16x32_bf16 v[196:199], a[172:175], a[76:79], v[196:199]\n"
    "s_lshl_b32 s63, s93, 13\n"
    "v_exp_f32_e32 v177, v177\n"
    "v_mfma_f32_16x16x32_bf16 v[204:207], a[172:175], a[108:111], v[204:207]\n"
    "s_xor_b32 s45, s63, 0x4000\n"
    "ds_read_b128 a[160:163], v240 offset:26624\n"
    "v_lshlrev_b32_e32 v251, 4, v251\n"
    "v_lshrrev_b32_e32 v253, 3, v253\n"
    "v_mfma_f32_16x16x32_bf16 v[204:207], a[168:171], a[104:107], v[204:207]\n"
    "s_mul_i32 s58, s93, 33792\n"
    "ds_write_addtid_b32 v150 offset:1536\n"
    "v_lshl_add_u32 v251, v253, 9, v251\n"
    "v_mfma_f32_16x16x32_bf16 v[212:215], a[168:171], a[136:139], v[212:215]\n"
    "s_add_u32 s57, s58, 5513216\n"
    "ds_write_addtid_b32 v151 offset:1792\n"
    "v_bfe_u32 v255, v250, 2, 4\n"
    "v_mfma_f32_16x16x32_bf16 v[212:215], a[172:175], a[140:143], v[212:215]\n"
    "s_waitcnt lgkmcnt(7)\n"
    "ds_read_b128 a[168:171], v240 offset:30720\n"
    "v_mov_b32_e32 v254, s39\n"
    "v_mfma_f32_16x16x32_bf16 v[212:215], a[176:179], a[144:147], v[212:215]\n"
    "s_add_u32 s58, s58, 5378048\n"
    "ds_read_b128 a[164:167], v241 offset:26624\n"
    "v_lshrrev_b32_e32 v252, 2, v250\n"
    "v_mfma_f32_16x16x32_bf16 v[212:215], a[180:183], a[148:151], v[212:215]\n"
    "s_add_u32 s36, s35, 0x8000\n"
    "ds_write_addtid_b32 v158 offset:2560\n"
    "v_cvt_pk_bf16_f32 v167, v170, v171\n"
    "v_mfma_f32_16x16x32_bf16 v[204:207], a[180:183], a[116:119], v[204:207]\n"
    "s_add_u32 s37, s35, 0x10000\n"
    "v_exp_f32_e32 v178, v178\n"
    "v_mfma_f32_16x16x32_bf16 v[204:207], a[176:179], a[112:115], v[204:207]\n"
    "s_cmp_lt_u32 s32, 2\n"
    "s_cselect_b32 s37, s37, 2.0\n"
    "ds_read_b128 a[172:175], v241 offset:30720\n"
    "v_mfma_f32_16x16x32_bf16 v[196:199], a[176:179], a[80:83], v[196:199]\n"
    "s_cmp_lt_u32 s36, 5120000\n"
    "s_cselect_b32 s36, s36, 2.0\n"
    "ds_write_addtid_b32 v159 offset:2816\n"
    "v_mfma_f32_16x16x32_bf16 v[196:199], a[180:183], a[84:87], v[196:199]\n"
    "s_waitcnt lgkmcnt(8)\n"
    "v_exp_f32_e32 v179, v179\n"
    "v_mfma_f32_16x16x32_bf16 v[196:199], a[184:187], a[88:91], v[196:199]\n"
    "s_cmp_lt_u32 s37, 5120000\n"
    "s_cselect_b32 s37, s37, 2.0\n"
    "ds_read_b128 a[176:179], v242 offset:26624\n"
    "v_mfma_f32_16x16x32_bf16 v[196:199], a[188:191], a[92:95], v[196:199]\n"
    "buffer_load_dword v253, v255, s[8:11], s52 offen offset:0 sc1\n"
    "v_mfma_f32_16x16x32_bf16 v[204:207], a[188:191], a[124:127], v[204:207]\n"
    "ds_read_b128 a[180:183], v243 offset:26624\n"
    "v_cvt_pk_bf16_f32 v174, v176, v177\n"
    "v_mfma_f32_16x16x32_bf16 v[204:207], a[184:187], a[120:123], v[204:207]\n"
    "ds_write_addtid_b32 v166 offset:3584\n"
    "v_cvt_pk_bf16_f32 v175, v178, v179\n"
    "v_mfma_f32_16x16x32_bf16 v[212:215], a[184:187], a[152:155], v[212:215]\n"
    "v_exp_f32_e32 v196, v196\n"
    "v_mfma_f32_16x16x32_bf16 v[212:215], a[188:191], a[156:159], v[212:215]\n"
    "s_waitcnt lgkmcnt(6)\n"
    "ds_write_addtid_b32 v167 offset:3840\n"
    "v_mfma_f32_16x16x32_bf16 v[216:219], a[160:163], a[128:131], v[236:239]\n"
    "ds_write_addtid_b32 v174 offset:4608\n"
    "ds_write_addtid_b32 v175 offset:4864\n"
    "v_mfma_f32_16x16x32_bf16 v[216:219], a[164:167], a[132:135], v[216:219]\n"
    "ds_read_b128 a[184:187], v242 offset:30720\n"
    "v_mfma_f32_16x16x32_bf16 v[208:211], a[164:167], a[100:103], v[232:235]\n"
    "ds_read_b128 a[188:191], v243 offset:30720\n"
    "v_mfma_f32_16x16x32_bf16 v[208:211], a[160:163], a[96:99], v[208:211]\n"
    "s_waitcnt lgkmcnt(2)\n"
    "v_mfma_f32_16x16x32_bf16 v[200:203], a[160:163], a[64:67], v[228:231]\n"
    "s_barrier\n"
    "v_mfma_f32_16x16x32_bf16 v[200:203], a[164:167], a[68:71], v[200:203]\n"
    "ds_read_b64_tr_b16 a[160:161], v244 offset:16384\n"
    "ds_read2st64_b32 v[178:179], v249 offset0:18 offset1:19\n"
    "v_mfma_f32_16x16x32_bf16 v[200:203], a[168:171], a[72:75], v[200:203]\n"
    "s_add_u32 m0, s43, 8192\n"
    "ds_read_b64_tr_b16 a[162:163], v244 offset:18432\n"
    "v_mfma_f32_16x16x32_bf16 v[200:203], a[172:175], a[76:79], v[200:203]\n"
    "buffer_load_dwordx4 v248, s[8:11], s40 offen offset:0 sc1 lds\n"
    "v_mfma_f32_16x16x32_bf16 v[208:211], a[172:175], a[108:111], v[208:211]\n"
    "v_exp_f32_e32 v197, v197\n"
    "v_mfma_f32_16x16x32_bf16 v[208:211], a[168:171], a[104:107], v[208:211]\n"
    "ds_read2st64_b32 v[176:177], v249 offset0:16 offset1:17\n"
    "v_mfma_f32_16x16x32_bf16 v[216:219], a[168:171], a[136:139], v[216:219]\n"
    "ds_read2st64_b32 v[170:171], v249 offset0:14 offset1:15\n"
    "v_mfma_f32_16x16x32_bf16 v[216:219], a[172:175], a[140:143], v[216:219]\n"
    "ds_read_b64_tr_b16 a[164:165], v244 offset:49152\n"
    "v_mfma_f32_16x16x32_bf16 v[216:219], a[176:179], a[144:147], v[216:219]\n"
    "buffer_load_dwordx4 v248, s[8:11], s40 offen offset:1024 sc1 lds\n"
    "v_mfma_f32_16x16x32_bf16 v[216:219], a[180:183], a[148:151], v[216:219]\n"
    "ds_read2st64_b32 v[162:163], v249 offset0:10 offset1:11\n"
    "ds_read2st64_b32 v[168:169], v249 offset0:12 offset1:13\n"
    "v_mfma_f32_16x16x32_bf16 v[208:211], a[180:183], a[116:119], v[208:211]\n"
    "ds_read_b64_tr_b16 a[166:167], v244 offset:51200\n"
    "v_mfma_f32_16x16x32_bf16 v[208:211], a[176:179], a[112:115], v[208:211]\n"
    "ds_read2st64_b32 v[154:155], v249 offset0:6 offset1:7\n"
    "ds_read2st64_b32 v[160:161], v249 offset0:8 offset1:9\n"
    "v_mfma_f32_16x16x32_bf16 v[200:203], a[176:179], a[80:83], v[200:203]\n"
    "ds_read2st64_b32 v[146:147], v249 offset0:2 offset1:3\n"
    "ds_read2st64_b32 v[152:153], v249 offset0:4 offset1:5\n"
    "v_mfma_f32_16x16x32_bf16 v[200:203], a[180:183], a[84:87], v[200:203]\n"
    "s_waitcnt lgkmcnt(13)\n"
    "ds_read2st64_b32 v[144:145], v249 offset0:0 offset1:1\n"
    "v_mfma_f32_16x16x32_bf16 v[200:203], a[184:187], a[88:91], v[200:203]\n"
    "buffer_load_dwordx4 v248, s[8:11], s40 offen offset:2048 sc1 lds\n"
    "v_mfma_f32_16x16x32_bf16 v[200:203], a[188:191], a[92:95], v[200:203]\n"
    "v_exp_f32_e32 v198, v198\n"
    "v_mfma_f32_16x16x32_bf16 v[208:211], a[188:191], a[124:127], v[208:211]\n"
    "ds_read_b64_tr_b16 a[176:177], v246 offset:16384\n"
    "v_mfma_f32_16x16x32_bf16 v[208:211], a[184:187], a[120:123], v[208:211]\n"
    "ds_read_b64_tr_b16 a[168:169], v245 offset:16384\n"
    "v_mfma_f32_16x16x32_bf16 v[216:219], a[184:187], a[152:155], v[216:219]\n"
    "ds_read_b64_tr_b16 a[170:171], v245 offset:18432\n"
    "v_mfma_f32_16x16x32_bf16 v[216:219], a[188:191], a[156:159], v[216:219]\n"
    "s_waitcnt lgkmcnt(3)\n"
    "ds_read_b64_tr_b16 a[184:185], v247 offset:16384\n"
    "v_mfma_f32_16x16x32_bf16 a[252:255], a[160:163], v[172:175], a[252:255]\n"
    "buffer_load_dwordx4 v248, s[8:11], s40 offen offset:3072 sc1 lds\n"
    "v_mfma_f32_16x16x32_bf16 a[252:255], a[164:167], v[176:179], a[252:255]\n"
    "s_add_u32 s40, s40, 0x8000\n"
    "ds_read_b64_tr_b16 a[172:173], v245 offset:49152\n"
    "v_mfma_f32_16x16x32_bf16 a[248:251], a[164:167], v[168:171], a[248:251]\n"
    "s_add_u32 m0, s43, 12288\n"
    "ds_read_b64_tr_b16 a[174:175], v245 offset:51200\n"
    "v_mfma_f32_16x16x32_bf16 a[248:251], a[160:163], v[164:167], a[248:251]\n"
    "v_exp_f32_e32 v199, v199\n"
    "v_mfma_f32_16x16x32_bf16 a[244:247], a[160:163], v[156:159], a[244:247]\n"
    "ds_read_b64_tr_b16 a[186:187], v247 offset:18432\n"
    "v_mfma_f32_16x16x32_bf16 a[244:247], a[164:167], v[160:163], a[244:247]\n"
    "buffer_load_dwordx4 v248, s[8:11], s41 offen offset:0 sc1 lds\n"
    "v_mfma_f32_16x16x32_bf16 a[240:243], a[164:167], v[152:155], a[240:243]\n"
    "ds_read_b64_tr_b16 a[178:179], v246 offset:18432\n"
    "v_mfma_f32_16x16x32_bf16 a[240:243], a[160:163], v[148:151], a[240:243]\n"
    "ds_read_b64_tr_b16 a[188:189], v247 offset:49152\n"
    "v_mfma_f32_16x16x32_bf16 a[236:239], a[160:163], v[140:143], a[236:239]\n"
    "ds_read_b64_tr_b16 a[180:181], v246 offset:49152\n"
    "v_mfma_f32_16x16x32_bf16 a[236:239], a[164:167], v[144:147], a[236:239]\n"
    "s_waitcnt lgkmcnt(4)\n"
    "ds_read_b64_tr_b16 a[182:183], v246 offset:51200\n"
    "v_cvt_pk_bf16_f32 v196, v196, v197\n"
    "v_mfma_f32_16x16x32_bf16 v[0:3], a[168:171], v[140:143], v[0:3]\n"
    "buffer_load_dwordx4 v248, s[8:11], s41 offen offset:1024 sc1 lds\n"
    "v_mfma_f32_16x16x32_bf16 v[0:3], a[172:175], v[144:147], v[0:3]\n"
    "ds_read_b64_tr_b16 a[190:191], v247 offset:51200\n"
    "v_mfma_f32_16x16x32_bf16 v[4:7], a[172:175], v[152:155], v[4:7]\n"
    "v_exp_f32_e32 v204, v204\n"
    "v_mfma_f32_16x16x32_bf16 v[4:7], a[168:171], v[148:151], v[4:7]\n"
    "ds_read_b64_tr_b16 a[160:161], v244 offset:20480\n"
    "v_subrev_f32_e32 v0, s4, v0\n"
    "v_mfma_f32_16x16x32_bf16 v[8:11], a[168:171], v[156:159], v[8:11]\n"
    "v_exp_f32_e32 v205, v205\n"
    "v_mfma_f32_16x16x32_bf16 v[8:11], a[172:175], v[160:163], v[8:11]\n"
    "buffer_load_dwordx4 v248, s[8:11], s41 offen offset:2048 sc1 lds\n"
    "v_mfma_f32_16x16x32_bf16 v[12:15], a[172:175], v[168:171], v[12:15]\n"
    "ds_read_b64_tr_b16 a[162:163], v244 offset:22528\n"
    "v_cvt_pk_bf16_f32 v197, v198, v199\n"
    "v_mfma_f32_16x16x32_bf16 v[12:15], a[168:171], v[164:167], v[12:15]\n"
    "s_waitcnt vmcnt(7)\n"
    "v_mfma_f32_16x16x32_bf16 v[16:19], a[168:171], v[172:175], v[16:19]\n"
    "s_barrier\n"
    "v_mfma_f32_16x16x32_bf16 v[16:19], a[172:175], v[176:179], v[16:19]\n"
    "s_waitcnt lgkmcnt(3)\n"
    "v_add_u32_e32 v249, s45, v252\n"
    "v_add_u32_e32 v252, s63, v252\n"
    "v_mfma_f32_16x16x32_bf16 v[36:39], a[176:179], v[172:175], v[36:39]\n"
    "buffer_store_dword v254, off, s[8:11], s84 offset:0 sc1\n"
    "v_mfma_f32_16x16x32_bf16 v[36:39], a[180:183], v[176:179], v[36:39]\n"
    "s_mul_i32 s45, s49, 63\n"
    "ds_read_b64_tr_b16 a[164:165], v244 offset:53248\n"
    "v_subrev_f32_e32 v12, s4, v12\n"
    "v_mfma_f32_16x16x32_bf16 v[32:35], a[180:183], v[168:171], v[32:35]\n"
    "s_add_u32 s45, s45, s33\n"
    "ds_read_b64_tr_b16 a[168:169], v245 offset:20480\n"
    "v_subrev_f32_e32 v16, s4, v16\n"
    "v_mfma_f32_16x16x32_bf16 v[32:35], a[176:179], v[164:167], v[32:35]\n"
    "buffer_load_dwordx4 v248, s[8:11], s41 offen offset:3072 sc1 lds\n"
    "v_mfma_f32_16x16x32_bf16 v[28:31], a[176:179], v[156:159], v[28:31]\n"
    "s_add_u32 s41, s41, 0x8000\n"
    "ds_read_b64_tr_b16 a[166:167], v244 offset:55296\n"
    "v_cmp_ne_u32_e32 vcc, s39, v253\n"
    "v_mfma_f32_16x16x32_bf16 v[28:31], a[180:183], v[160:163], v[28:31]\n"
    "s_mov_b32 m0, s63\n"
    "v_subrev_f32_e32 v36, s4, v36\n"
    "v_subrev_f32_e32 v32, s4, v32\n"
    "v_mfma_f32_16x16x32_bf16 v[24:27], a[180:183], v[152:155], v[24:27]\n"
    "s_mul_i32 s45, s45, 405504\n"
    "ds_read_b64_tr_b16 a[170:171], v245 offset:22528\n"
    "v_subrev_f32_e32 v37, s4, v37\n"
    "v_mfma_f32_16x16x32_bf16 v[24:27], a[176:179], v[148:151], v[24:27]\n"
    "s_cbranch_vccnz .Lat_wpoll\n"
    ".Lat_wpoll_ret:\n"
    "v_mfma_f32_16x16x32_bf16 v[20:23], a[176:179], v[140:143], v[20:23]\n"
    "s_add_u32 s62, s57, 0\n"
    "v_exp_f32_e32 v206, v206\n"
    "v_mfma_f32_16x16x32_bf16 v[20:23], a[180:183], v[144:147], v[20:23]\n"
    "s_waitcnt lgkmcnt(6)\n"
    "ds_read_b64_tr_b16 a[172:173], v245 offset:53248\n"
    "v_subrev_f32_e32 v28, s4, v28\n"
    "v_mfma_f32_16x16x32_bf16 v[40:43], a[184:187], v[140:143], v[40:43]\n"
    "buffer_load_dwordx4 a[216:219], v250, s[8:11], s62 offen offset:0 sc1\n"
    "v_mfma_f32_16x16x32_bf16 v[40:43], a[188:191], v[144:147], v[40:43]\n"
    "s_add_u32 s45, s45, 16588800\n"
    "ds_write_addtid_b32 v196 offset:0\n"
    "v_subrev_f32_e32 v8, s4, v8\n"
    "v_mfma_f32_16x16x32_bf16 v[44:47], a[188:191], v[152:155], v[44:47]\n"
    "s_lshl_b32 s46, s51, 12\n"
    "v_subrev_f32_e32 v9, s4, v9\n"
    "v_subrev_f32_e32 v10, s4, v10\n"
    "v_mfma_f32_16x16x32_bf16 v[44:47], a[184:187], v[148:151], v[44:47]\n"
    "s_lshl_b32 s48, s50, 15\n"
    "ds_read_b64_tr_b16 a[176:177], v246 offset:20480\n"
    "v_subrev_f32_e32 v40, s4, v40\n"
    "v_mfma_f32_16x16x32_bf16 v[48:51], a[184:187], v[156:159], v[48:51]\n"
    "s_add_u32 s46, s46, s48\n"
    "v_exp_f32_e32 v207, v207\n"
    "v_mfma_f32_16x16x32_bf16 v[48:51], a[188:191], v[160:163], v[48:51]\n"
    "buffer_load_dwordx4 a[220:223], v250, s[8:11], s62 offen offset:1024 sc1\n"
    "v_mfma_f32_16x16x32_bf16 v[52:55], a[188:191], v[168:171], v[52:55]\n"
    "s_cmp_eq_u32 s51, 0\n"
    "s_cselect_b32 s47, 2.0, 0\n"
    "ds_read_b64_tr_b16 a[178:179], v246 offset:22528\n"
    "v_mfma_f32_16x16x32_bf16 v[52:55], a[184:187], v[164:167], v[52:55]\n"
    "s_add_u32 s47, s47, 98304\n"
    "ds_read_b64_tr_b16 a[174:175], v245 offset:55296\n"
    "v_cvt_pk_bf16_f32 v204, v204, v205\n"
    "v_mfma_f32_16x16x32_bf16 v[56:59], a[184:187], v[172:175], v[56:59]\n"
    "s_lshl_b32 s61, s50, 9\n"
    "ds_read_b64_tr_b16 a[180:181], v246 offset:53248\n"
    "v_mfma_f32_16x16x32_bf16 v[56:59], a[188:191], v[176:179], v[56:59]\n"
    "s_waitcnt lgkmcnt(7)\n"
    "ds_write_addtid_b32 v197 offset:256\n"
    "v_subrev_f32_e32 v48, s4, v48\n"
    "v_mfma_f32_16x16x32_bf16 v[76:79], a[160:163], v[172:175], v[76:79]\n"
    "buffer_load_dwordx4 a[224:227], v250, s[8:11], s62 offen offset:2048 sc1\n"
    "v_mfma_f32_16x16x32_bf16 v[76:79], a[164:167], v[176:179], v[76:79]\n"
    "s_add_u32 s61, s61, s47\n"
    "v_exp_f32_e32 v212, v212\n"
    "v_mfma_f32_16x16x32_bf16 v[72:75], a[164:167], v[168:171], v[72:75]\n"
    "s_add_u32 s48, s32, 3\n"
    "ds_read_b64_tr_b16 a[184:185], v247 offset:20480\n"
    "v_subrev_f32_e32 v29, s4, v29\n"
    "v_mfma_f32_16x16x32_bf16 v[72:75], a[160:163], v[164:167], v[72:75]\n"
    "s_and_b32 s48, s48, 3\n"
    "v_subrev_f32_e32 v4, s4, v4\n"
    "v_mfma_f32_16x16x32_bf16 v[68:71], a[160:163], v[156:159], v[68:71]\n"
    "s_mul_i32 s48, s48, 101376\n"
    "v_exp_f32_e32 v213, v213\n"
    "v_mfma_f32_16x16x32_bf16 v[68:71], a[164:167], v[160:163], v[68:71]\n"
    "buffer_load_dwordx4 a[228:231], v250, s[8:11], s62 offen offset:3072 sc1\n"
    "v_mfma_f32_16x16x32_bf16 v[64:67], a[164:167], v[152:155], v[64:67]\n"
    "s_add_u32 s62, s57, 4096\n"
    "ds_read_b64_tr_b16 a[186:187], v247 offset:22528\n"
    "v_subrev_f32_e32 v76, s4, v76\n"
    "v_mfma_f32_16x16x32_bf16 v[64:67], a[160:163], v[148:151], v[64:67]\n"
    "s_add_u32 s48, s48, s45\n"
    "v_subrev_f32_e32 v33, s4, v33\n"
    "v_subrev_f32_e32 v52, s4, v52\n"
    "v_mfma_f32_16x16x32_bf16 v[60:63], a[160:163], v[140:143], v[60:63]\n"
    "s_add_u32 s29, s48, s46\n"
    "ds_read_b64_tr_b16 a[182:183], v246 offset:55296\n"
    "v_cvt_pk_bf16_f32 v205, v206, v207\n"
    "v_mfma_f32_16x16x32_bf16 v[60:63], a[164:167], v[144:147], v[60:63]\n"
    "s_waitcnt lgkmcnt(5)\n"
    "ds_read_b64_tr_b16 a[188:189], v247 offset:53248\n"
    "v_subrev_f32_e32 v68, s4, v68\n"
    "v_mfma_f32_16x16x32_bf16 v[80:83], a[168:171], v[140:143], v[80:83]\n"
    "buffer_load_dwordx4 a[232:235], v250, s[8:11], s62 offen offset:0 sc1\n"
    "v_mfma_f32_16x16x32_bf16 v[80:83], a[172:175], v[144:147], v[80:83]\n"
    "s_addk_i32 s29, 8192\n"
    "v_exp_f32_e32 v214, v214\n"
    "v_mfma_f32_16x16x32_bf16 v[84:87], a[172:175], v[152:155], v[84:87]\n"
    "s_add_u32 s89, s48, s61\n"
    "v_exp_f32_e32 v215, v215\n"
    "v_mfma_f32_16x16x32_bf16 v[84:87], a[168:171], v[148:151], v[84:87]\n"
    "s_addk_i32 s89, 128\n"
    "ds_write_addtid_b32 v204 offset:1024\n"
    "v_subrev_f32_e32 v38, s4, v38\n"
    "v_mfma_f32_16x16x32_bf16 v[88:91], a[168:171], v[156:159], v[88:91]\n"
    "s_add_u32 s48, s32, 2\n"
    "ds_read_b64_tr_b16 a[190:191], v247 offset:55296\n"
    "v_cvt_pk_bf16_f32 v212, v212, v213\n"
    "v_mfma_f32_16x16x32_bf16 v[88:91], a[172:175], v[160:163], v[88:91]\n"
    "buffer_load_dwordx4 v[192:195], v250, s[8:11], s62 offen offset:1024 sc1\n"
    "v_mfma_f32_16x16x32_bf16 v[92:95], a[172:175], v[168:171], v[92:95]\n"
    "s_and_b32 s48, s48, 3\n"
    "ds_write_addtid_b32 v205 offset:1280\n"
    "v_subrev_f32_e32 v17, s4, v17\n"
    "v_mfma_f32_16x16x32_bf16 v[92:95], a[168:171], v[164:167], v[92:95]\n"
    "s_mul_i32 s48, s48, 101376\n"
    "v_exp_f32_e32 v200, v200\n"
    "v_mfma_f32_16x16x32_bf16 v[96:99], a[168:171], v[172:175], v[96:99]\n"
    "s_add_u32 s48, s48, s45\n"
    "v_exp_f32_e32 v201, v201\n"
    "v_mfma_f32_16x16x32_bf16 v[96:99], a[172:175], v[176:179], v[96:99]\n"
    "s_waitcnt lgkmcnt(4)\n"
    "ds_read_b128 a[160:163], v240 offset:0\n"
    "v_cvt_pk_bf16_f32 v213, v214, v215\n"
    "v_mfma_f32_16x16x32_bf16 v[116:119], a[176:179], v[172:175], v[116:119]\n"
    "s_add_u32 s28, s48, s46\n"
    "ds_write_addtid_b32 v212 offset:2048\n"
    "v_subrev_f32_e32 v49, s4, v49\n"
    "v_mfma_f32_16x16x32_bf16 v[116:119], a[180:183], v[176:179], v[116:119]\n"
    "s_waitcnt lgkmcnt(3)\n"
    "v_mfma_f32_16x16x32_bf16 v[112:115], a[180:183], v[168:171], v[112:115]\n"
    "s_barrier\n"
    "v_mfma_f32_16x16x32_bf16 v[112:115], a[176:179], v[164:167], v[112:115]\n"
    "s_add_u32 m0, s43, 16384\n"
    "v_exp_f32_e32 v202, v202\n"
    "v_mfma_f32_16x16x32_bf16 v[108:111], a[176:179], v[156:159], v[108:111]\n"
    "buffer_load_dwordx4 v248, s[8:11], s40 offen offset:0 sc1 lds\n"
    "v_mfma_f32_16x16x32_bf16 v[108:111], a[180:183], v[160:163], v[108:111]\n"
    "s_mov_b32 m0, s63\n"
    "ds_read_b128 a[168:171], v240 offset:4096\n"
    "v_subrev_f32_e32 v72, s4, v72\n"
    "v_mfma_f32_16x16x32_bf16 v[104:107], a[180:183], v[152:155], v[104:107]\n"
    "s_addk_i32 s28, 16384\n"
    "ds_read_b128 a[164:167], v241 offset:0\n"
    "v_exp_f32_e32 v203, v203\n"
    "v_mfma_f32_16x16x32_bf16 v[104:107], a[176:179], v[148:151], v[104:107]\n"
    "s_add_u32 s88, s48, s61\n"
    "ds_read_b128 a[172:175], v241 offset:4096\n"
    "v_cvt_pk_bf16_f32 v198, v200, v201\n"
    "v_mfma_f32_16x16x32_bf16 v[100:103], a[176:179], v[140:143], v[100:103]\n"
    "s_add_u32 m0, s43, 16384\n"
    "v_exp_f32_e32 v208, v208\n"
    "v_mfma_f32_16x16x32_bf16 v[100:103], a[180:183], v[144:147], v[100:103]\n"
    "v_subrev_f32_e32 v13, s4, v13\n"
    "v_subrev_f32_e32 v108, s4, v108\n"
    "v_mfma_f32_16x16x32_bf16 v[120:123], a[184:187], v[140:143], v[120:123]\n"
    "buffer_load_dwordx4 v248, s[8:11], s40 offen offset:1024 sc1 lds\n"
    "v_mfma_f32_16x16x32_bf16 v[120:123], a[188:191], v[144:147], v[120:123]\n"
    "s_mov_b32 m0, s63\n"
    "v_subrev_f32_e32 v39, s4, v39\n"
    "v_subrev_f32_e32 v44, s4, v44\n"
    "v_mfma_f32_16x16x32_bf16 v[124:127], a[188:191], v[152:155], v[124:127]\n"
    "s_addk_i32 s88, 256\n"
    "v_exp_f32_e32 v209, v209\n"
    "v_mfma_f32_16x16x32_bf16 v[124:127], a[184:187], v[148:151], v[124:127]\n"
    "s_add_u32 s48, s32, 1\n"
    "ds_write_addtid_b32 v213 offset:2304\n"
    "v_cvt_pk_bf16_f32 v199, v202, v203\n"
    "v_mfma_f32_16x16x32_bf16 v[128:131], a[184:187], v[156:159], v[128:131]\n"
    "s_add_u32 m0, s43, 16384\n"
    "v_exp_f32_e32 v210, v210\n"
    "v_mfma_f32_16x16x32_bf16 v[128:131], a[188:191], v[160:163], v[128:131]\n"
    "buffer_load_dwordx4 v248, s[8:11], s40 offen offset:2048 sc1 lds\n"
    "v_mfma_f32_16x16x32_bf16 v[132:135], a[188:191], v[168:171], v[132:135]\n"
    "s_mov_b32 m0, s63\n"
    "v_exp_f32_e32 v211, v211\n"
    "v_mfma_f32_16x16x32_bf16 v[132:135], a[184:187], v[164:167], v[132:135]\n"
    "s_and_b32 s48, s48, 3\n"
    "ds_write_addtid_b32 v198 offset:512\n"
    "v_cvt_pk_bf16_f32 v206, v208, v209\n"
    "v_mfma_f32_16x16x32_bf16 v[136:139], a[184:187], v[172:175], v[136:139]\n"
    "s_mul_i32 s48, s48, 101376\n"
    "v_exp_f32_e32 v216, v216\n"
    "v_mfma_f32_16x16x32_bf16 v[136:139], a[188:191], v[176:179], v[136:139]\n"
    "s_waitcnt lgkmcnt(3)\n"
    "v_exp_f32_e32 v217, v217\n"
    "v_mfma_f32_16x16x32_bf16 v[172:175], a[160:163], a[128:131], v[236:239]\n"
    "s_add_u32 m0, s43, 16384\n"
    "ds_read_b128 a[176:179], v242 offset:0\n"
    "v_subrev_f32_e32 v73, s4, v73\n"
    "v_mfma_f32_16x16x32_bf16 v[172:175], a[164:167], a[132:135], v[172:175]\n"
    "buffer_load_dwordx4 v248, s[8:11], s40 offen offset:3072 sc1 lds\n"
    "v_mfma_f32_16x16x32_bf16 v[164:167], a[164:167], a[100:103], v[232:235]\n"
    "s_mov_b32 m0, s63\n"
    "ds_read_b128 a[180:183], v243 offset:0\n"
    "v_subrev_f32_e32 v18, s4, v18\n"
    "v_mfma_f32_16x16x32_bf16 v[164:167], a[160:163], a[96:99], v[164:167]\n"
    "s_add_u32 s48, s48, s45\n"
    "ds_write_addtid_b32 v199 offset:768\n"
    "v_cvt_pk_bf16_f32 v207, v210, v211\n"
    "v_mfma_f32_16x16x32_bf16 v[156:159], a[160:163], a[64:67], v[228:231]\n"
    "s_add_u32 s82, s48, s46\n"
    "ds_read_b128 a[184:187], v242 offset:4096\n"
    "v_subrev_f32_e32 v88, s4, v88\n"
    "v_mfma_f32_16x16x32_bf16 v[156:159], a[164:167], a[68:71], v[156:159]\n"
    "s_waitcnt lgkmcnt(6)\n"
    "v_exp_f32_e32 v218, v218\n"
    "v_mfma_f32_16x16x32_bf16 v[156:159], a[168:171], a[72:75], v[156:159]\n"
    "s_add_u32 m0, s43, 20480\n"
    "v_exp_f32_e32 v219, v219\n"
    "v_mfma_f32_16x16x32_bf16 v[156:159], a[172:175], a[76:79], v[156:159]\n"
    "buffer_load_dwordx4 v248, s[8:11], s41 offen offset:0 sc1 lds\n"
    "v_mfma_f32_16x16x32_bf16 v[164:167], a[172:175], a[108:111], v[164:167]\n"
    "s_mov_b32 m0, s63\n"
    "v_subrev_f32_e32 v14, s4, v14\n"
    "v_subrev_f32_e32 v19, s4, v19\n"
    "v_mfma_f32_16x16x32_bf16 v[164:167], a[168:171], a[104:107], v[164:167]\n"
    "s_addk_i32 s82, 24576\n"
    "ds_read_b128 a[188:191], v243 offset:4096\n"
    "v_subrev_f32_e32 v112, s4, v112\n"
    "v_mfma_f32_16x16x32_bf16 v[172:175], a[168:171], a[136:139], v[172:175]\n"
    "s_add_u32 s87, s48, s61\n"
    "ds_write_addtid_b32 v206 offset:1536\n"
    "v_cvt_pk_bf16_f32 v214, v216, v217\n"
    "v_mfma_f32_16x16x32_bf16 v[172:175], a[172:175], a[140:143], v[172:175]\n"
    "s_waitcnt lgkmcnt(4)\n"
    "ds_write_addtid_b32 v207 offset:1792\n"
    "v_subrev_f32_e32 v96, s4, v96\n"
    "v_mfma_f32_16x16x32_bf16 v[172:175], a[176:179], a[144:147], v[172:175]\n"
    "s_add_u32 m0, s43, 20480\n"
    "ds_read_b128 a[160:163], v240 offset:2048\n"
    "v_cvt_pk_bf16_f32 v215, v218, v219\n"
    "v_mfma_f32_16x16x32_bf16 v[172:175], a[180:183], a[148:151], v[172:175]\n"
    "buffer_load_dwordx4 v248, s[8:11], s41 offen offset:1024 sc1 lds\n"
    "v_mfma_f32_16x16x32_bf16 v[164:167], a[180:183], a[116:119], v[164:167]\n"
    "s_mov_b32 m0, s63\n"
    "ds_read_b128 a[164:167], v241 offset:2048\n"
    "v_subrev_f32_e32 v89, s4, v89\n"
    "v_mfma_f32_16x16x32_bf16 v[164:167], a[176:179], a[112:115], v[164:167]\n"
    "s_addk_i32 s87, 384\n"
    "ds_read_b128 a[168:171], v240 offset:6144\n"
    "v_subrev_f32_e32 v90, s4, v90\n"
    "v_mfma_f32_16x16x32_bf16 v[156:159], a[176:179], a[80:83], v[156:159]\n"
    "s_mul_i32 s48, s32, 101376\n"
    "ds_write_addtid_b32 v214 offset:2560\n"
    "v_subrev_f32_e32 v56, s4, v56\n"
    "v_mfma_f32_16x16x32_bf16 v[156:159], a[180:183], a[84:87], v[156:159]\n"
    "s_waitcnt lgkmcnt(6)\n"
    "ds_write_addtid_b32 v215 offset:2816\n"
    "v_subrev_f32_e32 v30, s4, v30\n"
    "v_mfma_f32_16x16x32_bf16 v[156:159], a[184:187], a[88:91], v[156:159]\n"
    "s_add_u32 m0, s43, 20480\n"
    "ds_read_b128 a[176:179], v242 offset:2048\n"
    "v_subrev_f32_e32 v77, s4, v77\n"
    "v_mfma_f32_16x16x32_bf16 v[156:159], a[188:191], a[92:95], v[156:159]\n"
    "buffer_load_dwordx4 v248, s[8:11], s41 offen offset:2048 sc1 lds\n"
    "v_mfma_f32_16x16x32_bf16 v[164:167], a[188:191], a[124:127], v[164:167]\n"
    "s_mov_b32 m0, s63\n"
    "ds_read_b128 a[172:175], v241 offset:6144\n"
    "v_subrev_f32_e32 v11, s4, v11\n"
    "v_mfma_f32_16x16x32_bf16 v[164:167], a[184:187], a[120:123], v[164:167]\n"
    "s_add_u32 s48, s48, s45\n"
    "ds_read_b128 a[180:183], v243 offset:2048\n"
    "v_subrev_f32_e32 v69, s4, v69\n"
    "v_mfma_f32_16x16x32_bf16 v[172:175], a[184:187], a[152:155], v[172:175]\n"
    "s_add_u32 s59, s48, s46\n"
    "v_exp_f32_e32 v156, v156\n"
    "v_mfma_f32_16x16x32_bf16 v[172:175], a[188:191], a[156:159], v[172:175]\n"
    "s_waitcnt lgkmcnt(6)\n"
    "v_exp_f32_e32 v157, v157\n"
    "v_mfma_f32_16x16x32_bf16 v[176:179], a[160:163], a[128:131], v[236:239]\n"
    "s_add_u32 m0, s43, 20480\n"
    "v_exp_f32_e32 v158, v158\n"
    "v_mfma_f32_16x16x32_bf16 v[176:179], a[164:167], a[132:135], v[176:179]\n"
    "buffer_load_dwordx4 v248, s[8:11], s41 offen offset:3072 sc1 lds\n"
    "v_mfma_f32_16x16x32_bf16 v[168:171], a[164:167], a[100:103], v[232:235]\n"
    "s_mov_b32 m0, s63\n"
    "ds_read_b128 a[184:187], v242 offset:6144\n"
    "ds_read_b128 a[188:191], v243 offset:6144\n"
    "v_mfma_f32_16x16x32_bf16 v[168:171], a[160:163], a[96:99], v[168:171]\n"
    "s_waitcnt lgkmcnt(5)\n"
    "s_waitcnt vmcnt(14)\n"
    "v_mfma_f32_16x16x32_bf16 v[160:163], a[160:163], a[64:67], v[228:231]\n"
    "s_barrier\n"
    "v_mfma_f32_16x16x32_bf16 v[160:163], a[164:167], a[68:71], v[160:163]\n"
    "s_waitcnt lgkmcnt(3)\n"
    "v_exp_f32_e32 v159, v159\n"
    "v_mfma_f32_16x16x32_bf16 v[160:163], a[168:171], a[72:75], v[160:163]\n"
    "s_add_u32 s60, s48, s61\n"
    "ds_read2st64_b32 a[196:197], v249 offset0:0 offset1:1\n"
    "v_cvt_pk_bf16_f32 v156, v156, v157\n"
    "v_mfma_f32_16x16x32_bf16 v[160:163], a[172:175], a[76:79], v[160:163]\n"
    "s_and_b32 s48, s32, 1\n"
    "v_exp_f32_e32 v164, v164\n"
    "v_mfma_f32_16x16x32_bf16 v[168:171], a[172:175], a[108:111], v[168:171]\n"
    "s_xor_b32 s48, s48, s50\n"
    "ds_write_addtid_b32 v156 offset:3072\n"
    "v_subrev_f32_e32 v132, s4, v132\n"
    "v_mfma_f32_16x16x32_bf16 v[168:171], a[168:171], a[104:107], v[168:171]\n"
    "s_sub_u32 s61, s32, s48\n"
    "ds_read2st64_b32 a[198:199], v249 offset0:2 offset1:3\n"
    "v_subrev_f32_e32 v109, s4, v109\n"
    "v_mfma_f32_16x16x32_bf16 v[176:179], a[168:171], a[136:139], v[176:179]\n"
    "s_and_b32 s61, s61, 3\n"
    "ds_read2st64_b32 a[192:193], v252 offset0:0 offset1:1\n"
    "v_subrev_f32_e32 v57, s4, v57\n"
    "v_mfma_f32_16x16x32_bf16 v[176:179], a[172:175], a[140:143], v[176:179]\n"
    "s_waitcnt lgkmcnt(6)\n"
    "v_exp_f32_e32 v165, v165\n"
    "v_mfma_f32_16x16x32_bf16 v[176:179], a[176:179], a[144:147], v[176:179]\n"
    "s_add_u32 s61, s61, 8\n"
    "ds_read2st64_b32 a[194:195], v252 offset0:2 offset1:3\n"
    "v_cvt_pk_bf16_f32 v157, v158, v159\n"
    "v_mfma_f32_16x16x32_bf16 v[176:179], a[180:183], a[148:151], v[176:179]\n"
    "s_mul_i32 s48, s48, 101376\n"
    "ds_read2st64_b32 a[204:205], v249 offset0:4 offset1:5\n"
    "v_exp_f32_e32 v166, v166\n"
    "v_mfma_f32_16x16x32_bf16 v[168:171], a[180:183], a[116:119], v[168:171]\n"
    "s_add_u32 s48, s48, s45\n"
    "ds_read_b128 a[160:163], v240 offset:8192\n"
    "v_exp_f32_e32 v167, v167\n"
    "v_mfma_f32_16x16x32_bf16 v[168:171], a[176:179], a[112:115], v[168:171]\n"
    "s_lshl_b32 s80, s61, 13\n"
    "ds_write_addtid_b32 v157 offset:3328\n"
    "v_subrev_f32_e32 v34, s4, v34\n"
    "v_mfma_f32_16x16x32_bf16 v[160:163], a[176:179], a[80:83], v[160:163]\n"
    "s_add_u32 s80, s80, s48\n"
    "ds_read_b128 a[164:167], v241 offset:8192\n"
    "v_subrev_f32_e32 v70, s4, v70\n"
    "v_mfma_f32_16x16x32_bf16 v[160:163], a[180:183], a[84:87], v[160:163]\n"
    "s_waitcnt lgkmcnt(9)\n"
    "ds_read_b128 a[176:179], v242 offset:8192\n"
    "v_cvt_pk_bf16_f32 v164, v164, v165\n"
    "v_mfma_f32_16x16x32_bf16 v[160:163], a[184:187], a[88:91], v[160:163]\n"
    "s_lshl_b32 s46, s51, 12\n"
    "ds_read_b128 a[168:171], v240 offset:12288\n"
    "v_exp_f32_e32 v172, v172\n"
    "v_mfma_f32_16x16x32_bf16 v[160:163], a[188:191], a[92:95], v[160:163]\n"
    "s_add_u32 s80, s80, s46\n"
    "ds_read_b128 a[180:183], v243 offset:8192\n"
    "v_exp_f32_e32 v173, v173\n"
    "v_mfma_f32_16x16x32_bf16 v[168:171], a[188:191], a[124:127], v[168:171]\n"
    "s_lshl_b32 s86, s61, 7\n"
    "ds_write_addtid_b32 v164 offset:4096\n"
    "v_cvt_pk_bf16_f32 v165, v166, v167\n"
    "v_mfma_f32_16x16x32_bf16 v[168:171], a[184:187], a[120:123], v[168:171]\n"
    "s_add_u32 s86, s86, s48\n"
    "ds_read_b128 a[172:175], v241 offset:12288\n"
    "v_subrev_f32_e32 v41, s4, v41\n"
    "v_mfma_f32_16x16x32_bf16 v[176:179], a[184:187], a[152:155], v[176:179]\n"
    "s_add_u32 s86, s86, s47\n"
    "ds_read2st64_b32 a[206:207], v249 offset0:6 offset1:7\n"
    "v_exp_f32_e32 v174, v174\n"
    "v_mfma_f32_16x16x32_bf16 v[176:179], a[188:191], a[156:159], v[176:179]\n"
    "s_waitcnt lgkmcnt(6)\n"
    ".long 0xbedf00ff\n"
    ".Lat_w10:\n"
    "s_setpc_b64 s[2:3]\n"
    "ds_read_b128 a[184:187], v242 offset:12288\n"
    "ds_read_b128 a[188:191], v243 offset:12288\n"
    "v_mfma_f32_16x16x32_bf16 v[180:183], a[160:163], a[128:131], v[236:239]\n"
    "s_lshl_b32 s46, s93, 7\n"
    "v_exp_f32_e32 v175, v175\n"
    "v_mfma_f32_16x16x32_bf16 v[180:183], a[164:167], a[132:135], v[180:183]\n"
    "s_lshl_b32 s55, s93, 15\n"
    "ds_read2st64_b32 a[200:201], v252 offset0:4 offset1:5\n"
    "v_subrev_f32_e32 v15, s4, v15\n"
    "v_mfma_f32_16x16x32_bf16 v[148:151], a[164:167], a[100:103], v[232:235]\n"
    "s_sub_u32 s54, s55, 24576\n"
    "ds_write_addtid_b32 v165 offset:4352\n"
    "v_cvt_pk_bf16_f32 v172, v172, v173\n"
    "v_mfma_f32_16x16x32_bf16 v[148:151], a[160:163], a[96:99], v[148:151]\n"
    "s_add_u32 s55, s55, 32768\n"
    "v_exp_f32_e32 v160, v160\n"
    "v_mfma_f32_16x16x32_bf16 v[140:143], a[160:163], a[64:67], v[228:231]\n"
    "s_lshl2_add_u32 s38, s34, s93\n"
    "v_exp_f32_e32 v161, v161\n"
    "v_mfma_f32_16x16x32_bf16 v[140:143], a[164:167], a[68:71], v[140:143]\n"
    "s_waitcnt lgkmcnt(5)\n"
    "ds_read_b128 a[160:163], v240 offset:10240\n"
    "v_cvt_pk_bf16_f32 v173, v174, v175\n"
    "v_mfma_f32_16x16x32_bf16 v[140:143], a[168:171], a[72:75], v[140:143]\n"
    "s_lshl1_add_u32 s38, s38, s49\n"
    "ds_read_b128 a[164:167], v241 offset:10240\n"
    "v_exp_f32_e32 v162, v162\n"
    "v_mfma_f32_16x16x32_bf16 v[140:143], a[172:175], a[76:79], v[140:143]\n"
    "s_lshl_b32 s38, s38, 7\n"
    "ds_read2st64_b32 a[202:203], v252 offset0:6 offset1:7\n"
    "v_exp_f32_e32 v163, v163\n"
    "v_mfma_f32_16x16x32_bf16 v[148:151], a[172:175], a[108:111], v[148:151]\n"
    "s_add_u32 s38, s38, 5648384\n"
    "v_cvt_pk_bf16_f32 v158, v160, v161\n"
    "v_exp_f32_e32 v168, v168\n"
    "v_mfma_f32_16x16x32_bf16 v[148:151], a[168:171], a[104:107], v[148:151]\n"
    "s_lshl3_add_u32 s90, s34, s49\n"
    "v_exp_f32_e32 v169, v169\n"
    "v_mfma_f32_16x16x32_bf16 v[180:183], a[168:171], a[136:139], v[180:183]\n"
    "s_lshl2_add_u32 s90, s90, s93\n"
    "ds_write_addtid_b32 v172 offset:5120\n"
    "v_cvt_pk_bf16_f32 v159, v162, v163\n"
    "v_mfma_f32_16x16x32_bf16 v[180:183], a[172:175], a[140:143], v[180:183]\n"
    "ds_read_b128 a[168:171], v240 offset:14336\n"
    "v_exp_f32_e32 v170, v170\n"
    "v_mfma_f32_16x16x32_bf16 v[180:183], a[176:179], a[144:147], v[180:183]\n"
    "s_lshl2_add_u32 s90, s90, 6041600\n"
    "v_cvt_pk_bf16_f32 v166, v168, v169\n"
    "v_exp_f32_e32 v171, v171\n"
    "v_mfma_f32_16x16x32_bf16 v[180:183], a[180:183], a[148:151], v[180:183]\n"
    "s_lshl_b32 s45, s51, 15\n"
    "ds_read_b128 a[172:175], v241 offset:14336\n"
    "v_exp_f32_e32 v176, v176\n"
    "v_mfma_f32_16x16x32_bf16 v[148:151], a[180:183], a[116:119], v[148:151]\n"
    "s_sub_u32 s45, 24576, s45\n"
    "v_exp_f32_e32 v177, v177\n"
    "v_mfma_f32_16x16x32_bf16 v[148:151], a[176:179], a[112:115], v[148:151]\n"
    "s_add_u32 s47, s46, 6103040\n"
    "ds_read2st64_b32 a[212:213], v249 offset0:8 offset1:9\n"
    "v_cvt_pk_bf16_f32 v167, v170, v171\n"
    "v_mfma_f32_16x16x32_bf16 v[140:143], a[176:179], a[80:83], v[140:143]\n"
    "s_andn2_b32 s48, s38, 0xc00\n"
    "ds_write_addtid_b32 v173 offset:5376\n"
    "v_subrev_f32_e32 v53, s4, v53\n"
    "v_mfma_f32_16x16x32_bf16 v[140:143], a[180:183], a[84:87], v[140:143]\n"
    "s_waitcnt lgkmcnt(10)\n"
    "ds_read2st64_b32 a[214:215], v249 offset0:10 offset1:11\n"
    "v_exp_f32_e32 v178, v178\n"
    "v_mfma_f32_16x16x32_bf16 v[140:143], a[184:187], a[88:91], v[140:143]\n"
    "s_andn2_b32 s61, s90, 0x18c\n"
    "ds_write_addtid_b32 v158 offset:3584\n"
    "v_exp_f32_e32 v179, v179\n"
    "v_mfma_f32_16x16x32_bf16 v[140:143], a[188:191], a[92:95], v[140:143]\n"
    "buffer_load_dword v188, v189, s[8:11], s81 offen offset:0 sc1\n"
    "ds_write_addtid_b32 v159 offset:3840\n"
    "v_mfma_f32_16x16x32_bf16 v[148:151], a[188:191], a[124:127], v[148:151]\n"
    "s_add_u32 s31, s46, s35\n"
    "ds_read2st64_b32 a[208:209], v252 offset0:8 offset1:9\n"
    "v_subrev_f32_e32 v74, s4, v74\n"
    "v_mfma_f32_16x16x32_bf16 v[148:151], a[184:187], a[120:123], v[148:151]\n"
    "s_add_u32 m0, s63, 6144\n"
    "ds_read2st64_b32 a[210:211], v252 offset0:10 offset1:11\n"
    "v_exp_f32_e32 v140, v140\n"
    "v_mfma_f32_16x16x32_bf16 v[180:183], a[184:187], a[152:155], v[180:183]\n"
    "buffer_load_dwordx4 v251, s[12:15], s31 offen offset:0 lds\n"
    "ds_read_b128 a[176:179], v242 offset:10240\n"
    "v_mfma_f32_16x16x32_bf16 v[180:183], a[188:191], a[156:159], v[180:183]\n"
    "s_waitcnt lgkmcnt(12)\n"
    "v_exp_f32_e32 v141, v141\n"
    "v_mfma_f32_16x16x32_bf16 v[184:187], a[160:163], a[128:131], v[236:239]\n"
    "s_mov_b32 m0, s63\n"
    "ds_read_b128 a[180:183], v243 offset:10240\n"
    "v_exp_f32_e32 v142, v142\n"
    "v_mfma_f32_16x16x32_bf16 v[184:187], a[164:167], a[132:135], v[184:187]\n"
    "ds_read_b128 a[184:187], v242 offset:14336\n"
    "v_exp_f32_e32 v143, v143\n"
    "v_mfma_f32_16x16x32_bf16 v[152:155], a[164:167], a[100:103], v[232:235]\n"
    "s_addk_i32 s31, 0x1000\n"
    "ds_write_addtid_b32 v166 offset:4608\n"
    "v_cvt_pk_bf16_f32 v140, v140, v141\n"
    "v_mfma_f32_16x16x32_bf16 v[152:155], a[160:163], a[96:99], v[152:155]\n"
    "s_add_u32 m0, s63, 7168\n"
    "v_exp_f32_e32 v148, v148\n"
    "v_mfma_f32_16x16x32_bf16 v[144:147], a[160:163], a[64:67], v[228:231]\n"
    "buffer_load_dwordx4 v251, s[12:15], s31 offen offset:0 lds\n"
    "v_mfma_f32_16x16x32_bf16 v[144:147], a[164:167], a[68:71], v[144:147]\n"
    "s_waitcnt lgkmcnt(11)\n"
    "s_waitcnt lgkmcnt(4)\n"
    "s_waitcnt vmcnt(3)\n"
    "v_mfma_f32_16x16x32_bf16 v[144:147], a[168:171], a[72:75], v[144:147]\n"
    "s_barrier\n"
    "v_mfma_f32_16x16x32_bf16 v[144:147], a[172:175], a[76:79], v[144:147]\n"
    "s_mov_b32 m0, s63\n"
    "ds_read_b128 a[188:191], v243 offset:14336\n"
    "v_cvt_pk_bf16_f32 v174, v176, v177\n"
    "v_mfma_f32_16x16x32_bf16 v[152:155], a[172:175], a[108:111], v[152:155]\n"
    "s_cmp_eq_u32 s93, 1\n"
    "s_cselect_b32 s31, s36, 2.0\n"
    "ds_write_addtid_b32 v140 offset:0\n"
    "v_mfma_f32_16x16x32_bf16 v[152:155], a[168:171], a[104:107], v[152:155]\n"
    "s_cmp_eq_u32 s93, 2\n"
    "s_cselect_b32 s31, s37, s31\n"
    "ds_read_b128 a[160:163], v240 offset:16384\n"
    "v_mfma_f32_16x16x32_bf16 v[184:187], a[168:171], a[136:139], v[184:187]\n"
    "buffer_load_dword v191, v190, s[12:15], s31 offen offset:0\n"
    "v_mfma_f32_16x16x32_bf16 v[184:187], a[172:175], a[140:143], v[184:187]\n"
    "s_waitcnt lgkmcnt(5)\n"
    "v_exp_f32_e32 v149, v149\n"
    "v_mfma_f32_16x16x32_bf16 v[184:187], a[176:179], a[144:147], v[184:187]\n"
    "s_cmp_eq_u32 s93, 0\n"
    "s_cselect_b32 s31, s35, s31\n"
    "ds_read_b128 a[164:167], v241 offset:16384\n"
    "v_cvt_pk_bf16_f32 v141, v142, v143\n"
    "v_mfma_f32_16x16x32_bf16 v[184:187], a[180:183], a[148:151], v[184:187]\n"
    "s_add_u32 s31, s31, 6103040\n"
    "v_exp_f32_e32 v150, v150\n"
    "v_mfma_f32_16x16x32_bf16 v[152:155], a[180:183], a[116:119], v[152:155]\n"
    "ds_read_b128 a[168:171], v240 offset:20480\n"
    "v_cvt_pk_bf16_f32 v175, v178, v179\n"
    "v_mfma_f32_16x16x32_bf16 v[152:155], a[176:179], a[112:115], v[152:155]\n"
    "v_exp_f32_e32 v151, v151\n"
    "v_mfma_f32_16x16x32_bf16 v[144:147], a[176:179], a[80:83], v[144:147]\n"
    "ds_write_addtid_b32 v167 offset:4864\n"
    "v_subrev_f32_e32 v75, s4, v75\n"
    "v_mfma_f32_16x16x32_bf16 v[144:147], a[180:183], a[84:87], v[144:147]\n"
    "s_waitcnt lgkmcnt(5)\n"
    "ds_read_b128 a[172:175], v241 offset:20480\n"
    "v_cvt_pk_bf16_f32 v148, v148, v149\n"
    "v_mfma_f32_16x16x32_bf16 v[144:147], a[184:187], a[88:91], v[144:147]\n"
    "ds_write_addtid_b32 v141 offset:256\n"
    "v_exp_f32_e32 v180, v180\n"
    "v_mfma_f32_16x16x32_bf16 v[144:147], a[188:191], a[92:95], v[144:147]\n"
    "ds_write_addtid_b32 v174 offset:5632\n"
    "v_exp_f32_e32 v181, v181\n"
    "v_mfma_f32_16x16x32_bf16 v[152:155], a[188:191], a[124:127], v[152:155]\n"
    "ds_write_addtid_b32 v148 offset:1024\n"
    "v_cvt_pk_bf16_f32 v149, v150, v151\n"
    "v_mfma_f32_16x16x32_bf16 v[152:155], a[184:187], a[120:123], v[152:155]\n"
    "v_exp_f32_e32 v182, v182\n"
    "v_mfma_f32_16x16x32_bf16 v[184:187], a[184:187], a[152:155], v[184:187]\n"
    "v_exp_f32_e32 v183, v183\n"
    "v_mfma_f32_16x16x32_bf16 v[184:187], a[188:191], a[156:159], v[184:187]\n"
    "s_waitcnt lgkmcnt(6)\n"
    "ds_read_b128 a[176:179], v242 offset:16384\n"
    "v_cvt_pk_bf16_f32 v180, v180, v181\n"
    "v_mfma_f32_16x16x32_bf16 v[212:215], a[160:163], a[128:131], v[236:239]\n"
    "ds_write_addtid_b32 v175 offset:5888\n"
    "v_exp_f32_e32 v144, v144\n"
    "v_mfma_f32_16x16x32_bf16 v[212:215], a[164:167], a[132:135], v[212:215]\n"
    "ds_read_b128 a[184:187], v242 offset:20480\n"
    "v_exp_f32_e32 v145, v145\n"
    "v_mfma_f32_16x16x32_bf16 v[204:207], a[164:167], a[100:103], v[232:235]\n"
    "ds_read_b128 a[180:183], v243 offset:16384\n"
    "v_cvt_pk_bf16_f32 v181, v182, v183\n"
    "v_mfma_f32_16x16x32_bf16 v[204:207], a[160:163], a[96:99], v[204:207]\n"
    "v_exp_f32_e32 v146, v146\n"
    "v_mfma_f32_16x16x32_bf16 v[196:199], a[160:163], a[64:67], v[228:231]\n"
    "v_exp_f32_e32 v147, v147\n"
    "v_mfma_f32_16x16x32_bf16 v[196:199], a[164:167], a[68:71], v[196:199]\n"
    "s_waitcnt lgkmcnt(7)\n"
    "ds_write_addtid_b32 v149 offset:1280\n"
    "v_subrev_f32_e32 v116, s4, v116\n"
    "v_mfma_f32_16x16x32_bf16 v[196:199], a[168:171], a[72:75], v[196:199]\n"
    "ds_write_addtid_b32 v180 offset:2048\n"
    "v_subrev_f32_e32 v35, s4, v35\n"
    "v_mfma_f32_16x16x32_bf16 v[196:199], a[172:175], a[76:79], v[196:199]\n"
    "s_waitcnt lgkmcnt(4)\n"
    "v_mfma_f32_16x16x32_bf16 v[204:207], a[172:175], a[108:111], v[204:207]\n"
    "s_barrier\n"
    "v_mfma_f32_16x16x32_bf16 v[204:207], a[168:171], a[104:107], v[204:207]\n"
    "ds_read_b128 a[188:191], v243 offset:20480\n"
    "v_cvt_pk_bf16_f32 v142, v144, v145\n"
    "v_mfma_f32_16x16x32_bf16 v[212:215], a[168:171], a[136:139], v[212:215]\n"
    "v_exp_f32_e32 v152, v152\n"
    "v_mfma_f32_16x16x32_bf16 v[212:215], a[172:175], a[140:143], v[212:215]\n"
    "s_waitcnt lgkmcnt(3)\n"
    "v_exp_f32_e32 v153, v153\n"
    "v_mfma_f32_16x16x32_bf16 v[212:215], a[176:179], a[144:147], v[212:215]\n"
    "ds_read_b128 a[168:171], v240 offset:22528\n"
    "v_cvt_pk_bf16_f32 v143, v146, v147\n"
    "v_mfma_f32_16x16x32_bf16 v[212:215], a[180:183], a[148:151], v[212:215]\n"
    "ds_read_b128 a[160:163], v240 offset:18432\n"
    "v_exp_f32_e32 v154, v154\n"
    "v_mfma_f32_16x16x32_bf16 v[204:207], a[180:183], a[116:119], v[204:207]\n"
    "ds_write_addtid_b32 v181 offset:2304\n"
    "v_exp_f32_e32 v155, v155\n"
    "v_mfma_f32_16x16x32_bf16 v[204:207], a[176:179], a[112:115], v[204:207]\n"
    "ds_read2st64_b32 v[160:161], v249 offset0:12 offset1:13\n"
    "v_subrev_f32_e32 v54, s4, v54\n"
    "v_mfma_f32_16x16x32_bf16 v[196:199], a[176:179], a[80:83], v[196:199]\n"
    "ds_read_b128 a[164:167], v241 offset:18432\n"
    "ds_read_b128 a[172:175], v241 offset:22528\n"
    "v_mfma_f32_16x16x32_bf16 v[196:199], a[180:183], a[84:87], v[196:199]\n"
    "s_waitcnt lgkmcnt(6)\n"
    "ds_write_addtid_b32 v142 offset:512\n"
    "v_cvt_pk_bf16_f32 v150, v152, v153\n"
    "v_mfma_f32_16x16x32_bf16 v[196:199], a[184:187], a[88:91], v[196:199]\n"
    "ds_write_addtid_b32 v143 offset:768\n"
    "v_exp_f32_e32 v184, v184\n"
    "v_mfma_f32_16x16x32_bf16 v[196:199], a[188:191], a[92:95], v[196:199]\n"
    "ds_read2st64_b32 v[162:163], v249 offset0:14 offset1:15\n"
    "v_exp_f32_e32 v185, v185\n"
    "v_mfma_f32_16x16x32_bf16 v[204:207], a[188:191], a[124:127], v[204:207]\n"
    "ds_read2st64_b32 v[168:169], v249 offset0:16 offset1:17\n"
    "v_cvt_pk_bf16_f32 v151, v154, v155\n"
    "v_mfma_f32_16x16x32_bf16 v[204:207], a[184:187], a[120:123], v[204:207]\n"
    "s_waitcnt vmcnt(3)\n"
    "v_cmp_ne_u32_e32 vcc, s39, v188\n"
    "v_exp_f32_e32 v186, v186\n"
    "v_mfma_f32_16x16x32_bf16 v[212:215], a[184:187], a[152:155], v[212:215]\n"
    "ds_read_b128 a[176:179], v242 offset:18432\n"
    "v_exp_f32_e32 v196, v196\n"
    "v_mfma_f32_16x16x32_bf16 v[212:215], a[188:191], a[156:159], v[212:215]\n"
    "s_waitcnt lgkmcnt(6)\n"
    "ds_read_b128 a[184:187], v242 offset:22528\n"
    "v_exp_f32_e32 v197, v197\n"
    "v_mfma_f32_16x16x32_bf16 v[216:219], a[160:163], a[128:131], v[236:239]\n"
    "s_cmp_eq_u64 vcc, 0\n"
    "s_cselect_b32 s31, s31, 2.0\n"
    "s_cselect_b32 s44, 0, 1\n"
    "v_mfma_f32_16x16x32_bf16 v[216:219], a[164:167], a[132:135], v[216:219]\n"
    "buffer_load_dword v191, v190, s[8:11], s31 offen offset:0 sc1\n"
    "ds_read_b128 a[180:183], v243 offset:18432\n"
    "v_mfma_f32_16x16x32_bf16 v[208:211], a[164:167], a[100:103], v[232:235]\n"
    "ds_write_addtid_b32 v150 offset:1536\n"
    "v_exp_f32_e32 v198, v198\n"
    "v_mfma_f32_16x16x32_bf16 v[208:211], a[160:163], a[96:99], v[208:211]\n"
    "ds_read2st64_b32 v[170:171], v249 offset0:18 offset1:19\n"
    "v_exp_f32_e32 v199, v199\n"
    "v_mfma_f32_16x16x32_bf16 v[200:203], a[160:163], a[64:67], v[228:231]\n"
    "ds_read_b128 a[188:191], v243 offset:22528\n"
    "v_exp_f32_e32 v187, v187\n"
    "v_mfma_f32_16x16x32_bf16 v[200:203], a[164:167], a[68:71], v[200:203]\n"
    "s_waitcnt lgkmcnt(10)\n"
    "ds_read_b64_tr_b16 a[160:161], v244 offset:24576\n"
    "v_cvt_pk_bf16_f32 v196, v196, v197\n"
    "v_subrev_f32_e32 v31, s4, v31\n"
    "v_mfma_f32_16x16x32_bf16 v[200:203], a[168:171], a[72:75], v[200:203]\n"
    "ds_read_b64_tr_b16 a[162:163], v244 offset:26624\n"
    "v_exp_f32_e32 v204, v204\n"
    "v_mfma_f32_16x16x32_bf16 v[200:203], a[172:175], a[76:79], v[200:203]\n"
    "v_exp_f32_e32 v205, v205\n"
    "v_mfma_f32_16x16x32_bf16 v[208:211], a[172:175], a[108:111], v[208:211]\n"
    "ds_write_addtid_b32 v151 offset:1792\n"
    "v_and_b32_e32 v248, 0xf0, v250\n"
    "v_lshrrev_b32_e32 v231, 3, v248\n"
    "v_mfma_f32_16x16x32_bf16 v[208:211], a[168:171], a[104:107], v[208:211]\n"
    "ds_read2st64_b32 v[176:177], v249 offset0:20 offset1:21\n"
    "v_cvt_pk_bf16_f32 v182, v184, v185\n"
    "v_cvt_pk_bf16_f32 v197, v198, v199\n"
    "v_mfma_f32_16x16x32_bf16 v[216:219], a[168:171], a[136:139], v[216:219]\n"
    "s_lshr_b32 s31, s35, 8\n"
    "ds_read2st64_b32 v[178:179], v249 offset0:22 offset1:23\n"
    "v_exp_f32_e32 v206, v206\n"
    "v_mfma_f32_16x16x32_bf16 v[216:219], a[172:175], a[140:143], v[216:219]\n"
    "s_waitcnt lgkmcnt(8)\n"
    "ds_write_addtid_b32 v182 offset:2560\n"
    "v_cvt_pk_bf16_f32 v183, v186, v187\n"
    "v_mfma_f32_16x16x32_bf16 v[216:219], a[176:179], a[144:147], v[216:219]\n"
    "buffer_load_ushort v228, v231, s[24:27], s31 offen\n"
    "ds_read_b64_tr_b16 a[164:165], v244 offset:57344\n"
    "v_mfma_f32_16x16x32_bf16 v[216:219], a[180:183], a[148:151], v[216:219]\n"
    "s_lshr_b32 s31, s36, 8\n"
    "ds_read_b64_tr_b16 a[166:167], v244 offset:59392\n"
    "v_exp_f32_e32 v207, v207\n"
    "v_mfma_f32_16x16x32_bf16 v[208:211], a[180:183], a[116:119], v[208:211]\n"
    "ds_read_b64_tr_b16 a[168:169], v245 offset:24576\n"
    "v_cvt_pk_bf16_f32 v204, v204, v205\n"
    "v_mfma_f32_16x16x32_bf16 v[208:211], a[176:179], a[112:115], v[208:211]\n"
    "ds_read_b64_tr_b16 a[170:171], v245 offset:26624\n"
    "v_exp_f32_e32 v212, v212\n"
    "v_mfma_f32_16x16x32_bf16 v[200:203], a[176:179], a[80:83], v[200:203]\n"
    "buffer_load_ushort v229, v231, s[24:27], s31 offen\n"
    "ds_read_b64_tr_b16 a[172:173], v245 offset:57344\n"
    "v_mfma_f32_16x16x32_bf16 v[200:203], a[180:183], a[84:87], v[200:203]\n"
    "s_waitcnt lgkmcnt(11)\n"
    "ds_write_addtid_b32 v183 offset:2816\n"
    "v_exp_f32_e32 v213, v213\n"
    "v_mfma_f32_16x16x32_bf16 v[200:203], a[184:187], a[88:91], v[200:203]\n"
    "s_lshr_b32 s31, s37, 8\n"
    "ds_read_b64_tr_b16 a[176:177], v246 offset:24576\n"
    "v_cvt_pk_bf16_f32 v205, v206, v207\n"
    "v_mfma_f32_16x16x32_bf16 v[200:203], a[188:191], a[92:95], v[200:203]\n"
    "ds_read_b64_tr_b16 a[178:179], v246 offset:26624\n"
    "v_exp_f32_e32 v214, v214\n"
    "v_mfma_f32_16x16x32_bf16 v[208:211], a[188:191], a[124:127], v[208:211]\n"
    "buffer_load_ushort v230, v231, s[24:27], s31 offen\n"
    "ds_read_b64_tr_b16 a[174:175], v245 offset:59392\n"
    "v_mfma_f32_16x16x32_bf16 v[208:211], a[184:187], a[120:123], v[208:211]\n"
    "s_waitcnt lgkmcnt(3)\n"
    "v_mfma_f32_16x16x32_bf16 v[216:219], a[184:187], a[152:155], v[216:219]\n"
    "s_barrier\n"
    "v_mfma_f32_16x16x32_bf16 v[216:219], a[188:191], a[156:159], v[216:219]\n"
    "ds_read_b64_tr_b16 a[184:185], v246 offset:0\n"
    "v_exp_f32_e32 v215, v215\n"
    "v_mfma_f32_16x16x32_bf16 a[252:255], a[160:163], a[208:211], a[252:255]\n"
    "s_add_u32 s62, s57, 4096\n"
    "ds_write_addtid_b32 v196 offset:3072\n"
    "v_cvt_pk_bf16_f32 v212, v212, v213\n"
    "v_mfma_f32_16x16x32_bf16 a[252:255], a[164:167], a[212:215], a[252:255]\n"
    "buffer_load_dwordx4 a[64:67], v250, s[8:11], s62 offen offset:2048 sc1\n"
    "ds_write_addtid_b32 v197 offset:3328\n"
    "v_mfma_f32_16x16x32_bf16 a[248:251], a[164:167], a[204:207], a[248:251]\n"
    "ds_read_b64_tr_b16 a[180:181], v246 offset:57344\n"
    "v_exp_f32_e32 v200, v200\n"
    "v_mfma_f32_16x16x32_bf16 a[248:251], a[160:163], a[200:203], a[248:251]\n"
    "ds_read_b64_tr_b16 a[186:187], v246 offset:2048\n"
    "v_exp_f32_e32 v201, v201\n"
    "v_mfma_f32_16x16x32_bf16 a[244:247], a[160:163], a[192:195], a[244:247]\n"
    "ds_read_b64_tr_b16 a[182:183], v246 offset:59392\n"
    "ds_read2st64_b32 v[144:145], v249 offset0:0 offset1:1\n"
    "v_mfma_f32_16x16x32_bf16 a[244:247], a[164:167], a[196:199], a[244:247]\n"
    "s_waitcnt lgkmcnt(7)\n"
    "ds_write_addtid_b32 v204 offset:4096\n"
    "v_lshrrev_b32_e32 v231, 2, v250\n"
    "v_mfma_f32_16x16x32_bf16 v[8:11], a[168:171], a[192:195], v[8:11]\n"
    "buffer_load_dwordx4 a[68:71], v250, s[8:11], s62 offen offset:3072 sc1\n"
    "ds_read_b64_tr_b16 a[160:161], v244 offset:0\n"
    "v_mfma_f32_16x16x32_bf16 v[8:11], a[172:175], a[196:199], v[8:11]\n"
    "s_add_u32 s62, s57, 8192\n"
    "ds_read_b64_tr_b16 a[188:189], v246 offset:32768\n"
    "ds_read_b64_tr_b16 a[162:163], v244 offset:2048\n"
    "v_cvt_pk_bf16_f32 v213, v214, v215\n"
    "v_mfma_f32_16x16x32_bf16 v[12:15], a[172:175], a[204:207], v[12:15]\n"
    "ds_read2st64_b32 v[146:147], v249 offset0:2 offset1:3\n"
    "v_exp_f32_e32 v202, v202\n"
    "v_mfma_f32_16x16x32_bf16 v[12:15], a[168:171], a[200:203], v[12:15]\n"
    "ds_write_addtid_b32 v205 offset:4352\n"
    "v_exp_f32_e32 v203, v203\n"
    "v_mfma_f32_16x16x32_bf16 v[16:19], a[168:171], a[208:211], v[16:19]\n"
    "ds_read_b64_tr_b16 a[190:191], v246 offset:34816\n"
    "v_cvt_pk_bf16_f32 v198, v200, v201\n"
    "v_mfma_f32_16x16x32_bf16 v[16:19], a[172:175], a[212:215], v[16:19]\n"
    "s_waitcnt lgkmcnt(8)\n"
    "ds_read_b64_tr_b16 a[164:165], v244 offset:32768\n"
    "v_exp_f32_e32 v208, v208\n"
    "v_mfma_f32_16x16x32_bf16 v[36:39], a[176:179], a[208:211], v[36:39]\n"
    "buffer_load_dwordx4 a[72:75], v250, s[8:11], s62 offen offset:0 sc1\n"
    "ds_read2st64_b32 v[152:153], v249 offset0:4 offset1:5\n"
    "v_mfma_f32_16x16x32_bf16 v[36:39], a[180:183], a[212:215], v[36:39]\n"
    "ds_read2st64_b32 v[154:155], v249 offset0:6 offset1:7\n"
    "v_exp_f32_e32 v209, v209\n"
    "v_mfma_f32_16x16x32_bf16 v[32:35], a[180:183], a[204:207], v[32:35]\n"
    "ds_read_b64_tr_b16 a[166:167], v244 offset:34816\n"
    "v_cvt_pk_bf16_f32 v199, v202, v203\n"
    "v_mfma_f32_16x16x32_bf16 v[32:35], a[176:179], a[200:203], v[32:35]\n"
    "ds_write_addtid_b32 v212 offset:5120\n"
    "v_exp_f32_e32 v210, v210\n"
    "v_mfma_f32_16x16x32_bf16 v[28:31], a[176:179], a[192:195], v[28:31]\n"
    "ds_read_b64_tr_b16 a[168:169], v245 offset:0\n"
    "v_subrev_u32_e32 v231, 0x80, v231\n"
    "v_mfma_f32_16x16x32_bf16 v[28:31], a[180:183], a[196:199], v[28:31]\n"
    "s_waitcnt lgkmcnt(6)\n"
    "ds_read_b64_tr_b16 a[170:171], v245 offset:2048\n"
    "v_min_u32_e32 v231, 2.0, v231\n"
    "v_lshrrev_b32_e32 v252, 4, v250\n"
    "v_mfma_f32_16x16x32_bf16 v[28:31], a[184:187], v[156:159], v[28:31]\n"
    "buffer_load_dwordx4 a[76:79], v250, s[8:11], s62 offen offset:1024 sc1\n"
    "ds_read2st64_b32 v[184:185], v249 offset0:8 offset1:9\n"
    "v_mfma_f32_16x16x32_bf16 v[28:31], a[188:191], v[160:163], v[28:31]\n"
    "ds_read2st64_b32 v[186:187], v249 offset0:10 offset1:11\n"
    "v_exp_f32_e32 v211, v211\n"
    "v_mfma_f32_16x16x32_bf16 v[32:35], a[188:191], v[168:171], v[32:35]\n"
    "ds_read_b64_tr_b16 a[176:177], v245 offset:8192\n"
    "v_cvt_pk_bf16_f32 v206, v208, v209\n"
    "v_mfma_f32_16x16x32_bf16 v[32:35], a[184:187], v[164:167], v[32:35]\n"
    "ds_read_b64_tr_b16 a[172:173], v245 offset:32768\n"
    "v_exp_f32_e32 v216, v216\n"
    "v_mfma_f32_16x16x32_bf16 v[36:39], a[184:187], v[172:175], v[36:39]\n"
    "ds_read_b64_tr_b16 a[174:175], v245 offset:34816\n"
    "v_lshlrev_b32_e32 v239, 5, v248\n"
    "v_mfma_f32_16x16x32_bf16 v[36:39], a[188:191], v[176:179], v[36:39]\n"
    "s_waitcnt lgkmcnt(8)\n"
    "ds_read_b64_tr_b16 a[178:179], v245 offset:10240\n"
    "v_exp_f32_e32 v217, v217\n"
    "v_mfma_f32_16x16x32_bf16 a[252:255], a[160:163], v[172:175], a[252:255]\n"
    "buffer_load_dwordx4 a[80:83], v250, s[8:11], s62 offen offset:2048 sc1\n"
    "ds_read_b64_tr_b16 a[180:181], v245 offset:40960\n"
    "v_mfma_f32_16x16x32_bf16 a[252:255], a[164:167], v[176:179], a[252:255]\n"
    "ds_read_b64_tr_b16 a[182:183], v245 offset:43008\n"
    "ds_read_b64_tr_b16 a[184:185], v244 offset:8192\n"
    "ds_read_b64_tr_b16 a[186:187], v244 offset:10240\n"
    "v_mfma_f32_16x16x32_bf16 a[248:251], a[164:167], v[168:171], a[248:251]\n"
    "ds_write_addtid_b32 v213 offset:5376\n"
    "v_cvt_pk_bf16_f32 v207, v210, v211\n"
    "v_mfma_f32_16x16x32_bf16 a[248:251], a[160:163], v[164:167], a[248:251]\n"
    "ds_read_b64_tr_b16 a[188:189], v244 offset:40960\n"
    "ds_write_addtid_b32 v198 offset:3584\n"
    "v_mfma_f32_16x16x32_bf16 a[244:247], a[160:163], v[156:159], a[244:247]\n"
    "ds_write_addtid_b32 v199 offset:3840\n"
    "v_exp_f32_e32 v218, v218\n"
    "v_mfma_f32_16x16x32_bf16 a[244:247], a[164:167], v[160:163], a[244:247]\n"
    "s_waitcnt lgkmcnt(9)\n"
    "ds_write_addtid_b32 v206 offset:4608\n"
    "v_exp_f32_e32 v219, v219\n"
    "v_mfma_f32_16x16x32_bf16 v[8:11], a[168:171], v[156:159], v[8:11]\n"
    "buffer_load_dwordx4 a[84:87], v250, s[8:11], s62 offen offset:3072 sc1\n"
    "ds_read_b64_tr_b16 a[160:161], v244 offset:16384\n"
    "v_mfma_f32_16x16x32_bf16 v[8:11], a[172:175], v[160:163], v[8:11]\n"
    "s_add_u32 s62, s57, 12288\n"
    "ds_write_addtid_b32 v207 offset:4864\n"
    "v_cvt_pk_bf16_f32 v214, v216, v217\n"
    "v_cvt_pk_bf16_f32 v215, v218, v219\n"
    "v_mfma_f32_16x16x32_bf16 v[12:15], a[172:175], v[168:171], v[12:15]\n"
    "ds_write_addtid_b32 v214 offset:5632\n"
    "ds_write_addtid_b32 v215 offset:5888\n"
    "v_mfma_f32_16x16x32_bf16 v[12:15], a[168:171], v[164:167], v[12:15]\n"
    "ds_read_b64_tr_b16 a[190:191], v244 offset:43008\n"
    "v_and_or_b32 v239, v252, 48, v239\n"
    "v_lshrrev_b32_e32 v248, 2, v248\n"
    "v_mfma_f32_16x16x32_bf16 v[16:19], a[168:171], v[172:175], v[16:19]\n"
    "ds_read_b64_tr_b16 a[162:163], v244 offset:18432\n"
    "ds_read_b64_tr_b16 a[164:165], v244 offset:49152\n"
    "v_mfma_f32_16x16x32_bf16 v[16:19], a[172:175], v[176:179], v[16:19]\n"
    "s_waitcnt lgkmcnt(14)\n"
    "s_waitcnt lgkmcnt(3)\n"
    "v_mfma_f32_16x16x32_bf16 v[16:19], a[176:179], v[180:183], v[16:19]\n"
    "s_barrier\n"
    "v_mfma_f32_16x16x32_bf16 v[16:19], a[180:183], v[184:187], v[16:19]\n"
    "buffer_load_dwordx4 a[88:91], v250, s[8:11], s62 offen offset:0 sc1\n"
    "ds_read2st64_b32 v[200:201], v249 offset0:12 offset1:13\n"
    "v_mfma_f32_16x16x32_bf16 v[12:15], a[180:183], v[152:155], v[12:15]\n"
    "ds_read_b64_tr_b16 a[166:167], v244 offset:51200\n"
    "ds_read2st64_b32 v[202:203], v249 offset0:14 offset1:15\n"
    "v_mfma_f32_16x16x32_bf16 v[12:15], a[176:179], v[148:151], v[12:15]\n"
    "ds_read2st64_b32 v[208:209], v249 offset0:16 offset1:17\n"
    "v_lshrrev_b32_e32 v253, 4, v252\n"
    "v_mfma_f32_16x16x32_bf16 v[8:11], a[176:179], v[140:143], v[8:11]\n"
    "ds_read2st64_b32 v[210:211], v249 offset0:18 offset1:19\n"
    "v_lshl_add_u32 v248, v253, 7, v248\n"
    "v_mfma_f32_16x16x32_bf16 v[8:11], a[180:183], v[144:147], v[8:11]\n"
    "s_waitcnt lgkmcnt(7)\n"
    "ds_read2st64_b32 v[216:217], v249 offset0:20 offset1:21\n"
    "ds_read2st64_b32 v[218:219], v249 offset0:22 offset1:23\n"
    "v_bfe_u32 v255, v252, 2, 2\n"
    "v_mfma_f32_16x16x32_bf16 a[244:247], a[184:187], v[140:143], a[244:247]\n"
    "buffer_load_dwordx4 a[92:95], v250, s[8:11], s62 offen offset:1024 sc1\n"
    "v_mfma_f32_16x16x32_bf16 a[244:247], a[188:191], v[144:147], a[244:247]\n"
    "ds_read_b64_tr_b16 a[168:169], v245 offset:16384\n"
    "v_subrev_f32_e32 v120, s4, v120\n"
    "v_mfma_f32_16x16x32_bf16 a[248:251], a[188:191], v[152:155], a[248:251]\n"
    "ds_read_b64_tr_b16 a[170:171], v245 offset:18432\n"
    "v_subrev_f32_e32 v58, s4, v58\n"
    "v_mfma_f32_16x16x32_bf16 a[248:251], a[184:187], v[148:151], a[248:251]\n"
    "ds_read_b64_tr_b16 a[172:173], v245 offset:49152\n"
    "v_max_u32_e32 v238, 1, v255\n"
    "v_mfma_f32_16x16x32_bf16 a[252:255], a[184:187], v[180:183], a[252:255]\n"
    "ds_read_b64_tr_b16 a[174:175], v245 offset:51200\n"
    "v_add_u32_e32 v238, s32, v238\n"
    "v_mfma_f32_16x16x32_bf16 a[252:255], a[188:191], v[184:187], a[252:255]\n"
    "s_waitcnt lgkmcnt(4)\n"
    "ds_read_b64_tr_b16 a[176:177], v246 offset:8192\n"
    "v_and_b32_e32 v238, 3, v238\n"
    "v_mfma_f32_16x16x32_bf16 a[252:255], a[160:163], v[212:215], a[252:255]\n"
    "buffer_load_dwordx4 a[96:99], v250, s[8:11], s62 offen offset:2048 sc1\n"
    "ds_read_b64_tr_b16 a[184:185], v246 offset:16384\n"
    "v_mfma_f32_16x16x32_bf16 a[252:255], a[164:167], v[216:219], a[252:255]\n"
    "ds_read_b64_tr_b16 a[178:179], v246 offset:10240\n"
    "v_subrev_f32_e32 v97, s4, v97\n"
    "v_mfma_f32_16x16x32_bf16 a[248:251], a[164:167], v[208:211], a[248:251]\n"
    "ds_read_b64_tr_b16 a[180:181], v246 offset:40960\n"
    "v_lshlrev_b32_e32 v238, 10, v238\n"
    "v_mfma_f32_16x16x32_bf16 a[248:251], a[160:163], v[204:207], a[248:251]\n"
    "ds_read_b64_tr_b16 a[186:187], v246 offset:18432\n"
    "v_and_b32_e32 v252, 3, v252\n"
    "v_mfma_f32_16x16x32_bf16 a[244:247], a[160:163], v[196:199], a[244:247]\n"
    "ds_read_b64_tr_b16 a[182:183], v246 offset:43008\n"
    "v_subrev_f32_e32 v128, s4, v128\n"
    "v_mfma_f32_16x16x32_bf16 a[244:247], a[164:167], v[200:203], a[244:247]\n"
    "s_waitcnt lgkmcnt(6)\n"
    "ds_read_b64_tr_b16 a[188:189], v246 offset:49152\n"
    "v_xor_b32_e32 v253, v253, v255\n"
    "v_mfma_f32_16x16x32_bf16 v[8:11], a[168:171], v[196:199], v[8:11]\n"
    "buffer_load_dwordx4 a[100:103], v250, s[8:11], s62 offen offset:3072 sc1\n"
    "ds_read_b64_tr_b16 a[160:161], v247 offset:24576\n"
    "v_mfma_f32_16x16x32_bf16 v[8:11], a[172:175], v[200:203], v[8:11]\n"
    "s_add_u32 s62, s57, 16384\n"
    "ds_read_b64_tr_b16 a[190:191], v246 offset:51200\n"
    "v_subrev_f32_e32 v55, s4, v55\n"
    "v_mfma_f32_16x16x32_bf16 v[12:15], a[172:175], v[208:211], v[12:15]\n"
    "ds_read_b64_tr_b16 a[162:163], v247 offset:26624\n"
    "v_subrev_f32_e32 v78, s4, v78\n"
    "v_mfma_f32_16x16x32_bf16 v[12:15], a[168:171], v[204:207], v[12:15]\n"
    "v_cvt_pk_bf16_f32 v11, v10, v11\n"
    "v_cvt_pk_bf16_f32 v10, v8, v9\n"
    "v_mfma_f32_16x16x32_bf16 v[16:19], a[168:171], v[212:215], v[16:19]\n"
    "ds_read_b64_tr_b16 a[164:165], v247 offset:57344\n"
    "v_subrev_f32_e32 v50, s4, v50\n"
    "v_mfma_f32_16x16x32_bf16 v[16:19], a[172:175], v[216:219], v[16:19]\n"
    "s_waitcnt lgkmcnt(5)\n"
    "v_accvgpr_read_b32 v8, a244\n"
    "v_accvgpr_read_b32 v9, a245\n"
    "v_mfma_f32_16x16x32_bf16 v[36:39], a[176:179], v[180:183], v[36:39]\n"
    "v_accvgpr_read_b32 v255, a247\n"
    "v_cvt_pk_bf16_f32 v8, v8, v9\n"
    "v_mfma_f32_16x16x32_bf16 v[36:39], a[180:183], v[184:187], v[36:39]\n"
    "v_accvgpr_read_b32 v9, a246\n"
    "v_cvt_pk_bf16_f32 v9, v9, v255\n"
    "v_mfma_f32_16x16x32_bf16 v[32:35], a[180:183], v[152:155], v[32:35]\n"
    "buffer_store_dwordx4 v[8:11], v250, s[8:11], s82 offen offset:0 sc1\n"
    "v_mfma_f32_16x16x32_bf16 v[32:35], a[176:179], v[148:151], v[32:35]\n"
    "ds_read_b64_tr_b16 a[166:167], v247 offset:59392\n"
    "v_cvt_pk_bf16_f32 v15, v14, v15\n"
    "v_mfma_f32_16x16x32_bf16 v[28:31], a[176:179], v[140:143], v[28:31]\n"
    "v_cvt_pk_bf16_f32 v14, v12, v13\n"
    "v_accvgpr_read_b32 v12, a248\n"
    "v_mfma_f32_16x16x32_bf16 v[28:31], a[180:183], v[144:147], v[28:31]\n"
    "s_waitcnt lgkmcnt(3)\n"
    "ds_read_b64_tr_b16 a[168:169], v247 offset:0\n"
    "v_accvgpr_read_b32 v13, a249\n"
    "v_mfma_f32_16x16x32_bf16 v[28:31], a[184:187], v[196:199], v[28:31]\n"
    "buffer_load_dwordx4 a[104:107], v250, s[8:11], s62 offen offset:0 sc1\n"
    "v_mfma_f32_16x16x32_bf16 v[28:31], a[188:191], v[200:203], v[28:31]\n"
    "ds_read_b64_tr_b16 a[170:171], v247 offset:2048\n"
    "v_accvgpr_read_b32 v255, a251\n"
    "v_cvt_pk_bf16_f32 v12, v12, v13\n"
    "v_mfma_f32_16x16x32_bf16 v[32:35], a[188:191], v[208:211], v[32:35]\n"
    "ds_read_b64_tr_b16 a[172:173], v247 offset:32768\n"
    "v_subrev_f32_e32 v59, s4, v59\n"
    "v_mfma_f32_16x16x32_bf16 v[32:35], a[184:187], v[204:207], v[32:35]\n"
    "ds_read_b64_tr_b16 a[176:177], v247 offset:8192\n"
    "v_subrev_f32_e32 v51, s4, v51\n"
    "v_mfma_f32_16x16x32_bf16 v[36:39], a[184:187], v[212:215], v[36:39]\n"
    "ds_read_b64_tr_b16 a[174:175], v247 offset:34816\n"
    "v_accvgpr_read_b32 v13, a250\n"
    "v_mfma_f32_16x16x32_bf16 v[36:39], a[188:191], v[216:219], v[36:39]\n"
    "s_waitcnt lgkmcnt(5)\n"
    "ds_read_b64_tr_b16 a[178:179], v247 offset:10240\n"
    "v_cvt_pk_bf16_f32 v13, v13, v255\n"
    "v_mfma_f32_16x16x32_bf16 v[56:59], a[160:163], a[208:211], v[56:59]\n"
    "buffer_load_dwordx4 a[108:111], v250, s[8:11], s62 offen offset:1024 sc1\n"
    "v_mfma_f32_16x16x32_bf16 v[56:59], a[164:167], a[212:215], v[56:59]\n"
    "ds_read_b64_tr_b16 a[184:185], v247 offset:16384\n"
    "v_lshl_or_b32 v252, v253, 2, v252\n"
    "v_mfma_f32_16x16x32_bf16 v[52:55], a[164:167], a[204:207], v[52:55]\n"
    "ds_read_b64_tr_b16 a[180:181], v247 offset:40960\n"
    "v_cvt_pk_bf16_f32 v28, v28, v29\n"
    "v_mfma_f32_16x16x32_bf16 v[52:55], a[160:163], a[200:203], v[52:55]\n"
    "buffer_store_dwordx4 v[12:15], v250, s[8:11], s28 offen offset:0 sc1\n"
    "v_mfma_f32_16x16x32_bf16 v[48:51], a[160:163], a[192:195], v[48:51]\n"
    "s_add_u32 s62, s57, 16384\n"
    "ds_read_b64_tr_b16 a[182:183], v247 offset:43008\n"
    "v_cvt_pk_bf16_f32 v19, v18, v19\n"
    "v_mfma_f32_16x16x32_bf16 v[48:51], a[164:167], a[196:199], v[48:51]\n"
    "s_waitcnt lgkmcnt(4)\n"
    "ds_read_b64_tr_b16 a[186:187], v247 offset:18432\n"
    "v_cvt_pk_bf16_f32 v18, v16, v17\n"
    "v_mfma_f32_16x16x32_bf16 v[48:51], a[168:171], v[156:159], v[48:51]\n"
    "buffer_load_dwordx4 a[112:115], v250, s[8:11], s62 offen offset:2048 sc1\n"
    "v_mfma_f32_16x16x32_bf16 v[48:51], a[172:175], v[160:163], v[48:51]\n"
    "ds_read_b64_tr_b16 a[160:161], v244 offset:28672\n"
    "v_accvgpr_read_b32 v16, a252\n"
    "v_mfma_f32_16x16x32_bf16 v[52:55], a[172:175], v[168:171], v[52:55]\n"
    "ds_read_b64_tr_b16 a[188:189], v247 offset:49152\n"
    "v_subrev_f32_e32 v79, s4, v79\n"
    "v_mfma_f32_16x16x32_bf16 v[52:55], a[168:171], v[164:167], v[52:55]\n"
    "v_accvgpr_read_b32 v17, a253\n"
    "v_accvgpr_read_b32 v255, a255\n"
    "v_mfma_f32_16x16x32_bf16 v[56:59], a[168:171], v[172:175], v[56:59]\n"
    "ds_read_b64_tr_b16 a[190:191], v247 offset:51200\n"
    "v_cvt_pk_bf16_f32 v16, v16, v17\n"
    "v_mfma_f32_16x16x32_bf16 v[56:59], a[172:175], v[176:179], v[56:59]\n"
    "s_waitcnt lgkmcnt(4)\n"
    "ds_read_b64_tr_b16 a[162:163], v244 offset:30720\n"
    "v_subrev_f32_e32 v98, s4, v98\n"
    "v_mfma_f32_16x16x32_bf16 v[56:59], a[176:179], v[180:183], v[56:59]\n"
    ".long 0xbedf00ff\n"
    ".Lat_w11:\n"
    "s_setpc_b64 s[2:3]\n"
    "v_accvgpr_read_b32 v17, a254\n"
    "v_cvt_pk_bf16_f32 v17, v17, v255\n"
    "v_mfma_f32_16x16x32_bf16 v[56:59], a[180:183], v[184:187], v[56:59]\n"
    "ds_read_b64_tr_b16 a[164:165], v244 offset:61440\n"
    "v_subrev_f32_e32 v80, s4, v80\n"
    "v_mfma_f32_16x16x32_bf16 v[52:55], a[180:183], v[152:155], v[52:55]\n"
    "ds_read_b64_tr_b16 a[168:169], v244 offset:4096\n"
    "v_subrev_f32_e32 v71, s4, v71\n"
    "v_mfma_f32_16x16x32_bf16 v[52:55], a[176:179], v[148:151], v[52:55]\n"
    "ds_read_b64_tr_b16 a[166:167], v244 offset:63488\n"
    "v_subrev_f32_e32 v133, s4, v133\n"
    "v_mfma_f32_16x16x32_bf16 v[48:51], a[176:179], v[140:143], v[48:51]\n"
    "buffer_store_dwordx4 v[16:19], v250, s[8:11], s29 offen offset:0 sc1\n"
    "v_mfma_f32_16x16x32_bf16 v[48:51], a[180:183], v[144:147], v[48:51]\n"
    "s_waitcnt lgkmcnt(4)\n"
    "ds_read_b64_tr_b16 a[170:171], v244 offset:6144\n"
    "v_cvt_pk_bf16_f32 v29, v30, v31\n"
    "v_mfma_f32_16x16x32_bf16 v[48:51], a[184:187], v[196:199], v[48:51]\n"
    "ds_read_b64_tr_b16 a[176:177], v244 offset:12288\n"
    "v_cvt_pk_bf16_f32 v32, v32, v33\n"
    "v_mfma_f32_16x16x32_bf16 v[48:51], a[188:191], v[200:203], v[48:51]\n"
    "buffer_load_dwordx4 a[116:119], v250, s[8:11], s62 offen offset:3072 sc1\n"
    "v_mfma_f32_16x16x32_bf16 v[52:55], a[188:191], v[208:211], v[52:55]\n"
    "s_add_u32 s62, s57, 20480\n"
    "ds_read_b64_tr_b16 a[178:179], v244 offset:14336\n"
    "v_subrev_f32_e32 v113, s4, v113\n"
    "v_mfma_f32_16x16x32_bf16 v[52:55], a[184:187], v[204:207], v[52:55]\n"
    "ds_read_b64_tr_b16 a[172:173], v244 offset:36864\n"
    "v_cvt_pk_bf16_f32 v33, v34, v35\n"
    "v_mfma_f32_16x16x32_bf16 v[56:59], a[184:187], v[212:215], v[56:59]\n"
    "ds_read_b64_tr_b16 a[174:175], v244 offset:38912\n"
    "v_subrev_f32_e32 v91, s4, v91\n"
    "v_mfma_f32_16x16x32_bf16 v[56:59], a[188:191], v[216:219], v[56:59]\n"
    "s_waitcnt lgkmcnt(5)\n"
    "ds_read_b64_tr_b16 a[180:181], v244 offset:45056\n"
    "v_cvt_pk_bf16_f32 v36, v36, v37\n"
    "v_mfma_f32_16x16x32_bf16 v[76:79], a[160:163], a[208:211], v[76:79]\n"
    "ds_read_b64_tr_b16 a[184:185], v244 offset:20480\n"
    "v_cvt_pk_bf16_f32 v37, v38, v39\n"
    "v_mfma_f32_16x16x32_bf16 v[76:79], a[164:167], a[212:215], v[76:79]\n"
    "ds_read_b64_tr_b16 a[182:183], v244 offset:47104\n"
    "ds_read_b64_tr_b16 a[186:187], v244 offset:22528\n"
    "v_mfma_f32_16x16x32_bf16 v[72:75], a[164:167], a[204:207], v[72:75]\n"
    "v_cvt_pk_bf16_f32 v30, v48, v49\n"
    "v_cvt_pk_bf16_f32 v31, v50, v51\n"
    "v_mfma_f32_16x16x32_bf16 v[72:75], a[160:163], a[200:203], v[72:75]\n"
    "buffer_load_dwordx4 a[120:123], v250, s[8:11], s62 offen offset:0 sc1\n"
    "v_mfma_f32_16x16x32_bf16 v[68:71], a[160:163], a[192:195], v[68:71]\n"
    "ds_read_b64_tr_b16 a[188:189], v244 offset:53248\n"
    "v_subrev_f32_e32 v99, s4, v99\n"
    "v_mfma_f32_16x16x32_bf16 v[68:71], a[164:167], a[196:199], v[68:71]\n"
    "s_waitcnt lgkmcnt(5)\n"
    "v_cvt_pk_bf16_f32 v34, v52, v53\n"
    "v_subrev_f32_e32 v92, s4, v92\n"
    "v_mfma_f32_16x16x32_bf16 v[68:71], a[168:171], v[156:159], v[68:71]\n"
    "buffer_store_dwordx4 v[28:31], v250, s[8:11], s82 offen offset:1024 sc1\n"
    "v_mfma_f32_16x16x32_bf16 v[68:71], a[172:175], v[160:163], v[68:71]\n"
    "ds_read_b64_tr_b16 a[190:191], v244 offset:55296\n"
    "v_subrev_f32_e32 v81, s4, v81\n"
    "v_mfma_f32_16x16x32_bf16 v[72:75], a[172:175], v[168:171], v[72:75]\n"
    "v_subrev_f32_e32 v117, s4, v117\n"
    "v_subrev_f32_e32 v118, s4, v118\n"
    "v_mfma_f32_16x16x32_bf16 v[72:75], a[168:171], v[164:167], v[72:75]\n"
    "ds_read_b64_tr_b16 a[160:161], v245 offset:28672\n"
    "v_subrev_f32_e32 v93, s4, v93\n"
    "v_mfma_f32_16x16x32_bf16 v[76:79], a[168:171], v[172:175], v[76:79]\n"
    "ds_read_b64_tr_b16 a[162:163], v245 offset:30720\n"
    "v_subrev_f32_e32 v114, s4, v114\n"
    "v_mfma_f32_16x16x32_bf16 v[76:79], a[172:175], v[176:179], v[76:79]\n"
    "s_waitcnt lgkmcnt(5)\n"
    "v_subrev_f32_e32 v94, s4, v94\n"
    "v_subrev_f32_e32 v119, s4, v119\n"
    "v_mfma_f32_16x16x32_bf16 v[76:79], a[176:179], v[180:183], v[76:79]\n"
    "buffer_load_dwordx4 a[124:127], v250, s[8:11], s62 offen offset:1024 sc1\n"
    "v_mfma_f32_16x16x32_bf16 v[76:79], a[180:183], v[184:187], v[76:79]\n"
    "ds_read_b64_tr_b16 a[168:169], v245 offset:4096\n"
    "v_subrev_f32_e32 v104, s4, v104\n"
    "v_mfma_f32_16x16x32_bf16 v[72:75], a[180:183], v[152:155], v[72:75]\n"
    "ds_read_b64_tr_b16 a[170:171], v245 offset:6144\n"
    "v_cvt_pk_bf16_f32 v35, v54, v55\n"
    "v_mfma_f32_16x16x32_bf16 v[72:75], a[176:179], v[148:151], v[72:75]\n"
    "ds_read_b64_tr_b16 a[164:165], v245 offset:61440\n"
    "v_mov_b32_e32 v253, 0x3f80\n"
    "v_mfma_f32_16x16x32_bf16 v[68:71], a[176:179], v[140:143], v[68:71]\n"
    "ds_read_b64_tr_b16 a[166:167], v245 offset:63488\n"
    "v_subrev_f32_e32 v82, s4, v82\n"
    "v_mfma_f32_16x16x32_bf16 v[68:71], a[180:183], v[144:147], v[68:71]\n"
    "s_waitcnt lgkmcnt(6)\n"
    "ds_read_b64_tr_b16 a[172:173], v245 offset:36864\n"
    "v_subrev_f32_e32 v95, s4, v95\n"
    "v_mfma_f32_16x16x32_bf16 v[68:71], a[184:187], v[196:199], v[68:71]\n"
    "ds_read_b64_tr_b16 a[176:177], v245 offset:12288\n"
    "v_mov_b64_e32 v[232:233], 0\n"
    "v_mfma_f32_16x16x32_bf16 v[68:71], a[188:191], v[200:203], v[68:71]\n"
    "buffer_store_dwordx4 v[32:35], v250, s[8:11], s28 offen offset:1024 sc1\n"
    "v_mfma_f32_16x16x32_bf16 v[72:75], a[188:191], v[208:211], v[72:75]\n"
    "ds_read_b64_tr_b16 a[178:179], v245 offset:14336\n"
    "v_subrev_f32_e32 v129, s4, v129\n"
    "v_mfma_f32_16x16x32_bf16 v[72:75], a[184:187], v[204:207], v[72:75]\n"
    "ds_read_b64_tr_b16 a[174:175], v245 offset:38912\n"
    "v_cvt_pk_bf16_f32 v38, v56, v57\n"
    "v_mfma_f32_16x16x32_bf16 v[76:79], a[184:187], v[212:215], v[76:79]\n"
    "buffer_load_dwordx4 a[128:131], v250, s[8:11], s62 offen offset:2048 sc1\n"
    "v_mfma_f32_16x16x32_bf16 v[76:79], a[188:191], v[216:219], v[76:79]\n"
    "s_waitcnt lgkmcnt(4)\n"
    "ds_read_b64_tr_b16 a[180:181], v245 offset:45056\n"
    "v_subrev_f32_e32 v110, s4, v110\n"
    "v_mfma_f32_16x16x32_bf16 v[96:99], a[160:163], a[208:211], v[96:99]\n"
    "v_mov_b64_e32 v[236:237], 0\n"
    "v_subrev_f32_e32 v1, s4, v1\n"
    "v_mfma_f32_16x16x32_bf16 v[96:99], a[164:167], a[212:215], v[96:99]\n"
    "ds_read_b64_tr_b16 a[184:185], v245 offset:20480\n"
    "v_cvt_pk_bf16_f32 v39, v58, v59\n"
    "v_mfma_f32_16x16x32_bf16 v[92:95], a[164:167], a[204:207], v[92:95]\n"
    "ds_read_b64_tr_b16 a[186:187], v245 offset:22528\n"
    "v_subrev_f32_e32 v24, s4, v24\n"
    "v_mfma_f32_16x16x32_bf16 v[92:95], a[160:163], a[200:203], v[92:95]\n"
    "ds_read_b64_tr_b16 a[182:183], v245 offset:47104\n"
    "v_cmp_eq_u32_e32 vcc, 0, v252\n"
    "v_mfma_f32_16x16x32_bf16 v[88:91], a[160:163], a[192:195], v[88:91]\n"
    "ds_read_b64_tr_b16 a[188:189], v245 offset:53248\n"
    "v_cndmask_b32_e32 v234, 0, v253, vcc\n"
    "v_mfma_f32_16x16x32_bf16 v[88:91], a[164:167], a[196:199], v[88:91]\n"
    "s_waitcnt lgkmcnt(5)\n"
    "v_cvt_pk_bf16_f32 v72, v72, v73\n"
    "v_subrev_f32_e32 v45, s4, v45\n"
    "v_mfma_f32_16x16x32_bf16 v[88:91], a[168:171], v[156:159], v[88:91]\n"
    "ds_read_b64_tr_b16 a[160:161], v246 offset:28672\n"
    "v_mfma_f32_16x16x32_bf16 v[88:91], a[172:175], v[160:163], v[88:91]\n"
    "v_subrev_f32_e32 v136, s4, v136\n"
    "v_subrev_f32_e32 v121, s4, v121\n"
    "v_mfma_f32_16x16x32_bf16 v[92:95], a[172:175], v[168:171], v[92:95]\n"
    "buffer_store_dwordx4 v[36:39], v250, s[8:11], s29 offen offset:1024 sc1\n"
    "v_mfma_f32_16x16x32_bf16 v[92:95], a[168:171], v[164:167], v[92:95]\n"
    "ds_read_b64_tr_b16 a[162:163], v246 offset:30720\n"
    "v_subrev_f32_e32 v130, s4, v130\n"
    "v_mfma_f32_16x16x32_bf16 v[96:99], a[168:171], v[172:175], v[96:99]\n"
    "ds_read_b64_tr_b16 a[190:191], v245 offset:55296\n"
    "v_subrev_f32_e32 v122, s4, v122\n"
    "v_mfma_f32_16x16x32_bf16 v[96:99], a[172:175], v[176:179], v[96:99]\n"
    "s_waitcnt lgkmcnt(4)\n"
    "ds_read_b64_tr_b16 a[168:169], v246 offset:4096\n"
    "v_subrev_f32_e32 v137, s4, v137\n"
    "v_mfma_f32_16x16x32_bf16 v[96:99], a[176:179], v[180:183], v[96:99]\n"
    "buffer_load_dwordx4 a[132:135], v250, s[8:11], s62 offen offset:3072 sc1\n"
    "v_mfma_f32_16x16x32_bf16 v[96:99], a[180:183], v[184:187], v[96:99]\n"
    "s_add_u32 s62, s57, 24576\n"
    "ds_read_b64_tr_b16 a[170:171], v246 offset:6144\n"
    "v_subrev_f32_e32 v138, s4, v138\n"
    "v_mfma_f32_16x16x32_bf16 v[92:95], a[180:183], v[152:155], v[92:95]\n"
    "ds_read_b64_tr_b16 a[164:165], v246 offset:61440\n"
    "v_subrev_f32_e32 v84, s4, v84\n"
    "v_mfma_f32_16x16x32_bf16 v[92:95], a[176:179], v[148:151], v[92:95]\n"
    "v_cmp_eq_u32_e32 vcc, 1, v252\n"
    "v_cvt_pk_bf16_f32 v73, v74, v75\n"
    "v_mfma_f32_16x16x32_bf16 v[88:91], a[176:179], v[140:143], v[88:91]\n"
    "ds_read_b64_tr_b16 a[166:167], v246 offset:63488\n"
    "v_cvt_pk_bf16_f32 v68, v68, v69\n"
    "v_mfma_f32_16x16x32_bf16 v[88:91], a[180:183], v[144:147], v[88:91]\n"
    "s_waitcnt lgkmcnt(4)\n"
    "ds_read_b64_tr_b16 a[176:177], v246 offset:12288\n"
    "v_cndmask_b32_e64 v234, v234, 1.0, vcc\n"
    "v_mfma_f32_16x16x32_bf16 v[88:91], a[184:187], v[196:199], v[88:91]\n"
    "ds_read_b64_tr_b16 a[172:173], v246 offset:36864\n"
    "v_subrev_f32_e32 v115, s4, v115\n"
    "v_mfma_f32_16x16x32_bf16 v[88:91], a[188:191], v[200:203], v[88:91]\n"
    "ds_read_b64_tr_b16 a[174:175], v246 offset:38912\n"
    "v_cvt_pk_bf16_f32 v69, v70, v71\n"
    "v_mfma_f32_16x16x32_bf16 v[92:95], a[188:191], v[208:211], v[92:95]\n"
    "buffer_load_dwordx4 a[136:139], v250, s[8:11], s62 offen offset:0 sc1\n"
    "v_mfma_f32_16x16x32_bf16 v[92:95], a[184:187], v[204:207], v[92:95]\n"
    "ds_read_b64_tr_b16 a[178:179], v246 offset:14336\n"
    "v_subrev_f32_e32 v5, s4, v5\n"
    "v_mfma_f32_16x16x32_bf16 v[96:99], a[184:187], v[212:215], v[96:99]\n"
    "v_cvt_pk_bf16_f32 v70, v88, v89\n"
    "v_cvt_pk_bf16_f32 v71, v90, v91\n"
    "v_mfma_f32_16x16x32_bf16 v[96:99], a[188:191], v[216:219], v[96:99]\n"
    "s_waitcnt lgkmcnt(4)\n"
    "ds_read_b64_tr_b16 a[180:181], v246 offset:45056\n"
    "v_cvt_pk_bf16_f32 v76, v76, v77\n"
    "v_mfma_f32_16x16x32_bf16 v[116:119], a[160:163], a[208:211], v[116:119]\n"
    "buffer_store_dwordx4 v[68:71], v250, s[8:11], s82 offen offset:2048 sc1\n"
    "v_mfma_f32_16x16x32_bf16 v[116:119], a[164:167], a[212:215], v[116:119]\n"
    "ds_read_b64_tr_b16 a[182:183], v246 offset:47104\n"
    "v_subrev_f32_e32 v111, s4, v111\n"
    "v_mfma_f32_16x16x32_bf16 v[112:115], a[164:167], a[204:207], v[112:115]\n"
    "ds_read_b64_tr_b16 a[184:185], v246 offset:20480\n"
    "v_subrev_f32_e32 v139, s4, v139\n"
    "v_mfma_f32_16x16x32_bf16 v[112:115], a[160:163], a[200:203], v[112:115]\n"
    "ds_read_b64_tr_b16 a[186:187], v246 offset:22528\n"
    "v_subrev_f32_e32 v134, s4, v134\n"
    "v_mfma_f32_16x16x32_bf16 v[108:111], a[160:163], a[192:195], v[108:111]\n"
    "buffer_load_dwordx4 a[140:143], v250, s[8:11], s62 offen offset:1024 sc1\n"
    "v_mfma_f32_16x16x32_bf16 v[108:111], a[164:167], a[196:199], v[108:111]\n"
    "s_waitcnt lgkmcnt(5)\n"
    "ds_read_b64_tr_b16 a[160:161], v247 offset:28672\n"
    "v_cmp_eq_u32_e32 vcc, 2, v252\n"
    "v_mfma_f32_16x16x32_bf16 v[108:111], a[168:171], v[156:159], v[108:111]\n"
    "v_cvt_pk_bf16_f32 v74, v92, v93\n"
    "v_subrev_f32_e32 v20, s4, v20\n"
    "v_mfma_f32_16x16x32_bf16 v[108:111], a[172:175], v[160:163], v[108:111]\n"
    "ds_read_b64_tr_b16 a[188:189], v246 offset:53248\n"
    "v_subrev_f32_e32 v25, s4, v25\n"
    "v_mfma_f32_16x16x32_bf16 v[112:115], a[172:175], v[168:171], v[112:115]\n"
    "ds_read_b64_tr_b16 a[162:163], v247 offset:30720\n"
    "v_subrev_f32_e32 v100, s4, v100\n"
    "v_mfma_f32_16x16x32_bf16 v[112:115], a[168:171], v[164:167], v[112:115]\n"
    "ds_read_b64_tr_b16 a[164:165], v247 offset:61440\n"
    "v_cndmask_b32_e32 v235, 0, v253, vcc\n"
    "v_mfma_f32_16x16x32_bf16 v[116:119], a[168:171], v[172:175], v[116:119]\n"
    "ds_read_b64_tr_b16 a[190:191], v246 offset:55296\n"
    "v_cvt_pk_bf16_f32 v77, v78, v79\n"
    "v_mfma_f32_16x16x32_bf16 v[116:119], a[172:175], v[176:179], v[116:119]\n"
    "s_waitcnt lgkmcnt(7)\n"
    "ds_read_b64_tr_b16 a[168:169], v247 offset:4096\n"
    "v_cvt_pk_bf16_f32 v75, v94, v95\n"
    "v_mfma_f32_16x16x32_bf16 v[116:119], a[176:179], v[180:183], v[116:119]\n"
    "ds_read_b64_tr_b16 a[170:171], v247 offset:6144\n"
    "v_subrev_f32_e32 v135, s4, v135\n"
    "v_mfma_f32_16x16x32_bf16 v[116:119], a[180:183], v[184:187], v[116:119]\n"
    "buffer_store_dwordx4 v[72:75], v250, s[8:11], s28 offen offset:2048 sc1\n"
    "v_mfma_f32_16x16x32_bf16 v[112:115], a[180:183], v[152:155], v[112:115]\n"
    "v_cvt_pk_bf16_f32 v78, v96, v97\n"
    "v_subrev_f32_e32 v6, s4, v6\n"
    "v_mfma_f32_16x16x32_bf16 v[112:115], a[176:179], v[148:151], v[112:115]\n"
    "ds_read_b64_tr_b16 a[166:167], v247 offset:63488\n"
    "v_subrev_f32_e32 v85, s4, v85\n"
    "v_mfma_f32_16x16x32_bf16 v[108:111], a[176:179], v[140:143], v[108:111]\n"
    "buffer_load_dwordx4 a[144:147], v250, s[8:11], s62 offen offset:2048 sc1\n"
    "v_mfma_f32_16x16x32_bf16 v[108:111], a[180:183], v[144:147], v[108:111]\n"
    "s_waitcnt lgkmcnt(3)\n"
    "ds_read_b64_tr_b16 a[172:173], v247 offset:36864\n"
    "v_cmp_eq_u32_e32 vcc, 3, v252\n"
    "v_mfma_f32_16x16x32_bf16 v[108:111], a[184:187], v[196:199], v[108:111]\n"
    "ds_read_b64_tr_b16 a[174:175], v247 offset:38912\n"
    "v_subrev_f32_e32 v83, s4, v83\n"
    "v_mfma_f32_16x16x32_bf16 v[108:111], a[188:191], v[200:203], v[108:111]\n"
    "ds_read_b64_tr_b16 a[176:177], v247 offset:12288\n"
    "v_cndmask_b32_e64 v235, v235, 1.0, vcc\n"
    "v_mfma_f32_16x16x32_bf16 v[112:115], a[188:191], v[208:211], v[112:115]\n"
    "v_subrev_f32_e32 v60, s4, v60\n"
    "v_subrev_f32_e32 v124, s4, v124\n"
    "v_mfma_f32_16x16x32_bf16 v[112:115], a[184:187], v[204:207], v[112:115]\n"
    "v_subrev_f32_e32 v131, s4, v131\n"
    "v_subrev_f32_e32 v123, s4, v123\n"
    "v_mfma_f32_16x16x32_bf16 v[116:119], a[184:187], v[212:215], v[116:119]\n"
    "ds_read_b64_tr_b16 a[178:179], v247 offset:14336\n"
    "v_subrev_f32_e32 v64, s4, v64\n"
    "v_mfma_f32_16x16x32_bf16 v[116:119], a[188:191], v[216:219], v[116:119]\n"
    "s_waitcnt lgkmcnt(4)\n"
    "v_cvt_pk_bf16_f32 v79, v98, v99\n"
    "v_cvt_pk_bf16_f32 v108, v108, v109\n"
    "v_mfma_f32_16x16x32_bf16 v[136:139], a[160:163], a[208:211], v[136:139]\n"
    "s_cmp_lg_u32 s44, 0\n"
    "s_cbranch_scc1 .Lat_glate\n"
    ".Lat_glate_ret:\n"
    "v_mfma_f32_16x16x32_bf16 v[136:139], a[164:167], a[212:215], v[136:139]\n"
    "ds_read_b64_tr_b16 a[180:181], v247 offset:45056\n"
    "v_mfma_f32_16x16x32_bf16 v[132:135], a[164:167], a[204:207], v[132:135]\n"
    "buffer_store_dwordx4 v[76:79], v250, s[8:11], s29 offen offset:2048 sc1\n"
    "v_mfma_f32_16x16x32_bf16 v[132:135], a[160:163], a[200:203], v[132:135]\n"
    "ds_read_b64_tr_b16 a[184:185], v247 offset:20480\n"
    "v_subrev_f32_e32 v65, s4, v65\n"
    "v_mfma_f32_16x16x32_bf16 v[128:131], a[160:163], a[192:195], v[128:131]\n"
    "ds_read_b64_tr_b16 a[182:183], v247 offset:47104\n"
    "v_mfma_f32_16x16x32_bf16 v[128:131], a[164:167], a[196:199], v[128:131]\n"
    "s_waitcnt lgkmcnt(5)\n"
    "ds_read_b64_tr_b16 a[186:187], v247 offset:22528\n"
    "v_subrev_f32_e32 v105, s4, v105\n"
    "v_mfma_f32_16x16x32_bf16 v[128:131], a[168:171], v[156:159], v[128:131]\n"
    "buffer_load_dwordx4 a[148:151], v250, s[8:11], s62 offen offset:3072 sc1\n"
    "v_mfma_f32_16x16x32_bf16 v[128:131], a[172:175], v[160:163], v[128:131]\n"
    "s_add_u32 s62, s57, 28672\n"
    "v_subrev_f32_e32 v106, s4, v106\n"
    "v_subrev_f32_e32 v101, s4, v101\n"
    "v_mfma_f32_16x16x32_bf16 v[132:135], a[172:175], v[168:171], v[132:135]\n"
    "ds_read_b64_tr_b16 a[188:189], v247 offset:53248\n"
    "v_subrev_f32_e32 v42, s4, v42\n"
    "v_mfma_f32_16x16x32_bf16 v[132:135], a[168:171], v[164:167], v[132:135]\n"
    "ds_read_b64_tr_b16 a[190:191], v247 offset:55296\n"
    "v_cvt_pk_bf16_f32 v112, v112, v113\n"
    "v_mfma_f32_16x16x32_bf16 v[136:139], a[168:171], v[172:175], v[136:139]\n"
    "ds_read_b128 a[160:163], v240 offset:24576\n"
    "v_subrev_f32_e32 v86, s4, v86\n"
    "v_mfma_f32_16x16x32_bf16 v[136:139], a[172:175], v[176:179], v[136:139]\n"
    "s_waitcnt lgkmcnt(4)\n"
    "v_subrev_f32_e32 v26, s4, v26\n"
    "v_mfma_f32_16x16x32_bf16 v[136:139], a[176:179], v[180:183], v[136:139]\n"
    "ds_read_b128 a[164:167], v241 offset:24576\n"
    "v_subrev_f32_e32 v107, s4, v107\n"
    "v_mfma_f32_16x16x32_bf16 v[136:139], a[180:183], v[184:187], v[136:139]\n"
    "v_subrev_f32_e32 v43, s4, v43\n"
    "v_subrev_f32_e32 v102, s4, v102\n"
    "v_mfma_f32_16x16x32_bf16 v[132:135], a[180:183], v[152:155], v[132:135]\n"
    "buffer_load_dwordx4 a[152:155], v250, s[8:11], s62 offen offset:0 sc1\n"
    "v_mfma_f32_16x16x32_bf16 v[132:135], a[176:179], v[148:151], v[132:135]\n"
    "v_subrev_f32_e32 v21, s4, v21\n"
    "v_subrev_f32_e32 v103, s4, v103\n"
    "v_mfma_f32_16x16x32_bf16 v[128:131], a[176:179], v[140:143], v[128:131]\n"
    "v_subrev_f32_e32 v2, s4, v2\n"
    "v_subrev_f32_e32 v61, s4, v61\n"
    "v_mfma_f32_16x16x32_bf16 v[128:131], a[180:183], v[144:147], v[128:131]\n"
    "s_waitcnt lgkmcnt(2)\n"
    "ds_read_b128 a[168:171], v240 offset:28672\n"
    "v_cvt_pk_bf16_f32 v116, v116, v117\n"
    "v_mfma_f32_16x16x32_bf16 v[128:131], a[184:187], v[196:199], v[128:131]\n"
    "v_cvt_pk_bf16_f32 v109, v110, v111\n"
    "v_subrev_f32_e32 v27, s4, v27\n"
    "v_mfma_f32_16x16x32_bf16 v[128:131], a[188:191], v[200:203], v[128:131]\n"
    "ds_read_b128 a[176:179], v242 offset:24576\n"
    "v_cvt_pk_bf16_f32 v117, v118, v119\n"
    "v_mfma_f32_16x16x32_bf16 v[132:135], a[188:191], v[208:211], v[132:135]\n"
    "ds_read_b128 a[172:175], v241 offset:28672\n"
    "v_subrev_f32_e32 v125, s4, v125\n"
    "v_mfma_f32_16x16x32_bf16 v[132:135], a[184:187], v[204:207], v[132:135]\n"
    "v_cvt_pk_bf16_f32 v110, v128, v129\n"
    "v_subrev_f32_e32 v87, s4, v87\n"
    "v_mfma_f32_16x16x32_bf16 v[136:139], a[184:187], v[212:215], v[136:139]\n"
    "ds_read_b128 a[180:183], v243 offset:24576\n"
    "v_cvt_pk_bf16_f32 v111, v130, v131\n"
    "v_mfma_f32_16x16x32_bf16 v[136:139], a[188:191], v[216:219], v[136:139]\n"
    "s_waitcnt lgkmcnt(4)\n"
    "ds_read_b128 a[184:187], v242 offset:28672\n"
    "v_subrev_f32_e32 v3, s4, v3\n"
    "v_mfma_f32_16x16x32_bf16 v[164:167], a[160:163], a[32:35], v[224:227]\n"
    "buffer_store_dwordx4 v[108:111], v250, s[8:11], s82 offen offset:3072 sc1\n"
    "v_mfma_f32_16x16x32_bf16 v[164:167], a[164:167], a[36:39], v[164:167]\n"
    "v_cvt_pk_bf16_f32 v113, v114, v115\n"
    "v_subrev_f32_e32 v46, s4, v46\n"
    "v_mfma_f32_16x16x32_bf16 v[156:159], a[164:167], a[4:7], v[220:223]\n"
    "ds_read_b128 a[188:191], v243 offset:28672\n"
    "v_mfma_f32_16x16x32_bf16 v[156:159], a[160:163], a[0:3], v[156:159]\n"
    "s_waitcnt lgkmcnt(3)\n"
    "v_subrev_f32_e32 v126, s4, v126\n"
    "v_mfma_f32_16x16x32_bf16 v[156:159], a[168:171], a[8:11], v[156:159]\n"
    "buffer_store_dword v131, v231, s[8:11], s87 offen offset:0 sc1\n"
    "v_mfma_f32_16x16x32_bf16 v[156:159], a[172:175], a[12:15], v[156:159]\n"
    "v_cvt_pk_bf16_f32 v114, v132, v133\n"
    "v_cvt_pk_bf16_f32 v118, v136, v137\n"
    "v_mfma_f32_16x16x32_bf16 v[164:167], a[172:175], a[44:47], v[164:167]\n"
    "ds_read_b128 a[160:163], v240 offset:26624\n"
    "v_cvt_pk_bf16_f32 v119, v138, v139\n"
    "v_mfma_f32_16x16x32_bf16 v[164:167], a[168:171], a[40:43], v[164:167]\n"
    "s_waitcnt lgkmcnt(3)\n"
    "ds_read_b128 a[164:167], v241 offset:26624\n"
    "v_subrev_f32_e32 v66, s4, v66\n"
    "v_mfma_f32_16x16x32_bf16 v[164:167], a[176:179], a[48:51], v[164:167]\n"
    "ds_read_b128 a[168:171], v240 offset:30720\n"
    "v_subrev_f32_e32 v67, s4, v67\n"
    "v_mfma_f32_16x16x32_bf16 v[164:167], a[180:183], a[52:55], v[164:167]\n"
    "buffer_store_dword v135, v231, s[8:11], s88 offen offset:0 sc1\n"
    "v_mfma_f32_16x16x32_bf16 v[156:159], a[180:183], a[20:23], v[156:159]\n"
    "v_cvt_pk_bf16_f32 v115, v134, v135\n"
    "v_subrev_f32_e32 v62, s4, v62\n"
    "v_mfma_f32_16x16x32_bf16 v[156:159], a[176:179], a[16:19], v[156:159]\n"
    "s_waitcnt lgkmcnt(3)\n"
    "ds_read_b128 a[172:175], v241 offset:30720\n"
    "v_subrev_f32_e32 v127, s4, v127\n"
    "v_mfma_f32_16x16x32_bf16 v[156:159], a[184:187], a[24:27], v[156:159]\n"
    "ds_read_b128 a[176:179], v242 offset:26624\n"
    "v_subrev_f32_e32 v22, s4, v22\n"
    "v_mfma_f32_16x16x32_bf16 v[156:159], a[188:191], a[28:31], v[156:159]\n"
    "buffer_store_dwordx4 v[112:115], v250, s[8:11], s28 offen offset:3072 sc1\n"
    "v_mfma_f32_16x16x32_bf16 v[164:167], a[188:191], a[60:63], v[164:167]\n"
    "ds_read_b128 a[180:183], v243 offset:26624\n"
    "v_subrev_f32_e32 v47, s4, v47\n"
    "v_mfma_f32_16x16x32_bf16 v[164:167], a[184:187], a[56:59], v[164:167]\n"
    "s_waitcnt lgkmcnt(4)\n"
    "ds_read_b128 a[184:187], v242 offset:30720\n"
    "ds_read_b128 a[188:191], v243 offset:30720\n"
    "v_mfma_f32_16x16x32_bf16 v[168:171], a[160:163], a[32:35], v[224:227]\n"
    "buffer_store_dword v139, v231, s[8:11], s89 offen offset:0 sc1\n"
    "v_mfma_f32_16x16x32_bf16 v[168:171], a[164:167], a[36:39], v[168:171]\n"
    "v_exp_f32_e32 v156, v156\n"
    "v_mfma_f32_16x16x32_bf16 v[160:163], a[164:167], a[4:7], v[220:223]\n"
    "v_exp_f32_e32 v157, v157\n"
    "v_mfma_f32_16x16x32_bf16 v[160:163], a[160:163], a[0:3], v[160:163]\n"
    "s_waitcnt lgkmcnt(4)\n"
    "ds_read_b128 a[160:163], v240 offset:0\n"
    "v_add_f32_e32 v128, v131, v135\n"
    "v_mfma_f32_16x16x32_bf16 v[160:163], a[168:171], a[8:11], v[160:163]\n"
    "buffer_load_dwordx4 a[156:159], v250, s[8:11], s62 offen offset:1024 sc1\n"
    "v_mfma_f32_16x16x32_bf16 v[160:163], a[172:175], a[12:15], v[160:163]\n"
    "s_add_u32 s62, s58, 0\n"
    "ds_read_b128 a[164:167], v241 offset:0\n"
    "v_min3_f32 v129, v131, v135, v139\n"
    "v_mfma_f32_16x16x32_bf16 v[168:171], a[172:175], a[44:47], v[168:171]\n"
    "v_exp_f32_e32 v158, v158\n"
    "v_mfma_f32_16x16x32_bf16 v[168:171], a[168:171], a[40:43], v[168:171]\n"
    "s_waitcnt lgkmcnt(4)\n"
    "ds_read_b128 a[168:171], v240 offset:4096\n"
    "v_add_f32_e32 v128, v128, v139\n"
    "v_mfma_f32_16x16x32_bf16 v[168:171], a[176:179], a[48:51], v[168:171]\n"
    "v_exp_f32_e32 v159, v159\n"
    "v_mfma_f32_16x16x32_bf16 v[168:171], a[180:183], a[52:55], v[168:171]\n"
    "ds_read_b128 a[172:175], v241 offset:4096\n"
    "v_cvt_pk_bf16_f32 v156, v156, v157\n"
    "v_mfma_f32_16x16x32_bf16 v[160:163], a[180:183], a[20:23], v[160:163]\n"
    "buffer_store_dwordx4 v[116:119], v250, s[8:11], s29 offen offset:3072 sc1\n"
    "v_mfma_f32_16x16x32_bf16 v[160:163], a[176:179], a[16:19], v[160:163]\n"
    "s_waitcnt lgkmcnt(4)\n"
    "v_exp_f32_e32 v164, v164\n"
    "v_mfma_f32_16x16x32_bf16 v[160:163], a[184:187], a[24:27], v[160:163]\n"
    "ds_write_addtid_b32 v156 offset:0\n"
    "v_exp_f32_e32 v165, v165\n"
    "v_mfma_f32_16x16x32_bf16 v[160:163], a[188:191], a[28:31], v[160:163]\n"
    "buffer_load_dwordx4 a[244:247], v250, s[8:11], s62 offen offset:0 sc1\n"
    "ds_read_b128 a[176:179], v242 offset:0\n"
    "v_mfma_f32_16x16x32_bf16 v[168:171], a[188:191], a[60:63], v[168:171]\n"
    "ds_read_b128 a[180:183], v243 offset:0\n"
    "v_cvt_pk_bf16_f32 v157, v158, v159\n"
    "v_mfma_f32_16x16x32_bf16 v[168:171], a[184:187], a[56:59], v[168:171]\n"
    "s_waitcnt lgkmcnt(5)\n"
    "v_exp_f32_e32 v166, v166\n"
    "v_mfma_f32_16x16x32_bf16 v[204:207], a[160:163], a[32:35], v[224:227]\n"
    "v_exp_f32_e32 v167, v167\n"
    "v_mfma_f32_16x16x32_bf16 v[204:207], a[164:167], a[36:39], v[204:207]\n"
    "ds_read_b128 a[184:187], v242 offset:4096\n"
    "v_cvt_pk_bf16_f32 v164, v164, v165\n"
    "v_mfma_f32_16x16x32_bf16 v[196:199], a[164:167], a[4:7], v[220:223]\n"
    "v_exp_f32_e32 v160, v160\n"
    "v_mfma_f32_16x16x32_bf16 v[196:199], a[160:163], a[0:3], v[196:199]\n"
    "s_waitcnt lgkmcnt(4)\n"
    "ds_read_b128 a[160:163], v240 offset:2048\n"
    "v_exp_f32_e32 v161, v161\n"
    "v_mfma_f32_16x16x32_bf16 v[196:199], a[168:171], a[8:11], v[196:199]\n"
    "ds_read_b128 a[188:191], v243 offset:4096\n"
    "ds_write_addtid_b32 v157 offset:256\n"
    "v_mfma_f32_16x16x32_bf16 v[196:199], a[172:175], a[12:15], v[196:199]\n"
    "buffer_load_dwordx4 v[8:11], v250, s[8:11], s62 offen offset:1024 sc1\n"
    "v_mfma_f32_16x16x32_bf16 v[204:207], a[172:175], a[44:47], v[204:207]\n"
    "ds_read_b128 a[164:167], v241 offset:2048\n"
    "v_cvt_pk_bf16_f32 v165, v166, v167\n"
    "v_mfma_f32_16x16x32_bf16 v[204:207], a[168:171], a[40:43], v[204:207]\n"
    "s_waitcnt lgkmcnt(5)\n"
    "v_exp_f32_e32 v162, v162\n"
    "v_mfma_f32_16x16x32_bf16 v[204:207], a[176:179], a[48:51], v[204:207]\n"
    "v_exp_f32_e32 v163, v163\n"
    "v_mfma_f32_16x16x32_bf16 v[204:207], a[180:183], a[52:55], v[204:207]\n"
    "ds_read_b128 a[168:171], v240 offset:6144\n"
    "ds_write_addtid_b32 v164 offset:1024\n"
    "v_mfma_f32_16x16x32_bf16 v[196:199], a[180:183], a[20:23], v[196:199]\n"
    "ds_read_b128 a[172:175], v241 offset:6144\n"
    "v_cvt_pk_bf16_f32 v158, v160, v161\n"
    "v_mfma_f32_16x16x32_bf16 v[196:199], a[176:179], a[16:19], v[196:199]\n"
    "s_waitcnt lgkmcnt(5)\n"
    "v_exp_f32_e32 v168, v168\n"
    "v_mfma_f32_16x16x32_bf16 v[196:199], a[184:187], a[24:27], v[196:199]\n"
    "v_exp_f32_e32 v169, v169\n"
    "v_mfma_f32_16x16x32_bf16 v[196:199], a[188:191], a[28:31], v[196:199]\n"
    "buffer_load_dwordx4 a[248:251], v250, s[8:11], s62 offen offset:2048 sc1\n"
    "v_mfma_f32_16x16x32_bf16 v[204:207], a[188:191], a[60:63], v[204:207]\n"
    "ds_read_b128 a[176:179], v242 offset:2048\n"
    "v_cvt_pk_bf16_f32 v159, v162, v163\n"
    "v_mfma_f32_16x16x32_bf16 v[204:207], a[184:187], a[56:59], v[204:207]\n"
    "s_waitcnt lgkmcnt(4)\n"
    "ds_read_b128 a[180:183], v243 offset:2048\n"
    "v_exp_f32_e32 v170, v170\n"
    "v_mfma_f32_16x16x32_bf16 v[208:211], a[160:163], a[32:35], v[224:227]\n"
    "ds_read_b128 a[184:187], v242 offset:6144\n"
    "ds_write_addtid_b32 v165 offset:1280\n"
    "v_mfma_f32_16x16x32_bf16 v[208:211], a[164:167], a[36:39], v[208:211]\n"
    "ds_read_b128 a[188:191], v243 offset:6144\n"
    "v_exp_f32_e32 v196, v196\n"
    "v_mfma_f32_16x16x32_bf16 v[200:203], a[164:167], a[4:7], v[220:223]\n"
    "v_exp_f32_e32 v171, v171\n"
    "v_mfma_f32_16x16x32_bf16 v[200:203], a[160:163], a[0:3], v[200:203]\n"
    "s_waitcnt lgkmcnt(5)\n"
    "v_exp_f32_e32 v197, v197\n"
    "v_mfma_f32_16x16x32_bf16 v[200:203], a[168:171], a[8:11], v[200:203]\n"
    "v_exp_f32_e32 v198, v198\n"
    "v_mfma_f32_16x16x32_bf16 v[200:203], a[172:175], a[12:15], v[200:203]\n"
    "buffer_load_dwordx4 v[12:15], v250, s[8:11], s62 offen offset:3072 sc1\n"
    "ds_read_b128 a[160:163], v240 offset:8192\n"
    "v_mfma_f32_16x16x32_bf16 v[208:211], a[172:175], a[44:47], v[208:211]\n"
    "s_add_u32 s62, s58, 4096\n"
    "v_exp_f32_e32 v199, v199\n"
    "v_mfma_f32_16x16x32_bf16 v[208:211], a[168:171], a[40:43], v[208:211]\n"
    "s_waitcnt lgkmcnt(4)\n"
    "ds_read_b128 a[164:167], v241 offset:8192\n"
    "v_cvt_pk_bf16_f32 v196, v196, v197\n"
    "v_mfma_f32_16x16x32_bf16 v[208:211], a[176:179], a[48:51], v[208:211]\n"
    "ds_read_b128 a[168:171], v240 offset:12288\n"
    "v_exp_f32_e32 v204, v204\n"
    "v_mfma_f32_16x16x32_bf16 v[208:211], a[180:183], a[52:55], v[208:211]\n"
    "v_exp_f32_e32 v205, v205\n"
    "v_mfma_f32_16x16x32_bf16 v[200:203], a[180:183], a[20:23], v[200:203]\n"
    "v_cvt_pk_bf16_f32 v197, v198, v199\n"
    "v_exp_f32_e32 v206, v206\n"
    "v_mfma_f32_16x16x32_bf16 v[200:203], a[176:179], a[16:19], v[200:203]\n"
    "s_waitcnt lgkmcnt(3)\n"
    "v_exp_f32_e32 v207, v207\n"
    "v_mfma_f32_16x16x32_bf16 v[200:203], a[184:187], a[24:27], v[200:203]\n"
    "ds_read_b128 a[172:175], v241 offset:12288\n"
    "v_cvt_pk_bf16_f32 v204, v204, v205\n"
    "v_mfma_f32_16x16x32_bf16 v[200:203], a[188:191], a[28:31], v[200:203]\n"
    "buffer_load_dwordx4 a[252:255], v250, s[8:11], s62 offen offset:0 sc1\n"
    "v_mfma_f32_16x16x32_bf16 v[208:211], a[188:191], a[60:63], v[208:211]\n"
    "ds_read_b128 a[176:179], v242 offset:8192\n"
    "v_cvt_pk_bf16_f32 v166, v168, v169\n"
    "v_mfma_f32_16x16x32_bf16 v[208:211], a[184:187], a[56:59], v[208:211]\n"
    "s_waitcnt lgkmcnt(3)\n"
    "v_exp_f32_e32 v200, v200\n"
    "v_mfma_f32_16x16x32_bf16 v[212:215], a[160:163], a[32:35], v[224:227]\n"
    "ds_read_b128 a[180:183], v243 offset:8192\n"
    "v_exp_f32_e32 v201, v201\n"
    "v_mfma_f32_16x16x32_bf16 v[212:215], a[164:167], a[36:39], v[212:215]\n"
    "v_cvt_pk_bf16_f32 v205, v206, v207\n"
    "v_exp_f32_e32 v202, v202\n"
    "v_mfma_f32_16x16x32_bf16 v[172:175], a[164:167], a[4:7], v[220:223]\n"
    "v_exp_f32_e32 v203, v203\n"
    "v_mfma_f32_16x16x32_bf16 v[172:175], a[160:163], a[0:3], v[172:175]\n"
    "s_waitcnt lgkmcnt(2)\n"
    "ds_read_b128 a[184:187], v242 offset:12288\n"
    "v_cvt_pk_bf16_f32 v198, v200, v201\n"
    "v_mfma_f32_16x16x32_bf16 v[172:175], a[168:171], a[8:11], v[172:175]\n"
    "ds_read_b128 a[188:191], v243 offset:12288\n"
    "v_exp_f32_e32 v208, v208\n"
    "v_mfma_f32_16x16x32_bf16 v[172:175], a[172:175], a[12:15], v[172:175]\n"
    "buffer_load_dwordx4 v[16:19], v250, s[8:11], s62 offen offset:1024 sc1\n"
    "v_mfma_f32_16x16x32_bf16 v[212:215], a[172:175], a[44:47], v[212:215]\n"
    "s_add_u32 s62, s57, 28672\n"
    "ds_read_b128 a[160:163], v240 offset:10240\n"
    "v_subrev_f32_e32 v23, s4, v23\n"
    "v_mfma_f32_16x16x32_bf16 v[212:215], a[168:171], a[40:43], v[212:215]\n"
    "s_waitcnt lgkmcnt(3)\n"
    "v_exp_f32_e32 v209, v209\n"
    "v_mfma_f32_16x16x32_bf16 v[212:215], a[176:179], a[48:51], v[212:215]\n"
    "ds_read_b128 a[164:167], v241 offset:10240\n"
    "ds_read_b128 a[168:171], v240 offset:14336\n"
    "v_mfma_f32_16x16x32_bf16 v[212:215], a[180:183], a[52:55], v[212:215]\n"
    "ds_write_addtid_b32 v158 offset:512\n"
    "ds_write_addtid_b32 v159 offset:768\n"
    "v_mfma_f32_16x16x32_bf16 v[172:175], a[180:183], a[20:23], v[172:175]\n"
    "ds_write_addtid_b32 v166 offset:1536\n"
    "v_cvt_pk_bf16_f32 v167, v170, v171\n"
    "v_mfma_f32_16x16x32_bf16 v[172:175], a[176:179], a[16:19], v[172:175]\n"
    "s_waitcnt lgkmcnt(6)\n"
    "ds_read_b128 a[172:175], v241 offset:14336\n"
    "ds_read_b128 a[176:179], v242 offset:10240\n"
    "v_mfma_f32_16x16x32_bf16 v[172:175], a[184:187], a[24:27], v[172:175]\n"
    "ds_read_b128 a[180:183], v243 offset:10240\n"
    "v_cvt_pk_bf16_f32 v199, v202, v203\n"
    "v_mfma_f32_16x16x32_bf16 v[172:175], a[188:191], a[28:31], v[172:175]\n"
    "buffer_load_dwordx4 v[180:183], v250, s[8:11], s62 offen offset:2048 sc1\n"
    "v_mfma_f32_16x16x32_bf16 v[212:215], a[188:191], a[60:63], v[212:215]\n"
    "ds_write_addtid_b32 v167 offset:1792\n"
    "v_subrev_f32_e32 v63, s4, v63\n"
    "v_mfma_f32_16x16x32_bf16 v[212:215], a[184:187], a[56:59], v[212:215]\n"
    "s_waitcnt lgkmcnt(8)\n"
    "ds_read_b128 a[184:187], v242 offset:14336\n"
    "v_exp_f32_e32 v210, v210\n"
    "v_mfma_f32_16x16x32_bf16 v[216:219], a[160:163], a[32:35], v[224:227]\n"
    "ds_read_b128 a[188:191], v243 offset:14336\n"
    "v_exp_f32_e32 v172, v172\n"
    "v_mfma_f32_16x16x32_bf16 v[216:219], a[164:167], a[36:39], v[216:219]\n"
    "s_waitcnt lgkmcnt(2)\n"
    "v_mfma_f32_16x16x32_bf16 v[176:179], a[164:167], a[4:7], v[220:223]\n"
    "s_barrier\n"
    "v_mfma_f32_16x16x32_bf16 v[176:179], a[160:163], a[0:3], v[176:179]\n"
    "s_waitcnt vmcnt(7)\n"
    "buffer_store_dword v254, v231, s[8:11], s38 offen offset:0 sc1\n"
    "v_mfma_f32_16x16x32_bf16 v[176:179], a[168:171], a[8:11], v[176:179]\n"
    "ds_read_b64_tr_b16 a[160:161], v244 offset:24576\n"
    "v_exp_f32_e32 v173, v173\n"
    "v_mfma_f32_16x16x32_bf16 v[176:179], a[172:175], a[12:15], v[176:179]\n"
    "ds_read_b64_tr_b16 a[162:163], v244 offset:26624\n"
    "ds_read2st64_b32 v[160:161], v249 offset0:0 offset1:1\n"
    "v_mfma_f32_16x16x32_bf16 v[216:219], a[172:175], a[44:47], v[216:219]\n"
    "buffer_load_dwordx4 v[184:187], v250, s[8:11], s62 offen offset:3072 sc1\n"
    "ds_read2st64_b32 v[162:163], v249 offset0:2 offset1:3\n"
    "v_mfma_f32_16x16x32_bf16 v[216:219], a[168:171], a[40:43], v[216:219]\n"
    "ds_read_b64_tr_b16 a[164:165], v244 offset:57344\n"
    "ds_read_b64_tr_b16 a[166:167], v244 offset:59392\n"
    "v_mfma_f32_16x16x32_bf16 v[216:219], a[176:179], a[48:51], v[216:219]\n"
    "ds_read2st64_b32 v[168:169], v249 offset0:4 offset1:5\n"
    "ds_read2st64_b32 v[170:171], v249 offset0:6 offset1:7\n"
    "v_mfma_f32_16x16x32_bf16 v[216:219], a[180:183], a[52:55], v[216:219]\n"
    "ds_write_addtid_b32 v196 offset:2048\n"
    "v_exp_f32_e32 v174, v174\n"
    "v_mfma_f32_16x16x32_bf16 v[176:179], a[180:183], a[20:23], v[176:179]\n"
    "s_add_u32 s62, s58, 4096\n"
    "ds_read_b64_tr_b16 a[168:169], v245 offset:24576\n"
    "v_exp_f32_e32 v175, v175\n"
    "v_mfma_f32_16x16x32_bf16 v[176:179], a[176:179], a[16:19], v[176:179]\n"
    "s_waitcnt lgkmcnt(10)\n"
    "ds_read_b64_tr_b16 a[170:171], v245 offset:26624\n"
    "ds_read_b64_tr_b16 a[172:173], v245 offset:57344\n"
    "v_mfma_f32_16x16x32_bf16 v[176:179], a[184:187], a[24:27], v[176:179]\n"
    "buffer_load_dwordx4 v[28:31], v250, s[8:11], s62 offen offset:2048 sc1\n"
    "ds_read_b64_tr_b16 a[174:175], v245 offset:59392\n"
    "v_mfma_f32_16x16x32_bf16 v[176:179], a[188:191], a[28:31], v[176:179]\n"
    ".long 0xbedf00ff\n"
    ".Lat_w12:\n"
    "s_setpc_b64 s[2:3]\n"
    "ds_read_b64_tr_b16 a[176:177], v246 offset:24576\n"
    "v_subrev_f32_e32 v7, s4, v7\n"
    "v_mfma_f32_16x16x32_bf16 v[216:219], a[188:191], a[60:63], v[216:219]\n"
    "ds_read_b64_tr_b16 a[178:179], v246 offset:26624\n"
    "v_cvt_pk_bf16_f32 v172, v172, v173\n"
    "v_mfma_f32_16x16x32_bf16 v[216:219], a[184:187], a[56:59], v[216:219]\n"
    "s_waitcnt lgkmcnt(7)\n"
    "ds_read_b64_tr_b16 a[180:181], v246 offset:57344\n"
    "ds_read_b64_tr_b16 a[182:183], v246 offset:59392\n"
    "v_mfma_f32_16x16x32_bf16 a[240:243], a[160:163], v[164:167], a[240:243]\n"
    "buffer_load_dwordx4 v[48:51], v250, s[8:11], s62 offen offset:3072 sc1\n"
    "ds_read_b64_tr_b16 a[184:185], v247 offset:24576\n"
    "v_mfma_f32_16x16x32_bf16 a[240:243], a[164:167], v[168:171], a[240:243]\n"
    "s_add_u32 s62, s58, 8192\n"
    "ds_read_b64_tr_b16 a[186:187], v247 offset:26624\n"
    "v_exp_f32_e32 v212, v212\n"
    "v_mfma_f32_16x16x32_bf16 a[236:239], a[164:167], v[160:163], a[236:239]\n"
    "ds_read_b64_tr_b16 a[188:189], v247 offset:57344\n"
    "v_exp_f32_e32 v213, v213\n"
    "v_mfma_f32_16x16x32_bf16 a[236:239], a[160:163], v[156:159], a[236:239]\n"
    "s_waitcnt lgkmcnt(7)\n"
    "ds_read_b64_tr_b16 a[160:161], v244 offset:28672\n"
    "v_cvt_pk_bf16_f32 v173, v174, v175\n"
    "v_mfma_f32_16x16x32_bf16 v[0:3], a[168:171], v[156:159], v[0:3]\n"
    "buffer_load_dwordx4 v[32:35], v250, s[8:11], s62 offen offset:0 sc1\n"
    "ds_read_b64_tr_b16 a[190:191], v247 offset:59392\n"
    "v_mfma_f32_16x16x32_bf16 v[0:3], a[172:175], v[160:163], v[0:3]\n"
    "ds_read_b64_tr_b16 a[162:163], v244 offset:30720\n"
    "v_exp_f32_e32 v214, v214\n"
    "v_mfma_f32_16x16x32_bf16 v[4:7], a[172:175], v[168:171], v[4:7]\n"
    "ds_read_b64_tr_b16 a[164:165], v244 offset:61440\n"
    "v_exp_f32_e32 v211, v211\n"
    "v_mfma_f32_16x16x32_bf16 v[4:7], a[168:171], v[164:167], v[4:7]\n"
    "s_waitcnt lgkmcnt(7)\n"
    "ds_read_b64_tr_b16 a[166:167], v244 offset:63488\n"
    "ds_read_b64_tr_b16 a[168:169], v245 offset:28672\n"
    "v_mfma_f32_16x16x32_bf16 v[24:27], a[176:179], v[164:167], v[24:27]\n"
    "buffer_load_dwordx4 v[52:55], v250, s[8:11], s62 offen offset:1024 sc1\n"
    "ds_read_b64_tr_b16 a[170:171], v245 offset:30720\n"
    "v_mfma_f32_16x16x32_bf16 v[24:27], a[180:183], v[168:171], v[24:27]\n"
    "ds_write_addtid_b32 v197 offset:2304\n"
    "v_exp_f32_e32 v215, v215\n"
    "v_mfma_f32_16x16x32_bf16 v[20:23], a[180:183], v[160:163], v[20:23]\n"
    "ds_read_b64_tr_b16 a[172:173], v245 offset:61440\n"
    "ds_write_addtid_b32 v172 offset:4096\n"
    "v_mfma_f32_16x16x32_bf16 v[20:23], a[176:179], v[156:159], v[20:23]\n"
    "s_waitcnt lgkmcnt(8)\n"
    "ds_read_b64_tr_b16 a[174:175], v245 offset:63488\n"
    "ds_read_b64_tr_b16 a[176:177], v246 offset:28672\n"
    "v_mfma_f32_16x16x32_bf16 v[40:43], a[184:187], v[156:159], v[40:43]\n"
    "buffer_load_dwordx4 v[36:39], v250, s[8:11], s62 offen offset:2048 sc1\n"
    "v_mfma_f32_16x16x32_bf16 v[40:43], a[188:191], v[160:163], v[40:43]\n"
    "ds_read_b64_tr_b16 a[178:179], v246 offset:30720\n"
    "v_cvt_pk_bf16_f32 v206, v208, v209\n"
    "v_mfma_f32_16x16x32_bf16 v[44:47], a[188:191], v[168:171], v[44:47]\n"
    "ds_read_b64_tr_b16 a[180:181], v246 offset:61440\n"
    "v_cvt_pk_bf16_f32 v212, v212, v213\n"
    "v_mfma_f32_16x16x32_bf16 v[44:47], a[184:187], v[164:167], v[44:47]\n"
    "s_waitcnt lgkmcnt(9)\n"
    "ds_read_b64_tr_b16 a[182:183], v246 offset:63488\n"
    "v_exp_f32_e32 v176, v176\n"
    "v_mfma_f32_16x16x32_bf16 v[64:67], a[160:163], v[164:167], v[64:67]\n"
    "buffer_load_dwordx4 v[56:59], v250, s[8:11], s62 offen offset:3072 sc1\n"
    "ds_read_b64_tr_b16 a[184:185], v247 offset:28672\n"
    "v_mfma_f32_16x16x32_bf16 v[64:67], a[164:167], v[168:171], v[64:67]\n"
    "s_add_u32 s62, s58, 12288\n"
    "v_exp_f32_e32 v177, v177\n"
    "v_mfma_f32_16x16x32_bf16 v[60:63], a[164:167], v[160:163], v[60:63]\n"
    "ds_read_b64_tr_b16 a[186:187], v247 offset:30720\n"
    "v_cvt_pk_bf16_f32 v213, v214, v215\n"
    "v_mfma_f32_16x16x32_bf16 v[60:63], a[160:163], v[156:159], v[60:63]\n"
    "s_waitcnt lgkmcnt(6)\n"
    "ds_read_b64_tr_b16 a[188:189], v247 offset:61440\n"
    "v_exp_f32_e32 v178, v178\n"
    "v_mfma_f32_16x16x32_bf16 v[80:83], a[168:171], v[156:159], v[80:83]\n"
    "buffer_load_dwordx4 v[68:71], v250, s[8:11], s62 offen offset:0 sc1\n"
    "ds_read_b64_tr_b16 a[190:191], v247 offset:63488\n"
    "v_mfma_f32_16x16x32_bf16 v[80:83], a[172:175], v[160:163], v[80:83]\n"
    "ds_write_addtid_b32 v204 offset:3072\n"
    "ds_write_addtid_b32 v205 offset:3328\n"
    "v_mfma_f32_16x16x32_bf16 v[84:87], a[172:175], v[168:171], v[84:87]\n"
    "ds_read_b128 a[160:163], v240 offset:16384\n"
    "ds_write_addtid_b32 v198 offset:2560\n"
    "v_mfma_f32_16x16x32_bf16 v[84:87], a[168:171], v[164:167], v[84:87]\n"
    "s_waitcnt lgkmcnt(8)\n"
    "ds_write_addtid_b32 v199 offset:2816\n"
    "v_exp_f32_e32 v179, v179\n"
    "v_mfma_f32_16x16x32_bf16 v[104:107], a[176:179], v[164:167], v[104:107]\n"
    "buffer_load_dwordx4 v[88:91], v250, s[8:11], s62 offen offset:1024 sc1\n"
    "ds_read_b128 a[164:167], v241 offset:16384\n"
    "v_mfma_f32_16x16x32_bf16 v[104:107], a[180:183], v[168:171], v[104:107]\n"
    "s_waitcnt lgkmcnt(6)\n"
    "v_mfma_f32_16x16x32_bf16 v[100:103], a[180:183], v[160:163], v[100:103]\n"
    "s_barrier\n"
    "v_mfma_f32_16x16x32_bf16 v[100:103], a[176:179], v[156:159], v[100:103]\n"
    "ds_read_b128 a[168:171], v240 offset:20480\n"
    "v_cvt_pk_bf16_f32 v174, v176, v177\n"
    "v_mfma_f32_16x16x32_bf16 v[120:123], a[184:187], v[156:159], v[120:123]\n"
    "s_add_u32 s31, s47, s35\n"
    "ds_read_b128 a[172:175], v241 offset:20480\n"
    "ds_read_b128 a[176:179], v242 offset:16384\n"
    "v_mfma_f32_16x16x32_bf16 v[120:123], a[188:191], v[160:163], v[120:123]\n"
    "s_add_u32 m0, s55, 24576\n"
    "ds_read_b128 a[180:183], v243 offset:16384\n"
    "v_exp_f32_e32 v216, v216\n"
    "v_mfma_f32_16x16x32_bf16 v[124:127], a[188:191], v[168:171], v[124:127]\n"
    "buffer_load_dwordx4 v251, s[8:11], s31 offen offset:0 sc1 lds\n"
    "v_mfma_f32_16x16x32_bf16 v[124:127], a[184:187], v[164:167], v[124:127]\n"
    "s_waitcnt lgkmcnt(4)\n"
    "v_exp_f32_e32 v217, v217\n"
    "v_mfma_f32_16x16x32_bf16 v[148:151], a[160:163], a[32:35], v[224:227]\n"
    "s_mov_b32 m0, s63\n"
    "ds_read_b128 a[184:187], v242 offset:20480\n"
    "v_cvt_pk_bf16_f32 v207, v210, v211\n"
    "v_mfma_f32_16x16x32_bf16 v[148:151], a[164:167], a[36:39], v[148:151]\n"
    "buffer_load_dword v253, v238, s[8:11], s48 offen offset:0 sc1\n"
    "v_mfma_f32_16x16x32_bf16 v[140:143], a[164:167], a[4:7], v[220:223]\n"
    "s_addk_i32 s31, 0x1000\n"
    "ds_write_addtid_b32 v206 offset:3584\n"
    "ds_write_addtid_b32 v173 offset:4352\n"
    "v_mfma_f32_16x16x32_bf16 v[140:143], a[160:163], a[0:3], v[140:143]\n"
    "s_waitcnt lgkmcnt(5)\n"
    "ds_read_b128 a[188:191], v243 offset:20480\n"
    "v_cvt_pk_bf16_f32 v175, v178, v179\n"
    "v_mfma_f32_16x16x32_bf16 v[140:143], a[168:171], a[8:11], v[140:143]\n"
    "s_add_u32 m0, s55, 25600\n"
    "ds_read_b128 a[160:163], v240 offset:18432\n"
    "ds_read_b128 a[164:167], v241 offset:18432\n"
    "v_mfma_f32_16x16x32_bf16 v[140:143], a[172:175], a[12:15], v[140:143]\n"
    "buffer_load_dwordx4 v251, s[8:11], s31 offen offset:0 sc1 lds\n"
    "v_mfma_f32_16x16x32_bf16 v[148:151], a[172:175], a[44:47], v[148:151]\n"
    "s_mov_b32 m0, s63\n"
    "v_exp_f32_e32 v218, v218\n"
    "v_mfma_f32_16x16x32_bf16 v[148:151], a[168:171], a[40:43], v[148:151]\n"
    "s_waitcnt lgkmcnt(6)\n"
    "ds_write_addtid_b32 v207 offset:3840\n"
    "v_exp_f32_e32 v219, v219\n"
    "v_mfma_f32_16x16x32_bf16 v[148:151], a[176:179], a[48:51], v[148:151]\n"
    "s_add_u32 s87, s59, 8192\n"
    "ds_write_addtid_b32 v212 offset:5120\n"
    "ds_write_addtid_b32 v213 offset:5376\n"
    "v_mfma_f32_16x16x32_bf16 v[148:151], a[180:183], a[52:55], v[148:151]\n"
    "s_add_u32 s31, s47, s36\n"
    "ds_write_addtid_b32 v174 offset:4608\n"
    "ds_write_addtid_b32 v175 offset:4864\n"
    "v_mfma_f32_16x16x32_bf16 v[140:143], a[180:183], a[20:23], v[140:143]\n"
    "s_add_u32 m0, s55, 26624\n"
    "ds_read_b128 a[168:171], v240 offset:22528\n"
    "v_cvt_pk_bf16_f32 v214, v216, v217\n"
    "v_mfma_f32_16x16x32_bf16 v[140:143], a[176:179], a[16:19], v[140:143]\n"
    "s_waitcnt lgkmcnt(8)\n"
    "ds_read_b128 a[172:175], v241 offset:22528\n"
    "ds_read_b128 a[176:179], v242 offset:18432\n"
    "v_mfma_f32_16x16x32_bf16 v[140:143], a[184:187], a[24:27], v[140:143]\n"
    "buffer_load_dwordx4 v251, s[8:11], s31 offen offset:0 sc1 lds\n"
    "v_mfma_f32_16x16x32_bf16 v[140:143], a[188:191], a[28:31], v[140:143]\n"
    "s_mov_b32 m0, s63\n"
    "ds_read_b128 a[180:183], v243 offset:18432\n"
    "v_cvt_pk_bf16_f32 v215, v218, v219\n"
    "v_mfma_f32_16x16x32_bf16 v[148:151], a[188:191], a[60:63], v[148:151]\n"
    "s_add_u32 s88, s59, 16384\n"
    "ds_write_addtid_b32 v214 offset:5632\n"
    "ds_write_addtid_b32 v215 offset:5888\n"
    "v_mfma_f32_16x16x32_bf16 v[148:151], a[184:187], a[56:59], v[148:151]\n"
    "s_waitcnt lgkmcnt(11)\n"
    "ds_read_b128 a[184:187], v242 offset:22528\n"
    "v_exp_f32_e32 v140, v140\n"
    "v_mfma_f32_16x16x32_bf16 v[152:155], a[160:163], a[32:35], v[224:227]\n"
    "s_addk_i32 s31, 0x1000\n"
    "ds_read_b128 a[188:191], v243 offset:22528\n"
    "v_exp_f32_e32 v141, v141\n"
    "v_mfma_f32_16x16x32_bf16 v[152:155], a[164:167], a[36:39], v[152:155]\n"
    "s_waitcnt lgkmcnt(2)\n"
    "v_mfma_f32_16x16x32_bf16 v[144:147], a[164:167], a[4:7], v[220:223]\n"
    "s_barrier\n"
    "v_mfma_f32_16x16x32_bf16 v[144:147], a[160:163], a[0:3], v[144:147]\n"
    "ds_read_b64_tr_b16 a[160:161], v244 offset:0\n"
    "ds_read2st64_b32 v[200:201], v249 offset0:8 offset1:9\n"
    "v_mfma_f32_16x16x32_bf16 v[144:147], a[168:171], a[8:11], v[144:147]\n"
    "s_add_u32 m0, s55, 27648\n"
    "ds_read_b64_tr_b16 a[162:163], v244 offset:2048\n"
    "v_exp_f32_e32 v142, v142\n"
    "v_mfma_f32_16x16x32_bf16 v[144:147], a[172:175], a[12:15], v[144:147]\n"
    "buffer_load_dwordx4 v251, s[8:11], s31 offen offset:0 sc1 lds\n"
    "ds_read2st64_b32 v[202:203], v249 offset0:10 offset1:11\n"
    "v_mfma_f32_16x16x32_bf16 v[152:155], a[172:175], a[44:47], v[152:155]\n"
    "s_mov_b32 m0, s63\n"
    "ds_read_b64_tr_b16 a[164:165], v244 offset:32768\n"
    "v_exp_f32_e32 v143, v143\n"
    "v_mfma_f32_16x16x32_bf16 v[152:155], a[168:171], a[40:43], v[152:155]\n"
    "ds_read2st64_b32 v[208:209], v249 offset0:12 offset1:13\n"
    "ds_read2st64_b32 v[210:211], v249 offset0:14 offset1:15\n"
    "v_cvt_pk_bf16_f32 v140, v140, v141\n"
    "v_mfma_f32_16x16x32_bf16 v[152:155], a[176:179], a[48:51], v[152:155]\n"
    "s_add_u32 s89, s59, 24576\n"
    "ds_read_b64_tr_b16 a[166:167], v244 offset:34816\n"
    "v_exp_f32_e32 v148, v148\n"
    "v_mfma_f32_16x16x32_bf16 v[152:155], a[180:183], a[52:55], v[152:155]\n"
    "s_add_u32 s31, s47, s37\n"
    "ds_read_b64_tr_b16 a[168:169], v245 offset:0\n"
    "v_exp_f32_e32 v149, v149\n"
    "v_mfma_f32_16x16x32_bf16 v[144:147], a[180:183], a[20:23], v[144:147]\n"
    "s_add_u32 m0, s55, 28672\n"
    "ds_read_b64_tr_b16 a[170:171], v245 offset:2048\n"
    "v_cvt_pk_bf16_f32 v141, v142, v143\n"
    "v_mfma_f32_16x16x32_bf16 v[144:147], a[176:179], a[16:19], v[144:147]\n"
    "s_waitcnt lgkmcnt(10)\n"
    "ds_read_b64_tr_b16 a[172:173], v245 offset:32768\n"
    "ds_read_b64_tr_b16 a[174:175], v245 offset:34816\n"
    "v_mfma_f32_16x16x32_bf16 v[144:147], a[184:187], a[24:27], v[144:147]\n"
    "buffer_load_dwordx4 v251, s[8:11], s31 offen offset:0 sc1 lds\n"
    "ds_read2st64_b32 v[176:177], v249 offset0:16 offset1:17\n"
    "v_mfma_f32_16x16x32_bf16 v[144:147], a[188:191], a[28:31], v[144:147]\n"
    "s_mov_b32 m0, s63\n"
    "ds_read_b64_tr_b16 a[176:177], v246 offset:0\n"
    "ds_read_b64_tr_b16 a[178:179], v246 offset:2048\n"
    "v_mfma_f32_16x16x32_bf16 v[152:155], a[188:191], a[60:63], v[152:155]\n"
    "ds_read2st64_b32 v[178:179], v249 offset0:18 offset1:19\n"
    "v_exp_f32_e32 v150, v150\n"
    "v_mfma_f32_16x16x32_bf16 v[152:155], a[184:187], a[56:59], v[152:155]\n"
    "s_waitcnt lgkmcnt(8)\n"
    "ds_read_b64_tr_b16 a[180:181], v246 offset:32768\n"
    "ds_read_b64_tr_b16 a[182:183], v246 offset:34816\n"
    "v_mfma_f32_16x16x32_bf16 a[240:243], a[160:163], v[204:207], a[240:243]\n"
    "s_addk_i32 s31, 0x1000\n"
    "ds_read_b64_tr_b16 a[184:185], v247 offset:0\n"
    "ds_write_addtid_b32 v140 offset:0\n"
    "v_mfma_f32_16x16x32_bf16 a[240:243], a[164:167], v[208:211], a[240:243]\n"
    "s_add_u32 m0, s55, 29696\n"
    "ds_read2st64_b32 v[216:217], v249 offset0:20 offset1:21\n"
    "v_exp_f32_e32 v151, v151\n"
    "v_mfma_f32_16x16x32_bf16 a[236:239], a[164:167], v[200:203], a[236:239]\n"
    "buffer_load_dwordx4 v251, s[8:11], s31 offen offset:0 sc1 lds\n"
    "ds_read_b64_tr_b16 a[186:187], v247 offset:2048\n"
    "v_mfma_f32_16x16x32_bf16 a[236:239], a[160:163], v[196:199], a[236:239]\n"
    "s_waitcnt lgkmcnt(10)\n"
    "ds_read_b64_tr_b16 a[188:189], v247 offset:32768\n"
    "ds_read2st64_b32 v[218:219], v249 offset0:22 offset1:23\n"
    "v_mfma_f32_16x16x32_bf16 v[0:3], a[168:171], v[196:199], v[0:3]\n"
    "s_mov_b32 m0, s63\n"
    "ds_read_b64_tr_b16 a[190:191], v247 offset:34816\n"
    "ds_read_b64_tr_b16 a[160:161], v244 offset:4096\n"
    "v_mfma_f32_16x16x32_bf16 v[0:3], a[172:175], v[200:203], v[0:3]\n"
    "buffer_load_dwordx4 v[72:75], v250, s[8:11], s62 offen offset:2048 sc1\n"
    "ds_read_b64_tr_b16 a[162:163], v244 offset:6144\n"
    "v_mfma_f32_16x16x32_bf16 v[4:7], a[172:175], v[208:211], v[4:7]\n"
    "s_add_u32 s31, s46, s36\n"
    "ds_read_b64_tr_b16 a[164:165], v244 offset:36864\n"
    "v_cvt_pk_bf16_f32 v148, v148, v149\n"
    "v_mfma_f32_16x16x32_bf16 v[4:7], a[168:171], v[204:207], v[4:7]\n"
    "s_waitcnt lgkmcnt(10)\n"
    "ds_read_b64_tr_b16 a[166:167], v244 offset:38912\n"
    "v_exp_f32_e32 v144, v144\n"
    "v_mfma_f32_16x16x32_bf16 v[24:27], a[176:179], v[204:207], v[24:27]\n"
    "s_add_u32 m0, s55, 30720\n"
    "s_waitcnt vmcnt(6)\n"
    "ds_read_b64_tr_b16 a[168:169], v245 offset:4096\n"
    "v_subrev_u32_e32 v253, s39, v253\n"
    "v_mfma_f32_16x16x32_bf16 v[24:27], a[180:183], v[208:211], v[24:27]\n"
    "buffer_load_dwordx4 v251, s[12:15], s31 offen offset:0 lds\n"
    "ds_read_b64_tr_b16 a[170:171], v245 offset:6144\n"
    "v_cmp_le_u32_e32 vcc, 1, v253\n"
    "v_min_u32_e32 v220, 0x13c, v248\n"
    "v_mfma_f32_16x16x32_bf16 v[20:23], a[180:183], v[200:203], v[20:23]\n"
    "s_mov_b32 m0, s63\n"
    "ds_read_b64_tr_b16 a[172:173], v245 offset:36864\n"
    "ds_read_b64_tr_b16 a[174:175], v245 offset:38912\n"
    "v_mfma_f32_16x16x32_bf16 v[20:23], a[176:179], v[196:199], v[20:23]\n"
    "s_waitcnt lgkmcnt(8)\n"
    "v_exp_f32_e32 v145, v145\n"
    "v_mfma_f32_16x16x32_bf16 v[40:43], a[184:187], v[196:199], v[40:43]\n"
    "s_cbranch_vccnz .Lat_k\n"
    ".Lat_kr:\n"
    ".Lat_kxr:\n"
    "v_mfma_f32_16x16x32_bf16 v[40:43], a[188:191], v[200:203], v[40:43]\n"
    "s_addk_i32 s31, 0x1000\n"
    "ds_read_b64_tr_b16 a[176:177], v246 offset:4096\n"
    "ds_read_b64_tr_b16 a[178:179], v246 offset:6144\n"
    "v_mfma_f32_16x16x32_bf16 v[44:47], a[188:191], v[208:211], v[44:47]\n"
    "s_add_u32 m0, s55, 31744\n"
    "ds_read_b64_tr_b16 a[180:181], v246 offset:36864\n"
    "v_cvt_pk_bf16_f32 v149, v150, v151\n"
    "v_mfma_f32_16x16x32_bf16 v[44:47], a[184:187], v[204:207], v[44:47]\n"
    "s_waitcnt lgkmcnt(7)\n"
    "v_exp_f32_e32 v146, v146\n"
    "v_mfma_f32_16x16x32_bf16 v[64:67], a[160:163], v[204:207], v[64:67]\n"
    "buffer_load_dwordx4 v251, s[12:15], s31 offen offset:0 lds\n"
    "ds_read_b64_tr_b16 a[182:183], v246 offset:38912\n"
    "v_mfma_f32_16x16x32_bf16 v[64:67], a[164:167], v[208:211], v[64:67]\n"
    "s_mov_b32 m0, s63\n"
    "ds_read_b64_tr_b16 a[184:185], v247 offset:4096\n"
    "v_exp_f32_e32 v147, v147\n"
    "v_mfma_f32_16x16x32_bf16 v[60:63], a[164:167], v[200:203], v[60:63]\n"
    "buffer_load_dwordx4 a[0:3], v250, s[8:11], s87 offen offset:0 sc1\n"
    "ds_read_b64_tr_b16 a[186:187], v247 offset:6144\n"
    "v_mfma_f32_16x16x32_bf16 v[60:63], a[160:163], v[196:199], v[60:63]\n"
    "s_waitcnt lgkmcnt(6)\n"
    "ds_read_b64_tr_b16 a[188:189], v247 offset:36864\n"
    "ds_read_b64_tr_b16 a[190:191], v247 offset:38912\n"
    "v_mfma_f32_16x16x32_bf16 v[80:83], a[168:171], v[196:199], v[80:83]\n"
    "ds_write_addtid_b32 v141 offset:256\n"
    "v_cvt_pk_bf16_f32 v142, v144, v145\n"
    "v_mfma_f32_16x16x32_bf16 v[80:83], a[172:175], v[200:203], v[80:83]\n"
    "buffer_load_dwordx4 v[92:95], v250, s[8:11], s62 offen offset:3072 sc1\n"
    "ds_read_b64_tr_b16 a[160:161], v244 offset:12288\n"
    "v_mfma_f32_16x16x32_bf16 v[84:87], a[172:175], v[208:211], v[84:87]\n"
    "ds_read_b64_tr_b16 a[162:163], v244 offset:14336\n"
    "v_exp_f32_e32 v152, v152\n"
    "v_mfma_f32_16x16x32_bf16 v[84:87], a[168:171], v[204:207], v[84:87]\n"
    "s_waitcnt lgkmcnt(7)\n"
    "ds_read_b64_tr_b16 a[164:165], v244 offset:45056\n"
    "ds_read_b64_tr_b16 a[166:167], v244 offset:47104\n"
    "v_mfma_f32_16x16x32_bf16 v[104:107], a[176:179], v[204:207], v[104:107]\n"
    "ds_read_b64_tr_b16 a[168:169], v245 offset:12288\n"
    "ds_read_b64_tr_b16 a[170:171], v245 offset:14336\n"
    "v_mfma_f32_16x16x32_bf16 v[104:107], a[180:183], v[208:211], v[104:107]\n"
    "buffer_load_dwordx4 a[4:7], v250, s[8:11], s88 offen offset:0 sc1\n"
    "ds_read_b64_tr_b16 a[172:173], v245 offset:45056\n"
    "v_mfma_f32_16x16x32_bf16 v[100:103], a[180:183], v[200:203], v[100:103]\n"
    "ds_read_b64_tr_b16 a[174:175], v245 offset:47104\n"
    "v_exp_f32_e32 v153, v153\n"
    "v_mfma_f32_16x16x32_bf16 v[100:103], a[176:179], v[196:199], v[100:103]\n"
    "s_waitcnt lgkmcnt(9)\n"
    "ds_write_addtid_b32 v148 offset:1024\n"
    "ds_write_addtid_b32 v149 offset:1280\n"
    "v_mfma_f32_16x16x32_bf16 v[120:123], a[184:187], v[196:199], v[120:123]\n"
    "ds_read_b64_tr_b16 a[176:177], v246 offset:12288\n"
    "v_cvt_pk_bf16_f32 v143, v146, v147\n"
    "v_mfma_f32_16x16x32_bf16 v[120:123], a[188:191], v[200:203], v[120:123]\n"
    "ds_read_b64_tr_b16 a[178:179], v246 offset:14336\n"
    "v_exp_f32_e32 v154, v154\n"
    "v_mfma_f32_16x16x32_bf16 v[124:127], a[188:191], v[208:211], v[124:127]\n"
    "ds_write_addtid_b32 v142 offset:512\n"
    "v_exp_f32_e32 v155, v155\n"
    "v_mfma_f32_16x16x32_bf16 v[124:127], a[184:187], v[204:207], v[124:127]\n"
    "s_waitcnt lgkmcnt(9)\n"
    "ds_read_b64_tr_b16 a[180:181], v246 offset:45056\n"
    "ds_read_b64_tr_b16 a[184:185], v247 offset:12288\n"
    "v_mfma_f32_16x16x32_bf16 v[64:67], a[160:163], v[212:215], v[64:67]\n"
    "buffer_load_dwordx4 a[8:11], v250, s[8:11], s89 offen offset:0 sc1\n"
    "ds_read_b64_tr_b16 a[182:183], v246 offset:47104\n"
    "v_mfma_f32_16x16x32_bf16 v[64:67], a[164:167], v[216:219], v[64:67]\n"
    "ds_read_b64_tr_b16 a[186:187], v247 offset:14336\n"
    "ds_read_b64_tr_b16 a[188:189], v247 offset:45056\n"
    "v_mfma_f32_16x16x32_bf16 v[60:63], a[164:167], v[176:179], v[60:63]\n"
    "ds_write_addtid_b32 v143 offset:768\n"
    "v_cvt_pk_bf16_f32 v150, v152, v153\n"
    "v_mfma_f32_16x16x32_bf16 v[60:63], a[160:163], v[172:175], v[60:63]\n"
    "s_waitcnt lgkmcnt(11)\n"
    "ds_read_b64_tr_b16 a[190:191], v247 offset:47104\n"
    "v_cvt_pk_bf16_f32 v151, v154, v155\n"
    "v_mfma_f32_16x16x32_bf16 v[80:83], a[168:171], v[172:175], v[80:83]\n"
    "ds_write_addtid_b32 v150 offset:1536\n"
    "ds_write_addtid_b32 v151 offset:1792\n"
    "v_mfma_f32_16x16x32_bf16 v[80:83], a[172:175], v[176:179], v[80:83]\n"
    "ds_read_b64_tr_b16 a[160:161], v244 offset:8192\n"
    "ds_read_b64_tr_b16 a[162:163], v244 offset:10240\n"
    "v_mfma_f32_16x16x32_bf16 v[84:87], a[172:175], v[216:219], v[84:87]\n"
    "ds_read_b64_tr_b16 a[164:165], v244 offset:40960\n"
    "ds_read_b64_tr_b16 a[166:167], v244 offset:43008\n"
    "v_mfma_f32_16x16x32_bf16 v[84:87], a[168:171], v[212:215], v[84:87]\n"
    "s_waitcnt lgkmcnt(10)\n"
    "s_waitcnt lgkmcnt(4)\n"
    "s_waitcnt vmcnt(7)\n"
    "v_mfma_f32_16x16x32_bf16 v[104:107], a[176:179], v[212:215], v[104:107]\n"
    "s_barrier\n"
    "v_mfma_f32_16x16x32_bf16 v[104:107], a[180:183], v[216:219], v[104:107]\n"
    "buffer_load_dword v220, v220, s[8:11], s60 offen offset:192 sc1\n"
    "ds_read_b64_tr_b16 a[168:169], v244 offset:16384\n"
    "v_mfma_f32_16x16x32_bf16 v[100:103], a[180:183], v[176:179], v[100:103]\n"
    "s_add_u32 s31, s46, s37\n"
    "ds_read2st64_b32 v[144:145], v249 offset0:0 offset1:1\n"
    "ds_read2st64_b32 v[146:147], v249 offset0:2 offset1:3\n"
    "v_mfma_f32_16x16x32_bf16 v[100:103], a[176:179], v[172:175], v[100:103]\n"
    "ds_read_b64_tr_b16 a[170:171], v244 offset:18432\n"
    "ds_read_b64_tr_b16 a[172:173], v244 offset:49152\n"
    "v_mfma_f32_16x16x32_bf16 v[120:123], a[184:187], v[172:175], v[120:123]\n"
    "s_add_u32 m0, s55, 4096\n"
    "ds_read_b64_tr_b16 a[174:175], v244 offset:51200\n"
    "ds_read2st64_b32 v[152:153], v249 offset0:4 offset1:5\n"
    "v_mfma_f32_16x16x32_bf16 v[120:123], a[188:191], v[176:179], v[120:123]\n"
    "buffer_load_dwordx4 v251, s[12:15], s31 offen offset:0 lds\n"
    "ds_read2st64_b32 v[154:155], v249 offset0:6 offset1:7\n"
    "v_mfma_f32_16x16x32_bf16 v[124:127], a[188:191], v[216:219], v[124:127]\n"
    "ds_read_b64_tr_b16 a[176:177], v245 offset:8192\n"
    "ds_read_b64_tr_b16 a[178:179], v245 offset:10240\n"
    "v_mfma_f32_16x16x32_bf16 v[124:127], a[184:187], v[212:215], v[124:127]\n"
    "s_waitcnt lgkmcnt(10)\n"
    "ds_read_b64_tr_b16 a[180:181], v245 offset:40960\n"
    "ds_read_b64_tr_b16 a[182:183], v245 offset:43008\n"
    "v_mfma_f32_16x16x32_bf16 a[240:243], a[160:163], v[212:215], a[240:243]\n"
    "buffer_load_dwordx4 a[12:15], v250, s[8:11], s87 offen offset:1024 sc1\n"
    "v_mfma_f32_16x16x32_bf16 a[240:243], a[164:167], v[216:219], a[240:243]\n"
    "s_addk_i32 s31, 0x1000\n"
    "ds_read_b64_tr_b16 a[184:185], v245 offset:16384\n"
    "v_add_u32_e32 v240, s45, v240\n"
    "v_mfma_f32_16x16x32_bf16 a[236:239], a[164:167], v[176:179], a[236:239]\n"
    "s_add_u32 m0, s55, 5120\n"
    "ds_read_b64_tr_b16 a[186:187], v245 offset:18432\n"
    "v_xor_b32_e32 v241, 64, v240\n"
    "v_mfma_f32_16x16x32_bf16 a[236:239], a[160:163], v[172:175], a[236:239]\n"
    "s_waitcnt lgkmcnt(6)\n"
    "ds_read_b64_tr_b16 a[188:189], v245 offset:49152\n"
    "ds_read_b64_tr_b16 a[190:191], v245 offset:51200\n"
    "v_mfma_f32_16x16x32_bf16 a[236:239], a[168:171], v[140:143], a[236:239]\n"
    "buffer_load_dwordx4 v251, s[12:15], s31 offen offset:0 lds\n"
    "v_mfma_f32_16x16x32_bf16 a[236:239], a[172:175], v[144:147], a[236:239]\n"
    "ds_read_b64_tr_b16 a[160:161], v246 offset:8192\n"
    "v_add_u32_e32 v242, 0x10000, v240\n"
    "v_mfma_f32_16x16x32_bf16 a[240:243], a[172:175], v[152:155], a[240:243]\n"
    "ds_read_b64_tr_b16 a[162:163], v246 offset:10240\n"
    "ds_read_b64_tr_b16 a[164:165], v246 offset:40960\n"
    "v_mfma_f32_16x16x32_bf16 a[240:243], a[168:171], v[148:151], a[240:243]\n"
    "s_waitcnt lgkmcnt(7)\n"
    "ds_read_b64_tr_b16 a[166:167], v246 offset:43008\n"
    "v_add_u32_e32 v243, 0x10000, v241\n"
    "v_mfma_f32_16x16x32_bf16 v[4:7], a[176:179], v[212:215], v[4:7]\n"
    "buffer_load_dwordx4 a[16:19], v250, s[8:11], s88 offen offset:1024 sc1\n"
    "v_mfma_f32_16x16x32_bf16 v[4:7], a[180:183], v[216:219], v[4:7]\n"
    "ds_read_b64_tr_b16 a[168:169], v246 offset:16384\n"
    "v_mfma_f32_16x16x32_bf16 v[0:3], a[180:183], v[176:179], v[0:3]\n"
    "ds_read_b64_tr_b16 a[170:171], v246 offset:18432\n"
    "v_add_u32_e32 v252, s54, v240\n"
    "v_mfma_f32_16x16x32_bf16 v[0:3], a[176:179], v[172:175], v[0:3]\n"
    "s_waitcnt lgkmcnt(6)\n"
    "ds_read_b64_tr_b16 a[172:173], v246 offset:49152\n"
    "v_xor_b32_e32 v255, 64, v252\n"
    "v_mfma_f32_16x16x32_bf16 v[0:3], a[184:187], v[140:143], v[0:3]\n"
    "ds_read_b64_tr_b16 a[174:175], v246 offset:51200\n"
    "v_lshlrev_b32_e32 v228, 16, v228\n"
    "v_mfma_f32_16x16x32_bf16 v[0:3], a[188:191], v[144:147], v[0:3]\n"
    "ds_read_b64_tr_b16 a[176:177], v247 offset:8192\n"
    "v_lshlrev_b32_e32 v229, 16, v229\n"
    "v_mfma_f32_16x16x32_bf16 v[4:7], a[188:191], v[152:155], v[4:7]\n"
    "ds_read_b64_tr_b16 a[178:179], v247 offset:10240\n"
    "v_lshlrev_b32_e32 v230, 16, v230\n"
    "v_mfma_f32_16x16x32_bf16 v[4:7], a[184:187], v[148:151], v[4:7]\n"
    "s_waitcnt lgkmcnt(6)\n"
    "ds_read_b64_tr_b16 a[180:181], v247 offset:40960\n"
    "ds_read_b64_tr_b16 a[182:183], v247 offset:43008\n"
    "v_mfma_f32_16x16x32_bf16 v[24:27], a[160:163], v[212:215], v[24:27]\n"
    "buffer_load_dwordx4 a[20:23], v250, s[8:11], s89 offen offset:1024 sc1\n"
    "v_mfma_f32_16x16x32_bf16 v[24:27], a[164:167], v[216:219], v[24:27]\n"
    "ds_read_b64_tr_b16 a[184:185], v247 offset:16384\n"
    "v_cvt_pk_bf16_f32 v7, v6, v7\n"
    "v_mfma_f32_16x16x32_bf16 v[20:23], a[164:167], v[176:179], v[20:23]\n"
    "ds_read_b64_tr_b16 a[186:187], v247 offset:18432\n"
    "v_cvt_pk_bf16_f32 v6, v4, v5\n"
    "v_mfma_f32_16x16x32_bf16 v[20:23], a[160:163], v[172:175], v[20:23]\n"
    "s_waitcnt lgkmcnt(6)\n"
    "ds_read_b64_tr_b16 a[160:161], v244 offset:20480\n"
    "v_accvgpr_read_b32 v4, a240\n"
    "v_mfma_f32_16x16x32_bf16 v[20:23], a[168:171], v[140:143], v[20:23]\n"
    "ds_read_b64_tr_b16 a[188:189], v247 offset:49152\n"
    "ds_read_b64_tr_b16 a[190:191], v247 offset:51200\n"
    "v_mfma_f32_16x16x32_bf16 v[20:23], a[172:175], v[144:147], v[20:23]\n"
    "ds_read_b64_tr_b16 a[162:163], v244 offset:22528\n"
    "v_mfma_f32_16x16x32_bf16 v[24:27], a[172:175], v[152:155], v[24:27]\n"
    "ds_read_b64_tr_b16 a[164:165], v244 offset:53248\n"
    "v_accvgpr_read_b32 v5, a241\n"
    "v_mfma_f32_16x16x32_bf16 v[24:27], a[168:171], v[148:151], v[24:27]\n"
    "s_waitcnt lgkmcnt(7)\n"
    "ds_read_b64_tr_b16 a[168:169], v245 offset:20480\n"
    "v_accvgpr_read_b32 v225, a243\n"
    "v_mfma_f32_16x16x32_bf16 v[44:47], a[176:179], v[212:215], v[44:47]\n"
    "ds_read_b64_tr_b16 a[166:167], v244 offset:55296\n"
    "ds_read_b64_tr_b16 a[170:171], v245 offset:22528\n"
    "v_mfma_f32_16x16x32_bf16 v[44:47], a[180:183], v[216:219], v[44:47]\n"
    "ds_read_b64_tr_b16 a[172:173], v245 offset:53248\n"
    "ds_read_b64_tr_b16 a[174:175], v245 offset:55296\n"
    "v_mfma_f32_16x16x32_bf16 v[40:43], a[180:183], v[176:179], v[40:43]\n"
    "v_cvt_pk_bf16_f32 v4, v4, v5\n"
    "v_mfma_f32_16x16x32_bf16 v[40:43], a[176:179], v[172:175], v[40:43]\n"
    "s_waitcnt lgkmcnt(7)\n"
    "ds_read_b64_tr_b16 a[176:177], v246 offset:20480\n"
    "v_accvgpr_read_b32 v5, a242\n"
    "v_mfma_f32_16x16x32_bf16 v[40:43], a[184:187], v[140:143], v[40:43]\n"
    "ds_read_b64_tr_b16 a[178:179], v246 offset:22528\n"
    "ds_read_b64_tr_b16 a[180:181], v246 offset:53248\n"
    "v_mfma_f32_16x16x32_bf16 v[40:43], a[188:191], v[144:147], v[40:43]\n"
    "ds_read_b64_tr_b16 a[182:183], v246 offset:55296\n"
    "v_cvt_pk_bf16_f32 v5, v5, v225\n"
    "v_mfma_f32_16x16x32_bf16 v[44:47], a[188:191], v[152:155], v[44:47]\n"
    "buffer_store_dwordx4 v[4:7], v250, s[8:11], s80 offen offset:0 sc1\n"
    "v_mfma_f32_16x16x32_bf16 v[44:47], a[184:187], v[148:151], v[44:47]\n"
    "s_waitcnt lgkmcnt(7)\n"
    "ds_read_b64_tr_b16 a[184:185], v247 offset:20480\n"
    "v_mfma_f32_16x16x32_bf16 v[64:67], a[160:163], v[148:151], v[64:67]\n"
    "ds_read_b64_tr_b16 a[186:187], v247 offset:22528\n"
    "v_mfma_f32_16x16x32_bf16 v[64:67], a[164:167], v[152:155], v[64:67]\n"
    "s_waitcnt lgkmcnt(6)\n"
    "ds_read_b64_tr_b16 a[188:189], v247 offset:53248\n"
    "ds_read_b64_tr_b16 a[190:191], v247 offset:55296\n"
    "v_mfma_f32_16x16x32_bf16 v[84:87], a[172:175], v[152:155], v[84:87]\n"
    "ds_read_b128 a[48:51], v240 offset:0\n"
    "v_cvt_pk_bf16_f32 v24, v24, v25\n"
    "v_mfma_f32_16x16x32_bf16 v[84:87], a[168:171], v[148:151], v[84:87]\n"
    "s_waitcnt lgkmcnt(5)\n"
    "ds_read_b128 a[52:55], v241 offset:0\n"
    "v_cvt_pk_bf16_f32 v25, v26, v27\n"
    "v_mfma_f32_16x16x32_bf16 v[104:107], a[176:179], v[148:151], v[104:107]\n"
    "v_cvt_pk_bf16_f32 v26, v44, v45\n"
    "v_mfma_f32_16x16x32_bf16 v[104:107], a[180:183], v[152:155], v[104:107]\n"
    "s_waitcnt lgkmcnt(2)\n"
    "ds_read_b128 a[56:59], v240 offset:32768\n"
    "v_cvt_pk_bf16_f32 v27, v46, v47\n"
    "v_mfma_f32_16x16x32_bf16 v[124:127], a[188:191], v[152:155], v[124:127]\n"
    "ds_read_b128 a[60:63], v241 offset:32768\n"
    "v_mfma_f32_16x16x32_bf16 v[124:127], a[184:187], v[148:151], v[124:127]\n"
    "buffer_store_dwordx4 v[24:27], v250, s[8:11], s80 offen offset:1024 sc1\n"
    "v_mfma_f32_16x16x32_bf16 v[60:63], a[164:167], v[144:147], v[60:63]\n"
    "v_cvt_pk_bf16_f32 v64, v64, v65\n"
    "v_mfma_f32_16x16x32_bf16 v[60:63], a[160:163], v[140:143], v[60:63]\n"
    "v_cvt_pk_bf16_f32 v65, v66, v67\n"
    "v_mfma_f32_16x16x32_bf16 v[80:83], a[168:171], v[140:143], v[80:83]\n"
    "v_cvt_pk_bf16_f32 v66, v84, v85\n"
    "v_cvt_pk_bf16_f32 v67, v86, v87\n"
    "v_mfma_f32_16x16x32_bf16 v[80:83], a[172:175], v[144:147], v[80:83]\n"
    "buffer_store_dwordx4 v[64:67], v250, s[8:11], s80 offen offset:2048 sc1\n"
    "v_mfma_f32_16x16x32_bf16 v[100:103], a[180:183], v[144:147], v[100:103]\n"
    "s_mov_b32 s45, 0x71800000\n"
    "v_cvt_pk_bf16_f32 v104, v104, v105\n"
    "v_cvt_pk_bf16_f32 v105, v106, v107\n"
    "v_mfma_f32_16x16x32_bf16 v[100:103], a[176:179], v[140:143], v[100:103]\n"
    "v_cvt_pk_bf16_f32 v106, v124, v125\n"
    "v_cvt_pk_bf16_f32 v107, v126, v127\n"
    "v_mfma_f32_16x16x32_bf16 v[120:123], a[184:187], v[140:143], v[120:123]\n"
    "buffer_store_dwordx4 v[104:107], v250, s[8:11], s80 offen offset:3072 sc1\n"
    "v_mfma_f32_16x16x32_bf16 v[120:123], a[188:191], v[144:147], v[120:123]\n"
    "s_waitcnt lgkmcnt(2)\n"
    "buffer_store_dword v127, v231, s[8:11], s86 offen offset:0 sc1\n"
    "v_mfma_f32_16x16x32_bf16 v[156:159], a[216:219], a[48:51], 0\n"
    "ds_read_b128 a[160:163], v242 offset:0\n"
    "v_mfma_f32_16x16x32_bf16 v[160:163], a[220:223], a[48:51], 0\n"
    "ds_read_b128 a[164:167], v243 offset:0\n"
    "v_mfma_f32_16x16x32_bf16 v[164:167], a[224:227], a[48:51], 0\n"
    "ds_read_b128 a[168:171], v242 offset:32768\n"
    "v_add_f32_e32 v225, v123, v127\n"
    "v_mfma_f32_16x16x32_bf16 v[168:171], a[228:231], a[48:51], 0\n"
    "ds_read_b128 a[172:175], v243 offset:32768\n"
    "v_min3_f32 v129, v129, v123, v127\n"
    "v_mfma_f32_16x16x32_bf16 v[156:159], a[232:235], a[52:55], v[156:159]\n"
    "ds_read_b128 a[176:179], v240 offset:2048\n"
    "v_add_f32_e32 v225, v225, v128\n"
    "v_mfma_f32_16x16x32_bf16 v[160:163], v[192:195], a[52:55], v[160:163]\n"
    "ds_read_b128 a[180:183], v241 offset:2048\n"
    "v_mov_b32_e32 v227, v123\n"
    "v_mfma_f32_16x16x32_bf16 v[164:167], a[64:67], a[52:55], v[164:167]\n"
    "ds_read_b128 a[184:187], v240 offset:34816\n"
    "v_mfma_f32_16x16x32_bf16 v[168:171], a[68:71], a[52:55], v[168:171]\n"
    "s_waitcnt lgkmcnt(7)\n"
    "ds_read_b128 a[188:191], v241 offset:34816\n"
    "v_cmp_nlt_f32_e32 vcc, 0x18000000, v129\n"
    "v_mfma_f32_16x16x32_bf16 v[156:159], a[72:75], a[56:59], v[156:159]\n"
    "ds_read_b128 a[48:51], v242 offset:2048\n"
    "v_cmp_nlt_f32_e64 s[28:29], v225, s45\n"
    "v_mfma_f32_16x16x32_bf16 v[160:163], a[76:79], a[56:59], v[160:163]\n"
    "buffer_load_dwordx4 a[24:27], v250, s[8:11], s87 offen offset:2048 sc1\n"
    "v_mfma_f32_16x16x32_bf16 v[164:167], a[80:83], a[56:59], v[164:167]\n"
    "s_or_b32 s45, s29, vcc_hi\n"
    "v_mfma_f32_16x16x32_bf16 v[168:171], a[84:87], a[56:59], v[168:171]\n"
    "s_lshr_b32 s45, s45, 16\n"
    "s_cselect_b32 s45, s51, 0\n"
    "v_mfma_f32_16x16x32_bf16 v[156:159], a[88:91], a[60:63], v[156:159]\n"
    "s_andn2_b32 s45, s45, s49\n"
    "ds_read_b128 a[52:55], v243 offset:2048\n"
    "v_mfma_f32_16x16x32_bf16 v[160:163], a[92:95], a[60:63], v[160:163]\n"
    "s_add_u32 s45, s45, s39\n"
    "v_mfma_f32_16x16x32_bf16 v[164:167], a[96:99], a[60:63], v[164:167]\n"
    "buffer_load_dwordx4 a[28:31], v250, s[8:11], s88 offen offset:2048 sc1\n"
    "v_mov_b32_e32 v254, s45\n"
    "v_mfma_f32_16x16x32_bf16 v[168:171], a[100:103], a[60:63], v[168:171]\n"
    "s_waitcnt lgkmcnt(8)\n"
    "v_mfma_f32_16x16x32_bf16 v[156:159], a[104:107], a[160:163], v[156:159]\n"
    "s_lshl1_add_u32 s55, s50, s51\n"
    "ds_read_b128 a[56:59], v242 offset:34816\n"
    ".long 0xbedf00ff\n"
    ".Lat_w13:\n"
    "s_setpc_b64 s[2:3]\n"
    "v_mfma_f32_16x16x32_bf16 v[160:163], a[108:111], a[160:163], v[160:163]\n"
    "s_lshl_b32 s55, s55, 12\n"
    "v_mfma_f32_16x16x32_bf16 v[164:167], a[112:115], a[160:163], v[164:167]\n"
    "s_xor_b32 s31, s93, 2\n"
    "v_mfma_f32_16x16x32_bf16 v[168:171], a[116:119], a[160:163], v[168:171]\n"
    "buffer_load_dwordx4 a[32:35], v250, s[8:11], s89 offen offset:2048 sc1\n"
    "v_mfma_f32_16x16x32_bf16 v[156:159], a[120:123], a[164:167], v[156:159]\n"
    "s_lshl_b32 s31, s31, 8\n"
    "ds_read_b128 a[60:63], v243 offset:34816\n"
    "v_mfma_f32_16x16x32_bf16 v[160:163], a[124:127], a[164:167], v[160:163]\n"
    "s_addk_i32 s31, 0x6000\n"
    "v_mfma_f32_16x16x32_bf16 v[164:167], a[128:131], a[164:167], v[164:167]\n"
    "s_mul_i32 s45, s49, 252\n"
    "v_mfma_f32_16x16x32_bf16 v[168:171], a[132:135], a[164:167], v[168:171]\n"
    "s_waitcnt lgkmcnt(8)\n"
    "v_mfma_f32_16x16x32_bf16 v[156:159], a[136:139], a[168:171], v[156:159]\n"
    "buffer_load_dwordx4 a[36:39], v250, s[8:11], s87 offen offset:3072 sc1\n"
    "ds_read_b128 a[160:163], v240 offset:4096\n"
    "v_mfma_f32_16x16x32_bf16 v[160:163], a[140:143], a[168:171], v[160:163]\n"
    "s_add_u32 s45, s45, s34\n"
    "v_and_b32_e32 v251, 0x7ff, v252\n"
    "v_mfma_f32_16x16x32_bf16 v[164:167], a[144:147], a[168:171], v[164:167]\n"
    "s_mul_i32 s45, s45, 101376\n"
    "v_mfma_f32_16x16x32_bf16 v[168:171], a[148:151], a[168:171], v[168:171]\n"
    "s_add_u32 s45, s45, 16654336\n"
    "v_add_u32_e32 v251, s63, v251\n"
    "v_mfma_f32_16x16x32_bf16 v[156:159], a[152:155], a[172:175], v[156:159]\n"
    "s_lshl_b32 s47, s93, 11\n"
    "ds_read_b128 a[164:167], v241 offset:4096\n"
    "v_mfma_f32_16x16x32_bf16 v[160:163], a[156:159], a[172:175], v[160:163]\n"
    "buffer_load_dwordx4 a[40:43], v250, s[8:11], s88 offen offset:3072 sc1\n"
    "v_mfma_f32_16x16x32_bf16 v[164:167], v[180:183], a[172:175], v[164:167]\n"
    "s_add_u32 s54, s47, s45\n"
    "v_mfma_f32_16x16x32_bf16 v[168:171], v[184:187], a[172:175], v[168:171]\n"
    "s_waitcnt lgkmcnt(8)\n"
    "v_mfma_f32_16x16x32_bf16 v[196:199], a[216:219], a[176:179], 0\n"
    "s_add_u32 s45, s45, 32768\n"
    "ds_read_b128 a[168:171], v240 offset:36864\n"
    "v_mfma_f32_16x16x32_bf16 v[200:203], a[220:223], a[176:179], 0\n"
    "s_cmp_lt_u32 s32, 2\n"
    "s_cselect_b32 s54, s54, 2.0\n"
    "s_cselect_b32 s45, s45, 2.0\n"
    "v_mfma_f32_16x16x32_bf16 v[204:207], a[224:227], a[176:179], 0\n"
    "buffer_load_dwordx4 a[44:47], v250, s[8:11], s89 offen offset:3072 sc1\n"
    "v_mfma_f32_16x16x32_bf16 v[208:211], a[228:231], a[176:179], 0\n"
    "s_add_u32 s80, s35, s46\n"
    "v_mul_f32_e32 v156, v156, v228\n"
    "v_mfma_f32_16x16x32_bf16 v[196:199], a[232:235], a[180:183], v[196:199]\n"
    "s_add_u32 s82, s36, s46\n"
    "ds_read_b128 a[172:175], v241 offset:36864\n"
    "v_mfma_f32_16x16x32_bf16 v[200:203], v[192:195], a[180:183], v[200:203]\n"
    "s_add_u32 s28, s37, s46\n"
    "v_xor_b32_e32 v249, 64, v251\n"
    "v_mfma_f32_16x16x32_bf16 v[204:207], a[64:67], a[180:183], v[204:207]\n"
    "v_add_u32_e32 v244, s55, v250\n"
    "v_mfma_f32_16x16x32_bf16 v[208:211], a[68:71], a[180:183], v[208:211]\n"
    "s_waitcnt lgkmcnt(8)\n"
    "v_mfma_f32_16x16x32_bf16 v[196:199], a[72:75], a[184:187], v[196:199]\n"
    "ds_read_b128 a[176:179], v242 offset:4096\n"
    "v_mul_f32_e32 v157, v157, v228\n"
    "v_mfma_f32_16x16x32_bf16 v[200:203], a[76:79], a[184:187], v[200:203]\n"
    "v_lshrrev_b32_e32 v247, 2, v250\n"
    "v_add_u32_e32 v247, s31, v247\n"
    "v_mfma_f32_16x16x32_bf16 v[204:207], a[80:83], a[184:187], v[204:207]\n"
    "s_waitcnt vmcnt(6)\n"
    "buffer_store_dword v254, off, s[8:11], s90 offset:0 sc1\n"
    "v_mfma_f32_16x16x32_bf16 v[208:211], a[84:87], a[184:187], v[208:211]\n"
    "v_add_u32_e32 v246, s47, v250\n"
    "v_mfma_f32_16x16x32_bf16 v[196:199], a[88:91], a[188:191], v[196:199]\n"
    "s_add_u32 s62, s58, 16384\n"
    "ds_read_b128 a[180:183], v243 offset:4096\n"
    "v_mul_f32_e32 v158, v158, v228\n"
    "v_mfma_f32_16x16x32_bf16 v[200:203], a[92:95], a[188:191], v[200:203]\n"
    "v_and_b32_e32 v245, 0x603f, v247\n"
    "v_mfma_f32_16x16x32_bf16 v[204:207], a[96:99], a[188:191], v[204:207]\n"
    "buffer_load_dwordx4 v[76:79], v250, s[8:11], s62 offen offset:0 sc1\n"
    "v_mfma_f32_16x16x32_bf16 v[208:211], a[100:103], a[188:191], v[208:211]\n"
    "s_waitcnt lgkmcnt(8)\n"
    "v_bfe_u32 v226, v250, 2, 4\n"
    "v_mfma_f32_16x16x32_bf16 v[196:199], a[104:107], a[48:51], v[196:199]\n"
    "ds_read_b128 a[184:187], v242 offset:36864\n"
    "v_mfma_f32_16x16x32_bf16 v[200:203], a[108:111], a[48:51], v[200:203]\n"
    "v_mul_f32_e32 v159, v159, v228\n"
    "v_mul_f32_e32 v160, v160, v228\n"
    "v_mfma_f32_16x16x32_bf16 v[204:207], a[112:115], a[48:51], v[204:207]\n"
    "s_waitcnt vmcnt(15)\n"
    "v_bfe_u32 v225, v250, 6, 2\n"
    "v_mfma_f32_16x16x32_bf16 v[208:211], a[116:119], a[48:51], v[208:211]\n"
    "v_lshl_or_b32 v226, v225, 7, v226\n"
    "v_and_b32_e32 v225, 0x60ff, v247\n"
    "v_mfma_f32_16x16x32_bf16 v[196:199], a[120:123], a[52:55], v[196:199]\n"
    "ds_read_b128 a[188:191], v243 offset:36864\n"
    "v_mfma_f32_16x16x32_bf16 v[200:203], a[124:127], a[52:55], v[200:203]\n"
    "buffer_load_dwordx4 v[96:99], v250, s[8:11], s62 offen offset:1024 sc1\n"
    "v_mfma_f32_16x16x32_bf16 v[204:207], a[128:131], a[52:55], v[204:207]\n"
    "v_mul_f32_e32 v161, v161, v228\n"
    "v_mfma_f32_16x16x32_bf16 v[208:211], a[132:135], a[52:55], v[208:211]\n"
    "s_waitcnt lgkmcnt(8)\n"
    "v_mul_f32_e32 v162, v162, v228\n"
    "v_mfma_f32_16x16x32_bf16 v[196:199], a[136:139], a[56:59], v[196:199]\n"
    "v_mfma_f32_16x16x32_bf16 v[200:203], a[140:143], a[56:59], v[200:203]\n"
    "buffer_load_dwordx4 a[192:195], v250, s[8:11], s62 offen offset:2048 sc1\n"
    "v_mfma_f32_16x16x32_bf16 v[204:207], a[144:147], a[56:59], v[204:207]\n"
    "v_mul_f32_e32 v163, v163, v228\n"
    "v_mfma_f32_16x16x32_bf16 v[208:211], a[148:151], a[56:59], v[208:211]\n"
    "v_mfma_f32_16x16x32_bf16 v[196:199], a[152:155], a[60:63], v[196:199]\n"
    "ds_read_b128 a[48:51], v251 offset:6144\n"
    "v_mfma_f32_16x16x32_bf16 v[200:203], a[156:159], a[60:63], v[200:203]\n"
    "buffer_load_dwordx4 a[196:199], v250, s[8:11], s62 offen offset:3072 sc1\n"
    "v_mfma_f32_16x16x32_bf16 v[204:207], v[180:183], a[60:63], v[204:207]\n"
    "s_add_u32 s62, s58, 20480\n"
    "v_mul_f32_e32 v164, v164, v228\n"
    "v_mfma_f32_16x16x32_bf16 v[208:211], v[184:187], a[60:63], v[208:211]\n"
    "s_waitcnt lgkmcnt(7)\n"
    "ds_read_b128 a[52:55], v249 offset:6144\n"
    "v_mul_f32_e32 v165, v165, v228\n"
    "v_mfma_f32_16x16x32_bf16 v[172:175], a[216:219], a[160:163], 0\n"
    "v_mfma_f32_16x16x32_bf16 v[176:179], a[220:223], a[160:163], 0\n"
    "buffer_load_dwordx4 a[200:203], v250, s[8:11], s62 offen offset:0 sc1\n"
    "v_mfma_f32_16x16x32_bf16 v[212:215], a[224:227], a[160:163], 0\n"
    "v_mfma_f32_16x16x32_bf16 v[216:219], a[228:231], a[160:163], 0\n"
    "v_mfma_f32_16x16x32_bf16 v[172:175], a[232:235], a[164:167], v[172:175]\n"
    "v_mul_f32_e32 v166, v166, v228\n"
    "v_mfma_f32_16x16x32_bf16 v[176:179], v[192:195], a[164:167], v[176:179]\n"
    "buffer_load_dwordx4 a[204:207], v250, s[8:11], s62 offen offset:1024 sc1\n"
    "v_mfma_f32_16x16x32_bf16 v[212:215], a[64:67], a[164:167], v[212:215]\n"
    "v_mfma_f32_16x16x32_bf16 v[216:219], a[68:71], a[164:167], v[216:219]\n"
    "s_waitcnt lgkmcnt(6)\n"
    "v_mfma_f32_16x16x32_bf16 v[172:175], a[72:75], a[168:171], v[172:175]\n"
    "v_mfma_f32_16x16x32_bf16 v[176:179], a[76:79], a[168:171], v[176:179]\n"
    "buffer_load_dwordx4 a[208:211], v250, s[8:11], s62 offen offset:2048 sc1\n"
    "v_mfma_f32_16x16x32_bf16 v[212:215], a[80:83], a[168:171], v[212:215]\n"
    "v_mfma_f32_16x16x32_bf16 v[216:219], a[84:87], a[168:171], v[216:219]\n"
    "v_mfma_f32_16x16x32_bf16 v[172:175], a[88:91], a[172:175], v[172:175]\n"
    "v_mul_f32_e32 v167, v167, v228\n"
    "v_mul_f32_e32 v168, v168, v228\n"
    "v_mfma_f32_16x16x32_bf16 v[176:179], a[92:95], a[172:175], v[176:179]\n"
    "buffer_load_dwordx4 a[212:215], v250, s[8:11], s62 offen offset:3072 sc1\n"
    "v_mfma_f32_16x16x32_bf16 v[212:215], a[96:99], a[172:175], v[212:215]\n"
    "s_add_u32 s62, s58, 24576\n"
    "v_mul_f32_e32 v169, v169, v228\n"
    "v_mfma_f32_16x16x32_bf16 v[216:219], a[100:103], a[172:175], v[216:219]\n"
    "s_waitcnt vmcnt(25)\n"
    "v_mul_f32_e32 v170, v170, v228\n"
    "v_mul_f32_e32 v171, v171, v228\n"
    "v_mfma_f32_16x16x32_bf16 a[236:239], v[234:237], a[0:3], a[236:239]\n"
    "v_mfma_f32_16x16x32_bf16 v[0:3], v[232:235], a[0:3], v[0:3]\n"
    "buffer_load_dwordx4 v[108:111], v250, s[8:11], s62 offen offset:0 sc1\n"
    "v_mfma_f32_16x16x32_bf16 a[236:239], v[234:237], a[4:7], a[236:239]\n"
    "v_cmp_le_u32_e32 vcc, 0x300, v250\n"
    "v_mfma_f32_16x16x32_bf16 v[0:3], v[232:235], a[4:7], v[0:3]\n"
    "v_mfma_f32_16x16x32_bf16 a[236:239], v[234:237], a[8:11], a[236:239]\n"
    "v_cndmask_b32_e32 v227, v220, v227, vcc\n"
    "v_mfma_f32_16x16x32_bf16 v[0:3], v[232:235], a[8:11], v[0:3]\n"
    "s_waitcnt lgkmcnt(0)\n"
    "s_waitcnt vmcnt(21)\n"
    "v_mfma_f32_16x16x32_bf16 v[20:23], v[234:237], a[12:15], v[20:23]\n"
    "s_barrier\n"
    "v_mfma_f32_16x16x32_bf16 v[40:43], v[232:235], a[12:15], v[40:43]\n"
    "buffer_load_dwordx4 v[112:115], v250, s[8:11], s62 offen offset:1024 sc1\n"
    "v_mfma_f32_16x16x32_bf16 v[20:23], v[234:237], a[16:19], v[20:23]\n"
    "v_accvgpr_read_b32 v64, a236\n"
    "v_accvgpr_read_b32 v65, a237\n"
    "v_mfma_f32_16x16x32_bf16 v[40:43], v[232:235], a[16:19], v[40:43]\n"
    "v_accvgpr_read_b32 v66, a238\n"
    "v_accvgpr_read_b32 v67, a239\n"
    "v_mfma_f32_16x16x32_bf16 v[20:23], v[234:237], a[20:23], v[20:23]\n"
    "ds_write_b32 v247, v227 offset:0\n"
    "v_mfma_f32_16x16x32_bf16 v[40:43], v[232:235], a[20:23], v[40:43]\n"
    "s_waitcnt vmcnt(14)\n"
    "buffer_load_dwordx4 v[132:135], v250, s[8:11], s62 offen offset:2048 sc1\n"
    "v_cvt_pk_bf16_f32 v64, v64, v65\n"
    "v_mfma_f32_16x16x32_bf16 v[60:63], v[234:237], a[24:27], v[60:63]\n"
    "v_cvt_pk_bf16_f32 v65, v66, v67\n"
    "v_cvt_pk_bf16_f32 v66, v0, v1\n"
    "v_cvt_pk_bf16_f32 v67, v2, v3\n"
    "v_mfma_f32_16x16x32_bf16 v[80:83], v[232:235], a[24:27], v[80:83]\n"
    "ds_write_b128 v244, v[64:67] offset:0\n"
    "v_mfma_f32_16x16x32_bf16 v[60:63], v[234:237], a[28:31], v[60:63]\n"
    "buffer_load_dword v253, v226, s[8:11], s61 offen offset:0 sc1\n"
    "v_mfma_f32_16x16x32_bf16 v[80:83], v[232:235], a[28:31], v[80:83]\n"
    "v_cvt_pk_bf16_f32 v20, v20, v21\n"
    "v_cvt_pk_bf16_f32 v21, v22, v23\n"
    "v_mfma_f32_16x16x32_bf16 v[60:63], v[234:237], a[32:35], v[60:63]\n"
    "buffer_load_dwordx4 v[116:119], v250, s[8:11], s62 offen offset:3072 sc1\n"
    "v_mfma_f32_16x16x32_bf16 v[80:83], v[232:235], a[32:35], v[80:83]\n"
    "s_add_u32 s62, s58, 28672\n"
    "s_waitcnt vmcnt(14)\n"
    "v_cvt_pk_bf16_f32 v22, v40, v41\n"
    "v_mfma_f32_16x16x32_bf16 v[100:103], v[234:237], a[36:39], v[100:103]\n"
    "v_cvt_pk_bf16_f32 v23, v42, v43\n"
    "v_mfma_f32_16x16x32_bf16 v[120:123], v[232:235], a[36:39], v[120:123]\n"
    "ds_write_b128 v244, v[20:23] offset:1024\n"
    "v_mfma_f32_16x16x32_bf16 v[100:103], v[234:237], a[40:43], v[100:103]\n"
    "buffer_load_dwordx4 v[136:139], v250, s[8:11], s62 offen offset:0 sc1\n"
    "v_mfma_f32_16x16x32_bf16 v[120:123], v[232:235], a[40:43], v[120:123]\n"
    "v_cvt_pk_bf16_f32 v60, v60, v61\n"
    "v_cvt_pk_bf16_f32 v61, v62, v63\n"
    "v_mfma_f32_16x16x32_bf16 v[100:103], v[234:237], a[44:47], v[100:103]\n"
    "v_cvt_pk_bf16_f32 v62, v80, v81\n"
    "v_cvt_pk_bf16_f32 v63, v82, v83\n"
    "v_mfma_f32_16x16x32_bf16 v[120:123], v[232:235], a[44:47], v[120:123]\n"
    "ds_write_b128 v244, v[60:63] offset:2048\n"
    "v_mfma_f32_16x16x32_bf16 v[172:175], a[104:107], a[176:179], v[172:175]\n"
    "buffer_load_dwordx4 a[240:243], v250, s[8:11], s62 offen offset:1024 sc1\n"
    "v_cvt_pk_bf16_f32 v100, v100, v101\n"
    "v_mfma_f32_16x16x32_bf16 v[176:179], a[108:111], a[176:179], v[176:179]\n"
    "v_cvt_pk_bf16_f32 v101, v102, v103\n"
    "v_cvt_pk_bf16_f32 v102, v120, v121\n"
    "v_cvt_pk_bf16_f32 v103, v122, v123\n"
    "v_mfma_f32_16x16x32_bf16 v[212:215], a[112:115], a[176:179], v[212:215]\n"
    "ds_write_b128 v244, v[100:103] offset:3072\n"
    "v_mfma_f32_16x16x32_bf16 v[216:219], a[116:119], a[176:179], v[216:219]\n"
    "v_mfma_f32_16x16x32_bf16 v[172:175], a[120:123], a[180:183], v[172:175]\n"
    "buffer_load_dwordx4 v[4:7], v250, s[8:11], s62 offen offset:2048 sc1\n"
    "v_mfma_f32_16x16x32_bf16 v[176:179], a[124:127], a[180:183], v[176:179]\n"
    "s_waitcnt lgkmcnt(0)\n"
    "v_mfma_f32_16x16x32_bf16 v[212:215], a[128:131], a[180:183], v[212:215]\n"
    "s_barrier\n"
    "v_mfma_f32_16x16x32_bf16 v[216:219], a[132:135], a[180:183], v[216:219]\n"
    "ds_read_b128 a[0:3], v250 offset:0\n"
    "ds_read_b128 a[4:7], v250 offset:1024\n"
    "ds_read_b128 a[8:11], v250 offset:2048\n"
    "v_mfma_f32_16x16x32_bf16 v[172:175], a[136:139], a[184:187], v[172:175]\n"
    "buffer_load_dwordx4 v[24:27], v250, s[8:11], s62 offen offset:3072 sc1\n"
    "ds_read_b128 a[12:15], v250 offset:3072\n"
    "v_mfma_f32_16x16x32_bf16 v[176:179], a[140:143], a[184:187], v[176:179]\n"
    "ds_read_b128 a[16:19], v250 offset:4096\n"
    "ds_read_b128 a[20:23], v250 offset:5120\n"
    "v_mfma_f32_16x16x32_bf16 v[212:215], a[144:147], a[184:187], v[212:215]\n"
    "ds_read_b128 a[24:27], v250 offset:6144\n"
    "ds_read_b128 a[28:31], v250 offset:7168\n"
    "ds_read2_b32 v[220:221], v245 offset0:0 offset1:16\n"
    "v_mfma_f32_16x16x32_bf16 v[216:219], a[148:151], a[184:187], v[216:219]\n"
    "ds_read2_b32 v[222:223], v245 offset0:32 offset1:48\n"
    "ds_read2_b32 v[80:81], v245 offset0:64 offset1:80\n"
    "ds_read2_b32 v[82:83], v245 offset0:96 offset1:112\n"
    "v_mfma_f32_16x16x32_bf16 v[172:175], a[152:155], a[188:191], v[172:175]\n"
    "v_mfma_f32_16x16x32_bf16 v[176:179], a[156:159], a[188:191], v[176:179]\n"
    "v_mfma_f32_16x16x32_bf16 v[212:215], v[180:183], a[188:191], v[212:215]\n"
    "v_mul_f32_e32 v196, v196, v229\n"
    "v_mul_f32_e32 v197, v197, v229\n"
    "v_mfma_f32_16x16x32_bf16 v[216:219], v[184:187], a[188:191], v[216:219]\n"
    "v_mul_f32_e32 v198, v198, v229\n"
    "v_mfma_f32_16x16x32_bf16 v[156:159], v[234:237], a[48:51], v[156:159]\n"
    "v_mul_f32_e32 v199, v199, v229\n"
    "v_mfma_f32_16x16x32_bf16 v[160:163], v[232:235], a[48:51], v[160:163]\n"
    "v_mul_f32_e32 v200, v200, v229\n"
    "v_mfma_f32_16x16x32_bf16 v[164:167], v[234:237], a[52:55], v[164:167]\n"
    "v_mul_f32_e32 v201, v201, v229\n"
    "v_mul_f32_e32 v202, v202, v229\n"
    "v_mfma_f32_16x16x32_bf16 v[168:171], v[232:235], a[52:55], v[168:171]\n"
    "s_waitcnt lgkmcnt(2)\n"
    "ds_read_b128 a[64:67], v250 offset:8192\n"
    "v_mfma_f32_16x16x32_bf16 v[140:143], a[244:247], a[0:3], 0\n"
    "ds_read_b128 a[68:71], v250 offset:9216\n"
    "v_mul_f32_e32 v203, v203, v229\n"
    "v_mfma_f32_16x16x32_bf16 v[144:147], v[8:11], a[0:3], 0\n"
    "ds_read_b128 a[72:75], v250 offset:10240\n"
    "v_add_f32_e32 v220, v220, v221\n"
    "v_mfma_f32_16x16x32_bf16 v[148:151], a[248:251], a[0:3], 0\n"
    "ds_read_b128 a[76:79], v250 offset:11264\n"
    "v_add_f32_e32 v222, v222, v223\n"
    "v_mfma_f32_16x16x32_bf16 v[152:155], v[12:15], a[0:3], 0\n"
    "s_waitcnt vmcnt(5)\n"
    "v_subrev_u32_e32 v253, s39, v253\n"
    "v_cmp_lt_u32_e32 vcc, 1, v253\n"
    "v_mfma_f32_16x16x32_bf16 v[140:143], a[252:255], a[4:7], v[140:143]\n"
    "ds_read_b128 a[80:83], v250 offset:12288\n"
    "v_mul_f32_e32 v204, v204, v229\n"
    "v_mfma_f32_16x16x32_bf16 v[144:147], v[16:19], a[4:7], v[144:147]\n"
    "ds_read_b128 a[84:87], v250 offset:13312\n"
    "v_mul_f32_e32 v205, v205, v229\n"
    "v_mfma_f32_16x16x32_bf16 v[148:151], v[28:31], a[4:7], v[148:151]\n"
    "s_cbranch_vccnz .Lat_ppoll\n"
    ".Lat_pok:\n"
    "v_cmp_eq_u32_e32 vcc, 1, v253\n"
    "v_mfma_f32_16x16x32_bf16 v[152:155], v[48:51], a[4:7], v[152:155]\n"
    "buffer_load_dwordx4 a[128:131], v250, s[8:11], s54 offen offset:0 sc1\n"
    "v_mfma_f32_16x16x32_bf16 v[140:143], v[32:35], a[8:11], v[140:143]\n"
    "v_mul_f32_e32 v206, v206, v229\n"
    "v_mul_f32_e32 v207, v207, v229\n"
    "v_mfma_f32_16x16x32_bf16 v[144:147], v[52:55], a[8:11], v[144:147]\n"
    "s_cbranch_vccnz .Lat_redo\n"
    "v_mfma_f32_16x16x32_bf16 v[148:151], v[36:39], a[8:11], v[148:151]\n"
    "buffer_load_dwordx4 a[132:135], v250, s[8:11], s54 offen offset:1024 sc1\n"
    "v_mfma_f32_16x16x32_bf16 v[152:155], v[56:59], a[8:11], v[152:155]\n"
    "s_addk_i32 s54, 8192\n"
    "ds_read_b128 a[88:91], v250 offset:14336\n"
    "v_mfma_f32_16x16x32_bf16 v[140:143], v[68:71], a[12:15], v[140:143]\n"
    "buffer_load_dwordx4 a[136:139], v250, s[8:11], s54 offen offset:0 sc1\n"
    "v_mfma_f32_16x16x32_bf16 v[144:147], v[88:91], a[12:15], v[144:147]\n"
    "ds_read_b128 a[92:95], v250 offset:15360\n"
    "v_mul_f32_e32 v208, v208, v229\n"
    "v_mfma_f32_16x16x32_bf16 v[148:151], v[72:75], a[12:15], v[148:151]\n"
    "s_waitcnt vmcnt(3)\n"
    "buffer_load_dwordx4 a[140:143], v250, s[8:11], s54 offen offset:1024 sc1\n"
    "v_mfma_f32_16x16x32_bf16 v[152:155], v[92:95], a[12:15], v[152:155]\n"
    "s_addk_i32 s54, 8192\n"
    "v_add_f32_e32 v220, v220, v222\n"
    "v_mfma_f32_16x16x32_bf16 v[140:143], v[76:79], a[16:19], v[140:143]\n"
    "buffer_load_dwordx4 a[144:147], v250, s[8:11], s54 offen offset:0 sc1\n"
    "v_mfma_f32_16x16x32_bf16 v[144:147], v[96:99], a[16:19], v[144:147]\n"
    "ds_read_b128 a[56:59], v252 offset:30720\n"
    "v_mfma_f32_16x16x32_bf16 v[148:151], a[192:195], a[16:19], v[148:151]\n"
    "buffer_load_dwordx4 a[148:151], v250, s[8:11], s54 offen offset:1024 sc1\n"
    "v_mfma_f32_16x16x32_bf16 v[152:155], a[196:199], a[16:19], v[152:155]\n"
    "s_addk_i32 s54, 8192\n"
    "ds_read_b128 a[60:63], v255 offset:30720\n"
    "v_mul_f32_e32 v209, v209, v229\n"
    "v_mfma_f32_16x16x32_bf16 v[140:143], a[200:203], a[20:23], v[140:143]\n"
    "buffer_load_dwordx4 a[152:155], v250, s[8:11], s54 offen offset:0 sc1\n"
    "v_mfma_f32_16x16x32_bf16 v[144:147], a[204:207], a[20:23], v[144:147]\n"
    "v_mul_f32_e32 v172, v172, v230\n"
    "v_mul_f32_e32 v173, v173, v230\n"
    "v_mfma_f32_16x16x32_bf16 v[148:151], a[208:211], a[20:23], v[148:151]\n"
    "buffer_load_dwordx4 a[156:159], v250, s[8:11], s54 offen offset:1024 sc1\n"
    "v_mfma_f32_16x16x32_bf16 v[152:155], a[212:215], a[20:23], v[152:155]\n"
    "s_waitcnt lgkmcnt(10)\n"
    "v_mul_f32_e32 v210, v210, v229\n"
    "v_mul_f32_e32 v211, v211, v229\n"
    "v_mfma_f32_16x16x32_bf16 v[140:143], v[108:111], a[24:27], v[140:143]\n"
    "buffer_load_dword v227, v248, s[8:11], s45 offen offset:1088 sc1\n"
    "v_mfma_f32_16x16x32_bf16 v[144:147], v[112:115], a[24:27], v[144:147]\n"
    "v_mfma_f32_16x16x32_bf16 v[148:151], v[132:135], a[24:27], v[148:151]\n"
    "v_mfma_f32_16x16x32_bf16 v[152:155], v[116:119], a[24:27], v[152:155]\n"
    "v_rcp_f32_e32 v220, v220\n"
    "v_mfma_f32_16x16x32_bf16 v[140:143], v[136:139], a[28:31], v[140:143]\n"
    "v_add_f32_e32 v80, v80, v81\n"
    "v_add_f32_e32 v82, v82, v83\n"
    "v_mfma_f32_16x16x32_bf16 v[144:147], a[240:243], a[28:31], v[144:147]\n"
    "ds_read_b128 a[160:163], v252 offset:4096\n"
    "v_add_f32_e32 v80, v80, v82\n"
    "v_mfma_f32_16x16x32_bf16 v[148:151], v[4:7], a[28:31], v[148:151]\n"
    "v_rcp_f32_e32 v80, v80\n"
    "v_mfma_f32_16x16x32_bf16 v[152:155], v[24:27], a[28:31], v[152:155]\n"
    "s_waitcnt lgkmcnt(9)\n"
    "ds_read_b128 a[164:167], v255 offset:4096\n"
    "v_mfma_f32_16x16x32_bf16 v[180:183], a[244:247], a[64:67], 0\n"
    "v_mul_f32_e32 v174, v174, v230\n"
    "v_mfma_f32_16x16x32_bf16 v[184:187], v[8:11], a[64:67], 0\n"
    "v_fma_f32 v140, v140, v220, v156\n"
    "v_mfma_f32_16x16x32_bf16 v[188:191], a[248:251], a[64:67], 0\n"
    "v_fma_f32 v141, v141, v220, v157\n"
    "v_fma_f32 v142, v142, v220, v158\n"
    "v_mfma_f32_16x16x32_bf16 v[192:195], v[12:15], a[64:67], 0\n"
    "v_mul_f32_e32 v175, v175, v230\n"
    "v_mfma_f32_16x16x32_bf16 v[180:183], a[252:255], a[68:71], v[180:183]\n"
    "v_fma_f32 v143, v143, v220, v159\n"
    "v_mfma_f32_16x16x32_bf16 v[184:187], v[16:19], a[68:71], v[184:187]\n"
    "v_fma_f32 v144, v144, v220, v160\n"
    "v_mfma_f32_16x16x32_bf16 v[188:191], v[28:31], a[68:71], v[188:191]\n"
    "v_fma_f32 v145, v145, v220, v161\n"
    "v_mfma_f32_16x16x32_bf16 v[192:195], v[48:51], a[68:71], v[192:195]\n"
    "s_waitcnt lgkmcnt(8)\n"
    "v_fma_f32 v146, v146, v220, v162\n"
    "v_mfma_f32_16x16x32_bf16 v[180:183], v[32:35], a[72:75], v[180:183]\n"
    "v_mul_f32_e32 v176, v176, v230\n"
    "v_fma_f32 v147, v147, v220, v163\n"
    "v_mfma_f32_16x16x32_bf16 v[184:187], v[52:55], a[72:75], v[184:187]\n"
    "v_cvt_pk_bf16_f32 v140, v140, v141\n"
    "v_mfma_f32_16x16x32_bf16 v[188:191], v[36:39], a[72:75], v[188:191]\n"
    "v_cvt_pk_bf16_f32 v141, v142, v143\n"
    "v_cvt_pk_bf16_f32 v142, v144, v145\n"
    "v_mfma_f32_16x16x32_bf16 v[192:195], v[56:59], a[72:75], v[192:195]\n"
    "v_cvt_pk_bf16_f32 v143, v146, v147\n"
    "v_mfma_f32_16x16x32_bf16 v[180:183], v[68:71], a[76:79], v[180:183]\n"
    "buffer_store_dwordx4 v[140:143], v239, s[16:19], s80 offen offset:0 sc0 sc1\n"
    "v_mfma_f32_16x16x32_bf16 v[184:187], v[88:91], a[76:79], v[184:187]\n"
    "v_fma_f32 v148, v148, v220, v164\n"
    "v_mfma_f32_16x16x32_bf16 v[188:191], v[72:75], a[76:79], v[188:191]\n"
    "v_fma_f32 v149, v149, v220, v165\n"
    "v_mfma_f32_16x16x32_bf16 v[192:195], v[92:95], a[76:79], v[192:195]\n"
    "s_waitcnt lgkmcnt(6)\n"
    "v_fma_f32 v150, v150, v220, v166\n"
    "v_fma_f32 v151, v151, v220, v167\n"
    "v_mfma_f32_16x16x32_bf16 v[180:183], v[76:79], a[80:83], v[180:183]\n"
    "v_fma_f32 v152, v152, v220, v168\n"
    "v_fma_f32 v153, v153, v220, v169\n"
    "v_mfma_f32_16x16x32_bf16 v[184:187], v[96:99], a[80:83], v[184:187]\n"
    "v_fma_f32 v154, v154, v220, v170\n"
    "v_fma_f32 v155, v155, v220, v171\n"
    "v_mfma_f32_16x16x32_bf16 v[188:191], a[192:195], a[80:83], v[188:191]\n"
    "v_cvt_pk_bf16_f32 v148, v148, v149\n"
    "v_cvt_pk_bf16_f32 v149, v150, v151\n"
    "v_mfma_f32_16x16x32_bf16 v[192:195], a[196:199], a[80:83], v[192:195]\n"
    "v_cvt_pk_bf16_f32 v150, v152, v153\n"
    "v_mfma_f32_16x16x32_bf16 v[180:183], a[200:203], a[84:87], v[180:183]\n"
    "v_cvt_pk_bf16_f32 v151, v154, v155\n"
    "v_mfma_f32_16x16x32_bf16 v[184:187], a[204:207], a[84:87], v[184:187]\n"
    "buffer_store_dwordx4 v[148:151], v239, s[16:19], s80 offen offset:64 sc0 sc1\n"
    "v_mfma_f32_16x16x32_bf16 v[188:191], a[208:211], a[84:87], v[188:191]\n"
    "v_mfma_f32_16x16x32_bf16 v[192:195], a[212:215], a[84:87], v[192:195]\n"
    "s_waitcnt lgkmcnt(4)\n"
    "v_mul_f32_e32 v177, v177, v230\n"
    "v_mfma_f32_16x16x32_bf16 v[180:183], v[108:111], a[88:91], v[180:183]\n"
    "v_mul_f32_e32 v178, v178, v230\n"
    "v_mfma_f32_16x16x32_bf16 v[184:187], v[112:115], a[88:91], v[184:187]\n"
    "v_mul_f32_e32 v179, v179, v230\n"
    "v_mfma_f32_16x16x32_bf16 v[188:191], v[132:135], a[88:91], v[188:191]\n"
    "v_mul_f32_e32 v212, v212, v230\n"
    "v_mfma_f32_16x16x32_bf16 v[192:195], v[116:119], a[88:91], v[192:195]\n"
    "s_waitcnt vmcnt(9)\n"
    "v_mfma_f32_16x16x32_bf16 v[64:67], v[234:237], a[128:131], 0\n"
    "v_mfma_f32_16x16x32_bf16 v[84:87], v[232:235], a[128:131], 0\n"
    "v_mfma_f32_16x16x32_bf16 v[104:107], v[234:237], a[132:135], 0\n"
    "v_mul_f32_e32 v213, v213, v230\n"
    "v_mfma_f32_16x16x32_bf16 v[124:127], v[232:235], a[132:135], 0\n"
    "s_waitcnt vmcnt(7)\n"
    "v_mfma_f32_16x16x32_bf16 v[64:67], v[234:237], a[136:139], v[64:67]\n"
    "v_mfma_f32_16x16x32_bf16 v[84:87], v[232:235], a[136:139], v[84:87]\n"
    "v_mul_f32_e32 v214, v214, v230\n"
    "v_mfma_f32_16x16x32_bf16 v[104:107], v[234:237], a[140:143], v[104:107]\n"
    "v_mfma_f32_16x16x32_bf16 v[124:127], v[232:235], a[140:143], v[124:127]\n"
    "s_waitcnt vmcnt(5)\n"
    "v_mfma_f32_16x16x32_bf16 v[64:67], v[234:237], a[144:147], v[64:67]\n"
    "v_mfma_f32_16x16x32_bf16 v[84:87], v[232:235], a[144:147], v[84:87]\n"
    "v_mul_f32_e32 v215, v215, v230\n"
    "v_mfma_f32_16x16x32_bf16 v[104:107], v[234:237], a[148:151], v[104:107]\n"
    "v_mul_f32_e32 v216, v216, v230\n"
    "v_mul_f32_e32 v217, v217, v230\n"
    "v_mfma_f32_16x16x32_bf16 v[124:127], v[232:235], a[148:151], v[124:127]\n"
    "s_waitcnt vmcnt(3)\n"
    "v_mul_f32_e32 v218, v218, v230\n"
    "v_mfma_f32_16x16x32_bf16 v[64:67], v[234:237], a[152:155], v[64:67]\n"
    "s_waitcnt vmcnt(2)\n"
    "ds_write_b32 v225, v227 offset:512\n"
    "v_mfma_f32_16x16x32_bf16 v[84:87], v[232:235], a[152:155], v[84:87]\n"
    "v_mul_f32_e32 v219, v219, v230\n"
    "v_mfma_f32_16x16x32_bf16 v[104:107], v[234:237], a[156:159], v[104:107]\n"
    "v_mfma_f32_16x16x32_bf16 v[124:127], v[232:235], a[156:159], v[124:127]\n"
    "v_cvt_pk_bf16_f32 v64, v64, v65\n"
    "v_cvt_pk_bf16_f32 v65, v66, v67\n"
    "v_mfma_f32_16x16x32_bf16 v[180:183], v[136:139], a[92:95], v[180:183]\n"
    "v_cvt_pk_bf16_f32 v66, v84, v85\n"
    "v_cvt_pk_bf16_f32 v67, v86, v87\n"
    "v_mfma_f32_16x16x32_bf16 v[184:187], a[240:243], a[92:95], v[184:187]\n"
    "ds_write_b128 v246, v[64:67] offset:16384\n"
    "v_mfma_f32_16x16x32_bf16 v[188:191], v[4:7], a[92:95], v[188:191]\n"
    "v_cvt_pk_bf16_f32 v104, v104, v105\n"
    "v_cvt_pk_bf16_f32 v105, v106, v107\n"
    "v_mfma_f32_16x16x32_bf16 v[192:195], v[24:27], a[92:95], v[192:195]\n"
    "s_waitcnt lgkmcnt(4)\n"
    "v_cvt_pk_bf16_f32 v106, v124, v125\n"
    "v_cvt_pk_bf16_f32 v107, v126, v127\n"
    "v_mfma_f32_16x16x32_bf16 v[196:199], v[234:237], a[56:59], v[196:199]\n"
    "ds_write_b128 v246, v[104:107] offset:17408\n"
    "v_mfma_f32_16x16x32_bf16 v[200:203], v[232:235], a[56:59], v[200:203]\n"
    "v_mfma_f32_16x16x32_bf16 v[204:207], v[234:237], a[60:63], v[204:207]\n"
    "s_waitcnt lgkmcnt(0)\n"
    "v_mfma_f32_16x16x32_bf16 v[208:211], v[232:235], a[60:63], v[208:211]\n"
    "s_barrier\n"
    "v_mfma_f32_16x16x32_bf16 v[172:175], v[234:237], a[160:163], v[172:175]\n"
    "ds_read_b128 a[96:99], v250 offset:16384\n"
    "v_mfma_f32_16x16x32_bf16 v[176:179], v[232:235], a[160:163], v[176:179]\n"
    "ds_read_b128 a[100:103], v250 offset:17408\n"
    "v_mfma_f32_16x16x32_bf16 v[212:215], v[234:237], a[164:167], v[212:215]\n"
    "ds_read_b128 a[104:107], v250 offset:18432\n"
    "ds_read_b128 a[108:111], v250 offset:19456\n"
    "v_mfma_f32_16x16x32_bf16 v[216:219], v[232:235], a[164:167], v[216:219]\n"
    "s_waitcnt lgkmcnt(2)\n"
    "ds_read_b128 a[112:115], v250 offset:20480\n"
    "ds_read_b128 a[116:119], v250 offset:21504\n"
    "v_mfma_f32_16x16x32_bf16 v[0:3], a[244:247], a[96:99], 0\n"
    "ds_read_b128 a[120:123], v250 offset:22528\n"
    "ds_read_b128 a[124:127], v250 offset:23552\n"
    "v_mfma_f32_16x16x32_bf16 v[20:23], v[8:11], a[96:99], 0\n"
    "ds_read2_b32 v[220:221], v245 offset0:128 offset1:144\n"
    "ds_read2_b32 v[222:223], v245 offset0:160 offset1:176\n"
    "v_fma_f32 v180, v180, v80, v196\n"
    "v_mfma_f32_16x16x32_bf16 v[0:3], a[252:255], a[100:103], v[0:3]\n"
    "v_fma_f32 v181, v181, v80, v197\n"
    "v_fma_f32 v182, v182, v80, v198\n"
    "v_mfma_f32_16x16x32_bf16 v[20:23], v[16:19], a[100:103], v[20:23]\n"
    "s_waitcnt lgkmcnt(6)\n"
    "v_fma_f32 v183, v183, v80, v199\n"
    "v_fma_f32 v184, v184, v80, v200\n"
    "v_mfma_f32_16x16x32_bf16 v[0:3], v[32:35], a[104:107], v[0:3]\n"
    "s_or_b32 s45, s34, s93\n"
    "s_cselect_b32 s45, 2.0, 6101760\n"
    "v_fma_f32 v185, v185, v80, v201\n"
    "v_mfma_f32_16x16x32_bf16 v[20:23], v[52:55], a[104:107], v[20:23]\n"
    "v_fma_f32 v186, v186, v80, v202\n"
    "v_fma_f32 v187, v187, v80, v203\n"
    "v_mfma_f32_16x16x32_bf16 v[0:3], v[68:71], a[108:111], v[0:3]\n"
    "v_cvt_pk_bf16_f32 v180, v180, v181\n"
    "v_cvt_pk_bf16_f32 v181, v182, v183\n"
    "v_mfma_f32_16x16x32_bf16 v[20:23], v[88:91], a[108:111], v[20:23]\n"
    "s_waitcnt lgkmcnt(4)\n"
    "v_cvt_pk_bf16_f32 v182, v184, v185\n"
    "v_cvt_pk_bf16_f32 v183, v186, v187\n"
    "v_mfma_f32_16x16x32_bf16 v[0:3], v[76:79], a[112:115], v[0:3]\n"
    "buffer_store_dwordx4 v[180:183], v239, s[16:19], s82 offen offset:0 sc0 sc1\n"
    "v_mfma_f32_16x16x32_bf16 v[20:23], v[96:99], a[112:115], v[20:23]\n"
    "v_fma_f32 v188, v188, v80, v204\n"
    "v_fma_f32 v189, v189, v80, v205\n"
    "v_mfma_f32_16x16x32_bf16 v[0:3], a[200:203], a[116:119], v[0:3]\n"
    "v_fma_f32 v190, v190, v80, v206\n"
    "v_fma_f32 v191, v191, v80, v207\n"
    "v_mfma_f32_16x16x32_bf16 v[20:23], a[204:207], a[116:119], v[20:23]\n"
    "s_waitcnt lgkmcnt(0)\n"
    "v_fma_f32 v192, v192, v80, v208\n"
    "v_fma_f32 v193, v193, v80, v209\n"
    "v_mfma_f32_16x16x32_bf16 v[0:3], v[108:111], a[120:123], v[0:3]\n"
    "v_fma_f32 v194, v194, v80, v210\n"
    "v_fma_f32 v195, v195, v80, v211\n"
    "v_mfma_f32_16x16x32_bf16 v[20:23], v[112:115], a[120:123], v[20:23]\n"
    "v_cvt_pk_bf16_f32 v188, v188, v189\n"
    "v_cvt_pk_bf16_f32 v189, v190, v191\n"
    "v_mfma_f32_16x16x32_bf16 v[0:3], v[136:139], a[124:127], v[0:3]\n"
    "v_cvt_pk_bf16_f32 v190, v192, v193\n"
    "v_cvt_pk_bf16_f32 v191, v194, v195\n"
    "v_mfma_f32_16x16x32_bf16 v[20:23], a[240:243], a[124:127], v[20:23]\n"
    "buffer_store_dwordx4 v[188:191], v239, s[16:19], s82 offen offset:64 sc0 sc1\n"
    "v_mfma_f32_16x16x32_bf16 v[40:43], a[248:251], a[96:99], 0\n"
    "v_add_f32_e32 v220, v220, v221\n"
    "v_add_f32_e32 v222, v222, v223\n"
    "v_add_f32_e32 v220, v220, v222\n"
    "v_mfma_f32_16x16x32_bf16 v[60:63], v[12:15], a[96:99], 0\n"
    "v_rcp_f32_e32 v220, v220\n"
    "v_mfma_f32_16x16x32_bf16 v[40:43], v[28:31], a[100:103], v[40:43]\n"
    "v_fma_f32 v0, v0, v220, v172\n"
    "v_fma_f32 v1, v1, v220, v173\n"
    "v_mfma_f32_16x16x32_bf16 v[60:63], v[48:51], a[100:103], v[60:63]\n"
    "v_fma_f32 v2, v2, v220, v174\n"
    "v_mfma_f32_16x16x32_bf16 v[40:43], v[36:39], a[104:107], v[40:43]\n"
    "v_fma_f32 v3, v3, v220, v175\n"
    "v_mfma_f32_16x16x32_bf16 v[60:63], v[56:59], a[104:107], v[60:63]\n"
    "v_fma_f32 v20, v20, v220, v176\n"
    "v_fma_f32 v21, v21, v220, v177\n"
    "v_mfma_f32_16x16x32_bf16 v[40:43], v[72:75], a[108:111], v[40:43]\n"
    "v_fma_f32 v22, v22, v220, v178\n"
    "v_fma_f32 v23, v23, v220, v179\n"
    "v_mfma_f32_16x16x32_bf16 v[60:63], v[92:95], a[108:111], v[60:63]\n"
    "v_cvt_pk_bf16_f32 v0, v0, v1\n"
    "v_cvt_pk_bf16_f32 v1, v2, v3\n"
    "v_mfma_f32_16x16x32_bf16 v[40:43], a[192:195], a[112:115], v[40:43]\n"
    "v_cvt_pk_bf16_f32 v2, v20, v21\n"
    "v_cvt_pk_bf16_f32 v3, v22, v23\n"
    "v_mfma_f32_16x16x32_bf16 v[60:63], a[196:199], a[112:115], v[60:63]\n"
    "v_mfma_f32_16x16x32_bf16 v[40:43], a[208:211], a[116:119], v[40:43]\n"
    "buffer_store_dwordx4 v[0:3], v239, s[16:19], s28 offen offset:0 sc0 sc1\n"
    "v_mfma_f32_16x16x32_bf16 v[60:63], a[212:215], a[116:119], v[60:63]\n"
    "v_mfma_f32_16x16x32_bf16 v[40:43], v[132:135], a[120:123], v[40:43]\n"
    "v_mfma_f32_16x16x32_bf16 v[60:63], v[116:119], a[120:123], v[60:63]\n"
    "v_mfma_f32_16x16x32_bf16 v[40:43], v[4:7], a[124:127], v[40:43]\n"
    "v_mfma_f32_16x16x32_bf16 v[60:63], v[24:27], a[124:127], v[60:63]\n"
    "v_mov_b32_e32 v254, s39\n"
    "s_nop 2\n"
    "v_fma_f32 v40, v40, v220, v212\n"
    "v_fma_f32 v41, v41, v220, v213\n"
    "v_fma_f32 v42, v42, v220, v214\n"
    "v_fma_f32 v43, v43, v220, v215\n"
    "v_fma_f32 v60, v60, v220, v216\n"
    "v_fma_f32 v61, v61, v220, v217\n"
    "v_fma_f32 v62, v62, v220, v218\n"
    "v_fma_f32 v63, v63, v220, v219\n"
    "v_cvt_pk_bf16_f32 v40, v40, v41\n"
    "v_cvt_pk_bf16_f32 v41, v42, v43\n"
    "v_cvt_pk_bf16_f32 v42, v60, v61\n"
    "v_cvt_pk_bf16_f32 v43, v62, v63\n"
    "buffer_store_dwordx4 v[40:43], v239, s[16:19], s28 offen offset:64 sc0 sc1\n"
    "buffer_store_dword v254, off, s[8:11], s45 offset:0 sc1\n"
    "s_endpgm\n"
    ".p2align 6\n"
    ".long 0xbedf00ff\n"
    ".Lat_w14:\n"
    "s_setpc_b64 s[2:3]\n"
    ".Lat_k:\n"
    "s_mov_b32 s53, 0x400000\n"
    ".Lat_kl:\n"
    "buffer_load_dword v253, v238, s[8:11], s48 offen offset:0 sc1\n"
    "s_waitcnt vmcnt(0)\n"
    "v_subrev_u32_e32 v253, s39, v253\n"
    "v_subrev_u32_e32 v253, 0, v253\n"
    "v_cmp_le_u32_e32 vcc, 1, v253\n"
    "s_sub_u32 s53, s53, 1\n"
    "s_cbranch_scc1 .Lat_kr\n"
    "s_nop 2\n"
    "s_cbranch_vccnz .Lat_kl\n"
    "s_branch .Lat_kr\n"
    ".Lat_kx:\n"
    "s_branch .Lat_kxr\n"
    ".Lat_ppoll:\n"
    "s_mov_b32 s53, 0x400000\n"
    ".Lat_ppl:\n"
    "buffer_load_dword v253, v226, s[8:11], s61 offen offset:0 sc1\n"
    "s_waitcnt vmcnt(0)\n"
    "v_subrev_u32_e32 v253, s39, v253\n"
    "v_cmp_lt_u32_e32 vcc, 1, v253\n"
    "s_sub_u32 s53, s53, 1\n"
    "s_cbranch_scc1 .Lat_pok\n"
    "s_nop 2\n"
    "s_cbranch_vccnz .Lat_ppl\n"
    "s_branch .Lat_pok\n"
    ".Lat_wpoll:\n"
    "s_mov_b32 s53, 0x400000\n"
    ".Lat_wpoll_l:\n"
    "s_sleep 1\n"
    "buffer_load_dword v253, v255, s[8:11], s52 offen offset:0 sc1\n"
    "s_waitcnt vmcnt(0)\n"
    "v_cmp_ne_u32_e32 vcc, s39, v253\n"
    "s_sub_u32 s53, s53, 1\n"
    "s_cbranch_scc1 .Lat_wpoll_ret\n"
    "s_nop 2\n"
    "s_cbranch_vccnz .Lat_wpoll_l\n"
    "s_branch .Lat_wpoll_ret\n"
    ".Lat_glate:\n"
    "s_mov_b32 s53, 0x400000\n"
    ".Lat_glate_l:\n"
    "s_sleep 2\n"
    "buffer_load_dword v188, v189, s[8:11], s81 offen offset:0 sc1\n"
    "s_waitcnt vmcnt(0)\n"
    "v_cmp_ne_u32_e32 vcc, s39, v188\n"
    "s_sub_u32 s53, s53, 1\n"
    "s_cbranch_scc1 .Lat_glate_ret\n"
    "s_nop 2\n"
    "s_cbranch_vccnz .Lat_glate_l\n"
    "s_branch .Lat_glate_ret\n"
    ".Lat_lp0:\n"
    "s_mov_b32 s53, 0x400000\n"
    ".Lat_lp:\n"
    "s_sleep 1\n"
    "buffer_load_dword v253, off, s[8:11], s82 sc1\n"
    "s_waitcnt vmcnt(0)\n"
    "v_readfirstlane_b32 s81, v253\n"
    "s_sub_u32 s53, s53, 1\n"
    "s_cbranch_scc1 .Lat_lpx\n"
    "s_nop 3\n"
    "s_cmp_lg_u32 s81, s39\n"
    "s_cbranch_scc1 .Lat_lp\n"
    ".Lat_lpx:\n"
    "s_mov_b32 m0, s30\n"
    "s_nop 0\n"
    "buffer_load_dwordx4 v250, s[8:11], s83 offen offset:0 sc1 lds\n"
    "s_mov_b32 m0, s42\n"
    "s_branch .Lat_g1b\n"
    ".Lat_h1poll:\n"
    "s_mov_b32 s53, 0x400000\n"
    ".Lat_h1poll_l:\n"
    "s_sleep 1\n"
    "buffer_load_dword v80, v83, s[8:11], s59 offen offset:0 sc1\n"
    "s_waitcnt vmcnt(0)\n"
    "v_cmp_ne_u32_e32 vcc, s39, v80\n"
    "s_sub_u32 s53, s53, 1\n"
    "s_cbranch_scc1 .Lat_h1poll_ret\n"
    "s_nop 2\n"
    "s_cbranch_vccnz .Lat_h1poll_l\n"
    "s_branch .Lat_h1poll_ret\n"
    ".Lat_hpoll:\n"
    "s_mov_b32 s53, 0x400000\n"
    ".Lat_hpoll_l:\n"
    "s_sleep 1\n"
    "buffer_load_dword v253, v250, s[8:11], s59 offen offset:0 sc1\n"
    "buffer_load_dword v254, v252, s[8:11], s59 offen offset:0 sc1\n"
    "s_waitcnt vmcnt(0)\n"
    "v_xor_b32_e32 v254, s39, v254\n"
    "v_xor_b32_e32 v253, s39, v253\n"
    "v_or_b32_e32 v253, v253, v254\n"
    "v_cmp_ne_u32_e32 vcc, 0, v253\n"
    "s_sub_u32 s53, s53, 1\n"
    "s_cbranch_scc1 .Lat_hpoll_ret\n"
    "s_nop 2\n"
    "s_cbranch_vccnz .Lat_hpoll_l\n"
    "s_branch .Lat_hpoll_ret\n"
    ".Lat_redo:\n"
    "s_waitcnt vmcnt(0) lgkmcnt(0)\n"
    "s_barrier\n"
    "s_mov_b32 s49, 1\n"
    "s_mov_b32 s4, 0\n"
    "s_mov_b32 s80, 0\n"
    "s_mov_b32 s82, 2.0\n"
    "v_lshrrev_b32_e32 v224, 4, v250\n"
    "s_and_b32 s50, s93, 1\n"
    "s_lshr_b32 s51, s93, 1\n"
    "v_lshrrev_b32_e32 v225, 3, v224\n"
    "v_and_b32_e32 v226, 6, v225\n"
    "v_and_b32_e32 v227, 7, v224\n"
    "v_xor_b32_e32 v226, v226, v227\n"
    "v_lshlrev_b32_e32 v226, 4, v226\n"
    "v_and_b32_e32 v227, 4, v225\n"
    "v_add_u32_e32 v227, v227, v225\n"
    "v_lshl_add_u32 v251, v227, 9, v226\n"
    "v_add_u32_e32 v252, 0x1c00, v251\n"
    "v_and_b32_e32 v225, 7, v224\n"
    "v_lshlrev_b32_e32 v227, 7, v225\n"
    "v_bfe_u32 v226, v224, 3, 1\n"
    "v_lshl_add_u32 v227, v226, 10, v227\n"
    "v_and_b32_e32 v225, 6, v224\n"
    "v_lshrrev_b32_e32 v226, 4, v224\n"
    "v_xor_b32_e32 v225, v225, v226\n"
    "v_lshl_add_u32 v240, v225, 4, v227\n"
    "v_lshrrev_b32_e32 v8, 4, v250\n"
    "v_and_b32_e32 v10, 0x70, v251\n"
    "v_bfe_u32 v9, v8, 3, 1\n"
    "v_lshl_add_u32 v248, v9, 9, v10\n"
    "v_lshrrev_b32_e32 v9, 4, v8\n"
    "v_lshl_add_u32 v248, v9, 12, v248\n"
    "s_mul_i32 s43, s50, 3\n"
    "s_xor_b32 s43, s43, s51\n"
    "s_lshl_b32 s43, s43, 15\n"
    "s_add_u32 s43, s43, 32768\n"
    "s_mul_i32 s42, s93, 5120\n"
    "s_lshl_b32 s45, s50, 8\n"
    "s_mul_i32 s40, s32, 1277952\n"
    "s_lshl_b32 s41, s51, 14\n"
    "s_add_u32 s40, s40, s41\n"
    "s_add_u32 s40, s40, s45\n"
    "s_add_u32 s41, s40, 128\n"
    "v_bfe_u32 v9, v8, 2, 3\n"
    "v_lshlrev_b32_e32 v244, 7, v9\n"
    "v_lshrrev_b32_e32 v10, 5, v8\n"
    "v_lshl_add_u32 v244, v10, 10, v244\n"
    "v_and_b32_e32 v9, 6, v9\n"
    "v_bfe_u32 v10, v8, 1, 1\n"
    "v_xor_b32_e32 v9, v9, v10\n"
    "v_lshl_add_u32 v244, v9, 4, v244\n"
    "v_and_b32_e32 v10, 1, v8\n"
    "v_lshl_add_u32 v244, v10, 3, v244\n"
    "s_lshl_b32 s45, s51, 16\n"
    "s_add_u32 s45, s45, 32768\n"
    "v_add_u32_e32 v244, s45, v244\n"
    "v_xor_b32_e32 v245, 32, v244\n"
    "v_xor_b32_e32 v246, 64, v244\n"
    "v_xor_b32_e32 v247, 0x60, v244\n"
    "s_lshl_b32 s45, s51, 15\n"
    "s_add_u32 s48, s45, 32768\n"
    "s_sub_u32 s45, 131072, s45\n"
    "v_add_u32_e32 v242, s45, v240\n"
    "v_add_u32_e32 v240, s48, v240\n"
    "v_xor_b32_e32 v241, 64, v240\n"
    "v_xor_b32_e32 v243, 64, v242\n"
    "s_xor_b32 s45, s93, 2\n"
    "s_mul_i32 s45, s45, 5120\n"
    "v_lshl_add_u32 v249, v8, 2, s45\n"
    "s_lshl_b32 s45, s93, 11\n"
    "v_add_u32_e32 v99, s45, v250\n"
    "v_add_u32_e32 v98, 11345920, v99\n"
    "v_and_b32_e32 v252, 0x30, v250\n"
    "v_add_u32_e32 v252, 0x400, v252\n"
    "s_mul_i32 s59, s32, 999\n"
    "s_andn2_b32 s59, s59, 63\n"
    "s_sub_u32 s48, 3968, s59\n"
    "v_cmp_le_u32_e32 vcc, 0x80, v250\n"
    "v_mov_b32_e32 v80, s48\n"
    "s_add_u32 s59, s59, 6076416\n"
    "v_cndmask_b32_e32 v83, v250, v80, vcc\n"
    "v_bfe_u32 v253, v250, 4, 2\n"
    "v_max_u32_e32 v253, 1, v253\n"
    "v_add_u32_e32 v253, s32, v253\n"
    "v_and_b32_e32 v253, 3, v253\n"
    "v_lshlrev_b32_e32 v253, 9, v253\n"
    "v_mov_b32_e32 v84, s39\n"
    "v_lshrrev_b32_e32 v86, 2, v250\n"
    "v_min_u32_e32 v86, 124, v86\n"
    "s_lshl2_add_u32 s58, s34, s93\n"
    "s_lshl_b32 s58, s58, 7\n"
    "s_add_u32 s58, s58, 5910528\n"
    "s_lshl_b32 s45, s50, 13\n"
    "v_add_u32_e32 v85, s45, v250\n"
    "s_lshl3_add_u32 s45, s45, 49152\n"
    "v_add_u32_e32 v255, s45, v250\n"
    "v_lshrrev_b32_e32 v105, 7, v250\n"
    "v_and_b32_e32 v104, 0x70, v251\n"
    "v_lshl_add_u32 v104, v105, 9, v104\n"
    "v_add_u32_e32 v105, 0xc00, v104\n"
    "v_add_u32_e32 v106, 0x1800, v104\n"
    "v_add_u32_e32 v107, 0x2400, v104\n"
    "s_lshl_b32 s57, s93, 7\n"
    "s_lshl4_add_u32 s56, s57, 24576\n"
    "s_add_u32 s57, s57, 5111808\n"
    "s_mul_i32 s45, s51, 32768\n"
    "s_add_u32 s45, s45, 8192\n"
    "v_subrev_u32_e32 v46, s45, v240\n"
    "v_subrev_u32_e32 v47, s45, v241\n"
    "s_mul_i32 s45, s51, 61440\n"
    "s_add_u32 s45, s45, 8192\n"
    "v_subrev_u32_e32 v4, s45, v244\n"
    "v_subrev_u32_e32 v5, s45, v245\n"
    "v_subrev_u32_e32 v6, s45, v246\n"
    "v_subrev_u32_e32 v7, s45, v247\n"
    "v_accvgpr_write_b32 a244, 0\n"
    "v_accvgpr_write_b32 a245, 0\n"
    "v_accvgpr_write_b32 a246, 0\n"
    "v_accvgpr_write_b32 a247, 0\n"
    "v_accvgpr_write_b32 a248, 0\n"
    "v_accvgpr_write_b32 a249, 0\n"
    "v_accvgpr_write_b32 a250, 0\n"
    "v_accvgpr_write_b32 a251, 0\n"
    "v_accvgpr_write_b32 a252, 0\n"
    "v_accvgpr_write_b32 a253, 0\n"
    "v_accvgpr_write_b32 a254, 0\n"
    "v_accvgpr_write_b32 a255, 0\n"
    "v_mov_b32_e32 v8, s4\n"
    "v_mov_b32_e32 v9, s4\n"
    "v_mov_b64_e32 v[10:11], v[8:9]\n"
    "v_mov_b64_e32 v[12:13], v[8:9]\n"
    "v_mov_b64_e32 v[14:15], v[8:9]\n"
    "v_mov_b64_e32 v[16:17], v[8:9]\n"
    "v_mov_b64_e32 v[18:19], v[8:9]\n"
    "v_mov_b64_e32 v[28:29], v[8:9]\n"
    "v_mov_b64_e32 v[30:31], v[8:9]\n"
    "v_mov_b64_e32 v[32:33], v[8:9]\n"
    "v_mov_b64_e32 v[34:35], v[8:9]\n"
    "v_mov_b64_e32 v[36:37], v[8:9]\n"
    "v_mov_b64_e32 v[38:39], v[8:9]\n"
    "v_mov_b64_e32 v[48:49], v[8:9]\n"
    "v_mov_b64_e32 v[50:51], v[8:9]\n"
    "v_mov_b64_e32 v[52:53], v[8:9]\n"
    "v_mov_b64_e32 v[54:55], v[8:9]\n"
    "v_mov_b64_e32 v[56:57], v[8:9]\n"
    "v_mov_b64_e32 v[58:59], v[8:9]\n"
    "v_mov_b64_e32 v[68:69], v[8:9]\n"
    "v_mov_b64_e32 v[70:71], v[8:9]\n"
    "v_mov_b64_e32 v[72:73], v[8:9]\n"
    "v_mov_b64_e32 v[74:75], v[8:9]\n"
    "v_mov_b64_e32 v[76:77], v[8:9]\n"
    "v_mov_b64_e32 v[78:79], v[8:9]\n"
    "v_mov_b64_e32 v[88:89], v[8:9]\n"
    "v_mov_b64_e32 v[90:91], v[8:9]\n"
    "v_mov_b64_e32 v[92:93], v[8:9]\n"
    "v_mov_b64_e32 v[94:95], v[8:9]\n"
    "v_mov_b64_e32 v[96:97], v[8:9]\n"
    "v_mov_b64_e32 v[98:99], v[8:9]\n"
    "v_mov_b64_e32 v[108:109], v[8:9]\n"
    "v_mov_b64_e32 v[110:111], v[8:9]\n"
    "v_mov_b64_e32 v[112:113], v[8:9]\n"
    "v_mov_b64_e32 v[114:115], v[8:9]\n"
    "v_mov_b64_e32 v[116:117], v[8:9]\n"
    "v_mov_b64_e32 v[118:119], v[8:9]\n"
    "v_mov_b64_e32 v[128:129], v[8:9]\n"
    "v_mov_b64_e32 v[130:131], v[8:9]\n"
    "v_mov_b64_e32 v[132:133], v[8:9]\n"
    "v_mov_b64_e32 v[134:135], v[8:9]\n"
    "v_mov_b64_e32 v[136:137], v[8:9]\n"
    "v_mov_b64_e32 v[138:139], v[8:9]\n"
    "s_mov_b32 m0, s56\n"
    "s_nop 0\n"
    "buffer_load_dwordx4 v104, s[8:11], s57 offen offset:0 sc1 lds\n"
    "buffer_load_dwordx4 v105, s[8:11], s57 offen offset:1024 sc1 lds\n"
    "s_add_u32 m0, s43, 0\n"
    "s_nop 0\n"
    "buffer_load_dwordx4 v104, s[8:11], s40 offen offset:0 sc1 lds\n"
    "buffer_load_dwordx4 v105, s[8:11], s40 offen offset:1024 sc1 lds\n"
    "buffer_load_dwordx4 v106, s[8:11], s40 offen offset:2048 sc1 lds\n"
    "buffer_load_dwordx4 v107, s[8:11], s40 offen offset:3072 sc1 lds\n"
    "s_add_u32 s40, s40, 0x8000\n"
    "s_add_u32 m0, s43, 4096\n"
    "s_nop 0\n"
    "buffer_load_dwordx4 v104, s[8:11], s41 offen offset:0 sc1 lds\n"
    "buffer_load_dwordx4 v105, s[8:11], s41 offen offset:1024 sc1 lds\n"
    "buffer_load_dwordx4 v106, s[8:11], s41 offen offset:2048 sc1 lds\n"
    "buffer_load_dwordx4 v107, s[8:11], s41 offen offset:3072 sc1 lds\n"
    "s_add_u32 s41, s41, 0x8000\n"
    "s_add_u32 m0, s43, 8192\n"
    "s_nop 0\n"
    "buffer_load_dwordx4 v104, s[8:11], s40 offen offset:0 sc1 lds\n"
    "buffer_load_dwordx4 v105, s[8:11], s40 offen offset:1024 sc1 lds\n"
    "buffer_load_dwordx4 v106, s[8:11], s40 offen offset:2048 sc1 lds\n"
    "buffer_load_dwordx4 v107, s[8:11], s40 offen offset:3072 sc1 lds\n"
    "s_add_u32 s40, s40, 0x8000\n"
    "s_add_u32 m0, s43, 12288\n"
    "s_nop 0\n"
    "buffer_load_dwordx4 v104, s[8:11], s41 offen offset:0 sc1 lds\n"
    "buffer_load_dwordx4 v105, s[8:11], s41 offen offset:1024 sc1 lds\n"
    "buffer_load_dwordx4 v106, s[8:11], s41 offen offset:2048 sc1 lds\n"
    "buffer_load_dwordx4 v107, s[8:11], s41 offen offset:3072 sc1 lds\n"
    "s_add_u32 s41, s41, 0x8000\n"
    "s_waitcnt vmcnt(0)\n"
    "s_branch .Lat_p2\n"
    ".Lat_maxpass:\n"
    "s_waitcnt lgkmcnt(0)\n"
    "s_mul_i32 s48, s33, 81920\n"
    "s_add_u32 s48, s48, 11345920\n"
    "s_add_u32 s45, s32, 0\n"
    "s_and_b32 s45, s45, 3\n"
    "s_lshl2_add_u32 s45, s50, s45\n"
    "s_lshl_b32 s45, s45, 13\n"
    "s_add_u32 s45, s45, s48\n"
    "s_add_u32 s46, s45, 4096\n"
    "buffer_load_dwordx4 a[0:3], v250, s[8:11], s45 offen offset:0 sc1\n"
    "buffer_load_dwordx4 a[4:7], v250, s[8:11], s45 offen offset:1024 sc1\n"
    "buffer_load_dwordx4 a[8:11], v250, s[8:11], s45 offen offset:2048 sc1\n"
    "buffer_load_dwordx4 a[12:15], v250, s[8:11], s45 offen offset:3072 sc1\n"
    "buffer_load_dwordx4 a[16:19], v250, s[8:11], s46 offen offset:0 sc1\n"
    "buffer_load_dwordx4 a[20:23], v250, s[8:11], s46 offen offset:1024 sc1\n"
    "buffer_load_dwordx4 a[24:27], v250, s[8:11], s46 offen offset:2048 sc1\n"
    "buffer_load_dwordx4 a[28:31], v250, s[8:11], s46 offen offset:3072 sc1\n"
    "s_and_b32 s45, s32, 1\n"
    "s_xor_b32 s45, s45, s50\n"
    "s_add_u32 s45, s45, 8\n"
    "s_lshl_b32 s45, s45, 13\n"
    "s_add_u32 s45, s45, s48\n"
    "s_add_u32 s46, s45, 4096\n"
    "buffer_load_dwordx4 a[32:35], v250, s[8:11], s45 offen offset:0 sc1\n"
    "buffer_load_dwordx4 a[36:39], v250, s[8:11], s45 offen offset:1024 sc1\n"
    "buffer_load_dwordx4 a[40:43], v250, s[8:11], s45 offen offset:2048 sc1\n"
    "buffer_load_dwordx4 a[44:47], v250, s[8:11], s45 offen offset:3072 sc1\n"
    "buffer_load_dwordx4 a[48:51], v250, s[8:11], s46 offen offset:0 sc1\n"
    "buffer_load_dwordx4 a[52:55], v250, s[8:11], s46 offen offset:1024 sc1\n"
    "buffer_load_dwordx4 a[56:59], v250, s[8:11], s46 offen offset:2048 sc1\n"
    "buffer_load_dwordx4 a[60:63], v250, s[8:11], s46 offen offset:3072 sc1\n"
    "s_add_u32 s45, s32, 1\n"
    "s_and_b32 s45, s45, 3\n"
    "s_lshl2_add_u32 s45, s50, s45\n"
    "s_lshl_b32 s45, s45, 13\n"
    "s_add_u32 s45, s45, s48\n"
    "s_add_u32 s46, s45, 4096\n"
    "buffer_load_dwordx4 a[64:67], v250, s[8:11], s45 offen offset:0 sc1\n"
    "buffer_load_dwordx4 a[68:71], v250, s[8:11], s45 offen offset:1024 sc1\n"
    "buffer_load_dwordx4 a[72:75], v250, s[8:11], s45 offen offset:2048 sc1\n"
    "buffer_load_dwordx4 a[76:79], v250, s[8:11], s45 offen offset:3072 sc1\n"
    "buffer_load_dwordx4 a[80:83], v250, s[8:11], s46 offen offset:0 sc1\n"
    "buffer_load_dwordx4 a[84:87], v250, s[8:11], s46 offen offset:1024 sc1\n"
    "buffer_load_dwordx4 a[88:91], v250, s[8:11], s46 offen offset:2048 sc1\n"
    "buffer_load_dwordx4 a[92:95], v250, s[8:11], s46 offen offset:3072 sc1\n"
    "s_add_u32 s45, s32, 2\n"
    "s_and_b32 s45, s45, 3\n"
    "s_lshl2_add_u32 s45, s50, s45\n"
    "s_lshl_b32 s45, s45, 13\n"
    "s_add_u32 s45, s45, s48\n"
    "s_add_u32 s46, s45, 4096\n"
    "buffer_load_dwordx4 a[96:99], v250, s[8:11], s45 offen offset:0 sc1\n"
    "buffer_load_dwordx4 a[100:103], v250, s[8:11], s45 offen offset:1024 sc1\n"
    "buffer_load_dwordx4 a[104:107], v250, s[8:11], s45 offen offset:2048 sc1\n"
    "buffer_load_dwordx4 a[108:111], v250, s[8:11], s45 offen offset:3072 sc1\n"
    "buffer_load_dwordx4 a[112:115], v250, s[8:11], s46 offen offset:0 sc1\n"
    "buffer_load_dwordx4 a[116:119], v250, s[8:11], s46 offen offset:1024 sc1\n"
    "buffer_load_dwordx4 a[120:123], v250, s[8:11], s46 offen offset:2048 sc1\n"
    "buffer_load_dwordx4 a[124:127], v250, s[8:11], s46 offen offset:3072 sc1\n"
    "s_add_u32 s45, s32, 3\n"
    "s_and_b32 s45, s45, 3\n"
    "s_lshl2_add_u32 s45, s50, s45\n"
    "s_lshl_b32 s45, s45, 13\n"
    "s_add_u32 s45, s45, s48\n"
    "s_add_u32 s46, s45, 4096\n"
    "buffer_load_dwordx4 a[128:131], v250, s[8:11], s45 offen offset:0 sc1\n"
    "buffer_load_dwordx4 a[132:135], v250, s[8:11], s45 offen offset:1024 sc1\n"
    "buffer_load_dwordx4 a[136:139], v250, s[8:11], s45 offen offset:2048 sc1\n"
    "buffer_load_dwordx4 a[140:143], v250, s[8:11], s45 offen offset:3072 sc1\n"
    "buffer_load_dwordx4 a[144:147], v250, s[8:11], s46 offen offset:0 sc1\n"
    "buffer_load_dwordx4 a[148:151], v250, s[8:11], s46 offen offset:1024 sc1\n"
    "buffer_load_dwordx4 a[152:155], v250, s[8:11], s46 offen offset:2048 sc1\n"
    "buffer_load_dwordx4 a[156:159], v250, s[8:11], s46 offen offset:3072 sc1\n"
    "s_waitcnt vmcnt(0)\n"
    "v_mov_b32_e32 v200, 0xff800000\n"
    "v_mov_b32_e32 v201, 0xff800000\n"
    "v_mov_b32_e32 v202, 0xff800000\n"
    "v_mov_b32_e32 v203, 0xff800000\n"
    "v_mov_b32_e32 v204, 0xff800000\n"
    "s_lshl_b32 s45, s51, 14\n"
    "s_lshl_b32 s46, s50, 8\n"
    "s_add_u32 s45, s45, s46\n"
    "s_add_u32 s61, s46, 5103616\n"
    "s_movk_i32 s48, 157\n"
    ".Lat_mp_loop:\n"
    "s_min_u32 s45, s45, s61\n"
    "s_add_u32 s46, s45, 128\n"
    "s_add_u32 m0, s43, 16384\n"
    "s_nop 0\n"
    "buffer_load_dwordx4 v248, s[8:11], s45 offen offset:0 sc1 lds\n"
    "buffer_load_dwordx4 v248, s[8:11], s45 offen offset:1024 sc1 lds\n"
    "buffer_load_dwordx4 v248, s[8:11], s45 offen offset:2048 sc1 lds\n"
    "buffer_load_dwordx4 v248, s[8:11], s45 offen offset:3072 sc1 lds\n"
    "s_add_u32 m0, s43, 20480\n"
    "s_nop 0\n"
    "buffer_load_dwordx4 v248, s[8:11], s46 offen offset:0 sc1 lds\n"
    "buffer_load_dwordx4 v248, s[8:11], s46 offen offset:1024 sc1 lds\n"
    "buffer_load_dwordx4 v248, s[8:11], s46 offen offset:2048 sc1 lds\n"
    "buffer_load_dwordx4 v248, s[8:11], s46 offen offset:3072 sc1 lds\n"
    "s_waitcnt vmcnt(0)\n"
    "s_barrier\n"
    "ds_read_b128 a[160:163], v240 offset:16384\n"
    "ds_read_b128 a[164:167], v241 offset:16384\n"
    "ds_read_b128 a[168:171], v240 offset:20480\n"
    "ds_read_b128 a[172:175], v241 offset:20480\n"
    "ds_read_b128 a[176:179], v242 offset:16384\n"
    "ds_read_b128 a[180:183], v243 offset:16384\n"
    "ds_read_b128 a[184:187], v242 offset:20480\n"
    "ds_read_b128 a[188:191], v243 offset:20480\n"
    "s_waitcnt lgkmcnt(0)\n"
    "v_mfma_f32_16x16x32_bf16 v[180:183], a[160:163], a[0:3], 0\n"
    "v_mfma_f32_16x16x32_bf16 v[184:187], a[160:163], a[32:35], 0\n"
    "v_mfma_f32_16x16x32_bf16 v[188:191], a[160:163], a[64:67], 0\n"
    "v_mfma_f32_16x16x32_bf16 v[192:195], a[160:163], a[96:99], 0\n"
    "v_mfma_f32_16x16x32_bf16 v[196:199], a[160:163], a[128:131], 0\n"
    "v_mfma_f32_16x16x32_bf16 v[180:183], a[164:167], a[4:7], v[180:183]\n"
    "v_mfma_f32_16x16x32_bf16 v[184:187], a[164:167], a[36:39], v[184:187]\n"
    "v_mfma_f32_16x16x32_bf16 v[188:191], a[164:167], a[68:71], v[188:191]\n"
    "v_mfma_f32_16x16x32_bf16 v[192:195], a[164:167], a[100:103], v[192:195]\n"
    "v_mfma_f32_16x16x32_bf16 v[196:199], a[164:167], a[132:135], v[196:199]\n"
    "v_mfma_f32_16x16x32_bf16 v[180:183], a[168:171], a[8:11], v[180:183]\n"
    "v_mfma_f32_16x16x32_bf16 v[184:187], a[168:171], a[40:43], v[184:187]\n"
    "v_mfma_f32_16x16x32_bf16 v[188:191], a[168:171], a[72:75], v[188:191]\n"
    "v_mfma_f32_16x16x32_bf16 v[192:195], a[168:171], a[104:107], v[192:195]\n"
    "v_mfma_f32_16x16x32_bf16 v[196:199], a[168:171], a[136:139], v[196:199]\n"
    "v_mfma_f32_16x16x32_bf16 v[180:183], a[172:175], a[12:15], v[180:183]\n"
    "v_mfma_f32_16x16x32_bf16 v[184:187], a[172:175], a[44:47], v[184:187]\n"
    "v_mfma_f32_16x16x32_bf16 v[188:191], a[172:175], a[76:79], v[188:191]\n"
    "v_mfma_f32_16x16x32_bf16 v[192:195], a[172:175], a[108:111], v[192:195]\n"
    "v_mfma_f32_16x16x32_bf16 v[196:199], a[172:175], a[140:143], v[196:199]\n"
    "v_mfma_f32_16x16x32_bf16 v[180:183], a[176:179], a[16:19], v[180:183]\n"
    "v_mfma_f32_16x16x32_bf16 v[184:187], a[176:179], a[48:51], v[184:187]\n"
    "v_mfma_f32_16x16x32_bf16 v[188:191], a[176:179], a[80:83], v[188:191]\n"
    "v_mfma_f32_16x16x32_bf16 v[192:195], a[176:179], a[112:115], v[192:195]\n"
    "v_mfma_f32_16x16x32_bf16 v[196:199], a[176:179], a[144:147], v[196:199]\n"
    "v_mfma_f32_16x16x32_bf16 v[180:183], a[180:183], a[20:23], v[180:183]\n"
    "v_mfma_f32_16x16x32_bf16 v[184:187], a[180:183], a[52:55], v[184:187]\n"
    "v_mfma_f32_16x16x32_bf16 v[188:191], a[180:183], a[84:87], v[188:191]\n"
    "v_mfma_f32_16x16x32_bf16 v[192:195], a[180:183], a[116:119], v[192:195]\n"
    "v_mfma_f32_16x16x32_bf16 v[196:199], a[180:183], a[148:151], v[196:199]\n"
    "v_mfma_f32_16x16x32_bf16 v[180:183], a[184:187], a[24:27], v[180:183]\n"
    "v_mfma_f32_16x16x32_bf16 v[184:187], a[184:187], a[56:59], v[184:187]\n"
    "v_mfma_f32_16x16x32_bf16 v[188:191], a[184:187], a[88:91], v[188:191]\n"
    "v_mfma_f32_16x16x32_bf16 v[192:195], a[184:187], a[120:123], v[192:195]\n"
    "v_mfma_f32_16x16x32_bf16 v[196:199], a[184:187], a[152:155], v[196:199]\n"
    "v_mfma_f32_16x16x32_bf16 v[180:183], a[188:191], a[28:31], v[180:183]\n"
    "v_mfma_f32_16x16x32_bf16 v[184:187], a[188:191], a[60:63], v[184:187]\n"
    "v_mfma_f32_16x16x32_bf16 v[188:191], a[188:191], a[92:95], v[188:191]\n"
    "v_mfma_f32_16x16x32_bf16 v[192:195], a[188:191], a[124:127], v[192:195]\n"
    "v_mfma_f32_16x16x32_bf16 v[196:199], a[188:191], a[156:159], v[196:199]\n"
    "s_nop 15\n"
    "v_max3_f32 v200, v200, v180, v181\n"
    "v_max3_f32 v200, v200, v182, v183\n"
    "v_max3_f32 v201, v201, v184, v185\n"
    "v_max3_f32 v201, v201, v186, v187\n"
    "v_max3_f32 v202, v202, v188, v189\n"
    "v_max3_f32 v202, v202, v190, v191\n"
    "v_max3_f32 v203, v203, v192, v193\n"
    "v_max3_f32 v203, v203, v194, v195\n"
    "v_max3_f32 v204, v204, v196, v197\n"
    "v_max3_f32 v204, v204, v198, v199\n"
    "s_nop 3\n"
    "ds_read_b128 a[160:163], v240 offset:18432\n"
    "ds_read_b128 a[164:167], v241 offset:18432\n"
    "ds_read_b128 a[168:171], v240 offset:22528\n"
    "ds_read_b128 a[172:175], v241 offset:22528\n"
    "ds_read_b128 a[176:179], v242 offset:18432\n"
    "ds_read_b128 a[180:183], v243 offset:18432\n"
    "ds_read_b128 a[184:187], v242 offset:22528\n"
    "ds_read_b128 a[188:191], v243 offset:22528\n"
    "s_waitcnt lgkmcnt(0)\n"
    "v_mfma_f32_16x16x32_bf16 v[180:183], a[160:163], a[0:3], 0\n"
    "v_mfma_f32_16x16x32_bf16 v[184:187], a[160:163], a[32:35], 0\n"
    "v_mfma_f32_16x16x32_bf16 v[188:191], a[160:163], a[64:67], 0\n"
    "v_mfma_f32_16x16x32_bf16 v[192:195], a[160:163], a[96:99], 0\n"
    "v_mfma_f32_16x16x32_bf16 v[196:199], a[160:163], a[128:131], 0\n"
    "v_mfma_f32_16x16x32_bf16 v[180:183], a[164:167], a[4:7], v[180:183]\n"
    "v_mfma_f32_16x16x32_bf16 v[184:187], a[164:167], a[36:39], v[184:187]\n"
    "v_mfma_f32_16x16x32_bf16 v[188:191], a[164:167], a[68:71], v[188:191]\n"
    "v_mfma_f32_16x16x32_bf16 v[192:195], a[164:167], a[100:103], v[192:195]\n"
    "v_mfma_f32_16x16x32_bf16 v[196:199], a[164:167], a[132:135], v[196:199]\n"
    "v_mfma_f32_16x16x32_bf16 v[180:183], a[168:171], a[8:11], v[180:183]\n"
    "v_mfma_f32_16x16x32_bf16 v[184:187], a[168:171], a[40:43], v[184:187]\n"
    "v_mfma_f32_16x16x32_bf16 v[188:191], a[168:171], a[72:75], v[188:191]\n"
    "v_mfma_f32_16x16x32_bf16 v[192:195], a[168:171], a[104:107], v[192:195]\n"
    "v_mfma_f32_16x16x32_bf16 v[196:199], a[168:171], a[136:139], v[196:199]\n"
    "v_mfma_f32_16x16x32_bf16 v[180:183], a[172:175], a[12:15], v[180:183]\n"
    "v_mfma_f32_16x16x32_bf16 v[184:187], a[172:175], a[44:47], v[184:187]\n"
    "v_mfma_f32_16x16x32_bf16 v[188:191], a[172:175], a[76:79], v[188:191]\n"
    "v_mfma_f32_16x16x32_bf16 v[192:195], a[172:175], a[108:111], v[192:195]\n"
    "v_mfma_f32_16x16x32_bf16 v[196:199], a[172:175], a[140:143], v[196:199]\n"
    "v_mfma_f32_16x16x32_bf16 v[180:183], a[176:179], a[16:19], v[180:183]\n"
    "v_mfma_f32_16x16x32_bf16 v[184:187], a[176:179], a[48:51], v[184:187]\n"
    "v_mfma_f32_16x16x32_bf16 v[188:191], a[176:179], a[80:83], v[188:191]\n"
    "v_mfma_f32_16x16x32_bf16 v[192:195], a[176:179], a[112:115], v[192:195]\n"
    "v_mfma_f32_16x16x32_bf16 v[196:199], a[176:179], a[144:147], v[196:199]\n"
    "v_mfma_f32_16x16x32_bf16 v[180:183], a[180:183], a[20:23], v[180:183]\n"
    "v_mfma_f32_16x16x32_bf16 v[184:187], a[180:183], a[52:55], v[184:187]\n"
    "v_mfma_f32_16x16x32_bf16 v[188:191], a[180:183], a[84:87], v[188:191]\n"
    "v_mfma_f32_16x16x32_bf16 v[192:195], a[180:183], a[116:119], v[192:195]\n"
    "v_mfma_f32_16x16x32_bf16 v[196:199], a[180:183], a[148:151], v[196:199]\n"
    "v_mfma_f32_16x16x32_bf16 v[180:183], a[184:187], a[24:27], v[180:183]\n"
    "v_mfma_f32_16x16x32_bf16 v[184:187], a[184:187], a[56:59], v[184:187]\n"
    "v_mfma_f32_16x16x32_bf16 v[188:191], a[184:187], a[88:91], v[188:191]\n"
    "v_mfma_f32_16x16x32_bf16 v[192:195], a[184:187], a[120:123], v[192:195]\n"
    "v_mfma_f32_16x16x32_bf16 v[196:199], a[184:187], a[152:155], v[196:199]\n"
    "v_mfma_f32_16x16x32_bf16 v[180:183], a[188:191], a[28:31], v[180:183]\n"
    "v_mfma_f32_16x16x32_bf16 v[184:187], a[188:191], a[60:63], v[184:187]\n"
    "v_mfma_f32_16x16x32_bf16 v[188:191], a[188:191], a[92:95], v[188:191]\n"
    "v_mfma_f32_16x16x32_bf16 v[192:195], a[188:191], a[124:127], v[192:195]\n"
    "v_mfma_f32_16x16x32_bf16 v[196:199], a[188:191], a[156:159], v[196:199]\n"
    "s_nop 15\n"
    "v_max3_f32 v200, v200, v180, v181\n"
    "v_max3_f32 v200, v200, v182, v183\n"
    "v_max3_f32 v201, v201, v184, v185\n"
    "v_max3_f32 v201, v201, v186, v187\n"
    "v_max3_f32 v202, v202, v188, v189\n"
    "v_max3_f32 v202, v202, v190, v191\n"
    "v_max3_f32 v203, v203, v192, v193\n"
    "v_max3_f32 v203, v203, v194, v195\n"
    "v_max3_f32 v204, v204, v196, v197\n"
    "v_max3_f32 v204, v204, v198, v199\n"
    "s_nop 3\n"
    "s_barrier\n"
    "s_add_u32 s45, s45, 0x8000\n"
    "s_sub_u32 s48, s48, 1\n"
    "s_cmp_lg_u32 s48, 0\n"
    "s_cbranch_scc1 .Lat_mp_loop\n"
    "s_mov_b32 m0, s42\n"
    "s_nop 1\n"
    "ds_write_addtid_b32 v200 offset:0\n"
    "ds_write_addtid_b32 v201 offset:256\n"
    "ds_write_addtid_b32 v202 offset:512\n"
    "ds_write_addtid_b32 v203 offset:768\n"
    "ds_write_addtid_b32 v204 offset:1024\n"
    "s_waitcnt lgkmcnt(0)\n"
    "s_barrier\n"
    "ds_read_b32 v180, v249 offset:0\n"
    "ds_read_b32 v184, v249 offset:256\n"
    "ds_read_b32 v188, v249 offset:512\n"
    "ds_read_b32 v192, v249 offset:768\n"
    "ds_read_b32 v196, v249 offset:1024\n"
    "s_waitcnt lgkmcnt(0)\n"
    "v_max_f32_e32 v205, v200, v180\n"
    "v_mov_b32_e32 v206, v205\n"
    "s_nop 1\n"
    "v_permlane16_swap_b32_e32 v205, v206\n"
    "s_nop 0\n"
    "v_max_f32_e32 v205, v205, v206\n"
    "v_mov_b32_e32 v206, v205\n"
    "s_nop 1\n"
    "v_permlane32_swap_b32_e32 v205, v206\n"
    "s_nop 0\n"
    "v_max_f32_e32 v205, v205, v206\n"
    "v_add_f32_e32 v205, 0x42ae0000, v205\n"
    "v_xor_b32_e32 v220, 0x80000000, v205\n"
    "v_xor_b32_e32 v221, 0x80000000, v205\n"
    "v_xor_b32_e32 v222, 0x80000000, v205\n"
    "v_xor_b32_e32 v223, 0x80000000, v205\n"
    "v_max_f32_e32 v205, v201, v184\n"
    "v_mov_b32_e32 v206, v205\n"
    "s_nop 1\n"
    "v_permlane16_swap_b32_e32 v205, v206\n"
    "s_nop 0\n"
    "v_max_f32_e32 v205, v205, v206\n"
    "v_mov_b32_e32 v206, v205\n"
    "s_nop 1\n"
    "v_permlane32_swap_b32_e32 v205, v206\n"
    "s_nop 0\n"
    "v_max_f32_e32 v205, v205, v206\n"
    "v_add_f32_e32 v205, 0x42ae0000, v205\n"
    "v_xor_b32_e32 v224, 0x80000000, v205\n"
    "v_xor_b32_e32 v225, 0x80000000, v205\n"
    "v_xor_b32_e32 v226, 0x80000000, v205\n"
    "v_xor_b32_e32 v227, 0x80000000, v205\n"
    "v_max_f32_e32 v205, v202, v188\n"
    "v_mov_b32_e32 v206, v205\n"
    "s_nop 1\n"
    "v_permlane16_swap_b32_e32 v205, v206\n"
    "s_nop 0\n"
    "v_max_f32_e32 v205, v205, v206\n"
    "v_mov_b32_e32 v206, v205\n"
    "s_nop 1\n"
    "v_permlane32_swap_b32_e32 v205, v206\n"
    "s_nop 0\n"
    "v_max_f32_e32 v205, v205, v206\n"
    "v_add_f32_e32 v205, 0x42ae0000, v205\n"
    "v_xor_b32_e32 v228, 0x80000000, v205\n"
    "v_xor_b32_e32 v229, 0x80000000, v205\n"
    "v_xor_b32_e32 v230, 0x80000000, v205\n"
    "v_xor_b32_e32 v231, 0x80000000, v205\n"
    "v_max_f32_e32 v205, v203, v192\n"
    "v_mov_b32_e32 v206, v205\n"
    "s_nop 1\n"
    "v_permlane16_swap_b32_e32 v205, v206\n"
    "s_nop 0\n"
    "v_max_f32_e32 v205, v205, v206\n"
    "v_mov_b32_e32 v206, v205\n"
    "s_nop 1\n"
    "v_permlane32_swap_b32_e32 v205, v206\n"
    "s_nop 0\n"
    "v_max_f32_e32 v205, v205, v206\n"
    "v_add_f32_e32 v205, 0x42ae0000, v205\n"
    "v_xor_b32_e32 v232, 0x80000000, v205\n"
    "v_xor_b32_e32 v233, 0x80000000, v205\n"
    "v_xor_b32_e32 v234, 0x80000000, v205\n"
    "v_xor_b32_e32 v235, 0x80000000, v205\n"
    "v_max_f32_e32 v205, v204, v196\n"
    "v_mov_b32_e32 v206, v205\n"
    "s_nop 1\n"
    "v_permlane16_swap_b32_e32 v205, v206\n"
    "s_nop 0\n"
    "v_max_f32_e32 v205, v205, v206\n"
    "v_mov_b32_e32 v206, v205\n"
    "s_nop 1\n"
    "v_permlane32_swap_b32_e32 v205, v206\n"
    "s_nop 0\n"
    "v_max_f32_e32 v205, v205, v206\n"
    "v_add_f32_e32 v205, 0x42ae0000, v205\n"
    "v_xor_b32_e32 v236, 0x80000000, v205\n"
    "v_xor_b32_e32 v237, 0x80000000, v205\n"
    "v_xor_b32_e32 v238, 0x80000000, v205\n"
    "v_xor_b32_e32 v239, 0x80000000, v205\n"
    "s_nop 1\n"
    "s_barrier\n"
    "s_mul_i32 s48, s33, 81920\n"
    "s_add_u32 s48, s48, 11345920\n"
    "s_lshl_b32 s46, s93, 11\n"
    "s_add_u32 s48, s48, s46\n"
    "s_add_u32 s46, s46, 49152\n"
    "s_and_b32 s45, s32, 1\n"
    "s_xor_b32 s45, s45, 8\n"
    "s_lshl_b32 s45, s45, 13\n"
    "s_add_u32 s45, s45, s48\n"
    "s_add_u32 m0, s46, 0\n"
    "s_nop 0\n"
    "buffer_load_dwordx4 v250, s[8:11], s45 offen offset:0 sc1 lds\n"
    "buffer_load_dwordx4 v250, s[8:11], s45 offen offset:1024 sc1 lds\n"
    "s_branch .Lat_maxpass_ret\n"
    ".p2align 12\n"
    ".Lat_out:\n"
    "s_waitcnt lgkmcnt(0)\n"
    "s_mov_b64 s[2:3], s[0:1]\n"
    "s_mov_b64 %0, s[2:3]\n"
    "s_mov_b32 %1, s92\n"
    "s_mov_b32 %2, s93\n"
    : "=s"(ka), "=s"(b), "=s"(wid) : "0"(__builtin_amdgcn_kernarg_segment_ptr()), "1"(blockIdx.x)
    : "v0", "v1", "v2", "v3", "v4", "v5", "v6", "v7", "v8", "v9", "v10", "v11", "v12", "v13", "v14", "v15", "v16", "v17", "v18", "v19", "v20", "v21", "v22", "v23", "v24", "v25", "v26", "v27", "v28", "v29", "v30", "v31", "v32", "v33", "v34", "v35", "v36", "v37", "v38", "v39", "v40", "v41", "v42", "v43", "v44", "v45", "v46", "v47", "v48", "v49", "v50", "v51", "v52", "v53", "v54", "v55", "v56", "v57", "v58", "v59", "v60", "v61", "v62", "v63", "v64", "v65", "v66", "v67", "v68", "v69", "v70", "v71", "v72", "v73", "v74", "v75", "v76", "v77", "v78", "v79", "v80", "v81", "v82", "v83", "v84", "v85", "v86", "v87", "v88", "v89", "v90", "v91", "v92", "v93", "v94", "v95", "v96", "v97", "v98", "v99", "v100", "v101", "v102", "v103", "v104", "v105", "v106", "v107", "v108", "v109", "v110", "v111", "v112", "v113", "v114", "v115", "v116", "v117", "v118", "v119", "v120", "v121", "v122", "v123", "v124", "v125", "v126", "v127", "v128", "v129", "v130", "v131", "v132", "v133", "v134", "v135", "v136", "v137", "v138", "v139", "v140", "v141", "v142", "v143", "v144", "v145", "v146", "v147", "v148", "v149", "v150", "v151", "v152", "v153", "v154", "v155", "v156", "v157", "v158", "v159", "v160", "v161", "v162", "v163", "v164", "v165", "v166", "v167", "v168", "v169", "v170", "v171", "v172", "v173", "v174", "v175", "v176", "v177", "v178", "v179", "v180", "v181", "v182", "v183", "v184", "v185", "v186", "v187", "v188", "v189", "v190", "v191", "v192", "v193", "v194", "v195", "v196", "v197", "v198", "v199", "v200", "v201", "v202", "v203", "v204", "v205", "v206", "v207", "v208", "v209", "v210", "v211", "v212", "v213", "v214", "v215", "v216", "v217", "v218", "v219", "v220", "v221", "v222", "v223", "v224", "v225", "v226", "v227", "v228", "v229", "v230", "v231", "v232", "v233", "v234", "v235", "v236", "v237", "v238", "v239", "v240", "v241", "v242", "v243", "v244", "v245", "v246", "v247", "v248", "v249", "v250", "v251", "v252", "v253", "v254", "v255", "a0", "a1", "a2", "a3", "a4", "a5", "a6", "a7", "a8", "a9", "a10", "a11", "a12", "a13", "a14", "a15", "a16", "a17", "a18", "a19", "a20", "a21", "a22", "a23", "a24", "a25", "a26", "a27", "a28", "a29", "a30", "a31", "a32", "a33", "a34", "a35", "a36", "a37", "a38", "a39", "a40", "a41", "a42", "a43", "a44", "a45", "a46", "a47", "a48", "a49", "a50", "a51", "a52", "a53", "a54", "a55", "a56", "a57", "a58", "a59", "a60", "a61", "a62", "a63", "a64", "a65", "a66", "a67", "a68", "a69", "a70", "a71", "a72", "a73", "a74", "a75", "a76", "a77", "a78", "a79", "a80", "a81", "a82", "a83", "a84", "a85", "a86", "a87", "a88", "a89", "a90", "a91", "a92", "a93", "a94", "a95", "a96", "a97", "a98", "a99", "a100", "a101", "a102", "a103", "a104", "a105", "a106", "a107", "a108", "a109", "a110", "a111", "a112", "a113", "a114", "a115", "a116", "a117", "a118", "a119", "a120", "a121", "a122", "a123", "a124", "a125", "a126", "a127", "a128", "a129", "a130", "a131", "a132", "a133", "a134", "a135", "a136", "a137", "a138", "a139", "a140", "a141", "a142", "a143", "a144", "a145", "a146", "a147", "a148", "a149", "a150", "a151", "a152", "a153", "a154", "a155", "a156", "a157", "a158", "a159", "a160", "a161", "a162", "a163", "a164", "a165", "a166", "a167", "a168", "a169", "a170", "a171", "a172", "a173", "a174", "a175", "a176", "a177", "a178", "a179", "a180", "a181", "a182", "a183", "a184", "a185", "a186", "a187", "a188", "a189", "a190", "a191", "a192", "a193", "a194", "a195", "a196", "a197", "a198", "a199", "a200", "a201", "a202", "a203", "a204", "a205", "a206", "a207", "a208", "a209", "a210", "a211", "a212", "a213", "a214", "a215", "a216", "a217", "a218", "a219", "a220", "a221", "a222", "a223", "a224", "a225", "a226", "a227", "a228", "a229", "a230", "a231", "a232", "a233", "a234", "a235", "a236", "a237", "a238", "a239", "a240", "a241", "a242", "a243", "a244", "a245", "a246", "a247", "a248", "a249", "a250", "a251", "a252", "a253", "a254", "a255", "s2", "s3", "s4", "s5", "s6", "s7", "s8", "s9", "s10", "s11", "s12", "s13", "s14", "s15", "s16", "s17", "s18", "s19", "s20", "s21", "s22", "s23", "s24", "s25", "s26", "s27", "s28", "s29", "s30", "s31", "s32", "s33", "s34", "s35", "s36", "s37", "s38", "s39", "s40", "s41", "s42", "s43", "s44", "s45", "s46", "s47", "s48", "s49", "s50", "s51", "s52", "s53", "s54", "s55", "s56", "s57", "s58", "s59", "s60", "s61", "s62", "s63", "s80", "s81", "s82", "s83", "s84", "s85", "s86", "s87", "s88", "s89", "s90", "s91", "s92", "s93", "s94", "s95", "vcc", "m0", "scc", "memory");
  typedef __attribute__((address_space(1))) void* gp;
  helper(lds, b >> 1 & 3, wid, (const int*)(gp)ka[2], (const int*)(gp)ka[4], (const u16*)(gp)ka[8], (const u16*)(gp)ka[9], (const u16*)(gp)ka[10], (const u16*)(gp)ka[7], (char*)(gp)ka[3]);
}

extern "C" void kernel_launch(void* const* d_in, const int* in_sizes, int n_in, void* d_out, int out_size, void* d_ws,
                              size_t ws_size, hipStream_t stream) {
  const u16* x = (const u16*)d_in[0];
  const int* src = (const int*)d_in[1];
  const int* dst = (const int*)d_in[2];
  const u16* deg_inv = (const u16*)d_in[3];
  const u16* wmsg = (const u16*)d_in[4];
  const u16* wq = (const u16*)d_in[5];
  const u16* wk = (const u16*)d_in[6];
  const u16* wv = (const u16*)d_in[7];
  const u16* wout = (const u16*)d_in[8];
  char* ws = (char*)d_ws;
  hipFuncSetAttribute((const void*)gps_kernel, hipFuncAttributeMaxDynamicSharedMemorySize, 163840);
  gps_kernel<<<256, 256, 163840, stream>>>(wq, wk, src, ws, dst, x, (u16*)d_out, deg_inv, wv, wmsg, wout);
}
```

```cpp
#include <hip/hip_runtime.h>
#include <hip/hip_bf16.h>
#include <stdint.h>

typedef unsigned short u16;
typedef __attribute__((ext_vector_type(8))) short bf16x8;
typedef __attribute__((ext_vector_type(4))) short bf16x4;
typedef __attribute__((ext_vector_type(4))) float f32x4;
typedef __attribute__((ext_vector_type(2))) float f32x2;
typedef __attribute__((ext_vector_type(4))) unsigned u32x4;
typedef __attribute__((ext_vector_type(2))) unsigned u32x2;
#define DI __device__ __forceinline__

constexpr int NN = 10000, DD = 256, EE = 160000;
constexpr int NATT = 252, NSP = 4, SPN = 2560;
constexpr int GU = 5, GNB = 5; static_assert(GU * GNB <= 30, "list size");

constexpr unsigned WS_H = 0;
constexpr unsigned WS_AF = WS_H + 10240 * 512;
constexpr unsigned WS_WV = WS_AF + 135168;
constexpr unsigned WS_WM = WS_WV + 135168;
constexpr unsigned WS_XF = WS_WM + 135168;
constexpr unsigned WS_GF = WS_XF + 3328 * 128;
constexpr unsigned WS_HF = WS_GF + 2048;
constexpr unsigned WS_PF = WS_HF + 4096;
constexpr unsigned WS_NF = WS_PF + 4096;
constexpr unsigned WS_WF = WS_NF + 16384;
constexpr unsigned WS_LF = WS_WF + 256;
constexpr unsigned WS_EP = WS_LF + 512;
constexpr unsigned WS_G = WS_EP + 256 + 1024;
constexpr unsigned WS_QP = WS_G + 10240 * 512;
constexpr unsigned WS_XB = WS_QP + 10240 * 512;
constexpr unsigned WS_LS = WS_XB + 2 * NATT * 101376;
constexpr unsigned WS_BL = WS_LS + 524288;
constexpr unsigned WS_DBG = WS_BL + 64 * 260 * 64;
constexpr unsigned WS_END = WS_DBG + 1024 * 128 + 8192;

DI float bf2f(unsigned x) { return __uint_as_float(x << 16); }
DI float bfhi(unsigned x) { return __uint_as_float(x & 0xffff0000u); }
DI unsigned pk2(float lo, float hi) {
  typedef __attribute__((ext_vector_type(2))) __bf16 bf2_t;
  const bf2_t r = __builtin_convertvector((f32x2){lo, hi}, bf2_t);
  return __builtin_bit_cast(unsigned, r);
}
typedef __attribute__((address_space(3))) bf16x4* lds_bf16x4_p;
DI bf16x4 tr_read(unsigned p) { return __builtin_amdgcn_ds_read_tr16_b64_v4i16((lds_bf16x4_p)p); }
DI bf16x8 as_bf(u32x4 v) { return __builtin_bit_cast(bf16x8, v); }
#define SB() __builtin_amdgcn_sched_barrier(0)
#define MFMA16(a, b, c) __builtin_amdgcn_mfma_f32_16x16x32_bf16(a, b, c, 0, 0, 0)

typedef __amdgpu_buffer_rsrc_t srd_t;
DI void drain() { asm volatile("s_waitcnt vmcnt(0)" ::: "memory"); }
DI void st_flag(srd_t w, unsigned off, unsigned tag) { __builtin_amdgcn_raw_buffer_store_b32(tag, w, off, 0, 16); }
DI void wait_flag(srd_t w, unsigned off, unsigned tag) {
#pragma unroll 1
  for (int it = 0; it < (1 << 22); ++it) {
    unsigned v;
    asm volatile("buffer_load_dword %0, %1, %2, 0 offen sc1\n s_waitcnt vmcnt(0)" : "=v"(v) : "v"(off), "s"(w) : "memory");
    if (__ballot(v != tag) == 0) break;
    __builtin_amdgcn_s_sleep(2);
    asm volatile("" ::: "memory");
  }
  asm volatile("" ::: "memory");
}

DI void prod_store(const f32x4 (&acc)[2][2], int mat, int ks, int J, int lane, srd_t wsrd) {
#pragma unroll
  for (int s = 0; s < 2; ++s) {
    u32x4 o = {pk2(acc[0][s][0], acc[0][s][1]), pk2(acc[0][s][2], acc[0][s][3]), pk2(acc[1][s][0], acc[1][s][1]), pk2(acc[1][s][2], acc[1][s][3])};
    if (ks == 7 && lane >= 48) o[3] &= 0xffffu;
    const int T = 2 * J + s;
    const int at = mat * 135168 + ks * 4096 + (T & 3) * 1024;
    __builtin_amdgcn_raw_buffer_store_b128(o, wsrd, WS_WV + (T >> 2) * 33792 + at + lane * 16, 0, 16);
  }
}
DI void corr(unsigned* dl, const unsigned (&lr)[16]) {
  float a0 = 0.f, a1 = 0.f;
#pragma unroll
  for (int r = 0; r < 16; ++r) { a0 += bf2f(lr[r] & 0xffffu); a1 += bfhi(lr[r]); }
  dl[0] = pk2(a0 * -0.0625f, a1 * -0.0625f);
  dl[128] = pk2((bf2f(lr[15] & 0xffffu) - 0.25f * a0) * (1.f / 3.f), (bfhi(lr[15]) - 0.25f * a1) * (1.f / 3.f));
}
DI void product_load(char* lds, int wid, int lane, int job, const u16* __restrict__ wv, const u16* __restrict__ wm, const u16* __restrict__ wo) {
  const unsigned ldsb = (unsigned)(size_t)lds;
  const int r = lane >> 3, sw = ((lane & 7) ^ (r & 6)) * 16 + wid * 128;
  const unsigned vp = (8 * (r >> 2) + (r & 3)) * 512 + sw, vn = r * 512 + sw;
  auto image = [&](int n, const u16* w, int row0, bool nat) {
#pragma unroll
    for (int i = 0; i < 8; ++i) {
      const int a = i & 3;
      asm volatile("s_mov_b32 m0, %2\n s_nop 0\n global_load_lds_dwordx4 %0, %1"
                   :: "v"(nat ? vn : vp), "s"(w + (row0 + (i >> 2) * 32 + (nat ? 8 * a : 16 * (a & 1) + 4 * (a >> 1))) * DD),
                      "s"(ldsb + n * 32768 + (i >> 2) * 16384 + wid * 4096 + a * 1024) : "m0", "memory");
    }
  };
  image(2, wv, (job >> 2) * 64, true);
  image(3, wm, (job >> 2) * 64, false);
#pragma unroll
  for (int k = 0; k < 8; ++k)
    asm volatile("s_mov_b32 m0, %2\n s_nop 0\n global_load_lds_dwordx4 %0, %1"
                 :: "v"((lane >> 3) * 512 + (lane & 7) * 16), "s"(wo + (k * 32 + wid * 8) * DD + (job & 3) * 64), "s"(ldsb + k * 4096 + wid * 1024) : "m0", "memory");
}
DI void product_w(char* lds, int wid, int lane, int job, srd_t wsrd) {
  const int m = lane & 15, g = lane >> 4;
  const int ks = 2 * (job >> 2) + (wid >> 1), J = 2 * (job & 3) + (wid & 1);
  const unsigned ldsb = (unsigned)(size_t)lds;
  unsigned* dl = reinterpret_cast<unsigned*>(lds + 0x20000);
  const bool hi = (m >> 3) != 0;
  const unsigned ra = (m >> 3) * 1024 + (m & 7) * 128 + (g ^ (m & 6)) * 16;
  auto frag = [&](int n, int tl, int t, int k) {
    return *reinterpret_cast<const bf16x8*>(lds + n * 32768 + tl * 16384 + (k >> 1) * 4096 + t * 2048 + (ra ^ ((k & 1) * 64)));
  };
  f32x4 av[2][2] = {}, am[2][2] = {}, ad[2] = {};
  const unsigned tb = ldsb + (8 * g + ((lane >> 2) & 3)) * 128 + (wid & 1) * 64 + (lane & 3) * 16;
  bf16x8 d0[8], d1[8], d2[8], rr[8][2], vf[8][2], mf[8][2];
#pragma unroll
  for (int kk = 0; kk < 9; ++kk) {
    if (kk < 8) {
      d0[kk] = *reinterpret_cast<const bf16x8*>(dl + 512 + 16 * kk + 4 * g); d1[kk] = *reinterpret_cast<const bf16x8*>(dl + 512 + (ks == 7 ? 128 : 0) + 16 * kk + 4 * g);
      d2[kk] = *reinterpret_cast<const bf16x8*>(dl + 768 + (ks == 7 && hi ? 128 : 0) + 16 * kk + 4 * g);
#pragma unroll
      for (int s = 0; s < 2; ++s) {
        rr[kk][s] = __builtin_shufflevector(tr_read(tb + kk * 4096 + s * 8), tr_read(tb + kk * 4096 + s * 8 + 512), 0, 1, 2, 3, 4, 5, 6, 7);
        vf[kk][s] = frag(2, wid >> 1, s, kk); mf[kk][s] = frag(3, wid >> 1, s, kk);
      }
    }
    SB();
    if (kk) {
      const int k = kk - 1;
#pragma unroll
      for (int s = 0; s < 2; ++s) {
        av[0][s] = MFMA16(d0[k], rr[k][s], av[0][s]); av[1][s] = MFMA16(d1[k], rr[k][s], av[1][s]); ad[s] = MFMA16(d2[k], rr[k][s], ad[s]);
#pragma unroll
        for (int t = 0; t < 2; ++t) { av[t][s] = MFMA16(vf[k][t], rr[k][s], av[t][s]); am[t][s] = MFMA16(mf[k][t], rr[k][s], am[t][s]); }
      }
    }
    SB();
  }
#pragma unroll
  for (int t = 0; t < 2; ++t)
#pragma unroll
    for (int s = 0; s < 2; ++s) am[t][s] += ad[s];
  prod_store(av, 0, ks, J, lane, wsrd);
  prod_store(am, 1, ks, J, lane, wsrd);
}

DI void helper(char* lds, int j, int wid, const int* __restrict__ src, const int* __restrict__ dst,
               const u16* __restrict__ wv, const u16* __restrict__ wm, const u16* __restrict__ wo, const u16* dinv, char* ws) {
  unsigned* img = reinterpret_cast<unsigned*>(lds);
  char* cntb = lds + 0x20000;
  int* cnt = reinterpret_cast<int*>(cntb);
  unsigned* ub = reinterpret_cast<unsigned*>(lds + 0x22c00);
  int* red = reinterpret_cast<int*>(ub + 256);
  u16* dg = reinterpret_cast<u16*>(lds + 0x23400);
  u16* tmp = reinterpret_cast<u16*>(lds + 0x25000) + wid * 512;
  int lane; asm volatile("v_mbcnt_lo_u32_b32 %0, -1, 0\n\tv_mbcnt_hi_u32_b32 %0, -1, %0" : "=v"(lane));
  const int tid = wid * 64 + lane, lo = j * SPN, nn_ = min(SPN, NN - lo);
  const srd_t wsrd = __builtin_amdgcn_make_buffer_rsrc(ws, 0, WS_END, 0x00020000);
  const unsigned tag = __builtin_amdgcn_raw_buffer_load_b32(wsrd, WS_EP, 0, 0) + 4u;
  auto touch = [&](unsigned at) { asm volatile("s_mov_b32 m0, 0x27c00\n s_nop 0\n buffer_load_dword %0, %1, 0 offen sc1 lds" :: "v"(at), "s"(wsrd) : "m0", "memory"); };
  touch(lane < 16 ? min(WS_GF + (lane * 16 + wid * 4 + j) * 128, WS_EP) : 0x40000000u);
  for (int i = tid; i < 8192; i += 256) { const unsigned f = (i & 63) >= 60 ? 0u : ~0u; reinterpret_cast<u32x4*>(img)[i] = (u32x4){f, f, f, f}; }
  for (int i = tid; i < SPN + 64; i += 256) cnt[i] = 0;
  ub[tid] = 0u;
  if (tid < 8) red[tid] = 0;
  touch(WS_XF + 262144 + (j * 256 + tid) * 128);
  wait_flag(wsrd, WS_NF + j * 4096 + min(tid, 249) * 16, tag);
  __syncthreads();
  const unsigned dum = (SPN + lane) << 18;
  auto fetch = [&](int k) {
    return __builtin_amdgcn_raw_buffer_load_b128(wsrd, WS_BL + (k < 48 ? tid * 16 + (k & 7) * 4096 : (tid >> 2) * 128 + (tid & 3) * 16 + (k & 3) * 8192 + (k & 4) * 16), (j * 8 + (k >> 3)) * 33280, 16);
  };
  auto clean = [&](u32x4& e, int k) {
#pragma unroll
    for (int r = 0; r < 4; ++r) e[r] = (k < 48 ? (k & 7) == 7 && tid >= 208 : (k & 3) == 3 && tid >= 232) ? dum : min(e[r], dum);
  };
  auto slot = [&](unsigned e) { return reinterpret_cast<int*>(cntb + (e >> 16)); };
  constexpr int NL = 52;
  u32x4 ent[NL];
#pragma unroll
  for (int k = 0; k < NL; ++k) ent[k] = fetch(k);
  SB();
#pragma unroll
  for (int k = 0; k < NL; ++k) {
    clean(ent[k], k);
    int* p[4];
#pragma unroll
    for (int r = 0; r < 4; ++r) p[r] = slot(ent[k][r]);
    SB();
#pragma unroll
    for (int r = 0; r < 4; ++r) atomicAdd(p[r], 2);
    SB();
  }
  unsigned use = 0;
  const unsigned dum3 = (tid & 3) == 3 ? dum : 0;
#pragma unroll
  for (int k = 0; k < 4; ++k) use |= __ballot(ent[NL - 4 + k][3] < dum3) ? 1u << k : 0u;
  if (use)
#pragma unroll 1
    for (int k = NL; k < 64; ++k)
      if (use >> (k - NL) & 1) {
        u32x4 e = fetch(k); clean(e, k);
#pragma unroll
        for (int r = 0; r < 4; ++r) atomicAdd(slot(e[r]), 2);
        if (__ballot(e[3] < dum3)) { use |= 16u << (k - NL); if (k >= 60) red[7] = 1; }
      }
  __syncthreads();
  int inc = 0, nu[10], cn[10], cm = 0;
  auto units = [&](int c) { return max(1, (c + 6) >> 3); };
#pragma unroll
  for (int i = 0; i < 10; i += 2) { const u32x2 v = *reinterpret_cast<u32x2*>(cnt + 10 * tid + i); cn[i] = v[0]; cn[i + 1] = v[1]; }
  const int nup = units(cnt[max(10 * tid - 1, 0)]);
  const bool act = 10 * tid < nn_;
#pragma unroll
  for (int i = 0; i < 10; ++i) { cm = max(cm, cn[i]); nu[i] = act ? units(cn[i]) : 0; inc += nu[i]; }
  const int own = inc;
  inc += __builtin_amdgcn_update_dpp(0, inc, 0x111, 15, 15, true); inc += __builtin_amdgcn_update_dpp(0, inc, 0x112, 15, 15, true);
  inc += __builtin_amdgcn_update_dpp(0, inc, 0x114, 15, 15, true); inc += __builtin_amdgcn_update_dpp(0, inc, 0x118, 15, 15, true);
  inc += __builtin_amdgcn_update_dpp(0, inc, 0x142, 10, 15, false); inc += __builtin_amdgcn_update_dpp(0, inc, 0x143, 12, 15, false);
  if (lane == 63) red[wid] = inc;
  const bool srt = __ballot(cm > 64);
  __syncthreads();
  const u32x4 rd = *reinterpret_cast<u32x4*>(red);
  const float rs_ = 64.f / (float)(rd[0] + rd[1] + rd[2] + rd[3]);
  inc += (wid > 0 ? rd[0] : 0) + (wid > 1 ? rd[1] : 0) + (wid > 2 ? rd[2] : 0) - own;
  int wv_[10];
  {
    int o = inc, wp = tid ? min(127, (int)((float)(2 * o - nup) * rs_)) : 128;
#pragma unroll
    for (int i = 0; i < 10; ++i) {
      wv_[i] = min(127, (int)((float)(2 * o + nu[i]) * rs_));
      ub[act && wv_[i] != wp ? wv_[i] : 128 + lane] = (unsigned)o << 12 | (unsigned)(10 * tid + i);
      wp = wv_[i]; o += nu[i];
    }
  }
  __syncthreads();
  {
    int u0[10], top = 0;
#pragma unroll
    for (int i = 0; i < 10; ++i) u0[i] = ub[wv_[i]] >> 12;
#pragma unroll
    for (int i = 0; i < 10; ++i) {
      const int lu = inc - u0[i], e = lu + nu[i] - 1 + GU, q = min(e / (4 * GU), GNB);
      u0[i] = wv_[i] * 1024 + lu * 8; top = max(top, q);
      atomicOr(img + wv_[i] * 256 + 244 + q, act ? 1u << (e - q * 4 * GU) : 0u);
      inc += nu[i];
    }
#pragma unroll
    for (int i = 0; i < 10; i += 2) *reinterpret_cast<u32x2*>(cnt + 10 * tid + i) = (u32x2){(unsigned)u0[i], (unsigned)u0[i + 1]};
    if (tid < 64) cnt[SPN + tid] = 0x30000;
    if (act && top == GNB) red[7] = 1;
  }
  __syncthreads();
  const bool big_ = red[7];
  if (tid < 128) {
    unsigned nb = 0;
#pragma unroll
    for (int q = 0; q < GNB; ++q) nb = img[tid * 256 + 244 + q] ? q + 1 : nb;
    *reinterpret_cast<u32x4*>(img + tid * 256 + 240) = (u32x4){big_ ? 0u : nb, WS_G + (lo + (ub[tid] & 4095u)) * 512, big_ || (tid & 3) ? 0x40000000u : WS_GF + j * 512 + tid * 4, 0u};
  }
  if (!big_) {
    const unsigned dmp = 0x27000 + lane * 4;
    typedef __attribute__((address_space(3))) u16* lds_u16_p;
    unsigned p[2][8];
#pragma unroll
    for (int k = 0; k < NL + 2; k += 2) {
      if (k < NL) {
        int* q[8];
#pragma unroll
        for (int r = 0; r < 8; ++r) q[r] = slot(ent[k + (r >> 2)][r & 3]);
        SB();
#pragma unroll
        for (int r = 0; r < 8; ++r) p[k >> 1 & 1][r] = atomicAdd(q[r], 2);
        SB();
      }
      if (k) {
#pragma unroll
        for (int r = 0; r < 8; ++r) p[~k >> 1 & 1][r] = min(p[~k >> 1 & 1][r], dmp);
        SB();
#pragma unroll
        for (int r = 0; r < 8; ++r) *(lds_u16_p)p[~k >> 1 & 1][r] = ent[k - 2 + (r >> 2)][r & 3];
        SB();
      }
    }
    if (use)
#pragma unroll 1
      for (int k = NL; k < 64; ++k)
        if (use >> (k - NL) & 1) {
          u32x4 e = fetch(k); clean(e, k);
#pragma unroll
          for (int r = 0; r < 4; ++r) *(lds_u16_p)min((unsigned)atomicAdd(slot(e[r]), 2), dmp) = e[r];
        }
    __syncthreads();
    if (srt) {
#pragma unroll
      for (int i = 0; i < 10; ++i) dg[10 * tid + i] = cn[i] >> 1;
      for (unsigned long long big = __ballot(cm > 64); big; big &= big - 1)
      for (int n = 10 * (wid * 64 + __builtin_ctzll(big)), n1 = n + 10; n < n1; ++n) {
        const int d = __builtin_amdgcn_readfirstlane(dg[n]);
        if (d <= 32) continue;
        u16* f = reinterpret_cast<u16*>(lds + cnt[n]) - d;
        for (int i = lane; i < d + 8; i += 64) tmp[i] = i < d ? f[i] : 0xffff;
        __builtin_amdgcn_wave_barrier();
        for (int i = lane; i < d; i += 64) {
          const unsigned key = tmp[i] << 9 | i; int p_ = 0;
          for (int q = 0; q < d; q += 8) {
            const u32x4 v = *reinterpret_cast<u32x4*>(tmp + q);
#pragma unroll
            for (int e = 0; e < 4; ++e) p_ += (((v[e] & 0xffffu) << 9 | (q + 2 * e)) < key) + (((v[e] >> 16) << 9 | (q + 2 * e + 1)) < key);
          }
          f[p_] = key >> 9;
        }
        __builtin_amdgcn_wave_barrier();
      }
    }
  }
  __syncthreads();
#pragma unroll 8
  for (int k = 0; k < 32; ++k)
    __builtin_amdgcn_raw_buffer_store_b128(reinterpret_cast<u32x4*>(img)[k * 256 + tid], wsrd, WS_LS + j * 131072 + (k * 256 + tid) * 16, 0, 16);
  unsigned lr[16];
#pragma unroll
  for (int r = 0; r < 16; ++r) lr[r] = reinterpret_cast<const unsigned*>((wid >= 2 ? wm : wv) + (240 + r) * DD)[tid & 127];
  wait_flag(wsrd, WS_HF + min(tid, NATT - 1) * 16, tag);
  drain();
  __syncthreads();
  if (tid == 0) st_flag(wsrd, WS_LF + j * 128, tag);
  asm volatile("s_mov_b32 m0, 0x27c00\n s_nop 0\n global_load_lds_dword %0, %1" :: "v"(min(tid * 128, NN * 2 - 4)), "s"(dinv) : "m0", "memory");
  touch(WS_XF + (j * 256 + tid) * 256);
  touch(wid ? 0x40000000u : WS_GF - 32768 + (j * 64 + lane) * 128);
  corr(reinterpret_cast<unsigned*>(lds + 0x20000) + 512 + (wid >> 1) * 256 + (tid & 127), lr);
#pragma unroll 1
  for (int job = 4 * j; job < 4 * j + 4; ++job) {
    product_load(lds, wid, lane, job, wv, wm, wo);
    drain();
    __syncthreads();
    product_w(lds, wid, lane, job, wsrd);
    __syncthreads();
  }
  drain();
  __syncthreads();
  if (tid == 0) st_flag(wsrd, WS_WF + j * 4, tag);
  if (big_) {
    const srd_t msrd = __builtin_amdgcn_make_buffer_rsrc(ws + WS_H, 0, 10240 * 512, 0x00020000);
    bf16x8 b1;
#pragma unroll
    for (int e = 0; e < 8; ++e) b1[e] = (lane & 15) == e + ((lane >> 1) & 8) ? 0x3f80 : 0;
    for (int n = wid; n < nn_; n += 4) {
      f32x4 acc = {};
      for (int e0 = 0; e0 < EE; e0 += 64) {
        unsigned long long m = __ballot(dst[e0 + lane] == lo + n);
        while (m) {
          const int e = e0 + __builtin_ctzll(m); m &= m - 1;
          const unsigned vo = lane < 32 ? (unsigned)src[e] * 512 + lane * 16 : 0x7fffff00u;
          acc = MFMA16(b1, as_bf(__builtin_amdgcn_raw_buffer_load_b128(msrd, vo, 0, 16)), acc);
        }
      }
      __builtin_amdgcn_raw_buffer_store_b64((u32x2){pk2(acc[0], acc[1]), pk2(acc[2], acc[3])}, wsrd,
                                            WS_G + (lo + n) * 512 + (lane >> 5) * 256 + (lane & 15) * 16 + (lane & 16) / 2, 0, 16);
    }
    drain();
    __syncthreads();
    if (tid < 32) st_flag(wsrd, WS_GF + j * 512 + tid * 16, tag);
  }
}

extern "C" __global__ __launch_bounds__(256, 1) void gps_kernel(const u16* wq, const u16* wk, char* ws, const int* dst, const u16* x, const int* src, u16* out,
                                                                const u16* deg_inv, const u16* wv, const u16* wm, const u16* wo) {
  extern __shared__ __attribute__((aligned(16))) char lds[];
  static_assert(WS_END == 69410816 && WS_EP == 6101760, "workspace layout of the generated code");
  typedef void* const __attribute__((address_space(4)))* arg_p;
  arg_p ka; int b, wid;
  asm volatile(
    "s_load_dwordx16 s[4:19], s[0:1], 0x0\n"
    "s_mov_b32 s92, s2\n"
    "s_getpc_b64 s[30:31]\n"
    "v_readfirstlane_b32 s93, v0\n"
    "s_bfe_u32 s45, s92, 0x40003\n"
    "v_lshlrev_b32_e32 v1, 5, v0\n"
    "s_lshl_b32 s45, s45, 13\n"
    "s_mov_b32 m0, 0x27f00\n"
    "v_add_u32_e32 v1, s45, v1\n"
    "v_min_u32_e32 v1, 76488, v1\n"
    "global_load_lds_dword v1, s[30:31]\n"
    "s_and_b32 s45, s92, 249\n"
    "s_lshr_b32 s93, s93, 6\n"
    "s_cmp_eq_u32 s45, 249\n"
    "s_cbranch_scc1 .Lat_out\n"
    "v_mbcnt_lo_u32_b32 v24, -1, 0\n"
    "v_mbcnt_hi_u32_b32 v24, -1, v24\n"
    "s_and_b32 s50, s93, 1\n"
    "s_lshr_b32 s51, s93, 1\n"
    "v_lshrrev_b32_e32 v25, 3, v24\n"
    "v_and_b32_e32 v26, 6, v25\n"
    "v_and_b32_e32 v27, 7, v24\n"
    "v_xor_b32_e32 v26, v26, v27\n"
    "v_lshlrev_b32_e32 v26, 4, v26\n"
    "v_and_b32_e32 v27, 4, v25\n"
    "v_add_u32_e32 v27, v27, v25\n"
    "v_lshl_add_u32 v251, v27, 9, v26\n"
    "v_add_u32_e32 v252, 0x1c00, v251\n"
    "s_mov_b32 s22, 0x20000\n"
    "s_mov_b32 s23, 0x20000\n"
    "s_mov_b32 s26, 0x20000\n"
    "s_mov_b32 s27, 0x20000\n"
    "s_lshr_b32 s36, s92, 7\n"
    "s_mul_i32 s36, s36, 3\n"
    "s_add_u32 s36, s36, s92\n"
    "s_and_b32 s36, s36, 127\n"
    "s_lshl_b32 s45, s93, 7\n"
    "s_lshl_b32 s46, s93, 12\n"
    "s_and_b32 s47, s36, 7\n"
    "s_lshl_b32 s47, s47, 14\n"
    "s_mov_b32 m0, s46\n"
    "s_add_u32 s47, s47, s45\n"
    "s_and_b32 s34, s92, 7\n"
    "s_lshl_b32 s34, s34, 5\n"
    "s_lshr_b32 s32, s92, 3\n"
    "s_add_u32 s34, s34, s32\n"
    "s_lshl_b32 s32, s34, 6\n"
    "s_or_b32 s32, s32, 63\n"
    "s_and_b32 s32, s32, 255\n"
    "s_cmp_ge_u32 s34, 252\n"
    "s_cselect_b32 s34, s32, s34\n"
    "s_and_b32 s32, s34, 3\n"
    "s_lshr_b32 s33, s34, 2\n"
    "s_or_b32 s38, s33, 16\n"
    "s_cmp_eq_u32 s38, 62\n"
    "s_cselect_b32 s38, 16, 0\n"
    "s_xor_b32 s33, s33, s38\n"
    "s_lshl2_add_u32 s34, s33, s32\n"
    "s_mul_i32 s38, s33, 81920\n"
    "s_lshl_b32 s35, s32, 13\n"
    "s_and_b32 s55, s35, 0x2000\n"
    "s_add_u32 s35, s35, s38\n"
    "s_add_u32 s55, s55, s38\n"
    "s_mov_b32 s39, 6101760\n"
    "s_waitcnt lgkmcnt(0)\n"
    "s_mov_b64 s[20:21], s[4:5]\n"
    "s_mov_b64 s[24:25], s[6:7]\n"
    "buffer_load_dwordx4 v251, s[20:23], s47 offen offset:0 lds\n"
    "s_add_u32 s48, s47, 1024\n"
    "buffer_load_dwordx4 v252, s[20:23], s47 offen offset:3072 lds\n"
    "s_add_u32 s47, s47, 0x1800\n"
    "buffer_load_dwordx4 v251, s[20:23], s48 offen offset:1024 lds\n"
    "buffer_load_dwordx4 v251, s[20:23], s47 offen offset:2048 lds\n"
    "s_lshr_b32 s47, s36, 4\n"
    "s_lshl_b32 s47, s47, 14\n"
    "s_and_b32 s48, s36, 8\n"
    "s_lshl_b32 s48, s48, 8\n"
    "s_add_u32 s47, s47, s48\n"
    "s_add_u32 m0, s46, 0x4000\n"
    "s_add_u32 s47, s47, s45\n"
    "buffer_load_dwordx4 v251, s[24:27], s47 offen offset:0 lds\n"
    "buffer_load_dwordx4 v252, s[24:27], s47 offen offset:1024 lds\n"
    "s_add_u32 s47, s45, 120832\n"
    "buffer_load_dwordx4 v251, s[24:27], s47 offen offset:2048 lds\n"
    "s_add_u32 s47, s47, 1024\n"
    "buffer_load_dwordx4 v251, s[24:27], s47 offen offset:3072 lds\n"
    "s_mov_b64 s[6:7], s[14:15]\n"
    "s_mov_b64 s[86:87], s[10:11]\n"
    "s_mov_b32 s14, 5120000\n"
    "s_mov_b32 s15, 0x20000\n"
    "v_mov_b32_e32 v102, s39\n"
    "global_load_dword v102, v102, s[8:9]\n"
    "s_lshl_b32 s45, s93, 2\n"
    "s_add_u32 s54, s35, 0x8000\n"
    "s_add_u32 s55, s55, 0x10000\n"
    "v_lshlrev_b32_e32 v250, 4, v24\n"
    "v_lshrrev_b32_e32 v28, 4, v24\n"
    "v_add_u32_e32 v28, s45, v28\n"
    "v_and_b32_e32 v104, 0xf0, v250\n"
    "v_lshl_add_u32 v104, v28, 9, v104\n"
    "buffer_load_dwordx4 v[0:3], v104, s[12:15], s35 offen offset:0\n"
    "buffer_load_dwordx4 v[4:7], v104, s[12:15], s35 offen offset:256\n"
    "s_cmp_lt_u32 s32, 2\n"
    "s_cselect_b32 s60, s55, 2.0\n"
    "buffer_load_dwordx4 v[8:11], v104, s[12:15], s54 offen offset:0\n"
    "buffer_load_dwordx4 v[12:15], v104, s[12:15], s54 offen offset:256\n"
    "buffer_load_dwordx4 v[16:19], v104, s[12:15], s55 offen offset:0\n"
    "buffer_load_dwordx4 v[20:23], v104, s[12:15], s55 offen offset:256\n"
    "s_min_u32 s46, s34, 249\n"
    "s_mul_i32 s46, s46, 2560\n"
    "s_lshl_b32 s47, s50, 10\n"
    "s_add_u32 s47, s47, s46\n"
    "v_add_u32_e32 v158, s47, v250\n"
    "s_lshl_b32 s47, s50, 8\n"
    "s_add_u32 s47, s47, s46\n"
    "s_add_u32 s47, s47, 2048\n"
    "v_lshl_add_u32 v159, v24, 2, s47\n"
    "s_cmp_lg_u32 s51, 0\n"
    "s_cselect_b64 exec, -1, 1\n"
    "global_load_dwordx4 v[144:147], v158, s[86:87]\n"
    "global_load_dword v148, v159, s[86:87]\n"
    "global_load_dwordx4 v[152:155], v158, s[6:7]\n"
    "global_load_dword v156, v159, s[6:7]\n"
    "s_mov_b64 exec, -1\n"
    "s_mov_b32 s10, 69410816\n"
    "s_mov_b32 s11, 0x20000\n"
    "s_mov_b32 s49, 0\n"
    "v_and_b32_e32 v25, 7, v24\n"
    "v_lshlrev_b32_e32 v27, 7, v25\n"
    "v_bfe_u32 v26, v24, 3, 1\n"
    "v_lshl_add_u32 v27, v26, 10, v27\n"
    "v_and_b32_e32 v25, 6, v24\n"
    "v_lshrrev_b32_e32 v26, 4, v24\n"
    "v_xor_b32_e32 v25, v25, v26\n"
    "v_lshl_add_u32 v240, v25, 4, v27\n"
    "s_lshl_b32 s48, s50, 11\n"
    "v_xor_b32_e32 v241, 64, v240\n"
    "v_add_u32_e32 v72, s48, v240\n"
    "v_add_u32_e32 v73, s48, v241\n"
    "s_lshl_b32 s47, s93, 10\n"
    "v_add_u32_e32 v108, s47, v250\n"
    "v_lshrrev_b32_e32 v109, 7, v108\n"
    "v_mul_u32_u24_e32 v109, 0x8200, v109\n"
    "v_and_b32_e32 v32, 0x70, v250\n"
    "v_or_b32_e32 v109, v109, v32\n"
    "v_add_u32_e32 v108, 81920, v108\n"
    "v_mov_b64_e32 v[32:33], -1\n"
    "v_mov_b64_e32 v[34:35], -1\n"
    "ds_write_b128 v108, v[32:35] offset:0\n"
    "ds_write_b128 v108, v[32:35] offset:16\n"
    "v_and_b32_e32 v29, 6, v28\n"
    "v_xor_b32_e32 v29, v29, v24\n"
    "v_and_b32_e32 v29, 7, v29\n"
    "v_lshlrev_b32_e32 v105, 7, v28\n"
    "v_lshl_add_u32 v105, v29, 4, v105\n"
    "v_bfe_u32 v29, v24, 3, 1\n"
    "v_subrev_u32_e32 v29, s32, v29\n"
    "v_and_b32_e32 v29, 3, v29\n"
    "v_lshl_add_u32 v105, v29, 11, v105\n"
    "v_add_u32_e32 v105, 32768, v105\n"
    "v_xor_b32_e32 v106, 0x1000, v105\n"
    "v_and_b32_e32 v29, 15, v24\n"
    "v_cmp_eq_u32_e32 vcc, 15, v29\n"
    "v_mov_b32_e32 v107, 0xffff\n"
    "s_mov_b32 s58, 0x3f803f80\n"
    "s_mov_b32 s57, 0x43c00000\n"
    "v_cndmask_b32_e32 v107, -1, v107, vcc\n"
    "s_mov_b32 s56, 0xc3c00000\n"
    "s_mov_b32 s62, 0xc000c000\n"
    "s_mov_b32 s63, 0xc000c000\n"
    "s_lshl_b32 s47, s93, 10\n"
    "s_add_u32 s47, s47, 6080512\n"
    "v_add_u32_e32 v100, s47, v250\n"
    "v_and_b32_e32 v29, 15, v24\n"
    "s_cmp_ge_u32 s36, 112\n"
    "s_cselect_b32 s47, 8, 16\n"
    "v_cmp_le_u32_e32 vcc, s47, v29\n"
    "v_mov_b32_e32 v64, 0xbd80bd80\n"
    "v_mov_b32_e32 v65, 0xbdabbdab\n"
    "v_mov_b32_e32 v66, 0x392b392b\n"
    "v_cndmask_b32_e32 v64, v64, v65, vcc\n"
    "v_cndmask_b32_e32 v66, 0, v66, vcc\n"
    "s_cselect_b32 s47, 56, 64\n"
    "v_cmp_le_u32_e32 vcc, s47, v24\n"
    "v_mov_b32_e32 v65, 0x3e80bdab\n"
    "v_mov_b32_e32 v67, 0x392b\n"
    "v_cndmask_b32_e32 v65, v64, v65, vcc\n"
    "v_cndmask_b32_e32 v67, v66, v67, vcc\n"
    "s_cmp_ge_u32 s36, 120\n"
    "s_cselect_b32 s47, 15, 16\n"
    "v_cmp_ne_u32_e32 vcc, s47, v29\n"
    "v_mov_b32_e32 v68, 0x3db8aa3b\n"
    "s_lshr_b32 s47, s36, 5\n"
    "v_cndmask_b32_e32 v68, 0, v68, vcc\n"
    "s_waitcnt vmcnt(11)\n"
    "s_waitcnt lgkmcnt(0)\n"
    "s_barrier\n"
    "s_cmp_lg_u32 s51, 0\n"
    "s_cbranch_scc1 .Lat_noj2\n"
    "ds_read_b128 v[112:115], v72 offset:0\n"
    "ds_read_b128 v[208:211], v240 offset:18432\n"
    "ds_read_b128 v[176:179], v240 offset:16384\n"
    "ds_read_b128 v[116:119], v73 offset:0\n"
    "ds_read_b128 v[212:215], v241 offset:18432\n"
    "ds_read_b128 v[180:183], v241 offset:16384\n"
    "ds_read_b128 v[120:123], v72 offset:4096\n"
    "ds_read_b128 v[216:219], v240 offset:22528\n"
    "ds_read_b128 v[184:187], v240 offset:20480\n"
    "ds_read_b128 v[124:127], v73 offset:4096\n"
    "ds_read_b128 v[220:223], v241 offset:22528\n"
    "ds_read_b128 v[188:191], v241 offset:20480\n"
    "ds_read_b128 v[128:131], v72 offset:8192\n"
    "ds_read_b128 v[224:227], v240 offset:26624\n"
    "ds_read_b128 v[192:195], v240 offset:24576\n"
    "s_waitcnt lgkmcnt(12)\n"
    "v_mfma_f32_16x16x32_bf16 v[56:59], v[208:211], v[112:115], 0\n"
    "ds_read_b128 v[132:135], v73 offset:8192\n"
    "ds_read_b128 v[228:231], v241 offset:26624\n"
    "v_mfma_f32_16x16x32_bf16 v[48:51], v[112:115], v[176:179], 0\n"
    "ds_read_b128 v[196:199], v241 offset:24576\n"
    "s_waitcnt lgkmcnt(12)\n"
    "v_mfma_f32_16x16x32_bf16 v[56:59], v[212:215], v[116:119], v[56:59]\n"
    "ds_read_b128 v[136:139], v72 offset:12288\n"
    "ds_read_b128 v[232:235], v240 offset:30720\n"
    "v_mfma_f32_16x16x32_bf16 v[48:51], v[116:119], v[180:183], v[48:51]\n"
    "ds_read_b128 v[200:203], v240 offset:28672\n"
    "s_waitcnt lgkmcnt(12)\n"
    "v_mfma_f32_16x16x32_bf16 v[56:59], v[216:219], v[120:123], v[56:59]\n"
    "ds_read_b128 v[140:143], v73 offset:12288\n"
    "ds_read_b128 v[236:239], v241 offset:30720\n"
    "v_mfma_f32_16x16x32_bf16 v[48:51], v[120:123], v[184:187], v[48:51]\n"
    "ds_read_b128 v[204:207], v241 offset:28672\n"
    "s_waitcnt lgkmcnt(12)\n"
    "v_mfma_f32_16x16x32_bf16 v[56:59], v[220:223], v[124:127], v[56:59]\n"
    "s_mul_i32 s47, s47, 33792\n"
    "s_and_b32 s48, s36, 7\n"
    "s_lshl_b32 s48, s48, 11\n"
    "v_mfma_f32_16x16x32_bf16 v[48:51], v[124:127], v[188:191], v[48:51]\n"
    "s_add_u32 s47, s47, s48\n"
    "s_and_b32 s48, s36, 8\n"
    "s_lshl_b32 s48, s48, 7\n"
    "s_waitcnt lgkmcnt(9)\n"
    "v_mfma_f32_16x16x32_bf16 v[56:59], v[224:227], v[128:131], v[56:59]\n"
    "s_add_u32 s47, s47, s48\n"
    "s_and_b32 s48, s36, 16\n"
    "s_lshl_b32 s48, s48, 10\n"
    "v_mfma_f32_16x16x32_bf16 v[48:51], v[128:131], v[192:195], v[48:51]\n"
    "s_add_u32 s47, s47, s48\n"
    "s_add_u32 s52, s47, 5242880\n"
    "s_lshl_b32 s48, s50, 9\n"
    "s_waitcnt lgkmcnt(6)\n"
    "v_mfma_f32_16x16x32_bf16 v[56:59], v[228:231], v[132:135], v[56:59]\n"
    "s_add_u32 s52, s52, s48\n"
    "v_and_b32_e32 v74, 0x1f0, v250\n"
    "v_lshrrev_b32_e32 v29, 5, v24\n"
    "v_mfma_f32_16x16x32_bf16 v[48:51], v[132:135], v[196:199], v[48:51]\n"
    "v_lshl_add_u32 v74, v29, 3, v74\n"
    "s_lshl1_add_u32 s53, s36, s50\n"
    "s_lshl4_add_u32 s53, s53, 6080512\n"
    "s_waitcnt lgkmcnt(3)\n"
    "v_mfma_f32_16x16x32_bf16 v[56:59], v[232:235], v[136:139], v[56:59]\n"
    "s_cmp_lg_u32 s51, 0\n"
    "v_mfma_f32_16x16x32_bf16 v[48:51], v[136:139], v[200:203], v[48:51]\n"
    "s_waitcnt lgkmcnt(0)\n"
    "v_mfma_f32_16x16x32_bf16 v[56:59], v[236:239], v[140:143], v[56:59]\n"
    "v_mfma_f32_16x16x32_bf16 v[48:51], v[140:143], v[204:207], v[48:51]\n"
    "s_nop 3\n"
    "v_cvt_pk_bf16_f32 v56, v56, v57\n"
    "v_cvt_pk_bf16_f32 v57, v58, v59\n"
    "s_nop 1\n"
    "v_mfma_f32_16x16x16_bf16 v[48:51], v[56:57], v[64:65], v[48:51]\n"
    "v_mfma_f32_16x16x16_bf16 v[48:51], v[56:57], v[66:67], v[48:51]\n"
    "s_nop 7\n"
    "v_mul_f32_e32 v48, v48, v68\n"
    "v_mul_f32_e32 v49, v49, v68\n"
    "v_mul_f32_e32 v50, v50, v68\n"
    "v_mul_f32_e32 v51, v51, v68\n"
    "v_cvt_pk_bf16_f32 v48, v48, v49\n"
    "v_cvt_pk_bf16_f32 v49, v50, v51\n"
    "buffer_store_dwordx2 v[48:49], v74, s[8:11], s52 offen offset:0 sc1\n"
    "s_waitcnt vmcnt(5)\n"
    "s_branch .Lat_ln\n"
    ".Lat_noj2:\n"
    "s_cmp_lg_u32 s93, 3\n"
    "s_cbranch_scc1 .Lat_wn10\n"
    "s_call_b64 s[2:3], .Lat_w0\n"
    "s_call_b64 s[2:3], .Lat_w2\n"
    "s_call_b64 s[2:3], .Lat_w4\n"
    ".Lat_wn10:\n"
    "s_cmp_lg_u32 s93, 2\n"
    "s_cbranch_scc1 .Lat_wn11\n"
    "s_call_b64 s[2:3], .Lat_w1\n"
    "s_call_b64 s[2:3], .Lat_w3\n"
    "s_call_b64 s[2:3], .Lat_w5\n"
    ".Lat_wn11:\n"
    "s_cmp_lg_u32 s51, 0\n"
    "s_waitcnt vmcnt(4)\n"
    ".Lat_ln:\n"
    "v_add_u32_e32 v102, 4, v102\n"
    "s_nop 1\n"
    "v_readfirstlane_b32 s39, v102\n"
    "v_dot2_f32_bf16 v24, v0, s58, 0\n"
    "v_dot2_f32_bf16 v32, v8, s58, 0\n"
    "v_dot2_f32_bf16 v40, v16, s58, 0\n"
    "v_dot2_f32_bf16 v25, v4, s58, 0\n"
    "v_dot2_f32_bf16 v33, v12, s58, 0\n"
    "v_dot2_f32_bf16 v41, v20, s58, 0\n"
    "v_dot2_f32_bf16 v26, v0, v0, 0\n"
    "v_dot2_f32_bf16 v34, v8, v8, 0\n"
    "v_dot2_f32_bf16 v42, v16, v16, 0\n"
    "v_dot2_f32_bf16 v27, v4, v4, 0\n"
    "v_dot2_f32_bf16 v35, v12, v12, 0\n"
    "v_dot2_f32_bf16 v43, v20, v20, 0\n"
    "v_dot2_f32_bf16 v24, v1, s58, v24\n"
    "v_dot2_f32_bf16 v32, v9, s58, v32\n"
    "v_dot2_f32_bf16 v40, v17, s58, v40\n"
    "v_dot2_f32_bf16 v25, v5, s58, v25\n"
    "v_dot2_f32_bf16 v33, v13, s58, v33\n"
    "v_dot2_f32_bf16 v41, v21, s58, v41\n"
    "v_dot2_f32_bf16 v26, v1, v1, v26\n"
    "v_dot2_f32_bf16 v34, v9, v9, v34\n"
    "v_dot2_f32_bf16 v42, v17, v17, v42\n"
    "v_dot2_f32_bf16 v27, v5, v5, v27\n"
    "v_dot2_f32_bf16 v35, v13, v13, v35\n"
    "v_dot2_f32_bf16 v43, v21, v21, v43\n"
    "v_dot2_f32_bf16 v24, v2, s58, v24\n"
    "v_dot2_f32_bf16 v32, v10, s58, v32\n"
    "v_dot2_f32_bf16 v40, v18, s58, v40\n"
    "v_dot2_f32_bf16 v25, v6, s58, v25\n"
    "v_dot2_f32_bf16 v33, v14, s58, v33\n"
    "v_dot2_f32_bf16 v41, v22, s58, v41\n"
    "v_dot2_f32_bf16 v26, v2, v2, v26\n"
    "v_dot2_f32_bf16 v34, v10, v10, v34\n"
    "v_dot2_f32_bf16 v42, v18, v18, v42\n"
    "v_dot2_f32_bf16 v27, v6, v6, v27\n"
    "v_dot2_f32_bf16 v35, v14, v14, v35\n"
    "v_dot2_f32_bf16 v43, v22, v22, v43\n"
    "v_dot2_f32_bf16 v24, v3, s58, v24\n"
    "v_dot2_f32_bf16 v32, v11, s58, v32\n"
    "v_dot2_f32_bf16 v40, v19, s58, v40\n"
    "v_dot2_f32_bf16 v25, v7, s58, v25\n"
    "v_dot2_f32_bf16 v33, v15, s58, v33\n"
    "v_dot2_f32_bf16 v41, v23, s58, v41\n"
    "v_dot2_f32_bf16 v26, v3, v3, v26\n"
    "v_dot2_f32_bf16 v34, v11, v11, v34\n"
    "v_dot2_f32_bf16 v42, v19, v19, v42\n"
    "v_dot2_f32_bf16 v27, v7, v7, v27\n"
    "v_dot2_f32_bf16 v35, v15, v15, v35\n"
    "v_dot2_f32_bf16 v43, v23, v23, v43\n"
    "v_and_b32_e32 v30, 0xffff0000, v7\n"
    "v_and_b32_e32 v38, 0xffff0000, v15\n"
    "v_and_b32_e32 v46, 0xffff0000, v23\n"
    "v_add_f32_e32 v24, v24, v25\n"
    "v_add_f32_e32 v32, v32, v33\n"
    "v_add_f32_e32 v40, v40, v41\n"
    "v_add_f32_e32 v26, v26, v27\n"
    "v_add_f32_e32 v34, v34, v35\n"
    "v_add_f32_e32 v42, v42, v43\n"
    "s_cbranch_scc1 .Lat_nojf\n"
    "s_waitcnt vmcnt(0)\n"
    "buffer_store_dword v102, off, s[8:11], s53 offset:0 sc1\n"
    ".Lat_nojf:\n"
    "v_add_f32_dpp v25, v25, v25 quad_perm:[1,0,3,2] row_mask:0xf bank_mask:0xf bound_ctrl:1\n"
    "v_lshlrev_b32_e32 v48, 16, v0\n"
    "v_add_f32_dpp v33, v33, v33 quad_perm:[1,0,3,2] row_mask:0xf bank_mask:0xf bound_ctrl:1\n"
    "v_and_b32_e32 v49, 0xffff0000, v0\n"
    "v_add_f32_dpp v41, v41, v41 quad_perm:[1,0,3,2] row_mask:0xf bank_mask:0xf bound_ctrl:1\n"
    "v_lshlrev_b32_e32 v50, 16, v1\n"
    "v_mov_b32_dpp v30, v30 quad_perm:[3,3,3,3] row_mask:0xf bank_mask:0xf bound_ctrl:1\n"
    "v_and_b32_e32 v51, 0xffff0000, v1\n"
    "v_mov_b32_dpp v38, v38 quad_perm:[3,3,3,3] row_mask:0xf bank_mask:0xf bound_ctrl:1\n"
    "v_lshlrev_b32_e32 v52, 16, v2\n"
    "v_mov_b32_dpp v46, v46 quad_perm:[3,3,3,3] row_mask:0xf bank_mask:0xf bound_ctrl:1\n"
    "v_and_b32_e32 v53, 0xffff0000, v2\n"
    "v_add_f32_dpp v24, v24, v24 quad_perm:[1,0,3,2] row_mask:0xf bank_mask:0xf bound_ctrl:1\n"
    "v_lshlrev_b32_e32 v54, 16, v3\n"
    "v_add_f32_dpp v32, v32, v32 quad_perm:[1,0,3,2] row_mask:0xf bank_mask:0xf bound_ctrl:1\n"
    "v_and_b32_e32 v55, 0xffff0000, v3\n"
    "v_add_f32_dpp v40, v40, v40 quad_perm:[1,0,3,2] row_mask:0xf bank_mask:0xf bound_ctrl:1\n"
    "v_lshlrev_b32_e32 v56, 16, v4\n"
    "v_add_f32_dpp v26, v26, v26 quad_perm:[1,0,3,2] row_mask:0xf bank_mask:0xf bound_ctrl:1\n"
    "v_and_b32_e32 v57, 0xffff0000, v4\n"
    "v_add_f32_dpp v34, v34, v34 quad_perm:[1,0,3,2] row_mask:0xf bank_mask:0xf bound_ctrl:1\n"
    "v_lshlrev_b32_e32 v58, 16, v5\n"
    "v_add_f32_dpp v42, v42, v42 quad_perm:[1,0,3,2] row_mask:0xf bank_mask:0xf bound_ctrl:1\n"
    "v_and_b32_e32 v59, 0xffff0000, v5\n"
    "v_add_f32_dpp v24, v24, v24 quad_perm:[2,3,0,1] row_mask:0xf bank_mask:0xf bound_ctrl:1\n"
    "v_lshlrev_b32_e32 v60, 16, v6\n"
    "v_add_f32_dpp v32, v32, v32 quad_perm:[2,3,0,1] row_mask:0xf bank_mask:0xf bound_ctrl:1\n"
    "v_and_b32_e32 v61, 0xffff0000, v6\n"
    "v_add_f32_dpp v40, v40, v40 quad_perm:[2,3,0,1] row_mask:0xf bank_mask:0xf bound_ctrl:1\n"
    "v_lshlrev_b32_e32 v62, 16, v7\n"
    "v_add_f32_dpp v26, v26, v26 quad_perm:[2,3,0,1] row_mask:0xf bank_mask:0xf bound_ctrl:1\n"
    "v_and_b32_e32 v63, 0xffff0000, v7\n"
    "v_add_f32_dpp v34, v34, v34 quad_perm:[2,3,0,1] row_mask:0xf bank_mask:0xf bound_ctrl:1\n"
    "v_lshlrev_b32_e32 v64, 16, v8\n"
    "v_add_f32_dpp v42, v42, v42 quad_perm:[2,3,0,1] row_mask:0xf bank_mask:0xf bound_ctrl:1\n"
    "v_and_b32_e32 v65, 0xffff0000, v8\n"
    "v_add_f32_dpp v24, v24, v24 row_half_mirror row_mask:0xf bank_mask:0xf bound_ctrl:1\n"
    "v_lshlrev_b32_e32 v66, 16, v9\n"
    "v_add_f32_dpp v32, v32, v32 row_half_mirror row_mask:0xf bank_mask:0xf bound_ctrl:1\n"
    "v_and_b32_e32 v67, 0xffff0000, v9\n"
    "v_add_f32_dpp v40, v40, v40 row_half_mirror row_mask:0xf bank_mask:0xf bound_ctrl:1\n"
    "v_lshlrev_b32_e32 v68, 16, v10\n"
    "v_add_f32_dpp v26, v26, v26 row_half_mirror row_mask:0xf bank_mask:0xf bound_ctrl:1\n"
    "v_and_b32_e32 v69, 0xffff0000, v10\n"
    "v_add_f32_dpp v34, v34, v34 row_half_mirror row_mask:0xf bank_mask:0xf bound_ctrl:1\n"
    "v_lshlrev_b32_e32 v70, 16, v11\n"
    "v_add_f32_dpp v42, v42, v42 row_half_mirror row_mask:0xf bank_mask:0xf bound_ctrl:1\n"
    "v_and_b32_e32 v71, 0xffff0000, v11\n"
    "v_add_f32_dpp v24, v24, v24 row_mirror row_mask:0xf bank_mask:0xf bound_ctrl:1\n"
    "v_lshlrev_b32_e32 v72, 16, v12\n"
    "v_add_f32_dpp v32, v32, v32 row_mirror row_mask:0xf bank_mask:0xf bound_ctrl:1\n"
    "v_and_b32_e32 v73, 0xffff0000, v12\n"
    "v_add_f32_dpp v40, v40, v40 row_mirror row_mask:0xf bank_mask:0xf bound_ctrl:1\n"
    "v_lshlrev_b32_e32 v74, 16, v13\n"
    "v_add_f32_dpp v26, v26, v26 row_mirror row_mask:0xf bank_mask:0xf bound_ctrl:1\n"
    "v_and_b32_e32 v75, 0xffff0000, v13\n"
    "v_add_f32_dpp v34, v34, v34 row_mirror row_mask:0xf bank_mask:0xf bound_ctrl:1\n"
    "v_lshlrev_b32_e32 v76, 16, v14\n"
    "v_add_f32_dpp v42, v42, v42 row_mirror row_mask:0xf bank_mask:0xf bound_ctrl:1\n"
    "v_and_b32_e32 v77, 0xffff0000, v14\n"
    "v_mul_f32_e32 v24, 0x3b800000, v24\n"
    "v_lshlrev_b32_e32 v78, 16, v15\n"
    "v_mul_f32_e32 v32, 0x3b800000, v32\n"
    "v_and_b32_e32 v79, 0xffff0000, v15\n"
    "v_mul_f32_e32 v40, 0x3b800000, v40\n"
    "v_lshlrev_b32_e32 v80, 16, v16\n"
    "v_mul_f32_e32 v25, 0.25, v25\n"
    "v_and_b32_e32 v81, 0xffff0000, v16\n"
    "v_mul_f32_e32 v33, 0.25, v33\n"
    "v_lshlrev_b32_e32 v82, 16, v17\n"
    "v_mul_f32_e32 v41, 0.25, v41\n"
    "v_and_b32_e32 v83, 0xffff0000, v17\n"
    "v_mul_f32_e64 v27, v24, -v24\n"
    "v_lshlrev_b32_e32 v84, 16, v18\n"
    "v_mul_f32_e64 v35, v32, -v32\n"
    "v_and_b32_e32 v85, 0xffff0000, v18\n"
    "v_mul_f32_e64 v43, v40, -v40\n"
    "v_lshlrev_b32_e32 v86, 16, v19\n"
    "v_sub_f32_e32 v25, v25, v30\n"
    "v_and_b32_e32 v87, 0xffff0000, v19\n"
    "v_sub_f32_e32 v33, v33, v38\n"
    "v_lshlrev_b32_e32 v88, 16, v20\n"
    "v_sub_f32_e32 v41, v41, v46\n"
    "v_and_b32_e32 v89, 0xffff0000, v20\n"
    "v_fmac_f32_e32 v27, 0x3b800000, v26\n"
    "v_lshlrev_b32_e32 v90, 16, v21\n"
    "v_fmac_f32_e32 v35, 0x3b800000, v34\n"
    "v_and_b32_e32 v91, 0xffff0000, v21\n"
    "v_fmac_f32_e32 v43, 0x3b800000, v42\n"
    "v_lshlrev_b32_e32 v92, 16, v22\n"
    "v_mul_f32_e32 v25, 0x3eaaaaab, v25\n"
    "v_and_b32_e32 v93, 0xffff0000, v22\n"
    "v_mul_f32_e32 v33, 0x3eaaaaab, v33\n"
    "v_lshlrev_b32_e32 v94, 16, v23\n"
    "v_mul_f32_e32 v41, 0x3eaaaaab, v41\n"
    "v_and_b32_e32 v95, 0xffff0000, v23\n"
    "v_max_f32_e32 v27, 0, v27\n"
    "v_max_f32_e32 v35, 0, v35\n"
    "v_max_f32_e32 v43, 0, v43\n"
    "s_cbranch_scc0 .Lat_bin\n"
    "s_movk_i32 s45, -2560\n"
    "s_mov_b32 s48, 81920\n"
    "v_mov_b32_e32 v172, 1\n"
    "s_waitcnt vmcnt(0)\n"
    "v_mul_u32_u24_e32 v160, 0x199a, v144\n"
    "v_mul_u32_u24_e32 v161, 0x199a, v145\n"
    "v_mul_u32_u24_e32 v162, 0x199a, v146\n"
    "v_mul_u32_u24_e32 v163, 0x199a, v147\n"
    "v_mul_u32_u24_e32 v164, 0x199a, v148\n"
    "v_bfe_u32 v160, v160, 24, 2\n"
    "v_bfe_u32 v161, v161, 24, 2\n"
    "v_bfe_u32 v162, v162, 24, 2\n"
    "v_bfe_u32 v163, v163, 24, 2\n"
    "v_bfe_u32 v164, v164, 24, 2\n"
    "v_lshl_add_u32 v166, v160, 2, s48\n"
    "v_lshl_add_u32 v167, v161, 2, s48\n"
    "v_lshl_add_u32 v168, v162, 2, s48\n"
    "v_lshl_add_u32 v169, v163, 2, s48\n"
    "v_lshl_add_u32 v170, v164, 2, s48\n"
    "ds_add_rtn_u32 v166, v166, v172 offset:4096\n"
    "ds_add_rtn_u32 v167, v167, v172 offset:4096\n"
    "ds_add_rtn_u32 v168, v168, v172 offset:4096\n"
    "ds_add_rtn_u32 v169, v169, v172 offset:4096\n"
    "ds_add_rtn_u32 v170, v170, v172 offset:4096\n"
    "v_mad_i32_i24 v144, v160, s45, v144\n"
    "v_mad_i32_i24 v145, v161, s45, v145\n"
    "v_mad_i32_i24 v146, v162, s45, v146\n"
    "v_mad_i32_i24 v147, v163, s45, v147\n"
    "v_mad_i32_i24 v148, v164, s45, v148\n"
    "v_lshl_or_b32 v144, v144, 18, v152\n"
    "v_lshl_or_b32 v145, v145, 18, v153\n"
    "v_lshl_or_b32 v146, v146, 18, v154\n"
    "v_lshl_or_b32 v147, v147, 18, v155\n"
    "v_lshl_or_b32 v148, v148, 18, v156\n"
    "v_lshl_add_u32 v160, v160, 10, s48\n"
    "v_lshl_add_u32 v161, v161, 10, s48\n"
    "v_lshl_add_u32 v162, v162, 10, s48\n"
    "v_lshl_add_u32 v163, v163, 10, s48\n"
    "v_lshl_add_u32 v164, v164, 10, s48\n"
    ".Lat_bin:\n"
    "v_cndmask_b32_e64 v25, v24, v25, s[62:63]\n"
    "v_cndmask_b32_e64 v33, v32, v33, s[62:63]\n"
    "v_cndmask_b32_e64 v41, v40, v41, s[62:63]\n"
    "v_add_f32_e32 v27, 0x3727c5ac, v27\n"
    "v_add_f32_e32 v35, 0x3727c5ac, v35\n"
    "v_add_f32_e32 v43, 0x3727c5ac, v43\n"
    "v_rsq_f32_e32 v28, v27\n"
    "v_rsq_f32_e32 v36, v35\n"
    "v_rsq_f32_e32 v44, v43\n"
    "v_fma_f32 v29, -v24, v28, s57\n"
    "v_fma_f32 v37, -v32, v36, s57\n"
    "v_fma_f32 v45, -v40, v44, s57\n"
    "v_fma_f32 v31, -v25, v28, s57\n"
    "v_fma_f32 v39, -v33, v36, s57\n"
    "v_fma_f32 v47, -v41, v44, s57\n"
    "v_sub_f32_e32 v24, v29, v31\n"
    "v_sub_f32_e32 v32, v37, v39\n"
    "v_sub_f32_e32 v40, v45, v47\n"
    "v_pk_fma_f32 v[48:49], v[48:49], v[28:29], v[28:29] op_sel:[0,0,1] op_sel_hi:[1,0,1]\n"
    "v_pk_fma_f32 v[50:51], v[50:51], v[28:29], v[28:29] op_sel:[0,0,1] op_sel_hi:[1,0,1]\n"
    "v_pk_fma_f32 v[52:53], v[52:53], v[28:29], v[28:29] op_sel:[0,0,1] op_sel_hi:[1,0,1]\n"
    "v_pk_fma_f32 v[54:55], v[54:55], v[28:29], v[28:29] op_sel:[0,0,1] op_sel_hi:[1,0,1]\n"
    "v_pk_fma_f32 v[56:57], v[56:57], v[28:29], v[30:31] op_sel:[0,0,1] op_sel_hi:[1,0,1]\n"
    "v_pk_fma_f32 v[58:59], v[58:59], v[28:29], v[30:31] op_sel:[0,0,1] op_sel_hi:[1,0,1]\n"
    "v_pk_fma_f32 v[60:61], v[60:61], v[28:29], v[30:31] op_sel:[0,0,1] op_sel_hi:[1,0,1]\n"
    "v_pk_fma_f32 v[62:63], v[62:63], v[28:29], v[30:31] op_sel:[0,0,1] op_sel_hi:[1,0,1]\n"
    "v_pk_add_f32 v[48:49], v[48:49], s[56:57] op_sel_hi:[1,0]\n"
    "v_pk_add_f32 v[50:51], v[50:51], s[56:57] op_sel_hi:[1,0]\n"
    "v_pk_add_f32 v[52:53], v[52:53], s[56:57] op_sel_hi:[1,0]\n"
    "v_pk_add_f32 v[54:55], v[54:55], s[56:57] op_sel_hi:[1,0]\n"
    "v_pk_add_f32 v[56:57], v[56:57], s[56:57] op_sel_hi:[1,0]\n"
    "v_pk_add_f32 v[58:59], v[58:59], s[56:57] op_sel_hi:[1,0]\n"
    "v_pk_add_f32 v[60:61], v[60:61], s[56:57] op_sel_hi:[1,0]\n"
    "v_pk_add_f32 v[62:63], v[62:63], s[56:57] op_sel_hi:[1,0]\n"
    "v_cvt_pk_bf16_f32 v0, v48, v49\n"
    "v_cvt_pk_bf16_f32 v1, v50, v51\n"
    "v_cvt_pk_bf16_f32 v2, v52, v53\n"
    "v_cvt_pk_bf16_f32 v3, v54, v55\n"
    "v_cvt_pk_bf16_f32 v4, v56, v57\n"
    "v_cvt_pk_bf16_f32 v5, v58, v59\n"
    "v_cvt_pk_bf16_f32 v6, v60, v61\n"
    "v_cvt_pk_bf16_f32 v7, v62, v63\n"
    "buffer_store_dwordx4 v[0:3], v104, s[8:11], s35 offen offset:0 sc1\n"
    "v_bfi_b32 v7, v107, v7, 1.0\n"
    "ds_write_b128 v105, v[0:3] offset:0\n"
    "buffer_store_dwordx4 v[4:7], v104, s[8:11], s35 offen offset:256 sc1\n"
    "v_pk_fma_f32 v[64:65], v[64:65], v[36:37], v[36:37] op_sel:[0,0,1] op_sel_hi:[1,0,1]\n"
    "v_pk_fma_f32 v[66:67], v[66:67], v[36:37], v[36:37] op_sel:[0,0,1] op_sel_hi:[1,0,1]\n"
    "v_pk_fma_f32 v[68:69], v[68:69], v[36:37], v[36:37] op_sel:[0,0,1] op_sel_hi:[1,0,1]\n"
    "v_pk_fma_f32 v[70:71], v[70:71], v[36:37], v[36:37] op_sel:[0,0,1] op_sel_hi:[1,0,1]\n"
    "v_pk_fma_f32 v[72:73], v[72:73], v[36:37], v[38:39] op_sel:[0,0,1] op_sel_hi:[1,0,1]\n"
    "v_pk_fma_f32 v[74:75], v[74:75], v[36:37], v[38:39] op_sel:[0,0,1] op_sel_hi:[1,0,1]\n"
    "v_pk_fma_f32 v[76:77], v[76:77], v[36:37], v[38:39] op_sel:[0,0,1] op_sel_hi:[1,0,1]\n"
    "v_pk_fma_f32 v[78:79], v[78:79], v[36:37], v[38:39] op_sel:[0,0,1] op_sel_hi:[1,0,1]\n"
    "v_pk_add_f32 v[64:65], v[64:65], s[56:57] op_sel_hi:[1,0]\n"
    "v_pk_add_f32 v[66:67], v[66:67], s[56:57] op_sel_hi:[1,0]\n"
    "v_pk_add_f32 v[68:69], v[68:69], s[56:57] op_sel_hi:[1,0]\n"
    "v_pk_add_f32 v[70:71], v[70:71], s[56:57] op_sel_hi:[1,0]\n"
    "v_pk_add_f32 v[72:73], v[72:73], s[56:57] op_sel_hi:[1,0]\n"
    "v_pk_add_f32 v[74:75], v[74:75], s[56:57] op_sel_hi:[1,0]\n"
    "v_pk_add_f32 v[76:77], v[76:77], s[56:57] op_sel_hi:[1,0]\n"
    "v_pk_add_f32 v[78:79], v[78:79], s[56:57] op_sel_hi:[1,0]\n"
    "v_cvt_pk_bf16_f32 v8, v64, v65\n"
    "v_cvt_pk_bf16_f32 v9, v66, v67\n"
    "v_cvt_pk_bf16_f32 v10, v68, v69\n"
    "v_cvt_pk_bf16_f32 v11, v70, v71\n"
    "v_cvt_pk_bf16_f32 v12, v72, v73\n"
    "v_cvt_pk_bf16_f32 v13, v74, v75\n"
    "v_cvt_pk_bf16_f32 v14, v76, v77\n"
    "v_cvt_pk_bf16_f32 v15, v78, v79\n"
    "buffer_store_dwordx4 v[8:11], v104, s[8:11], s54 offen offset:0 sc1\n"
    "v_bfi_b32 v15, v107, v15, 1.0\n"
    "ds_write_b128 v105, v[8:11] offset:8192\n"
    "buffer_store_dwordx4 v[12:15], v104, s[8:11], s54 offen offset:256 sc1\n"
    "v_pk_fma_f32 v[80:81], v[80:81], v[44:45], v[44:45] op_sel:[0,0,1] op_sel_hi:[1,0,1]\n"
    "v_pk_fma_f32 v[82:83], v[82:83], v[44:45], v[44:45] op_sel:[0,0,1] op_sel_hi:[1,0,1]\n"
    "v_pk_fma_f32 v[84:85], v[84:85], v[44:45], v[44:45] op_sel:[0,0,1] op_sel_hi:[1,0,1]\n"
    "v_pk_fma_f32 v[86:87], v[86:87], v[44:45], v[44:45] op_sel:[0,0,1] op_sel_hi:[1,0,1]\n"
    "v_pk_fma_f32 v[88:89], v[88:89], v[44:45], v[46:47] op_sel:[0,0,1] op_sel_hi:[1,0,1]\n"
    "v_pk_fma_f32 v[90:91], v[90:91], v[44:45], v[46:47] op_sel:[0,0,1] op_sel_hi:[1,0,1]\n"
    "v_pk_fma_f32 v[92:93], v[92:93], v[44:45], v[46:47] op_sel:[0,0,1] op_sel_hi:[1,0,1]\n"
    "v_pk_fma_f32 v[94:95], v[94:95], v[44:45], v[46:47] op_sel:[0,0,1] op_sel_hi:[1,0,1]\n"
    "v_pk_add_f32 v[80:81], v[80:81], s[56:57] op_sel_hi:[1,0]\n"
    "v_pk_add_f32 v[82:83], v[82:83], s[56:57] op_sel_hi:[1,0]\n"
    "v_pk_add_f32 v[84:85], v[84:85], s[56:57] op_sel_hi:[1,0]\n"
    "v_pk_add_f32 v[86:87], v[86:87], s[56:57] op_sel_hi:[1,0]\n"
    "v_pk_add_f32 v[88:89], v[88:89], s[56:57] op_sel_hi:[1,0]\n"
    "v_pk_add_f32 v[90:91], v[90:91], s[56:57] op_sel_hi:[1,0]\n"
    "v_pk_add_f32 v[92:93], v[92:93], s[56:57] op_sel_hi:[1,0]\n"
    "v_pk_add_f32 v[94:95], v[94:95], s[56:57] op_sel_hi:[1,0]\n"
    "v_cvt_pk_bf16_f32 v16, v80, v81\n"
    "v_cvt_pk_bf16_f32 v17, v82, v83\n"
    "v_cvt_pk_bf16_f32 v18, v84, v85\n"
    "v_cvt_pk_bf16_f32 v19, v86, v87\n"
    "v_cvt_pk_bf16_f32 v20, v88, v89\n"
    "v_cvt_pk_bf16_f32 v21, v90, v91\n"
    "v_cvt_pk_bf16_f32 v22, v92, v93\n"
    "v_cvt_pk_bf16_f32 v23, v94, v95\n"
    "buffer_store_dwordx4 v[16:19], v104, s[8:11], s60 offen offset:0 sc1\n"
    "v_bfi_b32 v23, v107, v23, 1.0\n"
    "ds_write_b128 v105, v[16:19] offset:16384\n"
    "buffer_store_dwordx4 v[20:23], v104, s[8:11], s60 offen offset:256 sc1\n"
    "v_pk_add_f32 v[56:57], v[56:57], v[24:25] op_sel_hi:[1,0]\n"
    "v_pk_add_f32 v[58:59], v[58:59], v[24:25] op_sel_hi:[1,0]\n"
    "v_pk_add_f32 v[60:61], v[60:61], v[24:25] op_sel_hi:[1,0]\n"
    "v_pk_add_f32 v[62:63], v[62:63], v[24:25] op_sel_hi:[1,0]\n"
    "v_cvt_pk_bf16_f32 v56, v56, v57\n"
    "v_cvt_pk_bf16_f32 v57, v58, v59\n"
    "v_cvt_pk_bf16_f32 v58, v60, v61\n"
    "v_cvt_pk_bf16_f32 v59, v62, v63\n"
    "ds_write_b128 v106, v[56:59] offset:0\n"
    "v_pk_add_f32 v[72:73], v[72:73], v[32:33] op_sel_hi:[1,0]\n"
    "v_pk_add_f32 v[74:75], v[74:75], v[32:33] op_sel_hi:[1,0]\n"
    "v_pk_add_f32 v[76:77], v[76:77], v[32:33] op_sel_hi:[1,0]\n"
    "v_pk_add_f32 v[78:79], v[78:79], v[32:33] op_sel_hi:[1,0]\n"
    "v_cvt_pk_bf16_f32 v72, v72, v73\n"
    "v_cvt_pk_bf16_f32 v73, v74, v75\n"
    "v_cvt_pk_bf16_f32 v74, v76, v77\n"
    "v_cvt_pk_bf16_f32 v75, v78, v79\n"
    "ds_write_b128 v106, v[72:75] offset:8192\n"
    ".long 0xbedf00ff\n"
    ".Lat_w0:\n"
    "s_setpc_b64 s[2:3]\n"
    "v_pk_add_f32 v[88:89], v[88:89], v[40:41] op_sel_hi:[1,0]\n"
    "v_pk_add_f32 v[90:91], v[90:91], v[40:41] op_sel_hi:[1,0]\n"
    "v_pk_add_f32 v[92:93], v[92:93], v[40:41] op_sel_hi:[1,0]\n"
    "v_pk_add_f32 v[94:95], v[94:95], v[40:41] op_sel_hi:[1,0]\n"
    "v_cvt_pk_bf16_f32 v88, v88, v89\n"
    "v_cvt_pk_bf16_f32 v89, v90, v91\n"
    "v_cvt_pk_bf16_f32 v90, v92, v93\n"
    "v_cvt_pk_bf16_f32 v91, v94, v95\n"
    "ds_write_b128 v106, v[88:91] offset:16384\n"
    "s_cbranch_scc0 .Lat_bin2\n"
    "s_waitcnt lgkmcnt(0)\n"
    "v_min_i32_e32 v166, 254, v166\n"
    "v_min_i32_e32 v167, 254, v167\n"
    "v_min_i32_e32 v168, 254, v168\n"
    "v_min_i32_e32 v169, 254, v169\n"
    "v_min_i32_e32 v170, 254, v170\n"
    "v_lshl_add_u32 v160, v166, 2, v160\n"
    "v_lshl_add_u32 v161, v167, 2, v161\n"
    "v_lshl_add_u32 v162, v168, 2, v162\n"
    "v_lshl_add_u32 v163, v169, 2, v163\n"
    "v_lshl_add_u32 v164, v170, 2, v164\n"
    "ds_write_b32 v160, v144 offset:4\n"
    "ds_write_b32 v161, v145 offset:4\n"
    "ds_write_b32 v162, v146 offset:4\n"
    "ds_write_b32 v163, v147 offset:4\n"
    "ds_write_b32 v164, v148 offset:4\n"
    ".Lat_bin2:\n"
    "s_waitcnt lgkmcnt(0)\n"
    "s_barrier\n"
    "ds_read_b128 a[0:3], v240 offset:32768\n"
    "ds_read_b128 a[32:35], v240 offset:40960\n"
    "ds_read_b128 a[64:67], v240 offset:49152\n"
    "ds_read_b128 a[4:7], v241 offset:32768\n"
    "ds_read_b128 a[36:39], v241 offset:40960\n"
    "ds_read_b128 a[68:71], v241 offset:49152\n"
    "ds_read_b128 a[8:11], v240 offset:34816\n"
    "ds_read_b128 a[40:43], v240 offset:43008\n"
    "ds_read_b128 a[72:75], v240 offset:51200\n"
    "ds_read_b128 a[12:15], v241 offset:34816\n"
    "ds_read_b128 a[44:47], v241 offset:43008\n"
    "ds_read_b128 a[76:79], v241 offset:51200\n"
    "buffer_load_dword v101, v100, s[8:11], 0 offen offset:0 sc1\n"
    "ds_read_b128 a[16:19], v240 offset:36864\n"
    "ds_read_b128 a[48:51], v240 offset:45056\n"
    "ds_read_b128 a[80:83], v240 offset:53248\n"
    "ds_read_b128 a[20:23], v241 offset:36864\n"
    "ds_read_b128 a[52:55], v241 offset:45056\n"
    "ds_read_b128 a[84:87], v241 offset:53248\n"
    "ds_read_b128 a[24:27], v240 offset:38912\n"
    "ds_read_b128 a[56:59], v240 offset:47104\n"
    "ds_read_b128 a[88:91], v240 offset:55296\n"
    "ds_read_b128 a[28:31], v241 offset:38912\n"
    "ds_read_b128 a[60:63], v241 offset:47104\n"
    "ds_read_b128 a[92:95], v241 offset:55296\n"
    "ds_read_b128 v[40:43], v108 offset:0\n"
    "s_mul_i32 s52, s93, 33792\n"
    "s_add_u32 s52, s52, 5242880\n"
    "s_lshl_b32 s61, s32, 12\n"
    "s_lshl4_add_u32 s46, s34, 6076416\n"
    "s_cmp_eq_u32 s93, 0\n"
    "s_cselect_b32 s46, s46, 2.0\n"
    "s_lshl_b32 s48, s34, 7\n"
    "s_add_u32 s48, s48, 68206592\n"
    "s_lshl_b32 s47, s93, 12\n"
    "s_waitcnt vmcnt(1)\n"
    "s_waitcnt lgkmcnt(0)\n"
    "s_barrier\n"
    "buffer_store_dword v102, off, s[8:11], s46 offset:0 sc1\n"
    "buffer_store_dwordx4 v[40:43], v109, s[8:11], s48 offen offset:0 sc1\n"
    "s_lshl4_add_u32 s46, s34, s47\n"
    "s_add_u32 s46, s46, 6084608\n"
    "s_waitcnt vmcnt(2)\n"
    "v_cmp_ne_u32_e32 vcc, s39, v101\n"
    "s_nop 4\n"
    "s_cbranch_vccz .Lat_apoll_ret\n"
    ".Lat_apoll:\n"
    "s_mov_b32 s53, 0x400000\n"
    ".Lat_apoll_l:\n"
    "s_sleep 1\n"
    "buffer_load_dword v101, v100, s[8:11], 0 offen offset:0 sc1\n"
    "s_waitcnt vmcnt(0)\n"
    "v_cmp_ne_u32_e32 vcc, s39, v101\n"
    "s_sub_u32 s53, s53, 1\n"
    "s_cbranch_scc1 .Lat_apoll_ret\n"
    "s_nop 2\n"
    "s_cbranch_vccnz .Lat_apoll_l\n"
    ".Lat_apoll_ret:\n"
    "s_add_u32 s47, s52, s61\n"
    "buffer_load_dwordx4 v[112:115], v250, s[8:11], s47 offen offset:0 sc1\n"
    "buffer_load_dwordx4 v[116:119], v250, s[8:11], s47 offen offset:1024 sc1\n"
    "buffer_load_dwordx4 v[128:131], v250, s[8:11], s47 offen offset:2048 sc1\n"
    "s_add_u32 s61, s61, 0x1000\n"
    "buffer_load_dwordx4 v[132:135], v250, s[8:11], s47 offen offset:3072 sc1\n"
    "s_bitset0_b32 s61, 14\n"
    "s_add_u32 s47, s52, s61\n"
    "buffer_load_dwordx4 v[144:147], v250, s[8:11], s47 offen offset:0 sc1\n"
    "buffer_load_dwordx4 v[148:151], v250, s[8:11], s47 offen offset:1024 sc1\n"
    "buffer_load_dwordx4 v[160:163], v250, s[8:11], s47 offen offset:2048 sc1\n"
    "s_add_u32 s61, s61, 0x1000\n"
    "buffer_load_dwordx4 v[164:167], v250, s[8:11], s47 offen offset:3072 sc1\n"
    "s_bitset0_b32 s61, 14\n"
    "s_add_u32 s47, s52, s61\n"
    "buffer_load_dwordx4 v[176:179], v250, s[8:11], s47 offen offset:0 sc1\n"
    "buffer_load_dwordx4 v[180:183], v250, s[8:11], s47 offen offset:1024 sc1\n"
    "buffer_load_dwordx4 v[192:195], v250, s[8:11], s47 offen offset:2048 sc1\n"
    "s_add_u32 s61, s61, 0x1000\n"
    "v_lshrrev_b32_e32 v8, 4, v250\n"
    "v_and_b32_e32 v10, 0x70, v251\n"
    "v_bfe_u32 v9, v8, 3, 1\n"
    "v_lshl_add_u32 v248, v9, 9, v10\n"
    "v_lshrrev_b32_e32 v9, 4, v8\n"
    "v_lshl_add_u32 v248, v9, 12, v248\n"
    "s_mul_i32 s43, s50, 3\n"
    "s_xor_b32 s43, s43, s51\n"
    "s_lshl_b32 s43, s43, 15\n"
    "s_add_u32 s43, s43, 32768\n"
    "s_mul_i32 s42, s93, 5120\n"
    "s_lshl_b32 s45, s50, 8\n"
    "s_mul_i32 s40, s32, 1277952\n"
    "s_lshl_b32 s41, s51, 14\n"
    "s_add_u32 s40, s40, s41\n"
    "s_add_u32 s40, s40, s45\n"
    "s_add_u32 s41, s40, 128\n"
    "v_bfe_u32 v9, v8, 2, 3\n"
    "v_lshlrev_b32_e32 v244, 7, v9\n"
    "v_lshrrev_b32_e32 v10, 5, v8\n"
    "v_lshl_add_u32 v244, v10, 10, v244\n"
    "v_and_b32_e32 v9, 6, v9\n"
    "v_bfe_u32 v10, v8, 1, 1\n"
    "v_xor_b32_e32 v9, v9, v10\n"
    "v_lshl_add_u32 v244, v9, 4, v244\n"
    "v_and_b32_e32 v10, 1, v8\n"
    "v_lshl_add_u32 v244, v10, 3, v244\n"
    "s_lshl_b32 s45, s51, 16\n"
    "s_add_u32 s45, s45, 32768\n"
    "v_add_u32_e32 v244, s45, v244\n"
    "v_xor_b32_e32 v245, 32, v244\n"
    "v_xor_b32_e32 v246, 64, v244\n"
    "v_xor_b32_e32 v247, 0x60, v244\n"
    "s_lshl_b32 s45, s51, 15\n"
    "s_add_u32 s48, s45, 32768\n"
    "s_sub_u32 s45, 131072, s45\n"
    "v_add_u32_e32 v242, s45, v240\n"
    "v_add_u32_e32 v240, s48, v240\n"
    "v_xor_b32_e32 v241, 64, v240\n"
    "v_xor_b32_e32 v243, 64, v242\n"
    "s_xor_b32 s45, s93, 2\n"
    "s_mul_i32 s45, s45, 5120\n"
    "v_lshl_add_u32 v249, v8, 2, s45\n"
    "s_lshl_b32 s45, s93, 11\n"
    "v_add_u32_e32 v99, s45, v250\n"
    "v_add_u32_e32 v98, 11345920, v99\n"
    "v_and_b32_e32 v252, 0x30, v250\n"
    "v_add_u32_e32 v252, 0x400, v252\n"
    "s_mul_i32 s59, s32, 999\n"
    "s_andn2_b32 s59, s59, 63\n"
    "s_sub_u32 s48, 3968, s59\n"
    "v_cmp_le_u32_e32 vcc, 0x80, v250\n"
    "v_mov_b32_e32 v80, s48\n"
    "s_add_u32 s59, s59, 6076416\n"
    "v_cndmask_b32_e32 v83, v250, v80, vcc\n"
    "v_bfe_u32 v253, v250, 4, 2\n"
    "v_max_u32_e32 v253, 1, v253\n"
    "v_add_u32_e32 v253, s32, v253\n"
    "v_and_b32_e32 v253, 3, v253\n"
    "v_lshlrev_b32_e32 v253, 9, v253\n"
    "v_mov_b32_e32 v84, s39\n"
    "v_lshrrev_b32_e32 v86, 2, v250\n"
    "v_min_u32_e32 v86, 124, v86\n"
    "s_lshl2_add_u32 s58, s34, s93\n"
    "s_lshl_b32 s58, s58, 7\n"
    "s_add_u32 s58, s58, 5910528\n"
    "s_lshl_b32 s45, s50, 13\n"
    "v_add_u32_e32 v85, s45, v250\n"
    "s_lshl3_add_u32 s45, s45, 49152\n"
    "v_add_u32_e32 v255, s45, v250\n"
    "v_lshrrev_b32_e32 v105, 7, v250\n"
    "v_and_b32_e32 v104, 0x70, v251\n"
    "v_lshl_add_u32 v104, v105, 9, v104\n"
    "v_add_u32_e32 v105, 0xc00, v104\n"
    "v_add_u32_e32 v106, 0x1800, v104\n"
    "v_add_u32_e32 v107, 0x2400, v104\n"
    "s_lshl_b32 s57, s93, 7\n"
    "s_lshl4_add_u32 s56, s57, 24576\n"
    "s_add_u32 s57, s57, 5111808\n"
    "s_mul_i32 s45, s51, 32768\n"
    "s_add_u32 s45, s45, 8192\n"
    "v_subrev_u32_e32 v46, s45, v240\n"
    "v_subrev_u32_e32 v47, s45, v241\n"
    "s_mul_i32 s45, s51, 61440\n"
    "s_add_u32 s45, s45, 8192\n"
    "v_subrev_u32_e32 v4, s45, v244\n"
    "v_subrev_u32_e32 v5, s45, v245\n"
    "v_subrev_u32_e32 v6, s45, v246\n"
    "v_subrev_u32_e32 v7, s45, v247\n"
    "s_waitcnt vmcnt(9)\n"
    "v_mfma_f32_16x16x32_bf16 v[24:27], v[112:115], a[0:3], 0\n"
    "buffer_load_dwordx4 v[196:199], v250, s[8:11], s47 offen offset:3072 sc1\n"
    "s_bitset0_b32 s61, 14\n"
    "v_mfma_f32_16x16x32_bf16 v[28:31], v[116:119], a[0:3], 0\n"
    "v_accvgpr_write_b32 a244, 0\n"
    "v_mfma_f32_16x16x32_bf16 v[48:51], v[112:115], a[32:35], 0\n"
    "v_accvgpr_write_b32 a245, 0\n"
    "v_mfma_f32_16x16x32_bf16 v[52:55], v[116:119], a[32:35], 0\n"
    "s_add_u32 s47, s52, s61\n"
    "buffer_load_dwordx4 v[208:211], v250, s[8:11], s47 offen offset:0 sc1\n"
    "v_mfma_f32_16x16x32_bf16 v[64:67], v[112:115], a[64:67], 0\n"
    "v_bfe_u32 v0, v250, 4, 2\n"
    "v_mfma_f32_16x16x32_bf16 v[68:71], v[116:119], a[64:67], 0\n"
    "s_mov_b64 s[24:25], s[18:19]\n"
    "v_accvgpr_write_b32 a246, 0\n"
    "s_waitcnt vmcnt(9)\n"
    "v_mfma_f32_16x16x32_bf16 v[24:27], v[128:131], a[4:7], v[24:27]\n"
    "buffer_load_dwordx4 v[212:215], v250, s[8:11], s47 offen offset:1024 sc1\n"
    "s_add_u32 s52, s52, 0x4000\n"
    "v_mfma_f32_16x16x32_bf16 v[28:31], v[132:135], a[4:7], v[28:31]\n"
    "buffer_store_dword v102, off, s[8:11], s46 offset:0 sc1\n"
    "v_accvgpr_write_b32 a247, 0\n"
    "v_mfma_f32_16x16x32_bf16 v[48:51], v[128:131], a[36:39], v[48:51]\n"
    "v_bfe_u32 v1, v250, 6, 2\n"
    "v_accvgpr_write_b32 a248, 0\n"
    "v_mfma_f32_16x16x32_bf16 v[52:55], v[132:135], a[36:39], v[52:55]\n"
    "buffer_load_dwordx4 v[224:227], v250, s[8:11], s47 offen offset:2048 sc1\n"
    "s_add_u32 s61, s61, 0x1000\n"
    "v_mfma_f32_16x16x32_bf16 v[64:67], v[128:131], a[68:71], v[64:67]\n"
    "s_mov_b32 s18, 5120000\n"
    "v_accvgpr_write_b32 a249, 0\n"
    "v_mfma_f32_16x16x32_bf16 v[68:71], v[132:135], a[68:71], v[68:71]\n"
    "v_mov_b32_e32 v2, 0x3f80\n"
    "s_waitcnt vmcnt(10)\n"
    "v_mfma_f32_16x16x32_bf16 v[24:27], v[144:147], a[8:11], v[24:27]\n"
    "buffer_load_dwordx4 v[228:231], v250, s[8:11], s47 offen offset:3072 sc1\n"
    "s_bitset0_b32 s61, 14\n"
    "v_mfma_f32_16x16x32_bf16 v[28:31], v[148:151], a[8:11], v[28:31]\n"
    "v_accvgpr_write_b32 a250, 0\n"
    "v_mfma_f32_16x16x32_bf16 v[48:51], v[144:147], a[40:43], v[48:51]\n"
    "s_mov_b32 s19, 0x20000\n"
    "v_accvgpr_write_b32 a251, 0\n"
    "v_mfma_f32_16x16x32_bf16 v[52:55], v[148:151], a[40:43], v[52:55]\n"
    "s_add_u32 s47, s52, s61\n"
    "buffer_load_dwordx4 v[120:123], v250, s[8:11], s47 offen offset:0 sc1\n"
    "v_mfma_f32_16x16x32_bf16 v[64:67], v[144:147], a[72:75], v[64:67]\n"
    "v_cmp_eq_u32_e32 vcc, 3, v0\n"
    "s_nop 1\n"
    "v_cndmask_b32_e64 v2, v2, 1.0, vcc\n"
    "v_mfma_f32_16x16x32_bf16 v[68:71], v[148:151], a[72:75], v[68:71]\n"
    "v_accvgpr_write_b32 a252, 0\n"
    "s_waitcnt vmcnt(10)\n"
    "v_mfma_f32_16x16x32_bf16 v[24:27], v[160:163], a[12:15], v[24:27]\n"
    "buffer_load_dwordx4 v[124:127], v250, s[8:11], s47 offen offset:1024 sc1\n"
    "v_accvgpr_write_b32 a253, 0\n"
    "v_mfma_f32_16x16x32_bf16 v[28:31], v[164:167], a[12:15], v[28:31]\n"
    "s_mov_b64 s[20:21], s[8:9]\n"
    "v_accvgpr_write_b32 a254, 0\n"
    "v_mfma_f32_16x16x32_bf16 v[48:51], v[160:163], a[44:47], v[48:51]\n"
    "v_cmp_eq_u32_e32 vcc, 0, v1\n"
    "s_nop 1\n"
    "v_cndmask_b32_e32 v3, 0, v2, vcc\n"
    "v_mfma_f32_16x16x32_bf16 v[52:55], v[164:167], a[44:47], v[52:55]\n"
    "buffer_load_dwordx4 v[136:139], v250, s[8:11], s47 offen offset:2048 sc1\n"
    "s_add_u32 s61, s61, 0x1000\n"
    "v_mfma_f32_16x16x32_bf16 v[64:67], v[160:163], a[76:79], v[64:67]\n"
    "v_accvgpr_write_b32 a232, v3\n"
    "v_accvgpr_write_b32 a255, 0\n"
    "v_mfma_f32_16x16x32_bf16 v[68:71], v[164:167], a[76:79], v[68:71]\n"
    "s_mov_b32 s22, 5242880\n"
    "v_accvgpr_write_b32 a192, 0\n"
    "s_waitcnt vmcnt(10)\n"
    "v_mfma_f32_16x16x32_bf16 v[24:27], v[176:179], a[16:19], v[24:27]\n"
    "buffer_load_dwordx4 v[140:143], v250, s[8:11], s47 offen offset:3072 sc1\n"
    "s_bitset0_b32 s61, 14\n"
    "v_mfma_f32_16x16x32_bf16 v[28:31], v[180:183], a[16:19], v[28:31]\n"
    "v_cmp_eq_u32_e32 vcc, 1, v1\n"
    "s_nop 1\n"
    "v_cndmask_b32_e32 v3, 0, v2, vcc\n"
    "v_mfma_f32_16x16x32_bf16 v[48:51], v[176:179], a[48:51], v[48:51]\n"
    "s_movk_i32 s26, 20000\n"
    "v_accvgpr_write_b32 a193, 0\n"
    "v_mfma_f32_16x16x32_bf16 v[52:55], v[180:183], a[48:51], v[52:55]\n"
    "s_add_u32 s47, s52, s61\n"
    "buffer_load_dwordx4 v[152:155], v250, s[8:11], s47 offen offset:0 sc1\n"
    "v_mfma_f32_16x16x32_bf16 v[64:67], v[176:179], a[80:83], v[64:67]\n"
    "buffer_load_dword v80, v83, s[8:11], s59 offen offset:0 sc1\n"
    "v_accvgpr_write_b32 a194, 0\n"
    "v_mfma_f32_16x16x32_bf16 v[68:71], v[180:183], a[80:83], v[68:71]\n"
    "v_accvgpr_write_b32 a233, v3\n"
    "v_accvgpr_write_b32 a195, 0\n"
    "s_waitcnt vmcnt(11)\n"
    "v_mfma_f32_16x16x32_bf16 v[24:27], v[192:195], a[20:23], v[24:27]\n"
    "buffer_load_dwordx4 v[156:159], v250, s[8:11], s47 offen offset:1024 sc1\n"
    "v_accvgpr_write_b32 a196, 0\n"
    "v_mfma_f32_16x16x32_bf16 v[28:31], v[196:199], a[20:23], v[28:31]\n"
    "s_bfe_u32 s88, s92, 0x20001\n"
    "v_mfma_f32_16x16x32_bf16 v[48:51], v[192:195], a[52:55], v[48:51]\n"
    "v_accvgpr_write_b32 a197, 0\n"
    "v_mfma_f32_16x16x32_bf16 v[52:55], v[196:199], a[52:55], v[52:55]\n"
    "buffer_load_dwordx4 v[168:171], v250, s[8:11], s47 offen offset:2048 sc1\n"
    "s_add_u32 s61, s61, 0x1000\n"
    "v_mfma_f32_16x16x32_bf16 v[64:67], v[192:195], a[84:87], v[64:67]\n"
    "v_cmp_eq_u32_e32 vcc, 2, v1\n"
    "s_nop 1\n"
    "v_cndmask_b32_e32 v3, 0, v2, vcc\n"
    "v_mfma_f32_16x16x32_bf16 v[68:71], v[196:199], a[84:87], v[68:71]\n"
    "s_lshl_b32 s82, s88, 7\n"
    "v_accvgpr_write_b32 a198, 0\n"
    "s_waitcnt vmcnt(11)\n"
    "v_mfma_f32_16x16x32_bf16 v[24:27], v[208:211], a[24:27], v[24:27]\n"
    "buffer_load_dwordx4 v[172:175], v250, s[8:11], s47 offen offset:3072 sc1\n"
    "s_bitset0_b32 s61, 14\n"
    "v_mfma_f32_16x16x32_bf16 v[28:31], v[212:215], a[24:27], v[28:31]\n"
    "v_accvgpr_write_b32 a234, v3\n"
    "v_accvgpr_write_b32 a199, 0\n"
    "v_mfma_f32_16x16x32_bf16 v[48:51], v[208:211], a[56:59], v[48:51]\n"
    "v_accvgpr_write_b32 a200, 0\n"
    "v_mfma_f32_16x16x32_bf16 v[52:55], v[212:215], a[56:59], v[52:55]\n"
    "s_add_u32 s47, s52, s61\n"
    "buffer_load_dwordx4 v[184:187], v250, s[8:11], s47 offen offset:0 sc1\n"
    "v_mfma_f32_16x16x32_bf16 v[64:67], v[208:211], a[88:91], v[64:67]\n"
    "s_add_u32 s82, s82, 6101248\n"
    "v_accvgpr_write_b32 a201, 0\n"
    "v_mfma_f32_16x16x32_bf16 v[68:71], v[212:215], a[88:91], v[68:71]\n"
    "v_cmp_eq_u32_e32 vcc, 3, v1\n"
    "s_nop 1\n"
    "v_cndmask_b32_e32 v3, 0, v2, vcc\n"
    "s_waitcnt vmcnt(10)\n"
    "v_mfma_f32_16x16x32_bf16 v[24:27], v[224:227], a[28:31], v[24:27]\n"
    "buffer_load_dwordx4 v[188:191], v250, s[8:11], s47 offen offset:1024 sc1\n"
    "v_accvgpr_write_b32 a202, 0\n"
    "v_mfma_f32_16x16x32_bf16 v[28:31], v[228:231], a[28:31], v[28:31]\n"
    "v_accvgpr_write_b32 a235, v3\n"
    "v_accvgpr_write_b32 a203, 0\n"
    "v_mfma_f32_16x16x32_bf16 v[48:51], v[224:227], a[60:63], v[48:51]\n"
    "s_lshl_b32 s83, s88, 17\n"
    "v_accvgpr_write_b32 a204, 0\n"
    "v_mfma_f32_16x16x32_bf16 v[52:55], v[228:231], a[60:63], v[52:55]\n"
    "buffer_load_dwordx4 v[200:203], v250, s[8:11], s47 offen offset:2048 sc1\n"
    "s_add_u32 s61, s61, 0x1000\n"
    "v_mfma_f32_16x16x32_bf16 v[64:67], v[224:227], a[92:95], v[64:67]\n"
    "s_and_b32 s89, s34, 31\n"
    "v_accvgpr_write_b32 a205, 0\n"
    "v_mfma_f32_16x16x32_bf16 v[68:71], v[228:231], a[92:95], v[68:71]\n"
    "s_lshl2_add_u32 s89, s89, s93\n"
    "v_accvgpr_write_b32 a206, 0\n"
    "s_waitcnt vmcnt(10)\n"
    "v_mfma_f32_16x16x32_bf16 v[32:35], v[120:123], a[0:3], 0\n"
    "buffer_load_dwordx4 v[204:207], v250, s[8:11], s47 offen offset:3072 sc1\n"
    "s_bitset0_b32 s61, 14\n"
    "v_mfma_f32_16x16x32_bf16 v[36:39], v[124:127], a[0:3], 0\n"
    "v_cvt_pk_bf16_f32 v24, v24, v25\n"
    "v_accvgpr_write_b32 a207, 0\n"
    "v_mfma_f32_16x16x32_bf16 v[56:59], v[120:123], a[32:35], 0\n"
    "v_cvt_pk_bf16_f32 v25, v26, v27\n"
    "v_accvgpr_write_b32 a208, 0\n"
    "v_mfma_f32_16x16x32_bf16 v[60:63], v[124:127], a[32:35], 0\n"
    "s_add_u32 s47, s52, s61\n"
    "buffer_load_dwordx4 v[216:219], v250, s[8:11], s47 offen offset:0 sc1\n"
    "v_mfma_f32_16x16x32_bf16 v[72:75], v[120:123], a[64:67], 0\n"
    "v_cvt_pk_bf16_f32 v26, v28, v29\n"
    "v_cvt_pk_bf16_f32 v27, v30, v31\n"
    "v_mfma_f32_16x16x32_bf16 v[76:79], v[124:127], a[64:67], 0\n"
    "buffer_store_dwordx4 v[24:27], v98, s[8:11], s35 offen offset:0 sc1\n"
    "v_accvgpr_write_b32 a209, 0\n"
    "s_waitcnt vmcnt(11)\n"
    "v_mfma_f32_16x16x32_bf16 v[32:35], v[136:139], a[4:7], v[32:35]\n"
    "buffer_load_dwordx4 v[220:223], v250, s[8:11], s47 offen offset:1024 sc1\n"
    "s_add_u32 s52, s52, 0x4000\n"
    "v_mfma_f32_16x16x32_bf16 v[36:39], v[140:143], a[4:7], v[36:39]\n"
    "v_cvt_pk_bf16_f32 v48, v48, v49\n"
    "v_accvgpr_write_b32 a210, 0\n"
    "v_mfma_f32_16x16x32_bf16 v[56:59], v[136:139], a[36:39], v[56:59]\n"
    "v_cvt_pk_bf16_f32 v49, v50, v51\n"
    "v_cvt_pk_bf16_f32 v50, v52, v53\n"
    "v_mfma_f32_16x16x32_bf16 v[60:63], v[140:143], a[36:39], v[60:63]\n"
    "buffer_load_dwordx4 v[232:235], v250, s[8:11], s47 offen offset:2048 sc1\n"
    "s_add_u32 s61, s61, 0x1000\n"
    "s_waitcnt vmcnt(11)\n"
    "v_cmp_ne_u32_e32 vcc, s39, v80\n"
    "v_mfma_f32_16x16x32_bf16 v[72:75], v[136:139], a[68:71], v[72:75]\n"
    "v_cvt_pk_bf16_f32 v51, v54, v55\n"
    "buffer_store_dwordx4 v[48:51], v98, s[8:11], s54 offen offset:0 sc1\n"
    "v_mfma_f32_16x16x32_bf16 v[76:79], v[140:143], a[68:71], v[76:79]\n"
    "v_cvt_pk_bf16_f32 v64, v64, v65\n"
    "v_cvt_pk_bf16_f32 v65, v66, v67\n"
    "s_waitcnt vmcnt(11)\n"
    "v_mfma_f32_16x16x32_bf16 v[32:35], v[152:155], a[8:11], v[32:35]\n"
    "buffer_load_dwordx4 v[236:239], v250, s[8:11], s47 offen offset:3072 sc1\n"
    "s_bitset0_b32 s61, 14\n"
    "v_mfma_f32_16x16x32_bf16 v[36:39], v[156:159], a[8:11], v[36:39]\n"
    "s_cbranch_vccnz .Lat_h1poll\n"
    ".Lat_h1poll_ret:\n"
    "s_mov_b32 m0, s56\n"
    "v_mfma_f32_16x16x32_bf16 v[56:59], v[152:155], a[40:43], v[56:59]\n"
    "buffer_load_dwordx4 v104, s[8:11], s57 offen offset:0 sc1 lds\n"
    "v_cvt_pk_bf16_f32 v66, v68, v69\n"
    "v_mfma_f32_16x16x32_bf16 v[60:63], v[156:159], a[40:43], v[60:63]\n"
    "v_cvt_pk_bf16_f32 v67, v70, v71\n"
    "buffer_store_dwordx4 v[64:67], v98, s[8:11], s60 offen offset:0 sc1\n"
    "v_mfma_f32_16x16x32_bf16 v[72:75], v[152:155], a[72:75], v[72:75]\n"
    "s_lshl_b32 s89, s89, 10\n"
    "v_accvgpr_write_b32 a211, 0\n"
    "v_mfma_f32_16x16x32_bf16 v[76:79], v[156:159], a[72:75], v[76:79]\n"
    "s_add_u32 s83, s83, s89\n"
    "v_accvgpr_write_b32 a212, 0\n"
    "s_waitcnt vmcnt(12)\n"
    "v_mfma_f32_16x16x32_bf16 v[32:35], v[168:171], a[12:15], v[32:35]\n"
    "buffer_load_dwordx4 v105, s[8:11], s57 offen offset:1024 sc1 lds\n"
    "v_accvgpr_write_b32 a213, 0\n"
    "v_mfma_f32_16x16x32_bf16 v[36:39], v[172:175], a[12:15], v[36:39]\n"
    "s_add_u32 s83, s83, 67682304\n"
    "v_accvgpr_write_b32 a214, 0\n"
    "v_mfma_f32_16x16x32_bf16 v[56:59], v[168:171], a[44:47], v[56:59]\n"
    "ds_write_b128 v99, v[24:27] offset:0\n"
    "v_accvgpr_write_b32 a215, 0\n"
    "v_mfma_f32_16x16x32_bf16 v[60:63], v[172:175], a[44:47], v[60:63]\n"
    "s_add_u32 m0, s43, 0\n"
    "v_accvgpr_write_b32 a216, 0\n"
    "v_mfma_f32_16x16x32_bf16 v[72:75], v[168:171], a[76:79], v[72:75]\n"
    "buffer_load_dwordx4 v104, s[8:11], s40 offen offset:0 sc1 lds\n"
    "v_accvgpr_write_b32 a217, 0\n"
    "v_mfma_f32_16x16x32_bf16 v[76:79], v[172:175], a[76:79], v[76:79]\n"
    "s_lshl_b32 s30, s93, 10\n"
    "s_waitcnt vmcnt(12)\n"
    "v_mfma_f32_16x16x32_bf16 v[32:35], v[184:187], a[16:19], v[32:35]\n"
    "v_accvgpr_write_b32 a218, 0\n"
    "v_mfma_f32_16x16x32_bf16 v[36:39], v[188:191], a[16:19], v[36:39]\n"
    "s_add_u32 s30, s30, 20480\n"
    "v_accvgpr_write_b32 a219, 0\n"
    "v_mfma_f32_16x16x32_bf16 v[56:59], v[184:187], a[48:51], v[56:59]\n"
    "buffer_load_dwordx4 v105, s[8:11], s40 offen offset:1024 sc1 lds\n"
    "v_mfma_f32_16x16x32_bf16 v[60:63], v[188:191], a[48:51], v[60:63]\n"
    "v_accvgpr_write_b32 a220, 0\n"
    "v_mfma_f32_16x16x32_bf16 v[72:75], v[184:187], a[80:83], v[72:75]\n"
    "s_bitcmp0_b32 s92, 0\n"
    "s_cselect_b32 s85, 1, 0\n"
    "s_cselect_b32 s80, 5, 0\n"
    "v_mfma_f32_16x16x32_bf16 v[76:79], v[188:191], a[80:83], v[76:79]\n"
    "ds_write_b128 v99, v[48:51] offset:8192\n"
    "v_accvgpr_write_b32 a221, 0\n"
    "s_waitcnt vmcnt(11)\n"
    "v_mfma_f32_16x16x32_bf16 v[32:35], v[200:203], a[20:23], v[32:35]\n"
    "buffer_load_dwordx4 v106, s[8:11], s40 offen offset:2048 sc1 lds\n"
    "v_mfma_f32_16x16x32_bf16 v[36:39], v[204:207], a[20:23], v[36:39]\n"
    "v_accvgpr_write_b32 a222, 0\n"
    "v_mfma_f32_16x16x32_bf16 v[56:59], v[200:203], a[52:55], v[56:59]\n"
    "v_accvgpr_write_b32 a223, 0\n"
    "v_mfma_f32_16x16x32_bf16 v[60:63], v[204:207], a[52:55], v[60:63]\n"
    "s_bitcmp0_b32 s92, 0\n"
    "s_cselect_b32 s82, s82, 2.0\n"
    "s_cselect_b32 s83, s83, 2.0\n"
    "v_mfma_f32_16x16x32_bf16 v[72:75], v[200:203], a[84:87], v[72:75]\n"
    "buffer_load_dwordx4 v107, s[8:11], s40 offen offset:3072 sc1 lds\n"
    "v_mfma_f32_16x16x32_bf16 v[76:79], v[204:207], a[84:87], v[76:79]\n"
    "v_accvgpr_write_b32 a224, 0\n"
    "s_waitcnt vmcnt(10)\n"
    "v_mfma_f32_16x16x32_bf16 v[32:35], v[216:219], a[24:27], v[32:35]\n"
    "v_accvgpr_write_b32 a225, 0\n"
    "v_mfma_f32_16x16x32_bf16 v[36:39], v[220:223], a[24:27], v[36:39]\n"
    "s_add_u32 s40, s40, 0x8000\n"
    "v_mfma_f32_16x16x32_bf16 v[56:59], v[216:219], a[56:59], v[56:59]\n"
    "ds_write_b128 v99, v[64:67] offset:49152\n"
    "v_mfma_f32_16x16x32_bf16 v[60:63], v[220:223], a[56:59], v[60:63]\n"
    "s_mov_b32 s84, 2.0\n"
    "v_mfma_f32_16x16x32_bf16 v[72:75], v[216:219], a[88:91], v[72:75]\n"
    "v_accvgpr_write_b32 a226, 0\n"
    "v_mfma_f32_16x16x32_bf16 v[76:79], v[220:223], a[88:91], v[76:79]\n"
    "v_accvgpr_write_b32 a227, 0\n"
    "s_waitcnt vmcnt(7)\n"
    "v_mfma_f32_16x16x32_bf16 v[32:35], v[232:235], a[28:31], v[32:35]\n"
    "s_movk_i32 s90, 512\n"
    "v_mfma_f32_16x16x32_bf16 v[36:39], v[236:239], a[28:31], v[36:39]\n"
    "v_accvgpr_write_b32 a228, 0\n"
    "v_mfma_f32_16x16x32_bf16 v[56:59], v[232:235], a[60:63], v[56:59]\n"
    "v_accvgpr_write_b32 a229, 0\n"
    "v_mfma_f32_16x16x32_bf16 v[60:63], v[236:239], a[60:63], v[60:63]\n"
    "v_accvgpr_write_b32 a230, 0\n"
    "v_mfma_f32_16x16x32_bf16 v[72:75], v[232:235], a[92:95], v[72:75]\n"
    "s_mov_b32 s4, 0x1c400000\n"
    "v_mfma_f32_16x16x32_bf16 v[76:79], v[236:239], a[92:95], v[76:79]\n"
    "v_accvgpr_write_b32 a231, 0\n"
    "v_cvt_pk_bf16_f32 v32, v32, v33\n"
    "v_cvt_pk_bf16_f32 v33, v34, v35\n"
    "v_cvt_pk_bf16_f32 v34, v36, v37\n"
    "v_cvt_pk_bf16_f32 v35, v38, v39\n"
    "ds_write_b128 v99, v[32:35] offset:1024\n"
    "buffer_store_dwordx4 v[32:35], v98, s[8:11], s35 offen offset:1024 sc1\n"
    "v_cvt_pk_bf16_f32 v56, v56, v57\n"
    "v_cvt_pk_bf16_f32 v57, v58, v59\n"
    "v_cvt_pk_bf16_f32 v58, v60, v61\n"
    "v_cvt_pk_bf16_f32 v59, v62, v63\n"
    "ds_write_b128 v99, v[56:59] offset:9216\n"
    "buffer_store_dwordx4 v[56:59], v98, s[8:11], s54 offen offset:1024 sc1\n"
    "v_cvt_pk_bf16_f32 v72, v72, v73\n"
    "v_cvt_pk_bf16_f32 v73, v74, v75\n"
    "v_cvt_pk_bf16_f32 v74, v76, v77\n"
    "v_cvt_pk_bf16_f32 v75, v78, v79\n"
    "ds_write_b128 v99, v[72:75] offset:50176\n"
    "buffer_store_dwordx4 v[72:75], v98, s[8:11], s60 offen offset:1024 sc1\n"
    "v_mov_b32_e32 v8, s4\n"
    "v_mov_b32_e32 v9, s4\n"
    "s_add_u32 m0, s43, 4096\n"
    "v_mov_b64_e32 v[10:11], v[8:9]\n"
    "v_mov_b64_e32 v[12:13], v[8:9]\n"
    "buffer_load_dwordx4 v104, s[8:11], s41 offen offset:0 sc1 lds\n"
    "v_mov_b64_e32 v[14:15], v[8:9]\n"
    "v_mov_b64_e32 v[16:17], v[8:9]\n"
    "buffer_load_dwordx4 v105, s[8:11], s41 offen offset:1024 sc1 lds\n"
    "v_mov_b64_e32 v[18:19], v[8:9]\n"
    "v_mov_b64_e32 v[28:29], v[8:9]\n"
    "buffer_load_dwordx4 v106, s[8:11], s41 offen offset:2048 sc1 lds\n"
    "v_mov_b64_e32 v[30:31], v[8:9]\n"
    "v_mov_b64_e32 v[32:33], v[8:9]\n"
    "buffer_load_dwordx4 v107, s[8:11], s41 offen offset:3072 sc1 lds\n"
    "v_mov_b64_e32 v[34:35], v[8:9]\n"
    "v_mov_b64_e32 v[36:37], v[8:9]\n"
    "s_add_u32 s41, s41, 0x8000\n"
    "v_mov_b64_e32 v[38:39], v[8:9]\n"
    "v_mov_b64_e32 v[48:49], v[8:9]\n"
    "v_mov_b64_e32 v[50:51], v[8:9]\n"
    "v_mov_b64_e32 v[52:53], v[8:9]\n"
    "v_mov_b64_e32 v[54:55], v[8:9]\n"
    "v_mov_b64_e32 v[56:57], v[8:9]\n"
    "v_mov_b64_e32 v[58:59], v[8:9]\n"
    "v_mov_b64_e32 v[68:69], v[8:9]\n"
    "v_mov_b64_e32 v[70:71], v[8:9]\n"
    "v_mov_b64_e32 v[72:73], v[8:9]\n"
    "v_mov_b64_e32 v[74:75], v[8:9]\n"
    "v_mov_b64_e32 v[76:77], v[8:9]\n"
    "s_cmp_lg_u32 s93, 3\n"
    "s_cbranch_scc1 .Lat_wn40\n"
    "s_call_b64 s[2:3], .Lat_w6\n"
    "s_call_b64 s[2:3], .Lat_w10\n"
    ".Lat_wn40:\n"
    "s_cmp_lg_u32 s93, 2\n"
    "s_cbranch_scc1 .Lat_wn41\n"
    "s_call_b64 s[2:3], .Lat_w7\n"
    "s_call_b64 s[2:3], .Lat_w11\n"
    ".Lat_wn41:\n"
    "s_cmp_lg_u32 s93, 1\n"
    "s_cbranch_scc1 .Lat_wn42\n"
    "s_call_b64 s[2:3], .Lat_w8\n"
    "s_call_b64 s[2:3], .Lat_w12\n"
    ".Lat_wn42:\n"
    "s_cmp_lg_u32 s93, 0\n"
    "s_cbranch_scc1 .Lat_wn43\n"
    "s_call_b64 s[2:3], .Lat_w9\n"
    "s_call_b64 s[2:3], .Lat_w13\n"
    ".Lat_wn43:\n"
    "s_waitcnt vmcnt(4)\n"
    "buffer_store_dword v84, v86, s[8:11], s58 offen offset:0 sc1\n"
    "v_mov_b64_e32 v[78:79], v[8:9]\n"
    "v_mov_b64_e32 v[88:89], v[8:9]\n"
    "s_add_u32 m0, s43, 8192\n"
    "v_mov_b64_e32 v[90:91], v[8:9]\n"
    "v_mov_b64_e32 v[92:93], v[8:9]\n"
    "buffer_load_dwordx4 v104, s[8:11], s40 offen offset:0 sc1 lds\n"
    "v_mov_b64_e32 v[94:95], v[8:9]\n"
    "v_mov_b64_e32 v[96:97], v[8:9]\n"
    "buffer_load_dwordx4 v105, s[8:11], s40 offen offset:1024 sc1 lds\n"
    "v_mov_b64_e32 v[98:99], v[8:9]\n"
    "v_mov_b64_e32 v[108:109], v[8:9]\n"
    "buffer_load_dwordx4 v106, s[8:11], s40 offen offset:2048 sc1 lds\n"
    "v_mov_b64_e32 v[110:111], v[8:9]\n"
    "v_mov_b64_e32 v[112:113], v[8:9]\n"
    "buffer_load_dwordx4 v107, s[8:11], s40 offen offset:3072 sc1 lds\n"
    "v_mov_b64_e32 v[114:115], v[8:9]\n"
    "v_mov_b64_e32 v[116:117], v[8:9]\n"
    "s_add_u32 s40, s40, 0x8000\n"
    "v_mov_b64_e32 v[118:119], v[8:9]\n"
    "v_mov_b64_e32 v[128:129], v[8:9]\n"
    "s_add_u32 m0, s43, 12288\n"
    "v_mov_b64_e32 v[130:131], v[8:9]\n"
    "v_mov_b64_e32 v[132:133], v[8:9]\n"
    "buffer_load_dwordx4 v104, s[8:11], s41 offen offset:0 sc1 lds\n"
    "v_mov_b64_e32 v[134:135], v[8:9]\n"
    "v_mov_b64_e32 v[136:137], v[8:9]\n"
    "buffer_load_dwordx4 v105, s[8:11], s41 offen offset:1024 sc1 lds\n"
    "v_mov_b64_e32 v[138:139], v[8:9]\n"
    "buffer_load_dwordx4 v106, s[8:11], s41 offen offset:2048 sc1 lds\n"
    "buffer_load_dwordx4 v107, s[8:11], s41 offen offset:3072 sc1 lds\n"
    "s_add_u32 s41, s41, 0x8000\n"
    ".Lat_p2:\n"
    "v_mov_b32_e32 v220, 0xc2ae0000\n"
    "v_mov_b32_e32 v221, 0xc2ae0000\n"
    "v_mov_b64_e32 v[222:223], v[220:221]\n"
    "v_mov_b64_e32 v[224:225], v[220:221]\n"
    "v_mov_b64_e32 v[226:227], v[220:221]\n"
    "v_mov_b64_e32 v[228:229], v[220:221]\n"
    "v_mov_b64_e32 v[230:231], v[220:221]\n"
    "v_mov_b64_e32 v[232:233], v[220:221]\n"
    "v_mov_b64_e32 v[234:235], v[220:221]\n"
    "v_mov_b64_e32 v[236:237], v[220:221]\n"
    "v_mov_b64_e32 v[238:239], v[220:221]\n"
    "s_andn2_b32 s47, s58, 0x600\n"
    "s_mov_b32 s44, 9\n"
    "s_waitcnt vmcnt(9)\n"
    "s_waitcnt lgkmcnt(0)\n"
    "s_barrier\n"
    "s_cmp_lg_u32 s49, 0\n"
    "s_cbranch_scc1 .Lat_maxpass\n"
    "ds_read_b128 a[0:3], v85 offset:0\n"
    "ds_read_b128 a[4:7], v85 offset:1024\n"
    "ds_read_b128 a[8:11], v85 offset:2048\n"
    "ds_read_b128 a[12:15], v85 offset:3072\n"
    "ds_read_b128 a[16:19], v85 offset:4096\n"
    "ds_read_b128 a[20:23], v85 offset:5120\n"
    "ds_read_b128 a[24:27], v85 offset:6144\n"
    "ds_read_b128 a[28:31], v85 offset:7168\n"
    "ds_read_b128 a[32:35], v255 offset:0\n"
    "ds_read_b128 a[36:39], v255 offset:1024\n"
    "ds_read_b128 a[40:43], v255 offset:2048\n"
    "ds_read_b128 a[44:47], v255 offset:3072\n"
    "ds_read_b128 a[48:51], v255 offset:4096\n"
    "ds_read_b128 a[52:55], v255 offset:5120\n"
    "ds_read_b128 a[56:59], v255 offset:6144\n"
    "ds_read_b128 a[60:63], v255 offset:7168\n"
    ".Lat_maxpass_ret:\n"
    "ds_read_b128 a[160:163], v46 offset:0\n"
    "ds_read_b128 a[164:167], v47 offset:0\n"
    "ds_read_b128 a[168:171], v46 offset:2048\n"
    "ds_read_b128 a[172:175], v47 offset:2048\n"
    "ds_read_b128 a[176:179], v46 offset:4096\n"
    "ds_read_b128 a[180:183], v47 offset:4096\n"
    "ds_read_b128 a[184:187], v46 offset:6144\n"
    "ds_read_b128 a[188:191], v47 offset:6144\n"
    "s_mul_i32 s48, s33, 81920\n"
    "s_add_u32 s48, s48, 11345920\n"
    "s_lshl_b32 s46, s93, 11\n"
    "s_add_u32 s48, s48, s46\n"
    "s_add_u32 s46, s46, 49152\n"
    "buffer_load_dword v254, v253, s[8:11], s47 offen offset:0 sc1\n"
    "s_waitcnt lgkmcnt(4)\n"
    "v_mfma_f32_16x16x32_bf16 v[212:215], a[160:163], a[0:3], v[220:223]\n"
    ".long 0xbedf00ff\n"
    ".Lat_w1:\n"
    "s_setpc_b64 s[2:3]\n"
    "s_and_b32 s45, s32, 1\n"
    "ds_read_b64_tr_b16 v[140:141], v4 offset:0\n"
    "v_mfma_f32_16x16x32_bf16 v[216:219], a[160:163], a[32:35], v[224:227]\n"
    "s_xor_b32 s45, s45, 9\n"
    "ds_read_b64_tr_b16 v[142:143], v5 offset:0\n"
    "v_mfma_f32_16x16x32_bf16 v[212:215], a[164:167], a[4:7], v[212:215]\n"
    "s_lshl_b32 s45, s45, 13\n"
    "ds_read_b64_tr_b16 v[144:145], v6 offset:0\n"
    "v_mfma_f32_16x16x32_bf16 v[216:219], a[164:167], a[36:39], v[216:219]\n"
    "s_add_u32 s45, s45, s48\n"
    "ds_read_b64_tr_b16 v[146:147], v7 offset:0\n"
    "v_mfma_f32_16x16x32_bf16 v[212:215], a[168:171], a[8:11], v[212:215]\n"
    "s_add_u32 s54, s32, 1\n"
    "ds_read_b64_tr_b16 v[148:149], v4 offset:2048\n"
    "v_mfma_f32_16x16x32_bf16 v[216:219], a[168:171], a[40:43], v[216:219]\n"
    "s_and_b32 s54, s54, 3\n"
    "ds_read_b64_tr_b16 v[150:151], v5 offset:2048\n"
    "v_mfma_f32_16x16x32_bf16 v[212:215], a[172:175], a[12:15], v[212:215]\n"
    "s_lshl_b32 s54, s54, 13\n"
    "ds_read_b64_tr_b16 v[152:153], v6 offset:2048\n"
    "v_mfma_f32_16x16x32_bf16 v[216:219], a[172:175], a[44:47], v[216:219]\n"
    "s_add_u32 s54, s54, s48\n"
    "ds_read_b64_tr_b16 v[154:155], v7 offset:2048\n"
    "s_waitcnt lgkmcnt(8)\n"
    "v_mfma_f32_16x16x32_bf16 v[212:215], a[176:179], a[16:19], v[212:215]\n"
    "s_add_u32 s55, s32, 2\n"
    "ds_read_b128 v[180:183], v240 offset:2048\n"
    "v_mfma_f32_16x16x32_bf16 v[216:219], a[176:179], a[48:51], v[216:219]\n"
    "s_and_b32 s55, s55, 3\n"
    "ds_read_b128 v[184:187], v241 offset:2048\n"
    "v_mfma_f32_16x16x32_bf16 v[212:215], a[180:183], a[20:23], v[212:215]\n"
    "s_lshl_b32 s55, s55, 13\n"
    "ds_read_b128 v[188:191], v240 offset:6144\n"
    "v_mfma_f32_16x16x32_bf16 v[216:219], a[180:183], a[52:55], v[216:219]\n"
    "s_add_u32 s55, s55, s48\n"
    "ds_read_b128 v[192:195], v241 offset:6144\n"
    "v_mfma_f32_16x16x32_bf16 v[212:215], a[184:187], a[24:27], v[212:215]\n"
    "s_add_u32 s60, s32, 3\n"
    "ds_read_b128 v[196:199], v242 offset:2048\n"
    "v_mfma_f32_16x16x32_bf16 v[216:219], a[184:187], a[56:59], v[216:219]\n"
    "s_and_b32 s60, s60, 3\n"
    "ds_read_b128 v[200:203], v243 offset:2048\n"
    "v_mfma_f32_16x16x32_bf16 v[212:215], a[188:191], a[28:31], v[212:215]\n"
    "s_lshl_b32 s60, s60, 13\n"
    "ds_read_b128 v[204:207], v242 offset:6144\n"
    "v_mfma_f32_16x16x32_bf16 v[216:219], a[188:191], a[60:63], v[216:219]\n"
    "s_add_u32 s60, s60, s48\n"
    "ds_read_b128 v[208:211], v243 offset:6144\n"
    "s_lshr_b32 s61, s32, 1\n"
    "s_or_b32 s61, s61, s50\n"
    "s_sub_u32 s61, s61, 1\n"
    "s_add_u32 m0, s46, 65536\n"
    "s_waitcnt vmcnt(0)\n"
    "v_cmp_ne_u32_e32 vcc, s39, v254\n"
    "ds_read_b128 a[160:163], v240 offset:0\n"
    "ds_read_b128 a[164:167], v241 offset:0\n"
    "ds_read_b128 a[168:171], v240 offset:4096\n"
    "ds_read_b128 a[172:175], v241 offset:4096\n"
    "ds_read_b128 a[176:179], v242 offset:0\n"
    "s_cbranch_vccz .Lat_qpoll_ret\n"
    ".Lat_qpoll:\n"
    "s_mov_b32 s53, 0x400000\n"
    ".Lat_qpoll_l:\n"
    "s_sleep 0\n"
    "buffer_load_dword v254, v253, s[8:11], s47 offen offset:0 sc1\n"
    "s_waitcnt vmcnt(0)\n"
    "v_cmp_ne_u32_e32 vcc, s39, v254\n"
    "s_sub_u32 s53, s53, 1\n"
    "s_cbranch_scc1 .Lat_qpoll_ret\n"
    "s_nop 2\n"
    "s_cbranch_vccnz .Lat_qpoll_l\n"
    ".Lat_qpoll_ret:\n"
    "buffer_load_dwordx4 v250, s[8:11], s45 offen offset:0 sc1 lds\n"
    "buffer_load_dwordx4 v250, s[8:11], s45 offen offset:1024 sc1 lds\n"
    "s_add_u32 m0, s46, 8192\n"
    "ds_read_b128 a[180:183], v243 offset:0\n"
    "s_nop 0\n"
    "ds_read_b128 a[184:187], v242 offset:4096\n"
    "buffer_load_dwordx4 v250, s[8:11], s54 offen offset:0 sc1 lds\n"
    "ds_read_b128 a[188:191], v243 offset:4096\n"
    "buffer_load_dwordx4 v250, s[8:11], s54 offen offset:1024 sc1 lds\n"
    "v_exp_f32_e32 v212, v212\n"
    "s_add_u32 m0, s46, 73728\n"
    "v_exp_f32_e32 v213, v213\n"
    "s_add_u32 s54, s54, 0x8000\n"
    "v_exp_f32_e32 v214, v214\n"
    "buffer_load_dwordx4 v250, s[8:11], s54 offen offset:0 sc1 lds\n"
    "v_exp_f32_e32 v215, v215\n"
    "buffer_load_dwordx4 v250, s[8:11], s54 offen offset:1024 sc1 lds\n"
    "v_exp_f32_e32 v216, v216\n"
    "s_add_u32 m0, s46, 32768\n"
    "v_exp_f32_e32 v217, v217\n"
    "s_nop 0\n"
    "v_exp_f32_e32 v218, v218\n"
    "buffer_load_dwordx4 v250, s[8:11], s55 offen offset:0 sc1 lds\n"
    "v_exp_f32_e32 v219, v219\n"
    "buffer_load_dwordx4 v250, s[8:11], s55 offen offset:1024 sc1 lds\n"
    "v_cvt_pk_bf16_f32 v212, v212, v213\n"
    "v_cvt_pk_bf16_f32 v213, v214, v215\n"
    "v_cvt_pk_bf16_f32 v216, v216, v217\n"
    "v_cvt_pk_bf16_f32 v217, v218, v219\n"
    "v_and_b32_e32 v216, s61, v216\n"
    "v_and_b32_e32 v217, s61, v217\n"
    "s_waitcnt lgkmcnt(15)\n"
    "v_mfma_f32_16x16x16_bf16 a[236:239], v[140:141], v[212:213], 0\n"
    "s_add_u32 m0, s46, 98304\n"
    "v_mfma_f32_16x16x16_bf16 v[0:3], v[142:143], v[212:213], v[136:139]\n"
    "s_add_u32 s55, s55, 0x8000\n"
    "v_mfma_f32_16x16x16_bf16 v[20:23], v[144:145], v[212:213], v[136:139]\n"
    "buffer_load_dwordx4 v250, s[8:11], s55 offen offset:0 sc1 lds\n"
    "v_mfma_f32_16x16x16_bf16 v[40:43], v[146:147], v[212:213], v[136:139]\n"
    "buffer_load_dwordx4 v250, s[8:11], s55 offen offset:1024 sc1 lds\n"
    "v_mfma_f32_16x16x16_bf16 v[60:63], v[148:149], v[212:213], v[136:139]\n"
    "s_add_u32 m0, s46, 40960\n"
    "v_mfma_f32_16x16x16_bf16 v[80:83], v[150:151], v[212:213], v[136:139]\n"
    "buffer_load_dwordx4 v250, s[8:11], s60 offen offset:0 sc1 lds\n"
    "v_mfma_f32_16x16x16_bf16 v[100:103], v[152:153], v[212:213], v[136:139]\n"
    "buffer_load_dwordx4 v250, s[8:11], s60 offen offset:1024 sc1 lds\n"
    "v_mfma_f32_16x16x16_bf16 v[120:123], v[154:155], v[212:213], v[136:139]\n"
    "s_add_u32 m0, s46, 106496\n"
    "v_mfma_f32_16x16x16_bf16 a[240:243], v[140:141], v[216:217], 0\n"
    "s_add_u32 s60, s60, 0x8000\n"
    "v_mfma_f32_16x16x16_bf16 v[4:7], v[142:143], v[216:217], v[136:139]\n"
    "buffer_load_dwordx4 v250, s[8:11], s60 offen offset:0 sc1 lds\n"
    "v_mfma_f32_16x16x16_bf16 v[24:27], v[144:145], v[216:217], v[136:139]\n"
    "buffer_load_dwordx4 v250, s[8:11], s60 offen offset:1024 sc1 lds\n"
    "v_mfma_f32_16x16x16_bf16 v[44:47], v[146:147], v[216:217], v[136:139]\n"
    "v_mfma_f32_16x16x16_bf16 v[64:67], v[148:149], v[216:217], v[136:139]\n"
    "buffer_load_dword v253, v250, s[8:11], s59 offen offset:0 sc1\n"
    "v_mfma_f32_16x16x16_bf16 v[84:87], v[150:151], v[216:217], v[136:139]\n"
    "v_mfma_f32_16x16x16_bf16 v[104:107], v[152:153], v[216:217], v[136:139]\n"
    "v_mfma_f32_16x16x16_bf16 v[124:127], v[154:155], v[216:217], v[136:139]\n"
    "s_waitcnt lgkmcnt(0)\n"
    "buffer_load_dword v254, v252, s[8:11], s59 offen offset:0 sc1\n"
    "v_mfma_f32_16x16x32_bf16 v[140:143], a[160:163], a[0:3], v[220:223]\n"
    "v_mfma_f32_16x16x32_bf16 v[140:143], a[164:167], a[4:7], v[140:143]\n"
    "v_mfma_f32_16x16x32_bf16 v[140:143], a[168:171], a[8:11], v[140:143]\n"
    "v_mfma_f32_16x16x32_bf16 v[140:143], a[172:175], a[12:15], v[140:143]\n"
    "v_mfma_f32_16x16x32_bf16 v[140:143], a[176:179], a[16:19], v[140:143]\n"
    "v_mfma_f32_16x16x32_bf16 v[140:143], a[180:183], a[20:23], v[140:143]\n"
    "v_mfma_f32_16x16x32_bf16 v[140:143], a[184:187], a[24:27], v[140:143]\n"
    "v_mfma_f32_16x16x32_bf16 v[140:143], a[188:191], a[28:31], v[140:143]\n"
    "v_mfma_f32_16x16x32_bf16 v[144:147], v[180:183], a[0:3], v[220:223]\n"
    "v_mfma_f32_16x16x32_bf16 v[144:147], v[184:187], a[4:7], v[144:147]\n"
    "s_waitcnt vmcnt(10)\n"
    "s_barrier\n"
    "v_mfma_f32_16x16x32_bf16 v[144:147], v[188:191], a[8:11], v[144:147]\n"
    "s_mov_b32 m0, s42\n"
    "ds_read_b128 a[32:35], v255 offset:0\n"
    "v_exp_f32_e32 v140, v140\n"
    "v_mfma_f32_16x16x32_bf16 v[144:147], v[192:195], a[12:15], v[144:147]\n"
    "ds_read_b128 a[36:39], v255 offset:1024\n"
    "v_exp_f32_e32 v141, v141\n"
    "v_mfma_f32_16x16x32_bf16 v[144:147], v[196:199], a[16:19], v[144:147]\n"
    "ds_read_b128 a[40:43], v255 offset:2048\n"
    "ds_read_b128 a[44:47], v255 offset:3072\n"
    "v_mfma_f32_16x16x32_bf16 v[144:147], v[200:203], a[20:23], v[144:147]\n"
    "ds_read_b128 a[48:51], v255 offset:4096\n"
    "ds_read_b128 a[52:55], v255 offset:5120\n"
    "v_mfma_f32_16x16x32_bf16 v[144:147], v[204:207], a[24:27], v[144:147]\n"
    "ds_read_b128 a[56:59], v255 offset:6144\n"
    "ds_read_b128 a[60:63], v255 offset:7168\n"
    "v_mfma_f32_16x16x32_bf16 v[144:147], v[208:211], a[28:31], v[144:147]\n"
    "s_waitcnt lgkmcnt(4)\n"
    "v_exp_f32_e32 v142, v142\n"
    "v_mfma_f32_16x16x32_bf16 v[148:151], a[160:163], a[32:35], v[224:227]\n"
    "ds_read_b128 a[64:67], v255 offset:8192\n"
    "v_exp_f32_e32 v143, v143\n"
    "v_mfma_f32_16x16x32_bf16 v[148:151], a[164:167], a[36:39], v[148:151]\n"
    "ds_read_b128 a[68:71], v255 offset:9216\n"
    "v_cvt_pk_bf16_f32 v140, v140, v141\n"
    "v_mfma_f32_16x16x32_bf16 v[148:151], a[168:171], a[40:43], v[148:151]\n"
    "ds_read_b128 a[72:75], v255 offset:10240\n"
    "v_cvt_pk_bf16_f32 v141, v142, v143\n"
    "v_mfma_f32_16x16x32_bf16 v[148:151], a[172:175], a[44:47], v[148:151]\n"
    "s_waitcnt lgkmcnt(3)\n"
    "v_exp_f32_e32 v144, v144\n"
    "v_mfma_f32_16x16x32_bf16 v[148:151], a[176:179], a[48:51], v[148:151]\n"
    "ds_read_b128 a[76:79], v255 offset:11264\n"
    "ds_write_addtid_b32 v140 offset:0\n"
    "v_mfma_f32_16x16x32_bf16 v[148:151], a[180:183], a[52:55], v[148:151]\n"
    "ds_read_b128 a[80:83], v255 offset:12288\n"
    "v_exp_f32_e32 v145, v145\n"
    "v_mfma_f32_16x16x32_bf16 v[148:151], a[184:187], a[56:59], v[148:151]\n"
    "v_exp_f32_e32 v146, v146\n"
    "v_mfma_f32_16x16x32_bf16 v[148:151], a[188:191], a[60:63], v[148:151]\n"
    "ds_read_b128 a[84:87], v255 offset:13312\n"
    "v_exp_f32_e32 v147, v147\n"
    "v_mfma_f32_16x16x32_bf16 v[152:155], v[180:183], a[32:35], v[224:227]\n"
    "ds_read_b128 a[88:91], v255 offset:14336\n"
    "v_cvt_pk_bf16_f32 v142, v144, v145\n"
    "v_mfma_f32_16x16x32_bf16 v[152:155], v[184:187], a[36:39], v[152:155]\n"
    "ds_read_b128 a[92:95], v255 offset:15360\n"
    "v_cvt_pk_bf16_f32 v143, v146, v147\n"
    "v_mfma_f32_16x16x32_bf16 v[152:155], v[188:191], a[40:43], v[152:155]\n"
    "v_exp_f32_e32 v148, v148\n"
    "v_mfma_f32_16x16x32_bf16 v[152:155], v[192:195], a[44:47], v[152:155]\n"
    "v_exp_f32_e32 v149, v149\n"
    "v_mfma_f32_16x16x32_bf16 v[152:155], v[196:199], a[48:51], v[152:155]\n"
    "v_exp_f32_e32 v150, v150\n"
    "v_mfma_f32_16x16x32_bf16 v[152:155], v[200:203], a[52:55], v[152:155]\n"
    "v_exp_f32_e32 v151, v151\n"
    "v_mfma_f32_16x16x32_bf16 v[152:155], v[204:207], a[56:59], v[152:155]\n"
    "s_waitcnt vmcnt(2)\n"
    "s_barrier\n"
    "v_mfma_f32_16x16x32_bf16 v[152:155], v[208:211], a[60:63], v[152:155]\n"
    "s_waitcnt lgkmcnt(5)\n"
    "ds_read_b128 a[96:99], v255 offset:32768\n"
    "ds_write_addtid_b32 v141 offset:256\n"
    "v_mfma_f32_16x16x32_bf16 v[156:159], a[160:163], a[64:67], v[228:231]\n"
    "ds_read_b128 a[100:103], v255 offset:33792\n"
    "ds_write_addtid_b32 v142 offset:512\n"
    "v_mfma_f32_16x16x32_bf16 v[156:159], a[164:167], a[68:71], v[156:159]\n"
    "ds_read_b128 a[104:107], v255 offset:34816\n"
    "v_mfma_f32_16x16x32_bf16 v[156:159], a[168:171], a[72:75], v[156:159]\n"
    "ds_read_b128 a[108:111], v255 offset:35840\n"
    "v_mfma_f32_16x16x32_bf16 v[156:159], a[172:175], a[76:79], v[156:159]\n"
    "s_waitcnt lgkmcnt(6)\n"
    "ds_read_b128 a[112:115], v255 offset:36864\n"
    "v_cvt_pk_bf16_f32 v148, v148, v149\n"
    "v_mfma_f32_16x16x32_bf16 v[156:159], a[176:179], a[80:83], v[156:159]\n"
    "ds_read_b128 a[116:119], v255 offset:37888\n"
    "v_cvt_pk_bf16_f32 v149, v150, v151\n"
    "v_mfma_f32_16x16x32_bf16 v[156:159], a[180:183], a[84:87], v[156:159]\n"
    "ds_read_b128 a[120:123], v255 offset:38912\n"
    "v_exp_f32_e32 v152, v152\n"
    "v_mfma_f32_16x16x32_bf16 v[156:159], a[184:187], a[88:91], v[156:159]\n"
    "ds_read_b128 a[124:127], v255 offset:39936\n"
    "v_exp_f32_e32 v153, v153\n"
    "v_mfma_f32_16x16x32_bf16 v[156:159], a[188:191], a[92:95], v[156:159]\n"
    "ds_read_b128 a[128:131], v255 offset:40960\n"
    "v_exp_f32_e32 v154, v154\n"
    "v_mfma_f32_16x16x32_bf16 v[160:163], v[180:183], a[64:67], v[228:231]\n"
    "ds_read_b128 a[132:135], v255 offset:41984\n"
    "v_exp_f32_e32 v155, v155\n"
    "v_mfma_f32_16x16x32_bf16 v[160:163], v[184:187], a[68:71], v[160:163]\n"
    "ds_read_b128 a[136:139], v255 offset:43008\n"
    "v_cvt_pk_bf16_f32 v150, v152, v153\n"
    "v_mfma_f32_16x16x32_bf16 v[160:163], v[188:191], a[72:75], v[160:163]\n"
    "ds_read_b128 a[140:143], v255 offset:44032\n"
    "v_cvt_pk_bf16_f32 v151, v154, v155\n"
    "v_mfma_f32_16x16x32_bf16 v[160:163], v[192:195], a[76:79], v[160:163]\n"
    "ds_read_b128 a[144:147], v255 offset:45056\n"
    "ds_read_b128 a[148:151], v255 offset:46080\n"
    "v_mfma_f32_16x16x32_bf16 v[160:163], v[196:199], a[80:83], v[160:163]\n"
    "ds_read_b128 a[152:155], v255 offset:47104\n"
    "ds_read_b128 a[156:159], v255 offset:48128\n"
    "v_mfma_f32_16x16x32_bf16 v[160:163], v[200:203], a[84:87], v[160:163]\n"
    "s_waitcnt vmcnt(0)\n"
    "ds_write_addtid_b32 v148 offset:1024\n"
    "v_xor_b32_e32 v254, s39, v254\n"
    "v_xor_b32_e32 v253, s39, v253\n"
    "v_mfma_f32_16x16x32_bf16 v[160:163], v[204:207], a[88:91], v[160:163]\n"
    "ds_write_addtid_b32 v149 offset:1280\n"
    "v_or_b32_e32 v253, v253, v254\n"
    "v_cmp_ne_u32_e32 vcc, 0, v253\n"
    "v_mfma_f32_16x16x32_bf16 v[160:163], v[208:211], a[92:95], v[160:163]\n"
    "s_waitcnt lgkmcnt(14)\n"
    "v_exp_f32_e32 v156, v156\n"
    "v_mfma_f32_16x16x32_bf16 v[164:167], a[160:163], a[96:99], v[232:235]\n"
    "v_exp_f32_e32 v157, v157\n"
    "v_mfma_f32_16x16x32_bf16 v[164:167], a[164:167], a[100:103], v[164:167]\n"
    "v_exp_f32_e32 v158, v158\n"
    "v_mfma_f32_16x16x32_bf16 v[164:167], a[168:171], a[104:107], v[164:167]\n"
    "s_cbranch_vccnz .Lat_hpoll\n"
    ".Lat_hpoll_ret:\n"
    "s_waitcnt lgkmcnt(2)\n"
    "s_waitcnt vmcnt(17)\n"
    "s_barrier\n"
    "v_mfma_f32_16x16x32_bf16 v[164:167], a[172:175], a[108:111], v[164:167]\n"
    "v_exp_f32_e32 v159, v159\n"
    "v_mfma_f32_16x16x32_bf16 v[164:167], a[176:179], a[112:115], v[164:167]\n"
    "s_add_u32 m0, s43, 16384\n"
    "v_cvt_pk_bf16_f32 v156, v156, v157\n"
    "v_mfma_f32_16x16x32_bf16 v[164:167], a[180:183], a[116:119], v[164:167]\n"
    "buffer_load_dwordx4 v248, s[8:11], s40 offen offset:0 sc1 lds\n"
    "v_cvt_pk_bf16_f32 v157, v158, v159\n"
    "v_mfma_f32_16x16x32_bf16 v[164:167], a[184:187], a[120:123], v[164:167]\n"
    "v_exp_f32_e32 v160, v160\n"
    "v_mfma_f32_16x16x32_bf16 v[164:167], a[188:191], a[124:127], v[164:167]\n"
    "v_exp_f32_e32 v161, v161\n"
    "v_mfma_f32_16x16x32_bf16 v[168:171], v[180:183], a[96:99], v[232:235]\n"
    "buffer_load_dwordx4 v248, s[8:11], s40 offen offset:1024 sc1 lds\n"
    "v_mfma_f32_16x16x32_bf16 v[168:171], v[184:187], a[100:103], v[168:171]\n"
    "v_exp_f32_e32 v162, v162\n"
    "v_mfma_f32_16x16x32_bf16 v[168:171], v[188:191], a[104:107], v[168:171]\n"
    "v_exp_f32_e32 v163, v163\n"
    "v_mfma_f32_16x16x32_bf16 v[168:171], v[192:195], a[108:111], v[168:171]\n"
    "buffer_load_dwordx4 v248, s[8:11], s40 offen offset:2048 sc1 lds\n"
    "v_mfma_f32_16x16x32_bf16 v[168:171], v[196:199], a[112:115], v[168:171]\n"
    "v_cvt_pk_bf16_f32 v158, v160, v161\n"
    "v_cvt_pk_bf16_f32 v159, v162, v163\n"
    "v_mfma_f32_16x16x32_bf16 v[168:171], v[200:203], a[116:119], v[168:171]\n"
    "v_exp_f32_e32 v164, v164\n"
    "v_mfma_f32_16x16x32_bf16 v[168:171], v[204:207], a[120:123], v[168:171]\n"
    "buffer_load_dwordx4 v248, s[8:11], s40 offen offset:3072 sc1 lds\n"
    "v_mfma_f32_16x16x32_bf16 v[168:171], v[208:211], a[124:127], v[168:171]\n"
    "s_add_u32 s40, s40, 0x8000\n"
    "v_exp_f32_e32 v165, v165\n"
    "v_mfma_f32_16x16x32_bf16 v[172:175], a[160:163], a[128:131], v[236:239]\n"
    "s_add_u32 m0, s43, 20480\n"
    "v_exp_f32_e32 v166, v166\n"
    "v_mfma_f32_16x16x32_bf16 v[172:175], a[164:167], a[132:135], v[172:175]\n"
    "buffer_load_dwordx4 v248, s[8:11], s41 offen offset:0 sc1 lds\n"
    "ds_read_b128 a[160:163], v240 offset:8192\n"
    "v_mfma_f32_16x16x32_bf16 v[172:175], a[168:171], a[136:139], v[172:175]\n"
    "ds_read_b128 a[164:167], v241 offset:8192\n"
    "v_mfma_f32_16x16x32_bf16 v[172:175], a[172:175], a[140:143], v[172:175]\n"
    "ds_read_b128 a[168:171], v240 offset:12288\n"
    "v_mfma_f32_16x16x32_bf16 v[172:175], a[176:179], a[144:147], v[172:175]\n"
    "buffer_load_dwordx4 v248, s[8:11], s41 offen offset:1024 sc1 lds\n"
    "ds_read_b128 a[172:175], v241 offset:12288\n"
    "v_mfma_f32_16x16x32_bf16 v[172:175], a[180:183], a[148:151], v[172:175]\n"
    "ds_read_b128 a[176:179], v242 offset:8192\n"
    "v_exp_f32_e32 v167, v167\n"
    "v_mfma_f32_16x16x32_bf16 v[172:175], a[184:187], a[152:155], v[172:175]\n"
    "ds_read_b128 a[180:183], v243 offset:8192\n"
    "v_cvt_pk_bf16_f32 v164, v164, v165\n"
    "v_mfma_f32_16x16x32_bf16 v[172:175], a[188:191], a[156:159], v[172:175]\n"
    "buffer_load_dwordx4 v248, s[8:11], s41 offen offset:2048 sc1 lds\n"
    "v_cvt_pk_bf16_f32 v165, v166, v167\n"
    "v_mfma_f32_16x16x32_bf16 v[176:179], v[180:183], a[128:131], v[236:239]\n"
    "v_exp_f32_e32 v168, v168\n"
    "v_mfma_f32_16x16x32_bf16 v[176:179], v[184:187], a[132:135], v[176:179]\n"
    "v_exp_f32_e32 v169, v169\n"
    "v_mfma_f32_16x16x32_bf16 v[176:179], v[188:191], a[136:139], v[176:179]\n"
    "buffer_load_dwordx4 v248, s[8:11], s41 offen offset:3072 sc1 lds\n"
    "v_mfma_f32_16x16x32_bf16 v[176:179], v[192:195], a[140:143], v[176:179]\n"
    "s_mov_b32 m0, s42\n"
    "s_add_u32 s41, s41, 0x8000\n"
    "v_exp_f32_e32 v172, v172\n"
    "v_mfma_f32_16x16x32_bf16 v[176:179], v[196:199], a[144:147], v[176:179]\n"
    "ds_write_addtid_b32 v156 offset:2048\n"
    "v_exp_f32_e32 v173, v173\n"
    "v_mfma_f32_16x16x32_bf16 v[176:179], v[200:203], a[148:151], v[176:179]\n"
    "ds_write_addtid_b32 v157 offset:2304\n"
    "v_exp_f32_e32 v174, v174\n"
    "v_mfma_f32_16x16x32_bf16 v[176:179], v[204:207], a[152:155], v[176:179]\n"
    "ds_write_addtid_b32 v164 offset:3072\n"
    "v_exp_f32_e32 v175, v175\n"
    "v_mfma_f32_16x16x32_bf16 v[176:179], v[208:211], a[156:159], v[176:179]\n"
    "ds_write_addtid_b32 v165 offset:3328\n"
    "v_cvt_pk_bf16_f32 v172, v172, v173\n"
    "v_cvt_pk_bf16_f32 v173, v174, v175\n"
    "ds_write_addtid_b32 v172 offset:4096\n"
    "ds_write_addtid_b32 v173 offset:4352\n"
    "s_mul_i32 s45, s33, 320\n"
    "s_lshl_b32 s46, s93, 7\n"
    "s_add_u32 s45, s45, s46\n"
    "buffer_load_dword v254, off, s[24:27], s45\n"
    "s_andn2_b32 s45, s34, 3\n"
    "s_add_u32 s45, s45, s93\n"
    "s_mul_i32 s45, s45, 101376\n"
    "s_add_u32 s45, s45, 16588800\n"
    "buffer_load_dword v254, off, s[8:11], s45\n"
    "s_waitcnt lgkmcnt(10)\n"
    "s_branch .Lat_in\n"
    ".fill 239, 4, 0xbf800000\n"
    ".long 0xbedf00ff\n"
    ".Lat_w2:\n"
    "s_setpc_b64 s[2:3]\n"
    ".Lat_loop:\n"
    "v_mfma_f32_16x16x32_bf16 v[212:215], a[160:163], a[128:131], v[236:239]\n"
    "v_mfma_f32_16x16x32_bf16 v[212:215], a[164:167], a[132:135], v[212:215]\n"
    "v_exp_f32_e32 v170, v170\n"
    "v_mfma_f32_16x16x32_bf16 v[204:207], a[164:167], a[100:103], v[232:235]\n"
    "ds_write_addtid_b32 v143 offset:768\n"
    "v_mfma_f32_16x16x32_bf16 v[204:207], a[160:163], a[96:99], v[204:207]\n"
    "ds_read_b128 a[184:187], v242 offset:28672\n"
    "v_mfma_f32_16x16x32_bf16 v[196:199], a[160:163], a[64:67], v[228:231]\n"
    "v_exp_f32_e32 v171, v171\n"
    "v_mfma_f32_16x16x32_bf16 v[196:199], a[164:167], a[68:71], v[196:199]\n"
    "ds_write_addtid_b32 v150 offset:1536\n"
    "v_mfma_f32_16x16x32_bf16 v[188:191], a[164:167], a[36:39], v[224:227]\n"
    "v_cvt_pk_bf16_f32 v166, v168, v169\n"
    "v_mfma_f32_16x16x32_bf16 v[188:191], a[160:163], a[32:35], v[188:191]\n"
    "v_mfma_f32_16x16x32_bf16 v[180:183], a[160:163], a[0:3], v[220:223]\n"
    "ds_write_addtid_b32 v151 offset:1792\n"
    "v_mfma_f32_16x16x32_bf16 v[180:183], a[164:167], a[4:7], v[180:183]\n"
    "s_waitcnt lgkmcnt(10)\n"
    "v_exp_f32_e32 v176, v176\n"
    "v_mfma_f32_16x16x32_bf16 v[180:183], a[168:171], a[8:11], v[180:183]\n"
    "ds_read_b128 a[188:191], v243 offset:28672\n"
    "v_mfma_f32_16x16x32_bf16 v[180:183], a[172:175], a[12:15], v[180:183]\n"
    "v_exp_f32_e32 v177, v177\n"
    "v_mfma_f32_16x16x32_bf16 v[188:191], a[172:175], a[44:47], v[188:191]\n"
    "ds_write_addtid_b32 v158 offset:2560\n"
    "v_mfma_f32_16x16x32_bf16 v[188:191], a[168:171], a[40:43], v[188:191]\n"
    "ds_read_b128 a[160:163], v240 offset:26624\n"
    "v_mfma_f32_16x16x32_bf16 v[196:199], a[168:171], a[72:75], v[196:199]\n"
    "v_cvt_pk_bf16_f32 v167, v170, v171\n"
    "v_mfma_f32_16x16x32_bf16 v[196:199], a[172:175], a[76:79], v[196:199]\n"
    "ds_write_addtid_b32 v159 offset:2816\n"
    "v_mfma_f32_16x16x32_bf16 v[204:207], a[172:175], a[108:111], v[204:207]\n"
    "v_exp_f32_e32 v178, v178\n"
    "v_mfma_f32_16x16x32_bf16 v[204:207], a[168:171], a[104:107], v[204:207]\n"
    "ds_write_addtid_b32 v166 offset:3584\n"
    "v_mfma_f32_16x16x32_bf16 v[212:215], a[168:171], a[136:139], v[212:215]\n"
    "v_mfma_f32_16x16x32_bf16 v[212:215], a[172:175], a[140:143], v[212:215]\n"
    "s_waitcnt lgkmcnt(10)\n"
    "v_exp_f32_e32 v179, v179\n"
    "v_mfma_f32_16x16x32_bf16 v[212:215], a[176:179], a[144:147], v[212:215]\n"
    "ds_read_b128 a[164:167], v241 offset:26624\n"
    "v_mfma_f32_16x16x32_bf16 v[212:215], a[180:183], a[148:151], v[212:215]\n"
    "v_cvt_pk_bf16_f32 v174, v176, v177\n"
    "v_mfma_f32_16x16x32_bf16 v[204:207], a[180:183], a[116:119], v[204:207]\n"
    "ds_write_addtid_b32 v167 offset:3840\n"
    "v_mfma_f32_16x16x32_bf16 v[204:207], a[176:179], a[112:115], v[204:207]\n"
    "ds_read_b128 a[168:171], v240 offset:30720\n"
    "v_mfma_f32_16x16x32_bf16 v[196:199], a[176:179], a[80:83], v[196:199]\n"
    "v_cvt_pk_bf16_f32 v175, v178, v179\n"
    "v_mfma_f32_16x16x32_bf16 v[196:199], a[180:183], a[84:87], v[196:199]\n"
    "ds_write_addtid_b32 v174 offset:4608\n"
    "v_mfma_f32_16x16x32_bf16 v[188:191], a[180:183], a[52:55], v[188:191]\n"
    "ds_write_addtid_b32 v175 offset:4864\n"
    "v_mfma_f32_16x16x32_bf16 v[188:191], a[176:179], a[48:51], v[188:191]\n"
    "v_mfma_f32_16x16x32_bf16 v[180:183], a[176:179], a[16:19], v[180:183]\n"
    "ds_read_b128 a[172:175], v241 offset:30720\n"
    "v_mfma_f32_16x16x32_bf16 v[180:183], a[180:183], a[20:23], v[180:183]\n"
    "s_waitcnt lgkmcnt(10)\n"
    "s_waitcnt lgkmcnt(1)\n"
    "v_mfma_f32_16x16x32_bf16 v[180:183], a[184:187], a[24:27], v[180:183]\n"
    "s_barrier\n"
    "v_mfma_f32_16x16x32_bf16 v[180:183], a[188:191], a[28:31], v[180:183]\n"
    "s_add_u32 m0, s43, 8192\n"
    "v_mfma_f32_16x16x32_bf16 v[188:191], a[188:191], a[60:63], v[188:191]\n"
    "ds_read_b128 a[176:179], v242 offset:26624\n"
    "v_mfma_f32_16x16x32_bf16 v[188:191], a[184:187], a[56:59], v[188:191]\n"
    "buffer_load_dwordx4 v248, s[8:11], s40 offen offset:0 sc1 lds\n"
    "v_mfma_f32_16x16x32_bf16 v[196:199], a[184:187], a[88:91], v[196:199]\n"
    "ds_read2st64_b32 v[144:145], v249 offset0:0 offset1:1\n"
    "v_mfma_f32_16x16x32_bf16 v[196:199], a[188:191], a[92:95], v[196:199]\n"
    "v_mfma_f32_16x16x32_bf16 v[204:207], a[188:191], a[124:127], v[204:207]\n"
    "ds_read2st64_b32 v[146:147], v249 offset0:2 offset1:3\n"
    "v_mfma_f32_16x16x32_bf16 v[204:207], a[184:187], a[120:123], v[204:207]\n"
    "buffer_load_dwordx4 v248, s[8:11], s40 offen offset:1024 sc1 lds\n"
    "v_mfma_f32_16x16x32_bf16 v[212:215], a[184:187], a[152:155], v[212:215]\n"
    "v_mfma_f32_16x16x32_bf16 v[212:215], a[188:191], a[156:159], v[212:215]\n"
    "v_mfma_f32_16x16x32_bf16 v[216:219], a[160:163], a[128:131], v[236:239]\n"
    "ds_read_b128 a[180:183], v243 offset:26624\n"
    "v_mfma_f32_16x16x32_bf16 v[216:219], a[164:167], a[132:135], v[216:219]\n"
    "buffer_load_dwordx4 v248, s[8:11], s40 offen offset:2048 sc1 lds\n"
    "v_mfma_f32_16x16x32_bf16 v[208:211], a[164:167], a[100:103], v[232:235]\n"
    "ds_read2st64_b32 v[152:153], v249 offset0:4 offset1:5\n"
    "v_mfma_f32_16x16x32_bf16 v[208:211], a[160:163], a[96:99], v[208:211]\n"
    "ds_read_b128 a[184:187], v242 offset:30720\n"
    "v_mfma_f32_16x16x32_bf16 v[200:203], a[160:163], a[64:67], v[228:231]\n"
    "ds_read2st64_b32 v[154:155], v249 offset0:6 offset1:7\n"
    "v_mfma_f32_16x16x32_bf16 v[200:203], a[164:167], a[68:71], v[200:203]\n"
    "buffer_load_dwordx4 v248, s[8:11], s40 offen offset:3072 sc1 lds\n"
    "v_mfma_f32_16x16x32_bf16 v[192:195], a[164:167], a[36:39], v[224:227]\n"
    "s_add_u32 m0, s43, 12288\n"
    "v_exp_f32_e32 v180, v180\n"
    "v_mfma_f32_16x16x32_bf16 v[192:195], a[160:163], a[32:35], v[192:195]\n"
    "s_add_u32 s40, s40, 0x8000\n"
    "v_exp_f32_e32 v181, v181\n"
    "v_mfma_f32_16x16x32_bf16 v[184:187], a[160:163], a[0:3], v[220:223]\n"
    "ds_read2st64_b32 v[160:161], v249 offset0:8 offset1:9\n"
    "v_mfma_f32_16x16x32_bf16 v[184:187], a[164:167], a[4:7], v[184:187]\n"
    "s_waitcnt lgkmcnt(8)\n"
    "buffer_load_dwordx4 v248, s[8:11], s41 offen offset:0 sc1 lds\n"
    "v_mfma_f32_16x16x32_bf16 v[184:187], a[168:171], a[8:11], v[184:187]\n"
    ".long 0xbedf00ff\n"
    ".Lat_w3:\n"
    "s_setpc_b64 s[2:3]\n"
    "ds_read_b128 a[188:191], v243 offset:30720\n"
    "v_mfma_f32_16x16x32_bf16 v[184:187], a[172:175], a[12:15], v[184:187]\n"
    "ds_read_b64_tr_b16 a[160:161], v244 offset:16384\n"
    "v_mfma_f32_16x16x32_bf16 v[192:195], a[172:175], a[44:47], v[192:195]\n"
    "ds_read2st64_b32 v[162:163], v249 offset0:10 offset1:11\n"
    "v_mfma_f32_16x16x32_bf16 v[192:195], a[168:171], a[40:43], v[192:195]\n"
    "buffer_load_dwordx4 v248, s[8:11], s41 offen offset:1024 sc1 lds\n"
    "v_mfma_f32_16x16x32_bf16 v[200:203], a[168:171], a[72:75], v[200:203]\n"
    "ds_read_b64_tr_b16 a[162:163], v244 offset:18432\n"
    "v_mfma_f32_16x16x32_bf16 v[200:203], a[172:175], a[76:79], v[200:203]\n"
    "v_exp_f32_e32 v182, v182\n"
    "v_mfma_f32_16x16x32_bf16 v[208:211], a[172:175], a[108:111], v[208:211]\n"
    "ds_read2st64_b32 v[168:169], v249 offset0:12 offset1:13\n"
    "v_mfma_f32_16x16x32_bf16 v[208:211], a[168:171], a[104:107], v[208:211]\n"
    "buffer_load_dwordx4 v248, s[8:11], s41 offen offset:2048 sc1 lds\n"
    "v_mfma_f32_16x16x32_bf16 v[216:219], a[168:171], a[136:139], v[216:219]\n"
    "ds_read_b64_tr_b16 a[164:165], v244 offset:49152\n"
    "v_mfma_f32_16x16x32_bf16 v[216:219], a[172:175], a[140:143], v[216:219]\n"
    "s_waitcnt lgkmcnt(10)\n"
    "ds_read2st64_b32 v[170:171], v249 offset0:14 offset1:15\n"
    "v_mfma_f32_16x16x32_bf16 v[216:219], a[176:179], a[144:147], v[216:219]\n"
    "ds_read_b64_tr_b16 a[168:169], v245 offset:16384\n"
    "v_mfma_f32_16x16x32_bf16 v[216:219], a[180:183], a[148:151], v[216:219]\n"
    "buffer_load_dwordx4 v248, s[8:11], s41 offen offset:3072 sc1 lds\n"
    "v_mfma_f32_16x16x32_bf16 v[208:211], a[180:183], a[116:119], v[208:211]\n"
    "ds_read_b64_tr_b16 a[166:167], v244 offset:51200\n"
    "v_mfma_f32_16x16x32_bf16 v[208:211], a[176:179], a[112:115], v[208:211]\n"
    "s_add_u32 s41, s41, 0x8000\n"
    "v_exp_f32_e32 v183, v183\n"
    "v_mfma_f32_16x16x32_bf16 v[200:203], a[176:179], a[80:83], v[200:203]\n"
    "ds_read_b64_tr_b16 a[170:171], v245 offset:18432\n"
    "v_cvt_pk_bf16_f32 v180, v180, v181\n"
    "v_mfma_f32_16x16x32_bf16 v[200:203], a[180:183], a[84:87], v[200:203]\n"
    "ds_read2st64_b32 v[176:177], v249 offset0:16 offset1:17\n"
    "v_mfma_f32_16x16x32_bf16 v[192:195], a[180:183], a[52:55], v[192:195]\n"
    "v_exp_f32_e32 v188, v188\n"
    "v_mfma_f32_16x16x32_bf16 v[192:195], a[176:179], a[48:51], v[192:195]\n"
    "v_exp_f32_e32 v189, v189\n"
    "v_mfma_f32_16x16x32_bf16 v[184:187], a[176:179], a[16:19], v[184:187]\n"
    "ds_read_b64_tr_b16 a[172:173], v245 offset:49152\n"
    "v_cvt_pk_bf16_f32 v181, v182, v183\n"
    "v_mfma_f32_16x16x32_bf16 v[184:187], a[180:183], a[20:23], v[184:187]\n"
    "s_waitcnt lgkmcnt(11)\n"
    "ds_read2st64_b32 v[178:179], v249 offset0:18 offset1:19\n"
    "v_mfma_f32_16x16x32_bf16 v[184:187], a[184:187], a[24:27], v[184:187]\n"
    "ds_read_b64_tr_b16 a[176:177], v246 offset:16384\n"
    "v_mfma_f32_16x16x32_bf16 v[184:187], a[188:191], a[28:31], v[184:187]\n"
    "ds_read_b64_tr_b16 a[174:175], v245 offset:51200\n"
    "v_mfma_f32_16x16x32_bf16 v[192:195], a[188:191], a[60:63], v[192:195]\n"
    "v_exp_f32_e32 v190, v190\n"
    "v_mfma_f32_16x16x32_bf16 v[192:195], a[184:187], a[56:59], v[192:195]\n"
    "v_exp_f32_e32 v191, v191\n"
    "v_mfma_f32_16x16x32_bf16 v[200:203], a[184:187], a[88:91], v[200:203]\n"
    "v_mfma_f32_16x16x32_bf16 v[200:203], a[188:191], a[92:95], v[200:203]\n"
    "ds_read_b64_tr_b16 a[178:179], v246 offset:18432\n"
    "v_mfma_f32_16x16x32_bf16 v[208:211], a[188:191], a[124:127], v[208:211]\n"
    "v_cvt_pk_bf16_f32 v188, v188, v189\n"
    "v_mfma_f32_16x16x32_bf16 v[208:211], a[184:187], a[120:123], v[208:211]\n"
    "v_exp_f32_e32 v196, v196\n"
    "v_mfma_f32_16x16x32_bf16 v[216:219], a[184:187], a[152:155], v[216:219]\n"
    "ds_read_b64_tr_b16 a[180:181], v246 offset:49152\n"
    "v_mfma_f32_16x16x32_bf16 v[216:219], a[188:191], a[156:159], v[216:219]\n"
    "s_waitcnt lgkmcnt(4)\n"
    "v_exp_f32_e32 v197, v197\n"
    "v_mfma_f32_16x16x32_bf16 a[236:239], a[160:163], v[140:143], a[236:239]\n"
    "v_mfma_f32_16x16x32_bf16 a[236:239], a[164:167], v[144:147], a[236:239]\n"
    "ds_read_b64_tr_b16 a[182:183], v246 offset:51200\n"
    "v_mfma_f32_16x16x32_bf16 a[240:243], a[164:167], v[152:155], a[240:243]\n"
    "ds_read_b64_tr_b16 a[184:185], v247 offset:16384\n"
    "v_mfma_f32_16x16x32_bf16 a[240:243], a[160:163], v[148:151], a[240:243]\n"
    "v_cvt_pk_bf16_f32 v189, v190, v191\n"
    "v_mfma_f32_16x16x32_bf16 a[244:247], a[160:163], v[156:159], a[244:247]\n"
    "ds_read_b64_tr_b16 a[186:187], v247 offset:18432\n"
    "v_mfma_f32_16x16x32_bf16 a[244:247], a[164:167], v[160:163], a[244:247]\n"
    "v_exp_f32_e32 v198, v198\n"
    "v_mfma_f32_16x16x32_bf16 a[248:251], a[164:167], v[168:171], a[248:251]\n"
    "v_exp_f32_e32 v199, v199\n"
    "v_mfma_f32_16x16x32_bf16 a[248:251], a[160:163], v[164:167], a[248:251]\n"
    "v_cvt_pk_bf16_f32 v196, v196, v197\n"
    "v_mfma_f32_16x16x32_bf16 a[252:255], a[160:163], v[172:175], a[252:255]\n"
    "ds_read_b64_tr_b16 a[188:189], v247 offset:49152\n"
    "v_mfma_f32_16x16x32_bf16 a[252:255], a[164:167], v[176:179], a[252:255]\n"
    "s_waitcnt lgkmcnt(6)\n"
    "v_exp_f32_e32 v204, v204\n"
    "v_mfma_f32_16x16x32_bf16 v[16:19], a[172:175], v[176:179], v[16:19]\n"
    "ds_read_b64_tr_b16 a[160:161], v244 offset:20480\n"
    "v_mfma_f32_16x16x32_bf16 v[16:19], a[168:171], v[172:175], v[16:19]\n"
    "ds_read_b64_tr_b16 a[190:191], v247 offset:51200\n"
    "v_mfma_f32_16x16x32_bf16 v[12:15], a[168:171], v[164:167], v[12:15]\n"
    "v_exp_f32_e32 v205, v205\n"
    "v_mfma_f32_16x16x32_bf16 v[12:15], a[172:175], v[168:171], v[12:15]\n"
    "v_cvt_pk_bf16_f32 v197, v198, v199\n"
    "v_mfma_f32_16x16x32_bf16 v[8:11], a[172:175], v[160:163], v[8:11]\n"
    "ds_read_b64_tr_b16 a[162:163], v244 offset:22528\n"
    "v_mfma_f32_16x16x32_bf16 v[8:11], a[168:171], v[156:159], v[8:11]\n"
    "v_exp_f32_e32 v206, v206\n"
    "v_mfma_f32_16x16x32_bf16 v[4:7], a[168:171], v[148:151], v[4:7]\n"
    "v_exp_f32_e32 v207, v207\n"
    "v_mfma_f32_16x16x32_bf16 v[4:7], a[172:175], v[152:155], v[4:7]\n"
    "v_cvt_pk_bf16_f32 v204, v204, v205\n"
    "v_mfma_f32_16x16x32_bf16 v[0:3], a[172:175], v[144:147], v[0:3]\n"
    "ds_read_b64_tr_b16 a[164:165], v244 offset:53248\n"
    "v_mfma_f32_16x16x32_bf16 v[0:3], a[168:171], v[140:143], v[0:3]\n"
    "s_waitcnt lgkmcnt(7)\n"
    "v_exp_f32_e32 v212, v212\n"
    "v_mfma_f32_16x16x32_bf16 v[20:23], a[176:179], v[140:143], v[20:23]\n"
    "v_mfma_f32_16x16x32_bf16 v[20:23], a[180:183], v[144:147], v[20:23]\n"
    "ds_read_b64_tr_b16 a[166:167], v244 offset:55296\n"
    "v_mfma_f32_16x16x32_bf16 v[24:27], a[180:183], v[152:155], v[24:27]\n"
    "ds_read_b64_tr_b16 a[168:169], v245 offset:20480\n"
    "v_mfma_f32_16x16x32_bf16 v[24:27], a[176:179], v[148:151], v[24:27]\n"
    "v_exp_f32_e32 v213, v213\n"
    "v_mfma_f32_16x16x32_bf16 v[28:31], a[176:179], v[156:159], v[28:31]\n"
    "ds_read_b64_tr_b16 a[170:171], v245 offset:22528\n"
    "v_mfma_f32_16x16x32_bf16 v[28:31], a[180:183], v[160:163], v[28:31]\n"
    "v_cvt_pk_bf16_f32 v205, v206, v207\n"
    "v_mfma_f32_16x16x32_bf16 v[32:35], a[180:183], v[168:171], v[32:35]\n"
    "v_exp_f32_e32 v214, v214\n"
    "v_mfma_f32_16x16x32_bf16 v[32:35], a[176:179], v[164:167], v[32:35]\n"
    "v_exp_f32_e32 v215, v215\n"
    "v_mfma_f32_16x16x32_bf16 v[36:39], a[176:179], v[172:175], v[36:39]\n"
    "ds_read_b64_tr_b16 a[172:173], v245 offset:53248\n"
    "v_mfma_f32_16x16x32_bf16 v[36:39], a[180:183], v[176:179], v[36:39]\n"
    "s_waitcnt lgkmcnt(6)\n"
    "v_cvt_pk_bf16_f32 v212, v212, v213\n"
    "v_mfma_f32_16x16x32_bf16 v[56:59], a[188:191], v[176:179], v[56:59]\n"
    "ds_read_b64_tr_b16 a[176:177], v246 offset:20480\n"
    "v_mfma_f32_16x16x32_bf16 v[56:59], a[184:187], v[172:175], v[56:59]\n"
    "ds_read_b64_tr_b16 a[174:175], v245 offset:55296\n"
    "v_mfma_f32_16x16x32_bf16 v[52:55], a[184:187], v[164:167], v[52:55]\n"
    "v_cvt_pk_bf16_f32 v213, v214, v215\n"
    "v_mfma_f32_16x16x32_bf16 v[52:55], a[188:191], v[168:171], v[52:55]\n"
    "v_exp_f32_e32 v184, v184\n"
    "v_mfma_f32_16x16x32_bf16 v[48:51], a[188:191], v[160:163], v[48:51]\n"
    "ds_read_b64_tr_b16 a[178:179], v246 offset:22528\n"
    "v_mfma_f32_16x16x32_bf16 v[48:51], a[184:187], v[156:159], v[48:51]\n"
    "v_mfma_f32_16x16x32_bf16 v[44:47], a[184:187], v[148:151], v[44:47]\n"
    "v_exp_f32_e32 v185, v185\n"
    "v_mfma_f32_16x16x32_bf16 v[44:47], a[188:191], v[152:155], v[44:47]\n"
    "v_exp_f32_e32 v186, v186\n"
    "v_mfma_f32_16x16x32_bf16 v[40:43], a[188:191], v[144:147], v[40:43]\n"
    "ds_read_b64_tr_b16 a[180:181], v246 offset:53248\n"
    "v_mfma_f32_16x16x32_bf16 v[40:43], a[184:187], v[140:143], v[40:43]\n"
    "s_waitcnt lgkmcnt(7)\n"
    "v_exp_f32_e32 v187, v187\n"
    "v_mfma_f32_16x16x32_bf16 v[60:63], a[160:163], v[140:143], v[60:63]\n"
    "ds_read_b64_tr_b16 a[184:185], v247 offset:20480\n"
    "v_mfma_f32_16x16x32_bf16 v[60:63], a[164:167], v[144:147], v[60:63]\n"
    "ds_read_b64_tr_b16 a[182:183], v246 offset:55296\n"
    "v_mfma_f32_16x16x32_bf16 v[64:67], a[164:167], v[152:155], v[64:67]\n"
    "ds_read_b64_tr_b16 a[186:187], v247 offset:22528\n"
    "v_mfma_f32_16x16x32_bf16 v[64:67], a[160:163], v[148:151], v[64:67]\n"
    "v_cvt_pk_bf16_f32 v182, v184, v185\n"
    "v_mfma_f32_16x16x32_bf16 v[68:71], a[160:163], v[156:159], v[68:71]\n"
    "v_exp_f32_e32 v192, v192\n"
    "v_mfma_f32_16x16x32_bf16 v[68:71], a[164:167], v[160:163], v[68:71]\n"
    "ds_read_b64_tr_b16 a[188:189], v247 offset:53248\n"
    "v_mfma_f32_16x16x32_bf16 v[72:75], a[164:167], v[168:171], v[72:75]\n"
    "ds_read_b64_tr_b16 a[190:191], v247 offset:55296\n"
    "v_mfma_f32_16x16x32_bf16 v[72:75], a[160:163], v[164:167], v[72:75]\n"
    "s_waitcnt vmcnt(9)\n"
    "v_mfma_f32_16x16x32_bf16 v[76:79], a[160:163], v[172:175], v[76:79]\n"
    "s_barrier\n"
    "v_mfma_f32_16x16x32_bf16 v[76:79], a[164:167], v[176:179], v[76:79]\n"
    "s_waitcnt lgkmcnt(7)\n"
    "s_mov_b32 m0, s42\n"
    "v_mfma_f32_16x16x32_bf16 v[96:99], a[172:175], v[176:179], v[96:99]\n"
    "v_exp_f32_e32 v193, v193\n"
    "v_mfma_f32_16x16x32_bf16 v[96:99], a[168:171], v[172:175], v[96:99]\n"
    "v_cvt_pk_bf16_f32 v183, v186, v187\n"
    "v_mfma_f32_16x16x32_bf16 v[92:95], a[168:171], v[164:167], v[92:95]\n"
    "v_exp_f32_e32 v194, v194\n"
    "v_mfma_f32_16x16x32_bf16 v[92:95], a[172:175], v[168:171], v[92:95]\n"
    "ds_read_b128 a[160:163], v240 offset:0\n"
    "v_mfma_f32_16x16x32_bf16 v[88:91], a[172:175], v[160:163], v[88:91]\n"
    "ds_write_addtid_b32 v180 offset:0\n"
    "v_mfma_f32_16x16x32_bf16 v[88:91], a[168:171], v[156:159], v[88:91]\n"
    "v_exp_f32_e32 v195, v195\n"
    "v_mfma_f32_16x16x32_bf16 v[84:87], a[168:171], v[148:151], v[84:87]\n"
    "ds_write_addtid_b32 v181 offset:256\n"
    "v_mfma_f32_16x16x32_bf16 v[84:87], a[172:175], v[152:155], v[84:87]\n"
    "ds_write_addtid_b32 v188 offset:1024\n"
    "v_mfma_f32_16x16x32_bf16 v[80:83], a[172:175], v[144:147], v[80:83]\n"
    "v_cvt_pk_bf16_f32 v190, v192, v193\n"
    "v_mfma_f32_16x16x32_bf16 v[80:83], a[168:171], v[140:143], v[80:83]\n"
    "s_waitcnt lgkmcnt(7)\n"
    "v_exp_f32_e32 v200, v200\n"
    "v_mfma_f32_16x16x32_bf16 v[100:103], a[176:179], v[140:143], v[100:103]\n"
    "ds_read_b128 a[164:167], v241 offset:0\n"
    "v_mfma_f32_16x16x32_bf16 v[100:103], a[180:183], v[144:147], v[100:103]\n"
    "ds_write_addtid_b32 v189 offset:1280\n"
    "v_mfma_f32_16x16x32_bf16 v[104:107], a[180:183], v[152:155], v[104:107]\n"
    "v_exp_f32_e32 v201, v201\n"
    "v_mfma_f32_16x16x32_bf16 v[104:107], a[176:179], v[148:151], v[104:107]\n"
    "ds_read_b128 a[168:171], v240 offset:4096\n"
    "v_mfma_f32_16x16x32_bf16 v[108:111], a[176:179], v[156:159], v[108:111]\n"
    "ds_write_addtid_b32 v196 offset:2048\n"
    "v_mfma_f32_16x16x32_bf16 v[108:111], a[180:183], v[160:163], v[108:111]\n"
    "v_cvt_pk_bf16_f32 v191, v194, v195\n"
    "v_mfma_f32_16x16x32_bf16 v[112:115], a[180:183], v[168:171], v[112:115]\n"
    "ds_write_addtid_b32 v197 offset:2304\n"
    "v_mfma_f32_16x16x32_bf16 v[112:115], a[176:179], v[164:167], v[112:115]\n"
    "v_exp_f32_e32 v202, v202\n"
    "v_mfma_f32_16x16x32_bf16 v[116:119], a[176:179], v[172:175], v[116:119]\n"
    "ds_write_addtid_b32 v204 offset:3072\n"
    "v_mfma_f32_16x16x32_bf16 v[116:119], a[180:183], v[176:179], v[116:119]\n"
    "s_waitcnt lgkmcnt(10)\n"
    "v_exp_f32_e32 v203, v203\n"
    "v_mfma_f32_16x16x32_bf16 v[136:139], a[188:191], v[176:179], v[136:139]\n"
    "ds_read_b128 a[172:175], v241 offset:4096\n"
    "v_mfma_f32_16x16x32_bf16 v[136:139], a[184:187], v[172:175], v[136:139]\n"
    "ds_write_addtid_b32 v205 offset:3328\n"
    "v_cvt_pk_bf16_f32 v198, v200, v201\n"
    "v_mfma_f32_16x16x32_bf16 v[132:135], a[184:187], v[164:167], v[132:135]\n"
    "ds_read_b128 a[176:179], v242 offset:0\n"
    "v_mfma_f32_16x16x32_bf16 v[132:135], a[188:191], v[168:171], v[132:135]\n"
    "ds_write_addtid_b32 v212 offset:4096\n"
    "v_mfma_f32_16x16x32_bf16 v[128:131], a[188:191], v[160:163], v[128:131]\n"
    "v_exp_f32_e32 v208, v208\n"
    "v_mfma_f32_16x16x32_bf16 v[128:131], a[184:187], v[156:159], v[128:131]\n"
    "ds_write_addtid_b32 v213 offset:4352\n"
    "v_mfma_f32_16x16x32_bf16 v[124:127], a[184:187], v[148:151], v[124:127]\n"
    "v_exp_f32_e32 v209, v209\n"
    "v_mfma_f32_16x16x32_bf16 v[124:127], a[188:191], v[152:155], v[124:127]\n"
    "ds_read_b128 a[180:183], v243 offset:0\n"
    "v_mfma_f32_16x16x32_bf16 v[120:123], a[188:191], v[144:147], v[120:123]\n"
    "ds_write_addtid_b32 v182 offset:512\n"
    "v_mfma_f32_16x16x32_bf16 v[120:123], a[184:187], v[140:143], v[120:123]\n"
    "s_waitcnt lgkmcnt(12)\n"
    "v_cvt_pk_bf16_f32 v199, v202, v203\n"
    "v_mfma_f32_16x16x32_bf16 v[172:175], a[160:163], a[128:131], v[236:239]\n"
    "v_mfma_f32_16x16x32_bf16 v[172:175], a[164:167], a[132:135], v[172:175]\n"
    "v_exp_f32_e32 v210, v210\n"
    "v_mfma_f32_16x16x32_bf16 v[164:167], a[164:167], a[100:103], v[232:235]\n"
    "ds_write_addtid_b32 v183 offset:768\n"
    "v_mfma_f32_16x16x32_bf16 v[164:167], a[160:163], a[96:99], v[164:167]\n"
    "ds_read_b128 a[184:187], v242 offset:4096\n"
    "v_mfma_f32_16x16x32_bf16 v[156:159], a[160:163], a[64:67], v[228:231]\n"
    "v_exp_f32_e32 v211, v211\n"
    "v_mfma_f32_16x16x32_bf16 v[156:159], a[164:167], a[68:71], v[156:159]\n"
    "ds_write_addtid_b32 v190 offset:1536\n"
    "v_mfma_f32_16x16x32_bf16 v[148:151], a[164:167], a[36:39], v[224:227]\n"
    "v_cvt_pk_bf16_f32 v206, v208, v209\n"
    "v_mfma_f32_16x16x32_bf16 v[148:151], a[160:163], a[32:35], v[148:151]\n"
    "v_mfma_f32_16x16x32_bf16 v[140:143], a[160:163], a[0:3], v[220:223]\n"
    "ds_write_addtid_b32 v191 offset:1792\n"
    "v_mfma_f32_16x16x32_bf16 v[140:143], a[164:167], a[4:7], v[140:143]\n"
    "s_waitcnt lgkmcnt(10)\n"
    "v_exp_f32_e32 v216, v216\n"
    "v_mfma_f32_16x16x32_bf16 v[140:143], a[168:171], a[8:11], v[140:143]\n"
    "ds_read_b128 a[188:191], v243 offset:4096\n"
    "v_mfma_f32_16x16x32_bf16 v[140:143], a[172:175], a[12:15], v[140:143]\n"
    "v_exp_f32_e32 v217, v217\n"
    "v_mfma_f32_16x16x32_bf16 v[148:151], a[172:175], a[44:47], v[148:151]\n"
    "ds_write_addtid_b32 v198 offset:2560\n"
    "v_mfma_f32_16x16x32_bf16 v[148:151], a[168:171], a[40:43], v[148:151]\n"
    "ds_read_b128 a[160:163], v240 offset:2048\n"
    "v_mfma_f32_16x16x32_bf16 v[156:159], a[168:171], a[72:75], v[156:159]\n"
    "v_cvt_pk_bf16_f32 v207, v210, v211\n"
    "v_mfma_f32_16x16x32_bf16 v[156:159], a[172:175], a[76:79], v[156:159]\n"
    "ds_write_addtid_b32 v199 offset:2816\n"
    "v_mfma_f32_16x16x32_bf16 v[164:167], a[172:175], a[108:111], v[164:167]\n"
    "v_exp_f32_e32 v218, v218\n"
    "v_mfma_f32_16x16x32_bf16 v[164:167], a[168:171], a[104:107], v[164:167]\n"
    "ds_write_addtid_b32 v206 offset:3584\n"
    "v_mfma_f32_16x16x32_bf16 v[172:175], a[168:171], a[136:139], v[172:175]\n"
    "v_mfma_f32_16x16x32_bf16 v[172:175], a[172:175], a[140:143], v[172:175]\n"
    "s_waitcnt lgkmcnt(10)\n"
    "v_exp_f32_e32 v219, v219\n"
    "v_mfma_f32_16x16x32_bf16 v[172:175], a[176:179], a[144:147], v[172:175]\n"
    "ds_read_b128 a[164:167], v241 offset:2048\n"
    "v_mfma_f32_16x16x32_bf16 v[172:175], a[180:183], a[148:151], v[172:175]\n"
    "v_cvt_pk_bf16_f32 v214, v216, v217\n"
    "v_mfma_f32_16x16x32_bf16 v[164:167], a[180:183], a[116:119], v[164:167]\n"
    "ds_write_addtid_b32 v207 offset:3840\n"
    "v_mfma_f32_16x16x32_bf16 v[164:167], a[176:179], a[112:115], v[164:167]\n"
    "ds_read_b128 a[168:171], v240 offset:6144\n"
    "v_mfma_f32_16x16x32_bf16 v[156:159], a[176:179], a[80:83], v[156:159]\n"
    "v_cvt_pk_bf16_f32 v215, v218, v219\n"
    "v_mfma_f32_16x16x32_bf16 v[156:159], a[180:183], a[84:87], v[156:159]\n"
    "ds_write_addtid_b32 v214 offset:4608\n"
    "v_mfma_f32_16x16x32_bf16 v[148:151], a[180:183], a[52:55], v[148:151]\n"
    "ds_write_addtid_b32 v215 offset:4864\n"
    "v_mfma_f32_16x16x32_bf16 v[148:151], a[176:179], a[48:51], v[148:151]\n"
    "v_mfma_f32_16x16x32_bf16 v[140:143], a[176:179], a[16:19], v[140:143]\n"
    "ds_read_b128 a[172:175], v241 offset:6144\n"
    "v_mfma_f32_16x16x32_bf16 v[140:143], a[180:183], a[20:23], v[140:143]\n"
    "s_waitcnt lgkmcnt(10)\n"
    "s_waitcnt lgkmcnt(1)\n"
    "v_mfma_f32_16x16x32_bf16 v[140:143], a[184:187], a[24:27], v[140:143]\n"
    "s_barrier\n"
    "v_mfma_f32_16x16x32_bf16 v[140:143], a[188:191], a[28:31], v[140:143]\n"
    "s_add_u32 m0, s43, 16384\n"
    "v_mfma_f32_16x16x32_bf16 v[148:151], a[188:191], a[60:63], v[148:151]\n"
    "ds_read_b128 a[176:179], v242 offset:2048\n"
    "v_mfma_f32_16x16x32_bf16 v[148:151], a[184:187], a[56:59], v[148:151]\n"
    "buffer_load_dwordx4 v248, s[8:11], s40 offen offset:0 sc1 lds\n"
    "v_mfma_f32_16x16x32_bf16 v[156:159], a[184:187], a[88:91], v[156:159]\n"
    "ds_read2st64_b32 v[184:185], v249 offset0:0 offset1:1\n"
    "v_mfma_f32_16x16x32_bf16 v[156:159], a[188:191], a[92:95], v[156:159]\n"
    "v_mfma_f32_16x16x32_bf16 v[164:167], a[188:191], a[124:127], v[164:167]\n"
    "ds_read2st64_b32 v[186:187], v249 offset0:2 offset1:3\n"
    "v_mfma_f32_16x16x32_bf16 v[164:167], a[184:187], a[120:123], v[164:167]\n"
    "buffer_load_dwordx4 v248, s[8:11], s40 offen offset:1024 sc1 lds\n"
    "v_mfma_f32_16x16x32_bf16 v[172:175], a[184:187], a[152:155], v[172:175]\n"
    "v_mfma_f32_16x16x32_bf16 v[172:175], a[188:191], a[156:159], v[172:175]\n"
    "v_mfma_f32_16x16x32_bf16 v[176:179], a[160:163], a[128:131], v[236:239]\n"
    "ds_read_b128 a[180:183], v243 offset:2048\n"
    "v_mfma_f32_16x16x32_bf16 v[176:179], a[164:167], a[132:135], v[176:179]\n"
    "buffer_load_dwordx4 v248, s[8:11], s40 offen offset:2048 sc1 lds\n"
    "v_mfma_f32_16x16x32_bf16 v[168:171], a[164:167], a[100:103], v[232:235]\n"
    "ds_read2st64_b32 v[192:193], v249 offset0:4 offset1:5\n"
    "v_mfma_f32_16x16x32_bf16 v[168:171], a[160:163], a[96:99], v[168:171]\n"
    "ds_read_b128 a[184:187], v242 offset:6144\n"
    "v_mfma_f32_16x16x32_bf16 v[160:163], a[160:163], a[64:67], v[228:231]\n"
    "ds_read2st64_b32 v[194:195], v249 offset0:6 offset1:7\n"
    "v_mfma_f32_16x16x32_bf16 v[160:163], a[164:167], a[68:71], v[160:163]\n"
    "buffer_load_dwordx4 v248, s[8:11], s40 offen offset:3072 sc1 lds\n"
    "v_mfma_f32_16x16x32_bf16 v[152:155], a[164:167], a[36:39], v[224:227]\n"
    "s_add_u32 m0, s43, 20480\n"
    "v_exp_f32_e32 v140, v140\n"
    "v_mfma_f32_16x16x32_bf16 v[152:155], a[160:163], a[32:35], v[152:155]\n"
    "s_add_u32 s40, s40, 0x8000\n"
    "v_exp_f32_e32 v141, v141\n"
    "v_mfma_f32_16x16x32_bf16 v[144:147], a[160:163], a[0:3], v[220:223]\n"
    "ds_read2st64_b32 v[200:201], v249 offset0:8 offset1:9\n"
    "v_mfma_f32_16x16x32_bf16 v[144:147], a[164:167], a[4:7], v[144:147]\n"
    "s_waitcnt lgkmcnt(8)\n"
    "buffer_load_dwordx4 v248, s[8:11], s41 offen offset:0 sc1 lds\n"
    "v_mfma_f32_16x16x32_bf16 v[144:147], a[168:171], a[8:11], v[144:147]\n"
    "ds_read_b128 a[188:191], v243 offset:6144\n"
    "v_mfma_f32_16x16x32_bf16 v[144:147], a[172:175], a[12:15], v[144:147]\n"
    "ds_read_b64_tr_b16 a[160:161], v244 offset:24576\n"
    "v_mfma_f32_16x16x32_bf16 v[152:155], a[172:175], a[44:47], v[152:155]\n"
    "ds_read2st64_b32 v[202:203], v249 offset0:10 offset1:11\n"
    "v_mfma_f32_16x16x32_bf16 v[152:155], a[168:171], a[40:43], v[152:155]\n"
    "buffer_load_dwordx4 v248, s[8:11], s41 offen offset:1024 sc1 lds\n"
    "v_mfma_f32_16x16x32_bf16 v[160:163], a[168:171], a[72:75], v[160:163]\n"
    "ds_read_b64_tr_b16 a[162:163], v244 offset:26624\n"
    "v_mfma_f32_16x16x32_bf16 v[160:163], a[172:175], a[76:79], v[160:163]\n"
    "v_exp_f32_e32 v142, v142\n"
    "v_mfma_f32_16x16x32_bf16 v[168:171], a[172:175], a[108:111], v[168:171]\n"
    "ds_read2st64_b32 v[208:209], v249 offset0:12 offset1:13\n"
    "v_mfma_f32_16x16x32_bf16 v[168:171], a[168:171], a[104:107], v[168:171]\n"
    "buffer_load_dwordx4 v248, s[8:11], s41 offen offset:2048 sc1 lds\n"
    "v_mfma_f32_16x16x32_bf16 v[176:179], a[168:171], a[136:139], v[176:179]\n"
    "ds_read_b64_tr_b16 a[164:165], v244 offset:57344\n"
    "v_mfma_f32_16x16x32_bf16 v[176:179], a[172:175], a[140:143], v[176:179]\n"
    "s_waitcnt lgkmcnt(10)\n"
    "ds_read2st64_b32 v[210:211], v249 offset0:14 offset1:15\n"
    "v_mfma_f32_16x16x32_bf16 v[176:179], a[176:179], a[144:147], v[176:179]\n"
    "ds_read_b64_tr_b16 a[168:169], v245 offset:24576\n"
    "v_mfma_f32_16x16x32_bf16 v[176:179], a[180:183], a[148:151], v[176:179]\n"
    "buffer_load_dwordx4 v248, s[8:11], s41 offen offset:3072 sc1 lds\n"
    "v_mfma_f32_16x16x32_bf16 v[168:171], a[180:183], a[116:119], v[168:171]\n"
    "ds_read_b64_tr_b16 a[166:167], v244 offset:59392\n"
    "v_mfma_f32_16x16x32_bf16 v[168:171], a[176:179], a[112:115], v[168:171]\n"
    "s_add_u32 s41, s41, 0x8000\n"
    "v_exp_f32_e32 v143, v143\n"
    "v_mfma_f32_16x16x32_bf16 v[160:163], a[176:179], a[80:83], v[160:163]\n"
    "ds_read_b64_tr_b16 a[170:171], v245 offset:26624\n"
    "v_cvt_pk_bf16_f32 v140, v140, v141\n"
    "v_mfma_f32_16x16x32_bf16 v[160:163], a[180:183], a[84:87], v[160:163]\n"
    "ds_read2st64_b32 v[216:217], v249 offset0:16 offset1:17\n"
    "v_mfma_f32_16x16x32_bf16 v[152:155], a[180:183], a[52:55], v[152:155]\n"
    "v_exp_f32_e32 v148, v148\n"
    "v_mfma_f32_16x16x32_bf16 v[152:155], a[176:179], a[48:51], v[152:155]\n"
    "v_exp_f32_e32 v149, v149\n"
    "v_mfma_f32_16x16x32_bf16 v[144:147], a[176:179], a[16:19], v[144:147]\n"
    "ds_read_b64_tr_b16 a[172:173], v245 offset:57344\n"
    "v_cvt_pk_bf16_f32 v141, v142, v143\n"
    "v_mfma_f32_16x16x32_bf16 v[144:147], a[180:183], a[20:23], v[144:147]\n"
    "s_waitcnt lgkmcnt(11)\n"
    "ds_read2st64_b32 v[218:219], v249 offset0:18 offset1:19\n"
    "v_mfma_f32_16x16x32_bf16 v[144:147], a[184:187], a[24:27], v[144:147]\n"
    "ds_read_b64_tr_b16 a[176:177], v246 offset:24576\n"
    "v_mfma_f32_16x16x32_bf16 v[144:147], a[188:191], a[28:31], v[144:147]\n"
    "ds_read_b64_tr_b16 a[174:175], v245 offset:59392\n"
    "v_mfma_f32_16x16x32_bf16 v[152:155], a[188:191], a[60:63], v[152:155]\n"
    "v_exp_f32_e32 v150, v150\n"
    "v_mfma_f32_16x16x32_bf16 v[152:155], a[184:187], a[56:59], v[152:155]\n"
    "v_exp_f32_e32 v151, v151\n"
    "v_mfma_f32_16x16x32_bf16 v[160:163], a[184:187], a[88:91], v[160:163]\n"
    "v_mfma_f32_16x16x32_bf16 v[160:163], a[188:191], a[92:95], v[160:163]\n"
    "ds_read_b64_tr_b16 a[178:179], v246 offset:26624\n"
    "v_mfma_f32_16x16x32_bf16 v[168:171], a[188:191], a[124:127], v[168:171]\n"
    "v_cvt_pk_bf16_f32 v148, v148, v149\n"
    "v_mfma_f32_16x16x32_bf16 v[168:171], a[184:187], a[120:123], v[168:171]\n"
    "v_exp_f32_e32 v156, v156\n"
    "v_mfma_f32_16x16x32_bf16 v[176:179], a[184:187], a[152:155], v[176:179]\n"
    "ds_read_b64_tr_b16 a[180:181], v246 offset:57344\n"
    "v_mfma_f32_16x16x32_bf16 v[176:179], a[188:191], a[156:159], v[176:179]\n"
    "s_waitcnt lgkmcnt(4)\n"
    "v_exp_f32_e32 v157, v157\n"
    "v_mfma_f32_16x16x32_bf16 a[236:239], a[160:163], v[180:183], a[236:239]\n"
    "v_mfma_f32_16x16x32_bf16 a[236:239], a[164:167], v[184:187], a[236:239]\n"
    "ds_read_b64_tr_b16 a[182:183], v246 offset:59392\n"
    "v_mfma_f32_16x16x32_bf16 a[240:243], a[164:167], v[192:195], a[240:243]\n"
    "ds_read_b64_tr_b16 a[184:185], v247 offset:24576\n"
    "v_mfma_f32_16x16x32_bf16 a[240:243], a[160:163], v[188:191], a[240:243]\n"
    "v_cvt_pk_bf16_f32 v149, v150, v151\n"
    "v_mfma_f32_16x16x32_bf16 a[244:247], a[160:163], v[196:199], a[244:247]\n"
    "ds_read_b64_tr_b16 a[186:187], v247 offset:26624\n"
    "v_mfma_f32_16x16x32_bf16 a[244:247], a[164:167], v[200:203], a[244:247]\n"
    "v_exp_f32_e32 v158, v158\n"
    "v_mfma_f32_16x16x32_bf16 a[248:251], a[164:167], v[208:211], a[248:251]\n"
    "v_exp_f32_e32 v159, v159\n"
    "v_mfma_f32_16x16x32_bf16 a[248:251], a[160:163], v[204:207], a[248:251]\n"
    "v_cvt_pk_bf16_f32 v156, v156, v157\n"
    "v_mfma_f32_16x16x32_bf16 a[252:255], a[160:163], v[212:215], a[252:255]\n"
    "ds_read_b64_tr_b16 a[188:189], v247 offset:57344\n"
    "v_mfma_f32_16x16x32_bf16 a[252:255], a[164:167], v[216:219], a[252:255]\n"
    "s_waitcnt lgkmcnt(6)\n"
    "v_exp_f32_e32 v164, v164\n"
    "v_mfma_f32_16x16x32_bf16 v[16:19], a[172:175], v[216:219], v[16:19]\n"
    "ds_read_b64_tr_b16 a[160:161], v244 offset:28672\n"
    "v_mfma_f32_16x16x32_bf16 v[16:19], a[168:171], v[212:215], v[16:19]\n"
    "ds_read_b64_tr_b16 a[190:191], v247 offset:59392\n"
    "v_mfma_f32_16x16x32_bf16 v[12:15], a[168:171], v[204:207], v[12:15]\n"
    "v_exp_f32_e32 v165, v165\n"
    "v_mfma_f32_16x16x32_bf16 v[12:15], a[172:175], v[208:211], v[12:15]\n"
    "v_cvt_pk_bf16_f32 v157, v158, v159\n"
    "v_mfma_f32_16x16x32_bf16 v[8:11], a[172:175], v[200:203], v[8:11]\n"
    "ds_read_b64_tr_b16 a[162:163], v244 offset:30720\n"
    "v_mfma_f32_16x16x32_bf16 v[8:11], a[168:171], v[196:199], v[8:11]\n"
    "v_exp_f32_e32 v166, v166\n"
    "v_mfma_f32_16x16x32_bf16 v[4:7], a[168:171], v[188:191], v[4:7]\n"
    "v_exp_f32_e32 v167, v167\n"
    "v_mfma_f32_16x16x32_bf16 v[4:7], a[172:175], v[192:195], v[4:7]\n"
    "v_cvt_pk_bf16_f32 v164, v164, v165\n"
    "v_mfma_f32_16x16x32_bf16 v[0:3], a[172:175], v[184:187], v[0:3]\n"
    "ds_read_b64_tr_b16 a[164:165], v244 offset:61440\n"
    "v_mfma_f32_16x16x32_bf16 v[0:3], a[168:171], v[180:183], v[0:3]\n"
    "s_waitcnt lgkmcnt(7)\n"
    "v_exp_f32_e32 v172, v172\n"
    "v_mfma_f32_16x16x32_bf16 v[20:23], a[176:179], v[180:183], v[20:23]\n"
    "v_mfma_f32_16x16x32_bf16 v[20:23], a[180:183], v[184:187], v[20:23]\n"
    "ds_read_b64_tr_b16 a[166:167], v244 offset:63488\n"
    "v_mfma_f32_16x16x32_bf16 v[24:27], a[180:183], v[192:195], v[24:27]\n"
    "ds_read_b64_tr_b16 a[168:169], v245 offset:28672\n"
    "v_mfma_f32_16x16x32_bf16 v[24:27], a[176:179], v[188:191], v[24:27]\n"
    "v_exp_f32_e32 v173, v173\n"
    "v_mfma_f32_16x16x32_bf16 v[28:31], a[176:179], v[196:199], v[28:31]\n"
    "ds_read_b64_tr_b16 a[170:171], v245 offset:30720\n"
    "v_mfma_f32_16x16x32_bf16 v[28:31], a[180:183], v[200:203], v[28:31]\n"
    "v_cvt_pk_bf16_f32 v165, v166, v167\n"
    "v_mfma_f32_16x16x32_bf16 v[32:35], a[180:183], v[208:211], v[32:35]\n"
    "v_exp_f32_e32 v174, v174\n"
    "v_mfma_f32_16x16x32_bf16 v[32:35], a[176:179], v[204:207], v[32:35]\n"
    "v_exp_f32_e32 v175, v175\n"
    "v_mfma_f32_16x16x32_bf16 v[36:39], a[176:179], v[212:215], v[36:39]\n"
    "ds_read_b64_tr_b16 a[172:173], v245 offset:61440\n"
    "v_mfma_f32_16x16x32_bf16 v[36:39], a[180:183], v[216:219], v[36:39]\n"
    "s_waitcnt lgkmcnt(6)\n"
    "v_cvt_pk_bf16_f32 v172, v172, v173\n"
    "v_mfma_f32_16x16x32_bf16 v[56:59], a[188:191], v[216:219], v[56:59]\n"
    "ds_read_b64_tr_b16 a[176:177], v246 offset:28672\n"
    "v_mfma_f32_16x16x32_bf16 v[56:59], a[184:187], v[212:215], v[56:59]\n"
    "ds_read_b64_tr_b16 a[174:175], v245 offset:63488\n"
    "v_mfma_f32_16x16x32_bf16 v[52:55], a[184:187], v[204:207], v[52:55]\n"
    "v_cvt_pk_bf16_f32 v173, v174, v175\n"
    "v_mfma_f32_16x16x32_bf16 v[52:55], a[188:191], v[208:211], v[52:55]\n"
    "v_exp_f32_e32 v144, v144\n"
    "v_mfma_f32_16x16x32_bf16 v[48:51], a[188:191], v[200:203], v[48:51]\n"
    "ds_read_b64_tr_b16 a[178:179], v246 offset:30720\n"
    "v_mfma_f32_16x16x32_bf16 v[48:51], a[184:187], v[196:199], v[48:51]\n"
    "v_mfma_f32_16x16x32_bf16 v[44:47], a[184:187], v[188:191], v[44:47]\n"
    "v_exp_f32_e32 v145, v145\n"
    "v_mfma_f32_16x16x32_bf16 v[44:47], a[188:191], v[192:195], v[44:47]\n"
    "v_exp_f32_e32 v146, v146\n"
    "v_mfma_f32_16x16x32_bf16 v[40:43], a[188:191], v[184:187], v[40:43]\n"
    "ds_read_b64_tr_b16 a[180:181], v246 offset:61440\n"
    "v_mfma_f32_16x16x32_bf16 v[40:43], a[184:187], v[180:183], v[40:43]\n"
    "s_waitcnt lgkmcnt(7)\n"
    "v_exp_f32_e32 v147, v147\n"
    "v_mfma_f32_16x16x32_bf16 v[60:63], a[160:163], v[180:183], v[60:63]\n"
    "ds_read_b64_tr_b16 a[184:185], v247 offset:28672\n"
    "v_mfma_f32_16x16x32_bf16 v[60:63], a[164:167], v[184:187], v[60:63]\n"
    "ds_read_b64_tr_b16 a[182:183], v246 offset:63488\n"
    "v_mfma_f32_16x16x32_bf16 v[64:67], a[164:167], v[192:195], v[64:67]\n"
    "ds_read_b64_tr_b16 a[186:187], v247 offset:30720\n"
    "v_mfma_f32_16x16x32_bf16 v[64:67], a[160:163], v[188:191], v[64:67]\n"
    "v_cvt_pk_bf16_f32 v142, v144, v145\n"
    "v_mfma_f32_16x16x32_bf16 v[68:71], a[160:163], v[196:199], v[68:71]\n"
    "v_exp_f32_e32 v152, v152\n"
    "v_mfma_f32_16x16x32_bf16 v[68:71], a[164:167], v[200:203], v[68:71]\n"
    "ds_read_b64_tr_b16 a[188:189], v247 offset:61440\n"
    "v_mfma_f32_16x16x32_bf16 v[72:75], a[164:167], v[208:211], v[72:75]\n"
    "ds_read_b64_tr_b16 a[190:191], v247 offset:63488\n"
    "v_mfma_f32_16x16x32_bf16 v[72:75], a[160:163], v[204:207], v[72:75]\n"
    "s_waitcnt vmcnt(8)\n"
    "v_mfma_f32_16x16x32_bf16 v[76:79], a[160:163], v[212:215], v[76:79]\n"
    "s_barrier\n"
    "v_mfma_f32_16x16x32_bf16 v[76:79], a[164:167], v[216:219], v[76:79]\n"
    "s_waitcnt lgkmcnt(7)\n"
    "s_mov_b32 m0, s42\n"
    "v_mfma_f32_16x16x32_bf16 v[96:99], a[172:175], v[216:219], v[96:99]\n"
    "v_exp_f32_e32 v153, v153\n"
    "v_mfma_f32_16x16x32_bf16 v[96:99], a[168:171], v[212:215], v[96:99]\n"
    "v_cvt_pk_bf16_f32 v143, v146, v147\n"
    "v_mfma_f32_16x16x32_bf16 v[92:95], a[168:171], v[204:207], v[92:95]\n"
    "v_exp_f32_e32 v154, v154\n"
    "v_mfma_f32_16x16x32_bf16 v[92:95], a[172:175], v[208:211], v[92:95]\n"
    "ds_read_b128 a[160:163], v240 offset:8192\n"
    "v_mfma_f32_16x16x32_bf16 v[88:91], a[172:175], v[200:203], v[88:91]\n"
    "ds_write_addtid_b32 v140 offset:0\n"
    "v_mfma_f32_16x16x32_bf16 v[88:91], a[168:171], v[196:199], v[88:91]\n"
    "v_exp_f32_e32 v155, v155\n"
    "v_mfma_f32_16x16x32_bf16 v[84:87], a[168:171], v[188:191], v[84:87]\n"
    "ds_write_addtid_b32 v141 offset:256\n"
    "v_mfma_f32_16x16x32_bf16 v[84:87], a[172:175], v[192:195], v[84:87]\n"
    "ds_write_addtid_b32 v148 offset:1024\n"
    "v_mfma_f32_16x16x32_bf16 v[80:83], a[172:175], v[184:187], v[80:83]\n"
    "v_cvt_pk_bf16_f32 v150, v152, v153\n"
    "v_mfma_f32_16x16x32_bf16 v[80:83], a[168:171], v[180:183], v[80:83]\n"
    "s_waitcnt lgkmcnt(7)\n"
    "v_exp_f32_e32 v160, v160\n"
    "v_mfma_f32_16x16x32_bf16 v[100:103], a[176:179], v[180:183], v[100:103]\n"
    "ds_read_b128 a[164:167], v241 offset:8192\n"
    "v_mfma_f32_16x16x32_bf16 v[100:103], a[180:183], v[184:187], v[100:103]\n"
    "ds_write_addtid_b32 v149 offset:1280\n"
    "v_mfma_f32_16x16x32_bf16 v[104:107], a[180:183], v[192:195], v[104:107]\n"
    "v_exp_f32_e32 v161, v161\n"
    "v_mfma_f32_16x16x32_bf16 v[104:107], a[176:179], v[188:191], v[104:107]\n"
    "ds_read_b128 a[168:171], v240 offset:12288\n"
    "v_mfma_f32_16x16x32_bf16 v[108:111], a[176:179], v[196:199], v[108:111]\n"
    "ds_write_addtid_b32 v156 offset:2048\n"
    "v_mfma_f32_16x16x32_bf16 v[108:111], a[180:183], v[200:203], v[108:111]\n"
    "v_cvt_pk_bf16_f32 v151, v154, v155\n"
    "v_mfma_f32_16x16x32_bf16 v[112:115], a[180:183], v[208:211], v[112:115]\n"
    "ds_write_addtid_b32 v157 offset:2304\n"
    "v_mfma_f32_16x16x32_bf16 v[112:115], a[176:179], v[204:207], v[112:115]\n"
    "v_exp_f32_e32 v162, v162\n"
    "v_mfma_f32_16x16x32_bf16 v[116:119], a[176:179], v[212:215], v[116:119]\n"
    "ds_write_addtid_b32 v164 offset:3072\n"
    "v_mfma_f32_16x16x32_bf16 v[116:119], a[180:183], v[216:219], v[116:119]\n"
    "s_waitcnt lgkmcnt(10)\n"
    "v_exp_f32_e32 v163, v163\n"
    "v_mfma_f32_16x16x32_bf16 v[136:139], a[188:191], v[216:219], v[136:139]\n"
    "ds_read_b128 a[172:175], v241 offset:12288\n"
    "v_mfma_f32_16x16x32_bf16 v[136:139], a[184:187], v[212:215], v[136:139]\n"
    "ds_write_addtid_b32 v165 offset:3328\n"
    "v_cvt_pk_bf16_f32 v158, v160, v161\n"
    "v_mfma_f32_16x16x32_bf16 v[132:135], a[184:187], v[204:207], v[132:135]\n"
    "ds_read_b128 a[176:179], v242 offset:8192\n"
    "v_mfma_f32_16x16x32_bf16 v[132:135], a[188:191], v[208:211], v[132:135]\n"
    "ds_write_addtid_b32 v172 offset:4096\n"
    "v_mfma_f32_16x16x32_bf16 v[128:131], a[188:191], v[200:203], v[128:131]\n"
    "v_exp_f32_e32 v168, v168\n"
    "v_mfma_f32_16x16x32_bf16 v[128:131], a[184:187], v[196:199], v[128:131]\n"
    "ds_write_addtid_b32 v173 offset:4352\n"
    "v_mfma_f32_16x16x32_bf16 v[124:127], a[184:187], v[188:191], v[124:127]\n"
    "v_exp_f32_e32 v169, v169\n"
    "v_mfma_f32_16x16x32_bf16 v[124:127], a[188:191], v[192:195], v[124:127]\n"
    "ds_read_b128 a[180:183], v243 offset:8192\n"
    "v_mfma_f32_16x16x32_bf16 v[120:123], a[188:191], v[184:187], v[120:123]\n"
    "ds_write_addtid_b32 v142 offset:512\n"
    "v_mfma_f32_16x16x32_bf16 v[120:123], a[184:187], v[180:183], v[120:123]\n"
    "s_waitcnt lgkmcnt(12)\n"
    "v_cvt_pk_bf16_f32 v159, v162, v163\n"
    ".Lat_in:\n"
    "v_mfma_f32_16x16x32_bf16 v[212:215], a[160:163], a[128:131], v[236:239]\n"
    ".long 0xbedf00ff\n"
    ".Lat_w4:\n"
    "s_setpc_b64 s[2:3]\n"
    "v_mfma_f32_16x16x32_bf16 v[212:215], a[164:167], a[132:135], v[212:215]\n"
    "v_exp_f32_e32 v170, v170\n"
    "v_mfma_f32_16x16x32_bf16 v[204:207], a[164:167], a[100:103], v[232:235]\n"
    "ds_write_addtid_b32 v143 offset:768\n"
    "v_mfma_f32_16x16x32_bf16 v[204:207], a[160:163], a[96:99], v[204:207]\n"
    "ds_read_b128 a[184:187], v242 offset:12288\n"
    "v_mfma_f32_16x16x32_bf16 v[196:199], a[160:163], a[64:67], v[228:231]\n"
    "v_exp_f32_e32 v171, v171\n"
    "v_mfma_f32_16x16x32_bf16 v[196:199], a[164:167], a[68:71], v[196:199]\n"
    "ds_write_addtid_b32 v150 offset:1536\n"
    "v_mfma_f32_16x16x32_bf16 v[188:191], a[164:167], a[36:39], v[224:227]\n"
    "v_cvt_pk_bf16_f32 v166, v168, v169\n"
    "v_mfma_f32_16x16x32_bf16 v[188:191], a[160:163], a[32:35], v[188:191]\n"
    "v_mfma_f32_16x16x32_bf16 v[180:183], a[160:163], a[0:3], v[220:223]\n"
    "ds_write_addtid_b32 v151 offset:1792\n"
    "v_mfma_f32_16x16x32_bf16 v[180:183], a[164:167], a[4:7], v[180:183]\n"
    "s_waitcnt lgkmcnt(10)\n"
    "v_exp_f32_e32 v176, v176\n"
    "v_mfma_f32_16x16x32_bf16 v[180:183], a[168:171], a[8:11], v[180:183]\n"
    "ds_read_b128 a[188:191], v243 offset:12288\n"
    "v_mfma_f32_16x16x32_bf16 v[180:183], a[172:175], a[12:15], v[180:183]\n"
    "v_exp_f32_e32 v177, v177\n"
    "v_mfma_f32_16x16x32_bf16 v[188:191], a[172:175], a[44:47], v[188:191]\n"
    "ds_write_addtid_b32 v158 offset:2560\n"
    "v_mfma_f32_16x16x32_bf16 v[188:191], a[168:171], a[40:43], v[188:191]\n"
    "ds_read_b128 a[160:163], v240 offset:10240\n"
    "v_mfma_f32_16x16x32_bf16 v[196:199], a[168:171], a[72:75], v[196:199]\n"
    "v_cvt_pk_bf16_f32 v167, v170, v171\n"
    "v_mfma_f32_16x16x32_bf16 v[196:199], a[172:175], a[76:79], v[196:199]\n"
    "ds_write_addtid_b32 v159 offset:2816\n"
    "v_mfma_f32_16x16x32_bf16 v[204:207], a[172:175], a[108:111], v[204:207]\n"
    "v_exp_f32_e32 v178, v178\n"
    "v_mfma_f32_16x16x32_bf16 v[204:207], a[168:171], a[104:107], v[204:207]\n"
    "ds_write_addtid_b32 v166 offset:3584\n"
    "v_mfma_f32_16x16x32_bf16 v[212:215], a[168:171], a[136:139], v[212:215]\n"
    "v_mfma_f32_16x16x32_bf16 v[212:215], a[172:175], a[140:143], v[212:215]\n"
    "s_waitcnt lgkmcnt(10)\n"
    "v_exp_f32_e32 v179, v179\n"
    "v_mfma_f32_16x16x32_bf16 v[212:215], a[176:179], a[144:147], v[212:215]\n"
    "ds_read_b128 a[164:167], v241 offset:10240\n"
    "v_mfma_f32_16x16x32_bf16 v[212:215], a[180:183], a[148:151], v[212:215]\n"
    "v_cvt_pk_bf16_f32 v174, v176, v177\n"
    "v_mfma_f32_16x16x32_bf16 v[204:207], a[180:183], a[116:119], v[204:207]\n"
    "ds_write_addtid_b32 v167 offset:3840\n"
    "v_mfma_f32_16x16x32_bf16 v[204:207], a[176:179], a[112:115], v[204:207]\n"
    "ds_read_b128 a[168:171], v240 offset:14336\n"
    "v_mfma_f32_16x16x32_bf16 v[196:199], a[176:179], a[80:83], v[196:199]\n"
    "v_cvt_pk_bf16_f32 v175, v178, v179\n"
    "v_mfma_f32_16x16x32_bf16 v[196:199], a[180:183], a[84:87], v[196:199]\n"
    "ds_write_addtid_b32 v174 offset:4608\n"
    "v_mfma_f32_16x16x32_bf16 v[188:191], a[180:183], a[52:55], v[188:191]\n"
    "ds_write_addtid_b32 v175 offset:4864\n"
    "v_mfma_f32_16x16x32_bf16 v[188:191], a[176:179], a[48:51], v[188:191]\n"
    "v_mfma_f32_16x16x32_bf16 v[180:183], a[176:179], a[16:19], v[180:183]\n"
    "ds_read_b128 a[172:175], v241 offset:14336\n"
    "v_mfma_f32_16x16x32_bf16 v[180:183], a[180:183], a[20:23], v[180:183]\n"
    "s_waitcnt lgkmcnt(10)\n"
    "s_waitcnt lgkmcnt(1)\n"
    "v_mfma_f32_16x16x32_bf16 v[180:183], a[184:187], a[24:27], v[180:183]\n"
    "s_barrier\n"
    "v_mfma_f32_16x16x32_bf16 v[180:183], a[188:191], a[28:31], v[180:183]\n"
    "s_add_u32 m0, s43, 24576\n"
    "v_mfma_f32_16x16x32_bf16 v[188:191], a[188:191], a[60:63], v[188:191]\n"
    "ds_read_b128 a[176:179], v242 offset:10240\n"
    "v_mfma_f32_16x16x32_bf16 v[188:191], a[184:187], a[56:59], v[188:191]\n"
    "buffer_load_dwordx4 v248, s[8:11], s40 offen offset:0 sc1 lds\n"
    "v_mfma_f32_16x16x32_bf16 v[196:199], a[184:187], a[88:91], v[196:199]\n"
    "ds_read2st64_b32 v[144:145], v249 offset0:0 offset1:1\n"
    "v_mfma_f32_16x16x32_bf16 v[196:199], a[188:191], a[92:95], v[196:199]\n"
    "v_mfma_f32_16x16x32_bf16 v[204:207], a[188:191], a[124:127], v[204:207]\n"
    "ds_read2st64_b32 v[146:147], v249 offset0:2 offset1:3\n"
    "v_mfma_f32_16x16x32_bf16 v[204:207], a[184:187], a[120:123], v[204:207]\n"
    "buffer_load_dwordx4 v248, s[8:11], s40 offen offset:1024 sc1 lds\n"
    "v_mfma_f32_16x16x32_bf16 v[212:215], a[184:187], a[152:155], v[212:215]\n"
    "v_mfma_f32_16x16x32_bf16 v[212:215], a[188:191], a[156:159], v[212:215]\n"
    "v_mfma_f32_16x16x32_bf16 v[216:219], a[160:163], a[128:131], v[236:239]\n"
    "ds_read_b128 a[180:183], v243 offset:10240\n"
    "v_mfma_f32_16x16x32_bf16 v[216:219], a[164:167], a[132:135], v[216:219]\n"
    "buffer_load_dwordx4 v248, s[8:11], s40 offen offset:2048 sc1 lds\n"
    "v_mfma_f32_16x16x32_bf16 v[208:211], a[164:167], a[100:103], v[232:235]\n"
    "ds_read2st64_b32 v[152:153], v249 offset0:4 offset1:5\n"
    "v_mfma_f32_16x16x32_bf16 v[208:211], a[160:163], a[96:99], v[208:211]\n"
    "ds_read_b128 a[184:187], v242 offset:14336\n"
    "v_mfma_f32_16x16x32_bf16 v[200:203], a[160:163], a[64:67], v[228:231]\n"
    "ds_read2st64_b32 v[154:155], v249 offset0:6 offset1:7\n"
    "v_mfma_f32_16x16x32_bf16 v[200:203], a[164:167], a[68:71], v[200:203]\n"
    "buffer_load_dwordx4 v248, s[8:11], s40 offen offset:3072 sc1 lds\n"
    "v_mfma_f32_16x16x32_bf16 v[192:195], a[164:167], a[36:39], v[224:227]\n"
    "s_add_u32 m0, s43, 28672\n"
    "v_exp_f32_e32 v180, v180\n"
    "v_mfma_f32_16x16x32_bf16 v[192:195], a[160:163], a[32:35], v[192:195]\n"
    "s_add_u32 s40, s40, 0x8000\n"
    "v_exp_f32_e32 v181, v181\n"
    "v_mfma_f32_16x16x32_bf16 v[184:187], a[160:163], a[0:3], v[220:223]\n"
    "ds_read2st64_b32 v[160:161], v249 offset0:8 offset1:9\n"
    "v_mfma_f32_16x16x32_bf16 v[184:187], a[164:167], a[4:7], v[184:187]\n"
    "s_waitcnt lgkmcnt(8)\n"
    "buffer_load_dwordx4 v248, s[8:11], s41 offen offset:0 sc1 lds\n"
    "v_mfma_f32_16x16x32_bf16 v[184:187], a[168:171], a[8:11], v[184:187]\n"
    "ds_read_b128 a[188:191], v243 offset:14336\n"
    "v_mfma_f32_16x16x32_bf16 v[184:187], a[172:175], a[12:15], v[184:187]\n"
    "ds_read_b64_tr_b16 a[160:161], v244 offset:0\n"
    "v_mfma_f32_16x16x32_bf16 v[192:195], a[172:175], a[44:47], v[192:195]\n"
    "ds_read2st64_b32 v[162:163], v249 offset0:10 offset1:11\n"
    "v_mfma_f32_16x16x32_bf16 v[192:195], a[168:171], a[40:43], v[192:195]\n"
    "buffer_load_dwordx4 v248, s[8:11], s41 offen offset:1024 sc1 lds\n"
    "v_mfma_f32_16x16x32_bf16 v[200:203], a[168:171], a[72:75], v[200:203]\n"
    "ds_read_b64_tr_b16 a[162:163], v244 offset:2048\n"
    "v_mfma_f32_16x16x32_bf16 v[200:203], a[172:175], a[76:79], v[200:203]\n"
    "v_exp_f32_e32 v182, v182\n"
    "v_mfma_f32_16x16x32_bf16 v[208:211], a[172:175], a[108:111], v[208:211]\n"
    "ds_read2st64_b32 v[168:169], v249 offset0:12 offset1:13\n"
    "v_mfma_f32_16x16x32_bf16 v[208:211], a[168:171], a[104:107], v[208:211]\n"
    "buffer_load_dwordx4 v248, s[8:11], s41 offen offset:2048 sc1 lds\n"
    "v_mfma_f32_16x16x32_bf16 v[216:219], a[168:171], a[136:139], v[216:219]\n"
    "ds_read_b64_tr_b16 a[164:165], v244 offset:32768\n"
    "v_mfma_f32_16x16x32_bf16 v[216:219], a[172:175], a[140:143], v[216:219]\n"
    "s_waitcnt lgkmcnt(10)\n"
    "ds_read2st64_b32 v[170:171], v249 offset0:14 offset1:15\n"
    "v_mfma_f32_16x16x32_bf16 v[216:219], a[176:179], a[144:147], v[216:219]\n"
    "ds_read_b64_tr_b16 a[168:169], v245 offset:0\n"
    "v_mfma_f32_16x16x32_bf16 v[216:219], a[180:183], a[148:151], v[216:219]\n"
    "buffer_load_dwordx4 v248, s[8:11], s41 offen offset:3072 sc1 lds\n"
    "v_mfma_f32_16x16x32_bf16 v[208:211], a[180:183], a[116:119], v[208:211]\n"
    "ds_read_b64_tr_b16 a[166:167], v244 offset:34816\n"
    "v_mfma_f32_16x16x32_bf16 v[208:211], a[176:179], a[112:115], v[208:211]\n"
    "s_add_u32 s41, s41, 0x8000\n"
    "v_exp_f32_e32 v183, v183\n"
    "v_mfma_f32_16x16x32_bf16 v[200:203], a[176:179], a[80:83], v[200:203]\n"
    "ds_read_b64_tr_b16 a[170:171], v245 offset:2048\n"
    "v_cvt_pk_bf16_f32 v180, v180, v181\n"
    "v_mfma_f32_16x16x32_bf16 v[200:203], a[180:183], a[84:87], v[200:203]\n"
    "ds_read2st64_b32 v[176:177], v249 offset0:16 offset1:17\n"
    "v_mfma_f32_16x16x32_bf16 v[192:195], a[180:183], a[52:55], v[192:195]\n"
    "v_exp_f32_e32 v188, v188\n"
    "v_mfma_f32_16x16x32_bf16 v[192:195], a[176:179], a[48:51], v[192:195]\n"
    "v_exp_f32_e32 v189, v189\n"
    "v_mfma_f32_16x16x32_bf16 v[184:187], a[176:179], a[16:19], v[184:187]\n"
    "ds_read_b64_tr_b16 a[172:173], v245 offset:32768\n"
    "v_cvt_pk_bf16_f32 v181, v182, v183\n"
    "v_mfma_f32_16x16x32_bf16 v[184:187], a[180:183], a[20:23], v[184:187]\n"
    "s_waitcnt lgkmcnt(11)\n"
    "ds_read2st64_b32 v[178:179], v249 offset0:18 offset1:19\n"
    "v_mfma_f32_16x16x32_bf16 v[184:187], a[184:187], a[24:27], v[184:187]\n"
    "ds_read_b64_tr_b16 a[176:177], v246 offset:0\n"
    "v_mfma_f32_16x16x32_bf16 v[184:187], a[188:191], a[28:31], v[184:187]\n"
    "ds_read_b64_tr_b16 a[174:175], v245 offset:34816\n"
    "v_mfma_f32_16x16x32_bf16 v[192:195], a[188:191], a[60:63], v[192:195]\n"
    "v_exp_f32_e32 v190, v190\n"
    "v_mfma_f32_16x16x32_bf16 v[192:195], a[184:187], a[56:59], v[192:195]\n"
    "v_exp_f32_e32 v191, v191\n"
    "v_mfma_f32_16x16x32_bf16 v[200:203], a[184:187], a[88:91], v[200:203]\n"
    "v_mfma_f32_16x16x32_bf16 v[200:203], a[188:191], a[92:95], v[200:203]\n"
    "ds_read_b64_tr_b16 a[178:179], v246 offset:2048\n"
    "v_mfma_f32_16x16x32_bf16 v[208:211], a[188:191], a[124:127], v[208:211]\n"
    "v_cvt_pk_bf16_f32 v188, v188, v189\n"
    "v_mfma_f32_16x16x32_bf16 v[208:211], a[184:187], a[120:123], v[208:211]\n"
    "v_exp_f32_e32 v196, v196\n"
    "v_mfma_f32_16x16x32_bf16 v[216:219], a[184:187], a[152:155], v[216:219]\n"
    "ds_read_b64_tr_b16 a[180:181], v246 offset:32768\n"
    "v_mfma_f32_16x16x32_bf16 v[216:219], a[188:191], a[156:159], v[216:219]\n"
    "s_waitcnt lgkmcnt(4)\n"
    "v_exp_f32_e32 v197, v197\n"
    "v_mfma_f32_16x16x32_bf16 a[236:239], a[160:163], v[140:143], a[236:239]\n"
    "v_mfma_f32_16x16x32_bf16 a[236:239], a[164:167], v[144:147], a[236:239]\n"
    "ds_read_b64_tr_b16 a[182:183], v246 offset:34816\n"
    "v_mfma_f32_16x16x32_bf16 a[240:243], a[164:167], v[152:155], a[240:243]\n"
    "ds_read_b64_tr_b16 a[184:185], v247 offset:0\n"
    "v_mfma_f32_16x16x32_bf16 a[240:243], a[160:163], v[148:151], a[240:243]\n"
    "v_cvt_pk_bf16_f32 v189, v190, v191\n"
    "v_mfma_f32_16x16x32_bf16 a[244:247], a[160:163], v[156:159], a[244:247]\n"
    "ds_read_b64_tr_b16 a[186:187], v247 offset:2048\n"
    "v_mfma_f32_16x16x32_bf16 a[244:247], a[164:167], v[160:163], a[244:247]\n"
    "v_exp_f32_e32 v198, v198\n"
    "v_mfma_f32_16x16x32_bf16 a[248:251], a[164:167], v[168:171], a[248:251]\n"
    "v_exp_f32_e32 v199, v199\n"
    "v_mfma_f32_16x16x32_bf16 a[248:251], a[160:163], v[164:167], a[248:251]\n"
    "v_cvt_pk_bf16_f32 v196, v196, v197\n"
    "v_mfma_f32_16x16x32_bf16 a[252:255], a[160:163], v[172:175], a[252:255]\n"
    "ds_read_b64_tr_b16 a[188:189], v247 offset:32768\n"
    "v_mfma_f32_16x16x32_bf16 a[252:255], a[164:167], v[176:179], a[252:255]\n"
    "s_waitcnt lgkmcnt(6)\n"
    "v_exp_f32_e32 v204, v204\n"
    "v_mfma_f32_16x16x32_bf16 v[16:19], a[172:175], v[176:179], v[16:19]\n"
    "ds_read_b64_tr_b16 a[160:161], v244 offset:4096\n"
    "v_mfma_f32_16x16x32_bf16 v[16:19], a[168:171], v[172:175], v[16:19]\n"
    "ds_read_b64_tr_b16 a[190:191], v247 offset:34816\n"
    "v_mfma_f32_16x16x32_bf16 v[12:15], a[168:171], v[164:167], v[12:15]\n"
    "v_exp_f32_e32 v205, v205\n"
    "v_mfma_f32_16x16x32_bf16 v[12:15], a[172:175], v[168:171], v[12:15]\n"
    "v_cvt_pk_bf16_f32 v197, v198, v199\n"
    "v_mfma_f32_16x16x32_bf16 v[8:11], a[172:175], v[160:163], v[8:11]\n"
    "ds_read_b64_tr_b16 a[162:163], v244 offset:6144\n"
    "v_mfma_f32_16x16x32_bf16 v[8:11], a[168:171], v[156:159], v[8:11]\n"
    "v_exp_f32_e32 v206, v206\n"
    "v_mfma_f32_16x16x32_bf16 v[4:7], a[168:171], v[148:151], v[4:7]\n"
    "v_exp_f32_e32 v207, v207\n"
    "v_mfma_f32_16x16x32_bf16 v[4:7], a[172:175], v[152:155], v[4:7]\n"
    "v_cvt_pk_bf16_f32 v204, v204, v205\n"
    "v_mfma_f32_16x16x32_bf16 v[0:3], a[172:175], v[144:147], v[0:3]\n"
    "ds_read_b64_tr_b16 a[164:165], v244 offset:36864\n"
    "v_mfma_f32_16x16x32_bf16 v[0:3], a[168:171], v[140:143], v[0:3]\n"
    "s_waitcnt lgkmcnt(7)\n"
    "v_exp_f32_e32 v212, v212\n"
    "v_mfma_f32_16x16x32_bf16 v[20:23], a[176:179], v[140:143], v[20:23]\n"
    "buffer_load_dword v253, off, s[8:11], s82 sc1\n"
    "v_mfma_f32_16x16x32_bf16 v[20:23], a[180:183], v[144:147], v[20:23]\n"
    "ds_read_b64_tr_b16 a[166:167], v244 offset:38912\n"
    "v_mfma_f32_16x16x32_bf16 v[24:27], a[180:183], v[152:155], v[24:27]\n"
    "ds_read_b64_tr_b16 a[168:169], v245 offset:4096\n"
    "v_mfma_f32_16x16x32_bf16 v[24:27], a[176:179], v[148:151], v[24:27]\n"
    "v_exp_f32_e32 v213, v213\n"
    "v_mfma_f32_16x16x32_bf16 v[28:31], a[176:179], v[156:159], v[28:31]\n"
    "ds_read_b64_tr_b16 a[170:171], v245 offset:6144\n"
    "v_mfma_f32_16x16x32_bf16 v[28:31], a[180:183], v[160:163], v[28:31]\n"
    "v_cvt_pk_bf16_f32 v205, v206, v207\n"
    "v_mfma_f32_16x16x32_bf16 v[32:35], a[180:183], v[168:171], v[32:35]\n"
    "v_exp_f32_e32 v214, v214\n"
    "v_mfma_f32_16x16x32_bf16 v[32:35], a[176:179], v[164:167], v[32:35]\n"
    "v_exp_f32_e32 v215, v215\n"
    "v_mfma_f32_16x16x32_bf16 v[36:39], a[176:179], v[172:175], v[36:39]\n"
    "ds_read_b64_tr_b16 a[172:173], v245 offset:36864\n"
    "v_mfma_f32_16x16x32_bf16 v[36:39], a[180:183], v[176:179], v[36:39]\n"
    "s_waitcnt lgkmcnt(6)\n"
    "v_cvt_pk_bf16_f32 v212, v212, v213\n"
    "v_mfma_f32_16x16x32_bf16 v[56:59], a[188:191], v[176:179], v[56:59]\n"
    "ds_read_b64_tr_b16 a[176:177], v246 offset:4096\n"
    "v_mfma_f32_16x16x32_bf16 v[56:59], a[184:187], v[172:175], v[56:59]\n"
    "ds_read_b64_tr_b16 a[174:175], v245 offset:38912\n"
    "v_mfma_f32_16x16x32_bf16 v[52:55], a[184:187], v[164:167], v[52:55]\n"
    "v_cvt_pk_bf16_f32 v213, v214, v215\n"
    "v_mfma_f32_16x16x32_bf16 v[52:55], a[188:191], v[168:171], v[52:55]\n"
    "v_exp_f32_e32 v184, v184\n"
    "v_mfma_f32_16x16x32_bf16 v[48:51], a[188:191], v[160:163], v[48:51]\n"
    "ds_read_b64_tr_b16 a[178:179], v246 offset:6144\n"
    "v_mfma_f32_16x16x32_bf16 v[48:51], a[184:187], v[156:159], v[48:51]\n"
    "v_mfma_f32_16x16x32_bf16 v[44:47], a[184:187], v[148:151], v[44:47]\n"
    "v_exp_f32_e32 v185, v185\n"
    "v_mfma_f32_16x16x32_bf16 v[44:47], a[188:191], v[152:155], v[44:47]\n"
    "v_exp_f32_e32 v186, v186\n"
    "v_mfma_f32_16x16x32_bf16 v[40:43], a[188:191], v[144:147], v[40:43]\n"
    "ds_read_b64_tr_b16 a[180:181], v246 offset:36864\n"
    "v_mfma_f32_16x16x32_bf16 v[40:43], a[184:187], v[140:143], v[40:43]\n"
    "s_waitcnt lgkmcnt(7)\n"
    "v_exp_f32_e32 v187, v187\n"
    "v_mfma_f32_16x16x32_bf16 v[60:63], a[160:163], v[140:143], v[60:63]\n"
    "ds_read_b64_tr_b16 a[184:185], v247 offset:4096\n"
    "v_mfma_f32_16x16x32_bf16 v[60:63], a[164:167], v[144:147], v[60:63]\n"
    "ds_read_b64_tr_b16 a[182:183], v246 offset:38912\n"
    "v_mfma_f32_16x16x32_bf16 v[64:67], a[164:167], v[152:155], v[64:67]\n"
    "ds_read_b64_tr_b16 a[186:187], v247 offset:6144\n"
    "v_mfma_f32_16x16x32_bf16 v[64:67], a[160:163], v[148:151], v[64:67]\n"
    "v_cvt_pk_bf16_f32 v182, v184, v185\n"
    "v_mfma_f32_16x16x32_bf16 v[68:71], a[160:163], v[156:159], v[68:71]\n"
    "v_exp_f32_e32 v192, v192\n"
    "v_mfma_f32_16x16x32_bf16 v[68:71], a[164:167], v[160:163], v[68:71]\n"
    "ds_read_b64_tr_b16 a[188:189], v247 offset:36864\n"
    "v_mfma_f32_16x16x32_bf16 v[72:75], a[164:167], v[168:171], v[72:75]\n"
    "ds_read_b64_tr_b16 a[190:191], v247 offset:38912\n"
    "v_mfma_f32_16x16x32_bf16 v[72:75], a[160:163], v[164:167], v[72:75]\n"
    "s_waitcnt vmcnt(9)\n"
    "v_mfma_f32_16x16x32_bf16 v[76:79], a[160:163], v[172:175], v[76:79]\n"
    "s_barrier\n"
    "v_mfma_f32_16x16x32_bf16 v[76:79], a[164:167], v[176:179], v[76:79]\n"
    "s_waitcnt lgkmcnt(7)\n"
    "s_mov_b32 m0, s42\n"
    "v_mfma_f32_16x16x32_bf16 v[96:99], a[172:175], v[176:179], v[96:99]\n"
    "v_exp_f32_e32 v193, v193\n"
    "v_mfma_f32_16x16x32_bf16 v[96:99], a[168:171], v[172:175], v[96:99]\n"
    "v_cvt_pk_bf16_f32 v183, v186, v187\n"
    "v_mfma_f32_16x16x32_bf16 v[92:95], a[168:171], v[164:167], v[92:95]\n"
    "v_exp_f32_e32 v194, v194\n"
    "v_mfma_f32_16x16x32_bf16 v[92:95], a[172:175], v[168:171], v[92:95]\n"
    "ds_read_b128 a[160:163], v240 offset:16384\n"
    "v_mfma_f32_16x16x32_bf16 v[88:91], a[172:175], v[160:163], v[88:91]\n"
    "ds_write_addtid_b32 v180 offset:0\n"
    "v_mfma_f32_16x16x32_bf16 v[88:91], a[168:171], v[156:159], v[88:91]\n"
    "v_exp_f32_e32 v195, v195\n"
    "v_mfma_f32_16x16x32_bf16 v[84:87], a[168:171], v[148:151], v[84:87]\n"
    "ds_write_addtid_b32 v181 offset:256\n"
    "v_mfma_f32_16x16x32_bf16 v[84:87], a[172:175], v[152:155], v[84:87]\n"
    "ds_write_addtid_b32 v188 offset:1024\n"
    "v_mfma_f32_16x16x32_bf16 v[80:83], a[172:175], v[144:147], v[80:83]\n"
    "v_cvt_pk_bf16_f32 v190, v192, v193\n"
    "v_mfma_f32_16x16x32_bf16 v[80:83], a[168:171], v[140:143], v[80:83]\n"
    "s_waitcnt lgkmcnt(7)\n"
    "v_exp_f32_e32 v200, v200\n"
    "v_mfma_f32_16x16x32_bf16 v[100:103], a[176:179], v[140:143], v[100:103]\n"
    "ds_read_b128 a[164:167], v241 offset:16384\n"
    "v_mfma_f32_16x16x32_bf16 v[100:103], a[180:183], v[144:147], v[100:103]\n"
    "ds_write_addtid_b32 v189 offset:1280\n"
    "v_mfma_f32_16x16x32_bf16 v[104:107], a[180:183], v[152:155], v[104:107]\n"
    "v_exp_f32_e32 v201, v201\n"
    "v_mfma_f32_16x16x32_bf16 v[104:107], a[176:179], v[148:151], v[104:107]\n"
    "ds_read_b128 a[168:171], v240 offset:20480\n"
    "v_mfma_f32_16x16x32_bf16 v[108:111], a[176:179], v[156:159], v[108:111]\n"
    "ds_write_addtid_b32 v196 offset:2048\n"
    "v_mfma_f32_16x16x32_bf16 v[108:111], a[180:183], v[160:163], v[108:111]\n"
    "v_cvt_pk_bf16_f32 v191, v194, v195\n"
    "v_mfma_f32_16x16x32_bf16 v[112:115], a[180:183], v[168:171], v[112:115]\n"
    "ds_write_addtid_b32 v197 offset:2304\n"
    "v_mfma_f32_16x16x32_bf16 v[112:115], a[176:179], v[164:167], v[112:115]\n"
    "v_exp_f32_e32 v202, v202\n"
    "v_mfma_f32_16x16x32_bf16 v[116:119], a[176:179], v[172:175], v[116:119]\n"
    "ds_write_addtid_b32 v204 offset:3072\n"
    "v_mfma_f32_16x16x32_bf16 v[116:119], a[180:183], v[176:179], v[116:119]\n"
    "s_waitcnt lgkmcnt(10)\n"
    "v_exp_f32_e32 v203, v203\n"
    "v_mfma_f32_16x16x32_bf16 v[136:139], a[188:191], v[176:179], v[136:139]\n"
    "ds_read_b128 a[172:175], v241 offset:20480\n"
    "v_mfma_f32_16x16x32_bf16 v[136:139], a[184:187], v[172:175], v[136:139]\n"
    "ds_write_addtid_b32 v205 offset:3328\n"
    "v_cvt_pk_bf16_f32 v198, v200, v201\n"
    "v_mfma_f32_16x16x32_bf16 v[132:135], a[184:187], v[164:167], v[132:135]\n"
    "ds_read_b128 a[176:179], v242 offset:16384\n"
    "v_mfma_f32_16x16x32_bf16 v[132:135], a[188:191], v[168:171], v[132:135]\n"
    "ds_write_addtid_b32 v212 offset:4096\n"
    "v_mfma_f32_16x16x32_bf16 v[128:131], a[188:191], v[160:163], v[128:131]\n"
    "v_exp_f32_e32 v208, v208\n"
    "v_mfma_f32_16x16x32_bf16 v[128:131], a[184:187], v[156:159], v[128:131]\n"
    "ds_write_addtid_b32 v213 offset:4352\n"
    "v_mfma_f32_16x16x32_bf16 v[124:127], a[184:187], v[148:151], v[124:127]\n"
    "v_exp_f32_e32 v209, v209\n"
    "v_mfma_f32_16x16x32_bf16 v[124:127], a[188:191], v[152:155], v[124:127]\n"
    "ds_read_b128 a[180:183], v243 offset:16384\n"
    "v_mfma_f32_16x16x32_bf16 v[120:123], a[188:191], v[144:147], v[120:123]\n"
    "ds_write_addtid_b32 v182 offset:512\n"
    "v_mfma_f32_16x16x32_bf16 v[120:123], a[184:187], v[140:143], v[120:123]\n"
    "s_waitcnt lgkmcnt(12)\n"
    "v_cvt_pk_bf16_f32 v199, v202, v203\n"
    "v_mfma_f32_16x16x32_bf16 v[172:175], a[160:163], a[128:131], v[236:239]\n"
    "v_mfma_f32_16x16x32_bf16 v[172:175], a[164:167], a[132:135], v[172:175]\n"
    "v_exp_f32_e32 v210, v210\n"
    "v_mfma_f32_16x16x32_bf16 v[164:167], a[164:167], a[100:103], v[232:235]\n"
    "ds_write_addtid_b32 v183 offset:768\n"
    "v_mfma_f32_16x16x32_bf16 v[164:167], a[160:163], a[96:99], v[164:167]\n"
    "ds_read_b128 a[184:187], v242 offset:20480\n"
    "v_mfma_f32_16x16x32_bf16 v[156:159], a[160:163], a[64:67], v[228:231]\n"
    "v_exp_f32_e32 v211, v211\n"
    "v_mfma_f32_16x16x32_bf16 v[156:159], a[164:167], a[68:71], v[156:159]\n"
    "ds_write_addtid_b32 v190 offset:1536\n"
    "v_mfma_f32_16x16x32_bf16 v[148:151], a[164:167], a[36:39], v[224:227]\n"
    "v_cvt_pk_bf16_f32 v206, v208, v209\n"
    "v_mfma_f32_16x16x32_bf16 v[148:151], a[160:163], a[32:35], v[148:151]\n"
    "v_mfma_f32_16x16x32_bf16 v[140:143], a[160:163], a[0:3], v[220:223]\n"
    "ds_write_addtid_b32 v191 offset:1792\n"
    "v_mfma_f32_16x16x32_bf16 v[140:143], a[164:167], a[4:7], v[140:143]\n"
    "s_waitcnt lgkmcnt(10)\n"
    "v_exp_f32_e32 v216, v216\n"
    "v_mfma_f32_16x16x32_bf16 v[140:143], a[168:171], a[8:11], v[140:143]\n"
    "ds_read_b128 a[188:191], v243 offset:20480\n"
    "v_mfma_f32_16x16x32_bf16 v[140:143], a[172:175], a[12:15], v[140:143]\n"
    "v_exp_f32_e32 v217, v217\n"
    "v_mfma_f32_16x16x32_bf16 v[148:151], a[172:175], a[44:47], v[148:151]\n"
    "ds_write_addtid_b32 v198 offset:2560\n"
    "v_mfma_f32_16x16x32_bf16 v[148:151], a[168:171], a[40:43], v[148:151]\n"
    "ds_read_b128 a[160:163], v240 offset:18432\n"
    "v_mfma_f32_16x16x32_bf16 v[156:159], a[168:171], a[72:75], v[156:159]\n"
    "v_cvt_pk_bf16_f32 v207, v210, v211\n"
    "v_mfma_f32_16x16x32_bf16 v[156:159], a[172:175], a[76:79], v[156:159]\n"
    "ds_write_addtid_b32 v199 offset:2816\n"
    "v_mfma_f32_16x16x32_bf16 v[164:167], a[172:175], a[108:111], v[164:167]\n"
    "v_exp_f32_e32 v218, v218\n"
    "v_mfma_f32_16x16x32_bf16 v[164:167], a[168:171], a[104:107], v[164:167]\n"
    "ds_write_addtid_b32 v206 offset:3584\n"
    "v_mfma_f32_16x16x32_bf16 v[172:175], a[168:171], a[136:139], v[172:175]\n"
    "v_mfma_f32_16x16x32_bf16 v[172:175], a[172:175], a[140:143], v[172:175]\n"
    "s_waitcnt lgkmcnt(10)\n"
    "v_exp_f32_e32 v219, v219\n"
    "v_mfma_f32_16x16x32_bf16 v[172:175], a[176:179], a[144:147], v[172:175]\n"
    "ds_read_b128 a[164:167], v241 offset:18432\n"
    "v_mfma_f32_16x16x32_bf16 v[172:175], a[180:183], a[148:151], v[172:175]\n"
    "v_cvt_pk_bf16_f32 v214, v216, v217\n"
    "v_mfma_f32_16x16x32_bf16 v[164:167], a[180:183], a[116:119], v[164:167]\n"
    "ds_write_addtid_b32 v207 offset:3840\n"
    "v_mfma_f32_16x16x32_bf16 v[164:167], a[176:179], a[112:115], v[164:167]\n"
    "ds_read_b128 a[168:171], v240 offset:22528\n"
    "v_mfma_f32_16x16x32_bf16 v[156:159], a[176:179], a[80:83], v[156:159]\n"
    "v_cvt_pk_bf16_f32 v215, v218, v219\n"
    "v_mfma_f32_16x16x32_bf16 v[156:159], a[180:183], a[84:87], v[156:159]\n"
    "ds_write_addtid_b32 v214 offset:4608\n"
    "v_mfma_f32_16x16x32_bf16 v[148:151], a[180:183], a[52:55], v[148:151]\n"
    "ds_write_addtid_b32 v215 offset:4864\n"
    "v_mfma_f32_16x16x32_bf16 v[148:151], a[176:179], a[48:51], v[148:151]\n"
    "v_mfma_f32_16x16x32_bf16 v[140:143], a[176:179], a[16:19], v[140:143]\n"
    "ds_read_b128 a[172:175], v241 offset:22528\n"
    "v_mfma_f32_16x16x32_bf16 v[140:143], a[180:183], a[20:23], v[140:143]\n"
    "s_waitcnt lgkmcnt(10)\n"
    "s_waitcnt lgkmcnt(1)\n"
    "v_mfma_f32_16x16x32_bf16 v[140:143], a[184:187], a[24:27], v[140:143]\n"
    "s_barrier\n"
    "v_mfma_f32_16x16x32_bf16 v[140:143], a[188:191], a[28:31], v[140:143]\n"
    "s_add_u32 m0, s43, 0\n"
    "v_mfma_f32_16x16x32_bf16 v[148:151], a[188:191], a[60:63], v[148:151]\n"
    "ds_read_b128 a[176:179], v242 offset:18432\n"
    "v_mfma_f32_16x16x32_bf16 v[148:151], a[184:187], a[56:59], v[148:151]\n"
    "buffer_load_dwordx4 v248, s[8:11], s40 offen offset:0 sc1 lds\n"
    "v_mfma_f32_16x16x32_bf16 v[156:159], a[184:187], a[88:91], v[156:159]\n"
    "ds_read2st64_b32 v[184:185], v249 offset0:0 offset1:1\n"
    "v_mfma_f32_16x16x32_bf16 v[156:159], a[188:191], a[92:95], v[156:159]\n"
    "v_mfma_f32_16x16x32_bf16 v[164:167], a[188:191], a[124:127], v[164:167]\n"
    "ds_read2st64_b32 v[186:187], v249 offset0:2 offset1:3\n"
    "v_mfma_f32_16x16x32_bf16 v[164:167], a[184:187], a[120:123], v[164:167]\n"
    "buffer_load_dwordx4 v248, s[8:11], s40 offen offset:1024 sc1 lds\n"
    "v_mfma_f32_16x16x32_bf16 v[172:175], a[184:187], a[152:155], v[172:175]\n"
    "v_mfma_f32_16x16x32_bf16 v[172:175], a[188:191], a[156:159], v[172:175]\n"
    "v_mfma_f32_16x16x32_bf16 v[176:179], a[160:163], a[128:131], v[236:239]\n"
    "ds_read_b128 a[180:183], v243 offset:18432\n"
    "v_mfma_f32_16x16x32_bf16 v[176:179], a[164:167], a[132:135], v[176:179]\n"
    "buffer_load_dwordx4 v248, s[8:11], s40 offen offset:2048 sc1 lds\n"
    "v_mfma_f32_16x16x32_bf16 v[168:171], a[164:167], a[100:103], v[232:235]\n"
    "ds_read2st64_b32 v[192:193], v249 offset0:4 offset1:5\n"
    "v_mfma_f32_16x16x32_bf16 v[168:171], a[160:163], a[96:99], v[168:171]\n"
    "ds_read_b128 a[184:187], v242 offset:22528\n"
    "v_mfma_f32_16x16x32_bf16 v[160:163], a[160:163], a[64:67], v[228:231]\n"
    "ds_read2st64_b32 v[194:195], v249 offset0:6 offset1:7\n"
    "v_mfma_f32_16x16x32_bf16 v[160:163], a[164:167], a[68:71], v[160:163]\n"
    "buffer_load_dwordx4 v248, s[8:11], s40 offen offset:3072 sc1 lds\n"
    "v_mfma_f32_16x16x32_bf16 v[152:155], a[164:167], a[36:39], v[224:227]\n"
    "s_add_u32 m0, s43, 4096\n"
    "v_exp_f32_e32 v140, v140\n"
    "v_mfma_f32_16x16x32_bf16 v[152:155], a[160:163], a[32:35], v[152:155]\n"
    "s_add_u32 s40, s40, 0x8000\n"
    "v_exp_f32_e32 v141, v141\n"
    "v_mfma_f32_16x16x32_bf16 v[144:147], a[160:163], a[0:3], v[220:223]\n"
    "ds_read2st64_b32 v[200:201], v249 offset0:8 offset1:9\n"
    "v_mfma_f32_16x16x32_bf16 v[144:147], a[164:167], a[4:7], v[144:147]\n"
    "s_waitcnt lgkmcnt(8)\n"
    "buffer_load_dwordx4 v248, s[8:11], s41 offen offset:0 sc1 lds\n"
    "v_mfma_f32_16x16x32_bf16 v[144:147], a[168:171], a[8:11], v[144:147]\n"
    "ds_read_b128 a[188:191], v243 offset:22528\n"
    "v_mfma_f32_16x16x32_bf16 v[144:147], a[172:175], a[12:15], v[144:147]\n"
    "ds_read_b64_tr_b16 a[160:161], v244 offset:8192\n"
    "v_mfma_f32_16x16x32_bf16 v[152:155], a[172:175], a[44:47], v[152:155]\n"
    "ds_read2st64_b32 v[202:203], v249 offset0:10 offset1:11\n"
    "v_mfma_f32_16x16x32_bf16 v[152:155], a[168:171], a[40:43], v[152:155]\n"
    "buffer_load_dwordx4 v248, s[8:11], s41 offen offset:1024 sc1 lds\n"
    "v_mfma_f32_16x16x32_bf16 v[160:163], a[168:171], a[72:75], v[160:163]\n"
    "ds_read_b64_tr_b16 a[162:163], v244 offset:10240\n"
    "v_mfma_f32_16x16x32_bf16 v[160:163], a[172:175], a[76:79], v[160:163]\n"
    "v_exp_f32_e32 v142, v142\n"
    "v_mfma_f32_16x16x32_bf16 v[168:171], a[172:175], a[108:111], v[168:171]\n"
    "ds_read2st64_b32 v[208:209], v249 offset0:12 offset1:13\n"
    "v_mfma_f32_16x16x32_bf16 v[168:171], a[168:171], a[104:107], v[168:171]\n"
    "buffer_load_dwordx4 v248, s[8:11], s41 offen offset:2048 sc1 lds\n"
    "v_mfma_f32_16x16x32_bf16 v[176:179], a[168:171], a[136:139], v[176:179]\n"
    "ds_read_b64_tr_b16 a[164:165], v244 offset:40960\n"
    "v_mfma_f32_16x16x32_bf16 v[176:179], a[172:175], a[140:143], v[176:179]\n"
    "s_waitcnt lgkmcnt(10)\n"
    "ds_read2st64_b32 v[210:211], v249 offset0:14 offset1:15\n"
    "v_mfma_f32_16x16x32_bf16 v[176:179], a[176:179], a[144:147], v[176:179]\n"
    "ds_read_b64_tr_b16 a[168:169], v245 offset:8192\n"
    "v_mfma_f32_16x16x32_bf16 v[176:179], a[180:183], a[148:151], v[176:179]\n"
    "buffer_load_dwordx4 v248, s[8:11], s41 offen offset:3072 sc1 lds\n"
    "v_mfma_f32_16x16x32_bf16 v[168:171], a[180:183], a[116:119], v[168:171]\n"
    "ds_read_b64_tr_b16 a[166:167], v244 offset:43008\n"
    "v_mfma_f32_16x16x32_bf16 v[168:171], a[176:179], a[112:115], v[168:171]\n"
    "s_add_u32 s41, s41, 0x8000\n"
    "v_exp_f32_e32 v143, v143\n"
    "v_mfma_f32_16x16x32_bf16 v[160:163], a[176:179], a[80:83], v[160:163]\n"
    "ds_read_b64_tr_b16 a[170:171], v245 offset:10240\n"
    "v_cvt_pk_bf16_f32 v140, v140, v141\n"
    "v_mfma_f32_16x16x32_bf16 v[160:163], a[180:183], a[84:87], v[160:163]\n"
    "ds_read2st64_b32 v[216:217], v249 offset0:16 offset1:17\n"
    "v_mfma_f32_16x16x32_bf16 v[152:155], a[180:183], a[52:55], v[152:155]\n"
    "v_exp_f32_e32 v148, v148\n"
    "v_mfma_f32_16x16x32_bf16 v[152:155], a[176:179], a[48:51], v[152:155]\n"
    "v_exp_f32_e32 v149, v149\n"
    "v_mfma_f32_16x16x32_bf16 v[144:147], a[176:179], a[16:19], v[144:147]\n"
    "ds_read_b64_tr_b16 a[172:173], v245 offset:40960\n"
    "v_cvt_pk_bf16_f32 v141, v142, v143\n"
    "v_mfma_f32_16x16x32_bf16 v[144:147], a[180:183], a[20:23], v[144:147]\n"
    "s_waitcnt lgkmcnt(11)\n"
    "ds_read2st64_b32 v[218:219], v249 offset0:18 offset1:19\n"
    "v_mfma_f32_16x16x32_bf16 v[144:147], a[184:187], a[24:27], v[144:147]\n"
    "ds_read_b64_tr_b16 a[176:177], v246 offset:8192\n"
    "v_mfma_f32_16x16x32_bf16 v[144:147], a[188:191], a[28:31], v[144:147]\n"
    "ds_read_b64_tr_b16 a[174:175], v245 offset:43008\n"
    "v_mfma_f32_16x16x32_bf16 v[152:155], a[188:191], a[60:63], v[152:155]\n"
    "v_exp_f32_e32 v150, v150\n"
    "v_mfma_f32_16x16x32_bf16 v[152:155], a[184:187], a[56:59], v[152:155]\n"
    "v_exp_f32_e32 v151, v151\n"
    "v_mfma_f32_16x16x32_bf16 v[160:163], a[184:187], a[88:91], v[160:163]\n"
    "s_waitcnt vmcnt(8)\n"
    "v_readfirstlane_b32 s81, v253\n"
    "v_mfma_f32_16x16x32_bf16 v[160:163], a[188:191], a[92:95], v[160:163]\n"
    "ds_read_b64_tr_b16 a[178:179], v246 offset:10240\n"
    "v_mfma_f32_16x16x32_bf16 v[168:171], a[188:191], a[124:127], v[168:171]\n"
    "s_cmp_eq_u32 s81, s39\n"
    "v_cvt_pk_bf16_f32 v148, v148, v149\n"
    "v_mfma_f32_16x16x32_bf16 v[168:171], a[184:187], a[120:123], v[168:171]\n"
    "s_cselect_b32 s88, s83, 2.0\n"
    "v_exp_f32_e32 v156, v156\n"
    "v_mfma_f32_16x16x32_bf16 v[176:179], a[184:187], a[152:155], v[176:179]\n"
    "s_mov_b32 m0, s30\n"
    "ds_read_b64_tr_b16 a[180:181], v246 offset:40960\n"
    "v_mfma_f32_16x16x32_bf16 v[176:179], a[188:191], a[156:159], v[176:179]\n"
    "s_waitcnt lgkmcnt(4)\n"
    "v_exp_f32_e32 v157, v157\n"
    "v_mfma_f32_16x16x32_bf16 a[236:239], a[160:163], v[180:183], a[236:239]\n"
    "buffer_load_dwordx4 v250, s[8:11], s88 offen offset:0 sc1 lds\n"
    "v_mfma_f32_16x16x32_bf16 a[236:239], a[164:167], v[184:187], a[236:239]\n"
    "ds_read_b64_tr_b16 a[182:183], v246 offset:43008\n"
    "v_mfma_f32_16x16x32_bf16 a[240:243], a[164:167], v[192:195], a[240:243]\n"
    "ds_read_b64_tr_b16 a[184:185], v247 offset:8192\n"
    "v_mfma_f32_16x16x32_bf16 a[240:243], a[160:163], v[188:191], a[240:243]\n"
    "v_cvt_pk_bf16_f32 v149, v150, v151\n"
    "v_mfma_f32_16x16x32_bf16 a[244:247], a[160:163], v[196:199], a[244:247]\n"
    "ds_read_b64_tr_b16 a[186:187], v247 offset:10240\n"
    "v_mfma_f32_16x16x32_bf16 a[244:247], a[164:167], v[200:203], a[244:247]\n"
    "v_exp_f32_e32 v158, v158\n"
    "v_mfma_f32_16x16x32_bf16 a[248:251], a[164:167], v[208:211], a[248:251]\n"
    "v_exp_f32_e32 v159, v159\n"
    "v_mfma_f32_16x16x32_bf16 a[248:251], a[160:163], v[204:207], a[248:251]\n"
    "v_cvt_pk_bf16_f32 v156, v156, v157\n"
    "v_mfma_f32_16x16x32_bf16 a[252:255], a[160:163], v[212:215], a[252:255]\n"
    "ds_read_b64_tr_b16 a[188:189], v247 offset:40960\n"
    "v_mfma_f32_16x16x32_bf16 a[252:255], a[164:167], v[216:219], a[252:255]\n"
    "s_waitcnt lgkmcnt(6)\n"
    "v_exp_f32_e32 v164, v164\n"
    "v_mfma_f32_16x16x32_bf16 v[16:19], a[172:175], v[216:219], v[16:19]\n"
    "ds_read_b64_tr_b16 a[160:161], v244 offset:12288\n"
    "v_mfma_f32_16x16x32_bf16 v[16:19], a[168:171], v[212:215], v[16:19]\n"
    "ds_read_b64_tr_b16 a[190:191], v247 offset:43008\n"
    "v_mfma_f32_16x16x32_bf16 v[12:15], a[168:171], v[204:207], v[12:15]\n"
    "v_exp_f32_e32 v165, v165\n"
    "v_mfma_f32_16x16x32_bf16 v[12:15], a[172:175], v[208:211], v[12:15]\n"
    "v_cvt_pk_bf16_f32 v157, v158, v159\n"
    "v_mfma_f32_16x16x32_bf16 v[8:11], a[172:175], v[200:203], v[8:11]\n"
    "ds_read_b64_tr_b16 a[162:163], v244 offset:14336\n"
    "v_mfma_f32_16x16x32_bf16 v[8:11], a[168:171], v[196:199], v[8:11]\n"
    "v_exp_f32_e32 v166, v166\n"
    "v_mfma_f32_16x16x32_bf16 v[4:7], a[168:171], v[188:191], v[4:7]\n"
    "v_exp_f32_e32 v167, v167\n"
    "v_mfma_f32_16x16x32_bf16 v[4:7], a[172:175], v[192:195], v[4:7]\n"
    "v_cvt_pk_bf16_f32 v164, v164, v165\n"
    "v_mfma_f32_16x16x32_bf16 v[0:3], a[172:175], v[184:187], v[0:3]\n"
    "ds_read_b64_tr_b16 a[164:165], v244 offset:45056\n"
    "v_mfma_f32_16x16x32_bf16 v[0:3], a[168:171], v[180:183], v[0:3]\n"
    "s_waitcnt lgkmcnt(7)\n"
    "v_exp_f32_e32 v172, v172\n"
    "v_mfma_f32_16x16x32_bf16 v[20:23], a[176:179], v[180:183], v[20:23]\n"
    "v_mfma_f32_16x16x32_bf16 v[20:23], a[180:183], v[184:187], v[20:23]\n"
    "ds_read_b64_tr_b16 a[166:167], v244 offset:47104\n"
    "v_mfma_f32_16x16x32_bf16 v[24:27], a[180:183], v[192:195], v[24:27]\n"
    "ds_read_b64_tr_b16 a[168:169], v245 offset:12288\n"
    "v_mfma_f32_16x16x32_bf16 v[24:27], a[176:179], v[188:191], v[24:27]\n"
    "v_exp_f32_e32 v173, v173\n"
    "v_mfma_f32_16x16x32_bf16 v[28:31], a[176:179], v[196:199], v[28:31]\n"
    "ds_read_b64_tr_b16 a[170:171], v245 offset:14336\n"
    "v_mfma_f32_16x16x32_bf16 v[28:31], a[180:183], v[200:203], v[28:31]\n"
    "v_cvt_pk_bf16_f32 v165, v166, v167\n"
    "v_mfma_f32_16x16x32_bf16 v[32:35], a[180:183], v[208:211], v[32:35]\n"
    "v_exp_f32_e32 v174, v174\n"
    "v_mfma_f32_16x16x32_bf16 v[32:35], a[176:179], v[204:207], v[32:35]\n"
    "v_exp_f32_e32 v175, v175\n"
    "v_mfma_f32_16x16x32_bf16 v[36:39], a[176:179], v[212:215], v[36:39]\n"
    "ds_read_b64_tr_b16 a[172:173], v245 offset:45056\n"
    "v_mfma_f32_16x16x32_bf16 v[36:39], a[180:183], v[216:219], v[36:39]\n"
    "s_waitcnt lgkmcnt(6)\n"
    "v_cvt_pk_bf16_f32 v172, v172, v173\n"
    "v_mfma_f32_16x16x32_bf16 v[56:59], a[188:191], v[216:219], v[56:59]\n"
    ".long 0xbedf00ff\n"
    ".Lat_w5:\n"
    "s_setpc_b64 s[2:3]\n"
    "ds_read_b64_tr_b16 a[176:177], v246 offset:12288\n"
    "v_mfma_f32_16x16x32_bf16 v[56:59], a[184:187], v[212:215], v[56:59]\n"
    "ds_read_b64_tr_b16 a[174:175], v245 offset:47104\n"
    "v_mfma_f32_16x16x32_bf16 v[52:55], a[184:187], v[204:207], v[52:55]\n"
    "v_cvt_pk_bf16_f32 v173, v174, v175\n"
    "v_mfma_f32_16x16x32_bf16 v[52:55], a[188:191], v[208:211], v[52:55]\n"
    "v_exp_f32_e32 v144, v144\n"
    "v_mfma_f32_16x16x32_bf16 v[48:51], a[188:191], v[200:203], v[48:51]\n"
    "ds_read_b64_tr_b16 a[178:179], v246 offset:14336\n"
    "v_mfma_f32_16x16x32_bf16 v[48:51], a[184:187], v[196:199], v[48:51]\n"
    "v_mfma_f32_16x16x32_bf16 v[44:47], a[184:187], v[188:191], v[44:47]\n"
    "v_exp_f32_e32 v145, v145\n"
    "v_mfma_f32_16x16x32_bf16 v[44:47], a[188:191], v[192:195], v[44:47]\n"
    "v_exp_f32_e32 v146, v146\n"
    "v_mfma_f32_16x16x32_bf16 v[40:43], a[188:191], v[184:187], v[40:43]\n"
    "ds_read_b64_tr_b16 a[180:181], v246 offset:45056\n"
    "v_mfma_f32_16x16x32_bf16 v[40:43], a[184:187], v[180:183], v[40:43]\n"
    "s_waitcnt lgkmcnt(7)\n"
    "v_exp_f32_e32 v147, v147\n"
    "v_mfma_f32_16x16x32_bf16 v[60:63], a[160:163], v[180:183], v[60:63]\n"
    "ds_read_b64_tr_b16 a[184:185], v247 offset:12288\n"
    "v_mfma_f32_16x16x32_bf16 v[60:63], a[164:167], v[184:187], v[60:63]\n"
    "ds_read_b64_tr_b16 a[182:183], v246 offset:47104\n"
    "v_mfma_f32_16x16x32_bf16 v[64:67], a[164:167], v[192:195], v[64:67]\n"
    "ds_read_b64_tr_b16 a[186:187], v247 offset:14336\n"
    "v_mfma_f32_16x16x32_bf16 v[64:67], a[160:163], v[188:191], v[64:67]\n"
    "v_cvt_pk_bf16_f32 v142, v144, v145\n"
    "v_mfma_f32_16x16x32_bf16 v[68:71], a[160:163], v[196:199], v[68:71]\n"
    "v_exp_f32_e32 v152, v152\n"
    "v_mfma_f32_16x16x32_bf16 v[68:71], a[164:167], v[200:203], v[68:71]\n"
    "ds_read_b64_tr_b16 a[188:189], v247 offset:45056\n"
    "v_mfma_f32_16x16x32_bf16 v[72:75], a[164:167], v[208:211], v[72:75]\n"
    "ds_read_b64_tr_b16 a[190:191], v247 offset:47104\n"
    "v_mfma_f32_16x16x32_bf16 v[72:75], a[160:163], v[204:207], v[72:75]\n"
    "s_waitcnt vmcnt(10)\n"
    "v_mfma_f32_16x16x32_bf16 v[76:79], a[160:163], v[212:215], v[76:79]\n"
    "s_barrier\n"
    "v_mfma_f32_16x16x32_bf16 v[76:79], a[164:167], v[216:219], v[76:79]\n"
    "s_waitcnt lgkmcnt(7)\n"
    "s_mov_b32 m0, s42\n"
    "v_mfma_f32_16x16x32_bf16 v[96:99], a[172:175], v[216:219], v[96:99]\n"
    "v_exp_f32_e32 v153, v153\n"
    "v_mfma_f32_16x16x32_bf16 v[96:99], a[168:171], v[212:215], v[96:99]\n"
    "v_cvt_pk_bf16_f32 v143, v146, v147\n"
    "v_mfma_f32_16x16x32_bf16 v[92:95], a[168:171], v[204:207], v[92:95]\n"
    "v_exp_f32_e32 v154, v154\n"
    "v_mfma_f32_16x16x32_bf16 v[92:95], a[172:175], v[208:211], v[92:95]\n"
    "ds_read_b128 a[160:163], v240 offset:24576\n"
    "v_mfma_f32_16x16x32_bf16 v[88:91], a[172:175], v[200:203], v[88:91]\n"
    "ds_write_addtid_b32 v140 offset:0\n"
    "v_mfma_f32_16x16x32_bf16 v[88:91], a[168:171], v[196:199], v[88:91]\n"
    "v_exp_f32_e32 v155, v155\n"
    "v_mfma_f32_16x16x32_bf16 v[84:87], a[168:171], v[188:191], v[84:87]\n"
    "ds_write_addtid_b32 v141 offset:256\n"
    "v_mfma_f32_16x16x32_bf16 v[84:87], a[172:175], v[192:195], v[84:87]\n"
    "ds_write_addtid_b32 v148 offset:1024\n"
    "v_mfma_f32_16x16x32_bf16 v[80:83], a[172:175], v[184:187], v[80:83]\n"
    "v_cvt_pk_bf16_f32 v150, v152, v153\n"
    "v_mfma_f32_16x16x32_bf16 v[80:83], a[168:171], v[180:183], v[80:83]\n"
    "s_waitcnt lgkmcnt(7)\n"
    "v_exp_f32_e32 v160, v160\n"
    "v_mfma_f32_16x16x32_bf16 v[100:103], a[176:179], v[180:183], v[100:103]\n"
    "ds_read_b128 a[164:167], v241 offset:24576\n"
    "v_mfma_f32_16x16x32_bf16 v[100:103], a[180:183], v[184:187], v[100:103]\n"
    "ds_write_addtid_b32 v149 offset:1280\n"
    "v_mfma_f32_16x16x32_bf16 v[104:107], a[180:183], v[192:195], v[104:107]\n"
    "v_exp_f32_e32 v161, v161\n"
    "v_mfma_f32_16x16x32_bf16 v[104:107], a[176:179], v[188:191], v[104:107]\n"
    "ds_read_b128 a[168:171], v240 offset:28672\n"
    "v_mfma_f32_16x16x32_bf16 v[108:111], a[176:179], v[196:199], v[108:111]\n"
    "ds_write_addtid_b32 v156 offset:2048\n"
    "v_mfma_f32_16x16x32_bf16 v[108:111], a[180:183], v[200:203], v[108:111]\n"
    "v_cvt_pk_bf16_f32 v151, v154, v155\n"
    "v_mfma_f32_16x16x32_bf16 v[112:115], a[180:183], v[208:211], v[112:115]\n"
    "ds_write_addtid_b32 v157 offset:2304\n"
    "v_mfma_f32_16x16x32_bf16 v[112:115], a[176:179], v[204:207], v[112:115]\n"
    "v_exp_f32_e32 v162, v162\n"
    "v_mfma_f32_16x16x32_bf16 v[116:119], a[176:179], v[212:215], v[116:119]\n"
    "ds_write_addtid_b32 v164 offset:3072\n"
    "v_mfma_f32_16x16x32_bf16 v[116:119], a[180:183], v[216:219], v[116:119]\n"
    "s_waitcnt lgkmcnt(10)\n"
    "v_exp_f32_e32 v163, v163\n"
    "v_mfma_f32_16x16x32_bf16 v[136:139], a[188:191], v[216:219], v[136:139]\n"
    "ds_read_b128 a[172:175], v241 offset:28672\n"
    "v_mfma_f32_16x16x32_bf16 v[136:139], a[184:187], v[212:215], v[136:139]\n"
    "ds_write_addtid_b32 v165 offset:3328\n"
    "v_cvt_pk_bf16_f32 v158, v160, v161\n"
    "v_mfma_f32_16x16x32_bf16 v[132:135], a[184:187], v[204:207], v[132:135]\n"
    "ds_read_b128 a[176:179], v242 offset:24576\n"
    "v_mfma_f32_16x16x32_bf16 v[132:135], a[188:191], v[208:211], v[132:135]\n"
    "ds_write_addtid_b32 v172 offset:4096\n"
    "v_mfma_f32_16x16x32_bf16 v[128:131], a[188:191], v[200:203], v[128:131]\n"
    "v_exp_f32_e32 v168, v168\n"
    "v_mfma_f32_16x16x32_bf16 v[128:131], a[184:187], v[196:199], v[128:131]\n"
    "s_sub_u32 s44, s44, 1\n"
    "ds_write_addtid_b32 v173 offset:4352\n"
    "v_mfma_f32_16x16x32_bf16 v[124:127], a[184:187], v[188:191], v[124:127]\n"
    "s_cmp_eq_u32 s44, s80\n"
    "v_exp_f32_e32 v169, v169\n"
    "v_mfma_f32_16x16x32_bf16 v[124:127], a[188:191], v[192:195], v[124:127]\n"
    "s_cselect_b32 s88, s39, s81\n"
    "ds_read_b128 a[180:183], v243 offset:24576\n"
    "v_mfma_f32_16x16x32_bf16 v[120:123], a[188:191], v[184:187], v[120:123]\n"
    "s_cmp_lg_u32 s88, s39\n"
    "ds_write_addtid_b32 v142 offset:512\n"
    "v_mfma_f32_16x16x32_bf16 v[120:123], a[184:187], v[180:183], v[120:123]\n"
    "s_waitcnt lgkmcnt(12)\n"
    "v_cvt_pk_bf16_f32 v159, v162, v163\n"
    "s_cbranch_scc1 .Lat_loop\n"
    "s_cmp_eq_u32 s44, 0\n"
    "s_cbranch_scc1 .Lat_tail\n"
    "s_cmp_eq_u32 s85, 0\n"
    "s_cbranch_scc1 .Lat_loop\n"
    "s_cmp_lg_u32 s81, s39\n"
    "s_cbranch_scc1 .Lat_lp0\n"
    ".Lat_g1b:\n"
    "v_lshrrev_b32_e32 v228, 2, v250\n"
    "v_add_u32_e32 v254, s30, v228\n"
    "s_waitcnt vmcnt(0)\n"
    "ds_read_b32 v254, v254 offset:960\n"
    "v_lshrrev_b32_e32 v228, 9, v250\n"
    "v_lshl_add_u32 v251, v228, 2, s30\n"
    "v_and_b32_e32 v252, 0xf0, v250\n"
    "v_lshl_add_u32 v252, v228, 8, v252\n"
    "v_bfe_u32 v229, v250, 8, 1\n"
    "v_lshl_add_u32 v252, v229, 3, v252\n"
    "v_and_b32_e32 v239, 0x1f0, v250\n"
    "v_bfe_u32 v228, v250, 2, 4\n"
    "s_mov_b32 s88, 0xcedc\n"
    "v_bfe_u32 v238, s88, v228, 4\n"
    "v_mul_u32_u24_e32 v238, 0x1111, v238\n"
    "v_mov_b64_e32 v[224:225], 0\n"
    "v_mov_b64_e32 v[226:227], 0\n"
    "s_mov_b32 s85, 0\n"
    "s_mov_b32 s82, 2.0\n"
    "s_mov_b32 s83, 2.0\n"
    "s_mov_b32 s89, 4\n"
    "s_waitcnt lgkmcnt(0)\n"
    "v_readlane_b32 s88, v254, 0\n"
    "v_readlane_b32 s87, v254, 1\n"
    "v_readlane_b32 s84, v254, 2\n"
    "v_readlane_b32 s86, v254, 4\n"
    "s_nop 3\n"
    "s_min_u32 s88, s88, s44\n"
    "s_sub_u32 s80, s44, s88\n"
    "s_cmp_eq_u32 s88, 0\n"
    "s_cbranch_scc1 .Lat_g2\n"
    ".Lat_gloop:\n"
    "v_mfma_f32_16x16x32_bf16 v[212:215], a[160:163], a[128:131], v[220:223]\n"
    "ds_write_addtid_b32 v143 offset:768\n"
    "v_mfma_f32_16x16x32_bf16 v[212:215], a[164:167], a[132:135], v[212:215]\n"
    "v_exp_f32_e32 v170, v170\n"
    "v_mfma_f32_16x16x32_bf16 v[204:207], a[164:167], a[100:103], v[220:223]\n"
    "ds_write_addtid_b32 v150 offset:1536\n"
    "v_mfma_f32_16x16x32_bf16 v[204:207], a[160:163], a[96:99], v[204:207]\n"
    "ds_read_b128 a[184:187], v242 offset:28672\n"
    "v_mfma_f32_16x16x32_bf16 v[196:199], a[160:163], a[64:67], v[220:223]\n"
    "v_exp_f32_e32 v171, v171\n"
    "v_mfma_f32_16x16x32_bf16 v[196:199], a[164:167], a[68:71], v[196:199]\n"
    "ds_write_addtid_b32 v151 offset:1792\n"
    "v_cvt_pk_bf16_f32 v166, v168, v169\n"
    "v_mfma_f32_16x16x32_bf16 v[188:191], a[164:167], a[36:39], v[220:223]\n"
    "v_exp_f32_e32 v176, v176\n"
    "v_mfma_f32_16x16x32_bf16 v[188:191], a[160:163], a[32:35], v[188:191]\n"
    "v_exp_f32_e32 v177, v177\n"
    "v_mfma_f32_16x16x32_bf16 v[180:183], a[160:163], a[0:3], v[220:223]\n"
    "ds_write_addtid_b32 v158 offset:2560\n"
    "v_cvt_pk_bf16_f32 v167, v170, v171\n"
    "v_mfma_f32_16x16x32_bf16 v[180:183], a[164:167], a[4:7], v[180:183]\n"
    "s_waitcnt vmcnt(20)\n"
    "v_exp_f32_e32 v178, v178\n"
    "v_smfmac_f32_16x16x64_bf16 v[224:227], a[232:235], a[192:199], v238\n"
    "s_waitcnt lgkmcnt(10)\n"
    "ds_read_b128 a[188:191], v243 offset:28672\n"
    "v_mfma_f32_16x16x32_bf16 v[180:183], a[168:171], a[8:11], v[180:183]\n"
    "s_bitcmp1_b32 s86, 0\n"
    "ds_read_b128 a[160:163], v240 offset:26624\n"
    "v_mfma_f32_16x16x32_bf16 v[180:183], a[172:175], a[12:15], v[180:183]\n"
    "s_cselect_b64 exec, -1, 0\n"
    "v_cvt_pk_bf16_f32 v236, v224, v225\n"
    "v_cvt_pk_bf16_f32 v237, v226, v227\n"
    "v_mfma_f32_16x16x32_bf16 v[188:191], a[172:175], a[44:47], v[188:191]\n"
    "s_cselect_b32 s88, 512, 0\n"
    "v_mov_b64_e32 v[224:225], 0\n"
    "v_mov_b64_e32 v[226:227], 0\n"
    "v_mfma_f32_16x16x32_bf16 v[188:191], a[168:171], a[40:43], v[188:191]\n"
    "buffer_store_dwordx2 v[236:237], v252, s[8:11], s87 offen sc1\n"
    "v_mfma_f32_16x16x32_bf16 v[196:199], a[168:171], a[72:75], v[196:199]\n"
    "s_mov_b64 exec, -1\n"
    "ds_read2_b32 v[228:229], v251 offset0:0 offset1:2\n"
    "v_mfma_f32_16x16x32_bf16 v[196:199], a[172:175], a[76:79], v[196:199]\n"
    "s_add_u32 s87, s87, s88\n"
    "ds_read2_b32 v[230:231], v251 offset0:4 offset1:6\n"
    "v_mfma_f32_16x16x32_bf16 v[204:207], a[172:175], a[108:111], v[204:207]\n"
    "ds_read_b32 v232, v251 offset:32\n"
    "v_mfma_f32_16x16x32_bf16 v[204:207], a[168:171], a[104:107], v[204:207]\n"
    "v_exp_f32_e32 v179, v179\n"
    "v_mfma_f32_16x16x32_bf16 v[212:215], a[168:171], a[136:139], v[212:215]\n"
    "ds_write_addtid_b32 v159 offset:2816\n"
    "v_cvt_pk_bf16_f32 v174, v176, v177\n"
    "v_mfma_f32_16x16x32_bf16 v[212:215], a[172:175], a[140:143], v[212:215]\n"
    "s_waitcnt lgkmcnt(13)\n"
    "ds_read_b128 a[164:167], v241 offset:26624\n"
    "v_mfma_f32_16x16x32_bf16 v[212:215], a[176:179], a[144:147], v[212:215]\n"
    "ds_write_addtid_b32 v166 offset:3584\n"
    "v_cvt_pk_bf16_f32 v175, v178, v179\n"
    "v_mfma_f32_16x16x32_bf16 v[212:215], a[180:183], a[148:151], v[212:215]\n"
    "ds_read_b128 a[168:171], v240 offset:30720\n"
    "v_mfma_f32_16x16x32_bf16 v[204:207], a[180:183], a[116:119], v[204:207]\n"
    "s_waitcnt lgkmcnt(6)\n"
    "ds_write_addtid_b32 v167 offset:3840\n"
    "v_mad_u32_u16 v234, v228, s90, v239 op_sel:[0,0,0,0]\n"
    "v_mfma_f32_16x16x32_bf16 v[204:207], a[176:179], a[112:115], v[204:207]\n"
    "buffer_load_dwordx4 a[192:195], v234, s[20:23], 0 offen sc1\n"
    "v_mad_u32_u16 v235, v228, s90, v239 op_sel:[1,0,0,0]\n"
    "v_mfma_f32_16x16x32_bf16 v[196:199], a[176:179], a[80:83], v[196:199]\n"
    "ds_write_addtid_b32 v174 offset:4608\n"
    "v_mfma_f32_16x16x32_bf16 v[196:199], a[180:183], a[84:87], v[196:199]\n"
    "ds_write_addtid_b32 v175 offset:4864\n"
    "v_mfma_f32_16x16x32_bf16 v[188:191], a[180:183], a[52:55], v[188:191]\n"
    "buffer_load_dwordx4 a[196:199], v235, s[20:23], 0 offen sc1\n"
    "v_mfma_f32_16x16x32_bf16 v[188:191], a[176:179], a[48:51], v[188:191]\n"
    "v_mfma_f32_16x16x32_bf16 v[180:183], a[176:179], a[16:19], v[180:183]\n"
    "s_waitcnt lgkmcnt(0)\n"
    "v_mfma_f32_16x16x32_bf16 v[180:183], a[180:183], a[20:23], v[180:183]\n"
    "s_barrier\n"
    "v_mfma_f32_16x16x32_bf16 v[180:183], a[184:187], a[24:27], v[180:183]\n"
    "s_add_u32 m0, s43, 8192\n"
    "ds_read_b128 a[172:175], v241 offset:30720\n"
    "v_mfma_f32_16x16x32_bf16 v[180:183], a[188:191], a[28:31], v[180:183]\n"
    "ds_read2st64_b32 v[144:145], v249 offset0:0 offset1:1\n"
    "v_mfma_f32_16x16x32_bf16 v[188:191], a[188:191], a[60:63], v[188:191]\n"
    "buffer_load_dwordx4 v248, s[8:11], s40 offen offset:0 sc1 lds\n"
    "v_mfma_f32_16x16x32_bf16 v[188:191], a[184:187], a[56:59], v[188:191]\n"
    "ds_read_b128 a[176:179], v242 offset:26624\n"
    "v_mfma_f32_16x16x32_bf16 v[196:199], a[184:187], a[88:91], v[196:199]\n"
    "v_mfma_f32_16x16x32_bf16 v[196:199], a[188:191], a[92:95], v[196:199]\n"
    "v_mfma_f32_16x16x32_bf16 v[204:207], a[188:191], a[124:127], v[204:207]\n"
    "buffer_load_dwordx4 v248, s[8:11], s40 offen offset:1024 sc1 lds\n"
    "v_mfma_f32_16x16x32_bf16 v[204:207], a[184:187], a[120:123], v[204:207]\n"
    "ds_read2st64_b32 v[146:147], v249 offset0:2 offset1:3\n"
    "v_mfma_f32_16x16x32_bf16 v[212:215], a[184:187], a[152:155], v[212:215]\n"
    "v_mfma_f32_16x16x32_bf16 v[212:215], a[188:191], a[156:159], v[212:215]\n"
    "ds_read_b128 a[180:183], v243 offset:26624\n"
    "v_mfma_f32_16x16x32_bf16 v[216:219], a[160:163], a[128:131], v[220:223]\n"
    "buffer_load_dwordx4 v248, s[8:11], s40 offen offset:2048 sc1 lds\n"
    "v_mfma_f32_16x16x32_bf16 v[216:219], a[164:167], a[132:135], v[216:219]\n"
    "ds_read2st64_b32 v[152:153], v249 offset0:4 offset1:5\n"
    "v_mfma_f32_16x16x32_bf16 v[208:211], a[164:167], a[100:103], v[220:223]\n"
    "v_exp_f32_e32 v180, v180\n"
    "v_mfma_f32_16x16x32_bf16 v[208:211], a[160:163], a[96:99], v[208:211]\n"
    "ds_read_b128 a[184:187], v242 offset:30720\n"
    "v_mfma_f32_16x16x32_bf16 v[200:203], a[160:163], a[64:67], v[220:223]\n"
    "buffer_load_dwordx4 v248, s[8:11], s40 offen offset:3072 sc1 lds\n"
    "v_mfma_f32_16x16x32_bf16 v[200:203], a[164:167], a[68:71], v[200:203]\n"
    "s_add_u32 m0, s43, 12288\n"
    "ds_read2st64_b32 v[154:155], v249 offset0:6 offset1:7\n"
    "v_mfma_f32_16x16x32_bf16 v[192:195], a[164:167], a[36:39], v[220:223]\n"
    "s_add_u32 s40, s40, 0x8000\n"
    "ds_read2st64_b32 v[160:161], v249 offset0:8 offset1:9\n"
    "v_mfma_f32_16x16x32_bf16 v[192:195], a[160:163], a[32:35], v[192:195]\n"
    "ds_read2st64_b32 v[162:163], v249 offset0:10 offset1:11\n"
    "v_mfma_f32_16x16x32_bf16 v[184:187], a[160:163], a[0:3], v[220:223]\n"
    "buffer_load_dwordx4 v248, s[8:11], s41 offen offset:0 sc1 lds\n"
    "v_mfma_f32_16x16x32_bf16 v[184:187], a[164:167], a[4:7], v[184:187]\n"
    "s_waitcnt vmcnt(17)\n"
    "ds_read_b128 a[188:191], v243 offset:30720\n"
    "v_smfmac_f32_16x16x64_bf16 v[224:227], a[232:235], a[200:207], v238\n"
    "s_waitcnt lgkmcnt(10)\n"
    "v_mad_u32_u16 v234, v229, s90, v239 op_sel:[0,0,0,0]\n"
    "v_mfma_f32_16x16x32_bf16 v[184:187], a[168:171], a[8:11], v[184:187]\n"
    "s_bitcmp1_b32 s86, 1\n"
    "buffer_load_dwordx4 a[200:203], v234, s[20:23], 0 offen sc1\n"
    "v_mad_u32_u16 v235, v229, s90, v239 op_sel:[1,0,0,0]\n"
    "v_mfma_f32_16x16x32_bf16 v[184:187], a[172:175], a[12:15], v[184:187]\n"
    "s_cselect_b64 exec, -1, 0\n"
    "v_cvt_pk_bf16_f32 v236, v224, v225\n"
    "v_cvt_pk_bf16_f32 v237, v226, v227\n"
    "v_mfma_f32_16x16x32_bf16 v[192:195], a[172:175], a[44:47], v[192:195]\n"
    "s_cselect_b32 s88, 512, 0\n"
    "v_mov_b64_e32 v[224:225], 0\n"
    "v_mov_b64_e32 v[226:227], 0\n"
    "v_mfma_f32_16x16x32_bf16 v[192:195], a[168:171], a[40:43], v[192:195]\n"
    "buffer_store_dwordx2 v[236:237], v252, s[8:11], s87 offen sc1\n"
    "v_mfma_f32_16x16x32_bf16 v[200:203], a[168:171], a[72:75], v[200:203]\n"
    "s_mov_b64 exec, -1\n"
    "ds_read_b64_tr_b16 a[160:161], v244 offset:16384\n"
    "v_mfma_f32_16x16x32_bf16 v[200:203], a[172:175], a[76:79], v[200:203]\n"
    "s_add_u32 s87, s87, s88\n"
    "ds_read_b64_tr_b16 a[162:163], v244 offset:18432\n"
    "v_mfma_f32_16x16x32_bf16 v[208:211], a[172:175], a[108:111], v[208:211]\n"
    "buffer_load_dwordx4 v248, s[8:11], s41 offen offset:1024 sc1 lds\n"
    "v_mfma_f32_16x16x32_bf16 v[208:211], a[168:171], a[104:107], v[208:211]\n"
    "ds_read_b64_tr_b16 a[164:165], v244 offset:49152\n"
    "v_mfma_f32_16x16x32_bf16 v[216:219], a[168:171], a[136:139], v[216:219]\n"
    "ds_read2st64_b32 v[168:169], v249 offset0:12 offset1:13\n"
    "v_mfma_f32_16x16x32_bf16 v[216:219], a[172:175], a[140:143], v[216:219]\n"
    "s_waitcnt lgkmcnt(10)\n"
    "ds_read2st64_b32 v[170:171], v249 offset0:14 offset1:15\n"
    "v_mfma_f32_16x16x32_bf16 v[216:219], a[176:179], a[144:147], v[216:219]\n"
    "buffer_load_dwordx4 v248, s[8:11], s41 offen offset:2048 sc1 lds\n"
    "v_mfma_f32_16x16x32_bf16 v[216:219], a[180:183], a[148:151], v[216:219]\n"
    "ds_read_b64_tr_b16 a[166:167], v244 offset:51200\n"
    "v_mfma_f32_16x16x32_bf16 v[208:211], a[180:183], a[116:119], v[208:211]\n"
    "ds_read_b64_tr_b16 a[168:169], v245 offset:16384\n"
    "v_mfma_f32_16x16x32_bf16 v[208:211], a[176:179], a[112:115], v[208:211]\n"
    "v_exp_f32_e32 v181, v181\n"
    "v_mfma_f32_16x16x32_bf16 v[200:203], a[176:179], a[80:83], v[200:203]\n"
    "buffer_load_dwordx4 v248, s[8:11], s41 offen offset:3072 sc1 lds\n"
    "v_mfma_f32_16x16x32_bf16 v[200:203], a[180:183], a[84:87], v[200:203]\n"
    "ds_read_b64_tr_b16 a[170:171], v245 offset:18432\n"
    "v_mfma_f32_16x16x32_bf16 v[192:195], a[180:183], a[52:55], v[192:195]\n"
    "s_add_u32 s41, s41, 0x8000\n"
    "ds_read2st64_b32 v[176:177], v249 offset0:16 offset1:17\n"
    "v_mfma_f32_16x16x32_bf16 v[192:195], a[176:179], a[48:51], v[192:195]\n"
    "buffer_load_dwordx4 a[204:207], v235, s[20:23], 0 offen sc1\n"
    "v_mfma_f32_16x16x32_bf16 v[184:187], a[176:179], a[16:19], v[184:187]\n"
    "ds_read_b64_tr_b16 a[172:173], v245 offset:49152\n"
    "v_mfma_f32_16x16x32_bf16 v[184:187], a[180:183], a[20:23], v[184:187]\n"
    "s_waitcnt lgkmcnt(10)\n"
    "ds_read2st64_b32 v[178:179], v249 offset0:18 offset1:19\n"
    "v_mfma_f32_16x16x32_bf16 v[184:187], a[184:187], a[24:27], v[184:187]\n"
    "ds_read_b64_tr_b16 a[176:177], v246 offset:16384\n"
    "v_mfma_f32_16x16x32_bf16 v[184:187], a[188:191], a[28:31], v[184:187]\n"
    "ds_read_b64_tr_b16 a[174:175], v245 offset:51200\n"
    "v_mfma_f32_16x16x32_bf16 v[192:195], a[188:191], a[60:63], v[192:195]\n"
    "v_exp_f32_e32 v182, v182\n"
    "v_mfma_f32_16x16x32_bf16 v[192:195], a[184:187], a[56:59], v[192:195]\n"
    "v_exp_f32_e32 v183, v183\n"
    "v_mfma_f32_16x16x32_bf16 v[200:203], a[184:187], a[88:91], v[200:203]\n"
    "ds_read_b64_tr_b16 a[178:179], v246 offset:18432\n"
    "v_cvt_pk_bf16_f32 v180, v180, v181\n"
    "v_mfma_f32_16x16x32_bf16 v[200:203], a[188:191], a[92:95], v[200:203]\n"
    "v_exp_f32_e32 v188, v188\n"
    "v_mfma_f32_16x16x32_bf16 v[208:211], a[188:191], a[124:127], v[208:211]\n"
    "v_exp_f32_e32 v189, v189\n"
    "v_mfma_f32_16x16x32_bf16 v[208:211], a[184:187], a[120:123], v[208:211]\n"
    "v_cvt_pk_bf16_f32 v181, v182, v183\n"
    "v_mfma_f32_16x16x32_bf16 v[216:219], a[184:187], a[152:155], v[216:219]\n"
    "ds_read_b64_tr_b16 a[180:181], v246 offset:49152\n"
    "v_mfma_f32_16x16x32_bf16 v[216:219], a[188:191], a[156:159], v[216:219]\n"
    "s_waitcnt vmcnt(20)\n"
    "ds_read_b64_tr_b16 a[182:183], v246 offset:51200\n"
    "v_smfmac_f32_16x16x64_bf16 v[224:227], a[232:235], a[208:215], v238\n"
    "s_waitcnt lgkmcnt(5)\n"
    "v_mad_u32_u16 v234, v230, s90, v239 op_sel:[0,0,0,0]\n"
    "v_mfma_f32_16x16x32_bf16 a[236:239], a[160:163], v[140:143], a[236:239]\n"
    "s_bitcmp1_b32 s86, 2\n"
    "buffer_load_dwordx4 a[208:211], v234, s[20:23], 0 offen sc1\n"
    "v_mad_u32_u16 v235, v230, s90, v239 op_sel:[1,0,0,0]\n"
    "v_mfma_f32_16x16x32_bf16 a[236:239], a[164:167], v[144:147], a[236:239]\n"
    "s_cselect_b64 exec, -1, 0\n"
    "v_cvt_pk_bf16_f32 v236, v224, v225\n"
    "v_cvt_pk_bf16_f32 v237, v226, v227\n"
    "v_mfma_f32_16x16x32_bf16 a[240:243], a[164:167], v[152:155], a[240:243]\n"
    "s_cselect_b32 s88, 512, 0\n"
    "v_mov_b64_e32 v[224:225], 0\n"
    "v_mov_b64_e32 v[226:227], 0\n"
    "v_mfma_f32_16x16x32_bf16 a[240:243], a[160:163], v[148:151], a[240:243]\n"
    "buffer_store_dwordx2 v[236:237], v252, s[8:11], s87 offen sc1\n"
    "v_mfma_f32_16x16x32_bf16 a[244:247], a[160:163], v[156:159], a[244:247]\n"
    "s_mov_b64 exec, -1\n"
    "ds_read_b64_tr_b16 a[184:185], v247 offset:16384\n"
    "v_mfma_f32_16x16x32_bf16 a[244:247], a[164:167], v[160:163], a[244:247]\n"
    "s_add_u32 s87, s87, s88\n"
    "ds_read_b64_tr_b16 a[186:187], v247 offset:18432\n"
    "v_mfma_f32_16x16x32_bf16 a[248:251], a[164:167], v[168:171], a[248:251]\n"
    "buffer_load_dwordx4 a[212:215], v235, s[20:23], 0 offen sc1\n"
    "v_mfma_f32_16x16x32_bf16 a[248:251], a[160:163], v[164:167], a[248:251]\n"
    "ds_read_b64_tr_b16 a[188:189], v247 offset:49152\n"
    "v_mfma_f32_16x16x32_bf16 a[252:255], a[160:163], v[172:175], a[252:255]\n"
    "v_exp_f32_e32 v190, v190\n"
    "v_mfma_f32_16x16x32_bf16 a[252:255], a[164:167], v[176:179], a[252:255]\n"
    "s_waitcnt lgkmcnt(6)\n"
    "v_exp_f32_e32 v191, v191\n"
    "v_mfma_f32_16x16x32_bf16 v[16:19], a[172:175], v[176:179], v[16:19]\n"
    "ds_read_b64_tr_b16 a[190:191], v247 offset:51200\n"
    "v_cvt_pk_bf16_f32 v188, v188, v189\n"
    "v_mfma_f32_16x16x32_bf16 v[16:19], a[168:171], v[172:175], v[16:19]\n"
    "ds_read_b64_tr_b16 a[160:161], v244 offset:20480\n"
    "v_mfma_f32_16x16x32_bf16 v[12:15], a[168:171], v[164:167], v[12:15]\n"
    "v_exp_f32_e32 v196, v196\n"
    "v_mfma_f32_16x16x32_bf16 v[12:15], a[172:175], v[168:171], v[12:15]\n"
    "v_exp_f32_e32 v197, v197\n"
    "v_mfma_f32_16x16x32_bf16 v[8:11], a[172:175], v[160:163], v[8:11]\n"
    "ds_read_b64_tr_b16 a[162:163], v244 offset:22528\n"
    "v_cvt_pk_bf16_f32 v189, v190, v191\n"
    "v_mfma_f32_16x16x32_bf16 v[8:11], a[168:171], v[156:159], v[8:11]\n"
    "v_exp_f32_e32 v198, v198\n"
    "v_mfma_f32_16x16x32_bf16 v[4:7], a[168:171], v[148:151], v[4:7]\n"
    "v_exp_f32_e32 v199, v199\n"
    "v_mfma_f32_16x16x32_bf16 v[4:7], a[172:175], v[152:155], v[4:7]\n"
    "v_cvt_pk_bf16_f32 v196, v196, v197\n"
    "v_mfma_f32_16x16x32_bf16 v[0:3], a[172:175], v[144:147], v[0:3]\n"
    "ds_read_b64_tr_b16 a[164:165], v244 offset:53248\n"
    "v_mfma_f32_16x16x32_bf16 v[0:3], a[168:171], v[140:143], v[0:3]\n"
    "s_waitcnt lgkmcnt(7)\n"
    "v_exp_f32_e32 v204, v204\n"
    "v_mfma_f32_16x16x32_bf16 v[20:23], a[176:179], v[140:143], v[20:23]\n"
    "ds_read_b64_tr_b16 a[168:169], v245 offset:20480\n"
    "v_mfma_f32_16x16x32_bf16 v[20:23], a[180:183], v[144:147], v[20:23]\n"
    "ds_read_b64_tr_b16 a[166:167], v244 offset:55296\n"
    "v_mfma_f32_16x16x32_bf16 v[24:27], a[180:183], v[152:155], v[24:27]\n"
    "v_exp_f32_e32 v205, v205\n"
    "v_mfma_f32_16x16x32_bf16 v[24:27], a[176:179], v[148:151], v[24:27]\n"
    "v_cvt_pk_bf16_f32 v197, v198, v199\n"
    "v_mfma_f32_16x16x32_bf16 v[28:31], a[176:179], v[156:159], v[28:31]\n"
    "ds_read_b64_tr_b16 a[170:171], v245 offset:22528\n"
    "v_mfma_f32_16x16x32_bf16 v[28:31], a[180:183], v[160:163], v[28:31]\n"
    "v_exp_f32_e32 v206, v206\n"
    "v_mfma_f32_16x16x32_bf16 v[32:35], a[180:183], v[168:171], v[32:35]\n"
    "v_exp_f32_e32 v207, v207\n"
    "v_mfma_f32_16x16x32_bf16 v[32:35], a[176:179], v[164:167], v[32:35]\n"
    "v_cvt_pk_bf16_f32 v204, v204, v205\n"
    "v_mfma_f32_16x16x32_bf16 v[36:39], a[176:179], v[172:175], v[36:39]\n"
    "ds_read_b64_tr_b16 a[172:173], v245 offset:53248\n"
    "v_mfma_f32_16x16x32_bf16 v[36:39], a[180:183], v[176:179], v[36:39]\n"
    "s_waitcnt vmcnt(20)\n"
    "ds_read_b64_tr_b16 a[174:175], v245 offset:55296\n"
    "v_smfmac_f32_16x16x64_bf16 v[224:227], a[232:235], a[216:223], v238\n"
    "s_waitcnt lgkmcnt(8)\n"
    "v_mad_u32_u16 v234, v231, s90, v239 op_sel:[0,0,0,0]\n"
    "v_mfma_f32_16x16x32_bf16 v[56:59], a[188:191], v[176:179], v[56:59]\n"
    "s_bitcmp1_b32 s86, 3\n"
    "buffer_load_dwordx4 a[216:219], v234, s[20:23], 0 offen sc1\n"
    "v_mad_u32_u16 v235, v231, s90, v239 op_sel:[1,0,0,0]\n"
    "v_mfma_f32_16x16x32_bf16 v[56:59], a[184:187], v[172:175], v[56:59]\n"
    "s_cselect_b64 exec, -1, 0\n"
    "v_cvt_pk_bf16_f32 v236, v224, v225\n"
    "v_cvt_pk_bf16_f32 v237, v226, v227\n"
    "v_mfma_f32_16x16x32_bf16 v[52:55], a[184:187], v[164:167], v[52:55]\n"
    "s_cselect_b32 s88, 512, 0\n"
    "v_mov_b64_e32 v[224:225], 0\n"
    "v_mov_b64_e32 v[226:227], 0\n"
    "v_mfma_f32_16x16x32_bf16 v[52:55], a[188:191], v[168:171], v[52:55]\n"
    "buffer_store_dwordx2 v[236:237], v252, s[8:11], s87 offen sc1\n"
    "v_mfma_f32_16x16x32_bf16 v[48:51], a[188:191], v[160:163], v[48:51]\n"
    "s_mov_b64 exec, -1\n"
    "ds_read_b64_tr_b16 a[176:177], v246 offset:20480\n"
    "v_mfma_f32_16x16x32_bf16 v[48:51], a[184:187], v[156:159], v[48:51]\n"
    "s_add_u32 s87, s87, s88\n"
    "ds_read_b64_tr_b16 a[178:179], v246 offset:22528\n"
    "v_mfma_f32_16x16x32_bf16 v[44:47], a[184:187], v[148:151], v[44:47]\n"
    "buffer_load_dwordx4 a[220:223], v235, s[20:23], 0 offen sc1\n"
    "v_mfma_f32_16x16x32_bf16 v[44:47], a[188:191], v[152:155], v[44:47]\n"
    "ds_read_b64_tr_b16 a[180:181], v246 offset:53248\n"
    "v_mfma_f32_16x16x32_bf16 v[40:43], a[188:191], v[144:147], v[40:43]\n"
    "v_exp_f32_e32 v212, v212\n"
    "v_mfma_f32_16x16x32_bf16 v[40:43], a[184:187], v[140:143], v[40:43]\n"
    "s_waitcnt lgkmcnt(6)\n"
    "v_exp_f32_e32 v213, v213\n"
    "v_mfma_f32_16x16x32_bf16 v[60:63], a[160:163], v[140:143], v[60:63]\n"
    "ds_read_b64_tr_b16 a[182:183], v246 offset:55296\n"
    "v_cvt_pk_bf16_f32 v205, v206, v207\n"
    "v_mfma_f32_16x16x32_bf16 v[60:63], a[164:167], v[144:147], v[60:63]\n"
    "ds_read_b64_tr_b16 a[184:185], v247 offset:20480\n"
    "v_mfma_f32_16x16x32_bf16 v[64:67], a[164:167], v[152:155], v[64:67]\n"
    "ds_read_b64_tr_b16 a[186:187], v247 offset:22528\n"
    "v_mfma_f32_16x16x32_bf16 v[64:67], a[160:163], v[148:151], v[64:67]\n"
    "v_exp_f32_e32 v214, v214\n"
    "v_mfma_f32_16x16x32_bf16 v[68:71], a[160:163], v[156:159], v[68:71]\n"
    "v_exp_f32_e32 v215, v215\n"
    "v_mfma_f32_16x16x32_bf16 v[68:71], a[164:167], v[160:163], v[68:71]\n"
    "ds_read_b64_tr_b16 a[188:189], v247 offset:53248\n"
    "v_cvt_pk_bf16_f32 v212, v212, v213\n"
    "v_mfma_f32_16x16x32_bf16 v[72:75], a[164:167], v[168:171], v[72:75]\n"
    "ds_read_b64_tr_b16 a[190:191], v247 offset:55296\n"
    "v_cvt_pk_bf16_f32 v213, v214, v215\n"
    "v_mfma_f32_16x16x32_bf16 v[72:75], a[160:163], v[164:167], v[72:75]\n"
    "s_waitcnt vmcnt(30)\n"
    "v_mfma_f32_16x16x32_bf16 v[76:79], a[160:163], v[172:175], v[76:79]\n"
    "s_barrier\n"
    "v_mfma_f32_16x16x32_bf16 v[76:79], a[164:167], v[176:179], v[76:79]\n"
    "s_waitcnt lgkmcnt(8)\n"
    "s_mov_b32 m0, s42\n"
    "v_mfma_f32_16x16x32_bf16 v[96:99], a[172:175], v[176:179], v[96:99]\n"
    "v_exp_f32_e32 v184, v184\n"
    "v_mfma_f32_16x16x32_bf16 v[96:99], a[168:171], v[172:175], v[96:99]\n"
    "v_exp_f32_e32 v185, v185\n"
    "v_mfma_f32_16x16x32_bf16 v[92:95], a[168:171], v[164:167], v[92:95]\n"
    "v_exp_f32_e32 v186, v186\n"
    "v_mfma_f32_16x16x32_bf16 v[92:95], a[172:175], v[168:171], v[92:95]\n"
    "ds_read_b128 a[160:163], v240 offset:0\n"
    "ds_write_addtid_b32 v180 offset:0\n"
    "v_mfma_f32_16x16x32_bf16 v[88:91], a[172:175], v[160:163], v[88:91]\n"
    "v_exp_f32_e32 v187, v187\n"
    "v_mfma_f32_16x16x32_bf16 v[88:91], a[168:171], v[156:159], v[88:91]\n"
    "ds_write_addtid_b32 v181 offset:256\n"
    "v_cvt_pk_bf16_f32 v182, v184, v185\n"
    "v_mfma_f32_16x16x32_bf16 v[84:87], a[168:171], v[148:151], v[84:87]\n"
    "v_exp_f32_e32 v192, v192\n"
    "v_mfma_f32_16x16x32_bf16 v[84:87], a[172:175], v[152:155], v[84:87]\n"
    "v_exp_f32_e32 v193, v193\n"
    "v_mfma_f32_16x16x32_bf16 v[80:83], a[172:175], v[144:147], v[80:83]\n"
    "ds_write_addtid_b32 v188 offset:1024\n"
    "v_cvt_pk_bf16_f32 v183, v186, v187\n"
    "v_mfma_f32_16x16x32_bf16 v[80:83], a[168:171], v[140:143], v[80:83]\n"
    "s_waitcnt vmcnt(20)\n"
    "ds_read_b128 a[164:167], v241 offset:0\n"
    "ds_write_addtid_b32 v189 offset:1280\n"
    "v_smfmac_f32_16x16x64_bf16 v[224:227], a[232:235], a[224:231], v238\n"
    "s_waitcnt lgkmcnt(10)\n"
    "ds_write_addtid_b32 v196 offset:2048\n"
    "v_mad_u32_u16 v234, v232, s90, v239 op_sel:[0,0,0,0]\n"
    "v_mfma_f32_16x16x32_bf16 v[100:103], a[176:179], v[140:143], v[100:103]\n"
    "s_bitcmp1_b32 s86, 4\n"
    "buffer_load_dwordx4 a[224:227], v234, s[20:23], 0 offen sc1\n"
    "v_mad_u32_u16 v235, v232, s90, v239 op_sel:[1,0,0,0]\n"
    "v_mfma_f32_16x16x32_bf16 v[100:103], a[180:183], v[144:147], v[100:103]\n"
    "s_cselect_b64 exec, -1, 0\n"
    "v_cvt_pk_bf16_f32 v236, v224, v225\n"
    "v_cvt_pk_bf16_f32 v237, v226, v227\n"
    "v_mfma_f32_16x16x32_bf16 v[104:107], a[180:183], v[152:155], v[104:107]\n"
    "s_cselect_b32 s88, 512, 0\n"
    "v_mov_b64_e32 v[224:225], 0\n"
    "v_mov_b64_e32 v[226:227], 0\n"
    "v_mfma_f32_16x16x32_bf16 v[104:107], a[176:179], v[148:151], v[104:107]\n"
    "buffer_store_dwordx2 v[236:237], v252, s[8:11], s87 offen sc1\n"
    "v_mfma_f32_16x16x32_bf16 v[108:111], a[176:179], v[156:159], v[108:111]\n"
    "s_mov_b64 exec, -1\n"
    "ds_read_b128 a[168:171], v240 offset:4096\n"
    "ds_write_addtid_b32 v197 offset:2304\n"
    "v_mfma_f32_16x16x32_bf16 v[108:111], a[180:183], v[160:163], v[108:111]\n"
    "s_add_u32 s87, s87, s88\n"
    "v_exp_f32_e32 v194, v194\n"
    "v_mfma_f32_16x16x32_bf16 v[112:115], a[180:183], v[168:171], v[112:115]\n"
    "buffer_load_dwordx4 a[228:231], v235, s[20:23], 0 offen sc1\n"
    "ds_write_addtid_b32 v204 offset:3072\n"
    "v_mfma_f32_16x16x32_bf16 v[112:115], a[176:179], v[164:167], v[112:115]\n"
    "ds_write_addtid_b32 v205 offset:3328\n"
    "v_mfma_f32_16x16x32_bf16 v[116:119], a[176:179], v[172:175], v[116:119]\n"
    "v_exp_f32_e32 v195, v195\n"
    "v_mfma_f32_16x16x32_bf16 v[116:119], a[180:183], v[176:179], v[116:119]\n"
    "s_waitcnt lgkmcnt(11)\n"
    "ds_read_b128 a[172:175], v241 offset:4096\n"
    "v_cvt_pk_bf16_f32 v190, v192, v193\n"
    "v_mfma_f32_16x16x32_bf16 v[136:139], a[188:191], v[176:179], v[136:139]\n"
    "v_exp_f32_e32 v200, v200\n"
    "v_mfma_f32_16x16x32_bf16 v[136:139], a[184:187], v[172:175], v[136:139]\n"
    "v_exp_f32_e32 v201, v201\n"
    "v_mfma_f32_16x16x32_bf16 v[132:135], a[184:187], v[164:167], v[132:135]\n"
    "ds_read_b128 a[176:179], v242 offset:0\n"
    "v_cvt_pk_bf16_f32 v191, v194, v195\n"
    "v_mfma_f32_16x16x32_bf16 v[132:135], a[188:191], v[168:171], v[132:135]\n"
    "v_exp_f32_e32 v202, v202\n"
    "v_mfma_f32_16x16x32_bf16 v[128:131], a[188:191], v[160:163], v[128:131]\n"
    "v_exp_f32_e32 v203, v203\n"
    "v_mfma_f32_16x16x32_bf16 v[128:131], a[184:187], v[156:159], v[128:131]\n"
    "ds_write_addtid_b32 v212 offset:4096\n"
    "v_cvt_pk_bf16_f32 v198, v200, v201\n"
    "v_mfma_f32_16x16x32_bf16 v[124:127], a[184:187], v[148:151], v[124:127]\n"
    "v_exp_f32_e32 v208, v208\n"
    "v_mfma_f32_16x16x32_bf16 v[124:127], a[188:191], v[152:155], v[124:127]\n"
    "ds_read_b128 a[180:183], v243 offset:0\n"
    "ds_write_addtid_b32 v213 offset:4352\n"
    "v_mfma_f32_16x16x32_bf16 v[120:123], a[188:191], v[144:147], v[120:123]\n"
    "v_exp_f32_e32 v209, v209\n"
    "v_mfma_f32_16x16x32_bf16 v[120:123], a[184:187], v[140:143], v[120:123]\n"
    "s_waitcnt lgkmcnt(11)\n"
    "ds_write_addtid_b32 v182 offset:512\n"
    "v_cvt_pk_bf16_f32 v199, v202, v203\n"
    "v_mfma_f32_16x16x32_bf16 v[172:175], a[160:163], a[128:131], v[220:223]\n"
    "ds_write_addtid_b32 v183 offset:768\n"
    "v_mfma_f32_16x16x32_bf16 v[172:175], a[164:167], a[132:135], v[172:175]\n"
    "v_exp_f32_e32 v210, v210\n"
    "v_mfma_f32_16x16x32_bf16 v[164:167], a[164:167], a[100:103], v[220:223]\n"
    "ds_write_addtid_b32 v190 offset:1536\n"
    "v_mfma_f32_16x16x32_bf16 v[164:167], a[160:163], a[96:99], v[164:167]\n"
    "ds_read_b128 a[184:187], v242 offset:4096\n"
    "v_mfma_f32_16x16x32_bf16 v[156:159], a[160:163], a[64:67], v[220:223]\n"
    "v_exp_f32_e32 v211, v211\n"
    "v_mfma_f32_16x16x32_bf16 v[156:159], a[164:167], a[68:71], v[156:159]\n"
    "ds_write_addtid_b32 v191 offset:1792\n"
    "v_cvt_pk_bf16_f32 v206, v208, v209\n"
    "v_mfma_f32_16x16x32_bf16 v[148:151], a[164:167], a[36:39], v[220:223]\n"
    "v_exp_f32_e32 v216, v216\n"
    "v_mfma_f32_16x16x32_bf16 v[148:151], a[160:163], a[32:35], v[148:151]\n"
    "v_exp_f32_e32 v217, v217\n"
    "v_mfma_f32_16x16x32_bf16 v[140:143], a[160:163], a[0:3], v[220:223]\n"
    "ds_write_addtid_b32 v198 offset:2560\n"
    "v_cvt_pk_bf16_f32 v207, v210, v211\n"
    "v_mfma_f32_16x16x32_bf16 v[140:143], a[164:167], a[4:7], v[140:143]\n"
    "s_waitcnt vmcnt(20)\n"
    "v_exp_f32_e32 v218, v218\n"
    "v_smfmac_f32_16x16x64_bf16 v[224:227], a[232:235], a[192:199], v238\n"
    "s_waitcnt lgkmcnt(10)\n"
    "ds_read_b128 a[188:191], v243 offset:4096\n"
    "v_mfma_f32_16x16x32_bf16 v[140:143], a[168:171], a[8:11], v[140:143]\n"
    "s_bitcmp1_b32 s86, 5\n"
    "ds_read_b128 a[160:163], v240 offset:2048\n"
    "v_mfma_f32_16x16x32_bf16 v[140:143], a[172:175], a[12:15], v[140:143]\n"
    "s_cselect_b64 exec, -1, 0\n"
    "v_cvt_pk_bf16_f32 v236, v224, v225\n"
    "v_cvt_pk_bf16_f32 v237, v226, v227\n"
    "v_mfma_f32_16x16x32_bf16 v[148:151], a[172:175], a[44:47], v[148:151]\n"
    ".long 0xbedf00ff\n"
    ".Lat_w6:\n"
    "s_setpc_b64 s[2:3]\n"
    "s_cselect_b32 s88, 512, 0\n"
    "v_mov_b64_e32 v[224:225], 0\n"
    "v_mov_b64_e32 v[226:227], 0\n"
    "v_mfma_f32_16x16x32_bf16 v[148:151], a[168:171], a[40:43], v[148:151]\n"
    "buffer_store_dwordx2 v[236:237], v252, s[8:11], s87 offen sc1\n"
    "v_mfma_f32_16x16x32_bf16 v[156:159], a[168:171], a[72:75], v[156:159]\n"
    "s_mov_b64 exec, -1\n"
    "ds_read2_b32 v[228:229], v251 offset0:10 offset1:12\n"
    "v_mfma_f32_16x16x32_bf16 v[156:159], a[172:175], a[76:79], v[156:159]\n"
    "s_add_u32 s87, s87, s88\n"
    "ds_read2_b32 v[230:231], v251 offset0:14 offset1:16\n"
    "v_mfma_f32_16x16x32_bf16 v[164:167], a[172:175], a[108:111], v[164:167]\n"
    "ds_read_b32 v232, v251 offset:72\n"
    "v_mfma_f32_16x16x32_bf16 v[164:167], a[168:171], a[104:107], v[164:167]\n"
    "v_exp_f32_e32 v219, v219\n"
    "v_mfma_f32_16x16x32_bf16 v[172:175], a[168:171], a[136:139], v[172:175]\n"
    "ds_write_addtid_b32 v199 offset:2816\n"
    "v_cvt_pk_bf16_f32 v214, v216, v217\n"
    "v_mfma_f32_16x16x32_bf16 v[172:175], a[172:175], a[140:143], v[172:175]\n"
    "s_waitcnt lgkmcnt(13)\n"
    "ds_read_b128 a[164:167], v241 offset:2048\n"
    "v_mfma_f32_16x16x32_bf16 v[172:175], a[176:179], a[144:147], v[172:175]\n"
    "ds_write_addtid_b32 v206 offset:3584\n"
    "v_cvt_pk_bf16_f32 v215, v218, v219\n"
    "v_mfma_f32_16x16x32_bf16 v[172:175], a[180:183], a[148:151], v[172:175]\n"
    "ds_read_b128 a[168:171], v240 offset:6144\n"
    "v_mfma_f32_16x16x32_bf16 v[164:167], a[180:183], a[116:119], v[164:167]\n"
    "s_waitcnt lgkmcnt(6)\n"
    "ds_write_addtid_b32 v207 offset:3840\n"
    "v_mad_u32_u16 v234, v228, s90, v239 op_sel:[0,0,0,0]\n"
    "v_mfma_f32_16x16x32_bf16 v[164:167], a[176:179], a[112:115], v[164:167]\n"
    "buffer_load_dwordx4 a[192:195], v234, s[20:23], 0 offen sc1\n"
    "v_mad_u32_u16 v235, v228, s90, v239 op_sel:[1,0,0,0]\n"
    "v_mfma_f32_16x16x32_bf16 v[156:159], a[176:179], a[80:83], v[156:159]\n"
    "ds_write_addtid_b32 v214 offset:4608\n"
    "v_mfma_f32_16x16x32_bf16 v[156:159], a[180:183], a[84:87], v[156:159]\n"
    "ds_write_addtid_b32 v215 offset:4864\n"
    "v_mfma_f32_16x16x32_bf16 v[148:151], a[180:183], a[52:55], v[148:151]\n"
    "buffer_load_dwordx4 a[196:199], v235, s[20:23], 0 offen sc1\n"
    "v_mfma_f32_16x16x32_bf16 v[148:151], a[176:179], a[48:51], v[148:151]\n"
    "v_mfma_f32_16x16x32_bf16 v[140:143], a[176:179], a[16:19], v[140:143]\n"
    "s_waitcnt lgkmcnt(0)\n"
    "v_mfma_f32_16x16x32_bf16 v[140:143], a[180:183], a[20:23], v[140:143]\n"
    "s_barrier\n"
    "v_mfma_f32_16x16x32_bf16 v[140:143], a[184:187], a[24:27], v[140:143]\n"
    "s_add_u32 m0, s43, 16384\n"
    "ds_read_b128 a[172:175], v241 offset:6144\n"
    "v_mfma_f32_16x16x32_bf16 v[140:143], a[188:191], a[28:31], v[140:143]\n"
    "ds_read2st64_b32 v[184:185], v249 offset0:0 offset1:1\n"
    "v_mfma_f32_16x16x32_bf16 v[148:151], a[188:191], a[60:63], v[148:151]\n"
    "buffer_load_dwordx4 v248, s[8:11], s40 offen offset:0 sc1 lds\n"
    "v_mfma_f32_16x16x32_bf16 v[148:151], a[184:187], a[56:59], v[148:151]\n"
    "ds_read_b128 a[176:179], v242 offset:2048\n"
    "v_mfma_f32_16x16x32_bf16 v[156:159], a[184:187], a[88:91], v[156:159]\n"
    "v_mfma_f32_16x16x32_bf16 v[156:159], a[188:191], a[92:95], v[156:159]\n"
    "v_mfma_f32_16x16x32_bf16 v[164:167], a[188:191], a[124:127], v[164:167]\n"
    "buffer_load_dwordx4 v248, s[8:11], s40 offen offset:1024 sc1 lds\n"
    "v_mfma_f32_16x16x32_bf16 v[164:167], a[184:187], a[120:123], v[164:167]\n"
    "ds_read2st64_b32 v[186:187], v249 offset0:2 offset1:3\n"
    "v_mfma_f32_16x16x32_bf16 v[172:175], a[184:187], a[152:155], v[172:175]\n"
    "v_mfma_f32_16x16x32_bf16 v[172:175], a[188:191], a[156:159], v[172:175]\n"
    "ds_read_b128 a[180:183], v243 offset:2048\n"
    "v_mfma_f32_16x16x32_bf16 v[176:179], a[160:163], a[128:131], v[220:223]\n"
    "buffer_load_dwordx4 v248, s[8:11], s40 offen offset:2048 sc1 lds\n"
    "v_mfma_f32_16x16x32_bf16 v[176:179], a[164:167], a[132:135], v[176:179]\n"
    "ds_read2st64_b32 v[192:193], v249 offset0:4 offset1:5\n"
    "v_mfma_f32_16x16x32_bf16 v[168:171], a[164:167], a[100:103], v[220:223]\n"
    "v_exp_f32_e32 v140, v140\n"
    "v_mfma_f32_16x16x32_bf16 v[168:171], a[160:163], a[96:99], v[168:171]\n"
    "ds_read_b128 a[184:187], v242 offset:6144\n"
    "v_mfma_f32_16x16x32_bf16 v[160:163], a[160:163], a[64:67], v[220:223]\n"
    "buffer_load_dwordx4 v248, s[8:11], s40 offen offset:3072 sc1 lds\n"
    "v_mfma_f32_16x16x32_bf16 v[160:163], a[164:167], a[68:71], v[160:163]\n"
    "s_add_u32 m0, s43, 20480\n"
    "ds_read2st64_b32 v[194:195], v249 offset0:6 offset1:7\n"
    "v_mfma_f32_16x16x32_bf16 v[152:155], a[164:167], a[36:39], v[220:223]\n"
    "s_add_u32 s40, s40, 0x8000\n"
    "ds_read2st64_b32 v[200:201], v249 offset0:8 offset1:9\n"
    "v_mfma_f32_16x16x32_bf16 v[152:155], a[160:163], a[32:35], v[152:155]\n"
    "ds_read2st64_b32 v[202:203], v249 offset0:10 offset1:11\n"
    "v_mfma_f32_16x16x32_bf16 v[144:147], a[160:163], a[0:3], v[220:223]\n"
    "buffer_load_dwordx4 v248, s[8:11], s41 offen offset:0 sc1 lds\n"
    "v_mfma_f32_16x16x32_bf16 v[144:147], a[164:167], a[4:7], v[144:147]\n"
    "s_waitcnt vmcnt(17)\n"
    "ds_read_b128 a[188:191], v243 offset:6144\n"
    "v_smfmac_f32_16x16x64_bf16 v[224:227], a[232:235], a[200:207], v238\n"
    "s_waitcnt lgkmcnt(10)\n"
    "v_mad_u32_u16 v234, v229, s90, v239 op_sel:[0,0,0,0]\n"
    "v_mfma_f32_16x16x32_bf16 v[144:147], a[168:171], a[8:11], v[144:147]\n"
    "s_bitcmp1_b32 s86, 6\n"
    "buffer_load_dwordx4 a[200:203], v234, s[20:23], 0 offen sc1\n"
    "v_mad_u32_u16 v235, v229, s90, v239 op_sel:[1,0,0,0]\n"
    "v_mfma_f32_16x16x32_bf16 v[144:147], a[172:175], a[12:15], v[144:147]\n"
    "s_cselect_b64 exec, -1, 0\n"
    "v_cvt_pk_bf16_f32 v236, v224, v225\n"
    "v_cvt_pk_bf16_f32 v237, v226, v227\n"
    "v_mfma_f32_16x16x32_bf16 v[152:155], a[172:175], a[44:47], v[152:155]\n"
    "s_cselect_b32 s88, 512, 0\n"
    "v_mov_b64_e32 v[224:225], 0\n"
    "v_mov_b64_e32 v[226:227], 0\n"
    "v_mfma_f32_16x16x32_bf16 v[152:155], a[168:171], a[40:43], v[152:155]\n"
    "buffer_store_dwordx2 v[236:237], v252, s[8:11], s87 offen sc1\n"
    "v_mfma_f32_16x16x32_bf16 v[160:163], a[168:171], a[72:75], v[160:163]\n"
    "s_mov_b64 exec, -1\n"
    "ds_read_b64_tr_b16 a[160:161], v244 offset:24576\n"
    "v_mfma_f32_16x16x32_bf16 v[160:163], a[172:175], a[76:79], v[160:163]\n"
    "s_add_u32 s87, s87, s88\n"
    "ds_read_b64_tr_b16 a[162:163], v244 offset:26624\n"
    "v_mfma_f32_16x16x32_bf16 v[168:171], a[172:175], a[108:111], v[168:171]\n"
    "buffer_load_dwordx4 v248, s[8:11], s41 offen offset:1024 sc1 lds\n"
    "v_mfma_f32_16x16x32_bf16 v[168:171], a[168:171], a[104:107], v[168:171]\n"
    "ds_read_b64_tr_b16 a[164:165], v244 offset:57344\n"
    "v_mfma_f32_16x16x32_bf16 v[176:179], a[168:171], a[136:139], v[176:179]\n"
    "ds_read2st64_b32 v[208:209], v249 offset0:12 offset1:13\n"
    "v_mfma_f32_16x16x32_bf16 v[176:179], a[172:175], a[140:143], v[176:179]\n"
    "s_waitcnt lgkmcnt(10)\n"
    "ds_read2st64_b32 v[210:211], v249 offset0:14 offset1:15\n"
    "v_mfma_f32_16x16x32_bf16 v[176:179], a[176:179], a[144:147], v[176:179]\n"
    "buffer_load_dwordx4 v248, s[8:11], s41 offen offset:2048 sc1 lds\n"
    "v_mfma_f32_16x16x32_bf16 v[176:179], a[180:183], a[148:151], v[176:179]\n"
    "ds_read_b64_tr_b16 a[166:167], v244 offset:59392\n"
    "v_mfma_f32_16x16x32_bf16 v[168:171], a[180:183], a[116:119], v[168:171]\n"
    "ds_read_b64_tr_b16 a[168:169], v245 offset:24576\n"
    "v_mfma_f32_16x16x32_bf16 v[168:171], a[176:179], a[112:115], v[168:171]\n"
    "v_exp_f32_e32 v141, v141\n"
    "v_mfma_f32_16x16x32_bf16 v[160:163], a[176:179], a[80:83], v[160:163]\n"
    "buffer_load_dwordx4 v248, s[8:11], s41 offen offset:3072 sc1 lds\n"
    "v_mfma_f32_16x16x32_bf16 v[160:163], a[180:183], a[84:87], v[160:163]\n"
    "ds_read_b64_tr_b16 a[170:171], v245 offset:26624\n"
    "v_mfma_f32_16x16x32_bf16 v[152:155], a[180:183], a[52:55], v[152:155]\n"
    "s_add_u32 s41, s41, 0x8000\n"
    "ds_read2st64_b32 v[216:217], v249 offset0:16 offset1:17\n"
    "v_mfma_f32_16x16x32_bf16 v[152:155], a[176:179], a[48:51], v[152:155]\n"
    "buffer_load_dwordx4 a[204:207], v235, s[20:23], 0 offen sc1\n"
    "v_mfma_f32_16x16x32_bf16 v[144:147], a[176:179], a[16:19], v[144:147]\n"
    "ds_read_b64_tr_b16 a[172:173], v245 offset:57344\n"
    "v_mfma_f32_16x16x32_bf16 v[144:147], a[180:183], a[20:23], v[144:147]\n"
    "s_waitcnt lgkmcnt(10)\n"
    "ds_read2st64_b32 v[218:219], v249 offset0:18 offset1:19\n"
    "v_mfma_f32_16x16x32_bf16 v[144:147], a[184:187], a[24:27], v[144:147]\n"
    "ds_read_b64_tr_b16 a[176:177], v246 offset:24576\n"
    "v_mfma_f32_16x16x32_bf16 v[144:147], a[188:191], a[28:31], v[144:147]\n"
    "ds_read_b64_tr_b16 a[174:175], v245 offset:59392\n"
    "v_mfma_f32_16x16x32_bf16 v[152:155], a[188:191], a[60:63], v[152:155]\n"
    "v_exp_f32_e32 v142, v142\n"
    "v_mfma_f32_16x16x32_bf16 v[152:155], a[184:187], a[56:59], v[152:155]\n"
    "v_exp_f32_e32 v143, v143\n"
    "v_mfma_f32_16x16x32_bf16 v[160:163], a[184:187], a[88:91], v[160:163]\n"
    "ds_read_b64_tr_b16 a[178:179], v246 offset:26624\n"
    "v_cvt_pk_bf16_f32 v140, v140, v141\n"
    "v_mfma_f32_16x16x32_bf16 v[160:163], a[188:191], a[92:95], v[160:163]\n"
    "v_exp_f32_e32 v148, v148\n"
    "v_mfma_f32_16x16x32_bf16 v[168:171], a[188:191], a[124:127], v[168:171]\n"
    "v_exp_f32_e32 v149, v149\n"
    "v_mfma_f32_16x16x32_bf16 v[168:171], a[184:187], a[120:123], v[168:171]\n"
    "v_cvt_pk_bf16_f32 v141, v142, v143\n"
    "v_mfma_f32_16x16x32_bf16 v[176:179], a[184:187], a[152:155], v[176:179]\n"
    "ds_read_b64_tr_b16 a[180:181], v246 offset:57344\n"
    "v_mfma_f32_16x16x32_bf16 v[176:179], a[188:191], a[156:159], v[176:179]\n"
    "s_waitcnt vmcnt(20)\n"
    "ds_read_b64_tr_b16 a[182:183], v246 offset:59392\n"
    "v_smfmac_f32_16x16x64_bf16 v[224:227], a[232:235], a[208:215], v238\n"
    "s_waitcnt lgkmcnt(5)\n"
    "v_mad_u32_u16 v234, v230, s90, v239 op_sel:[0,0,0,0]\n"
    "v_mfma_f32_16x16x32_bf16 a[236:239], a[160:163], v[180:183], a[236:239]\n"
    "s_bitcmp1_b32 s86, 7\n"
    "buffer_load_dwordx4 a[208:211], v234, s[20:23], 0 offen sc1\n"
    "v_mad_u32_u16 v235, v230, s90, v239 op_sel:[1,0,0,0]\n"
    "v_mfma_f32_16x16x32_bf16 a[236:239], a[164:167], v[184:187], a[236:239]\n"
    "s_cselect_b64 exec, -1, 0\n"
    "v_cvt_pk_bf16_f32 v236, v224, v225\n"
    "v_cvt_pk_bf16_f32 v237, v226, v227\n"
    "v_mfma_f32_16x16x32_bf16 a[240:243], a[164:167], v[192:195], a[240:243]\n"
    "s_cselect_b32 s88, 512, 0\n"
    "v_mov_b64_e32 v[224:225], 0\n"
    "v_mov_b64_e32 v[226:227], 0\n"
    "v_mfma_f32_16x16x32_bf16 a[240:243], a[160:163], v[188:191], a[240:243]\n"
    "buffer_store_dwordx2 v[236:237], v252, s[8:11], s87 offen sc1\n"
    "v_mfma_f32_16x16x32_bf16 a[244:247], a[160:163], v[196:199], a[244:247]\n"
    "s_mov_b64 exec, -1\n"
    "ds_read_b64_tr_b16 a[184:185], v247 offset:24576\n"
    "v_mfma_f32_16x16x32_bf16 a[244:247], a[164:167], v[200:203], a[244:247]\n"
    "s_add_u32 s87, s87, s88\n"
    "ds_read_b64_tr_b16 a[186:187], v247 offset:26624\n"
    "v_mfma_f32_16x16x32_bf16 a[248:251], a[164:167], v[208:211], a[248:251]\n"
    "buffer_load_dwordx4 a[212:215], v235, s[20:23], 0 offen sc1\n"
    "v_mfma_f32_16x16x32_bf16 a[248:251], a[160:163], v[204:207], a[248:251]\n"
    "ds_read_b64_tr_b16 a[188:189], v247 offset:57344\n"
    "v_mfma_f32_16x16x32_bf16 a[252:255], a[160:163], v[212:215], a[252:255]\n"
    "v_exp_f32_e32 v150, v150\n"
    "v_mfma_f32_16x16x32_bf16 a[252:255], a[164:167], v[216:219], a[252:255]\n"
    "s_waitcnt lgkmcnt(6)\n"
    "v_exp_f32_e32 v151, v151\n"
    "v_mfma_f32_16x16x32_bf16 v[16:19], a[172:175], v[216:219], v[16:19]\n"
    "ds_read_b64_tr_b16 a[190:191], v247 offset:59392\n"
    "v_cvt_pk_bf16_f32 v148, v148, v149\n"
    "v_mfma_f32_16x16x32_bf16 v[16:19], a[168:171], v[212:215], v[16:19]\n"
    "ds_read_b64_tr_b16 a[160:161], v244 offset:28672\n"
    "v_mfma_f32_16x16x32_bf16 v[12:15], a[168:171], v[204:207], v[12:15]\n"
    "v_exp_f32_e32 v156, v156\n"
    "v_mfma_f32_16x16x32_bf16 v[12:15], a[172:175], v[208:211], v[12:15]\n"
    "v_exp_f32_e32 v157, v157\n"
    "v_mfma_f32_16x16x32_bf16 v[8:11], a[172:175], v[200:203], v[8:11]\n"
    "ds_read_b64_tr_b16 a[162:163], v244 offset:30720\n"
    "v_cvt_pk_bf16_f32 v149, v150, v151\n"
    "v_mfma_f32_16x16x32_bf16 v[8:11], a[168:171], v[196:199], v[8:11]\n"
    "v_exp_f32_e32 v158, v158\n"
    "v_mfma_f32_16x16x32_bf16 v[4:7], a[168:171], v[188:191], v[4:7]\n"
    "v_exp_f32_e32 v159, v159\n"
    "v_mfma_f32_16x16x32_bf16 v[4:7], a[172:175], v[192:195], v[4:7]\n"
    "v_cvt_pk_bf16_f32 v156, v156, v157\n"
    "v_mfma_f32_16x16x32_bf16 v[0:3], a[172:175], v[184:187], v[0:3]\n"
    "ds_read_b64_tr_b16 a[164:165], v244 offset:61440\n"
    "v_mfma_f32_16x16x32_bf16 v[0:3], a[168:171], v[180:183], v[0:3]\n"
    "s_waitcnt lgkmcnt(7)\n"
    "v_exp_f32_e32 v164, v164\n"
    "v_mfma_f32_16x16x32_bf16 v[20:23], a[176:179], v[180:183], v[20:23]\n"
    "ds_read_b64_tr_b16 a[168:169], v245 offset:28672\n"
    "v_mfma_f32_16x16x32_bf16 v[20:23], a[180:183], v[184:187], v[20:23]\n"
    "ds_read_b64_tr_b16 a[166:167], v244 offset:63488\n"
    "v_mfma_f32_16x16x32_bf16 v[24:27], a[180:183], v[192:195], v[24:27]\n"
    "v_exp_f32_e32 v165, v165\n"
    "v_mfma_f32_16x16x32_bf16 v[24:27], a[176:179], v[188:191], v[24:27]\n"
    "v_cvt_pk_bf16_f32 v157, v158, v159\n"
    "v_mfma_f32_16x16x32_bf16 v[28:31], a[176:179], v[196:199], v[28:31]\n"
    "ds_read_b64_tr_b16 a[170:171], v245 offset:30720\n"
    "v_mfma_f32_16x16x32_bf16 v[28:31], a[180:183], v[200:203], v[28:31]\n"
    "v_exp_f32_e32 v166, v166\n"
    "v_mfma_f32_16x16x32_bf16 v[32:35], a[180:183], v[208:211], v[32:35]\n"
    "v_exp_f32_e32 v167, v167\n"
    "v_mfma_f32_16x16x32_bf16 v[32:35], a[176:179], v[204:207], v[32:35]\n"
    "v_cvt_pk_bf16_f32 v164, v164, v165\n"
    "v_mfma_f32_16x16x32_bf16 v[36:39], a[176:179], v[212:215], v[36:39]\n"
    "ds_read_b64_tr_b16 a[172:173], v245 offset:61440\n"
    "v_mfma_f32_16x16x32_bf16 v[36:39], a[180:183], v[216:219], v[36:39]\n"
    "s_waitcnt vmcnt(20)\n"
    "ds_read_b64_tr_b16 a[174:175], v245 offset:63488\n"
    "v_smfmac_f32_16x16x64_bf16 v[224:227], a[232:235], a[216:223], v238\n"
    "s_waitcnt lgkmcnt(8)\n"
    "v_mad_u32_u16 v234, v231, s90, v239 op_sel:[0,0,0,0]\n"
    "v_mfma_f32_16x16x32_bf16 v[56:59], a[188:191], v[216:219], v[56:59]\n"
    "s_bitcmp1_b32 s86, 8\n"
    "buffer_load_dwordx4 a[216:219], v234, s[20:23], 0 offen sc1\n"
    "v_mad_u32_u16 v235, v231, s90, v239 op_sel:[1,0,0,0]\n"
    "v_mfma_f32_16x16x32_bf16 v[56:59], a[184:187], v[212:215], v[56:59]\n"
    "s_cselect_b64 exec, -1, 0\n"
    "v_cvt_pk_bf16_f32 v236, v224, v225\n"
    "v_cvt_pk_bf16_f32 v237, v226, v227\n"
    "v_mfma_f32_16x16x32_bf16 v[52:55], a[184:187], v[204:207], v[52:55]\n"
    "s_cselect_b32 s88, 512, 0\n"
    "v_mov_b64_e32 v[224:225], 0\n"
    "v_mov_b64_e32 v[226:227], 0\n"
    "v_mfma_f32_16x16x32_bf16 v[52:55], a[188:191], v[208:211], v[52:55]\n"
    "buffer_store_dwordx2 v[236:237], v252, s[8:11], s87 offen sc1\n"
    "v_mfma_f32_16x16x32_bf16 v[48:51], a[188:191], v[200:203], v[48:51]\n"
    "s_mov_b64 exec, -1\n"
    "ds_read_b64_tr_b16 a[176:177], v246 offset:28672\n"
    "v_mfma_f32_16x16x32_bf16 v[48:51], a[184:187], v[196:199], v[48:51]\n"
    "s_add_u32 s87, s87, s88\n"
    "ds_read_b64_tr_b16 a[178:179], v246 offset:30720\n"
    "v_mfma_f32_16x16x32_bf16 v[44:47], a[184:187], v[188:191], v[44:47]\n"
    "buffer_load_dwordx4 a[220:223], v235, s[20:23], 0 offen sc1\n"
    "v_mfma_f32_16x16x32_bf16 v[44:47], a[188:191], v[192:195], v[44:47]\n"
    "ds_read_b64_tr_b16 a[180:181], v246 offset:61440\n"
    "v_mfma_f32_16x16x32_bf16 v[40:43], a[188:191], v[184:187], v[40:43]\n"
    "v_exp_f32_e32 v172, v172\n"
    "v_mfma_f32_16x16x32_bf16 v[40:43], a[184:187], v[180:183], v[40:43]\n"
    "s_waitcnt lgkmcnt(6)\n"
    "v_exp_f32_e32 v173, v173\n"
    "v_mfma_f32_16x16x32_bf16 v[60:63], a[160:163], v[180:183], v[60:63]\n"
    "ds_read_b64_tr_b16 a[182:183], v246 offset:63488\n"
    "v_cvt_pk_bf16_f32 v165, v166, v167\n"
    "v_mfma_f32_16x16x32_bf16 v[60:63], a[164:167], v[184:187], v[60:63]\n"
    "ds_read_b64_tr_b16 a[184:185], v247 offset:28672\n"
    "v_mfma_f32_16x16x32_bf16 v[64:67], a[164:167], v[192:195], v[64:67]\n"
    "ds_read_b64_tr_b16 a[186:187], v247 offset:30720\n"
    "v_mfma_f32_16x16x32_bf16 v[64:67], a[160:163], v[188:191], v[64:67]\n"
    "v_exp_f32_e32 v174, v174\n"
    "v_mfma_f32_16x16x32_bf16 v[68:71], a[160:163], v[196:199], v[68:71]\n"
    "v_exp_f32_e32 v175, v175\n"
    "v_mfma_f32_16x16x32_bf16 v[68:71], a[164:167], v[200:203], v[68:71]\n"
    "ds_read_b64_tr_b16 a[188:189], v247 offset:61440\n"
    "v_cvt_pk_bf16_f32 v172, v172, v173\n"
    "v_mfma_f32_16x16x32_bf16 v[72:75], a[164:167], v[208:211], v[72:75]\n"
    "ds_read_b64_tr_b16 a[190:191], v247 offset:63488\n"
    "v_cvt_pk_bf16_f32 v173, v174, v175\n"
    "v_mfma_f32_16x16x32_bf16 v[72:75], a[160:163], v[204:207], v[72:75]\n"
    "s_waitcnt vmcnt(30)\n"
    "v_mfma_f32_16x16x32_bf16 v[76:79], a[160:163], v[212:215], v[76:79]\n"
    "s_barrier\n"
    "v_mfma_f32_16x16x32_bf16 v[76:79], a[164:167], v[216:219], v[76:79]\n"
    "s_waitcnt lgkmcnt(8)\n"
    "s_mov_b32 m0, s42\n"
    "v_mfma_f32_16x16x32_bf16 v[96:99], a[172:175], v[216:219], v[96:99]\n"
    "v_exp_f32_e32 v144, v144\n"
    "v_mfma_f32_16x16x32_bf16 v[96:99], a[168:171], v[212:215], v[96:99]\n"
    "v_exp_f32_e32 v145, v145\n"
    "v_mfma_f32_16x16x32_bf16 v[92:95], a[168:171], v[204:207], v[92:95]\n"
    "v_exp_f32_e32 v146, v146\n"
    "v_mfma_f32_16x16x32_bf16 v[92:95], a[172:175], v[208:211], v[92:95]\n"
    "ds_read_b128 a[160:163], v240 offset:8192\n"
    "ds_write_addtid_b32 v140 offset:0\n"
    "v_mfma_f32_16x16x32_bf16 v[88:91], a[172:175], v[200:203], v[88:91]\n"
    "v_exp_f32_e32 v147, v147\n"
    "v_mfma_f32_16x16x32_bf16 v[88:91], a[168:171], v[196:199], v[88:91]\n"
    "ds_write_addtid_b32 v141 offset:256\n"
    "v_cvt_pk_bf16_f32 v142, v144, v145\n"
    "v_mfma_f32_16x16x32_bf16 v[84:87], a[168:171], v[188:191], v[84:87]\n"
    "v_exp_f32_e32 v152, v152\n"
    "v_mfma_f32_16x16x32_bf16 v[84:87], a[172:175], v[192:195], v[84:87]\n"
    "v_exp_f32_e32 v153, v153\n"
    "v_mfma_f32_16x16x32_bf16 v[80:83], a[172:175], v[184:187], v[80:83]\n"
    "ds_write_addtid_b32 v148 offset:1024\n"
    "v_cvt_pk_bf16_f32 v143, v146, v147\n"
    "v_mfma_f32_16x16x32_bf16 v[80:83], a[168:171], v[180:183], v[80:83]\n"
    "s_waitcnt vmcnt(20)\n"
    "ds_read_b128 a[164:167], v241 offset:8192\n"
    "ds_write_addtid_b32 v149 offset:1280\n"
    "v_smfmac_f32_16x16x64_bf16 v[224:227], a[232:235], a[224:231], v238\n"
    "s_waitcnt lgkmcnt(10)\n"
    "ds_write_addtid_b32 v156 offset:2048\n"
    "v_mad_u32_u16 v234, v232, s90, v239 op_sel:[0,0,0,0]\n"
    "v_mfma_f32_16x16x32_bf16 v[100:103], a[176:179], v[180:183], v[100:103]\n"
    "s_bitcmp1_b32 s86, 9\n"
    "buffer_load_dwordx4 a[224:227], v234, s[20:23], 0 offen sc1\n"
    "v_mad_u32_u16 v235, v232, s90, v239 op_sel:[1,0,0,0]\n"
    "v_mfma_f32_16x16x32_bf16 v[100:103], a[180:183], v[184:187], v[100:103]\n"
    "s_cselect_b64 exec, -1, 0\n"
    "v_cvt_pk_bf16_f32 v236, v224, v225\n"
    "v_cvt_pk_bf16_f32 v237, v226, v227\n"
    "v_mfma_f32_16x16x32_bf16 v[104:107], a[180:183], v[192:195], v[104:107]\n"
    "s_cselect_b32 s88, 512, 0\n"
    "v_mov_b64_e32 v[224:225], 0\n"
    "v_mov_b64_e32 v[226:227], 0\n"
    "v_mfma_f32_16x16x32_bf16 v[104:107], a[176:179], v[188:191], v[104:107]\n"
    "buffer_store_dwordx2 v[236:237], v252, s[8:11], s87 offen sc1\n"
    "v_mfma_f32_16x16x32_bf16 v[108:111], a[176:179], v[196:199], v[108:111]\n"
    "s_mov_b64 exec, -1\n"
    "ds_read_b128 a[168:171], v240 offset:12288\n"
    "ds_write_addtid_b32 v157 offset:2304\n"
    "v_mfma_f32_16x16x32_bf16 v[108:111], a[180:183], v[200:203], v[108:111]\n"
    "s_add_u32 s87, s87, s88\n"
    "v_exp_f32_e32 v154, v154\n"
    "v_mfma_f32_16x16x32_bf16 v[112:115], a[180:183], v[208:211], v[112:115]\n"
    "buffer_load_dwordx4 a[228:231], v235, s[20:23], 0 offen sc1\n"
    "ds_write_addtid_b32 v164 offset:3072\n"
    "v_mfma_f32_16x16x32_bf16 v[112:115], a[176:179], v[204:207], v[112:115]\n"
    "ds_write_addtid_b32 v165 offset:3328\n"
    "v_mfma_f32_16x16x32_bf16 v[116:119], a[176:179], v[212:215], v[116:119]\n"
    "v_exp_f32_e32 v155, v155\n"
    "v_mfma_f32_16x16x32_bf16 v[116:119], a[180:183], v[216:219], v[116:119]\n"
    "s_waitcnt lgkmcnt(11)\n"
    "ds_read_b128 a[172:175], v241 offset:12288\n"
    "v_cvt_pk_bf16_f32 v150, v152, v153\n"
    "v_mfma_f32_16x16x32_bf16 v[136:139], a[188:191], v[216:219], v[136:139]\n"
    "v_exp_f32_e32 v160, v160\n"
    "v_mfma_f32_16x16x32_bf16 v[136:139], a[184:187], v[212:215], v[136:139]\n"
    "v_exp_f32_e32 v161, v161\n"
    "v_mfma_f32_16x16x32_bf16 v[132:135], a[184:187], v[204:207], v[132:135]\n"
    "ds_read_b128 a[176:179], v242 offset:8192\n"
    "v_cvt_pk_bf16_f32 v151, v154, v155\n"
    "v_mfma_f32_16x16x32_bf16 v[132:135], a[188:191], v[208:211], v[132:135]\n"
    "v_exp_f32_e32 v162, v162\n"
    "v_mfma_f32_16x16x32_bf16 v[128:131], a[188:191], v[200:203], v[128:131]\n"
    "v_exp_f32_e32 v163, v163\n"
    "v_mfma_f32_16x16x32_bf16 v[128:131], a[184:187], v[196:199], v[128:131]\n"
    "ds_write_addtid_b32 v172 offset:4096\n"
    "v_cvt_pk_bf16_f32 v158, v160, v161\n"
    "v_mfma_f32_16x16x32_bf16 v[124:127], a[184:187], v[188:191], v[124:127]\n"
    "v_exp_f32_e32 v168, v168\n"
    "v_mfma_f32_16x16x32_bf16 v[124:127], a[188:191], v[192:195], v[124:127]\n"
    "ds_read_b128 a[180:183], v243 offset:8192\n"
    "ds_write_addtid_b32 v173 offset:4352\n"
    "v_mfma_f32_16x16x32_bf16 v[120:123], a[188:191], v[184:187], v[120:123]\n"
    "v_exp_f32_e32 v169, v169\n"
    "v_mfma_f32_16x16x32_bf16 v[120:123], a[184:187], v[180:183], v[120:123]\n"
    "s_waitcnt lgkmcnt(11)\n"
    "ds_write_addtid_b32 v142 offset:512\n"
    "v_cvt_pk_bf16_f32 v159, v162, v163\n"
    "v_mfma_f32_16x16x32_bf16 v[212:215], a[160:163], a[128:131], v[220:223]\n"
    "ds_write_addtid_b32 v143 offset:768\n"
    "v_mfma_f32_16x16x32_bf16 v[212:215], a[164:167], a[132:135], v[212:215]\n"
    "v_exp_f32_e32 v170, v170\n"
    "v_mfma_f32_16x16x32_bf16 v[204:207], a[164:167], a[100:103], v[220:223]\n"
    "ds_write_addtid_b32 v150 offset:1536\n"
    "v_mfma_f32_16x16x32_bf16 v[204:207], a[160:163], a[96:99], v[204:207]\n"
    "ds_read_b128 a[184:187], v242 offset:12288\n"
    "v_mfma_f32_16x16x32_bf16 v[196:199], a[160:163], a[64:67], v[220:223]\n"
    "v_exp_f32_e32 v171, v171\n"
    "v_mfma_f32_16x16x32_bf16 v[196:199], a[164:167], a[68:71], v[196:199]\n"
    "ds_write_addtid_b32 v151 offset:1792\n"
    "v_cvt_pk_bf16_f32 v166, v168, v169\n"
    "v_mfma_f32_16x16x32_bf16 v[188:191], a[164:167], a[36:39], v[220:223]\n"
    "v_exp_f32_e32 v176, v176\n"
    "v_mfma_f32_16x16x32_bf16 v[188:191], a[160:163], a[32:35], v[188:191]\n"
    "v_exp_f32_e32 v177, v177\n"
    "v_mfma_f32_16x16x32_bf16 v[180:183], a[160:163], a[0:3], v[220:223]\n"
    "ds_write_addtid_b32 v158 offset:2560\n"
    "v_cvt_pk_bf16_f32 v167, v170, v171\n"
    "v_mfma_f32_16x16x32_bf16 v[180:183], a[164:167], a[4:7], v[180:183]\n"
    "s_waitcnt vmcnt(20)\n"
    "v_exp_f32_e32 v178, v178\n"
    "v_smfmac_f32_16x16x64_bf16 v[224:227], a[232:235], a[192:199], v238\n"
    "s_waitcnt lgkmcnt(10)\n"
    "ds_read_b128 a[188:191], v243 offset:12288\n"
    "v_mfma_f32_16x16x32_bf16 v[180:183], a[168:171], a[8:11], v[180:183]\n"
    "s_bitcmp1_b32 s86, 10\n"
    "ds_read_b128 a[160:163], v240 offset:10240\n"
    "v_mfma_f32_16x16x32_bf16 v[180:183], a[172:175], a[12:15], v[180:183]\n"
    "s_cselect_b64 exec, -1, 0\n"
    "v_cvt_pk_bf16_f32 v236, v224, v225\n"
    "v_cvt_pk_bf16_f32 v237, v226, v227\n"
    "v_mfma_f32_16x16x32_bf16 v[188:191], a[172:175], a[44:47], v[188:191]\n"
    "s_cselect_b32 s88, 512, 0\n"
    "v_mov_b64_e32 v[224:225], 0\n"
    "v_mov_b64_e32 v[226:227], 0\n"
    "v_mfma_f32_16x16x32_bf16 v[188:191], a[168:171], a[40:43], v[188:191]\n"
    "buffer_store_dwordx2 v[236:237], v252, s[8:11], s87 offen sc1\n"
    "v_mfma_f32_16x16x32_bf16 v[196:199], a[168:171], a[72:75], v[196:199]\n"
    "s_mov_b64 exec, -1\n"
    "ds_read2_b32 v[228:229], v251 offset0:20 offset1:22\n"
    "v_mfma_f32_16x16x32_bf16 v[196:199], a[172:175], a[76:79], v[196:199]\n"
    "s_add_u32 s87, s87, s88\n"
    "ds_read2_b32 v[230:231], v251 offset0:24 offset1:26\n"
    "v_mfma_f32_16x16x32_bf16 v[204:207], a[172:175], a[108:111], v[204:207]\n"
    "ds_read_b32 v232, v251 offset:112\n"
    "v_mfma_f32_16x16x32_bf16 v[204:207], a[168:171], a[104:107], v[204:207]\n"
    "v_exp_f32_e32 v179, v179\n"
    "v_mfma_f32_16x16x32_bf16 v[212:215], a[168:171], a[136:139], v[212:215]\n"
    "ds_write_addtid_b32 v159 offset:2816\n"
    "v_cvt_pk_bf16_f32 v174, v176, v177\n"
    "v_mfma_f32_16x16x32_bf16 v[212:215], a[172:175], a[140:143], v[212:215]\n"
    "s_waitcnt lgkmcnt(13)\n"
    "ds_read_b128 a[164:167], v241 offset:10240\n"
    "v_mfma_f32_16x16x32_bf16 v[212:215], a[176:179], a[144:147], v[212:215]\n"
    "ds_write_addtid_b32 v166 offset:3584\n"
    "v_cvt_pk_bf16_f32 v175, v178, v179\n"
    "v_mfma_f32_16x16x32_bf16 v[212:215], a[180:183], a[148:151], v[212:215]\n"
    "ds_read_b128 a[168:171], v240 offset:14336\n"
    "v_mfma_f32_16x16x32_bf16 v[204:207], a[180:183], a[116:119], v[204:207]\n"
    "s_waitcnt lgkmcnt(6)\n"
    "ds_write_addtid_b32 v167 offset:3840\n"
    "v_mad_u32_u16 v234, v228, s90, v239 op_sel:[0,0,0,0]\n"
    "v_mfma_f32_16x16x32_bf16 v[204:207], a[176:179], a[112:115], v[204:207]\n"
    "buffer_load_dwordx4 a[192:195], v234, s[20:23], 0 offen sc1\n"
    "v_mad_u32_u16 v235, v228, s90, v239 op_sel:[1,0,0,0]\n"
    "v_mfma_f32_16x16x32_bf16 v[196:199], a[176:179], a[80:83], v[196:199]\n"
    "ds_write_addtid_b32 v174 offset:4608\n"
    "v_mfma_f32_16x16x32_bf16 v[196:199], a[180:183], a[84:87], v[196:199]\n"
    "ds_write_addtid_b32 v175 offset:4864\n"
    "v_mfma_f32_16x16x32_bf16 v[188:191], a[180:183], a[52:55], v[188:191]\n"
    "buffer_load_dwordx4 a[196:199], v235, s[20:23], 0 offen sc1\n"
    "v_mfma_f32_16x16x32_bf16 v[188:191], a[176:179], a[48:51], v[188:191]\n"
    "v_mfma_f32_16x16x32_bf16 v[180:183], a[176:179], a[16:19], v[180:183]\n"
    "s_waitcnt lgkmcnt(0)\n"
    "v_mfma_f32_16x16x32_bf16 v[180:183], a[180:183], a[20:23], v[180:183]\n"
    "s_barrier\n"
    "v_mfma_f32_16x16x32_bf16 v[180:183], a[184:187], a[24:27], v[180:183]\n"
    "s_add_u32 m0, s43, 24576\n"
    "ds_read_b128 a[172:175], v241 offset:14336\n"
    "v_mfma_f32_16x16x32_bf16 v[180:183], a[188:191], a[28:31], v[180:183]\n"
    "ds_read2st64_b32 v[144:145], v249 offset0:0 offset1:1\n"
    "v_mfma_f32_16x16x32_bf16 v[188:191], a[188:191], a[60:63], v[188:191]\n"
    "buffer_load_dwordx4 v248, s[8:11], s40 offen offset:0 sc1 lds\n"
    "v_mfma_f32_16x16x32_bf16 v[188:191], a[184:187], a[56:59], v[188:191]\n"
    "ds_read_b128 a[176:179], v242 offset:10240\n"
    "v_mfma_f32_16x16x32_bf16 v[196:199], a[184:187], a[88:91], v[196:199]\n"
    "v_mfma_f32_16x16x32_bf16 v[196:199], a[188:191], a[92:95], v[196:199]\n"
    "v_mfma_f32_16x16x32_bf16 v[204:207], a[188:191], a[124:127], v[204:207]\n"
    "buffer_load_dwordx4 v248, s[8:11], s40 offen offset:1024 sc1 lds\n"
    "v_mfma_f32_16x16x32_bf16 v[204:207], a[184:187], a[120:123], v[204:207]\n"
    "ds_read2st64_b32 v[146:147], v249 offset0:2 offset1:3\n"
    "v_mfma_f32_16x16x32_bf16 v[212:215], a[184:187], a[152:155], v[212:215]\n"
    "v_mfma_f32_16x16x32_bf16 v[212:215], a[188:191], a[156:159], v[212:215]\n"
    "ds_read_b128 a[180:183], v243 offset:10240\n"
    "v_mfma_f32_16x16x32_bf16 v[216:219], a[160:163], a[128:131], v[220:223]\n"
    "buffer_load_dwordx4 v248, s[8:11], s40 offen offset:2048 sc1 lds\n"
    "v_mfma_f32_16x16x32_bf16 v[216:219], a[164:167], a[132:135], v[216:219]\n"
    "ds_read2st64_b32 v[152:153], v249 offset0:4 offset1:5\n"
    "v_mfma_f32_16x16x32_bf16 v[208:211], a[164:167], a[100:103], v[220:223]\n"
    "v_exp_f32_e32 v180, v180\n"
    "v_mfma_f32_16x16x32_bf16 v[208:211], a[160:163], a[96:99], v[208:211]\n"
    "ds_read_b128 a[184:187], v242 offset:14336\n"
    "v_mfma_f32_16x16x32_bf16 v[200:203], a[160:163], a[64:67], v[220:223]\n"
    "buffer_load_dwordx4 v248, s[8:11], s40 offen offset:3072 sc1 lds\n"
    "v_mfma_f32_16x16x32_bf16 v[200:203], a[164:167], a[68:71], v[200:203]\n"
    "s_add_u32 m0, s43, 28672\n"
    "ds_read2st64_b32 v[154:155], v249 offset0:6 offset1:7\n"
    "v_mfma_f32_16x16x32_bf16 v[192:195], a[164:167], a[36:39], v[220:223]\n"
    "s_add_u32 s40, s40, 0x8000\n"
    "ds_read2st64_b32 v[160:161], v249 offset0:8 offset1:9\n"
    "v_mfma_f32_16x16x32_bf16 v[192:195], a[160:163], a[32:35], v[192:195]\n"
    "ds_read2st64_b32 v[162:163], v249 offset0:10 offset1:11\n"
    "v_mfma_f32_16x16x32_bf16 v[184:187], a[160:163], a[0:3], v[220:223]\n"
    "buffer_load_dwordx4 v248, s[8:11], s41 offen offset:0 sc1 lds\n"
    "v_mfma_f32_16x16x32_bf16 v[184:187], a[164:167], a[4:7], v[184:187]\n"
    "s_waitcnt vmcnt(17)\n"
    "ds_read_b128 a[188:191], v243 offset:14336\n"
    "v_smfmac_f32_16x16x64_bf16 v[224:227], a[232:235], a[200:207], v238\n"
    "s_waitcnt lgkmcnt(10)\n"
    "v_mad_u32_u16 v234, v229, s90, v239 op_sel:[0,0,0,0]\n"
    "v_mfma_f32_16x16x32_bf16 v[184:187], a[168:171], a[8:11], v[184:187]\n"
    "s_bitcmp1_b32 s86, 11\n"
    "buffer_load_dwordx4 a[200:203], v234, s[20:23], 0 offen sc1\n"
    "v_mad_u32_u16 v235, v229, s90, v239 op_sel:[1,0,0,0]\n"
    "v_mfma_f32_16x16x32_bf16 v[184:187], a[172:175], a[12:15], v[184:187]\n"
    "s_cselect_b64 exec, -1, 0\n"
    "v_cvt_pk_bf16_f32 v236, v224, v225\n"
    "v_cvt_pk_bf16_f32 v237, v226, v227\n"
    "v_mfma_f32_16x16x32_bf16 v[192:195], a[172:175], a[44:47], v[192:195]\n"
    "s_cselect_b32 s88, 512, 0\n"
    "v_mov_b64_e32 v[224:225], 0\n"
    "v_mov_b64_e32 v[226:227], 0\n"
    "v_mfma_f32_16x16x32_bf16 v[192:195], a[168:171], a[40:43], v[192:195]\n"
    "buffer_store_dwordx2 v[236:237], v252, s[8:11], s87 offen sc1\n"
    "v_mfma_f32_16x16x32_bf16 v[200:203], a[168:171], a[72:75], v[200:203]\n"
    "s_mov_b64 exec, -1\n"
    "ds_read_b64_tr_b16 a[160:161], v244 offset:0\n"
    "v_mfma_f32_16x16x32_bf16 v[200:203], a[172:175], a[76:79], v[200:203]\n"
    "s_add_u32 s87, s87, s88\n"
    "ds_read_b64_tr_b16 a[162:163], v244 offset:2048\n"
    "v_mfma_f32_16x16x32_bf16 v[208:211], a[172:175], a[108:111], v[208:211]\n"
    "buffer_load_dwordx4 v248, s[8:11], s41 offen offset:1024 sc1 lds\n"
    "v_mfma_f32_16x16x32_bf16 v[208:211], a[168:171], a[104:107], v[208:211]\n"
    "ds_read_b64_tr_b16 a[164:165], v244 offset:32768\n"
    "v_mfma_f32_16x16x32_bf16 v[216:219], a[168:171], a[136:139], v[216:219]\n"
    "ds_read2st64_b32 v[168:169], v249 offset0:12 offset1:13\n"
    "v_mfma_f32_16x16x32_bf16 v[216:219], a[172:175], a[140:143], v[216:219]\n"
    "s_waitcnt lgkmcnt(10)\n"
    "ds_read2st64_b32 v[170:171], v249 offset0:14 offset1:15\n"
    "v_mfma_f32_16x16x32_bf16 v[216:219], a[176:179], a[144:147], v[216:219]\n"
    "buffer_load_dwordx4 v248, s[8:11], s41 offen offset:2048 sc1 lds\n"
    "v_mfma_f32_16x16x32_bf16 v[216:219], a[180:183], a[148:151], v[216:219]\n"
    "ds_read_b64_tr_b16 a[166:167], v244 offset:34816\n"
    "v_mfma_f32_16x16x32_bf16 v[208:211], a[180:183], a[116:119], v[208:211]\n"
    "ds_read_b64_tr_b16 a[168:169], v245 offset:0\n"
    "v_mfma_f32_16x16x32_bf16 v[208:211], a[176:179], a[112:115], v[208:211]\n"
    "v_exp_f32_e32 v181, v181\n"
    "v_mfma_f32_16x16x32_bf16 v[200:203], a[176:179], a[80:83], v[200:203]\n"
    "buffer_load_dwordx4 v248, s[8:11], s41 offen offset:3072 sc1 lds\n"
    "v_mfma_f32_16x16x32_bf16 v[200:203], a[180:183], a[84:87], v[200:203]\n"
    "ds_read_b64_tr_b16 a[170:171], v245 offset:2048\n"
    "v_mfma_f32_16x16x32_bf16 v[192:195], a[180:183], a[52:55], v[192:195]\n"
    "s_add_u32 s41, s41, 0x8000\n"
    "ds_read2st64_b32 v[176:177], v249 offset0:16 offset1:17\n"
    "v_mfma_f32_16x16x32_bf16 v[192:195], a[176:179], a[48:51], v[192:195]\n"
    "buffer_load_dwordx4 a[204:207], v235, s[20:23], 0 offen sc1\n"
    "v_mfma_f32_16x16x32_bf16 v[184:187], a[176:179], a[16:19], v[184:187]\n"
    "ds_read_b64_tr_b16 a[172:173], v245 offset:32768\n"
    "v_mfma_f32_16x16x32_bf16 v[184:187], a[180:183], a[20:23], v[184:187]\n"
    "s_waitcnt lgkmcnt(10)\n"
    "ds_read2st64_b32 v[178:179], v249 offset0:18 offset1:19\n"
    "v_mfma_f32_16x16x32_bf16 v[184:187], a[184:187], a[24:27], v[184:187]\n"
    "ds_read_b64_tr_b16 a[176:177], v246 offset:0\n"
    "v_mfma_f32_16x16x32_bf16 v[184:187], a[188:191], a[28:31], v[184:187]\n"
    "ds_read_b64_tr_b16 a[174:175], v245 offset:34816\n"
    "v_mfma_f32_16x16x32_bf16 v[192:195], a[188:191], a[60:63], v[192:195]\n"
    "v_exp_f32_e32 v182, v182\n"
    "v_mfma_f32_16x16x32_bf16 v[192:195], a[184:187], a[56:59], v[192:195]\n"
    "v_exp_f32_e32 v183, v183\n"
    "v_mfma_f32_16x16x32_bf16 v[200:203], a[184:187], a[88:91], v[200:203]\n"
    "ds_read_b64_tr_b16 a[178:179], v246 offset:2048\n"
    "v_cvt_pk_bf16_f32 v180, v180, v181\n"
    "v_mfma_f32_16x16x32_bf16 v[200:203], a[188:191], a[92:95], v[200:203]\n"
    "v_exp_f32_e32 v188, v188\n"
    "v_mfma_f32_16x16x32_bf16 v[208:211], a[188:191], a[124:127], v[208:211]\n"
    ".long 0xbedf00ff\n"
    ".Lat_w7:\n"
    "s_setpc_b64 s[2:3]\n"
    "v_exp_f32_e32 v189, v189\n"
    "v_mfma_f32_16x16x32_bf16 v[208:211], a[184:187], a[120:123], v[208:211]\n"
    "v_cvt_pk_bf16_f32 v181, v182, v183\n"
    "v_mfma_f32_16x16x32_bf16 v[216:219], a[184:187], a[152:155], v[216:219]\n"
    "ds_read_b64_tr_b16 a[180:181], v246 offset:32768\n"
    "v_mfma_f32_16x16x32_bf16 v[216:219], a[188:191], a[156:159], v[216:219]\n"
    "s_waitcnt vmcnt(20)\n"
    "ds_read_b64_tr_b16 a[182:183], v246 offset:34816\n"
    "v_smfmac_f32_16x16x64_bf16 v[224:227], a[232:235], a[208:215], v238\n"
    "s_waitcnt lgkmcnt(5)\n"
    "v_mad_u32_u16 v234, v230, s90, v239 op_sel:[0,0,0,0]\n"
    "v_mfma_f32_16x16x32_bf16 a[236:239], a[160:163], v[140:143], a[236:239]\n"
    "s_bitcmp1_b32 s86, 12\n"
    "buffer_load_dwordx4 a[208:211], v234, s[20:23], 0 offen sc1\n"
    "v_mad_u32_u16 v235, v230, s90, v239 op_sel:[1,0,0,0]\n"
    "v_mfma_f32_16x16x32_bf16 a[236:239], a[164:167], v[144:147], a[236:239]\n"
    "s_cselect_b64 exec, -1, 0\n"
    "v_cvt_pk_bf16_f32 v236, v224, v225\n"
    "v_cvt_pk_bf16_f32 v237, v226, v227\n"
    "v_mfma_f32_16x16x32_bf16 a[240:243], a[164:167], v[152:155], a[240:243]\n"
    "s_cselect_b32 s88, 512, 0\n"
    "v_mov_b64_e32 v[224:225], 0\n"
    "v_mov_b64_e32 v[226:227], 0\n"
    "v_mfma_f32_16x16x32_bf16 a[240:243], a[160:163], v[148:151], a[240:243]\n"
    "buffer_store_dwordx2 v[236:237], v252, s[8:11], s87 offen sc1\n"
    "v_mfma_f32_16x16x32_bf16 a[244:247], a[160:163], v[156:159], a[244:247]\n"
    "s_mov_b64 exec, -1\n"
    "ds_read_b64_tr_b16 a[184:185], v247 offset:0\n"
    "v_mfma_f32_16x16x32_bf16 a[244:247], a[164:167], v[160:163], a[244:247]\n"
    "s_add_u32 s87, s87, s88\n"
    "ds_read_b64_tr_b16 a[186:187], v247 offset:2048\n"
    "v_mfma_f32_16x16x32_bf16 a[248:251], a[164:167], v[168:171], a[248:251]\n"
    "buffer_load_dwordx4 a[212:215], v235, s[20:23], 0 offen sc1\n"
    "v_mfma_f32_16x16x32_bf16 a[248:251], a[160:163], v[164:167], a[248:251]\n"
    "ds_read_b64_tr_b16 a[188:189], v247 offset:32768\n"
    "v_mfma_f32_16x16x32_bf16 a[252:255], a[160:163], v[172:175], a[252:255]\n"
    "v_exp_f32_e32 v190, v190\n"
    "v_mfma_f32_16x16x32_bf16 a[252:255], a[164:167], v[176:179], a[252:255]\n"
    "s_waitcnt lgkmcnt(6)\n"
    "v_exp_f32_e32 v191, v191\n"
    "v_mfma_f32_16x16x32_bf16 v[16:19], a[172:175], v[176:179], v[16:19]\n"
    "ds_read_b64_tr_b16 a[190:191], v247 offset:34816\n"
    "v_cvt_pk_bf16_f32 v188, v188, v189\n"
    "v_mfma_f32_16x16x32_bf16 v[16:19], a[168:171], v[172:175], v[16:19]\n"
    "ds_read_b64_tr_b16 a[160:161], v244 offset:4096\n"
    "v_mfma_f32_16x16x32_bf16 v[12:15], a[168:171], v[164:167], v[12:15]\n"
    "v_exp_f32_e32 v196, v196\n"
    "v_mfma_f32_16x16x32_bf16 v[12:15], a[172:175], v[168:171], v[12:15]\n"
    "v_exp_f32_e32 v197, v197\n"
    "v_mfma_f32_16x16x32_bf16 v[8:11], a[172:175], v[160:163], v[8:11]\n"
    "ds_read_b64_tr_b16 a[162:163], v244 offset:6144\n"
    "v_cvt_pk_bf16_f32 v189, v190, v191\n"
    "v_mfma_f32_16x16x32_bf16 v[8:11], a[168:171], v[156:159], v[8:11]\n"
    "v_exp_f32_e32 v198, v198\n"
    "v_mfma_f32_16x16x32_bf16 v[4:7], a[168:171], v[148:151], v[4:7]\n"
    "v_exp_f32_e32 v199, v199\n"
    "v_mfma_f32_16x16x32_bf16 v[4:7], a[172:175], v[152:155], v[4:7]\n"
    "v_cvt_pk_bf16_f32 v196, v196, v197\n"
    "v_mfma_f32_16x16x32_bf16 v[0:3], a[172:175], v[144:147], v[0:3]\n"
    "ds_read_b64_tr_b16 a[164:165], v244 offset:36864\n"
    "v_mfma_f32_16x16x32_bf16 v[0:3], a[168:171], v[140:143], v[0:3]\n"
    "s_waitcnt lgkmcnt(7)\n"
    "v_exp_f32_e32 v204, v204\n"
    "v_mfma_f32_16x16x32_bf16 v[20:23], a[176:179], v[140:143], v[20:23]\n"
    "ds_read_b64_tr_b16 a[168:169], v245 offset:4096\n"
    "v_mfma_f32_16x16x32_bf16 v[20:23], a[180:183], v[144:147], v[20:23]\n"
    "ds_read_b64_tr_b16 a[166:167], v244 offset:38912\n"
    "v_mfma_f32_16x16x32_bf16 v[24:27], a[180:183], v[152:155], v[24:27]\n"
    "v_exp_f32_e32 v205, v205\n"
    "v_mfma_f32_16x16x32_bf16 v[24:27], a[176:179], v[148:151], v[24:27]\n"
    "v_cvt_pk_bf16_f32 v197, v198, v199\n"
    "v_mfma_f32_16x16x32_bf16 v[28:31], a[176:179], v[156:159], v[28:31]\n"
    "ds_read_b64_tr_b16 a[170:171], v245 offset:6144\n"
    "v_mfma_f32_16x16x32_bf16 v[28:31], a[180:183], v[160:163], v[28:31]\n"
    "v_exp_f32_e32 v206, v206\n"
    "v_mfma_f32_16x16x32_bf16 v[32:35], a[180:183], v[168:171], v[32:35]\n"
    "v_exp_f32_e32 v207, v207\n"
    "v_mfma_f32_16x16x32_bf16 v[32:35], a[176:179], v[164:167], v[32:35]\n"
    "v_cvt_pk_bf16_f32 v204, v204, v205\n"
    "v_mfma_f32_16x16x32_bf16 v[36:39], a[176:179], v[172:175], v[36:39]\n"
    "ds_read_b64_tr_b16 a[172:173], v245 offset:36864\n"
    "v_mfma_f32_16x16x32_bf16 v[36:39], a[180:183], v[176:179], v[36:39]\n"
    "s_waitcnt vmcnt(20)\n"
    "ds_read_b64_tr_b16 a[174:175], v245 offset:38912\n"
    "v_smfmac_f32_16x16x64_bf16 v[224:227], a[232:235], a[216:223], v238\n"
    "s_waitcnt lgkmcnt(8)\n"
    "v_mad_u32_u16 v234, v231, s90, v239 op_sel:[0,0,0,0]\n"
    "v_mfma_f32_16x16x32_bf16 v[56:59], a[188:191], v[176:179], v[56:59]\n"
    "s_bitcmp1_b32 s86, 13\n"
    "buffer_load_dwordx4 a[216:219], v234, s[20:23], 0 offen sc1\n"
    "v_mad_u32_u16 v235, v231, s90, v239 op_sel:[1,0,0,0]\n"
    "v_mfma_f32_16x16x32_bf16 v[56:59], a[184:187], v[172:175], v[56:59]\n"
    "s_cselect_b64 exec, -1, 0\n"
    "v_cvt_pk_bf16_f32 v236, v224, v225\n"
    "v_cvt_pk_bf16_f32 v237, v226, v227\n"
    "v_mfma_f32_16x16x32_bf16 v[52:55], a[184:187], v[164:167], v[52:55]\n"
    "s_cselect_b32 s88, 512, 0\n"
    "v_mov_b64_e32 v[224:225], 0\n"
    "v_mov_b64_e32 v[226:227], 0\n"
    "v_mfma_f32_16x16x32_bf16 v[52:55], a[188:191], v[168:171], v[52:55]\n"
    "buffer_store_dwordx2 v[236:237], v252, s[8:11], s87 offen sc1\n"
    "v_mfma_f32_16x16x32_bf16 v[48:51], a[188:191], v[160:163], v[48:51]\n"
    "s_mov_b64 exec, -1\n"
    "ds_read_b64_tr_b16 a[176:177], v246 offset:4096\n"
    "v_mfma_f32_16x16x32_bf16 v[48:51], a[184:187], v[156:159], v[48:51]\n"
    "s_add_u32 s87, s87, s88\n"
    "ds_read_b64_tr_b16 a[178:179], v246 offset:6144\n"
    "v_mfma_f32_16x16x32_bf16 v[44:47], a[184:187], v[148:151], v[44:47]\n"
    "buffer_load_dwordx4 a[220:223], v235, s[20:23], 0 offen sc1\n"
    "v_mfma_f32_16x16x32_bf16 v[44:47], a[188:191], v[152:155], v[44:47]\n"
    "ds_read_b64_tr_b16 a[180:181], v246 offset:36864\n"
    "v_mfma_f32_16x16x32_bf16 v[40:43], a[188:191], v[144:147], v[40:43]\n"
    "v_exp_f32_e32 v212, v212\n"
    "v_mfma_f32_16x16x32_bf16 v[40:43], a[184:187], v[140:143], v[40:43]\n"
    "s_waitcnt lgkmcnt(6)\n"
    "v_exp_f32_e32 v213, v213\n"
    "v_mfma_f32_16x16x32_bf16 v[60:63], a[160:163], v[140:143], v[60:63]\n"
    "ds_read_b64_tr_b16 a[182:183], v246 offset:38912\n"
    "v_cvt_pk_bf16_f32 v205, v206, v207\n"
    "v_mfma_f32_16x16x32_bf16 v[60:63], a[164:167], v[144:147], v[60:63]\n"
    "ds_read_b64_tr_b16 a[184:185], v247 offset:4096\n"
    "v_mfma_f32_16x16x32_bf16 v[64:67], a[164:167], v[152:155], v[64:67]\n"
    "ds_read_b64_tr_b16 a[186:187], v247 offset:6144\n"
    "v_mfma_f32_16x16x32_bf16 v[64:67], a[160:163], v[148:151], v[64:67]\n"
    "v_exp_f32_e32 v214, v214\n"
    "v_mfma_f32_16x16x32_bf16 v[68:71], a[160:163], v[156:159], v[68:71]\n"
    "v_exp_f32_e32 v215, v215\n"
    "v_mfma_f32_16x16x32_bf16 v[68:71], a[164:167], v[160:163], v[68:71]\n"
    "ds_read_b64_tr_b16 a[188:189], v247 offset:36864\n"
    "v_cvt_pk_bf16_f32 v212, v212, v213\n"
    "v_mfma_f32_16x16x32_bf16 v[72:75], a[164:167], v[168:171], v[72:75]\n"
    "ds_read_b64_tr_b16 a[190:191], v247 offset:38912\n"
    "v_cvt_pk_bf16_f32 v213, v214, v215\n"
    "v_mfma_f32_16x16x32_bf16 v[72:75], a[160:163], v[164:167], v[72:75]\n"
    "s_waitcnt vmcnt(30)\n"
    "v_mfma_f32_16x16x32_bf16 v[76:79], a[160:163], v[172:175], v[76:79]\n"
    "s_barrier\n"
    "v_mfma_f32_16x16x32_bf16 v[76:79], a[164:167], v[176:179], v[76:79]\n"
    "s_waitcnt lgkmcnt(8)\n"
    "s_mov_b32 m0, s42\n"
    "v_mfma_f32_16x16x32_bf16 v[96:99], a[172:175], v[176:179], v[96:99]\n"
    "v_exp_f32_e32 v184, v184\n"
    "v_mfma_f32_16x16x32_bf16 v[96:99], a[168:171], v[172:175], v[96:99]\n"
    "v_exp_f32_e32 v185, v185\n"
    "v_mfma_f32_16x16x32_bf16 v[92:95], a[168:171], v[164:167], v[92:95]\n"
    "v_exp_f32_e32 v186, v186\n"
    "v_mfma_f32_16x16x32_bf16 v[92:95], a[172:175], v[168:171], v[92:95]\n"
    "ds_read_b128 a[160:163], v240 offset:16384\n"
    "ds_write_addtid_b32 v180 offset:0\n"
    "v_mfma_f32_16x16x32_bf16 v[88:91], a[172:175], v[160:163], v[88:91]\n"
    "v_exp_f32_e32 v187, v187\n"
    "v_mfma_f32_16x16x32_bf16 v[88:91], a[168:171], v[156:159], v[88:91]\n"
    "ds_write_addtid_b32 v181 offset:256\n"
    "v_cvt_pk_bf16_f32 v182, v184, v185\n"
    "v_mfma_f32_16x16x32_bf16 v[84:87], a[168:171], v[148:151], v[84:87]\n"
    "v_exp_f32_e32 v192, v192\n"
    "v_mfma_f32_16x16x32_bf16 v[84:87], a[172:175], v[152:155], v[84:87]\n"
    "v_exp_f32_e32 v193, v193\n"
    "v_mfma_f32_16x16x32_bf16 v[80:83], a[172:175], v[144:147], v[80:83]\n"
    "ds_write_addtid_b32 v188 offset:1024\n"
    "v_cvt_pk_bf16_f32 v183, v186, v187\n"
    "v_mfma_f32_16x16x32_bf16 v[80:83], a[168:171], v[140:143], v[80:83]\n"
    "s_waitcnt vmcnt(20)\n"
    "ds_read_b128 a[164:167], v241 offset:16384\n"
    "ds_write_addtid_b32 v189 offset:1280\n"
    "v_smfmac_f32_16x16x64_bf16 v[224:227], a[232:235], a[224:231], v238\n"
    "s_waitcnt lgkmcnt(10)\n"
    "ds_write_addtid_b32 v196 offset:2048\n"
    "v_mad_u32_u16 v234, v232, s90, v239 op_sel:[0,0,0,0]\n"
    "v_mfma_f32_16x16x32_bf16 v[100:103], a[176:179], v[140:143], v[100:103]\n"
    "s_bitcmp1_b32 s86, 14\n"
    "buffer_load_dwordx4 a[224:227], v234, s[20:23], 0 offen sc1\n"
    "v_mad_u32_u16 v235, v232, s90, v239 op_sel:[1,0,0,0]\n"
    "v_mfma_f32_16x16x32_bf16 v[100:103], a[180:183], v[144:147], v[100:103]\n"
    "s_cselect_b64 exec, -1, 0\n"
    "v_cvt_pk_bf16_f32 v236, v224, v225\n"
    "v_cvt_pk_bf16_f32 v237, v226, v227\n"
    "v_mfma_f32_16x16x32_bf16 v[104:107], a[180:183], v[152:155], v[104:107]\n"
    "s_cselect_b32 s88, 512, 0\n"
    "v_mov_b64_e32 v[224:225], 0\n"
    "v_mov_b64_e32 v[226:227], 0\n"
    "v_mfma_f32_16x16x32_bf16 v[104:107], a[176:179], v[148:151], v[104:107]\n"
    "buffer_store_dwordx2 v[236:237], v252, s[8:11], s87 offen sc1\n"
    "v_mfma_f32_16x16x32_bf16 v[108:111], a[176:179], v[156:159], v[108:111]\n"
    "s_mov_b64 exec, -1\n"
    "ds_read_b128 a[168:171], v240 offset:20480\n"
    "ds_write_addtid_b32 v197 offset:2304\n"
    "v_mfma_f32_16x16x32_bf16 v[108:111], a[180:183], v[160:163], v[108:111]\n"
    "s_add_u32 s87, s87, s88\n"
    "v_exp_f32_e32 v194, v194\n"
    "v_mfma_f32_16x16x32_bf16 v[112:115], a[180:183], v[168:171], v[112:115]\n"
    "buffer_load_dwordx4 a[228:231], v235, s[20:23], 0 offen sc1\n"
    "ds_write_addtid_b32 v204 offset:3072\n"
    "v_mfma_f32_16x16x32_bf16 v[112:115], a[176:179], v[164:167], v[112:115]\n"
    "ds_write_addtid_b32 v205 offset:3328\n"
    "v_mfma_f32_16x16x32_bf16 v[116:119], a[176:179], v[172:175], v[116:119]\n"
    "v_exp_f32_e32 v195, v195\n"
    "v_mfma_f32_16x16x32_bf16 v[116:119], a[180:183], v[176:179], v[116:119]\n"
    "s_waitcnt lgkmcnt(11)\n"
    "ds_read_b128 a[172:175], v241 offset:20480\n"
    "v_cvt_pk_bf16_f32 v190, v192, v193\n"
    "v_mfma_f32_16x16x32_bf16 v[136:139], a[188:191], v[176:179], v[136:139]\n"
    "v_exp_f32_e32 v200, v200\n"
    "v_mfma_f32_16x16x32_bf16 v[136:139], a[184:187], v[172:175], v[136:139]\n"
    "v_exp_f32_e32 v201, v201\n"
    "v_mfma_f32_16x16x32_bf16 v[132:135], a[184:187], v[164:167], v[132:135]\n"
    "ds_read_b128 a[176:179], v242 offset:16384\n"
    "v_cvt_pk_bf16_f32 v191, v194, v195\n"
    "v_mfma_f32_16x16x32_bf16 v[132:135], a[188:191], v[168:171], v[132:135]\n"
    "v_exp_f32_e32 v202, v202\n"
    "v_mfma_f32_16x16x32_bf16 v[128:131], a[188:191], v[160:163], v[128:131]\n"
    "v_exp_f32_e32 v203, v203\n"
    "v_mfma_f32_16x16x32_bf16 v[128:131], a[184:187], v[156:159], v[128:131]\n"
    "ds_write_addtid_b32 v212 offset:4096\n"
    "v_cvt_pk_bf16_f32 v198, v200, v201\n"
    "v_mfma_f32_16x16x32_bf16 v[124:127], a[184:187], v[148:151], v[124:127]\n"
    "v_exp_f32_e32 v208, v208\n"
    "v_mfma_f32_16x16x32_bf16 v[124:127], a[188:191], v[152:155], v[124:127]\n"
    "ds_read_b128 a[180:183], v243 offset:16384\n"
    "ds_write_addtid_b32 v213 offset:4352\n"
    "v_mfma_f32_16x16x32_bf16 v[120:123], a[188:191], v[144:147], v[120:123]\n"
    "v_exp_f32_e32 v209, v209\n"
    "v_mfma_f32_16x16x32_bf16 v[120:123], a[184:187], v[140:143], v[120:123]\n"
    "s_waitcnt lgkmcnt(11)\n"
    "ds_write_addtid_b32 v182 offset:512\n"
    "v_cvt_pk_bf16_f32 v199, v202, v203\n"
    "v_mfma_f32_16x16x32_bf16 v[172:175], a[160:163], a[128:131], v[220:223]\n"
    "ds_write_addtid_b32 v183 offset:768\n"
    "v_mfma_f32_16x16x32_bf16 v[172:175], a[164:167], a[132:135], v[172:175]\n"
    "v_exp_f32_e32 v210, v210\n"
    "v_mfma_f32_16x16x32_bf16 v[164:167], a[164:167], a[100:103], v[220:223]\n"
    "ds_write_addtid_b32 v190 offset:1536\n"
    "v_mfma_f32_16x16x32_bf16 v[164:167], a[160:163], a[96:99], v[164:167]\n"
    "ds_read_b128 a[184:187], v242 offset:20480\n"
    "v_mfma_f32_16x16x32_bf16 v[156:159], a[160:163], a[64:67], v[220:223]\n"
    "v_exp_f32_e32 v211, v211\n"
    "v_mfma_f32_16x16x32_bf16 v[156:159], a[164:167], a[68:71], v[156:159]\n"
    "ds_write_addtid_b32 v191 offset:1792\n"
    "v_cvt_pk_bf16_f32 v206, v208, v209\n"
    "v_mfma_f32_16x16x32_bf16 v[148:151], a[164:167], a[36:39], v[220:223]\n"
    "v_exp_f32_e32 v216, v216\n"
    "v_mfma_f32_16x16x32_bf16 v[148:151], a[160:163], a[32:35], v[148:151]\n"
    "v_exp_f32_e32 v217, v217\n"
    "v_mfma_f32_16x16x32_bf16 v[140:143], a[160:163], a[0:3], v[220:223]\n"
    "ds_write_addtid_b32 v198 offset:2560\n"
    "v_cvt_pk_bf16_f32 v207, v210, v211\n"
    "v_mfma_f32_16x16x32_bf16 v[140:143], a[164:167], a[4:7], v[140:143]\n"
    "s_waitcnt vmcnt(20)\n"
    "v_exp_f32_e32 v218, v218\n"
    "v_smfmac_f32_16x16x64_bf16 v[224:227], a[232:235], a[192:199], v238\n"
    "s_waitcnt lgkmcnt(10)\n"
    "ds_read_b128 a[188:191], v243 offset:20480\n"
    "v_mfma_f32_16x16x32_bf16 v[140:143], a[168:171], a[8:11], v[140:143]\n"
    "s_bitcmp1_b32 s86, 15\n"
    "ds_read_b128 a[160:163], v240 offset:18432\n"
    "v_mfma_f32_16x16x32_bf16 v[140:143], a[172:175], a[12:15], v[140:143]\n"
    "s_cselect_b64 exec, -1, 0\n"
    "v_cvt_pk_bf16_f32 v236, v224, v225\n"
    "v_cvt_pk_bf16_f32 v237, v226, v227\n"
    "v_mfma_f32_16x16x32_bf16 v[148:151], a[172:175], a[44:47], v[148:151]\n"
    "s_cselect_b32 s88, 512, 0\n"
    "v_mov_b64_e32 v[224:225], 0\n"
    "v_mov_b64_e32 v[226:227], 0\n"
    "v_mfma_f32_16x16x32_bf16 v[148:151], a[168:171], a[40:43], v[148:151]\n"
    "buffer_store_dwordx2 v[236:237], v252, s[8:11], s87 offen sc1\n"
    "v_mfma_f32_16x16x32_bf16 v[156:159], a[168:171], a[72:75], v[156:159]\n"
    "s_mov_b64 exec, -1\n"
    "ds_read2_b32 v[228:229], v251 offset0:30 offset1:32\n"
    "v_mfma_f32_16x16x32_bf16 v[156:159], a[172:175], a[76:79], v[156:159]\n"
    "s_add_u32 s87, s87, s88\n"
    "ds_read2_b32 v[230:231], v251 offset0:34 offset1:36\n"
    "v_mfma_f32_16x16x32_bf16 v[164:167], a[172:175], a[108:111], v[164:167]\n"
    "ds_read_b32 v232, v251 offset:152\n"
    "v_mfma_f32_16x16x32_bf16 v[164:167], a[168:171], a[104:107], v[164:167]\n"
    "v_exp_f32_e32 v219, v219\n"
    "v_mfma_f32_16x16x32_bf16 v[172:175], a[168:171], a[136:139], v[172:175]\n"
    "ds_write_addtid_b32 v199 offset:2816\n"
    "v_cvt_pk_bf16_f32 v214, v216, v217\n"
    "v_mfma_f32_16x16x32_bf16 v[172:175], a[172:175], a[140:143], v[172:175]\n"
    "s_waitcnt lgkmcnt(13)\n"
    "ds_read_b128 a[164:167], v241 offset:18432\n"
    "v_mfma_f32_16x16x32_bf16 v[172:175], a[176:179], a[144:147], v[172:175]\n"
    "ds_write_addtid_b32 v206 offset:3584\n"
    "v_cvt_pk_bf16_f32 v215, v218, v219\n"
    "v_mfma_f32_16x16x32_bf16 v[172:175], a[180:183], a[148:151], v[172:175]\n"
    "ds_read_b128 a[168:171], v240 offset:22528\n"
    "v_mfma_f32_16x16x32_bf16 v[164:167], a[180:183], a[116:119], v[164:167]\n"
    "s_waitcnt lgkmcnt(6)\n"
    "ds_write_addtid_b32 v207 offset:3840\n"
    "v_mad_u32_u16 v234, v228, s90, v239 op_sel:[0,0,0,0]\n"
    "v_mfma_f32_16x16x32_bf16 v[164:167], a[176:179], a[112:115], v[164:167]\n"
    "buffer_load_dwordx4 a[192:195], v234, s[20:23], 0 offen sc1\n"
    "v_mad_u32_u16 v235, v228, s90, v239 op_sel:[1,0,0,0]\n"
    "v_mfma_f32_16x16x32_bf16 v[156:159], a[176:179], a[80:83], v[156:159]\n"
    "ds_write_addtid_b32 v214 offset:4608\n"
    "v_add_u32_e32 v251, 160, v251\n"
    "v_mfma_f32_16x16x32_bf16 v[156:159], a[180:183], a[84:87], v[156:159]\n"
    "ds_write_addtid_b32 v215 offset:4864\n"
    "v_mfma_f32_16x16x32_bf16 v[148:151], a[180:183], a[52:55], v[148:151]\n"
    "buffer_load_dwordx4 a[196:199], v235, s[20:23], 0 offen sc1\n"
    "v_mfma_f32_16x16x32_bf16 v[148:151], a[176:179], a[48:51], v[148:151]\n"
    "v_mfma_f32_16x16x32_bf16 v[140:143], a[176:179], a[16:19], v[140:143]\n"
    "s_waitcnt lgkmcnt(0)\n"
    "v_mfma_f32_16x16x32_bf16 v[140:143], a[180:183], a[20:23], v[140:143]\n"
    "s_barrier\n"
    "v_mfma_f32_16x16x32_bf16 v[140:143], a[184:187], a[24:27], v[140:143]\n"
    "s_add_u32 m0, s43, 0\n"
    "ds_read_b128 a[172:175], v241 offset:22528\n"
    "v_mfma_f32_16x16x32_bf16 v[140:143], a[188:191], a[28:31], v[140:143]\n"
    "ds_read2st64_b32 v[184:185], v249 offset0:0 offset1:1\n"
    "v_mfma_f32_16x16x32_bf16 v[148:151], a[188:191], a[60:63], v[148:151]\n"
    "buffer_load_dwordx4 v248, s[8:11], s40 offen offset:0 sc1 lds\n"
    "v_mfma_f32_16x16x32_bf16 v[148:151], a[184:187], a[56:59], v[148:151]\n"
    "ds_read_b128 a[176:179], v242 offset:18432\n"
    "v_mfma_f32_16x16x32_bf16 v[156:159], a[184:187], a[88:91], v[156:159]\n"
    "v_mfma_f32_16x16x32_bf16 v[156:159], a[188:191], a[92:95], v[156:159]\n"
    "v_mfma_f32_16x16x32_bf16 v[164:167], a[188:191], a[124:127], v[164:167]\n"
    "buffer_load_dwordx4 v248, s[8:11], s40 offen offset:1024 sc1 lds\n"
    "v_mfma_f32_16x16x32_bf16 v[164:167], a[184:187], a[120:123], v[164:167]\n"
    "ds_read2st64_b32 v[186:187], v249 offset0:2 offset1:3\n"
    "v_mfma_f32_16x16x32_bf16 v[172:175], a[184:187], a[152:155], v[172:175]\n"
    "v_mfma_f32_16x16x32_bf16 v[172:175], a[188:191], a[156:159], v[172:175]\n"
    "ds_read_b128 a[180:183], v243 offset:18432\n"
    "v_mfma_f32_16x16x32_bf16 v[176:179], a[160:163], a[128:131], v[220:223]\n"
    "buffer_load_dwordx4 v248, s[8:11], s40 offen offset:2048 sc1 lds\n"
    "v_mfma_f32_16x16x32_bf16 v[176:179], a[164:167], a[132:135], v[176:179]\n"
    "ds_read2st64_b32 v[192:193], v249 offset0:4 offset1:5\n"
    "v_mfma_f32_16x16x32_bf16 v[168:171], a[164:167], a[100:103], v[220:223]\n"
    "v_exp_f32_e32 v140, v140\n"
    "v_mfma_f32_16x16x32_bf16 v[168:171], a[160:163], a[96:99], v[168:171]\n"
    "ds_read_b128 a[184:187], v242 offset:22528\n"
    "v_mfma_f32_16x16x32_bf16 v[160:163], a[160:163], a[64:67], v[220:223]\n"
    "buffer_load_dwordx4 v248, s[8:11], s40 offen offset:3072 sc1 lds\n"
    "v_mfma_f32_16x16x32_bf16 v[160:163], a[164:167], a[68:71], v[160:163]\n"
    "s_add_u32 m0, s43, 4096\n"
    "ds_read2st64_b32 v[194:195], v249 offset0:6 offset1:7\n"
    "v_mfma_f32_16x16x32_bf16 v[152:155], a[164:167], a[36:39], v[220:223]\n"
    "s_add_u32 s40, s40, 0x8000\n"
    "ds_read2st64_b32 v[200:201], v249 offset0:8 offset1:9\n"
    "v_mfma_f32_16x16x32_bf16 v[152:155], a[160:163], a[32:35], v[152:155]\n"
    "ds_read2st64_b32 v[202:203], v249 offset0:10 offset1:11\n"
    "v_mfma_f32_16x16x32_bf16 v[144:147], a[160:163], a[0:3], v[220:223]\n"
    "buffer_load_dwordx4 v248, s[8:11], s41 offen offset:0 sc1 lds\n"
    "v_mfma_f32_16x16x32_bf16 v[144:147], a[164:167], a[4:7], v[144:147]\n"
    "s_waitcnt vmcnt(17)\n"
    "ds_read_b128 a[188:191], v243 offset:22528\n"
    "v_smfmac_f32_16x16x64_bf16 v[224:227], a[232:235], a[200:207], v238\n"
    "s_waitcnt lgkmcnt(10)\n"
    "v_mad_u32_u16 v234, v229, s90, v239 op_sel:[0,0,0,0]\n"
    "v_mfma_f32_16x16x32_bf16 v[144:147], a[168:171], a[8:11], v[144:147]\n"
    "s_bitcmp1_b32 s86, 16\n"
    "buffer_load_dwordx4 a[200:203], v234, s[20:23], 0 offen sc1\n"
    "v_mad_u32_u16 v235, v229, s90, v239 op_sel:[1,0,0,0]\n"
    "v_mfma_f32_16x16x32_bf16 v[144:147], a[172:175], a[12:15], v[144:147]\n"
    "s_cselect_b64 exec, -1, 0\n"
    "v_cvt_pk_bf16_f32 v236, v224, v225\n"
    "v_cvt_pk_bf16_f32 v237, v226, v227\n"
    "v_mfma_f32_16x16x32_bf16 v[152:155], a[172:175], a[44:47], v[152:155]\n"
    "s_cselect_b32 s88, 512, 0\n"
    "v_mov_b64_e32 v[224:225], 0\n"
    "v_mov_b64_e32 v[226:227], 0\n"
    "v_mfma_f32_16x16x32_bf16 v[152:155], a[168:171], a[40:43], v[152:155]\n"
    "buffer_store_dwordx2 v[236:237], v252, s[8:11], s87 offen sc1\n"
    "v_mfma_f32_16x16x32_bf16 v[160:163], a[168:171], a[72:75], v[160:163]\n"
    "s_mov_b64 exec, -1\n"
    "ds_read_b64_tr_b16 a[160:161], v244 offset:8192\n"
    "v_mfma_f32_16x16x32_bf16 v[160:163], a[172:175], a[76:79], v[160:163]\n"
    "s_add_u32 s87, s87, s88\n"
    "ds_read_b64_tr_b16 a[162:163], v244 offset:10240\n"
    "v_mfma_f32_16x16x32_bf16 v[168:171], a[172:175], a[108:111], v[168:171]\n"
    "buffer_load_dwordx4 v248, s[8:11], s41 offen offset:1024 sc1 lds\n"
    "v_mfma_f32_16x16x32_bf16 v[168:171], a[168:171], a[104:107], v[168:171]\n"
    "ds_read_b64_tr_b16 a[164:165], v244 offset:40960\n"
    "v_mfma_f32_16x16x32_bf16 v[176:179], a[168:171], a[136:139], v[176:179]\n"
    "ds_read2st64_b32 v[208:209], v249 offset0:12 offset1:13\n"
    "v_mfma_f32_16x16x32_bf16 v[176:179], a[172:175], a[140:143], v[176:179]\n"
    "s_waitcnt lgkmcnt(10)\n"
    "ds_read2st64_b32 v[210:211], v249 offset0:14 offset1:15\n"
    "v_mfma_f32_16x16x32_bf16 v[176:179], a[176:179], a[144:147], v[176:179]\n"
    "buffer_load_dwordx4 v248, s[8:11], s41 offen offset:2048 sc1 lds\n"
    "v_mfma_f32_16x16x32_bf16 v[176:179], a[180:183], a[148:151], v[176:179]\n"
    "ds_read_b64_tr_b16 a[166:167], v244 offset:43008\n"
    "v_mfma_f32_16x16x32_bf16 v[168:171], a[180:183], a[116:119], v[168:171]\n"
    "ds_read_b64_tr_b16 a[168:169], v245 offset:8192\n"
    "v_mfma_f32_16x16x32_bf16 v[168:171], a[176:179], a[112:115], v[168:171]\n"
    "v_exp_f32_e32 v141, v141\n"
    "v_mfma_f32_16x16x32_bf16 v[160:163], a[176:179], a[80:83], v[160:163]\n"
    "buffer_load_dwordx4 v248, s[8:11], s41 offen offset:3072 sc1 lds\n"
    "v_mfma_f32_16x16x32_bf16 v[160:163], a[180:183], a[84:87], v[160:163]\n"
    "ds_read_b64_tr_b16 a[170:171], v245 offset:10240\n"
    "v_mfma_f32_16x16x32_bf16 v[152:155], a[180:183], a[52:55], v[152:155]\n"
    "s_add_u32 s41, s41, 0x8000\n"
    "ds_read2st64_b32 v[216:217], v249 offset0:16 offset1:17\n"
    "v_mfma_f32_16x16x32_bf16 v[152:155], a[176:179], a[48:51], v[152:155]\n"
    "buffer_load_dwordx4 a[204:207], v235, s[20:23], 0 offen sc1\n"
    "v_mfma_f32_16x16x32_bf16 v[144:147], a[176:179], a[16:19], v[144:147]\n"
    "ds_read_b64_tr_b16 a[172:173], v245 offset:40960\n"
    "v_mfma_f32_16x16x32_bf16 v[144:147], a[180:183], a[20:23], v[144:147]\n"
    "s_waitcnt lgkmcnt(10)\n"
    "ds_read2st64_b32 v[218:219], v249 offset0:18 offset1:19\n"
    "v_mfma_f32_16x16x32_bf16 v[144:147], a[184:187], a[24:27], v[144:147]\n"
    "ds_read_b64_tr_b16 a[176:177], v246 offset:8192\n"
    "v_mfma_f32_16x16x32_bf16 v[144:147], a[188:191], a[28:31], v[144:147]\n"
    "ds_read_b64_tr_b16 a[174:175], v245 offset:43008\n"
    "v_mfma_f32_16x16x32_bf16 v[152:155], a[188:191], a[60:63], v[152:155]\n"
    "v_exp_f32_e32 v142, v142\n"
    "v_mfma_f32_16x16x32_bf16 v[152:155], a[184:187], a[56:59], v[152:155]\n"
    "v_exp_f32_e32 v143, v143\n"
    "v_mfma_f32_16x16x32_bf16 v[160:163], a[184:187], a[88:91], v[160:163]\n"
    "ds_read_b64_tr_b16 a[178:179], v246 offset:10240\n"
    "v_cvt_pk_bf16_f32 v140, v140, v141\n"
    "v_mfma_f32_16x16x32_bf16 v[160:163], a[188:191], a[92:95], v[160:163]\n"
    "v_exp_f32_e32 v148, v148\n"
    "v_mfma_f32_16x16x32_bf16 v[168:171], a[188:191], a[124:127], v[168:171]\n"
    "v_exp_f32_e32 v149, v149\n"
    "v_mfma_f32_16x16x32_bf16 v[168:171], a[184:187], a[120:123], v[168:171]\n"
    "v_cvt_pk_bf16_f32 v141, v142, v143\n"
    "v_mfma_f32_16x16x32_bf16 v[176:179], a[184:187], a[152:155], v[176:179]\n"
    "ds_read_b64_tr_b16 a[180:181], v246 offset:40960\n"
    "v_mfma_f32_16x16x32_bf16 v[176:179], a[188:191], a[156:159], v[176:179]\n"
    "s_waitcnt vmcnt(20)\n"
    "ds_read_b64_tr_b16 a[182:183], v246 offset:43008\n"
    "v_smfmac_f32_16x16x64_bf16 v[224:227], a[232:235], a[208:215], v238\n"
    "s_waitcnt lgkmcnt(5)\n"
    "v_mad_u32_u16 v234, v230, s90, v239 op_sel:[0,0,0,0]\n"
    "v_mfma_f32_16x16x32_bf16 a[236:239], a[160:163], v[180:183], a[236:239]\n"
    "s_bitcmp1_b32 s86, 17\n"
    "buffer_load_dwordx4 a[208:211], v234, s[20:23], 0 offen sc1\n"
    "v_mad_u32_u16 v235, v230, s90, v239 op_sel:[1,0,0,0]\n"
    "v_mfma_f32_16x16x32_bf16 a[236:239], a[164:167], v[184:187], a[236:239]\n"
    "s_cselect_b64 exec, -1, 0\n"
    "v_cvt_pk_bf16_f32 v236, v224, v225\n"
    "v_cvt_pk_bf16_f32 v237, v226, v227\n"
    "v_mfma_f32_16x16x32_bf16 a[240:243], a[164:167], v[192:195], a[240:243]\n"
    "s_cselect_b32 s88, 512, 0\n"
    "v_mov_b64_e32 v[224:225], 0\n"
    "v_mov_b64_e32 v[226:227], 0\n"
    "v_mfma_f32_16x16x32_bf16 a[240:243], a[160:163], v[188:191], a[240:243]\n"
    "buffer_store_dwordx2 v[236:237], v252, s[8:11], s87 offen sc1\n"
    "v_mfma_f32_16x16x32_bf16 a[244:247], a[160:163], v[196:199], a[244:247]\n"
    "s_mov_b64 exec, -1\n"
    "ds_read_b64_tr_b16 a[184:185], v247 offset:8192\n"
    "v_mfma_f32_16x16x32_bf16 a[244:247], a[164:167], v[200:203], a[244:247]\n"
    "s_add_u32 s87, s87, s88\n"
    "ds_read_b64_tr_b16 a[186:187], v247 offset:10240\n"
    "v_mfma_f32_16x16x32_bf16 a[248:251], a[164:167], v[208:211], a[248:251]\n"
    "buffer_load_dwordx4 a[212:215], v235, s[20:23], 0 offen sc1\n"
    "v_mfma_f32_16x16x32_bf16 a[248:251], a[160:163], v[204:207], a[248:251]\n"
    "ds_read_b64_tr_b16 a[188:189], v247 offset:40960\n"
    "v_mfma_f32_16x16x32_bf16 a[252:255], a[160:163], v[212:215], a[252:255]\n"
    "v_exp_f32_e32 v150, v150\n"
    "v_mfma_f32_16x16x32_bf16 a[252:255], a[164:167], v[216:219], a[252:255]\n"
    "s_waitcnt lgkmcnt(6)\n"
    "v_exp_f32_e32 v151, v151\n"
    "v_mfma_f32_16x16x32_bf16 v[16:19], a[172:175], v[216:219], v[16:19]\n"
    "ds_read_b64_tr_b16 a[190:191], v247 offset:43008\n"
    "v_cvt_pk_bf16_f32 v148, v148, v149\n"
    "v_mfma_f32_16x16x32_bf16 v[16:19], a[168:171], v[212:215], v[16:19]\n"
    "ds_read_b64_tr_b16 a[160:161], v244 offset:12288\n"
    "v_mfma_f32_16x16x32_bf16 v[12:15], a[168:171], v[204:207], v[12:15]\n"
    "v_exp_f32_e32 v156, v156\n"
    "v_mfma_f32_16x16x32_bf16 v[12:15], a[172:175], v[208:211], v[12:15]\n"
    "v_exp_f32_e32 v157, v157\n"
    "v_mfma_f32_16x16x32_bf16 v[8:11], a[172:175], v[200:203], v[8:11]\n"
    "ds_read_b64_tr_b16 a[162:163], v244 offset:14336\n"
    "v_cvt_pk_bf16_f32 v149, v150, v151\n"
    "v_mfma_f32_16x16x32_bf16 v[8:11], a[168:171], v[196:199], v[8:11]\n"
    "v_exp_f32_e32 v158, v158\n"
    "v_mfma_f32_16x16x32_bf16 v[4:7], a[168:171], v[188:191], v[4:7]\n"
    "v_exp_f32_e32 v159, v159\n"
    "v_mfma_f32_16x16x32_bf16 v[4:7], a[172:175], v[192:195], v[4:7]\n"
    "v_cvt_pk_bf16_f32 v156, v156, v157\n"
    "v_mfma_f32_16x16x32_bf16 v[0:3], a[172:175], v[184:187], v[0:3]\n"
    "ds_read_b64_tr_b16 a[164:165], v244 offset:45056\n"
    "v_mfma_f32_16x16x32_bf16 v[0:3], a[168:171], v[180:183], v[0:3]\n"
    "s_waitcnt lgkmcnt(7)\n"
    "v_exp_f32_e32 v164, v164\n"
    "v_mfma_f32_16x16x32_bf16 v[20:23], a[176:179], v[180:183], v[20:23]\n"
    "ds_read_b64_tr_b16 a[168:169], v245 offset:12288\n"
    "v_mfma_f32_16x16x32_bf16 v[20:23], a[180:183], v[184:187], v[20:23]\n"
    "ds_read_b64_tr_b16 a[166:167], v244 offset:47104\n"
    "v_mfma_f32_16x16x32_bf16 v[24:27], a[180:183], v[192:195], v[24:27]\n"
    "v_exp_f32_e32 v165, v165\n"
    "v_mfma_f32_16x16x32_bf16 v[24:27], a[176:179], v[188:191], v[24:27]\n"
    "v_cvt_pk_bf16_f32 v157, v158, v159\n"
    "v_mfma_f32_16x16x32_bf16 v[28:31], a[176:179], v[196:199], v[28:31]\n"
    "ds_read_b64_tr_b16 a[170:171], v245 offset:14336\n"
    "v_mfma_f32_16x16x32_bf16 v[28:31], a[180:183], v[200:203], v[28:31]\n"
    "v_exp_f32_e32 v166, v166\n"
    "v_mfma_f32_16x16x32_bf16 v[32:35], a[180:183], v[208:211], v[32:35]\n"
    "v_exp_f32_e32 v167, v167\n"
    "v_mfma_f32_16x16x32_bf16 v[32:35], a[176:179], v[204:207], v[32:35]\n"
    "v_cvt_pk_bf16_f32 v164, v164, v165\n"
    "v_mfma_f32_16x16x32_bf16 v[36:39], a[176:179], v[212:215], v[36:39]\n"
    "ds_read_b64_tr_b16 a[172:173], v245 offset:45056\n"
    "v_mfma_f32_16x16x32_bf16 v[36:39], a[180:183], v[216:219], v[36:39]\n"
    "s_waitcnt vmcnt(20)\n"
    "ds_read_b64_tr_b16 a[174:175], v245 offset:47104\n"
    "v_smfmac_f32_16x16x64_bf16 v[224:227], a[232:235], a[216:223], v238\n"
    "s_waitcnt lgkmcnt(8)\n"
    "v_mad_u32_u16 v234, v231, s90, v239 op_sel:[0,0,0,0]\n"
    "v_mfma_f32_16x16x32_bf16 v[56:59], a[188:191], v[216:219], v[56:59]\n"
    "s_bitcmp1_b32 s86, 18\n"
    "buffer_load_dwordx4 a[216:219], v234, s[20:23], 0 offen sc1\n"
    "v_mad_u32_u16 v235, v231, s90, v239 op_sel:[1,0,0,0]\n"
    "v_mfma_f32_16x16x32_bf16 v[56:59], a[184:187], v[212:215], v[56:59]\n"
    "s_cselect_b64 exec, -1, 0\n"
    "v_cvt_pk_bf16_f32 v236, v224, v225\n"
    "v_cvt_pk_bf16_f32 v237, v226, v227\n"
    "v_mfma_f32_16x16x32_bf16 v[52:55], a[184:187], v[204:207], v[52:55]\n"
    "s_cselect_b32 s88, 512, 0\n"
    "v_mov_b64_e32 v[224:225], 0\n"
    "v_mov_b64_e32 v[226:227], 0\n"
    "v_mfma_f32_16x16x32_bf16 v[52:55], a[188:191], v[208:211], v[52:55]\n"
    "buffer_store_dwordx2 v[236:237], v252, s[8:11], s87 offen sc1\n"
    "v_mfma_f32_16x16x32_bf16 v[48:51], a[188:191], v[200:203], v[48:51]\n"
    "s_mov_b64 exec, -1\n"
    "ds_read_b64_tr_b16 a[176:177], v246 offset:12288\n"
    "v_mfma_f32_16x16x32_bf16 v[48:51], a[184:187], v[196:199], v[48:51]\n"
    "s_add_u32 s87, s87, s88\n"
    "ds_read_b64_tr_b16 a[178:179], v246 offset:14336\n"
    "v_mfma_f32_16x16x32_bf16 v[44:47], a[184:187], v[188:191], v[44:47]\n"
    "buffer_load_dwordx4 a[220:223], v235, s[20:23], 0 offen sc1\n"
    "v_mfma_f32_16x16x32_bf16 v[44:47], a[188:191], v[192:195], v[44:47]\n"
    "ds_read_b64_tr_b16 a[180:181], v246 offset:45056\n"
    "v_mfma_f32_16x16x32_bf16 v[40:43], a[188:191], v[184:187], v[40:43]\n"
    "v_exp_f32_e32 v172, v172\n"
    "v_mfma_f32_16x16x32_bf16 v[40:43], a[184:187], v[180:183], v[40:43]\n"
    "s_waitcnt lgkmcnt(6)\n"
    "v_exp_f32_e32 v173, v173\n"
    "v_mfma_f32_16x16x32_bf16 v[60:63], a[160:163], v[180:183], v[60:63]\n"
    "ds_read_b64_tr_b16 a[182:183], v246 offset:47104\n"
    "v_cvt_pk_bf16_f32 v165, v166, v167\n"
    "v_mfma_f32_16x16x32_bf16 v[60:63], a[164:167], v[184:187], v[60:63]\n"
    "ds_read_b64_tr_b16 a[184:185], v247 offset:12288\n"
    "v_mfma_f32_16x16x32_bf16 v[64:67], a[164:167], v[192:195], v[64:67]\n"
    "ds_read_b64_tr_b16 a[186:187], v247 offset:14336\n"
    "v_mfma_f32_16x16x32_bf16 v[64:67], a[160:163], v[188:191], v[64:67]\n"
    "v_exp_f32_e32 v174, v174\n"
    "v_mfma_f32_16x16x32_bf16 v[68:71], a[160:163], v[196:199], v[68:71]\n"
    "v_exp_f32_e32 v175, v175\n"
    "v_mfma_f32_16x16x32_bf16 v[68:71], a[164:167], v[200:203], v[68:71]\n"
    "ds_read_b64_tr_b16 a[188:189], v247 offset:45056\n"
    "v_cvt_pk_bf16_f32 v172, v172, v173\n"
    "v_mfma_f32_16x16x32_bf16 v[72:75], a[164:167], v[208:211], v[72:75]\n"
    "ds_read_b64_tr_b16 a[190:191], v247 offset:47104\n"
    "v_cvt_pk_bf16_f32 v173, v174, v175\n"
    "v_mfma_f32_16x16x32_bf16 v[72:75], a[160:163], v[204:207], v[72:75]\n"
    "s_waitcnt vmcnt(30)\n"
    "v_mfma_f32_16x16x32_bf16 v[76:79], a[160:163], v[212:215], v[76:79]\n"
    "s_barrier\n"
    "v_mfma_f32_16x16x32_bf16 v[76:79], a[164:167], v[216:219], v[76:79]\n"
    "s_waitcnt lgkmcnt(8)\n"
    "s_mov_b32 m0, s42\n"
    "v_mfma_f32_16x16x32_bf16 v[96:99], a[172:175], v[216:219], v[96:99]\n"
    "v_exp_f32_e32 v144, v144\n"
    "v_mfma_f32_16x16x32_bf16 v[96:99], a[168:171], v[212:215], v[96:99]\n"
    "v_exp_f32_e32 v145, v145\n"
    "v_mfma_f32_16x16x32_bf16 v[92:95], a[168:171], v[204:207], v[92:95]\n"
    "v_exp_f32_e32 v146, v146\n"
    "v_mfma_f32_16x16x32_bf16 v[92:95], a[172:175], v[208:211], v[92:95]\n"
    "ds_read_b128 a[160:163], v240 offset:24576\n"
    "ds_write_addtid_b32 v140 offset:0\n"
    "v_mfma_f32_16x16x32_bf16 v[88:91], a[172:175], v[200:203], v[88:91]\n"
    "v_exp_f32_e32 v147, v147\n"
    "v_mfma_f32_16x16x32_bf16 v[88:91], a[168:171], v[196:199], v[88:91]\n"
    "ds_write_addtid_b32 v141 offset:256\n"
    "v_cvt_pk_bf16_f32 v142, v144, v145\n"
    "v_mfma_f32_16x16x32_bf16 v[84:87], a[168:171], v[188:191], v[84:87]\n"
    "v_exp_f32_e32 v152, v152\n"
    "v_mfma_f32_16x16x32_bf16 v[84:87], a[172:175], v[192:195], v[84:87]\n"
    "v_exp_f32_e32 v153, v153\n"
    "v_mfma_f32_16x16x32_bf16 v[80:83], a[172:175], v[184:187], v[80:83]\n"
    "ds_write_addtid_b32 v148 offset:1024\n"
    "v_cvt_pk_bf16_f32 v143, v146, v147\n"
    "v_mfma_f32_16x16x32_bf16 v[80:83], a[168:171], v[180:183], v[80:83]\n"
    "s_waitcnt vmcnt(20)\n"
    "ds_read_b128 a[164:167], v241 offset:24576\n"
    "ds_write_addtid_b32 v149 offset:1280\n"
    "v_smfmac_f32_16x16x64_bf16 v[224:227], a[232:235], a[224:231], v238\n"
    "s_waitcnt lgkmcnt(10)\n"
    "ds_write_addtid_b32 v156 offset:2048\n"
    "v_mad_u32_u16 v234, v232, s90, v239 op_sel:[0,0,0,0]\n"
    "v_mfma_f32_16x16x32_bf16 v[100:103], a[176:179], v[180:183], v[100:103]\n"
    "s_bitcmp1_b32 s86, 19\n"
    "buffer_load_dwordx4 a[224:227], v234, s[20:23], 0 offen sc1\n"
    "v_mad_u32_u16 v235, v232, s90, v239 op_sel:[1,0,0,0]\n"
    ".long 0xbedf00ff\n"
    ".Lat_w8:\n"
    "s_setpc_b64 s[2:3]\n"
    "v_mfma_f32_16x16x32_bf16 v[100:103], a[180:183], v[184:187], v[100:103]\n"
    "s_cselect_b64 exec, -1, 0\n"
    "v_cvt_pk_bf16_f32 v236, v224, v225\n"
    "v_cvt_pk_bf16_f32 v237, v226, v227\n"
    "v_mfma_f32_16x16x32_bf16 v[104:107], a[180:183], v[192:195], v[104:107]\n"
    "s_cselect_b32 s88, 512, 0\n"
    "v_mov_b64_e32 v[224:225], 0\n"
    "v_mov_b64_e32 v[226:227], 0\n"
    "v_mfma_f32_16x16x32_bf16 v[104:107], a[176:179], v[188:191], v[104:107]\n"
    "buffer_store_dwordx2 v[236:237], v252, s[8:11], s87 offen sc1\n"
    "v_mfma_f32_16x16x32_bf16 v[108:111], a[176:179], v[196:199], v[108:111]\n"
    "s_mov_b64 exec, -1\n"
    "ds_read_b128 a[168:171], v240 offset:28672\n"
    "ds_write_addtid_b32 v157 offset:2304\n"
    "v_mfma_f32_16x16x32_bf16 v[108:111], a[180:183], v[200:203], v[108:111]\n"
    "s_add_u32 s87, s87, s88\n"
    "v_exp_f32_e32 v154, v154\n"
    "v_mfma_f32_16x16x32_bf16 v[112:115], a[180:183], v[208:211], v[112:115]\n"
    "s_add_u32 s89, s89, 1\n"
    "buffer_load_dwordx4 a[228:231], v235, s[20:23], 0 offen sc1\n"
    "ds_write_addtid_b32 v164 offset:3072\n"
    "v_mfma_f32_16x16x32_bf16 v[112:115], a[176:179], v[204:207], v[112:115]\n"
    "ds_write_addtid_b32 v165 offset:3328\n"
    "v_readlane_b32 s86, v254, s89\n"
    "v_mfma_f32_16x16x32_bf16 v[116:119], a[176:179], v[212:215], v[116:119]\n"
    "v_exp_f32_e32 v155, v155\n"
    "v_mfma_f32_16x16x32_bf16 v[116:119], a[180:183], v[216:219], v[116:119]\n"
    "s_waitcnt lgkmcnt(11)\n"
    "ds_read_b128 a[172:175], v241 offset:28672\n"
    "v_cvt_pk_bf16_f32 v150, v152, v153\n"
    "v_mfma_f32_16x16x32_bf16 v[136:139], a[188:191], v[216:219], v[136:139]\n"
    "v_exp_f32_e32 v160, v160\n"
    "v_mfma_f32_16x16x32_bf16 v[136:139], a[184:187], v[212:215], v[136:139]\n"
    "v_exp_f32_e32 v161, v161\n"
    "v_mfma_f32_16x16x32_bf16 v[132:135], a[184:187], v[204:207], v[132:135]\n"
    "ds_read_b128 a[176:179], v242 offset:24576\n"
    "v_cvt_pk_bf16_f32 v151, v154, v155\n"
    "v_mfma_f32_16x16x32_bf16 v[132:135], a[188:191], v[208:211], v[132:135]\n"
    "v_exp_f32_e32 v162, v162\n"
    "v_mfma_f32_16x16x32_bf16 v[128:131], a[188:191], v[200:203], v[128:131]\n"
    "v_exp_f32_e32 v163, v163\n"
    "v_mfma_f32_16x16x32_bf16 v[128:131], a[184:187], v[196:199], v[128:131]\n"
    "ds_write_addtid_b32 v172 offset:4096\n"
    "v_cvt_pk_bf16_f32 v158, v160, v161\n"
    "v_mfma_f32_16x16x32_bf16 v[124:127], a[184:187], v[188:191], v[124:127]\n"
    "v_exp_f32_e32 v168, v168\n"
    "v_mfma_f32_16x16x32_bf16 v[124:127], a[188:191], v[192:195], v[124:127]\n"
    "s_sub_u32 s44, s44, 1\n"
    "ds_read_b128 a[180:183], v243 offset:24576\n"
    "ds_write_addtid_b32 v173 offset:4352\n"
    "v_mfma_f32_16x16x32_bf16 v[120:123], a[188:191], v[184:187], v[120:123]\n"
    "s_cmp_lg_u32 s44, s80\n"
    "v_exp_f32_e32 v169, v169\n"
    "v_mfma_f32_16x16x32_bf16 v[120:123], a[184:187], v[180:183], v[120:123]\n"
    "s_waitcnt lgkmcnt(11)\n"
    "ds_write_addtid_b32 v142 offset:512\n"
    "v_cvt_pk_bf16_f32 v159, v162, v163\n"
    "s_cbranch_scc1 .Lat_gloop\n"
    ".Lat_g2:\n"
    "v_mov_b64_e32 v[224:225], v[220:221]\n"
    "v_mov_b64_e32 v[226:227], v[220:221]\n"
    "v_mov_b64_e32 v[228:229], v[220:221]\n"
    "v_mov_b64_e32 v[230:231], v[220:221]\n"
    "v_mov_b64_e32 v[232:233], v[220:221]\n"
    "v_mov_b64_e32 v[234:235], v[220:221]\n"
    "v_mov_b64_e32 v[236:237], v[220:221]\n"
    "v_mov_b64_e32 v[238:239], v[220:221]\n"
    "s_mov_b32 s80, 0\n"
    "s_waitcnt lgkmcnt(0)\n"
    "s_cmp_lg_u32 s44, 0\n"
    "s_cbranch_scc1 .Lat_loop\n"
    ".Lat_tail:\n"
    "v_mfma_f32_16x16x32_bf16 v[212:215], a[160:163], a[128:131], v[236:239]\n"
    ".long 0xbedf00ff\n"
    ".Lat_w9:\n"
    "s_setpc_b64 s[2:3]\n"
    "ds_read_b128 a[184:187], v242 offset:28672\n"
    "v_and_b32_e32 v189, 0x1f0, v250\n"
    "v_lshlrev_b32_e32 v190, 3, v250\n"
    "v_mfma_f32_16x16x32_bf16 v[212:215], a[164:167], a[132:135], v[212:215]\n"
    "ds_write_addtid_b32 v143 offset:768\n"
    "v_lshrrev_b32_e32 v253, 4, v250\n"
    "v_mfma_f32_16x16x32_bf16 v[204:207], a[164:167], a[100:103], v[232:235]\n"
    "s_mov_b32 s52, 6100992\n"
    "v_exp_f32_e32 v170, v170\n"
    "v_bfe_u32 v251, v253, 4, 2\n"
    "v_mfma_f32_16x16x32_bf16 v[204:207], a[160:163], a[96:99], v[204:207]\n"
    "s_lshr_b32 s81, s34, 6\n"
    "v_exp_f32_e32 v171, v171\n"
    "v_mfma_f32_16x16x32_bf16 v[196:199], a[160:163], a[64:67], v[228:231]\n"
    "s_lshl_b32 s81, s81, 9\n"
    "v_cvt_pk_bf16_f32 v166, v168, v169\n"
    "v_lshlrev_b32_e32 v251, 1, v251\n"
    "v_xor_b32_e32 v251, v251, v253\n"
    "v_mfma_f32_16x16x32_bf16 v[196:199], a[164:167], a[68:71], v[196:199]\n"
    "s_waitcnt lgkmcnt(8)\n"
    "ds_read_b128 a[188:191], v243 offset:28672\n"
    "v_and_b32_e32 v251, 7, v251\n"
    "v_mfma_f32_16x16x32_bf16 v[196:199], a[168:171], a[72:75], v[196:199]\n"
    "s_add_u32 s81, s81, 6074368\n"
    "v_exp_f32_e32 v176, v176\n"
    "v_mfma_f32_16x16x32_bf16 v[196:199], a[172:175], a[76:79], v[196:199]\n"
    "s_lshl_b32 s63, s93, 13\n"
    "v_exp_f32_e32 v177, v177\n"
    "v_mfma_f32_16x16x32_bf16 v[204:207], a[172:175], a[108:111], v[204:207]\n"
    "s_xor_b32 s45, s63, 0x4000\n"
    "ds_read_b128 a[160:163], v240 offset:26624\n"
    "v_lshlrev_b32_e32 v251, 4, v251\n"
    "v_lshrrev_b32_e32 v253, 3, v253\n"
    "v_mfma_f32_16x16x32_bf16 v[204:207], a[168:171], a[104:107], v[204:207]\n"
    "s_mul_i32 s58, s93, 33792\n"
    "ds_write_addtid_b32 v150 offset:1536\n"
    "v_lshl_add_u32 v251, v253, 9, v251\n"
    "v_mfma_f32_16x16x32_bf16 v[212:215], a[168:171], a[136:139], v[212:215]\n"
    "s_add_u32 s57, s58, 5513216\n"
    "ds_write_addtid_b32 v151 offset:1792\n"
    "v_bfe_u32 v255, v250, 2, 4\n"
    "v_mfma_f32_16x16x32_bf16 v[212:215], a[172:175], a[140:143], v[212:215]\n"
    "s_waitcnt lgkmcnt(7)\n"
    "ds_read_b128 a[168:171], v240 offset:30720\n"
    "v_mov_b32_e32 v254, s39\n"
    "v_mfma_f32_16x16x32_bf16 v[212:215], a[176:179], a[144:147], v[212:215]\n"
    "s_add_u32 s58, s58, 5378048\n"
    "ds_read_b128 a[164:167], v241 offset:26624\n"
    "v_lshrrev_b32_e32 v252, 2, v250\n"
    "v_mfma_f32_16x16x32_bf16 v[212:215], a[180:183], a[148:151], v[212:215]\n"
    "s_add_u32 s36, s35, 0x8000\n"
    "ds_write_addtid_b32 v158 offset:2560\n"
    "v_cvt_pk_bf16_f32 v167, v170, v171\n"
    "v_mfma_f32_16x16x32_bf16 v[204:207], a[180:183], a[116:119], v[204:207]\n"
    "s_add_u32 s37, s35, 0x10000\n"
    "v_exp_f32_e32 v178, v178\n"
    "v_mfma_f32_16x16x32_bf16 v[204:207], a[176:179], a[112:115], v[204:207]\n"
    "s_cmp_lt_u32 s32, 2\n"
    "s_cselect_b32 s37, s37, 2.0\n"
    "ds_read_b128 a[172:175], v241 offset:30720\n"
    "v_mfma_f32_16x16x32_bf16 v[196:199], a[176:179], a[80:83], v[196:199]\n"
    "s_cmp_lt_u32 s36, 5120000\n"
    "s_cselect_b32 s36, s36, 2.0\n"
    "ds_write_addtid_b32 v159 offset:2816\n"
    "v_mfma_f32_16x16x32_bf16 v[196:199], a[180:183], a[84:87], v[196:199]\n"
    "s_waitcnt lgkmcnt(8)\n"
    "v_exp_f32_e32 v179, v179\n"
    "v_mfma_f32_16x16x32_bf16 v[196:199], a[184:187], a[88:91], v[196:199]\n"
    "s_cmp_lt_u32 s37, 5120000\n"
    "s_cselect_b32 s37, s37, 2.0\n"
    "ds_read_b128 a[176:179], v242 offset:26624\n"
    "v_mfma_f32_16x16x32_bf16 v[196:199], a[188:191], a[92:95], v[196:199]\n"
    "buffer_load_dword v253, v255, s[8:11], s52 offen offset:0 sc1\n"
    "v_mfma_f32_16x16x32_bf16 v[204:207], a[188:191], a[124:127], v[204:207]\n"
    "ds_read_b128 a[180:183], v243 offset:26624\n"
    "v_cvt_pk_bf16_f32 v174, v176, v177\n"
    "v_mfma_f32_16x16x32_bf16 v[204:207], a[184:187], a[120:123], v[204:207]\n"
    "ds_write_addtid_b32 v166 offset:3584\n"
    "v_cvt_pk_bf16_f32 v175, v178, v179\n"
    "v_mfma_f32_16x16x32_bf16 v[212:215], a[184:187], a[152:155], v[212:215]\n"
    "v_exp_f32_e32 v196, v196\n"
    "v_mfma_f32_16x16x32_bf16 v[212:215], a[188:191], a[156:159], v[212:215]\n"
    "s_waitcnt lgkmcnt(6)\n"
    "ds_write_addtid_b32 v167 offset:3840\n"
    "v_mfma_f32_16x16x32_bf16 v[216:219], a[160:163], a[128:131], v[236:239]\n"
    "ds_write_addtid_b32 v174 offset:4608\n"
    "ds_write_addtid_b32 v175 offset:4864\n"
    "v_mfma_f32_16x16x32_bf16 v[216:219], a[164:167], a[132:135], v[216:219]\n"
    "ds_read_b128 a[184:187], v242 offset:30720\n"
    "v_mfma_f32_16x16x32_bf16 v[208:211], a[164:167], a[100:103], v[232:235]\n"
    "ds_read_b128 a[188:191], v243 offset:30720\n"
    "v_mfma_f32_16x16x32_bf16 v[208:211], a[160:163], a[96:99], v[208:211]\n"
    "s_waitcnt lgkmcnt(2)\n"
    "v_mfma_f32_16x16x32_bf16 v[200:203], a[160:163], a[64:67], v[228:231]\n"
    "s_barrier\n"
    "v_mfma_f32_16x16x32_bf16 v[200:203], a[164:167], a[68:71], v[200:203]\n"
    "ds_read_b64_tr_b16 a[160:161], v244 offset:16384\n"
    "ds_read2st64_b32 v[178:179], v249 offset0:18 offset1:19\n"
    "v_mfma_f32_16x16x32_bf16 v[200:203], a[168:171], a[72:75], v[200:203]\n"
    "s_add_u32 m0, s43, 8192\n"
    "ds_read_b64_tr_b16 a[162:163], v244 offset:18432\n"
    "v_mfma_f32_16x16x32_bf16 v[200:203], a[172:175], a[76:79], v[200:203]\n"
    "buffer_load_dwordx4 v248, s[8:11], s40 offen offset:0 sc1 lds\n"
    "v_mfma_f32_16x16x32_bf16 v[208:211], a[172:175], a[108:111], v[208:211]\n"
    "v_exp_f32_e32 v197, v197\n"
    "v_mfma_f32_16x16x32_bf16 v[208:211], a[168:171], a[104:107], v[208:211]\n"
    "ds_read2st64_b32 v[176:177], v249 offset0:16 offset1:17\n"
    "v_mfma_f32_16x16x32_bf16 v[216:219], a[168:171], a[136:139], v[216:219]\n"
    "ds_read2st64_b32 v[170:171], v249 offset0:14 offset1:15\n"
    "v_mfma_f32_16x16x32_bf16 v[216:219], a[172:175], a[140:143], v[216:219]\n"
    "ds_read_b64_tr_b16 a[164:165], v244 offset:49152\n"
    "v_mfma_f32_16x16x32_bf16 v[216:219], a[176:179], a[144:147], v[216:219]\n"
    "buffer_load_dwordx4 v248, s[8:11], s40 offen offset:1024 sc1 lds\n"
    "v_mfma_f32_16x16x32_bf16 v[216:219], a[180:183], a[148:151], v[216:219]\n"
    "ds_read2st64_b32 v[162:163], v249 offset0:10 offset1:11\n"
    "ds_read2st64_b32 v[168:169], v249 offset0:12 offset1:13\n"
    "v_mfma_f32_16x16x32_bf16 v[208:211], a[180:183], a[116:119], v[208:211]\n"
    "ds_read_b64_tr_b16 a[166:167], v244 offset:51200\n"
    "v_mfma_f32_16x16x32_bf16 v[208:211], a[176:179], a[112:115], v[208:211]\n"
    "ds_read2st64_b32 v[154:155], v249 offset0:6 offset1:7\n"
    "ds_read2st64_b32 v[160:161], v249 offset0:8 offset1:9\n"
    "v_mfma_f32_16x16x32_bf16 v[200:203], a[176:179], a[80:83], v[200:203]\n"
    "ds_read2st64_b32 v[146:147], v249 offset0:2 offset1:3\n"
    "ds_read2st64_b32 v[152:153], v249 offset0:4 offset1:5\n"
    "v_mfma_f32_16x16x32_bf16 v[200:203], a[180:183], a[84:87], v[200:203]\n"
    "s_waitcnt lgkmcnt(13)\n"
    "ds_read2st64_b32 v[144:145], v249 offset0:0 offset1:1\n"
    "v_mfma_f32_16x16x32_bf16 v[200:203], a[184:187], a[88:91], v[200:203]\n"
    "buffer_load_dwordx4 v248, s[8:11], s40 offen offset:2048 sc1 lds\n"
    "v_mfma_f32_16x16x32_bf16 v[200:203], a[188:191], a[92:95], v[200:203]\n"
    "v_exp_f32_e32 v198, v198\n"
    "v_mfma_f32_16x16x32_bf16 v[208:211], a[188:191], a[124:127], v[208:211]\n"
    "ds_read_b64_tr_b16 a[176:177], v246 offset:16384\n"
    "v_mfma_f32_16x16x32_bf16 v[208:211], a[184:187], a[120:123], v[208:211]\n"
    "ds_read_b64_tr_b16 a[168:169], v245 offset:16384\n"
    "v_mfma_f32_16x16x32_bf16 v[216:219], a[184:187], a[152:155], v[216:219]\n"
    "ds_read_b64_tr_b16 a[170:171], v245 offset:18432\n"
    "v_mfma_f32_16x16x32_bf16 v[216:219], a[188:191], a[156:159], v[216:219]\n"
    "s_waitcnt lgkmcnt(3)\n"
    "ds_read_b64_tr_b16 a[184:185], v247 offset:16384\n"
    "v_mfma_f32_16x16x32_bf16 a[252:255], a[160:163], v[172:175], a[252:255]\n"
    "buffer_load_dwordx4 v248, s[8:11], s40 offen offset:3072 sc1 lds\n"
    "v_mfma_f32_16x16x32_bf16 a[252:255], a[164:167], v[176:179], a[252:255]\n"
    "s_add_u32 s40, s40, 0x8000\n"
    "ds_read_b64_tr_b16 a[172:173], v245 offset:49152\n"
    "v_mfma_f32_16x16x32_bf16 a[248:251], a[164:167], v[168:171], a[248:251]\n"
    "s_add_u32 m0, s43, 12288\n"
    "ds_read_b64_tr_b16 a[174:175], v245 offset:51200\n"
    "v_mfma_f32_16x16x32_bf16 a[248:251], a[160:163], v[164:167], a[248:251]\n"
    "v_exp_f32_e32 v199, v199\n"
    "v_mfma_f32_16x16x32_bf16 a[244:247], a[160:163], v[156:159], a[244:247]\n"
    "ds_read_b64_tr_b16 a[186:187], v247 offset:18432\n"
    "v_mfma_f32_16x16x32_bf16 a[244:247], a[164:167], v[160:163], a[244:247]\n"
    "buffer_load_dwordx4 v248, s[8:11], s41 offen offset:0 sc1 lds\n"
    "v_mfma_f32_16x16x32_bf16 a[240:243], a[164:167], v[152:155], a[240:243]\n"
    "ds_read_b64_tr_b16 a[178:179], v246 offset:18432\n"
    "v_mfma_f32_16x16x32_bf16 a[240:243], a[160:163], v[148:151], a[240:243]\n"
    "ds_read_b64_tr_b16 a[188:189], v247 offset:49152\n"
    "v_mfma_f32_16x16x32_bf16 a[236:239], a[160:163], v[140:143], a[236:239]\n"
    "ds_read_b64_tr_b16 a[180:181], v246 offset:49152\n"
    "v_mfma_f32_16x16x32_bf16 a[236:239], a[164:167], v[144:147], a[236:239]\n"
    "s_waitcnt lgkmcnt(4)\n"
    "ds_read_b64_tr_b16 a[182:183], v246 offset:51200\n"
    "v_cvt_pk_bf16_f32 v196, v196, v197\n"
    "v_mfma_f32_16x16x32_bf16 v[0:3], a[168:171], v[140:143], v[0:3]\n"
    "buffer_load_dwordx4 v248, s[8:11], s41 offen offset:1024 sc1 lds\n"
    "v_mfma_f32_16x16x32_bf16 v[0:3], a[172:175], v[144:147], v[0:3]\n"
    "ds_read_b64_tr_b16 a[190:191], v247 offset:51200\n"
    "v_mfma_f32_16x16x32_bf16 v[4:7], a[172:175], v[152:155], v[4:7]\n"
    "v_exp_f32_e32 v204, v204\n"
    "v_mfma_f32_16x16x32_bf16 v[4:7], a[168:171], v[148:151], v[4:7]\n"
    "ds_read_b64_tr_b16 a[160:161], v244 offset:20480\n"
    "v_subrev_f32_e32 v0, s4, v0\n"
    "v_mfma_f32_16x16x32_bf16 v[8:11], a[168:171], v[156:159], v[8:11]\n"
    "v_exp_f32_e32 v205, v205\n"
    "v_mfma_f32_16x16x32_bf16 v[8:11], a[172:175], v[160:163], v[8:11]\n"
    "buffer_load_dwordx4 v248, s[8:11], s41 offen offset:2048 sc1 lds\n"
    "v_mfma_f32_16x16x32_bf16 v[12:15], a[172:175], v[168:171], v[12:15]\n"
    "ds_read_b64_tr_b16 a[162:163], v244 offset:22528\n"
    "v_cvt_pk_bf16_f32 v197, v198, v199\n"
    "v_mfma_f32_16x16x32_bf16 v[12:15], a[168:171], v[164:167], v[12:15]\n"
    "s_waitcnt vmcnt(7)\n"
    "v_mfma_f32_16x16x32_bf16 v[16:19], a[168:171], v[172:175], v[16:19]\n"
    "s_barrier\n"
    "v_mfma_f32_16x16x32_bf16 v[16:19], a[172:175], v[176:179], v[16:19]\n"
    "s_waitcnt lgkmcnt(3)\n"
    "v_add_u32_e32 v249, s45, v252\n"
    "v_add_u32_e32 v252, s63, v252\n"
    "v_mfma_f32_16x16x32_bf16 v[36:39], a[176:179], v[172:175], v[36:39]\n"
    "buffer_store_dword v254, off, s[8:11], s84 offset:0 sc1\n"
    "v_mfma_f32_16x16x32_bf16 v[36:39], a[180:183], v[176:179], v[36:39]\n"
    "s_mul_i32 s45, s49, 63\n"
    "ds_read_b64_tr_b16 a[164:165], v244 offset:53248\n"
    "v_subrev_f32_e32 v12, s4, v12\n"
    "v_mfma_f32_16x16x32_bf16 v[32:35], a[180:183], v[168:171], v[32:35]\n"
    "s_add_u32 s45, s45, s33\n"
    "ds_read_b64_tr_b16 a[168:169], v245 offset:20480\n"
    "v_subrev_f32_e32 v16, s4, v16\n"
    "v_mfma_f32_16x16x32_bf16 v[32:35], a[176:179], v[164:167], v[32:35]\n"
    "buffer_load_dwordx4 v248, s[8:11], s41 offen offset:3072 sc1 lds\n"
    "v_mfma_f32_16x16x32_bf16 v[28:31], a[176:179], v[156:159], v[28:31]\n"
    "s_add_u32 s41, s41, 0x8000\n"
    "ds_read_b64_tr_b16 a[166:167], v244 offset:55296\n"
    "v_cmp_ne_u32_e32 vcc, s39, v253\n"
    "v_mfma_f32_16x16x32_bf16 v[28:31], a[180:183], v[160:163], v[28:31]\n"
    "s_mov_b32 m0, s63\n"
    "v_subrev_f32_e32 v36, s4, v36\n"
    "v_subrev_f32_e32 v32, s4, v32\n"
    "v_mfma_f32_16x16x32_bf16 v[24:27], a[180:183], v[152:155], v[24:27]\n"
    "s_mul_i32 s45, s45, 405504\n"
    "ds_read_b64_tr_b16 a[170:171], v245 offset:22528\n"
    "v_subrev_f32_e32 v37, s4, v37\n"
    "v_mfma_f32_16x16x32_bf16 v[24:27], a[176:179], v[148:151], v[24:27]\n"
    "s_cbranch_vccnz .Lat_wpoll\n"
    ".Lat_wpoll_ret:\n"
    "v_mfma_f32_16x16x32_bf16 v[20:23], a[176:179], v[140:143], v[20:23]\n"
    "s_add_u32 s62, s57, 0\n"
    "v_exp_f32_e32 v206, v206\n"
    "v_mfma_f32_16x16x32_bf16 v[20:23], a[180:183], v[144:147], v[20:23]\n"
    "s_waitcnt lgkmcnt(6)\n"
    "ds_read_b64_tr_b16 a[172:173], v245 offset:53248\n"
    "v_subrev_f32_e32 v28, s4, v28\n"
    "v_mfma_f32_16x16x32_bf16 v[40:43], a[184:187], v[140:143], v[40:43]\n"
    "buffer_load_dwordx4 a[216:219], v250, s[8:11], s62 offen offset:0 sc1\n"
    "v_mfma_f32_16x16x32_bf16 v[40:43], a[188:191], v[144:147], v[40:43]\n"
    "s_add_u32 s45, s45, 16588800\n"
    "ds_write_addtid_b32 v196 offset:0\n"
    "v_subrev_f32_e32 v8, s4, v8\n"
    "v_mfma_f32_16x16x32_bf16 v[44:47], a[188:191], v[152:155], v[44:47]\n"
    "s_lshl_b32 s46, s51, 12\n"
    "v_subrev_f32_e32 v9, s4, v9\n"
    "v_subrev_f32_e32 v10, s4, v10\n"
    "v_mfma_f32_16x16x32_bf16 v[44:47], a[184:187], v[148:151], v[44:47]\n"
    "s_lshl_b32 s48, s50, 15\n"
    "ds_read_b64_tr_b16 a[176:177], v246 offset:20480\n"
    "v_subrev_f32_e32 v40, s4, v40\n"
    "v_mfma_f32_16x16x32_bf16 v[48:51], a[184:187], v[156:159], v[48:51]\n"
    "s_add_u32 s46, s46, s48\n"
    "v_exp_f32_e32 v207, v207\n"
    "v_mfma_f32_16x16x32_bf16 v[48:51], a[188:191], v[160:163], v[48:51]\n"
    "buffer_load_dwordx4 a[220:223], v250, s[8:11], s62 offen offset:1024 sc1\n"
    "v_mfma_f32_16x16x32_bf16 v[52:55], a[188:191], v[168:171], v[52:55]\n"
    "s_cmp_eq_u32 s51, 0\n"
    "s_cselect_b32 s47, 2.0, 0\n"
    "ds_read_b64_tr_b16 a[178:179], v246 offset:22528\n"
    "v_mfma_f32_16x16x32_bf16 v[52:55], a[184:187], v[164:167], v[52:55]\n"
    "s_add_u32 s47, s47, 98304\n"
    "ds_read_b64_tr_b16 a[174:175], v245 offset:55296\n"
    "v_cvt_pk_bf16_f32 v204, v204, v205\n"
    "v_mfma_f32_16x16x32_bf16 v[56:59], a[184:187], v[172:175], v[56:59]\n"
    "s_lshl_b32 s61, s50, 9\n"
    "ds_read_b64_tr_b16 a[180:181], v246 offset:53248\n"
    "v_mfma_f32_16x16x32_bf16 v[56:59], a[188:191], v[176:179], v[56:59]\n"
    "s_waitcnt lgkmcnt(7)\n"
    "ds_write_addtid_b32 v197 offset:256\n"
    "v_subrev_f32_e32 v48, s4, v48\n"
    "v_mfma_f32_16x16x32_bf16 v[76:79], a[160:163], v[172:175], v[76:79]\n"
    "buffer_load_dwordx4 a[224:227], v250, s[8:11], s62 offen offset:2048 sc1\n"
    "v_mfma_f32_16x16x32_bf16 v[76:79], a[164:167], v[176:179], v[76:79]\n"
    "s_add_u32 s61, s61, s47\n"
    "v_exp_f32_e32 v212, v212\n"
    "v_mfma_f32_16x16x32_bf16 v[72:75], a[164:167], v[168:171], v[72:75]\n"
    "s_add_u32 s48, s32, 3\n"
    "ds_read_b64_tr_b16 a[184:185], v247 offset:20480\n"
    "v_subrev_f32_e32 v29, s4, v29\n"
    "v_mfma_f32_16x16x32_bf16 v[72:75], a[160:163], v[164:167], v[72:75]\n"
    "s_and_b32 s48, s48, 3\n"
    "v_subrev_f32_e32 v4, s4, v4\n"
    "v_mfma_f32_16x16x32_bf16 v[68:71], a[160:163], v[156:159], v[68:71]\n"
    "s_mul_i32 s48, s48, 101376\n"
    "v_exp_f32_e32 v213, v213\n"
    "v_mfma_f32_16x16x32_bf16 v[68:71], a[164:167], v[160:163], v[68:71]\n"
    "buffer_load_dwordx4 a[228:231], v250, s[8:11], s62 offen offset:3072 sc1\n"
    "v_mfma_f32_16x16x32_bf16 v[64:67], a[164:167], v[152:155], v[64:67]\n"
    "s_add_u32 s62, s57, 4096\n"
    "ds_read_b64_tr_b16 a[186:187], v247 offset:22528\n"
    "v_subrev_f32_e32 v76, s4, v76\n"
    "v_mfma_f32_16x16x32_bf16 v[64:67], a[160:163], v[148:151], v[64:67]\n"
    "s_add_u32 s48, s48, s45\n"
    "v_subrev_f32_e32 v33, s4, v33\n"
    "v_subrev_f32_e32 v52, s4, v52\n"
    "v_mfma_f32_16x16x32_bf16 v[60:63], a[160:163], v[140:143], v[60:63]\n"
    "s_add_u32 s29, s48, s46\n"
    "ds_read_b64_tr_b16 a[182:183], v246 offset:55296\n"
    "v_cvt_pk_bf16_f32 v205, v206, v207\n"
    "v_mfma_f32_16x16x32_bf16 v[60:63], a[164:167], v[144:147], v[60:63]\n"
    "s_waitcnt lgkmcnt(5)\n"
    "ds_read_b64_tr_b16 a[188:189], v247 offset:53248\n"
    "v_subrev_f32_e32 v68, s4, v68\n"
    "v_mfma_f32_16x16x32_bf16 v[80:83], a[168:171], v[140:143], v[80:83]\n"
    "buffer_load_dwordx4 a[232:235], v250, s[8:11], s62 offen offset:0 sc1\n"
    "v_mfma_f32_16x16x32_bf16 v[80:83], a[172:175], v[144:147], v[80:83]\n"
    "s_addk_i32 s29, 8192\n"
    "v_exp_f32_e32 v214, v214\n"
    "v_mfma_f32_16x16x32_bf16 v[84:87], a[172:175], v[152:155], v[84:87]\n"
    "s_add_u32 s89, s48, s61\n"
    "v_exp_f32_e32 v215, v215\n"
    "v_mfma_f32_16x16x32_bf16 v[84:87], a[168:171], v[148:151], v[84:87]\n"
    "s_addk_i32 s89, 128\n"
    "ds_write_addtid_b32 v204 offset:1024\n"
    "v_subrev_f32_e32 v38, s4, v38\n"
    "v_mfma_f32_16x16x32_bf16 v[88:91], a[168:171], v[156:159], v[88:91]\n"
    "s_add_u32 s48, s32, 2\n"
    "ds_read_b64_tr_b16 a[190:191], v247 offset:55296\n"
    "v_cvt_pk_bf16_f32 v212, v212, v213\n"
    "v_mfma_f32_16x16x32_bf16 v[88:91], a[172:175], v[160:163], v[88:91]\n"
    "buffer_load_dwordx4 v[192:195], v250, s[8:11], s62 offen offset:1024 sc1\n"
    "v_mfma_f32_16x16x32_bf16 v[92:95], a[172:175], v[168:171], v[92:95]\n"
    "s_and_b32 s48, s48, 3\n"
    "ds_write_addtid_b32 v205 offset:1280\n"
    "v_subrev_f32_e32 v17, s4, v17\n"
    "v_mfma_f32_16x16x32_bf16 v[92:95], a[168:171], v[164:167], v[92:95]\n"
    "s_mul_i32 s48, s48, 101376\n"
    "v_exp_f32_e32 v200, v200\n"
    "v_mfma_f32_16x16x32_bf16 v[96:99], a[168:171], v[172:175], v[96:99]\n"
    "s_add_u32 s48, s48, s45\n"
    "v_exp_f32_e32 v201, v201\n"
    "v_mfma_f32_16x16x32_bf16 v[96:99], a[172:175], v[176:179], v[96:99]\n"
    "s_waitcnt lgkmcnt(4)\n"
    "ds_read_b128 a[160:163], v240 offset:0\n"
    "v_cvt_pk_bf16_f32 v213, v214, v215\n"
    "v_mfma_f32_16x16x32_bf16 v[116:119], a[176:179], v[172:175], v[116:119]\n"
    "s_add_u32 s28, s48, s46\n"
    "ds_write_addtid_b32 v212 offset:2048\n"
    "v_subrev_f32_e32 v49, s4, v49\n"
    "v_mfma_f32_16x16x32_bf16 v[116:119], a[180:183], v[176:179], v[116:119]\n"
    "s_waitcnt lgkmcnt(3)\n"
    "v_mfma_f32_16x16x32_bf16 v[112:115], a[180:183], v[168:171], v[112:115]\n"
    "s_barrier\n"
    "v_mfma_f32_16x16x32_bf16 v[112:115], a[176:179], v[164:167], v[112:115]\n"
    "s_add_u32 m0, s43, 16384\n"
    "v_exp_f32_e32 v202, v202\n"
    "v_mfma_f32_16x16x32_bf16 v[108:111], a[176:179], v[156:159], v[108:111]\n"
    "buffer_load_dwordx4 v248, s[8:11], s40 offen offset:0 sc1 lds\n"
    "v_mfma_f32_16x16x32_bf16 v[108:111], a[180:183], v[160:163], v[108:111]\n"
    "s_mov_b32 m0, s63\n"
    "ds_read_b128 a[168:171], v240 offset:4096\n"
    "v_subrev_f32_e32 v72, s4, v72\n"
    "v_mfma_f32_16x16x32_bf16 v[104:107], a[180:183], v[152:155], v[104:107]\n"
    "s_addk_i32 s28, 16384\n"
    "ds_read_b128 a[164:167], v241 offset:0\n"
    "v_exp_f32_e32 v203, v203\n"
    "v_mfma_f32_16x16x32_bf16 v[104:107], a[176:179], v[148:151], v[104:107]\n"
    "s_add_u32 s88, s48, s61\n"
    "ds_read_b128 a[172:175], v241 offset:4096\n"
    "v_cvt_pk_bf16_f32 v198, v200, v201\n"
    "v_mfma_f32_16x16x32_bf16 v[100:103], a[176:179], v[140:143], v[100:103]\n"
    "s_add_u32 m0, s43, 16384\n"
    "v_exp_f32_e32 v208, v208\n"
    "v_mfma_f32_16x16x32_bf16 v[100:103], a[180:183], v[144:147], v[100:103]\n"
    "v_subrev_f32_e32 v13, s4, v13\n"
    "v_subrev_f32_e32 v108, s4, v108\n"
    "v_mfma_f32_16x16x32_bf16 v[120:123], a[184:187], v[140:143], v[120:123]\n"
    "buffer_load_dwordx4 v248, s[8:11], s40 offen offset:1024 sc1 lds\n"
    "v_mfma_f32_16x16x32_bf16 v[120:123], a[188:191], v[144:147], v[120:123]\n"
    "s_mov_b32 m0, s63\n"
    "v_subrev_f32_e32 v39, s4, v39\n"
    "v_subrev_f32_e32 v44, s4, v44\n"
    "v_mfma_f32_16x16x32_bf16 v[124:127], a[188:191], v[152:155], v[124:127]\n"
    "s_addk_i32 s88, 256\n"
    "v_exp_f32_e32 v209, v209\n"
    "v_mfma_f32_16x16x32_bf16 v[124:127], a[184:187], v[148:151], v[124:127]\n"
    "s_add_u32 s48, s32, 1\n"
    "ds_write_addtid_b32 v213 offset:2304\n"
    "v_cvt_pk_bf16_f32 v199, v202, v203\n"
    "v_mfma_f32_16x16x32_bf16 v[128:131], a[184:187], v[156:159], v[128:131]\n"
    "s_add_u32 m0, s43, 16384\n"
    "v_exp_f32_e32 v210, v210\n"
    "v_mfma_f32_16x16x32_bf16 v[128:131], a[188:191], v[160:163], v[128:131]\n"
    "buffer_load_dwordx4 v248, s[8:11], s40 offen offset:2048 sc1 lds\n"
    "v_mfma_f32_16x16x32_bf16 v[132:135], a[188:191], v[168:171], v[132:135]\n"
    "s_mov_b32 m0, s63\n"
    "v_exp_f32_e32 v211, v211\n"
    "v_mfma_f32_16x16x32_bf16 v[132:135], a[184:187], v[164:167], v[132:135]\n"
    "s_and_b32 s48, s48, 3\n"
    "ds_write_addtid_b32 v198 offset:512\n"
    "v_cvt_pk_bf16_f32 v206, v208, v209\n"
    "v_mfma_f32_16x16x32_bf16 v[136:139], a[184:187], v[172:175], v[136:139]\n"
    "s_mul_i32 s48, s48, 101376\n"
    "v_exp_f32_e32 v216, v216\n"
    "v_mfma_f32_16x16x32_bf16 v[136:139], a[188:191], v[176:179], v[136:139]\n"
    "s_waitcnt lgkmcnt(3)\n"
    "v_exp_f32_e32 v217, v217\n"
    "v_mfma_f32_16x16x32_bf16 v[172:175], a[160:163], a[128:131], v[236:239]\n"
    "s_add_u32 m0, s43, 16384\n"
    "ds_read_b128 a[176:179], v242 offset:0\n"
    "v_subrev_f32_e32 v73, s4, v73\n"
    "v_mfma_f32_16x16x32_bf16 v[172:175], a[164:167], a[132:135], v[172:175]\n"
    "buffer_load_dwordx4 v248, s[8:11], s40 offen offset:3072 sc1 lds\n"
    "v_mfma_f32_16x16x32_bf16 v[164:167], a[164:167], a[100:103], v[232:235]\n"
    "s_mov_b32 m0, s63\n"
    "ds_read_b128 a[180:183], v243 offset:0\n"
    "v_subrev_f32_e32 v18, s4, v18\n"
    "v_mfma_f32_16x16x32_bf16 v[164:167], a[160:163], a[96:99], v[164:167]\n"
    "s_add_u32 s48, s48, s45\n"
    "ds_write_addtid_b32 v199 offset:768\n"
    "v_cvt_pk_bf16_f32 v207, v210, v211\n"
    "v_mfma_f32_16x16x32_bf16 v[156:159], a[160:163], a[64:67], v[228:231]\n"
    "s_add_u32 s82, s48, s46\n"
    "ds_read_b128 a[184:187], v242 offset:4096\n"
    "v_subrev_f32_e32 v88, s4, v88\n"
    "v_mfma_f32_16x16x32_bf16 v[156:159], a[164:167], a[68:71], v[156:159]\n"
    "s_waitcnt lgkmcnt(6)\n"
    "v_exp_f32_e32 v218, v218\n"
    "v_mfma_f32_16x16x32_bf16 v[156:159], a[168:171], a[72:75], v[156:159]\n"
    "s_add_u32 m0, s43, 20480\n"
    "v_exp_f32_e32 v219, v219\n"
    "v_mfma_f32_16x16x32_bf16 v[156:159], a[172:175], a[76:79], v[156:159]\n"
    "buffer_load_dwordx4 v248, s[8:11], s41 offen offset:0 sc1 lds\n"
    "v_mfma_f32_16x16x32_bf16 v[164:167], a[172:175], a[108:111], v[164:167]\n"
    "s_mov_b32 m0, s63\n"
    "v_subrev_f32_e32 v14, s4, v14\n"
    "v_subrev_f32_e32 v19, s4, v19\n"
    "v_mfma_f32_16x16x32_bf16 v[164:167], a[168:171], a[104:107], v[164:167]\n"
    "s_addk_i32 s82, 24576\n"
    "ds_read_b128 a[188:191], v243 offset:4096\n"
    "v_subrev_f32_e32 v112, s4, v112\n"
    "v_mfma_f32_16x16x32_bf16 v[172:175], a[168:171], a[136:139], v[172:175]\n"
    "s_add_u32 s87, s48, s61\n"
    "ds_write_addtid_b32 v206 offset:1536\n"
    "v_cvt_pk_bf16_f32 v214, v216, v217\n"
    "v_mfma_f32_16x16x32_bf16 v[172:175], a[172:175], a[140:143], v[172:175]\n"
    "s_waitcnt lgkmcnt(4)\n"
    "ds_write_addtid_b32 v207 offset:1792\n"
    "v_subrev_f32_e32 v96, s4, v96\n"
    "v_mfma_f32_16x16x32_bf16 v[172:175], a[176:179], a[144:147], v[172:175]\n"
    "s_add_u32 m0, s43, 20480\n"
    "ds_read_b128 a[160:163], v240 offset:2048\n"
    "v_cvt_pk_bf16_f32 v215, v218, v219\n"
    "v_mfma_f32_16x16x32_bf16 v[172:175], a[180:183], a[148:151], v[172:175]\n"
    "buffer_load_dwordx4 v248, s[8:11], s41 offen offset:1024 sc1 lds\n"
    "v_mfma_f32_16x16x32_bf16 v[164:167], a[180:183], a[116:119], v[164:167]\n"
    "s_mov_b32 m0, s63\n"
    "ds_read_b128 a[164:167], v241 offset:2048\n"
    "v_subrev_f32_e32 v89, s4, v89\n"
    "v_mfma_f32_16x16x32_bf16 v[164:167], a[176:179], a[112:115], v[164:167]\n"
    "s_addk_i32 s87, 384\n"
    "ds_read_b128 a[168:171], v240 offset:6144\n"
    "v_subrev_f32_e32 v90, s4, v90\n"
    "v_mfma_f32_16x16x32_bf16 v[156:159], a[176:179], a[80:83], v[156:159]\n"
    "s_mul_i32 s48, s32, 101376\n"
    "ds_write_addtid_b32 v214 offset:2560\n"
    "v_subrev_f32_e32 v56, s4, v56\n"
    "v_mfma_f32_16x16x32_bf16 v[156:159], a[180:183], a[84:87], v[156:159]\n"
    "s_waitcnt lgkmcnt(6)\n"
    "ds_write_addtid_b32 v215 offset:2816\n"
    "v_subrev_f32_e32 v30, s4, v30\n"
    "v_mfma_f32_16x16x32_bf16 v[156:159], a[184:187], a[88:91], v[156:159]\n"
    "s_add_u32 m0, s43, 20480\n"
    "ds_read_b128 a[176:179], v242 offset:2048\n"
    "v_subrev_f32_e32 v77, s4, v77\n"
    "v_mfma_f32_16x16x32_bf16 v[156:159], a[188:191], a[92:95], v[156:159]\n"
    "buffer_load_dwordx4 v248, s[8:11], s41 offen offset:2048 sc1 lds\n"
    "v_mfma_f32_16x16x32_bf16 v[164:167], a[188:191], a[124:127], v[164:167]\n"
    "s_mov_b32 m0, s63\n"
    "ds_read_b128 a[172:175], v241 offset:6144\n"
    "v_subrev_f32_e32 v11, s4, v11\n"
    "v_mfma_f32_16x16x32_bf16 v[164:167], a[184:187], a[120:123], v[164:167]\n"
    "s_add_u32 s48, s48, s45\n"
    "ds_read_b128 a[180:183], v243 offset:2048\n"
    "v_subrev_f32_e32 v69, s4, v69\n"
    "v_mfma_f32_16x16x32_bf16 v[172:175], a[184:187], a[152:155], v[172:175]\n"
    "s_add_u32 s59, s48, s46\n"
    "v_exp_f32_e32 v156, v156\n"
    "v_mfma_f32_16x16x32_bf16 v[172:175], a[188:191], a[156:159], v[172:175]\n"
    "s_waitcnt lgkmcnt(6)\n"
    "v_exp_f32_e32 v157, v157\n"
    "v_mfma_f32_16x16x32_bf16 v[176:179], a[160:163], a[128:131], v[236:239]\n"
    "s_add_u32 m0, s43, 20480\n"
    "v_exp_f32_e32 v158, v158\n"
    "v_mfma_f32_16x16x32_bf16 v[176:179], a[164:167], a[132:135], v[176:179]\n"
    "buffer_load_dwordx4 v248, s[8:11], s41 offen offset:3072 sc1 lds\n"
    "v_mfma_f32_16x16x32_bf16 v[168:171], a[164:167], a[100:103], v[232:235]\n"
    "s_mov_b32 m0, s63\n"
    "ds_read_b128 a[184:187], v242 offset:6144\n"
    "ds_read_b128 a[188:191], v243 offset:6144\n"
    "v_mfma_f32_16x16x32_bf16 v[168:171], a[160:163], a[96:99], v[168:171]\n"
    "s_waitcnt lgkmcnt(5)\n"
    "s_waitcnt vmcnt(14)\n"
    "v_mfma_f32_16x16x32_bf16 v[160:163], a[160:163], a[64:67], v[228:231]\n"
    "s_barrier\n"
    "v_mfma_f32_16x16x32_bf16 v[160:163], a[164:167], a[68:71], v[160:163]\n"
    "s_waitcnt lgkmcnt(3)\n"
    "v_exp_f32_e32 v159, v159\n"
    "v_mfma_f32_16x16x32_bf16 v[160:163], a[168:171], a[72:75], v[160:163]\n"
    "s_add_u32 s60, s48, s61\n"
    "ds_read2st64_b32 a[196:197], v249 offset0:0 offset1:1\n"
    "v_cvt_pk_bf16_f32 v156, v156, v157\n"
    "v_mfma_f32_16x16x32_bf16 v[160:163], a[172:175], a[76:79], v[160:163]\n"
    "s_and_b32 s48, s32, 1\n"
    "v_exp_f32_e32 v164, v164\n"
    "v_mfma_f32_16x16x32_bf16 v[168:171], a[172:175], a[108:111], v[168:171]\n"
    "s_xor_b32 s48, s48, s50\n"
    "ds_write_addtid_b32 v156 offset:3072\n"
    "v_subrev_f32_e32 v132, s4, v132\n"
    "v_mfma_f32_16x16x32_bf16 v[168:171], a[168:171], a[104:107], v[168:171]\n"
    "s_sub_u32 s61, s32, s48\n"
    "ds_read2st64_b32 a[198:199], v249 offset0:2 offset1:3\n"
    "v_subrev_f32_e32 v109, s4, v109\n"
    "v_mfma_f32_16x16x32_bf16 v[176:179], a[168:171], a[136:139], v[176:179]\n"
    "s_and_b32 s61, s61, 3\n"
    "ds_read2st64_b32 a[192:193], v252 offset0:0 offset1:1\n"
    "v_subrev_f32_e32 v57, s4, v57\n"
    "v_mfma_f32_16x16x32_bf16 v[176:179], a[172:175], a[140:143], v[176:179]\n"
    "s_waitcnt lgkmcnt(6)\n"
    "v_exp_f32_e32 v165, v165\n"
    "v_mfma_f32_16x16x32_bf16 v[176:179], a[176:179], a[144:147], v[176:179]\n"
    "s_add_u32 s61, s61, 8\n"
    "ds_read2st64_b32 a[194:195], v252 offset0:2 offset1:3\n"
    "v_cvt_pk_bf16_f32 v157, v158, v159\n"
    "v_mfma_f32_16x16x32_bf16 v[176:179], a[180:183], a[148:151], v[176:179]\n"
    "s_mul_i32 s48, s48, 101376\n"
    "ds_read2st64_b32 a[204:205], v249 offset0:4 offset1:5\n"
    "v_exp_f32_e32 v166, v166\n"
    "v_mfma_f32_16x16x32_bf16 v[168:171], a[180:183], a[116:119], v[168:171]\n"
    "s_add_u32 s48, s48, s45\n"
    "ds_read_b128 a[160:163], v240 offset:8192\n"
    "v_exp_f32_e32 v167, v167\n"
    "v_mfma_f32_16x16x32_bf16 v[168:171], a[176:179], a[112:115], v[168:171]\n"
    "s_lshl_b32 s80, s61, 13\n"
    "ds_write_addtid_b32 v157 offset:3328\n"
    "v_subrev_f32_e32 v34, s4, v34\n"
    "v_mfma_f32_16x16x32_bf16 v[160:163], a[176:179], a[80:83], v[160:163]\n"
    "s_add_u32 s80, s80, s48\n"
    "ds_read_b128 a[164:167], v241 offset:8192\n"
    "v_subrev_f32_e32 v70, s4, v70\n"
    "v_mfma_f32_16x16x32_bf16 v[160:163], a[180:183], a[84:87], v[160:163]\n"
    "s_waitcnt lgkmcnt(9)\n"
    "ds_read_b128 a[176:179], v242 offset:8192\n"
    "v_cvt_pk_bf16_f32 v164, v164, v165\n"
    "v_mfma_f32_16x16x32_bf16 v[160:163], a[184:187], a[88:91], v[160:163]\n"
    "s_lshl_b32 s46, s51, 12\n"
    "ds_read_b128 a[168:171], v240 offset:12288\n"
    "v_exp_f32_e32 v172, v172\n"
    "v_mfma_f32_16x16x32_bf16 v[160:163], a[188:191], a[92:95], v[160:163]\n"
    "s_add_u32 s80, s80, s46\n"
    "ds_read_b128 a[180:183], v243 offset:8192\n"
    "v_exp_f32_e32 v173, v173\n"
    "v_mfma_f32_16x16x32_bf16 v[168:171], a[188:191], a[124:127], v[168:171]\n"
    "s_lshl_b32 s86, s61, 7\n"
    "ds_write_addtid_b32 v164 offset:4096\n"
    "v_cvt_pk_bf16_f32 v165, v166, v167\n"
    "v_mfma_f32_16x16x32_bf16 v[168:171], a[184:187], a[120:123], v[168:171]\n"
    "s_add_u32 s86, s86, s48\n"
    "ds_read_b128 a[172:175], v241 offset:12288\n"
    "v_subrev_f32_e32 v41, s4, v41\n"
    "v_mfma_f32_16x16x32_bf16 v[176:179], a[184:187], a[152:155], v[176:179]\n"
    "s_add_u32 s86, s86, s47\n"
    "ds_read2st64_b32 a[206:207], v249 offset0:6 offset1:7\n"
    "v_exp_f32_e32 v174, v174\n"
    "v_mfma_f32_16x16x32_bf16 v[176:179], a[188:191], a[156:159], v[176:179]\n"
    "s_waitcnt lgkmcnt(6)\n"
    ".long 0xbedf00ff\n"
    ".Lat_w10:\n"
    "s_setpc_b64 s[2:3]\n"
    "ds_read_b128 a[184:187], v242 offset:12288\n"
    "ds_read_b128 a[188:191], v243 offset:12288\n"
    "v_mfma_f32_16x16x32_bf16 v[180:183], a[160:163], a[128:131], v[236:239]\n"
    "s_lshl_b32 s46, s93, 7\n"
    "v_exp_f32_e32 v175, v175\n"
    "v_mfma_f32_16x16x32_bf16 v[180:183], a[164:167], a[132:135], v[180:183]\n"
    "s_lshl_b32 s55, s93, 15\n"
    "ds_read2st64_b32 a[200:201], v252 offset0:4 offset1:5\n"
    "v_subrev_f32_e32 v15, s4, v15\n"
    "v_mfma_f32_16x16x32_bf16 v[148:151], a[164:167], a[100:103], v[232:235]\n"
    "s_sub_u32 s54, s55, 24576\n"
    "ds_write_addtid_b32 v165 offset:4352\n"
    "v_cvt_pk_bf16_f32 v172, v172, v173\n"
    "v_mfma_f32_16x16x32_bf16 v[148:151], a[160:163], a[96:99], v[148:151]\n"
    "s_add_u32 s55, s55, 32768\n"
    "v_exp_f32_e32 v160, v160\n"
    "v_mfma_f32_16x16x32_bf16 v[140:143], a[160:163], a[64:67], v[228:231]\n"
    "s_lshl2_add_u32 s38, s34, s93\n"
    "v_exp_f32_e32 v161, v161\n"
    "v_mfma_f32_16x16x32_bf16 v[140:143], a[164:167], a[68:71], v[140:143]\n"
    "s_waitcnt lgkmcnt(5)\n"
    "ds_read_b128 a[160:163], v240 offset:10240\n"
    "v_cvt_pk_bf16_f32 v173, v174, v175\n"
    "v_mfma_f32_16x16x32_bf16 v[140:143], a[168:171], a[72:75], v[140:143]\n"
    "s_lshl1_add_u32 s38, s38, s49\n"
    "ds_read_b128 a[164:167], v241 offset:10240\n"
    "v_exp_f32_e32 v162, v162\n"
    "v_mfma_f32_16x16x32_bf16 v[140:143], a[172:175], a[76:79], v[140:143]\n"
    "s_lshl_b32 s38, s38, 7\n"
    "ds_read2st64_b32 a[202:203], v252 offset0:6 offset1:7\n"
    "v_exp_f32_e32 v163, v163\n"
    "v_mfma_f32_16x16x32_bf16 v[148:151], a[172:175], a[108:111], v[148:151]\n"
    "s_add_u32 s38, s38, 5648384\n"
    "v_cvt_pk_bf16_f32 v158, v160, v161\n"
    "v_exp_f32_e32 v168, v168\n"
    "v_mfma_f32_16x16x32_bf16 v[148:151], a[168:171], a[104:107], v[148:151]\n"
    "s_lshl3_add_u32 s90, s34, s49\n"
    "v_exp_f32_e32 v169, v169\n"
    "v_mfma_f32_16x16x32_bf16 v[180:183], a[168:171], a[136:139], v[180:183]\n"
    "s_lshl2_add_u32 s90, s90, s93\n"
    "ds_write_addtid_b32 v172 offset:5120\n"
    "v_cvt_pk_bf16_f32 v159, v162, v163\n"
    "v_mfma_f32_16x16x32_bf16 v[180:183], a[172:175], a[140:143], v[180:183]\n"
    "ds_read_b128 a[168:171], v240 offset:14336\n"
    "v_exp_f32_e32 v170, v170\n"
    "v_mfma_f32_16x16x32_bf16 v[180:183], a[176:179], a[144:147], v[180:183]\n"
    "s_lshl2_add_u32 s90, s90, 6041600\n"
    "v_cvt_pk_bf16_f32 v166, v168, v169\n"
    "v_exp_f32_e32 v171, v171\n"
    "v_mfma_f32_16x16x32_bf16 v[180:183], a[180:183], a[148:151], v[180:183]\n"
    "s_lshl_b32 s45, s51, 15\n"
    "ds_read_b128 a[172:175], v241 offset:14336\n"
    "v_exp_f32_e32 v176, v176\n"
    "v_mfma_f32_16x16x32_bf16 v[148:151], a[180:183], a[116:119], v[148:151]\n"
    "s_sub_u32 s45, 24576, s45\n"
    "v_exp_f32_e32 v177, v177\n"
    "v_mfma_f32_16x16x32_bf16 v[148:151], a[176:179], a[112:115], v[148:151]\n"
    "s_add_u32 s47, s46, 6103040\n"
    "ds_read2st64_b32 a[212:213], v249 offset0:8 offset1:9\n"
    "v_cvt_pk_bf16_f32 v167, v170, v171\n"
    "v_mfma_f32_16x16x32_bf16 v[140:143], a[176:179], a[80:83], v[140:143]\n"
    "s_andn2_b32 s48, s38, 0xc00\n"
    "ds_write_addtid_b32 v173 offset:5376\n"
    "v_subrev_f32_e32 v53, s4, v53\n"
    "v_mfma_f32_16x16x32_bf16 v[140:143], a[180:183], a[84:87], v[140:143]\n"
    "s_waitcnt lgkmcnt(10)\n"
    "ds_read2st64_b32 a[214:215], v249 offset0:10 offset1:11\n"
    "v_exp_f32_e32 v178, v178\n"
    "v_mfma_f32_16x16x32_bf16 v[140:143], a[184:187], a[88:91], v[140:143]\n"
    "s_andn2_b32 s61, s90, 0x18c\n"
    "ds_write_addtid_b32 v158 offset:3584\n"
    "v_exp_f32_e32 v179, v179\n"
    "v_mfma_f32_16x16x32_bf16 v[140:143], a[188:191], a[92:95], v[140:143]\n"
    "buffer_load_dword v188, v189, s[8:11], s81 offen offset:0 sc1\n"
    "ds_write_addtid_b32 v159 offset:3840\n"
    "v_mfma_f32_16x16x32_bf16 v[148:151], a[188:191], a[124:127], v[148:151]\n"
    "s_add_u32 s31, s46, s35\n"
    "ds_read2st64_b32 a[208:209], v252 offset0:8 offset1:9\n"
    "v_subrev_f32_e32 v74, s4, v74\n"
    "v_mfma_f32_16x16x32_bf16 v[148:151], a[184:187], a[120:123], v[148:151]\n"
    "s_add_u32 m0, s63, 6144\n"
    "ds_read2st64_b32 a[210:211], v252 offset0:10 offset1:11\n"
    "v_exp_f32_e32 v140, v140\n"
    "v_mfma_f32_16x16x32_bf16 v[180:183], a[184:187], a[152:155], v[180:183]\n"
    "buffer_load_dwordx4 v251, s[12:15], s31 offen offset:0 lds\n"
    "ds_read_b128 a[176:179], v242 offset:10240\n"
    "v_mfma_f32_16x16x32_bf16 v[180:183], a[188:191], a[156:159], v[180:183]\n"
    "s_waitcnt lgkmcnt(12)\n"
    "v_exp_f32_e32 v141, v141\n"
    "v_mfma_f32_16x16x32_bf16 v[184:187], a[160:163], a[128:131], v[236:239]\n"
    "s_mov_b32 m0, s63\n"
    "ds_read_b128 a[180:183], v243 offset:10240\n"
    "v_exp_f32_e32 v142, v142\n"
    "v_mfma_f32_16x16x32_bf16 v[184:187], a[164:167], a[132:135], v[184:187]\n"
    "ds_read_b128 a[184:187], v242 offset:14336\n"
    "v_exp_f32_e32 v143, v143\n"
    "v_mfma_f32_16x16x32_bf16 v[152:155], a[164:167], a[100:103], v[232:235]\n"
    "s_addk_i32 s31, 0x1000\n"
    "ds_write_addtid_b32 v166 offset:4608\n"
    "v_cvt_pk_bf16_f32 v140, v140, v141\n"
    "v_mfma_f32_16x16x32_bf16 v[152:155], a[160:163], a[96:99], v[152:155]\n"
    "s_add_u32 m0, s63, 7168\n"
    "v_exp_f32_e32 v148, v148\n"
    "v_mfma_f32_16x16x32_bf16 v[144:147], a[160:163], a[64:67], v[228:231]\n"
    "buffer_load_dwordx4 v251, s[12:15], s31 offen offset:0 lds\n"
    "v_mfma_f32_16x16x32_bf16 v[144:147], a[164:167], a[68:71], v[144:147]\n"
    "s_waitcnt lgkmcnt(11)\n"
    "s_waitcnt lgkmcnt(4)\n"
    "s_waitcnt vmcnt(3)\n"
    "v_mfma_f32_16x16x32_bf16 v[144:147], a[168:171], a[72:75], v[144:147]\n"
    "s_barrier\n"
    "v_mfma_f32_16x16x32_bf16 v[144:147], a[172:175], a[76:79], v[144:147]\n"
    "s_mov_b32 m0, s63\n"
    "ds_read_b128 a[188:191], v243 offset:14336\n"
    "v_cvt_pk_bf16_f32 v174, v176, v177\n"
    "v_mfma_f32_16x16x32_bf16 v[152:155], a[172:175], a[108:111], v[152:155]\n"
    "s_cmp_eq_u32 s93, 1\n"
    "s_cselect_b32 s31, s36, 2.0\n"
    "ds_write_addtid_b32 v140 offset:0\n"
    "v_mfma_f32_16x16x32_bf16 v[152:155], a[168:171], a[104:107], v[152:155]\n"
    "s_cmp_eq_u32 s93, 2\n"
    "s_cselect_b32 s31, s37, s31\n"
    "ds_read_b128 a[160:163], v240 offset:16384\n"
    "v_mfma_f32_16x16x32_bf16 v[184:187], a[168:171], a[136:139], v[184:187]\n"
    "buffer_load_dword v191, v190, s[12:15], s31 offen offset:0\n"
    "v_mfma_f32_16x16x32_bf16 v[184:187], a[172:175], a[140:143], v[184:187]\n"
    "s_waitcnt lgkmcnt(5)\n"
    "v_exp_f32_e32 v149, v149\n"
    "v_mfma_f32_16x16x32_bf16 v[184:187], a[176:179], a[144:147], v[184:187]\n"
    "s_cmp_eq_u32 s93, 0\n"
    "s_cselect_b32 s31, s35, s31\n"
    "ds_read_b128 a[164:167], v241 offset:16384\n"
    "v_cvt_pk_bf16_f32 v141, v142, v143\n"
    "v_mfma_f32_16x16x32_bf16 v[184:187], a[180:183], a[148:151], v[184:187]\n"
    "s_add_u32 s31, s31, 6103040\n"
    "v_exp_f32_e32 v150, v150\n"
    "v_mfma_f32_16x16x32_bf16 v[152:155], a[180:183], a[116:119], v[152:155]\n"
    "ds_read_b128 a[168:171], v240 offset:20480\n"
    "v_cvt_pk_bf16_f32 v175, v178, v179\n"
    "v_mfma_f32_16x16x32_bf16 v[152:155], a[176:179], a[112:115], v[152:155]\n"
    "v_exp_f32_e32 v151, v151\n"
    "v_mfma_f32_16x16x32_bf16 v[144:147], a[176:179], a[80:83], v[144:147]\n"
    "ds_write_addtid_b32 v167 offset:4864\n"
    "v_subrev_f32_e32 v75, s4, v75\n"
    "v_mfma_f32_16x16x32_bf16 v[144:147], a[180:183], a[84:87], v[144:147]\n"
    "s_waitcnt lgkmcnt(5)\n"
    "ds_read_b128 a[172:175], v241 offset:20480\n"
    "v_cvt_pk_bf16_f32 v148, v148, v149\n"
    "v_mfma_f32_16x16x32_bf16 v[144:147], a[184:187], a[88:91], v[144:147]\n"
    "ds_write_addtid_b32 v141 offset:256\n"
    "v_exp_f32_e32 v180, v180\n"
    "v_mfma_f32_16x16x32_bf16 v[144:147], a[188:191], a[92:95], v[144:147]\n"
    "ds_write_addtid_b32 v174 offset:5632\n"
    "v_exp_f32_e32 v181, v181\n"
    "v_mfma_f32_16x16x32_bf16 v[152:155], a[188:191], a[124:127], v[152:155]\n"
    "ds_write_addtid_b32 v148 offset:1024\n"
    "v_cvt_pk_bf16_f32 v149, v150, v151\n"
    "v_mfma_f32_16x16x32_bf16 v[152:155], a[184:187], a[120:123], v[152:155]\n"
    "v_exp_f32_e32 v182, v182\n"
    "v_mfma_f32_16x16x32_bf16 v[184:187], a[184:187], a[152:155], v[184:187]\n"
    "v_exp_f32_e32 v183, v183\n"
    "v_mfma_f32_16x16x32_bf16 v[184:187], a[188:191], a[156:159], v[184:187]\n"
    "s_waitcnt lgkmcnt(6)\n"
    "ds_read_b128 a[176:179], v242 offset:16384\n"
    "v_cvt_pk_bf16_f32 v180, v180, v181\n"
    "v_mfma_f32_16x16x32_bf16 v[212:215], a[160:163], a[128:131], v[236:239]\n"
    "ds_write_addtid_b32 v175 offset:5888\n"
    "v_exp_f32_e32 v144, v144\n"
    "v_mfma_f32_16x16x32_bf16 v[212:215], a[164:167], a[132:135], v[212:215]\n"
    "ds_read_b128 a[184:187], v242 offset:20480\n"
    "v_exp_f32_e32 v145, v145\n"
    "v_mfma_f32_16x16x32_bf16 v[204:207], a[164:167], a[100:103], v[232:235]\n"
    "ds_read_b128 a[180:183], v243 offset:16384\n"
    "v_cvt_pk_bf16_f32 v181, v182, v183\n"
    "v_mfma_f32_16x16x32_bf16 v[204:207], a[160:163], a[96:99], v[204:207]\n"
    "v_exp_f32_e32 v146, v146\n"
    "v_mfma_f32_16x16x32_bf16 v[196:199], a[160:163], a[64:67], v[228:231]\n"
    "v_exp_f32_e32 v147, v147\n"
    "v_mfma_f32_16x16x32_bf16 v[196:199], a[164:167], a[68:71], v[196:199]\n"
    "s_waitcnt lgkmcnt(7)\n"
    "ds_write_addtid_b32 v149 offset:1280\n"
    "v_subrev_f32_e32 v116, s4, v116\n"
    "v_mfma_f32_16x16x32_bf16 v[196:199], a[168:171], a[72:75], v[196:199]\n"
    "ds_write_addtid_b32 v180 offset:2048\n"
    "v_subrev_f32_e32 v35, s4, v35\n"
    "v_mfma_f32_16x16x32_bf16 v[196:199], a[172:175], a[76:79], v[196:199]\n"
    "s_waitcnt lgkmcnt(4)\n"
    "v_mfma_f32_16x16x32_bf16 v[204:207], a[172:175], a[108:111], v[204:207]\n"
    "s_barrier\n"
    "v_mfma_f32_16x16x32_bf16 v[204:207], a[168:171], a[104:107], v[204:207]\n"
    "ds_read_b128 a[188:191], v243 offset:20480\n"
    "v_cvt_pk_bf16_f32 v142, v144, v145\n"
    "v_mfma_f32_16x16x32_bf16 v[212:215], a[168:171], a[136:139], v[212:215]\n"
    "v_exp_f32_e32 v152, v152\n"
    "v_mfma_f32_16x16x32_bf16 v[212:215], a[172:175], a[140:143], v[212:215]\n"
    "s_waitcnt lgkmcnt(3)\n"
    "v_exp_f32_e32 v153, v153\n"
    "v_mfma_f32_16x16x32_bf16 v[212:215], a[176:179], a[144:147], v[212:215]\n"
    "ds_read_b128 a[168:171], v240 offset:22528\n"
    "v_cvt_pk_bf16_f32 v143, v146, v147\n"
    "v_mfma_f32_16x16x32_bf16 v[212:215], a[180:183], a[148:151], v[212:215]\n"
    "ds_read_b128 a[160:163], v240 offset:18432\n"
    "v_exp_f32_e32 v154, v154\n"
    "v_mfma_f32_16x16x32_bf16 v[204:207], a[180:183], a[116:119], v[204:207]\n"
    "ds_write_addtid_b32 v181 offset:2304\n"
    "v_exp_f32_e32 v155, v155\n"
    "v_mfma_f32_16x16x32_bf16 v[204:207], a[176:179], a[112:115], v[204:207]\n"
    "ds_read2st64_b32 v[160:161], v249 offset0:12 offset1:13\n"
    "v_subrev_f32_e32 v54, s4, v54\n"
    "v_mfma_f32_16x16x32_bf16 v[196:199], a[176:179], a[80:83], v[196:199]\n"
    "ds_read_b128 a[164:167], v241 offset:18432\n"
    "ds_read_b128 a[172:175], v241 offset:22528\n"
    "v_mfma_f32_16x16x32_bf16 v[196:199], a[180:183], a[84:87], v[196:199]\n"
    "s_waitcnt lgkmcnt(6)\n"
    "ds_write_addtid_b32 v142 offset:512\n"
    "v_cvt_pk_bf16_f32 v150, v152, v153\n"
    "v_mfma_f32_16x16x32_bf16 v[196:199], a[184:187], a[88:91], v[196:199]\n"
    "ds_write_addtid_b32 v143 offset:768\n"
    "v_exp_f32_e32 v184, v184\n"
    "v_mfma_f32_16x16x32_bf16 v[196:199], a[188:191], a[92:95], v[196:199]\n"
    "ds_read2st64_b32 v[162:163], v249 offset0:14 offset1:15\n"
    "v_exp_f32_e32 v185, v185\n"
    "v_mfma_f32_16x16x32_bf16 v[204:207], a[188:191], a[124:127], v[204:207]\n"
    "ds_read2st64_b32 v[168:169], v249 offset0:16 offset1:17\n"
    "v_cvt_pk_bf16_f32 v151, v154, v155\n"
    "v_mfma_f32_16x16x32_bf16 v[204:207], a[184:187], a[120:123], v[204:207]\n"
    "s_waitcnt vmcnt(3)\n"
    "v_cmp_ne_u32_e32 vcc, s39, v188\n"
    "v_exp_f32_e32 v186, v186\n"
    "v_mfma_f32_16x16x32_bf16 v[212:215], a[184:187], a[152:155], v[212:215]\n"
    "ds_read_b128 a[176:179], v242 offset:18432\n"
    "v_exp_f32_e32 v196, v196\n"
    "v_mfma_f32_16x16x32_bf16 v[212:215], a[188:191], a[156:159], v[212:215]\n"
    "s_waitcnt lgkmcnt(6)\n"
    "ds_read_b128 a[184:187], v242 offset:22528\n"
    "v_exp_f32_e32 v197, v197\n"
    "v_mfma_f32_16x16x32_bf16 v[216:219], a[160:163], a[128:131], v[236:239]\n"
    "s_cmp_eq_u64 vcc, 0\n"
    "s_cselect_b32 s31, s31, 2.0\n"
    "s_cselect_b32 s44, 0, 1\n"
    "v_mfma_f32_16x16x32_bf16 v[216:219], a[164:167], a[132:135], v[216:219]\n"
    "buffer_load_dword v191, v190, s[8:11], s31 offen offset:0 sc1\n"
    "ds_read_b128 a[180:183], v243 offset:18432\n"
    "v_mfma_f32_16x16x32_bf16 v[208:211], a[164:167], a[100:103], v[232:235]\n"
    "ds_write_addtid_b32 v150 offset:1536\n"
    "v_exp_f32_e32 v198, v198\n"
    "v_mfma_f32_16x16x32_bf16 v[208:211], a[160:163], a[96:99], v[208:211]\n"
    "ds_read2st64_b32 v[170:171], v249 offset0:18 offset1:19\n"
    "v_exp_f32_e32 v199, v199\n"
    "v_mfma_f32_16x16x32_bf16 v[200:203], a[160:163], a[64:67], v[228:231]\n"
    "ds_read_b128 a[188:191], v243 offset:22528\n"
    "v_exp_f32_e32 v187, v187\n"
    "v_mfma_f32_16x16x32_bf16 v[200:203], a[164:167], a[68:71], v[200:203]\n"
    "s_waitcnt lgkmcnt(10)\n"
    "ds_read_b64_tr_b16 a[160:161], v244 offset:24576\n"
    "v_cvt_pk_bf16_f32 v196, v196, v197\n"
    "v_subrev_f32_e32 v31, s4, v31\n"
    "v_mfma_f32_16x16x32_bf16 v[200:203], a[168:171], a[72:75], v[200:203]\n"
    "ds_read_b64_tr_b16 a[162:163], v244 offset:26624\n"
    "v_exp_f32_e32 v204, v204\n"
    "v_mfma_f32_16x16x32_bf16 v[200:203], a[172:175], a[76:79], v[200:203]\n"
    "v_exp_f32_e32 v205, v205\n"
    "v_mfma_f32_16x16x32_bf16 v[208:211], a[172:175], a[108:111], v[208:211]\n"
    "ds_write_addtid_b32 v151 offset:1792\n"
    "v_and_b32_e32 v248, 0xf0, v250\n"
    "v_lshrrev_b32_e32 v231, 3, v248\n"
    "v_mfma_f32_16x16x32_bf16 v[208:211], a[168:171], a[104:107], v[208:211]\n"
    "ds_read2st64_b32 v[176:177], v249 offset0:20 offset1:21\n"
    "v_cvt_pk_bf16_f32 v182, v184, v185\n"
    "v_cvt_pk_bf16_f32 v197, v198, v199\n"
    "v_mfma_f32_16x16x32_bf16 v[216:219], a[168:171], a[136:139], v[216:219]\n"
    "s_lshr_b32 s31, s35, 8\n"
    "ds_read2st64_b32 v[178:179], v249 offset0:22 offset1:23\n"
    "v_exp_f32_e32 v206, v206\n"
    "v_mfma_f32_16x16x32_bf16 v[216:219], a[172:175], a[140:143], v[216:219]\n"
    "s_waitcnt lgkmcnt(8)\n"
    "ds_write_addtid_b32 v182 offset:2560\n"
    "v_cvt_pk_bf16_f32 v183, v186, v187\n"
    "v_mfma_f32_16x16x32_bf16 v[216:219], a[176:179], a[144:147], v[216:219]\n"
    "buffer_load_ushort v228, v231, s[24:27], s31 offen\n"
    "ds_read_b64_tr_b16 a[164:165], v244 offset:57344\n"
    "v_mfma_f32_16x16x32_bf16 v[216:219], a[180:183], a[148:151], v[216:219]\n"
    "s_lshr_b32 s31, s36, 8\n"
    "ds_read_b64_tr_b16 a[166:167], v244 offset:59392\n"
    "v_exp_f32_e32 v207, v207\n"
    "v_mfma_f32_16x16x32_bf16 v[208:211], a[180:183], a[116:119], v[208:211]\n"
    "ds_read_b64_tr_b16 a[168:169], v245 offset:24576\n"
    "v_cvt_pk_bf16_f32 v204, v204, v205\n"
    "v_mfma_f32_16x16x32_bf16 v[208:211], a[176:179], a[112:115], v[208:211]\n"
    "ds_read_b64_tr_b16 a[170:171], v245 offset:26624\n"
    "v_exp_f32_e32 v212, v212\n"
    "v_mfma_f32_16x16x32_bf16 v[200:203], a[176:179], a[80:83], v[200:203]\n"
    "buffer_load_ushort v229, v231, s[24:27], s31 offen\n"
    "ds_read_b64_tr_b16 a[172:173], v245 offset:57344\n"
    "v_mfma_f32_16x16x32_bf16 v[200:203], a[180:183], a[84:87], v[200:203]\n"
    "s_waitcnt lgkmcnt(11)\n"
    "ds_write_addtid_b32 v183 offset:2816\n"
    "v_exp_f32_e32 v213, v213\n"
    "v_mfma_f32_16x16x32_bf16 v[200:203], a[184:187], a[88:91], v[200:203]\n"
    "s_lshr_b32 s31, s37, 8\n"
    "ds_read_b64_tr_b16 a[176:177], v246 offset:24576\n"
    "v_cvt_pk_bf16_f32 v205, v206, v207\n"
    "v_mfma_f32_16x16x32_bf16 v[200:203], a[188:191], a[92:95], v[200:203]\n"
    "ds_read_b64_tr_b16 a[178:179], v246 offset:26624\n"
    "v_exp_f32_e32 v214, v214\n"
    "v_mfma_f32_16x16x32_bf16 v[208:211], a[188:191], a[124:127], v[208:211]\n"
    "buffer_load_ushort v230, v231, s[24:27], s31 offen\n"
    "ds_read_b64_tr_b16 a[174:175], v245 offset:59392\n"
    "v_mfma_f32_16x16x32_bf16 v[208:211], a[184:187], a[120:123], v[208:211]\n"
    "s_waitcnt lgkmcnt(3)\n"
    "v_mfma_f32_16x16x32_bf16 v[216:219], a[184:187], a[152:155], v[216:219]\n"
    "s_barrier\n"
    "v_mfma_f32_16x16x32_bf16 v[216:219], a[188:191], a[156:159], v[216:219]\n"
    "ds_read_b64_tr_b16 a[184:185], v246 offset:0\n"
    "v_exp_f32_e32 v215, v215\n"
    "v_mfma_f32_16x16x32_bf16 a[252:255], a[160:163], a[208:211], a[252:255]\n"
    "s_add_u32 s62, s57, 4096\n"
    "ds_write_addtid_b32 v196 offset:3072\n"
    "v_cvt_pk_bf16_f32 v212, v212, v213\n"
    "v_mfma_f32_16x16x32_bf16 a[252:255], a[164:167], a[212:215], a[252:255]\n"
    "buffer_load_dwordx4 a[64:67], v250, s[8:11], s62 offen offset:2048 sc1\n"
    "ds_write_addtid_b32 v197 offset:3328\n"
    "v_mfma_f32_16x16x32_bf16 a[248:251], a[164:167], a[204:207], a[248:251]\n"
    "ds_read_b64_tr_b16 a[180:181], v246 offset:57344\n"
    "v_exp_f32_e32 v200, v200\n"
    "v_mfma_f32_16x16x32_bf16 a[248:251], a[160:163], a[200:203], a[248:251]\n"
    "ds_read_b64_tr_b16 a[186:187], v246 offset:2048\n"
    "v_exp_f32_e32 v201, v201\n"
    "v_mfma_f32_16x16x32_bf16 a[244:247], a[160:163], a[192:195], a[244:247]\n"
    "ds_read_b64_tr_b16 a[182:183], v246 offset:59392\n"
    "ds_read2st64_b32 v[144:145], v249 offset0:0 offset1:1\n"
    "v_mfma_f32_16x16x32_bf16 a[244:247], a[164:167], a[196:199], a[244:247]\n"
    "s_waitcnt lgkmcnt(7)\n"
    "ds_write_addtid_b32 v204 offset:4096\n"
    "v_lshrrev_b32_e32 v231, 2, v250\n"
    "v_mfma_f32_16x16x32_bf16 v[8:11], a[168:171], a[192:195], v[8:11]\n"
    "buffer_load_dwordx4 a[68:71], v250, s[8:11], s62 offen offset:3072 sc1\n"
    "ds_read_b64_tr_b16 a[160:161], v244 offset:0\n"
    "v_mfma_f32_16x16x32_bf16 v[8:11], a[172:175], a[196:199], v[8:11]\n"
    "s_add_u32 s62, s57, 8192\n"
    "ds_read_b64_tr_b16 a[188:189], v246 offset:32768\n"
    "ds_read_b64_tr_b16 a[162:163], v244 offset:2048\n"
    "v_cvt_pk_bf16_f32 v213, v214, v215\n"
    "v_mfma_f32_16x16x32_bf16 v[12:15], a[172:175], a[204:207], v[12:15]\n"
    "ds_read2st64_b32 v[146:147], v249 offset0:2 offset1:3\n"
    "v_exp_f32_e32 v202, v202\n"
    "v_mfma_f32_16x16x32_bf16 v[12:15], a[168:171], a[200:203], v[12:15]\n"
    "ds_write_addtid_b32 v205 offset:4352\n"
    "v_exp_f32_e32 v203, v203\n"
    "v_mfma_f32_16x16x32_bf16 v[16:19], a[168:171], a[208:211], v[16:19]\n"
    "ds_read_b64_tr_b16 a[190:191], v246 offset:34816\n"
    "v_cvt_pk_bf16_f32 v198, v200, v201\n"
    "v_mfma_f32_16x16x32_bf16 v[16:19], a[172:175], a[212:215], v[16:19]\n"
    "s_waitcnt lgkmcnt(8)\n"
    "ds_read_b64_tr_b16 a[164:165], v244 offset:32768\n"
    "v_exp_f32_e32 v208, v208\n"
    "v_mfma_f32_16x16x32_bf16 v[36:39], a[176:179], a[208:211], v[36:39]\n"
    "buffer_load_dwordx4 a[72:75], v250, s[8:11], s62 offen offset:0 sc1\n"
    "ds_read2st64_b32 v[152:153], v249 offset0:4 offset1:5\n"
    "v_mfma_f32_16x16x32_bf16 v[36:39], a[180:183], a[212:215], v[36:39]\n"
    "ds_read2st64_b32 v[154:155], v249 offset0:6 offset1:7\n"
    "v_exp_f32_e32 v209, v209\n"
    "v_mfma_f32_16x16x32_bf16 v[32:35], a[180:183], a[204:207], v[32:35]\n"
    "ds_read_b64_tr_b16 a[166:167], v244 offset:34816\n"
    "v_cvt_pk_bf16_f32 v199, v202, v203\n"
    "v_mfma_f32_16x16x32_bf16 v[32:35], a[176:179], a[200:203], v[32:35]\n"
    "ds_write_addtid_b32 v212 offset:5120\n"
    "v_exp_f32_e32 v210, v210\n"
    "v_mfma_f32_16x16x32_bf16 v[28:31], a[176:179], a[192:195], v[28:31]\n"
    "ds_read_b64_tr_b16 a[168:169], v245 offset:0\n"
    "v_subrev_u32_e32 v231, 0x80, v231\n"
    "v_mfma_f32_16x16x32_bf16 v[28:31], a[180:183], a[196:199], v[28:31]\n"
    "s_waitcnt lgkmcnt(6)\n"
    "ds_read_b64_tr_b16 a[170:171], v245 offset:2048\n"
    "v_min_u32_e32 v231, 2.0, v231\n"
    "v_lshrrev_b32_e32 v252, 4, v250\n"
    "v_mfma_f32_16x16x32_bf16 v[28:31], a[184:187], v[156:159], v[28:31]\n"
    "buffer_load_dwordx4 a[76:79], v250, s[8:11], s62 offen offset:1024 sc1\n"
    "ds_read2st64_b32 v[184:185], v249 offset0:8 offset1:9\n"
    "v_mfma_f32_16x16x32_bf16 v[28:31], a[188:191], v[160:163], v[28:31]\n"
    "ds_read2st64_b32 v[186:187], v249 offset0:10 offset1:11\n"
    "v_exp_f32_e32 v211, v211\n"
    "v_mfma_f32_16x16x32_bf16 v[32:35], a[188:191], v[168:171], v[32:35]\n"
    "ds_read_b64_tr_b16 a[176:177], v245 offset:8192\n"
    "v_cvt_pk_bf16_f32 v206, v208, v209\n"
    "v_mfma_f32_16x16x32_bf16 v[32:35], a[184:187], v[164:167], v[32:35]\n"
    "ds_read_b64_tr_b16 a[172:173], v245 offset:32768\n"
    "v_exp_f32_e32 v216, v216\n"
    "v_mfma_f32_16x16x32_bf16 v[36:39], a[184:187], v[172:175], v[36:39]\n"
    "ds_read_b64_tr_b16 a[174:175], v245 offset:34816\n"
    "v_lshlrev_b32_e32 v239, 5, v248\n"
    "v_mfma_f32_16x16x32_bf16 v[36:39], a[188:191], v[176:179], v[36:39]\n"
    "s_waitcnt lgkmcnt(8)\n"
    "ds_read_b64_tr_b16 a[178:179], v245 offset:10240\n"
    "v_exp_f32_e32 v217, v217\n"
    "v_mfma_f32_16x16x32_bf16 a[252:255], a[160:163], v[172:175], a[252:255]\n"
    "buffer_load_dwordx4 a[80:83], v250, s[8:11], s62 offen offset:2048 sc1\n"
    "ds_read_b64_tr_b16 a[180:181], v245 offset:40960\n"
    "v_mfma_f32_16x16x32_bf16 a[252:255], a[164:167], v[176:179], a[252:255]\n"
    "ds_read_b64_tr_b16 a[182:183], v245 offset:43008\n"
    "ds_read_b64_tr_b16 a[184:185], v244 offset:8192\n"
    "ds_read_b64_tr_b16 a[186:187], v244 offset:10240\n"
    "v_mfma_f32_16x16x32_bf16 a[248:251], a[164:167], v[168:171], a[248:251]\n"
    "ds_write_addtid_b32 v213 offset:5376\n"
    "v_cvt_pk_bf16_f32 v207, v210, v211\n"
    "v_mfma_f32_16x16x32_bf16 a[248:251], a[160:163], v[164:167], a[248:251]\n"
    "ds_read_b64_tr_b16 a[188:189], v244 offset:40960\n"
    "ds_write_addtid_b32 v198 offset:3584\n"
    "v_mfma_f32_16x16x32_bf16 a[244:247], a[160:163], v[156:159], a[244:247]\n"
    "ds_write_addtid_b32 v199 offset:3840\n"
    "v_exp_f32_e32 v218, v218\n"
    "v_mfma_f32_16x16x32_bf16 a[244:247], a[164:167], v[160:163], a[244:247]\n"
    "s_waitcnt lgkmcnt(9)\n"
    "ds_write_addtid_b32 v206 offset:4608\n"
    "v_exp_f32_e32 v219, v219\n"
    "v_mfma_f32_16x16x32_bf16 v[8:11], a[168:171], v[156:159], v[8:11]\n"
    "buffer_load_dwordx4 a[84:87], v250, s[8:11], s62 offen offset:3072 sc1\n"
    "ds_read_b64_tr_b16 a[160:161], v244 offset:16384\n"
    "v_mfma_f32_16x16x32_bf16 v[8:11], a[172:175], v[160:163], v[8:11]\n"
    "s_add_u32 s62, s57, 12288\n"
    "ds_write_addtid_b32 v207 offset:4864\n"
    "v_cvt_pk_bf16_f32 v214, v216, v217\n"
    "v_cvt_pk_bf16_f32 v215, v218, v219\n"
    "v_mfma_f32_16x16x32_bf16 v[12:15], a[172:175], v[168:171], v[12:15]\n"
    "ds_write_addtid_b32 v214 offset:5632\n"
    "ds_write_addtid_b32 v215 offset:5888\n"
    "v_mfma_f32_16x16x32_bf16 v[12:15], a[168:171], v[164:167], v[12:15]\n"
    "ds_read_b64_tr_b16 a[190:191], v244 offset:43008\n"
    "v_and_or_b32 v239, v252, 48, v239\n"
    "v_lshrrev_b32_e32 v248, 2, v248\n"
    "v_mfma_f32_16x16x32_bf16 v[16:19], a[168:171], v[172:175], v[16:19]\n"
    "ds_read_b64_tr_b16 a[162:163], v244 offset:18432\n"
    "ds_read_b64_tr_b16 a[164:165], v244 offset:49152\n"
    "v_mfma_f32_16x16x32_bf16 v[16:19], a[172:175], v[176:179], v[16:19]\n"
    "s_waitcnt lgkmcnt(14)\n"
    "s_waitcnt lgkmcnt(3)\n"
    "v_mfma_f32_16x16x32_bf16 v[16:19], a[176:179], v[180:183], v[16:19]\n"
    "s_barrier\n"
    "v_mfma_f32_16x16x32_bf16 v[16:19], a[180:183], v[184:187], v[16:19]\n"
    "buffer_load_dwordx4 a[88:91], v250, s[8:11], s62 offen offset:0 sc1\n"
    "ds_read2st64_b32 v[200:201], v249 offset0:12 offset1:13\n"
    "v_mfma_f32_16x16x32_bf16 v[12:15], a[180:183], v[152:155], v[12:15]\n"
    "ds_read_b64_tr_b16 a[166:167], v244 offset:51200\n"
    "ds_read2st64_b32 v[202:203], v249 offset0:14 offset1:15\n"
    "v_mfma_f32_16x16x32_bf16 v[12:15], a[176:179], v[148:151], v[12:15]\n"
    "ds_read2st64_b32 v[208:209], v249 offset0:16 offset1:17\n"
    "v_lshrrev_b32_e32 v253, 4, v252\n"
    "v_mfma_f32_16x16x32_bf16 v[8:11], a[176:179], v[140:143], v[8:11]\n"
    "ds_read2st64_b32 v[210:211], v249 offset0:18 offset1:19\n"
    "v_lshl_add_u32 v248, v253, 7, v248\n"
    "v_mfma_f32_16x16x32_bf16 v[8:11], a[180:183], v[144:147], v[8:11]\n"
    "s_waitcnt lgkmcnt(7)\n"
    "ds_read2st64_b32 v[216:217], v249 offset0:20 offset1:21\n"
    "ds_read2st64_b32 v[218:219], v249 offset0:22 offset1:23\n"
    "v_bfe_u32 v255, v252, 2, 2\n"
    "v_mfma_f32_16x16x32_bf16 a[244:247], a[184:187], v[140:143], a[244:247]\n"
    "buffer_load_dwordx4 a[92:95], v250, s[8:11], s62 offen offset:1024 sc1\n"
    "v_mfma_f32_16x16x32_bf16 a[244:247], a[188:191], v[144:147], a[244:247]\n"
    "ds_read_b64_tr_b16 a[168:169], v245 offset:16384\n"
    "v_subrev_f32_e32 v120, s4, v120\n"
    "v_mfma_f32_16x16x32_bf16 a[248:251], a[188:191], v[152:155], a[248:251]\n"
    "ds_read_b64_tr_b16 a[170:171], v245 offset:18432\n"
    "v_subrev_f32_e32 v58, s4, v58\n"
    "v_mfma_f32_16x16x32_bf16 a[248:251], a[184:187], v[148:151], a[248:251]\n"
    "ds_read_b64_tr_b16 a[172:173], v245 offset:49152\n"
    "v_max_u32_e32 v238, 1, v255\n"
    "v_mfma_f32_16x16x32_bf16 a[252:255], a[184:187], v[180:183], a[252:255]\n"
    "ds_read_b64_tr_b16 a[174:175], v245 offset:51200\n"
    "v_add_u32_e32 v238, s32, v238\n"
    "v_mfma_f32_16x16x32_bf16 a[252:255], a[188:191], v[184:187], a[252:255]\n"
    "s_waitcnt lgkmcnt(4)\n"
    "ds_read_b64_tr_b16 a[176:177], v246 offset:8192\n"
    "v_and_b32_e32 v238, 3, v238\n"
    "v_mfma_f32_16x16x32_bf16 a[252:255], a[160:163], v[212:215], a[252:255]\n"
    "buffer_load_dwordx4 a[96:99], v250, s[8:11], s62 offen offset:2048 sc1\n"
    "ds_read_b64_tr_b16 a[184:185], v246 offset:16384\n"
    "v_mfma_f32_16x16x32_bf16 a[252:255], a[164:167], v[216:219], a[252:255]\n"
    "ds_read_b64_tr_b16 a[178:179], v246 offset:10240\n"
    "v_subrev_f32_e32 v97, s4, v97\n"
    "v_mfma_f32_16x16x32_bf16 a[248:251], a[164:167], v[208:211], a[248:251]\n"
    "ds_read_b64_tr_b16 a[180:181], v246 offset:40960\n"
    "v_lshlrev_b32_e32 v238, 10, v238\n"
    "v_mfma_f32_16x16x32_bf16 a[248:251], a[160:163], v[204:207], a[248:251]\n"
    "ds_read_b64_tr_b16 a[186:187], v246 offset:18432\n"
    "v_and_b32_e32 v252, 3, v252\n"
    "v_mfma_f32_16x16x32_bf16 a[244:247], a[160:163], v[196:199], a[244:247]\n"
    "ds_read_b64_tr_b16 a[182:183], v246 offset:43008\n"
    "v_subrev_f32_e32 v128, s4, v128\n"
    "v_mfma_f32_16x16x32_bf16 a[244:247], a[164:167], v[200:203], a[244:247]\n"
    "s_waitcnt lgkmcnt(6)\n"
    "ds_read_b64_tr_b16 a[188:189], v246 offset:49152\n"
    "v_xor_b32_e32 v253, v253, v255\n"
    "v_mfma_f32_16x16x32_bf16 v[8:11], a[168:171], v[196:199], v[8:11]\n"
    "buffer_load_dwordx4 a[100:103], v250, s[8:11], s62 offen offset:3072 sc1\n"
    "ds_read_b64_tr_b16 a[160:161], v247 offset:24576\n"
    "v_mfma_f32_16x16x32_bf16 v[8:11], a[172:175], v[200:203], v[8:11]\n"
    "s_add_u32 s62, s57, 16384\n"
    "ds_read_b64_tr_b16 a[190:191], v246 offset:51200\n"
    "v_subrev_f32_e32 v55, s4, v55\n"
    "v_mfma_f32_16x16x32_bf16 v[12:15], a[172:175], v[208:211], v[12:15]\n"
    "ds_read_b64_tr_b16 a[162:163], v247 offset:26624\n"
    "v_subrev_f32_e32 v78, s4, v78\n"
    "v_mfma_f32_16x16x32_bf16 v[12:15], a[168:171], v[204:207], v[12:15]\n"
    "v_cvt_pk_bf16_f32 v11, v10, v11\n"
    "v_cvt_pk_bf16_f32 v10, v8, v9\n"
    "v_mfma_f32_16x16x32_bf16 v[16:19], a[168:171], v[212:215], v[16:19]\n"
    "ds_read_b64_tr_b16 a[164:165], v247 offset:57344\n"
    "v_subrev_f32_e32 v50, s4, v50\n"
    "v_mfma_f32_16x16x32_bf16 v[16:19], a[172:175], v[216:219], v[16:19]\n"
    "s_waitcnt lgkmcnt(5)\n"
    "v_accvgpr_read_b32 v8, a244\n"
    "v_accvgpr_read_b32 v9, a245\n"
    "v_mfma_f32_16x16x32_bf16 v[36:39], a[176:179], v[180:183], v[36:39]\n"
    "v_accvgpr_read_b32 v255, a247\n"
    "v_cvt_pk_bf16_f32 v8, v8, v9\n"
    "v_mfma_f32_16x16x32_bf16 v[36:39], a[180:183], v[184:187], v[36:39]\n"
    "v_accvgpr_read_b32 v9, a246\n"
    "v_cvt_pk_bf16_f32 v9, v9, v255\n"
    "v_mfma_f32_16x16x32_bf16 v[32:35], a[180:183], v[152:155], v[32:35]\n"
    "buffer_store_dwordx4 v[8:11], v250, s[8:11], s82 offen offset:0 sc1\n"
    "v_mfma_f32_16x16x32_bf16 v[32:35], a[176:179], v[148:151], v[32:35]\n"
    "ds_read_b64_tr_b16 a[166:167], v247 offset:59392\n"
    "v_cvt_pk_bf16_f32 v15, v14, v15\n"
    "v_mfma_f32_16x16x32_bf16 v[28:31], a[176:179], v[140:143], v[28:31]\n"
    "v_cvt_pk_bf16_f32 v14, v12, v13\n"
    "v_accvgpr_read_b32 v12, a248\n"
    "v_mfma_f32_16x16x32_bf16 v[28:31], a[180:183], v[144:147], v[28:31]\n"
    "s_waitcnt lgkmcnt(3)\n"
    "ds_read_b64_tr_b16 a[168:169], v247 offset:0\n"
    "v_accvgpr_read_b32 v13, a249\n"
    "v_mfma_f32_16x16x32_bf16 v[28:31], a[184:187], v[196:199], v[28:31]\n"
    "buffer_load_dwordx4 a[104:107], v250, s[8:11], s62 offen offset:0 sc1\n"
    "v_mfma_f32_16x16x32_bf16 v[28:31], a[188:191], v[200:203], v[28:31]\n"
    "ds_read_b64_tr_b16 a[170:171], v247 offset:2048\n"
    "v_accvgpr_read_b32 v255, a251\n"
    "v_cvt_pk_bf16_f32 v12, v12, v13\n"
    "v_mfma_f32_16x16x32_bf16 v[32:35], a[188:191], v[208:211], v[32:35]\n"
    "ds_read_b64_tr_b16 a[172:173], v247 offset:32768\n"
    "v_subrev_f32_e32 v59, s4, v59\n"
    "v_mfma_f32_16x16x32_bf16 v[32:35], a[184:187], v[204:207], v[32:35]\n"
    "ds_read_b64_tr_b16 a[176:177], v247 offset:8192\n"
    "v_subrev_f32_e32 v51, s4, v51\n"
    "v_mfma_f32_16x16x32_bf16 v[36:39], a[184:187], v[212:215], v[36:39]\n"
    "ds_read_b64_tr_b16 a[174:175], v247 offset:34816\n"
    "v_accvgpr_read_b32 v13, a250\n"
    "v_mfma_f32_16x16x32_bf16 v[36:39], a[188:191], v[216:219], v[36:39]\n"
    "s_waitcnt lgkmcnt(5)\n"
    "ds_read_b64_tr_b16 a[178:179], v247 offset:10240\n"
    "v_cvt_pk_bf16_f32 v13, v13, v255\n"
    "v_mfma_f32_16x16x32_bf16 v[56:59], a[160:163], a[208:211], v[56:59]\n"
    "buffer_load_dwordx4 a[108:111], v250, s[8:11], s62 offen offset:1024 sc1\n"
    "v_mfma_f32_16x16x32_bf16 v[56:59], a[164:167], a[212:215], v[56:59]\n"
    "ds_read_b64_tr_b16 a[184:185], v247 offset:16384\n"
    "v_lshl_or_b32 v252, v253, 2, v252\n"
    "v_mfma_f32_16x16x32_bf16 v[52:55], a[164:167], a[204:207], v[52:55]\n"
    "ds_read_b64_tr_b16 a[180:181], v247 offset:40960\n"
    "v_cvt_pk_bf16_f32 v28, v28, v29\n"
    "v_mfma_f32_16x16x32_bf16 v[52:55], a[160:163], a[200:203], v[52:55]\n"
    "buffer_store_dwordx4 v[12:15], v250, s[8:11], s28 offen offset:0 sc1\n"
    "v_mfma_f32_16x16x32_bf16 v[48:51], a[160:163], a[192:195], v[48:51]\n"
    "s_add_u32 s62, s57, 16384\n"
    "ds_read_b64_tr_b16 a[182:183], v247 offset:43008\n"
    "v_cvt_pk_bf16_f32 v19, v18, v19\n"
    "v_mfma_f32_16x16x32_bf16 v[48:51], a[164:167], a[196:199], v[48:51]\n"
    "s_waitcnt lgkmcnt(4)\n"
    "ds_read_b64_tr_b16 a[186:187], v247 offset:18432\n"
    "v_cvt_pk_bf16_f32 v18, v16, v17\n"
    "v_mfma_f32_16x16x32_bf16 v[48:51], a[168:171], v[156:159], v[48:51]\n"
    "buffer_load_dwordx4 a[112:115], v250, s[8:11], s62 offen offset:2048 sc1\n"
    "v_mfma_f32_16x16x32_bf16 v[48:51], a[172:175], v[160:163], v[48:51]\n"
    "ds_read_b64_tr_b16 a[160:161], v244 offset:28672\n"
    "v_accvgpr_read_b32 v16, a252\n"
    "v_mfma_f32_16x16x32_bf16 v[52:55], a[172:175], v[168:171], v[52:55]\n"
    "ds_read_b64_tr_b16 a[188:189], v247 offset:49152\n"
    "v_subrev_f32_e32 v79, s4, v79\n"
    "v_mfma_f32_16x16x32_bf16 v[52:55], a[168:171], v[164:167], v[52:55]\n"
    "v_accvgpr_read_b32 v17, a253\n"
    "v_accvgpr_read_b32 v255, a255\n"
    "v_mfma_f32_16x16x32_bf16 v[56:59], a[168:171], v[172:175], v[56:59]\n"
    "ds_read_b64_tr_b16 a[190:191], v247 offset:51200\n"
    "v_cvt_pk_bf16_f32 v16, v16, v17\n"
    "v_mfma_f32_16x16x32_bf16 v[56:59], a[172:175], v[176:179], v[56:59]\n"
    "s_waitcnt lgkmcnt(4)\n"
    "ds_read_b64_tr_b16 a[162:163], v244 offset:30720\n"
    "v_subrev_f32_e32 v98, s4, v98\n"
    "v_mfma_f32_16x16x32_bf16 v[56:59], a[176:179], v[180:183], v[56:59]\n"
    ".long 0xbedf00ff\n"
    ".Lat_w11:\n"
    "s_setpc_b64 s[2:3]\n"
    "v_accvgpr_read_b32 v17, a254\n"
    "v_cvt_pk_bf16_f32 v17, v17, v255\n"
    "v_mfma_f32_16x16x32_bf16 v[56:59], a[180:183], v[184:187], v[56:59]\n"
    "ds_read_b64_tr_b16 a[164:165], v244 offset:61440\n"
    "v_subrev_f32_e32 v80, s4, v80\n"
    "v_mfma_f32_16x16x32_bf16 v[52:55], a[180:183], v[152:155], v[52:55]\n"
    "ds_read_b64_tr_b16 a[168:169], v244 offset:4096\n"
    "v_subrev_f32_e32 v71, s4, v71\n"
    "v_mfma_f32_16x16x32_bf16 v[52:55], a[176:179], v[148:151], v[52:55]\n"
    "ds_read_b64_tr_b16 a[166:167], v244 offset:63488\n"
    "v_subrev_f32_e32 v133, s4, v133\n"
    "v_mfma_f32_16x16x32_bf16 v[48:51], a[176:179], v[140:143], v[48:51]\n"
    "buffer_store_dwordx4 v[16:19], v250, s[8:11], s29 offen offset:0 sc1\n"
    "v_mfma_f32_16x16x32_bf16 v[48:51], a[180:183], v[144:147], v[48:51]\n"
    "s_waitcnt lgkmcnt(4)\n"
    "ds_read_b64_tr_b16 a[170:171], v244 offset:6144\n"
    "v_cvt_pk_bf16_f32 v29, v30, v31\n"
    "v_mfma_f32_16x16x32_bf16 v[48:51], a[184:187], v[196:199], v[48:51]\n"
    "ds_read_b64_tr_b16 a[176:177], v244 offset:12288\n"
    "v_cvt_pk_bf16_f32 v32, v32, v33\n"
    "v_mfma_f32_16x16x32_bf16 v[48:51], a[188:191], v[200:203], v[48:51]\n"
    "buffer_load_dwordx4 a[116:119], v250, s[8:11], s62 offen offset:3072 sc1\n"
    "v_mfma_f32_16x16x32_bf16 v[52:55], a[188:191], v[208:211], v[52:55]\n"
    "s_add_u32 s62, s57, 20480\n"
    "ds_read_b64_tr_b16 a[178:179], v244 offset:14336\n"
    "v_subrev_f32_e32 v113, s4, v113\n"
    "v_mfma_f32_16x16x32_bf16 v[52:55], a[184:187], v[204:207], v[52:55]\n"
    "ds_read_b64_tr_b16 a[172:173], v244 offset:36864\n"
    "v_cvt_pk_bf16_f32 v33, v34, v35\n"
    "v_mfma_f32_16x16x32_bf16 v[56:59], a[184:187], v[212:215], v[56:59]\n"
    "ds_read_b64_tr_b16 a[174:175], v244 offset:38912\n"
    "v_subrev_f32_e32 v91, s4, v91\n"
    "v_mfma_f32_16x16x32_bf16 v[56:59], a[188:191], v[216:219], v[56:59]\n"
    "s_waitcnt lgkmcnt(5)\n"
    "ds_read_b64_tr_b16 a[180:181], v244 offset:45056\n"
    "v_cvt_pk_bf16_f32 v36, v36, v37\n"
    "v_mfma_f32_16x16x32_bf16 v[76:79], a[160:163], a[208:211], v[76:79]\n"
    "ds_read_b64_tr_b16 a[184:185], v244 offset:20480\n"
    "v_cvt_pk_bf16_f32 v37, v38, v39\n"
    "v_mfma_f32_16x16x32_bf16 v[76:79], a[164:167], a[212:215], v[76:79]\n"
    "ds_read_b64_tr_b16 a[182:183], v244 offset:47104\n"
    "ds_read_b64_tr_b16 a[186:187], v244 offset:22528\n"
    "v_mfma_f32_16x16x32_bf16 v[72:75], a[164:167], a[204:207], v[72:75]\n"
    "v_cvt_pk_bf16_f32 v30, v48, v49\n"
    "v_cvt_pk_bf16_f32 v31, v50, v51\n"
    "v_mfma_f32_16x16x32_bf16 v[72:75], a[160:163], a[200:203], v[72:75]\n"
    "buffer_load_dwordx4 a[120:123], v250, s[8:11], s62 offen offset:0 sc1\n"
    "v_mfma_f32_16x16x32_bf16 v[68:71], a[160:163], a[192:195], v[68:71]\n"
    "ds_read_b64_tr_b16 a[188:189], v244 offset:53248\n"
    "v_subrev_f32_e32 v99, s4, v99\n"
    "v_mfma_f32_16x16x32_bf16 v[68:71], a[164:167], a[196:199], v[68:71]\n"
    "s_waitcnt lgkmcnt(5)\n"
    "v_cvt_pk_bf16_f32 v34, v52, v53\n"
    "v_subrev_f32_e32 v92, s4, v92\n"
    "v_mfma_f32_16x16x32_bf16 v[68:71], a[168:171], v[156:159], v[68:71]\n"
    "buffer_store_dwordx4 v[28:31], v250, s[8:11], s82 offen offset:1024 sc1\n"
    "v_mfma_f32_16x16x32_bf16 v[68:71], a[172:175], v[160:163], v[68:71]\n"
    "ds_read_b64_tr_b16 a[190:191], v244 offset:55296\n"
    "v_subrev_f32_e32 v81, s4, v81\n"
    "v_mfma_f32_16x16x32_bf16 v[72:75], a[172:175], v[168:171], v[72:75]\n"
    "v_subrev_f32_e32 v117, s4, v117\n"
    "v_subrev_f32_e32 v118, s4, v118\n"
    "v_mfma_f32_16x16x32_bf16 v[72:75], a[168:171], v[164:167], v[72:75]\n"
    "ds_read_b64_tr_b16 a[160:161], v245 offset:28672\n"
    "v_subrev_f32_e32 v93, s4, v93\n"
    "v_mfma_f32_16x16x32_bf16 v[76:79], a[168:171], v[172:175], v[76:79]\n"
    "ds_read_b64_tr_b16 a[162:163], v245 offset:30720\n"
    "v_subrev_f32_e32 v114, s4, v114\n"
    "v_mfma_f32_16x16x32_bf16 v[76:79], a[172:175], v[176:179], v[76:79]\n"
    "s_waitcnt lgkmcnt(5)\n"
    "v_subrev_f32_e32 v94, s4, v94\n"
    "v_subrev_f32_e32 v119, s4, v119\n"
    "v_mfma_f32_16x16x32_bf16 v[76:79], a[176:179], v[180:183], v[76:79]\n"
    "buffer_load_dwordx4 a[124:127], v250, s[8:11], s62 offen offset:1024 sc1\n"
    "v_mfma_f32_16x16x32_bf16 v[76:79], a[180:183], v[184:187], v[76:79]\n"
    "ds_read_b64_tr_b16 a[168:169], v245 offset:4096\n"
    "v_subrev_f32_e32 v104, s4, v104\n"
    "v_mfma_f32_16x16x32_bf16 v[72:75], a[180:183], v[152:155], v[72:75]\n"
    "ds_read_b64_tr_b16 a[170:171], v245 offset:6144\n"
    "v_cvt_pk_bf16_f32 v35, v54, v55\n"
    "v_mfma_f32_16x16x32_bf16 v[72:75], a[176:179], v[148:151], v[72:75]\n"
    "ds_read_b64_tr_b16 a[164:165], v245 offset:61440\n"
    "v_mov_b32_e32 v253, 0x3f80\n"
    "v_mfma_f32_16x16x32_bf16 v[68:71], a[176:179], v[140:143], v[68:71]\n"
    "ds_read_b64_tr_b16 a[166:167], v245 offset:63488\n"
    "v_subrev_f32_e32 v82, s4, v82\n"
    "v_mfma_f32_16x16x32_bf16 v[68:71], a[180:183], v[144:147], v[68:71]\n"
    "s_waitcnt lgkmcnt(6)\n"
    "ds_read_b64_tr_b16 a[172:173], v245 offset:36864\n"
    "v_subrev_f32_e32 v95, s4, v95\n"
    "v_mfma_f32_16x16x32_bf16 v[68:71], a[184:187], v[196:199], v[68:71]\n"
    "ds_read_b64_tr_b16 a[176:177], v245 offset:12288\n"
    "v_mov_b64_e32 v[232:233], 0\n"
    "v_mfma_f32_16x16x32_bf16 v[68:71], a[188:191], v[200:203], v[68:71]\n"
    "buffer_store_dwordx4 v[32:35], v250, s[8:11], s28 offen offset:1024 sc1\n"
    "v_mfma_f32_16x16x32_bf16 v[72:75], a[188:191], v[208:211], v[72:75]\n"
    "ds_read_b64_tr_b16 a[178:179], v245 offset:14336\n"
    "v_subrev_f32_e32 v129, s4, v129\n"
    "v_mfma_f32_16x16x32_bf16 v[72:75], a[184:187], v[204:207], v[72:75]\n"
    "ds_read_b64_tr_b16 a[174:175], v245 offset:38912\n"
    "v_cvt_pk_bf16_f32 v38, v56, v57\n"
    "v_mfma_f32_16x16x32_bf16 v[76:79], a[184:187], v[212:215], v[76:79]\n"
    "buffer_load_dwordx4 a[128:131], v250, s[8:11], s62 offen offset:2048 sc1\n"
    "v_mfma_f32_16x16x32_bf16 v[76:79], a[188:191], v[216:219], v[76:79]\n"
    "s_waitcnt lgkmcnt(4)\n"
    "ds_read_b64_tr_b16 a[180:181], v245 offset:45056\n"
    "v_subrev_f32_e32 v110, s4, v110\n"
    "v_mfma_f32_16x16x32_bf16 v[96:99], a[160:163], a[208:211], v[96:99]\n"
    "v_mov_b64_e32 v[236:237], 0\n"
    "v_subrev_f32_e32 v1, s4, v1\n"
    "v_mfma_f32_16x16x32_bf16 v[96:99], a[164:167], a[212:215], v[96:99]\n"
    "ds_read_b64_tr_b16 a[184:185], v245 offset:20480\n"
    "v_cvt_pk_bf16_f32 v39, v58, v59\n"
    "v_mfma_f32_16x16x32_bf16 v[92:95], a[164:167], a[204:207], v[92:95]\n"
    "ds_read_b64_tr_b16 a[186:187], v245 offset:22528\n"
    "v_subrev_f32_e32 v24, s4, v24\n"
    "v_mfma_f32_16x16x32_bf16 v[92:95], a[160:163], a[200:203], v[92:95]\n"
    "ds_read_b64_tr_b16 a[182:183], v245 offset:47104\n"
    "v_cmp_eq_u32_e32 vcc, 0, v252\n"
    "v_mfma_f32_16x16x32_bf16 v[88:91], a[160:163], a[192:195], v[88:91]\n"
    "ds_read_b64_tr_b16 a[188:189], v245 offset:53248\n"
    "v_cndmask_b32_e32 v234, 0, v253, vcc\n"
    "v_mfma_f32_16x16x32_bf16 v[88:91], a[164:167], a[196:199], v[88:91]\n"
    "s_waitcnt lgkmcnt(5)\n"
    "v_cvt_pk_bf16_f32 v72, v72, v73\n"
    "v_subrev_f32_e32 v45, s4, v45\n"
    "v_mfma_f32_16x16x32_bf16 v[88:91], a[168:171], v[156:159], v[88:91]\n"
    "ds_read_b64_tr_b16 a[160:161], v246 offset:28672\n"
    "v_mfma_f32_16x16x32_bf16 v[88:91], a[172:175], v[160:163], v[88:91]\n"
    "v_subrev_f32_e32 v136, s4, v136\n"
    "v_subrev_f32_e32 v121, s4, v121\n"
    "v_mfma_f32_16x16x32_bf16 v[92:95], a[172:175], v[168:171], v[92:95]\n"
    "buffer_store_dwordx4 v[36:39], v250, s[8:11], s29 offen offset:1024 sc1\n"
    "v_mfma_f32_16x16x32_bf16 v[92:95], a[168:171], v[164:167], v[92:95]\n"
    "ds_read_b64_tr_b16 a[162:163], v246 offset:30720\n"
    "v_subrev_f32_e32 v130, s4, v130\n"
    "v_mfma_f32_16x16x32_bf16 v[96:99], a[168:171], v[172:175], v[96:99]\n"
    "ds_read_b64_tr_b16 a[190:191], v245 offset:55296\n"
    "v_subrev_f32_e32 v122, s4, v122\n"
    "v_mfma_f32_16x16x32_bf16 v[96:99], a[172:175], v[176:179], v[96:99]\n"
    "s_waitcnt lgkmcnt(4)\n"
    "ds_read_b64_tr_b16 a[168:169], v246 offset:4096\n"
    "v_subrev_f32_e32 v137, s4, v137\n"
    "v_mfma_f32_16x16x32_bf16 v[96:99], a[176:179], v[180:183], v[96:99]\n"
    "buffer_load_dwordx4 a[132:135], v250, s[8:11], s62 offen offset:3072 sc1\n"
    "v_mfma_f32_16x16x32_bf16 v[96:99], a[180:183], v[184:187], v[96:99]\n"
    "s_add_u32 s62, s57, 24576\n"
    "ds_read_b64_tr_b16 a[170:171], v246 offset:6144\n"
    "v_subrev_f32_e32 v138, s4, v138\n"
    "v_mfma_f32_16x16x32_bf16 v[92:95], a[180:183], v[152:155], v[92:95]\n"
    "ds_read_b64_tr_b16 a[164:165], v246 offset:61440\n"
    "v_subrev_f32_e32 v84, s4, v84\n"
    "v_mfma_f32_16x16x32_bf16 v[92:95], a[176:179], v[148:151], v[92:95]\n"
    "v_cmp_eq_u32_e32 vcc, 1, v252\n"
    "v_cvt_pk_bf16_f32 v73, v74, v75\n"
    "v_mfma_f32_16x16x32_bf16 v[88:91], a[176:179], v[140:143], v[88:91]\n"
    "ds_read_b64_tr_b16 a[166:167], v246 offset:63488\n"
    "v_cvt_pk_bf16_f32 v68, v68, v69\n"
    "v_mfma_f32_16x16x32_bf16 v[88:91], a[180:183], v[144:147], v[88:91]\n"
    "s_waitcnt lgkmcnt(4)\n"
    "ds_read_b64_tr_b16 a[176:177], v246 offset:12288\n"
    "v_cndmask_b32_e64 v234, v234, 1.0, vcc\n"
    "v_mfma_f32_16x16x32_bf16 v[88:91], a[184:187], v[196:199], v[88:91]\n"
    "ds_read_b64_tr_b16 a[172:173], v246 offset:36864\n"
    "v_subrev_f32_e32 v115, s4, v115\n"
    "v_mfma_f32_16x16x32_bf16 v[88:91], a[188:191], v[200:203], v[88:91]\n"
    "ds_read_b64_tr_b16 a[174:175], v246 offset:38912\n"
    "v_cvt_pk_bf16_f32 v69, v70, v71\n"
    "v_mfma_f32_16x16x32_bf16 v[92:95], a[188:191], v[208:211], v[92:95]\n"
    "buffer_load_dwordx4 a[136:139], v250, s[8:11], s62 offen offset:0 sc1\n"
    "v_mfma_f32_16x16x32_bf16 v[92:95], a[184:187], v[204:207], v[92:95]\n"
    "ds_read_b64_tr_b16 a[178:179], v246 offset:14336\n"
    "v_subrev_f32_e32 v5, s4, v5\n"
    "v_mfma_f32_16x16x32_bf16 v[96:99], a[184:187], v[212:215], v[96:99]\n"
    "v_cvt_pk_bf16_f32 v70, v88, v89\n"
    "v_cvt_pk_bf16_f32 v71, v90, v91\n"
    "v_mfma_f32_16x16x32_bf16 v[96:99], a[188:191], v[216:219], v[96:99]\n"
    "s_waitcnt lgkmcnt(4)\n"
    "ds_read_b64_tr_b16 a[180:181], v246 offset:45056\n"
    "v_cvt_pk_bf16_f32 v76, v76, v77\n"
    "v_mfma_f32_16x16x32_bf16 v[116:119], a[160:163], a[208:211], v[116:119]\n"
    "buffer_store_dwordx4 v[68:71], v250, s[8:11], s82 offen offset:2048 sc1\n"
    "v_mfma_f32_16x16x32_bf16 v[116:119], a[164:167], a[212:215], v[116:119]\n"
    "ds_read_b64_tr_b16 a[182:183], v246 offset:47104\n"
    "v_subrev_f32_e32 v111, s4, v111\n"
    "v_mfma_f32_16x16x32_bf16 v[112:115], a[164:167], a[204:207], v[112:115]\n"
    "ds_read_b64_tr_b16 a[184:185], v246 offset:20480\n"
    "v_subrev_f32_e32 v139, s4, v139\n"
    "v_mfma_f32_16x16x32_bf16 v[112:115], a[160:163], a[200:203], v[112:115]\n"
    "ds_read_b64_tr_b16 a[186:187], v246 offset:22528\n"
    "v_subrev_f32_e32 v134, s4, v134\n"
    "v_mfma_f32_16x16x32_bf16 v[108:111], a[160:163], a[192:195], v[108:111]\n"
    "buffer_load_dwordx4 a[140:143], v250, s[8:11], s62 offen offset:1024 sc1\n"
    "v_mfma_f32_16x16x32_bf16 v[108:111], a[164:167], a[196:199], v[108:111]\n"
    "s_waitcnt lgkmcnt(5)\n"
    "ds_read_b64_tr_b16 a[160:161], v247 offset:28672\n"
    "v_cmp_eq_u32_e32 vcc, 2, v252\n"
    "v_mfma_f32_16x16x32_bf16 v[108:111], a[168:171], v[156:159], v[108:111]\n"
    "v_cvt_pk_bf16_f32 v74, v92, v93\n"
    "v_subrev_f32_e32 v20, s4, v20\n"
    "v_mfma_f32_16x16x32_bf16 v[108:111], a[172:175], v[160:163], v[108:111]\n"
    "ds_read_b64_tr_b16 a[188:189], v246 offset:53248\n"
    "v_subrev_f32_e32 v25, s4, v25\n"
    "v_mfma_f32_16x16x32_bf16 v[112:115], a[172:175], v[168:171], v[112:115]\n"
    "ds_read_b64_tr_b16 a[162:163], v247 offset:30720\n"
    "v_subrev_f32_e32 v100, s4, v100\n"
    "v_mfma_f32_16x16x32_bf16 v[112:115], a[168:171], v[164:167], v[112:115]\n"
    "ds_read_b64_tr_b16 a[164:165], v247 offset:61440\n"
    "v_cndmask_b32_e32 v235, 0, v253, vcc\n"
    "v_mfma_f32_16x16x32_bf16 v[116:119], a[168:171], v[172:175], v[116:119]\n"
    "ds_read_b64_tr_b16 a[190:191], v246 offset:55296\n"
    "v_cvt_pk_bf16_f32 v77, v78, v79\n"
    "v_mfma_f32_16x16x32_bf16 v[116:119], a[172:175], v[176:179], v[116:119]\n"
    "s_waitcnt lgkmcnt(7)\n"
    "ds_read_b64_tr_b16 a[168:169], v247 offset:4096\n"
    "v_cvt_pk_bf16_f32 v75, v94, v95\n"
    "v_mfma_f32_16x16x32_bf16 v[116:119], a[176:179], v[180:183], v[116:119]\n"
    "ds_read_b64_tr_b16 a[170:171], v247 offset:6144\n"
    "v_subrev_f32_e32 v135, s4, v135\n"
    "v_mfma_f32_16x16x32_bf16 v[116:119], a[180:183], v[184:187], v[116:119]\n"
    "buffer_store_dwordx4 v[72:75], v250, s[8:11], s28 offen offset:2048 sc1\n"
    "v_mfma_f32_16x16x32_bf16 v[112:115], a[180:183], v[152:155], v[112:115]\n"
    "v_cvt_pk_bf16_f32 v78, v96, v97\n"
    "v_subrev_f32_e32 v6, s4, v6\n"
    "v_mfma_f32_16x16x32_bf16 v[112:115], a[176:179], v[148:151], v[112:115]\n"
    "ds_read_b64_tr_b16 a[166:167], v247 offset:63488\n"
    "v_subrev_f32_e32 v85, s4, v85\n"
    "v_mfma_f32_16x16x32_bf16 v[108:111], a[176:179], v[140:143], v[108:111]\n"
    "buffer_load_dwordx4 a[144:147], v250, s[8:11], s62 offen offset:2048 sc1\n"
    "v_mfma_f32_16x16x32_bf16 v[108:111], a[180:183], v[144:147], v[108:111]\n"
    "s_waitcnt lgkmcnt(3)\n"
    "ds_read_b64_tr_b16 a[172:173], v247 offset:36864\n"
    "v_cmp_eq_u32_e32 vcc, 3, v252\n"
    "v_mfma_f32_16x16x32_bf16 v[108:111], a[184:187], v[196:199], v[108:111]\n"
    "ds_read_b64_tr_b16 a[174:175], v247 offset:38912\n"
    "v_subrev_f32_e32 v83, s4, v83\n"
    "v_mfma_f32_16x16x32_bf16 v[108:111], a[188:191], v[200:203], v[108:111]\n"
    "ds_read_b64_tr_b16 a[176:177], v247 offset:12288\n"
    "v_cndmask_b32_e64 v235, v235, 1.0, vcc\n"
    "v_mfma_f32_16x16x32_bf16 v[112:115], a[188:191], v[208:211], v[112:115]\n"
    "v_subrev_f32_e32 v60, s4, v60\n"
    "v_subrev_f32_e32 v124, s4, v124\n"
    "v_mfma_f32_16x16x32_bf16 v[112:115], a[184:187], v[204:207], v[112:115]\n"
    "v_subrev_f32_e32 v131, s4, v131\n"
    "v_subrev_f32_e32 v123, s4, v123\n"
    "v_mfma_f32_16x16x32_bf16 v[116:119], a[184:187], v[212:215], v[116:119]\n"
    "ds_read_b64_tr_b16 a[178:179], v247 offset:14336\n"
    "v_subrev_f32_e32 v64, s4, v64\n"
    "v_mfma_f32_16x16x32_bf16 v[116:119], a[188:191], v[216:219], v[116:119]\n"
    "s_waitcnt lgkmcnt(4)\n"
    "v_cvt_pk_bf16_f32 v79, v98, v99\n"
    "v_cvt_pk_bf16_f32 v108, v108, v109\n"
    "v_mfma_f32_16x16x32_bf16 v[136:139], a[160:163], a[208:211], v[136:139]\n"
    "s_cmp_lg_u32 s44, 0\n"
    "s_cbranch_scc1 .Lat_glate\n"
    ".Lat_glate_ret:\n"
    "v_mfma_f32_16x16x32_bf16 v[136:139], a[164:167], a[212:215], v[136:139]\n"
    "ds_read_b64_tr_b16 a[180:181], v247 offset:45056\n"
    "v_mfma_f32_16x16x32_bf16 v[132:135], a[164:167], a[204:207], v[132:135]\n"
    "buffer_store_dwordx4 v[76:79], v250, s[8:11], s29 offen offset:2048 sc1\n"
    "v_mfma_f32_16x16x32_bf16 v[132:135], a[160:163], a[200:203], v[132:135]\n"
    "ds_read_b64_tr_b16 a[184:185], v247 offset:20480\n"
    "v_subrev_f32_e32 v65, s4, v65\n"
    "v_mfma_f32_16x16x32_bf16 v[128:131], a[160:163], a[192:195], v[128:131]\n"
    "ds_read_b64_tr_b16 a[182:183], v247 offset:47104\n"
    "v_mfma_f32_16x16x32_bf16 v[128:131], a[164:167], a[196:199], v[128:131]\n"
    "s_waitcnt lgkmcnt(5)\n"
    "ds_read_b64_tr_b16 a[186:187], v247 offset:22528\n"
    "v_subrev_f32_e32 v105, s4, v105\n"
    "v_mfma_f32_16x16x32_bf16 v[128:131], a[168:171], v[156:159], v[128:131]\n"
    "buffer_load_dwordx4 a[148:151], v250, s[8:11], s62 offen offset:3072 sc1\n"
    "v_mfma_f32_16x16x32_bf16 v[128:131], a[172:175], v[160:163], v[128:131]\n"
    "s_add_u32 s62, s57, 28672\n"
    "v_subrev_f32_e32 v106, s4, v106\n"
    "v_subrev_f32_e32 v101, s4, v101\n"
    "v_mfma_f32_16x16x32_bf16 v[132:135], a[172:175], v[168:171], v[132:135]\n"
    "ds_read_b64_tr_b16 a[188:189], v247 offset:53248\n"
    "v_subrev_f32_e32 v42, s4, v42\n"
    "v_mfma_f32_16x16x32_bf16 v[132:135], a[168:171], v[164:167], v[132:135]\n"
    "ds_read_b64_tr_b16 a[190:191], v247 offset:55296\n"
    "v_cvt_pk_bf16_f32 v112, v112, v113\n"
    "v_mfma_f32_16x16x32_bf16 v[136:139], a[168:171], v[172:175], v[136:139]\n"
    "ds_read_b128 a[160:163], v240 offset:24576\n"
    "v_subrev_f32_e32 v86, s4, v86\n"
    "v_mfma_f32_16x16x32_bf16 v[136:139], a[172:175], v[176:179], v[136:139]\n"
    "s_waitcnt lgkmcnt(4)\n"
    "v_subrev_f32_e32 v26, s4, v26\n"
    "v_mfma_f32_16x16x32_bf16 v[136:139], a[176:179], v[180:183], v[136:139]\n"
    "ds_read_b128 a[164:167], v241 offset:24576\n"
    "v_subrev_f32_e32 v107, s4, v107\n"
    "v_mfma_f32_16x16x32_bf16 v[136:139], a[180:183], v[184:187], v[136:139]\n"
    "v_subrev_f32_e32 v43, s4, v43\n"
    "v_subrev_f32_e32 v102, s4, v102\n"
    "v_mfma_f32_16x16x32_bf16 v[132:135], a[180:183], v[152:155], v[132:135]\n"
    "buffer_load_dwordx4 a[152:155], v250, s[8:11], s62 offen offset:0 sc1\n"
    "v_mfma_f32_16x16x32_bf16 v[132:135], a[176:179], v[148:151], v[132:135]\n"
    "v_subrev_f32_e32 v21, s4, v21\n"
    "v_subrev_f32_e32 v103, s4, v103\n"
    "v_mfma_f32_16x16x32_bf16 v[128:131], a[176:179], v[140:143], v[128:131]\n"
    "v_subrev_f32_e32 v2, s4, v2\n"
    "v_subrev_f32_e32 v61, s4, v61\n"
    "v_mfma_f32_16x16x32_bf16 v[128:131], a[180:183], v[144:147], v[128:131]\n"
    "s_waitcnt lgkmcnt(2)\n"
    "ds_read_b128 a[168:171], v240 offset:28672\n"
    "v_cvt_pk_bf16_f32 v116, v116, v117\n"
    "v_mfma_f32_16x16x32_bf16 v[128:131], a[184:187], v[196:199], v[128:131]\n"
    "v_cvt_pk_bf16_f32 v109, v110, v111\n"
    "v_subrev_f32_e32 v27, s4, v27\n"
    "v_mfma_f32_16x16x32_bf16 v[128:131], a[188:191], v[200:203], v[128:131]\n"
    "ds_read_b128 a[176:179], v242 offset:24576\n"
    "v_cvt_pk_bf16_f32 v117, v118, v119\n"
    "v_mfma_f32_16x16x32_bf16 v[132:135], a[188:191], v[208:211], v[132:135]\n"
    "ds_read_b128 a[172:175], v241 offset:28672\n"
    "v_subrev_f32_e32 v125, s4, v125\n"
    "v_mfma_f32_16x16x32_bf16 v[132:135], a[184:187], v[204:207], v[132:135]\n"
    "v_cvt_pk_bf16_f32 v110, v128, v129\n"
    "v_subrev_f32_e32 v87, s4, v87\n"
    "v_mfma_f32_16x16x32_bf16 v[136:139], a[184:187], v[212:215], v[136:139]\n"
    "ds_read_b128 a[180:183], v243 offset:24576\n"
    "v_cvt_pk_bf16_f32 v111, v130, v131\n"
    "v_mfma_f32_16x16x32_bf16 v[136:139], a[188:191], v[216:219], v[136:139]\n"
    "s_waitcnt lgkmcnt(4)\n"
    "ds_read_b128 a[184:187], v242 offset:28672\n"
    "v_subrev_f32_e32 v3, s4, v3\n"
    "v_mfma_f32_16x16x32_bf16 v[164:167], a[160:163], a[32:35], v[224:227]\n"
    "buffer_store_dwordx4 v[108:111], v250, s[8:11], s82 offen offset:3072 sc1\n"
    "v_mfma_f32_16x16x32_bf16 v[164:167], a[164:167], a[36:39], v[164:167]\n"
    "v_cvt_pk_bf16_f32 v113, v114, v115\n"
    "v_subrev_f32_e32 v46, s4, v46\n"
    "v_mfma_f32_16x16x32_bf16 v[156:159], a[164:167], a[4:7], v[220:223]\n"
    "ds_read_b128 a[188:191], v243 offset:28672\n"
    "v_mfma_f32_16x16x32_bf16 v[156:159], a[160:163], a[0:3], v[156:159]\n"
    "s_waitcnt lgkmcnt(3)\n"
    "v_subrev_f32_e32 v126, s4, v126\n"
    "v_mfma_f32_16x16x32_bf16 v[156:159], a[168:171], a[8:11], v[156:159]\n"
    "buffer_store_dword v131, v231, s[8:11], s87 offen offset:0 sc1\n"
    "v_mfma_f32_16x16x32_bf16 v[156:159], a[172:175], a[12:15], v[156:159]\n"
    "v_cvt_pk_bf16_f32 v114, v132, v133\n"
    "v_cvt_pk_bf16_f32 v118, v136, v137\n"
    "v_mfma_f32_16x16x32_bf16 v[164:167], a[172:175], a[44:47], v[164:167]\n"
    "ds_read_b128 a[160:163], v240 offset:26624\n"
    "v_cvt_pk_bf16_f32 v119, v138, v139\n"
    "v_mfma_f32_16x16x32_bf16 v[164:167], a[168:171], a[40:43], v[164:167]\n"
    "s_waitcnt lgkmcnt(3)\n"
    "ds_read_b128 a[164:167], v241 offset:26624\n"
    "v_subrev_f32_e32 v66, s4, v66\n"
    "v_mfma_f32_16x16x32_bf16 v[164:167], a[176:179], a[48:51], v[164:167]\n"
    "ds_read_b128 a[168:171], v240 offset:30720\n"
    "v_subrev_f32_e32 v67, s4, v67\n"
    "v_mfma_f32_16x16x32_bf16 v[164:167], a[180:183], a[52:55], v[164:167]\n"
    "buffer_store_dword v135, v231, s[8:11], s88 offen offset:0 sc1\n"
    "v_mfma_f32_16x16x32_bf16 v[156:159], a[180:183], a[20:23], v[156:159]\n"
    "v_cvt_pk_bf16_f32 v115, v134, v135\n"
    "v_subrev_f32_e32 v62, s4, v62\n"
    "v_mfma_f32_16x16x32_bf16 v[156:159], a[176:179], a[16:19], v[156:159]\n"
    "s_waitcnt lgkmcnt(3)\n"
    "ds_read_b128 a[172:175], v241 offset:30720\n"
    "v_subrev_f32_e32 v127, s4, v127\n"
    "v_mfma_f32_16x16x32_bf16 v[156:159], a[184:187], a[24:27], v[156:159]\n"
    "ds_read_b128 a[176:179], v242 offset:26624\n"
    "v_subrev_f32_e32 v22, s4, v22\n"
    "v_mfma_f32_16x16x32_bf16 v[156:159], a[188:191], a[28:31], v[156:159]\n"
    "buffer_store_dwordx4 v[112:115], v250, s[8:11], s28 offen offset:3072 sc1\n"
    "v_mfma_f32_16x16x32_bf16 v[164:167], a[188:191], a[60:63], v[164:167]\n"
    "ds_read_b128 a[180:183], v243 offset:26624\n"
    "v_subrev_f32_e32 v47, s4, v47\n"
    "v_mfma_f32_16x16x32_bf16 v[164:167], a[184:187], a[56:59], v[164:167]\n"
    "s_waitcnt lgkmcnt(4)\n"
    "ds_read_b128 a[184:187], v242 offset:30720\n"
    "ds_read_b128 a[188:191], v243 offset:30720\n"
    "v_mfma_f32_16x16x32_bf16 v[168:171], a[160:163], a[32:35], v[224:227]\n"
    "buffer_store_dword v139, v231, s[8:11], s89 offen offset:0 sc1\n"
    "v_mfma_f32_16x16x32_bf16 v[168:171], a[164:167], a[36:39], v[168:171]\n"
    "v_exp_f32_e32 v156, v156\n"
    "v_mfma_f32_16x16x32_bf16 v[160:163], a[164:167], a[4:7], v[220:223]\n"
    "v_exp_f32_e32 v157, v157\n"
    "v_mfma_f32_16x16x32_bf16 v[160:163], a[160:163], a[0:3], v[160:163]\n"
    "s_waitcnt lgkmcnt(4)\n"
    "ds_read_b128 a[160:163], v240 offset:0\n"
    "v_add_f32_e32 v128, v131, v135\n"
    "v_mfma_f32_16x16x32_bf16 v[160:163], a[168:171], a[8:11], v[160:163]\n"
    "buffer_load_dwordx4 a[156:159], v250, s[8:11], s62 offen offset:1024 sc1\n"
    "v_mfma_f32_16x16x32_bf16 v[160:163], a[172:175], a[12:15], v[160:163]\n"
    "s_add_u32 s62, s58, 0\n"
    "ds_read_b128 a[164:167], v241 offset:0\n"
    "v_min3_f32 v129, v131, v135, v139\n"
    "v_mfma_f32_16x16x32_bf16 v[168:171], a[172:175], a[44:47], v[168:171]\n"
    "v_exp_f32_e32 v158, v158\n"
    "v_mfma_f32_16x16x32_bf16 v[168:171], a[168:171], a[40:43], v[168:171]\n"
    "s_waitcnt lgkmcnt(4)\n"
    "ds_read_b128 a[168:171], v240 offset:4096\n"
    "v_add_f32_e32 v128, v128, v139\n"
    "v_mfma_f32_16x16x32_bf16 v[168:171], a[176:179], a[48:51], v[168:171]\n"
    "v_exp_f32_e32 v159, v159\n"
    "v_mfma_f32_16x16x32_bf16 v[168:171], a[180:183], a[52:55], v[168:171]\n"
    "ds_read_b128 a[172:175], v241 offset:4096\n"
    "v_cvt_pk_bf16_f32 v156, v156, v157\n"
    "v_mfma_f32_16x16x32_bf16 v[160:163], a[180:183], a[20:23], v[160:163]\n"
    "buffer_store_dwordx4 v[116:119], v250, s[8:11], s29 offen offset:3072 sc1\n"
    "v_mfma_f32_16x16x32_bf16 v[160:163], a[176:179], a[16:19], v[160:163]\n"
    "s_waitcnt lgkmcnt(4)\n"
    "v_exp_f32_e32 v164, v164\n"
    "v_mfma_f32_16x16x32_bf16 v[160:163], a[184:187], a[24:27], v[160:163]\n"
    "ds_write_addtid_b32 v156 offset:0\n"
    "v_exp_f32_e32 v165, v165\n"
    "v_mfma_f32_16x16x32_bf16 v[160:163], a[188:191], a[28:31], v[160:163]\n"
    "buffer_load_dwordx4 a[244:247], v250, s[8:11], s62 offen offset:0 sc1\n"
    "ds_read_b128 a[176:179], v242 offset:0\n"
    "v_mfma_f32_16x16x32_bf16 v[168:171], a[188:191], a[60:63], v[168:171]\n"
    "ds_read_b128 a[180:183], v243 offset:0\n"
    "v_cvt_pk_bf16_f32 v157, v158, v159\n"
    "v_mfma_f32_16x16x32_bf16 v[168:171], a[184:187], a[56:59], v[168:171]\n"
    "s_waitcnt lgkmcnt(5)\n"
    "v_exp_f32_e32 v166, v166\n"
    "v_mfma_f32_16x16x32_bf16 v[204:207], a[160:163], a[32:35], v[224:227]\n"
    "v_exp_f32_e32 v167, v167\n"
    "v_mfma_f32_16x16x32_bf16 v[204:207], a[164:167], a[36:39], v[204:207]\n"
    "ds_read_b128 a[184:187], v242 offset:4096\n"
    "v_cvt_pk_bf16_f32 v164, v164, v165\n"
    "v_mfma_f32_16x16x32_bf16 v[196:199], a[164:167], a[4:7], v[220:223]\n"
    "v_exp_f32_e32 v160, v160\n"
    "v_mfma_f32_16x16x32_bf16 v[196:199], a[160:163], a[0:3], v[196:199]\n"
    "s_waitcnt lgkmcnt(4)\n"
    "ds_read_b128 a[160:163], v240 offset:2048\n"
    "v_exp_f32_e32 v161, v161\n"
    "v_mfma_f32_16x16x32_bf16 v[196:199], a[168:171], a[8:11], v[196:199]\n"
    "ds_read_b128 a[188:191], v243 offset:4096\n"
    "ds_write_addtid_b32 v157 offset:256\n"
    "v_mfma_f32_16x16x32_bf16 v[196:199], a[172:175], a[12:15], v[196:199]\n"
    "buffer_load_dwordx4 v[8:11], v250, s[8:11], s62 offen offset:1024 sc1\n"
    "v_mfma_f32_16x16x32_bf16 v[204:207], a[172:175], a[44:47], v[204:207]\n"
    "ds_read_b128 a[164:167], v241 offset:2048\n"
    "v_cvt_pk_bf16_f32 v165, v166, v167\n"
    "v_mfma_f32_16x16x32_bf16 v[204:207], a[168:171], a[40:43], v[204:207]\n"
    "s_waitcnt lgkmcnt(5)\n"
    "v_exp_f32_e32 v162, v162\n"
    "v_mfma_f32_16x16x32_bf16 v[204:207], a[176:179], a[48:51], v[204:207]\n"
    "v_exp_f32_e32 v163, v163\n"
    "v_mfma_f32_16x16x32_bf16 v[204:207], a[180:183], a[52:55], v[204:207]\n"
    "ds_read_b128 a[168:171], v240 offset:6144\n"
    "ds_write_addtid_b32 v164 offset:1024\n"
    "v_mfma_f32_16x16x32_bf16 v[196:199], a[180:183], a[20:23], v[196:199]\n"
    "ds_read_b128 a[172:175], v241 offset:6144\n"
    "v_cvt_pk_bf16_f32 v158, v160, v161\n"
    "v_mfma_f32_16x16x32_bf16 v[196:199], a[176:179], a[16:19], v[196:199]\n"
    "s_waitcnt lgkmcnt(5)\n"
    "v_exp_f32_e32 v168, v168\n"
    "v_mfma_f32_16x16x32_bf16 v[196:199], a[184:187], a[24:27], v[196:199]\n"
    "v_exp_f32_e32 v169, v169\n"
    "v_mfma_f32_16x16x32_bf16 v[196:199], a[188:191], a[28:31], v[196:199]\n"
    "buffer_load_dwordx4 a[248:251], v250, s[8:11], s62 offen offset:2048 sc1\n"
    "v_mfma_f32_16x16x32_bf16 v[204:207], a[188:191], a[60:63], v[204:207]\n"
    "ds_read_b128 a[176:179], v242 offset:2048\n"
    "v_cvt_pk_bf16_f32 v159, v162, v163\n"
    "v_mfma_f32_16x16x32_bf16 v[204:207], a[184:187], a[56:59], v[204:207]\n"
    "s_waitcnt lgkmcnt(4)\n"
    "ds_read_b128 a[180:183], v243 offset:2048\n"
    "v_exp_f32_e32 v170, v170\n"
    "v_mfma_f32_16x16x32_bf16 v[208:211], a[160:163], a[32:35], v[224:227]\n"
    "ds_read_b128 a[184:187], v242 offset:6144\n"
    "ds_write_addtid_b32 v165 offset:1280\n"
    "v_mfma_f32_16x16x32_bf16 v[208:211], a[164:167], a[36:39], v[208:211]\n"
    "ds_read_b128 a[188:191], v243 offset:6144\n"
    "v_exp_f32_e32 v196, v196\n"
    "v_mfma_f32_16x16x32_bf16 v[200:203], a[164:167], a[4:7], v[220:223]\n"
    "v_exp_f32_e32 v171, v171\n"
    "v_mfma_f32_16x16x32_bf16 v[200:203], a[160:163], a[0:3], v[200:203]\n"
    "s_waitcnt lgkmcnt(5)\n"
    "v_exp_f32_e32 v197, v197\n"
    "v_mfma_f32_16x16x32_bf16 v[200:203], a[168:171], a[8:11], v[200:203]\n"
    "v_exp_f32_e32 v198, v198\n"
    "v_mfma_f32_16x16x32_bf16 v[200:203], a[172:175], a[12:15], v[200:203]\n"
    "buffer_load_dwordx4 v[12:15], v250, s[8:11], s62 offen offset:3072 sc1\n"
    "ds_read_b128 a[160:163], v240 offset:8192\n"
    "v_mfma_f32_16x16x32_bf16 v[208:211], a[172:175], a[44:47], v[208:211]\n"
    "s_add_u32 s62, s58, 4096\n"
    "v_exp_f32_e32 v199, v199\n"
    "v_mfma_f32_16x16x32_bf16 v[208:211], a[168:171], a[40:43], v[208:211]\n"
    "s_waitcnt lgkmcnt(4)\n"
    "ds_read_b128 a[164:167], v241 offset:8192\n"
    "v_cvt_pk_bf16_f32 v196, v196, v197\n"
    "v_mfma_f32_16x16x32_bf16 v[208:211], a[176:179], a[48:51], v[208:211]\n"
    "ds_read_b128 a[168:171], v240 offset:12288\n"
    "v_exp_f32_e32 v204, v204\n"
    "v_mfma_f32_16x16x32_bf16 v[208:211], a[180:183], a[52:55], v[208:211]\n"
    "v_exp_f32_e32 v205, v205\n"
    "v_mfma_f32_16x16x32_bf16 v[200:203], a[180:183], a[20:23], v[200:203]\n"
    "v_cvt_pk_bf16_f32 v197, v198, v199\n"
    "v_exp_f32_e32 v206, v206\n"
    "v_mfma_f32_16x16x32_bf16 v[200:203], a[176:179], a[16:19], v[200:203]\n"
    "s_waitcnt lgkmcnt(3)\n"
    "v_exp_f32_e32 v207, v207\n"
    "v_mfma_f32_16x16x32_bf16 v[200:203], a[184:187], a[24:27], v[200:203]\n"
    "ds_read_b128 a[172:175], v241 offset:12288\n"
    "v_cvt_pk_bf16_f32 v204, v204, v205\n"
    "v_mfma_f32_16x16x32_bf16 v[200:203], a[188:191], a[28:31], v[200:203]\n"
    "buffer_load_dwordx4 a[252:255], v250, s[8:11], s62 offen offset:0 sc1\n"
    "v_mfma_f32_16x16x32_bf16 v[208:211], a[188:191], a[60:63], v[208:211]\n"
    "ds_read_b128 a[176:179], v242 offset:8192\n"
    "v_cvt_pk_bf16_f32 v166, v168, v169\n"
    "v_mfma_f32_16x16x32_bf16 v[208:211], a[184:187], a[56:59], v[208:211]\n"
    "s_waitcnt lgkmcnt(3)\n"
    "v_exp_f32_e32 v200, v200\n"
    "v_mfma_f32_16x16x32_bf16 v[212:215], a[160:163], a[32:35], v[224:227]\n"
    "ds_read_b128 a[180:183], v243 offset:8192\n"
    "v_exp_f32_e32 v201, v201\n"
    "v_mfma_f32_16x16x32_bf16 v[212:215], a[164:167], a[36:39], v[212:215]\n"
    "v_cvt_pk_bf16_f32 v205, v206, v207\n"
    "v_exp_f32_e32 v202, v202\n"
    "v_mfma_f32_16x16x32_bf16 v[172:175], a[164:167], a[4:7], v[220:223]\n"
    "v_exp_f32_e32 v203, v203\n"
    "v_mfma_f32_16x16x32_bf16 v[172:175], a[160:163], a[0:3], v[172:175]\n"
    "s_waitcnt lgkmcnt(2)\n"
    "ds_read_b128 a[184:187], v242 offset:12288\n"
    "v_cvt_pk_bf16_f32 v198, v200, v201\n"
    "v_mfma_f32_16x16x32_bf16 v[172:175], a[168:171], a[8:11], v[172:175]\n"
    "ds_read_b128 a[188:191], v243 offset:12288\n"
    "v_exp_f32_e32 v208, v208\n"
    "v_mfma_f32_16x16x32_bf16 v[172:175], a[172:175], a[12:15], v[172:175]\n"
    "buffer_load_dwordx4 v[16:19], v250, s[8:11], s62 offen offset:1024 sc1\n"
    "v_mfma_f32_16x16x32_bf16 v[212:215], a[172:175], a[44:47], v[212:215]\n"
    "s_add_u32 s62, s57, 28672\n"
    "ds_read_b128 a[160:163], v240 offset:10240\n"
    "v_subrev_f32_e32 v23, s4, v23\n"
    "v_mfma_f32_16x16x32_bf16 v[212:215], a[168:171], a[40:43], v[212:215]\n"
    "s_waitcnt lgkmcnt(3)\n"
    "v_exp_f32_e32 v209, v209\n"
    "v_mfma_f32_16x16x32_bf16 v[212:215], a[176:179], a[48:51], v[212:215]\n"
    "ds_read_b128 a[164:167], v241 offset:10240\n"
    "ds_read_b128 a[168:171], v240 offset:14336\n"
    "v_mfma_f32_16x16x32_bf16 v[212:215], a[180:183], a[52:55], v[212:215]\n"
    "ds_write_addtid_b32 v158 offset:512\n"
    "ds_write_addtid_b32 v159 offset:768\n"
    "v_mfma_f32_16x16x32_bf16 v[172:175], a[180:183], a[20:23], v[172:175]\n"
    "ds_write_addtid_b32 v166 offset:1536\n"
    "v_cvt_pk_bf16_f32 v167, v170, v171\n"
    "v_mfma_f32_16x16x32_bf16 v[172:175], a[176:179], a[16:19], v[172:175]\n"
    "s_waitcnt lgkmcnt(6)\n"
    "ds_read_b128 a[172:175], v241 offset:14336\n"
    "ds_read_b128 a[176:179], v242 offset:10240\n"
    "v_mfma_f32_16x16x32_bf16 v[172:175], a[184:187], a[24:27], v[172:175]\n"
    "ds_read_b128 a[180:183], v243 offset:10240\n"
    "v_cvt_pk_bf16_f32 v199, v202, v203\n"
    "v_mfma_f32_16x16x32_bf16 v[172:175], a[188:191], a[28:31], v[172:175]\n"
    "buffer_load_dwordx4 v[180:183], v250, s[8:11], s62 offen offset:2048 sc1\n"
    "v_mfma_f32_16x16x32_bf16 v[212:215], a[188:191], a[60:63], v[212:215]\n"
    "ds_write_addtid_b32 v167 offset:1792\n"
    "v_subrev_f32_e32 v63, s4, v63\n"
    "v_mfma_f32_16x16x32_bf16 v[212:215], a[184:187], a[56:59], v[212:215]\n"
    "s_waitcnt lgkmcnt(8)\n"
    "ds_read_b128 a[184:187], v242 offset:14336\n"
    "v_exp_f32_e32 v210, v210\n"
    "v_mfma_f32_16x16x32_bf16 v[216:219], a[160:163], a[32:35], v[224:227]\n"
    "ds_read_b128 a[188:191], v243 offset:14336\n"
    "v_exp_f32_e32 v172, v172\n"
    "v_mfma_f32_16x16x32_bf16 v[216:219], a[164:167], a[36:39], v[216:219]\n"
    "s_waitcnt lgkmcnt(2)\n"
    "v_mfma_f32_16x16x32_bf16 v[176:179], a[164:167], a[4:7], v[220:223]\n"
    "s_barrier\n"
    "v_mfma_f32_16x16x32_bf16 v[176:179], a[160:163], a[0:3], v[176:179]\n"
    "s_waitcnt vmcnt(7)\n"
    "buffer_store_dword v254, v231, s[8:11], s38 offen offset:0 sc1\n"
    "v_mfma_f32_16x16x32_bf16 v[176:179], a[168:171], a[8:11], v[176:179]\n"
    "ds_read_b64_tr_b16 a[160:161], v244 offset:24576\n"
    "v_exp_f32_e32 v173, v173\n"
    "v_mfma_f32_16x16x32_bf16 v[176:179], a[172:175], a[12:15], v[176:179]\n"
    "ds_read_b64_tr_b16 a[162:163], v244 offset:26624\n"
    "ds_read2st64_b32 v[160:161], v249 offset0:0 offset1:1\n"
    "v_mfma_f32_16x16x32_bf16 v[216:219], a[172:175], a[44:47], v[216:219]\n"
    "buffer_load_dwordx4 v[184:187], v250, s[8:11], s62 offen offset:3072 sc1\n"
    "ds_read2st64_b32 v[162:163], v249 offset0:2 offset1:3\n"
    "v_mfma_f32_16x16x32_bf16 v[216:219], a[168:171], a[40:43], v[216:219]\n"
    "ds_read_b64_tr_b16 a[164:165], v244 offset:57344\n"
    "ds_read_b64_tr_b16 a[166:167], v244 offset:59392\n"
    "v_mfma_f32_16x16x32_bf16 v[216:219], a[176:179], a[48:51], v[216:219]\n"
    "ds_read2st64_b32 v[168:169], v249 offset0:4 offset1:5\n"
    "ds_read2st64_b32 v[170:171], v249 offset0:6 offset1:7\n"
    "v_mfma_f32_16x16x32_bf16 v[216:219], a[180:183], a[52:55], v[216:219]\n"
    "ds_write_addtid_b32 v196 offset:2048\n"
    "v_exp_f32_e32 v174, v174\n"
    "v_mfma_f32_16x16x32_bf16 v[176:179], a[180:183], a[20:23], v[176:179]\n"
    "s_add_u32 s62, s58, 4096\n"
    "ds_read_b64_tr_b16 a[168:169], v245 offset:24576\n"
    "v_exp_f32_e32 v175, v175\n"
    "v_mfma_f32_16x16x32_bf16 v[176:179], a[176:179], a[16:19], v[176:179]\n"
    "s_waitcnt lgkmcnt(10)\n"
    "ds_read_b64_tr_b16 a[170:171], v245 offset:26624\n"
    "ds_read_b64_tr_b16 a[172:173], v245 offset:57344\n"
    "v_mfma_f32_16x16x32_bf16 v[176:179], a[184:187], a[24:27], v[176:179]\n"
    "buffer_load_dwordx4 v[28:31], v250, s[8:11], s62 offen offset:2048 sc1\n"
    "ds_read_b64_tr_b16 a[174:175], v245 offset:59392\n"
    "v_mfma_f32_16x16x32_bf16 v[176:179], a[188:191], a[28:31], v[176:179]\n"
    ".long 0xbedf00ff\n"
    ".Lat_w12:\n"
    "s_setpc_b64 s[2:3]\n"
    "ds_read_b64_tr_b16 a[176:177], v246 offset:24576\n"
    "v_subrev_f32_e32 v7, s4, v7\n"
    "v_mfma_f32_16x16x32_bf16 v[216:219], a[188:191], a[60:63], v[216:219]\n"
    "ds_read_b64_tr_b16 a[178:179], v246 offset:26624\n"
    "v_cvt_pk_bf16_f32 v172, v172, v173\n"
    "v_mfma_f32_16x16x32_bf16 v[216:219], a[184:187], a[56:59], v[216:219]\n"
    "s_waitcnt lgkmcnt(7)\n"
    "ds_read_b64_tr_b16 a[180:181], v246 offset:57344\n"
    "ds_read_b64_tr_b16 a[182:183], v246 offset:59392\n"
    "v_mfma_f32_16x16x32_bf16 a[240:243], a[160:163], v[164:167], a[240:243]\n"
    "buffer_load_dwordx4 v[48:51], v250, s[8:11], s62 offen offset:3072 sc1\n"
    "ds_read_b64_tr_b16 a[184:185], v247 offset:24576\n"
    "v_mfma_f32_16x16x32_bf16 a[240:243], a[164:167], v[168:171], a[240:243]\n"
    "s_add_u32 s62, s58, 8192\n"
    "ds_read_b64_tr_b16 a[186:187], v247 offset:26624\n"
    "v_exp_f32_e32 v212, v212\n"
    "v_mfma_f32_16x16x32_bf16 a[236:239], a[164:167], v[160:163], a[236:239]\n"
    "ds_read_b64_tr_b16 a[188:189], v247 offset:57344\n"
    "v_exp_f32_e32 v213, v213\n"
    "v_mfma_f32_16x16x32_bf16 a[236:239], a[160:163], v[156:159], a[236:239]\n"
    "s_waitcnt lgkmcnt(7)\n"
    "ds_read_b64_tr_b16 a[160:161], v244 offset:28672\n"
    "v_cvt_pk_bf16_f32 v173, v174, v175\n"
    "v_mfma_f32_16x16x32_bf16 v[0:3], a[168:171], v[156:159], v[0:3]\n"
    "buffer_load_dwordx4 v[32:35], v250, s[8:11], s62 offen offset:0 sc1\n"
    "ds_read_b64_tr_b16 a[190:191], v247 offset:59392\n"
    "v_mfma_f32_16x16x32_bf16 v[0:3], a[172:175], v[160:163], v[0:3]\n"
    "ds_read_b64_tr_b16 a[162:163], v244 offset:30720\n"
    "v_exp_f32_e32 v214, v214\n"
    "v_mfma_f32_16x16x32_bf16 v[4:7], a[172:175], v[168:171], v[4:7]\n"
    "ds_read_b64_tr_b16 a[164:165], v244 offset:61440\n"
    "v_exp_f32_e32 v211, v211\n"
    "v_mfma_f32_16x16x32_bf16 v[4:7], a[168:171], v[164:167], v[4:7]\n"
    "s_waitcnt lgkmcnt(7)\n"
    "ds_read_b64_tr_b16 a[166:167], v244 offset:63488\n"
    "ds_read_b64_tr_b16 a[168:169], v245 offset:28672\n"
    "v_mfma_f32_16x16x32_bf16 v[24:27], a[176:179], v[164:167], v[24:27]\n"
    "buffer_load_dwordx4 v[52:55], v250, s[8:11], s62 offen offset:1024 sc1\n"
    "ds_read_b64_tr_b16 a[170:171], v245 offset:30720\n"
    "v_mfma_f32_16x16x32_bf16 v[24:27], a[180:183], v[168:171], v[24:27]\n"
    "ds_write_addtid_b32 v197 offset:2304\n"
    "v_exp_f32_e32 v215, v215\n"
    "v_mfma_f32_16x16x32_bf16 v[20:23], a[180:183], v[160:163], v[20:23]\n"
    "ds_read_b64_tr_b16 a[172:173], v245 offset:61440\n"
    "ds_write_addtid_b32 v172 offset:4096\n"
    "v_mfma_f32_16x16x32_bf16 v[20:23], a[176:179], v[156:159], v[20:23]\n"
    "s_waitcnt lgkmcnt(8)\n"
    "ds_read_b64_tr_b16 a[174:175], v245 offset:63488\n"
    "ds_read_b64_tr_b16 a[176:177], v246 offset:28672\n"
    "v_mfma_f32_16x16x32_bf16 v[40:43], a[184:187], v[156:159], v[40:43]\n"
    "buffer_load_dwordx4 v[36:39], v250, s[8:11], s62 offen offset:2048 sc1\n"
    "v_mfma_f32_16x16x32_bf16 v[40:43], a[188:191], v[160:163], v[40:43]\n"
    "ds_read_b64_tr_b16 a[178:179], v246 offset:30720\n"
    "v_cvt_pk_bf16_f32 v206, v208, v209\n"
    "v_mfma_f32_16x16x32_bf16 v[44:47], a[188:191], v[168:171], v[44:47]\n"
    "ds_read_b64_tr_b16 a[180:181], v246 offset:61440\n"
    "v_cvt_pk_bf16_f32 v212, v212, v213\n"
    "v_mfma_f32_16x16x32_bf16 v[44:47], a[184:187], v[164:167], v[44:47]\n"
    "s_waitcnt lgkmcnt(9)\n"
    "ds_read_b64_tr_b16 a[182:183], v246 offset:63488\n"
    "v_exp_f32_e32 v176, v176\n"
    "v_mfma_f32_16x16x32_bf16 v[64:67], a[160:163], v[164:167], v[64:67]\n"
    "buffer_load_dwordx4 v[56:59], v250, s[8:11], s62 offen offset:3072 sc1\n"
    "ds_read_b64_tr_b16 a[184:185], v247 offset:28672\n"
    "v_mfma_f32_16x16x32_bf16 v[64:67], a[164:167], v[168:171], v[64:67]\n"
    "s_add_u32 s62, s58, 12288\n"
    "v_exp_f32_e32 v177, v177\n"
    "v_mfma_f32_16x16x32_bf16 v[60:63], a[164:167], v[160:163], v[60:63]\n"
    "ds_read_b64_tr_b16 a[186:187], v247 offset:30720\n"
    "v_cvt_pk_bf16_f32 v213, v214, v215\n"
    "v_mfma_f32_16x16x32_bf16 v[60:63], a[160:163], v[156:159], v[60:63]\n"
    "s_waitcnt lgkmcnt(6)\n"
    "ds_read_b64_tr_b16 a[188:189], v247 offset:61440\n"
    "v_exp_f32_e32 v178, v178\n"
    "v_mfma_f32_16x16x32_bf16 v[80:83], a[168:171], v[156:159], v[80:83]\n"
    "buffer_load_dwordx4 v[68:71], v250, s[8:11], s62 offen offset:0 sc1\n"
    "ds_read_b64_tr_b16 a[190:191], v247 offset:63488\n"
    "v_mfma_f32_16x16x32_bf16 v[80:83], a[172:175], v[160:163], v[80:83]\n"
    "ds_write_addtid_b32 v204 offset:3072\n"
    "ds_write_addtid_b32 v205 offset:3328\n"
    "v_mfma_f32_16x16x32_bf16 v[84:87], a[172:175], v[168:171], v[84:87]\n"
    "ds_read_b128 a[160:163], v240 offset:16384\n"
    "ds_write_addtid_b32 v198 offset:2560\n"
    "v_mfma_f32_16x16x32_bf16 v[84:87], a[168:171], v[164:167], v[84:87]\n"
    "s_waitcnt lgkmcnt(8)\n"
    "ds_write_addtid_b32 v199 offset:2816\n"
    "v_exp_f32_e32 v179, v179\n"
    "v_mfma_f32_16x16x32_bf16 v[104:107], a[176:179], v[164:167], v[104:107]\n"
    "buffer_load_dwordx4 v[88:91], v250, s[8:11], s62 offen offset:1024 sc1\n"
    "ds_read_b128 a[164:167], v241 offset:16384\n"
    "v_mfma_f32_16x16x32_bf16 v[104:107], a[180:183], v[168:171], v[104:107]\n"
    "s_waitcnt lgkmcnt(6)\n"
    "v_mfma_f32_16x16x32_bf16 v[100:103], a[180:183], v[160:163], v[100:103]\n"
    "s_barrier\n"
    "v_mfma_f32_16x16x32_bf16 v[100:103], a[176:179], v[156:159], v[100:103]\n"
    "ds_read_b128 a[168:171], v240 offset:20480\n"
    "v_cvt_pk_bf16_f32 v174, v176, v177\n"
    "v_mfma_f32_16x16x32_bf16 v[120:123], a[184:187], v[156:159], v[120:123]\n"
    "s_add_u32 s31, s47, s35\n"
    "ds_read_b128 a[172:175], v241 offset:20480\n"
    "ds_read_b128 a[176:179], v242 offset:16384\n"
    "v_mfma_f32_16x16x32_bf16 v[120:123], a[188:191], v[160:163], v[120:123]\n"
    "s_add_u32 m0, s55, 24576\n"
    "ds_read_b128 a[180:183], v243 offset:16384\n"
    "v_exp_f32_e32 v216, v216\n"
    "v_mfma_f32_16x16x32_bf16 v[124:127], a[188:191], v[168:171], v[124:127]\n"
    "buffer_load_dwordx4 v251, s[8:11], s31 offen offset:0 sc1 lds\n"
    "v_mfma_f32_16x16x32_bf16 v[124:127], a[184:187], v[164:167], v[124:127]\n"
    "s_waitcnt lgkmcnt(4)\n"
    "v_exp_f32_e32 v217, v217\n"
    "v_mfma_f32_16x16x32_bf16 v[148:151], a[160:163], a[32:35], v[224:227]\n"
    "s_mov_b32 m0, s63\n"
    "ds_read_b128 a[184:187], v242 offset:20480\n"
    "v_cvt_pk_bf16_f32 v207, v210, v211\n"
    "v_mfma_f32_16x16x32_bf16 v[148:151], a[164:167], a[36:39], v[148:151]\n"
    "buffer_load_dword v253, v238, s[8:11], s48 offen offset:0 sc1\n"
    "v_mfma_f32_16x16x32_bf16 v[140:143], a[164:167], a[4:7], v[220:223]\n"
    "s_addk_i32 s31, 0x1000\n"
    "ds_write_addtid_b32 v206 offset:3584\n"
    "ds_write_addtid_b32 v173 offset:4352\n"
    "v_mfma_f32_16x16x32_bf16 v[140:143], a[160:163], a[0:3], v[140:143]\n"
    "s_waitcnt lgkmcnt(5)\n"
    "ds_read_b128 a[188:191], v243 offset:20480\n"
    "v_cvt_pk_bf16_f32 v175, v178, v179\n"
    "v_mfma_f32_16x16x32_bf16 v[140:143], a[168:171], a[8:11], v[140:143]\n"
    "s_add_u32 m0, s55, 25600\n"
    "ds_read_b128 a[160:163], v240 offset:18432\n"
    "ds_read_b128 a[164:167], v241 offset:18432\n"
    "v_mfma_f32_16x16x32_bf16 v[140:143], a[172:175], a[12:15], v[140:143]\n"
    "buffer_load_dwordx4 v251, s[8:11], s31 offen offset:0 sc1 lds\n"
    "v_mfma_f32_16x16x32_bf16 v[148:151], a[172:175], a[44:47], v[148:151]\n"
    "s_mov_b32 m0, s63\n"
    "v_exp_f32_e32 v218, v218\n"
    "v_mfma_f32_16x16x32_bf16 v[148:151], a[168:171], a[40:43], v[148:151]\n"
    "s_waitcnt lgkmcnt(6)\n"
    "ds_write_addtid_b32 v207 offset:3840\n"
    "v_exp_f32_e32 v219, v219\n"
    "v_mfma_f32_16x16x32_bf16 v[148:151], a[176:179], a[48:51], v[148:151]\n"
    "s_add_u32 s87, s59, 8192\n"
    "ds_write_addtid_b32 v212 offset:5120\n"
    "ds_write_addtid_b32 v213 offset:5376\n"
    "v_mfma_f32_16x16x32_bf16 v[148:151], a[180:183], a[52:55], v[148:151]\n"
    "s_add_u32 s31, s47, s36\n"
    "ds_write_addtid_b32 v174 offset:4608\n"
    "ds_write_addtid_b32 v175 offset:4864\n"
    "v_mfma_f32_16x16x32_bf16 v[140:143], a[180:183], a[20:23], v[140:143]\n"
    "s_add_u32 m0, s55, 26624\n"
    "ds_read_b128 a[168:171], v240 offset:22528\n"
    "v_cvt_pk_bf16_f32 v214, v216, v217\n"
    "v_mfma_f32_16x16x32_bf16 v[140:143], a[176:179], a[16:19], v[140:143]\n"
    "s_waitcnt lgkmcnt(8)\n"
    "ds_read_b128 a[172:175], v241 offset:22528\n"
    "ds_read_b128 a[176:179], v242 offset:18432\n"
    "v_mfma_f32_16x16x32_bf16 v[140:143], a[184:187], a[24:27], v[140:143]\n"
    "buffer_load_dwordx4 v251, s[8:11], s31 offen offset:0 sc1 lds\n"
    "v_mfma_f32_16x16x32_bf16 v[140:143], a[188:191], a[28:31], v[140:143]\n"
    "s_mov_b32 m0, s63\n"
    "ds_read_b128 a[180:183], v243 offset:18432\n"
    "v_cvt_pk_bf16_f32 v215, v218, v219\n"
    "v_mfma_f32_16x16x32_bf16 v[148:151], a[188:191], a[60:63], v[148:151]\n"
    "s_add_u32 s88, s59, 16384\n"
    "ds_write_addtid_b32 v214 offset:5632\n"
    "ds_write_addtid_b32 v215 offset:5888\n"
    "v_mfma_f32_16x16x32_bf16 v[148:151], a[184:187], a[56:59], v[148:151]\n"
    "s_waitcnt lgkmcnt(11)\n"
    "ds_read_b128 a[184:187], v242 offset:22528\n"
    "v_exp_f32_e32 v140, v140\n"
    "v_mfma_f32_16x16x32_bf16 v[152:155], a[160:163], a[32:35], v[224:227]\n"
    "s_addk_i32 s31, 0x1000\n"
    "ds_read_b128 a[188:191], v243 offset:22528\n"
    "v_exp_f32_e32 v141, v141\n"
    "v_mfma_f32_16x16x32_bf16 v[152:155], a[164:167], a[36:39], v[152:155]\n"
    "s_waitcnt lgkmcnt(2)\n"
    "v_mfma_f32_16x16x32_bf16 v[144:147], a[164:167], a[4:7], v[220:223]\n"
    "s_barrier\n"
    "v_mfma_f32_16x16x32_bf16 v[144:147], a[160:163], a[0:3], v[144:147]\n"
    "ds_read_b64_tr_b16 a[160:161], v244 offset:0\n"
    "ds_read2st64_b32 v[200:201], v249 offset0:8 offset1:9\n"
    "v_mfma_f32_16x16x32_bf16 v[144:147], a[168:171], a[8:11], v[144:147]\n"
    "s_add_u32 m0, s55, 27648\n"
    "ds_read_b64_tr_b16 a[162:163], v244 offset:2048\n"
    "v_exp_f32_e32 v142, v142\n"
    "v_mfma_f32_16x16x32_bf16 v[144:147], a[172:175], a[12:15], v[144:147]\n"
    "buffer_load_dwordx4 v251, s[8:11], s31 offen offset:0 sc1 lds\n"
    "ds_read2st64_b32 v[202:203], v249 offset0:10 offset1:11\n"
    "v_mfma_f32_16x16x32_bf16 v[152:155], a[172:175], a[44:47], v[152:155]\n"
    "s_mov_b32 m0, s63\n"
    "ds_read_b64_tr_b16 a[164:165], v244 offset:32768\n"
    "v_exp_f32_e32 v143, v143\n"
    "v_mfma_f32_16x16x32_bf16 v[152:155], a[168:171], a[40:43], v[152:155]\n"
    "ds_read2st64_b32 v[208:209], v249 offset0:12 offset1:13\n"
    "ds_read2st64_b32 v[210:211], v249 offset0:14 offset1:15\n"
    "v_cvt_pk_bf16_f32 v140, v140, v141\n"
    "v_mfma_f32_16x16x32_bf16 v[152:155], a[176:179], a[48:51], v[152:155]\n"
    "s_add_u32 s89, s59, 24576\n"
    "ds_read_b64_tr_b16 a[166:167], v244 offset:34816\n"
    "v_exp_f32_e32 v148, v148\n"
    "v_mfma_f32_16x16x32_bf16 v[152:155], a[180:183], a[52:55], v[152:155]\n"
    "s_add_u32 s31, s47, s37\n"
    "ds_read_b64_tr_b16 a[168:169], v245 offset:0\n"
    "v_exp_f32_e32 v149, v149\n"
    "v_mfma_f32_16x16x32_bf16 v[144:147], a[180:183], a[20:23], v[144:147]\n"
    "s_add_u32 m0, s55, 28672\n"
    "ds_read_b64_tr_b16 a[170:171], v245 offset:2048\n"
    "v_cvt_pk_bf16_f32 v141, v142, v143\n"
    "v_mfma_f32_16x16x32_bf16 v[144:147], a[176:179], a[16:19], v[144:147]\n"
    "s_waitcnt lgkmcnt(10)\n"
    "ds_read_b64_tr_b16 a[172:173], v245 offset:32768\n"
    "ds_read_b64_tr_b16 a[174:175], v245 offset:34816\n"
    "v_mfma_f32_16x16x32_bf16 v[144:147], a[184:187], a[24:27], v[144:147]\n"
    "buffer_load_dwordx4 v251, s[8:11], s31 offen offset:0 sc1 lds\n"
    "ds_read2st64_b32 v[176:177], v249 offset0:16 offset1:17\n"
    "v_mfma_f32_16x16x32_bf16 v[144:147], a[188:191], a[28:31], v[144:147]\n"
    "s_mov_b32 m0, s63\n"
    "ds_read_b64_tr_b16 a[176:177], v246 offset:0\n"
    "ds_read_b64_tr_b16 a[178:179], v246 offset:2048\n"
    "v_mfma_f32_16x16x32_bf16 v[152:155], a[188:191], a[60:63], v[152:155]\n"
    "ds_read2st64_b32 v[178:179], v249 offset0:18 offset1:19\n"
    "v_exp_f32_e32 v150, v150\n"
    "v_mfma_f32_16x16x32_bf16 v[152:155], a[184:187], a[56:59], v[152:155]\n"
    "s_waitcnt lgkmcnt(8)\n"
    "ds_read_b64_tr_b16 a[180:181], v246 offset:32768\n"
    "ds_read_b64_tr_b16 a[182:183], v246 offset:34816\n"
    "v_mfma_f32_16x16x32_bf16 a[240:243], a[160:163], v[204:207], a[240:243]\n"
    "s_addk_i32 s31, 0x1000\n"
    "ds_read_b64_tr_b16 a[184:185], v247 offset:0\n"
    "ds_write_addtid_b32 v140 offset:0\n"
    "v_mfma_f32_16x16x32_bf16 a[240:243], a[164:167], v[208:211], a[240:243]\n"
    "s_add_u32 m0, s55, 29696\n"
    "ds_read2st64_b32 v[216:217], v249 offset0:20 offset1:21\n"
    "v_exp_f32_e32 v151, v151\n"
    "v_mfma_f32_16x16x32_bf16 a[236:239], a[164:167], v[200:203], a[236:239]\n"
    "buffer_load_dwordx4 v251, s[8:11], s31 offen offset:0 sc1 lds\n"
    "ds_read_b64_tr_b16 a[186:187], v247 offset:2048\n"
    "v_mfma_f32_16x16x32_bf16 a[236:239], a[160:163], v[196:199], a[236:239]\n"
    "s_waitcnt lgkmcnt(10)\n"
    "ds_read_b64_tr_b16 a[188:189], v247 offset:32768\n"
    "ds_read2st64_b32 v[218:219], v249 offset0:22 offset1:23\n"
    "v_mfma_f32_16x16x32_bf16 v[0:3], a[168:171], v[196:199], v[0:3]\n"
    "s_mov_b32 m0, s63\n"
    "ds_read_b64_tr_b16 a[190:191], v247 offset:34816\n"
    "ds_read_b64_tr_b16 a[160:161], v244 offset:4096\n"
    "v_mfma_f32_16x16x32_bf16 v[0:3], a[172:175], v[200:203], v[0:3]\n"
    "buffer_load_dwordx4 v[72:75], v250, s[8:11], s62 offen offset:2048 sc1\n"
    "ds_read_b64_tr_b16 a[162:163], v244 offset:6144\n"
    "v_mfma_f32_16x16x32_bf16 v[4:7], a[172:175], v[208:211], v[4:7]\n"
    "s_add_u32 s31, s46, s36\n"
    "ds_read_b64_tr_b16 a[164:165], v244 offset:36864\n"
    "v_cvt_pk_bf16_f32 v148, v148, v149\n"
    "v_mfma_f32_16x16x32_bf16 v[4:7], a[168:171], v[204:207], v[4:7]\n"
    "s_waitcnt lgkmcnt(10)\n"
    "ds_read_b64_tr_b16 a[166:167], v244 offset:38912\n"
    "v_exp_f32_e32 v144, v144\n"
    "v_mfma_f32_16x16x32_bf16 v[24:27], a[176:179], v[204:207], v[24:27]\n"
    "s_add_u32 m0, s55, 30720\n"
    "s_waitcnt vmcnt(6)\n"
    "ds_read_b64_tr_b16 a[168:169], v245 offset:4096\n"
    "v_subrev_u32_e32 v253, s39, v253\n"
    "v_mfma_f32_16x16x32_bf16 v[24:27], a[180:183], v[208:211], v[24:27]\n"
    "buffer_load_dwordx4 v251, s[12:15], s31 offen offset:0 lds\n"
    "ds_read_b64_tr_b16 a[170:171], v245 offset:6144\n"
    "v_cmp_le_u32_e32 vcc, 1, v253\n"
    "v_min_u32_e32 v220, 0x13c, v248\n"
    "v_mfma_f32_16x16x32_bf16 v[20:23], a[180:183], v[200:203], v[20:23]\n"
    "s_mov_b32 m0, s63\n"
    "ds_read_b64_tr_b16 a[172:173], v245 offset:36864\n"
    "ds_read_b64_tr_b16 a[174:175], v245 offset:38912\n"
    "v_mfma_f32_16x16x32_bf16 v[20:23], a[176:179], v[196:199], v[20:23]\n"
    "s_waitcnt lgkmcnt(8)\n"
    "v_exp_f32_e32 v145, v145\n"
    "v_mfma_f32_16x16x32_bf16 v[40:43], a[184:187], v[196:199], v[40:43]\n"
    "s_cbranch_vccnz .Lat_k\n"
    ".Lat_kr:\n"
    ".Lat_kxr:\n"
    "v_mfma_f32_16x16x32_bf16 v[40:43], a[188:191], v[200:203], v[40:43]\n"
    "s_addk_i32 s31, 0x1000\n"
    "ds_read_b64_tr_b16 a[176:177], v246 offset:4096\n"
    "ds_read_b64_tr_b16 a[178:179], v246 offset:6144\n"
    "v_mfma_f32_16x16x32_bf16 v[44:47], a[188:191], v[208:211], v[44:47]\n"
    "s_add_u32 m0, s55, 31744\n"
    "ds_read_b64_tr_b16 a[180:181], v246 offset:36864\n"
    "v_cvt_pk_bf16_f32 v149, v150, v151\n"
    "v_mfma_f32_16x16x32_bf16 v[44:47], a[184:187], v[204:207], v[44:47]\n"
    "s_waitcnt lgkmcnt(7)\n"
    "v_exp_f32_e32 v146, v146\n"
    "v_mfma_f32_16x16x32_bf16 v[64:67], a[160:163], v[204:207], v[64:67]\n"
    "buffer_load_dwordx4 v251, s[12:15], s31 offen offset:0 lds\n"
    "ds_read_b64_tr_b16 a[182:183], v246 offset:38912\n"
    "v_mfma_f32_16x16x32_bf16 v[64:67], a[164:167], v[208:211], v[64:67]\n"
    "s_mov_b32 m0, s63\n"
    "ds_read_b64_tr_b16 a[184:185], v247 offset:4096\n"
    "v_exp_f32_e32 v147, v147\n"
    "v_mfma_f32_16x16x32_bf16 v[60:63], a[164:167], v[200:203], v[60:63]\n"
    "buffer_load_dwordx4 a[0:3], v250, s[8:11], s87 offen offset:0 sc1\n"
    "ds_read_b64_tr_b16 a[186:187], v247 offset:6144\n"
    "v_mfma_f32_16x16x32_bf16 v[60:63], a[160:163], v[196:199], v[60:63]\n"
    "s_waitcnt lgkmcnt(6)\n"
    "ds_read_b64_tr_b16 a[188:189], v247 offset:36864\n"
    "ds_read_b64_tr_b16 a[190:191], v247 offset:38912\n"
    "v_mfma_f32_16x16x32_bf16 v[80:83], a[168:171], v[196:199], v[80:83]\n"
    "ds_write_addtid_b32 v141 offset:256\n"
    "v_cvt_pk_bf16_f32 v142, v144, v145\n"
    "v_mfma_f32_16x16x32_bf16 v[80:83], a[172:175], v[200:203], v[80:83]\n"
    "buffer_load_dwordx4 v[92:95], v250, s[8:11], s62 offen offset:3072 sc1\n"
    "ds_read_b64_tr_b16 a[160:161], v244 offset:12288\n"
    "v_mfma_f32_16x16x32_bf16 v[84:87], a[172:175], v[208:211], v[84:87]\n"
    "ds_read_b64_tr_b16 a[162:163], v244 offset:14336\n"
    "v_exp_f32_e32 v152, v152\n"
    "v_mfma_f32_16x16x32_bf16 v[84:87], a[168:171], v[204:207], v[84:87]\n"
    "s_waitcnt lgkmcnt(7)\n"
    "ds_read_b64_tr_b16 a[164:165], v244 offset:45056\n"
    "ds_read_b64_tr_b16 a[166:167], v244 offset:47104\n"
    "v_mfma_f32_16x16x32_bf16 v[104:107], a[176:179], v[204:207], v[104:107]\n"
    "ds_read_b64_tr_b16 a[168:169], v245 offset:12288\n"
    "ds_read_b64_tr_b16 a[170:171], v245 offset:14336\n"
    "v_mfma_f32_16x16x32_bf16 v[104:107], a[180:183], v[208:211], v[104:107]\n"
    "buffer_load_dwordx4 a[4:7], v250, s[8:11], s88 offen offset:0 sc1\n"
    "ds_read_b64_tr_b16 a[172:173], v245 offset:45056\n"
    "v_mfma_f32_16x16x32_bf16 v[100:103], a[180:183], v[200:203], v[100:103]\n"
    "ds_read_b64_tr_b16 a[174:175], v245 offset:47104\n"
    "v_exp_f32_e32 v153, v153\n"
    "v_mfma_f32_16x16x32_bf16 v[100:103], a[176:179], v[196:199], v[100:103]\n"
    "s_waitcnt lgkmcnt(9)\n"
    "ds_write_addtid_b32 v148 offset:1024\n"
    "ds_write_addtid_b32 v149 offset:1280\n"
    "v_mfma_f32_16x16x32_bf16 v[120:123], a[184:187], v[196:199], v[120:123]\n"
    "ds_read_b64_tr_b16 a[176:177], v246 offset:12288\n"
    "v_cvt_pk_bf16_f32 v143, v146, v147\n"
    "v_mfma_f32_16x16x32_bf16 v[120:123], a[188:191], v[200:203], v[120:123]\n"
    "ds_read_b64_tr_b16 a[178:179], v246 offset:14336\n"
    "v_exp_f32_e32 v154, v154\n"
    "v_mfma_f32_16x16x32_bf16 v[124:127], a[188:191], v[208:211], v[124:127]\n"
    "ds_write_addtid_b32 v142 offset:512\n"
    "v_exp_f32_e32 v155, v155\n"
    "v_mfma_f32_16x16x32_bf16 v[124:127], a[184:187], v[204:207], v[124:127]\n"
    "s_waitcnt lgkmcnt(9)\n"
    "ds_read_b64_tr_b16 a[180:181], v246 offset:45056\n"
    "ds_read_b64_tr_b16 a[184:185], v247 offset:12288\n"
    "v_mfma_f32_16x16x32_bf16 v[64:67], a[160:163], v[212:215], v[64:67]\n"
    "buffer_load_dwordx4 a[8:11], v250, s[8:11], s89 offen offset:0 sc1\n"
    "ds_read_b64_tr_b16 a[182:183], v246 offset:47104\n"
    "v_mfma_f32_16x16x32_bf16 v[64:67], a[164:167], v[216:219], v[64:67]\n"
    "ds_read_b64_tr_b16 a[186:187], v247 offset:14336\n"
    "ds_read_b64_tr_b16 a[188:189], v247 offset:45056\n"
    "v_mfma_f32_16x16x32_bf16 v[60:63], a[164:167], v[176:179], v[60:63]\n"
    "ds_write_addtid_b32 v143 offset:768\n"
    "v_cvt_pk_bf16_f32 v150, v152, v153\n"
    "v_mfma_f32_16x16x32_bf16 v[60:63], a[160:163], v[172:175], v[60:63]\n"
    "s_waitcnt lgkmcnt(11)\n"
    "ds_read_b64_tr_b16 a[190:191], v247 offset:47104\n"
    "v_cvt_pk_bf16_f32 v151, v154, v155\n"
    "v_mfma_f32_16x16x32_bf16 v[80:83], a[168:171], v[172:175], v[80:83]\n"
    "ds_write_addtid_b32 v150 offset:1536\n"
    "ds_write_addtid_b32 v151 offset:1792\n"
    "v_mfma_f32_16x16x32_bf16 v[80:83], a[172:175], v[176:179], v[80:83]\n"
    "ds_read_b64_tr_b16 a[160:161], v244 offset:8192\n"
    "ds_read_b64_tr_b16 a[162:163], v244 offset:10240\n"
    "v_mfma_f32_16x16x32_bf16 v[84:87], a[172:175], v[216:219], v[84:87]\n"
    "ds_read_b64_tr_b16 a[164:165], v244 offset:40960\n"
    "ds_read_b64_tr_b16 a[166:167], v244 offset:43008\n"
    "v_mfma_f32_16x16x32_bf16 v[84:87], a[168:171], v[212:215], v[84:87]\n"
    "s_waitcnt lgkmcnt(10)\n"
    "s_waitcnt lgkmcnt(4)\n"
    "s_waitcnt vmcnt(7)\n"
    "v_mfma_f32_16x16x32_bf16 v[104:107], a[176:179], v[212:215], v[104:107]\n"
    "s_barrier\n"
    "v_mfma_f32_16x16x32_bf16 v[104:107], a[180:183], v[216:219], v[104:107]\n"
    "buffer_load_dword v220, v220, s[8:11], s60 offen offset:192 sc1\n"
    "ds_read_b64_tr_b16 a[168:169], v244 offset:16384\n"
    "v_mfma_f32_16x16x32_bf16 v[100:103], a[180:183], v[176:179], v[100:103]\n"
    "s_add_u32 s31, s46, s37\n"
    "ds_read2st64_b32 v[144:145], v249 offset0:0 offset1:1\n"
    "ds_read2st64_b32 v[146:147], v249 offset0:2 offset1:3\n"
    "v_mfma_f32_16x16x32_bf16 v[100:103], a[176:179], v[172:175], v[100:103]\n"
    "ds_read_b64_tr_b16 a[170:171], v244 offset:18432\n"
    "ds_read_b64_tr_b16 a[172:173], v244 offset:49152\n"
    "v_mfma_f32_16x16x32_bf16 v[120:123], a[184:187], v[172:175], v[120:123]\n"
    "s_add_u32 m0, s55, 4096\n"
    "ds_read_b64_tr_b16 a[174:175], v244 offset:51200\n"
    "ds_read2st64_b32 v[152:153], v249 offset0:4 offset1:5\n"
    "v_mfma_f32_16x16x32_bf16 v[120:123], a[188:191], v[176:179], v[120:123]\n"
    "buffer_load_dwordx4 v251, s[12:15], s31 offen offset:0 lds\n"
    "ds_read2st64_b32 v[154:155], v249 offset0:6 offset1:7\n"
    "v_mfma_f32_16x16x32_bf16 v[124:127], a[188:191], v[216:219], v[124:127]\n"
    "ds_read_b64_tr_b16 a[176:177], v245 offset:8192\n"
    "ds_read_b64_tr_b16 a[178:179], v245 offset:10240\n"
    "v_mfma_f32_16x16x32_bf16 v[124:127], a[184:187], v[212:215], v[124:127]\n"
    "s_waitcnt lgkmcnt(10)\n"
    "ds_read_b64_tr_b16 a[180:181], v245 offset:40960\n"
    "ds_read_b64_tr_b16 a[182:183], v245 offset:43008\n"
    "v_mfma_f32_16x16x32_bf16 a[240:243], a[160:163], v[212:215], a[240:243]\n"
    "buffer_load_dwordx4 a[12:15], v250, s[8:11], s87 offen offset:1024 sc1\n"
    "v_mfma_f32_16x16x32_bf16 a[240:243], a[164:167], v[216:219], a[240:243]\n"
    "s_addk_i32 s31, 0x1000\n"
    "ds_read_b64_tr_b16 a[184:185], v245 offset:16384\n"
    "v_add_u32_e32 v240, s45, v240\n"
    "v_mfma_f32_16x16x32_bf16 a[236:239], a[164:167], v[176:179], a[236:239]\n"
    "s_add_u32 m0, s55, 5120\n"
    "ds_read_b64_tr_b16 a[186:187], v245 offset:18432\n"
    "v_xor_b32_e32 v241, 64, v240\n"
    "v_mfma_f32_16x16x32_bf16 a[236:239], a[160:163], v[172:175], a[236:239]\n"
    "s_waitcnt lgkmcnt(6)\n"
    "ds_read_b64_tr_b16 a[188:189], v245 offset:49152\n"
    "ds_read_b64_tr_b16 a[190:191], v245 offset:51200\n"
    "v_mfma_f32_16x16x32_bf16 a[236:239], a[168:171], v[140:143], a[236:239]\n"
    "buffer_load_dwordx4 v251, s[12:15], s31 offen offset:0 lds\n"
    "v_mfma_f32_16x16x32_bf16 a[236:239], a[172:175], v[144:147], a[236:239]\n"
    "ds_read_b64_tr_b16 a[160:161], v246 offset:8192\n"
    "v_add_u32_e32 v242, 0x10000, v240\n"
    "v_mfma_f32_16x16x32_bf16 a[240:243], a[172:175], v[152:155], a[240:243]\n"
    "ds_read_b64_tr_b16 a[162:163], v246 offset:10240\n"
    "ds_read_b64_tr_b16 a[164:165], v246 offset:40960\n"
    "v_mfma_f32_16x16x32_bf16 a[240:243], a[168:171], v[148:151], a[240:243]\n"
    "s_waitcnt lgkmcnt(7)\n"
    "ds_read_b64_tr_b16 a[166:167], v246 offset:43008\n"
    "v_add_u32_e32 v243, 0x10000, v241\n"
    "v_mfma_f32_16x16x32_bf16 v[4:7], a[176:179], v[212:215], v[4:7]\n"
    "buffer_load_dwordx4 a[16:19], v250, s[8:11], s88 offen offset:1024 sc1\n"
    "v_mfma_f32_16x16x32_bf16 v[4:7], a[180:183], v[216:219], v[4:7]\n"
    "ds_read_b64_tr_b16 a[168:169], v246 offset:16384\n"
    "v_mfma_f32_16x16x32_bf16 v[0:3], a[180:183], v[176:179], v[0:3]\n"
    "ds_read_b64_tr_b16 a[170:171], v246 offset:18432\n"
    "v_add_u32_e32 v252, s54, v240\n"
    "v_mfma_f32_16x16x32_bf16 v[0:3], a[176:179], v[172:175], v[0:3]\n"
    "s_waitcnt lgkmcnt(6)\n"
    "ds_read_b64_tr_b16 a[172:173], v246 offset:49152\n"
    "v_xor_b32_e32 v255, 64, v252\n"
    "v_mfma_f32_16x16x32_bf16 v[0:3], a[184:187], v[140:143], v[0:3]\n"
    "ds_read_b64_tr_b16 a[174:175], v246 offset:51200\n"
    "v_lshlrev_b32_e32 v228, 16, v228\n"
    "v_mfma_f32_16x16x32_bf16 v[0:3], a[188:191], v[144:147], v[0:3]\n"
    "ds_read_b64_tr_b16 a[176:177], v247 offset:8192\n"
    "v_lshlrev_b32_e32 v229, 16, v229\n"
    "v_mfma_f32_16x16x32_bf16 v[4:7], a[188:191], v[152:155], v[4:7]\n"
    "ds_read_b64_tr_b16 a[178:179], v247 offset:10240\n"
    "v_lshlrev_b32_e32 v230, 16, v230\n"
    "v_mfma_f32_16x16x32_bf16 v[4:7], a[184:187], v[148:151], v[4:7]\n"
    "s_waitcnt lgkmcnt(6)\n"
    "ds_read_b64_tr_b16 a[180:181], v247 offset:40960\n"
    "ds_read_b64_tr_b16 a[182:183], v247 offset:43008\n"
    "v_mfma_f32_16x16x32_bf16 v[24:27], a[160:163], v[212:215], v[24:27]\n"
    "buffer_load_dwordx4 a[20:23], v250, s[8:11], s89 offen offset:1024 sc1\n"
    "v_mfma_f32_16x16x32_bf16 v[24:27], a[164:167], v[216:219], v[24:27]\n"
    "ds_read_b64_tr_b16 a[184:185], v247 offset:16384\n"
    "v_cvt_pk_bf16_f32 v7, v6, v7\n"
    "v_mfma_f32_16x16x32_bf16 v[20:23], a[164:167], v[176:179], v[20:23]\n"
    "ds_read_b64_tr_b16 a[186:187], v247 offset:18432\n"
    "v_cvt_pk_bf16_f32 v6, v4, v5\n"
    "v_mfma_f32_16x16x32_bf16 v[20:23], a[160:163], v[172:175], v[20:23]\n"
    "s_waitcnt lgkmcnt(6)\n"
    "ds_read_b64_tr_b16 a[160:161], v244 offset:20480\n"
    "v_accvgpr_read_b32 v4, a240\n"
    "v_mfma_f32_16x16x32_bf16 v[20:23], a[168:171], v[140:143], v[20:23]\n"
    "ds_read_b64_tr_b16 a[188:189], v247 offset:49152\n"
    "ds_read_b64_tr_b16 a[190:191], v247 offset:51200\n"
    "v_mfma_f32_16x16x32_bf16 v[20:23], a[172:175], v[144:147], v[20:23]\n"
    "ds_read_b64_tr_b16 a[162:163], v244 offset:22528\n"
    "v_mfma_f32_16x16x32_bf16 v[24:27], a[172:175], v[152:155], v[24:27]\n"
    "ds_read_b64_tr_b16 a[164:165], v244 offset:53248\n"
    "v_accvgpr_read_b32 v5, a241\n"
    "v_mfma_f32_16x16x32_bf16 v[24:27], a[168:171], v[148:151], v[24:27]\n"
    "s_waitcnt lgkmcnt(7)\n"
    "ds_read_b64_tr_b16 a[168:169], v245 offset:20480\n"
    "v_accvgpr_read_b32 v225, a243\n"
    "v_mfma_f32_16x16x32_bf16 v[44:47], a[176:179], v[212:215], v[44:47]\n"
    "ds_read_b64_tr_b16 a[166:167], v244 offset:55296\n"
    "ds_read_b64_tr_b16 a[170:171], v245 offset:22528\n"
    "v_mfma_f32_16x16x32_bf16 v[44:47], a[180:183], v[216:219], v[44:47]\n"
    "ds_read_b64_tr_b16 a[172:173], v245 offset:53248\n"
    "ds_read_b64_tr_b16 a[174:175], v245 offset:55296\n"
    "v_mfma_f32_16x16x32_bf16 v[40:43], a[180:183], v[176:179], v[40:43]\n"
    "v_cvt_pk_bf16_f32 v4, v4, v5\n"
    "v_mfma_f32_16x16x32_bf16 v[40:43], a[176:179], v[172:175], v[40:43]\n"
    "s_waitcnt lgkmcnt(7)\n"
    "ds_read_b64_tr_b16 a[176:177], v246 offset:20480\n"
    "v_accvgpr_read_b32 v5, a242\n"
    "v_mfma_f32_16x16x32_bf16 v[40:43], a[184:187], v[140:143], v[40:43]\n"
    "ds_read_b64_tr_b16 a[178:179], v246 offset:22528\n"
    "ds_read_b64_tr_b16 a[180:181], v246 offset:53248\n"
    "v_mfma_f32_16x16x32_bf16 v[40:43], a[188:191], v[144:147], v[40:43]\n"
    "ds_read_b64_tr_b16 a[182:183], v246 offset:55296\n"
    "v_cvt_pk_bf16_f32 v5, v5, v225\n"
    "v_mfma_f32_16x16x32_bf16 v[44:47], a[188:191], v[152:155], v[44:47]\n"
    "buffer_store_dwordx4 v[4:7], v250, s[8:11], s80 offen offset:0 sc1\n"
    "v_mfma_f32_16x16x32_bf16 v[44:47], a[184:187], v[148:151], v[44:47]\n"
    "s_waitcnt lgkmcnt(7)\n"
    "ds_read_b64_tr_b16 a[184:185], v247 offset:20480\n"
    "v_mfma_f32_16x16x32_bf16 v[64:67], a[160:163], v[148:151], v[64:67]\n"
    "ds_read_b64_tr_b16 a[186:187], v247 offset:22528\n"
    "v_mfma_f32_16x16x32_bf16 v[64:67], a[164:167], v[152:155], v[64:67]\n"
    "s_waitcnt lgkmcnt(6)\n"
    "ds_read_b64_tr_b16 a[188:189], v247 offset:53248\n"
    "ds_read_b64_tr_b16 a[190:191], v247 offset:55296\n"
    "v_mfma_f32_16x16x32_bf16 v[84:87], a[172:175], v[152:155], v[84:87]\n"
    "ds_read_b128 a[48:51], v240 offset:0\n"
    "v_cvt_pk_bf16_f32 v24, v24, v25\n"
    "v_mfma_f32_16x16x32_bf16 v[84:87], a[168:171], v[148:151], v[84:87]\n"
    "s_waitcnt lgkmcnt(5)\n"
    "ds_read_b128 a[52:55], v241 offset:0\n"
    "v_cvt_pk_bf16_f32 v25, v26, v27\n"
    "v_mfma_f32_16x16x32_bf16 v[104:107], a[176:179], v[148:151], v[104:107]\n"
    "v_cvt_pk_bf16_f32 v26, v44, v45\n"
    "v_mfma_f32_16x16x32_bf16 v[104:107], a[180:183], v[152:155], v[104:107]\n"
    "s_waitcnt lgkmcnt(2)\n"
    "ds_read_b128 a[56:59], v240 offset:32768\n"
    "v_cvt_pk_bf16_f32 v27, v46, v47\n"
    "v_mfma_f32_16x16x32_bf16 v[124:127], a[188:191], v[152:155], v[124:127]\n"
    "ds_read_b128 a[60:63], v241 offset:32768\n"
    "v_mfma_f32_16x16x32_bf16 v[124:127], a[184:187], v[148:151], v[124:127]\n"
    "buffer_store_dwordx4 v[24:27], v250, s[8:11], s80 offen offset:1024 sc1\n"
    "v_mfma_f32_16x16x32_bf16 v[60:63], a[164:167], v[144:147], v[60:63]\n"
    "v_cvt_pk_bf16_f32 v64, v64, v65\n"
    "v_mfma_f32_16x16x32_bf16 v[60:63], a[160:163], v[140:143], v[60:63]\n"
    "v_cvt_pk_bf16_f32 v65, v66, v67\n"
    "v_mfma_f32_16x16x32_bf16 v[80:83], a[168:171], v[140:143], v[80:83]\n"
    "v_cvt_pk_bf16_f32 v66, v84, v85\n"
    "v_cvt_pk_bf16_f32 v67, v86, v87\n"
    "v_mfma_f32_16x16x32_bf16 v[80:83], a[172:175], v[144:147], v[80:83]\n"
    "buffer_store_dwordx4 v[64:67], v250, s[8:11], s80 offen offset:2048 sc1\n"
    "v_mfma_f32_16x16x32_bf16 v[100:103], a[180:183], v[144:147], v[100:103]\n"
    "s_mov_b32 s45, 0x71800000\n"
    "v_cvt_pk_bf16_f32 v104, v104, v105\n"
    "v_cvt_pk_bf16_f32 v105, v106, v107\n"
    "v_mfma_f32_16x16x32_bf16 v[100:103], a[176:179], v[140:143], v[100:103]\n"
    "v_cvt_pk_bf16_f32 v106, v124, v125\n"
    "v_cvt_pk_bf16_f32 v107, v126, v127\n"
    "v_mfma_f32_16x16x32_bf16 v[120:123], a[184:187], v[140:143], v[120:123]\n"
    "buffer_store_dwordx4 v[104:107], v250, s[8:11], s80 offen offset:3072 sc1\n"
    "v_mfma_f32_16x16x32_bf16 v[120:123], a[188:191], v[144:147], v[120:123]\n"
    "s_waitcnt lgkmcnt(2)\n"
    "buffer_store_dword v127, v231, s[8:11], s86 offen offset:0 sc1\n"
    "v_mfma_f32_16x16x32_bf16 v[156:159], a[216:219], a[48:51], 0\n"
    "ds_read_b128 a[160:163], v242 offset:0\n"
    "v_mfma_f32_16x16x32_bf16 v[160:163], a[220:223], a[48:51], 0\n"
    "ds_read_b128 a[164:167], v243 offset:0\n"
    "v_mfma_f32_16x16x32_bf16 v[164:167], a[224:227], a[48:51], 0\n"
    "ds_read_b128 a[168:171], v242 offset:32768\n"
    "v_add_f32_e32 v225, v123, v127\n"
    "v_mfma_f32_16x16x32_bf16 v[168:171], a[228:231], a[48:51], 0\n"
    "ds_read_b128 a[172:175], v243 offset:32768\n"
    "v_min3_f32 v129, v129, v123, v127\n"
    "v_mfma_f32_16x16x32_bf16 v[156:159], a[232:235], a[52:55], v[156:159]\n"
    "ds_read_b128 a[176:179], v240 offset:2048\n"
    "v_add_f32_e32 v225, v225, v128\n"
    "v_mfma_f32_16x16x32_bf16 v[160:163], v[192:195], a[52:55], v[160:163]\n"
    "ds_read_b128 a[180:183], v241 offset:2048\n"
    "v_mov_b32_e32 v227, v123\n"
    "v_mfma_f32_16x16x32_bf16 v[164:167], a[64:67], a[52:55], v[164:167]\n"
    "ds_read_b128 a[184:187], v240 offset:34816\n"
    "v_mfma_f32_16x16x32_bf16 v[168:171], a[68:71], a[52:55], v[168:171]\n"
    "s_waitcnt lgkmcnt(7)\n"
    "ds_read_b128 a[188:191], v241 offset:34816\n"
    "v_cmp_nlt_f32_e32 vcc, 0x18000000, v129\n"
    "v_mfma_f32_16x16x32_bf16 v[156:159], a[72:75], a[56:59], v[156:159]\n"
    "ds_read_b128 a[48:51], v242 offset:2048\n"
    "v_cmp_nlt_f32_e64 s[28:29], v225, s45\n"
    "v_mfma_f32_16x16x32_bf16 v[160:163], a[76:79], a[56:59], v[160:163]\n"
    "buffer_load_dwordx4 a[24:27], v250, s[8:11], s87 offen offset:2048 sc1\n"
    "v_mfma_f32_16x16x32_bf16 v[164:167], a[80:83], a[56:59], v[164:167]\n"
    "s_or_b32 s45, s29, vcc_hi\n"
    "v_mfma_f32_16x16x32_bf16 v[168:171], a[84:87], a[56:59], v[168:171]\n"
    "s_lshr_b32 s45, s45, 16\n"
    "s_cselect_b32 s45, s51, 0\n"
    "v_mfma_f32_16x16x32_bf16 v[156:159], a[88:91], a[60:63], v[156:159]\n"
    "s_andn2_b32 s45, s45, s49\n"
    "ds_read_b128 a[52:55], v243 offset:2048\n"
    "v_mfma_f32_16x16x32_bf16 v[160:163], a[92:95], a[60:63], v[160:163]\n"
    "s_add_u32 s45, s45, s39\n"
    "v_mfma_f32_16x16x32_bf16 v[164:167], a[96:99], a[60:63], v[164:167]\n"
    "buffer_load_dwordx4 a[28:31], v250, s[8:11], s88 offen offset:2048 sc1\n"
    "v_mov_b32_e32 v254, s45\n"
    "v_mfma_f32_16x16x32_bf16 v[168:171], a[100:103], a[60:63], v[168:171]\n"
    "s_waitcnt lgkmcnt(8)\n"
    "v_mfma_f32_16x16x32_bf16 v[156:159], a[104:107], a[160:163], v[156:159]\n"
    "s_lshl1_add_u32 s55, s50, s51\n"
    "ds_read_b128 a[56:59], v242 offset:34816\n"
    ".long 0xbedf00ff\n"
    ".Lat_w13:\n"
    "s_setpc_b64 s[2:3]\n"
    "v_mfma_f32_16x16x32_bf16 v[160:163], a[108:111], a[160:163], v[160:163]\n"
    "s_lshl_b32 s55, s55, 12\n"
    "v_mfma_f32_16x16x32_bf16 v[164:167], a[112:115], a[160:163], v[164:167]\n"
    "s_xor_b32 s31, s93, 2\n"
    "v_mfma_f32_16x16x32_bf16 v[168:171], a[116:119], a[160:163], v[168:171]\n"
    "buffer_load_dwordx4 a[32:35], v250, s[8:11], s89 offen offset:2048 sc1\n"
    "v_mfma_f32_16x16x32_bf16 v[156:159], a[120:123], a[164:167], v[156:159]\n"
    "s_lshl_b32 s31, s31, 8\n"
    "ds_read_b128 a[60:63], v243 offset:34816\n"
    "v_mfma_f32_16x16x32_bf16 v[160:163], a[124:127], a[164:167], v[160:163]\n"
    "s_addk_i32 s31, 0x6000\n"
    "v_mfma_f32_16x16x32_bf16 v[164:167], a[128:131], a[164:167], v[164:167]\n"
    "s_mul_i32 s45, s49, 252\n"
    "v_mfma_f32_16x16x32_bf16 v[168:171], a[132:135], a[164:167], v[168:171]\n"
    "s_waitcnt lgkmcnt(8)\n"
    "v_mfma_f32_16x16x32_bf16 v[156:159], a[136:139], a[168:171], v[156:159]\n"
    "buffer_load_dwordx4 a[36:39], v250, s[8:11], s87 offen offset:3072 sc1\n"
    "ds_read_b128 a[160:163], v240 offset:4096\n"
    "v_mfma_f32_16x16x32_bf16 v[160:163], a[140:143], a[168:171], v[160:163]\n"
    "s_add_u32 s45, s45, s34\n"
    "v_and_b32_e32 v251, 0x7ff, v252\n"
    "v_mfma_f32_16x16x32_bf16 v[164:167], a[144:147], a[168:171], v[164:167]\n"
    "s_mul_i32 s45, s45, 101376\n"
    "v_mfma_f32_16x16x32_bf16 v[168:171], a[148:151], a[168:171], v[168:171]\n"
    "s_add_u32 s45, s45, 16654336\n"
    "v_add_u32_e32 v251, s63, v251\n"
    "v_mfma_f32_16x16x32_bf16 v[156:159], a[152:155], a[172:175], v[156:159]\n"
    "s_lshl_b32 s47, s93, 11\n"
    "ds_read_b128 a[164:167], v241 offset:4096\n"
    "v_mfma_f32_16x16x32_bf16 v[160:163], a[156:159], a[172:175], v[160:163]\n"
    "buffer_load_dwordx4 a[40:43], v250, s[8:11], s88 offen offset:3072 sc1\n"
    "v_mfma_f32_16x16x32_bf16 v[164:167], v[180:183], a[172:175], v[164:167]\n"
    "s_add_u32 s54, s47, s45\n"
    "v_mfma_f32_16x16x32_bf16 v[168:171], v[184:187], a[172:175], v[168:171]\n"
    "s_waitcnt lgkmcnt(8)\n"
    "v_mfma_f32_16x16x32_bf16 v[196:199], a[216:219], a[176:179], 0\n"
    "s_add_u32 s45, s45, 32768\n"
    "ds_read_b128 a[168:171], v240 offset:36864\n"
    "v_mfma_f32_16x16x32_bf16 v[200:203], a[220:223], a[176:179], 0\n"
    "s_cmp_lt_u32 s32, 2\n"
    "s_cselect_b32 s54, s54, 2.0\n"
    "s_cselect_b32 s45, s45, 2.0\n"
    "v_mfma_f32_16x16x32_bf16 v[204:207], a[224:227], a[176:179], 0\n"
    "buffer_load_dwordx4 a[44:47], v250, s[8:11], s89 offen offset:3072 sc1\n"
    "v_mfma_f32_16x16x32_bf16 v[208:211], a[228:231], a[176:179], 0\n"
    "s_add_u32 s80, s35, s46\n"
    "v_mul_f32_e32 v156, v156, v228\n"
    "v_mfma_f32_16x16x32_bf16 v[196:199], a[232:235], a[180:183], v[196:199]\n"
    "s_add_u32 s82, s36, s46\n"
    "ds_read_b128 a[172:175], v241 offset:36864\n"
    "v_mfma_f32_16x16x32_bf16 v[200:203], v[192:195], a[180:183], v[200:203]\n"
    "s_add_u32 s28, s37, s46\n"
    "v_xor_b32_e32 v249, 64, v251\n"
    "v_mfma_f32_16x16x32_bf16 v[204:207], a[64:67], a[180:183], v[204:207]\n"
    "v_add_u32_e32 v244, s55, v250\n"
    "v_mfma_f32_16x16x32_bf16 v[208:211], a[68:71], a[180:183], v[208:211]\n"
    "s_waitcnt lgkmcnt(8)\n"
    "v_mfma_f32_16x16x32_bf16 v[196:199], a[72:75], a[184:187], v[196:199]\n"
    "ds_read_b128 a[176:179], v242 offset:4096\n"
    "v_mul_f32_e32 v157, v157, v228\n"
    "v_mfma_f32_16x16x32_bf16 v[200:203], a[76:79], a[184:187], v[200:203]\n"
    "v_lshrrev_b32_e32 v247, 2, v250\n"
    "v_add_u32_e32 v247, s31, v247\n"
    "v_mfma_f32_16x16x32_bf16 v[204:207], a[80:83], a[184:187], v[204:207]\n"
    "s_waitcnt vmcnt(6)\n"
    "buffer_store_dword v254, off, s[8:11], s90 offset:0 sc1\n"
    "v_mfma_f32_16x16x32_bf16 v[208:211], a[84:87], a[184:187], v[208:211]\n"
    "v_add_u32_e32 v246, s47, v250\n"
    "v_mfma_f32_16x16x32_bf16 v[196:199], a[88:91], a[188:191], v[196:199]\n"
    "s_add_u32 s62, s58, 16384\n"
    "ds_read_b128 a[180:183], v243 offset:4096\n"
    "v_mul_f32_e32 v158, v158, v228\n"
    "v_mfma_f32_16x16x32_bf16 v[200:203], a[92:95], a[188:191], v[200:203]\n"
    "v_and_b32_e32 v245, 0x603f, v247\n"
    "v_mfma_f32_16x16x32_bf16 v[204:207], a[96:99], a[188:191], v[204:207]\n"
    "buffer_load_dwordx4 v[76:79], v250, s[8:11], s62 offen offset:0 sc1\n"
    "v_mfma_f32_16x16x32_bf16 v[208:211], a[100:103], a[188:191], v[208:211]\n"
    "s_waitcnt lgkmcnt(8)\n"
    "v_bfe_u32 v226, v250, 2, 4\n"
    "v_mfma_f32_16x16x32_bf16 v[196:199], a[104:107], a[48:51], v[196:199]\n"
    "ds_read_b128 a[184:187], v242 offset:36864\n"
    "v_mfma_f32_16x16x32_bf16 v[200:203], a[108:111], a[48:51], v[200:203]\n"
    "v_mul_f32_e32 v159, v159, v228\n"
    "v_mul_f32_e32 v160, v160, v228\n"
    "v_mfma_f32_16x16x32_bf16 v[204:207], a[112:115], a[48:51], v[204:207]\n"
    "s_waitcnt vmcnt(15)\n"
    "v_bfe_u32 v225, v250, 6, 2\n"
    "v_mfma_f32_16x16x32_bf16 v[208:211], a[116:119], a[48:51], v[208:211]\n"
    "v_lshl_or_b32 v226, v225, 7, v226\n"
    "v_and_b32_e32 v225, 0x60ff, v247\n"
    "v_mfma_f32_16x16x32_bf16 v[196:199], a[120:123], a[52:55], v[196:199]\n"
    "ds_read_b128 a[188:191], v243 offset:36864\n"
    "v_mfma_f32_16x16x32_bf16 v[200:203], a[124:127], a[52:55], v[200:203]\n"
    "buffer_load_dwordx4 v[96:99], v250, s[8:11], s62 offen offset:1024 sc1\n"
    "v_mfma_f32_16x16x32_bf16 v[204:207], a[128:131], a[52:55], v[204:207]\n"
    "v_mul_f32_e32 v161, v161, v228\n"
    "v_mfma_f32_16x16x32_bf16 v[208:211], a[132:135], a[52:55], v[208:211]\n"
    "s_waitcnt lgkmcnt(8)\n"
    "v_mul_f32_e32 v162, v162, v228\n"
    "v_mfma_f32_16x16x32_bf16 v[196:199], a[136:139], a[56:59], v[196:199]\n"
    "v_mfma_f32_16x16x32_bf16 v[200:203], a[140:143], a[56:59], v[200:203]\n"
    "buffer_load_dwordx4 a[192:195], v250, s[8:11], s62 offen offset:2048 sc1\n"
    "v_mfma_f32_16x16x32_bf16 v[204:207], a[144:147], a[56:59], v[204:207]\n"
    "v_mul_f32_e32 v163, v163, v228\n"
    "v_mfma_f32_16x16x32_bf16 v[208:211], a[148:151], a[56:59], v[208:211]\n"
    "v_mfma_f32_16x16x32_bf16 v[196:199], a[152:155], a[60:63], v[196:199]\n"
    "ds_read_b128 a[48:51], v251 offset:6144\n"
    "v_mfma_f32_16x16x32_bf16 v[200:203], a[156:159], a[60:63], v[200:203]\n"
    "buffer_load_dwordx4 a[196:199], v250, s[8:11], s62 offen offset:3072 sc1\n"
    "v_mfma_f32_16x16x32_bf16 v[204:207], v[180:183], a[60:63], v[204:207]\n"
    "s_add_u32 s62, s58, 20480\n"
    "v_mul_f32_e32 v164, v164, v228\n"
    "v_mfma_f32_16x16x32_bf16 v[208:211], v[184:187], a[60:63], v[208:211]\n"
    "s_waitcnt lgkmcnt(7)\n"
    "ds_read_b128 a[52:55], v249 offset:6144\n"
    "v_mul_f32_e32 v165, v165, v228\n"
    "v_mfma_f32_16x16x32_bf16 v[172:175], a[216:219], a[160:163], 0\n"
    "v_mfma_f32_16x16x32_bf16 v[176:179], a[220:223], a[160:163], 0\n"
    "buffer_load_dwordx4 a[200:203], v250, s[8:11], s62 offen offset:0 sc1\n"
    "v_mfma_f32_16x16x32_bf16 v[212:215], a[224:227], a[160:163], 0\n"
    "v_mfma_f32_16x16x32_bf16 v[216:219], a[228:231], a[160:163], 0\n"
    "v_mfma_f32_16x16x32_bf16 v[172:175], a[232:235], a[164:167], v[172:175]\n"
    "v_mul_f32_e32 v166, v166, v228\n"
    "v_mfma_f32_16x16x32_bf16 v[176:179], v[192:195], a[164:167], v[176:179]\n"
    "buffer_load_dwordx4 a[204:207], v250, s[8:11], s62 offen offset:1024 sc1\n"
    "v_mfma_f32_16x16x32_bf16 v[212:215], a[64:67], a[164:167], v[212:215]\n"
    "v_mfma_f32_16x16x32_bf16 v[216:219], a[68:71], a[164:167], v[216:219]\n"
    "s_waitcnt lgkmcnt(6)\n"
    "v_mfma_f32_16x16x32_bf16 v[172:175], a[72:75], a[168:171], v[172:175]\n"
    "v_mfma_f32_16x16x32_bf16 v[176:179], a[76:79], a[168:171], v[176:179]\n"
    "buffer_load_dwordx4 a[208:211], v250, s[8:11], s62 offen offset:2048 sc1\n"
    "v_mfma_f32_16x16x32_bf16 v[212:215], a[80:83], a[168:171], v[212:215]\n"
    "v_mfma_f32_16x16x32_bf16 v[216:219], a[84:87], a[168:171], v[216:219]\n"
    "v_mfma_f32_16x16x32_bf16 v[172:175], a[88:91], a[172:175], v[172:175]\n"
    "v_mul_f32_e32 v167, v167, v228\n"
    "v_mul_f32_e32 v168, v168, v228\n"
    "v_mfma_f32_16x16x32_bf16 v[176:179], a[92:95], a[172:175], v[176:179]\n"
    "buffer_load_dwordx4 a[212:215], v250, s[8:11], s62 offen offset:3072 sc1\n"
    "v_mfma_f32_16x16x32_bf16 v[212:215], a[96:99], a[172:175], v[212:215]\n"
    "s_add_u32 s62, s58, 24576\n"
    "v_mul_f32_e32 v169, v169, v228\n"
    "v_mfma_f32_16x16x32_bf16 v[216:219], a[100:103], a[172:175], v[216:219]\n"
    "s_waitcnt vmcnt(25)\n"
    "v_mul_f32_e32 v170, v170, v228\n"
    "v_mul_f32_e32 v171, v171, v228\n"
    "v_mfma_f32_16x16x32_bf16 a[236:239], v[234:237], a[0:3], a[236:239]\n"
    "v_mfma_f32_16x16x32_bf16 v[0:3], v[232:235], a[0:3], v[0:3]\n"
    "buffer_load_dwordx4 v[108:111], v250, s[8:11], s62 offen offset:0 sc1\n"
    "v_mfma_f32_16x16x32_bf16 a[236:239], v[234:237], a[4:7], a[236:239]\n"
    "v_cmp_le_u32_e32 vcc, 0x300, v250\n"
    "v_mfma_f32_16x16x32_bf16 v[0:3], v[232:235], a[4:7], v[0:3]\n"
    "v_mfma_f32_16x16x32_bf16 a[236:239], v[234:237], a[8:11], a[236:239]\n"
    "v_cndmask_b32_e32 v227, v220, v227, vcc\n"
    "v_mfma_f32_16x16x32_bf16 v[0:3], v[232:235], a[8:11], v[0:3]\n"
    "s_waitcnt lgkmcnt(0)\n"
    "s_waitcnt vmcnt(21)\n"
    "v_mfma_f32_16x16x32_bf16 v[20:23], v[234:237], a[12:15], v[20:23]\n"
    "s_barrier\n"
    "v_mfma_f32_16x16x32_bf16 v[40:43], v[232:235], a[12:15], v[40:43]\n"
    "buffer_load_dwordx4 v[112:115], v250, s[8:11], s62 offen offset:1024 sc1\n"
    "v_mfma_f32_16x16x32_bf16 v[20:23], v[234:237], a[16:19], v[20:23]\n"
    "v_accvgpr_read_b32 v64, a236\n"
    "v_accvgpr_read_b32 v65, a237\n"
    "v_mfma_f32_16x16x32_bf16 v[40:43], v[232:235], a[16:19], v[40:43]\n"
    "v_accvgpr_read_b32 v66, a238\n"
    "v_accvgpr_read_b32 v67, a239\n"
    "v_mfma_f32_16x16x32_bf16 v[20:23], v[234:237], a[20:23], v[20:23]\n"
    "ds_write_b32 v247, v227 offset:0\n"
    "v_mfma_f32_16x16x32_bf16 v[40:43], v[232:235], a[20:23], v[40:43]\n"
    "s_waitcnt vmcnt(14)\n"
    "buffer_load_dwordx4 v[132:135], v250, s[8:11], s62 offen offset:2048 sc1\n"
    "v_cvt_pk_bf16_f32 v64, v64, v65\n"
    "v_mfma_f32_16x16x32_bf16 v[60:63], v[234:237], a[24:27], v[60:63]\n"
    "v_cvt_pk_bf16_f32 v65, v66, v67\n"
    "v_cvt_pk_bf16_f32 v66, v0, v1\n"
    "v_cvt_pk_bf16_f32 v67, v2, v3\n"
    "v_mfma_f32_16x16x32_bf16 v[80:83], v[232:235], a[24:27], v[80:83]\n"
    "ds_write_b128 v244, v[64:67] offset:0\n"
    "v_mfma_f32_16x16x32_bf16 v[60:63], v[234:237], a[28:31], v[60:63]\n"
    "buffer_load_dword v253, v226, s[8:11], s61 offen offset:0 sc1\n"
    "v_mfma_f32_16x16x32_bf16 v[80:83], v[232:235], a[28:31], v[80:83]\n"
    "v_cvt_pk_bf16_f32 v20, v20, v21\n"
    "v_cvt_pk_bf16_f32 v21, v22, v23\n"
    "v_mfma_f32_16x16x32_bf16 v[60:63], v[234:237], a[32:35], v[60:63]\n"
    "buffer_load_dwordx4 v[116:119], v250, s[8:11], s62 offen offset:3072 sc1\n"
    "v_mfma_f32_16x16x32_bf16 v[80:83], v[232:235], a[32:35], v[80:83]\n"
    "s_add_u32 s62, s58, 28672\n"
    "s_waitcnt vmcnt(14)\n"
    "v_cvt_pk_bf16_f32 v22, v40, v41\n"
    "v_mfma_f32_16x16x32_bf16 v[100:103], v[234:237], a[36:39], v[100:103]\n"
    "v_cvt_pk_bf16_f32 v23, v42, v43\n"
    "v_mfma_f32_16x16x32_bf16 v[120:123], v[232:235], a[36:39], v[120:123]\n"
    "ds_write_b128 v244, v[20:23] offset:1024\n"
    "v_mfma_f32_16x16x32_bf16 v[100:103], v[234:237], a[40:43], v[100:103]\n"
    "buffer_load_dwordx4 v[136:139], v250, s[8:11], s62 offen offset:0 sc1\n"
    "v_mfma_f32_16x16x32_bf16 v[120:123], v[232:235], a[40:43], v[120:123]\n"
    "v_cvt_pk_bf16_f32 v60, v60, v61\n"
    "v_cvt_pk_bf16_f32 v61, v62, v63\n"
    "v_mfma_f32_16x16x32_bf16 v[100:103], v[234:237], a[44:47], v[100:103]\n"
    "v_cvt_pk_bf16_f32 v62, v80, v81\n"
    "v_cvt_pk_bf16_f32 v63, v82, v83\n"
    "v_mfma_f32_16x16x32_bf16 v[120:123], v[232:235], a[44:47], v[120:123]\n"
    "ds_write_b128 v244, v[60:63] offset:2048\n"
    "v_mfma_f32_16x16x32_bf16 v[172:175], a[104:107], a[176:179], v[172:175]\n"
    "buffer_load_dwordx4 a[240:243], v250, s[8:11], s62 offen offset:1024 sc1\n"
    "v_cvt_pk_bf16_f32 v100, v100, v101\n"
    "v_mfma_f32_16x16x32_bf16 v[176:179], a[108:111], a[176:179], v[176:179]\n"
    "v_cvt_pk_bf16_f32 v101, v102, v103\n"
    "v_cvt_pk_bf16_f32 v102, v120, v121\n"
    "v_cvt_pk_bf16_f32 v103, v122, v123\n"
    "v_mfma_f32_16x16x32_bf16 v[212:215], a[112:115], a[176:179], v[212:215]\n"
    "ds_write_b128 v244, v[100:103] offset:3072\n"
    "v_mfma_f32_16x16x32_bf16 v[216:219], a[116:119], a[176:179], v[216:219]\n"
    "v_mfma_f32_16x16x32_bf16 v[172:175], a[120:123], a[180:183], v[172:175]\n"
    "buffer_load_dwordx4 v[4:7], v250, s[8:11], s62 offen offset:2048 sc1\n"
    "v_mfma_f32_16x16x32_bf16 v[176:179], a[124:127], a[180:183], v[176:179]\n"
    "s_waitcnt lgkmcnt(0)\n"
    "v_mfma_f32_16x16x32_bf16 v[212:215], a[128:131], a[180:183], v[212:215]\n"
    "s_barrier\n"
    "v_mfma_f32_16x16x32_bf16 v[216:219], a[132:135], a[180:183], v[216:219]\n"
    "ds_read_b128 a[0:3], v250 offset:0\n"
    "ds_read_b128 a[4:7], v250 offset:1024\n"
    "ds_read_b128 a[8:11], v250 offset:2048\n"
    "v_mfma_f32_16x16x32_bf16 v[172:175], a[136:139], a[184:187], v[172:175]\n"
    "buffer_load_dwordx4 v[24:27], v250, s[8:11], s62 offen offset:3072 sc1\n"
    "ds_read_b128 a[12:15], v250 offset:3072\n"
    "v_mfma_f32_16x16x32_bf16 v[176:179], a[140:143], a[184:187], v[176:179]\n"
    "ds_read_b128 a[16:19], v250 offset:4096\n"
    "ds_read_b128 a[20:23], v250 offset:5120\n"
    "v_mfma_f32_16x16x32_bf16 v[212:215], a[144:147], a[184:187], v[212:215]\n"
    "ds_read_b128 a[24:27], v250 offset:6144\n"
    "ds_read_b128 a[28:31], v250 offset:7168\n"
    "ds_read2_b32 v[220:221], v245 offset0:0 offset1:16\n"
    "v_mfma_f32_16x16x32_bf16 v[216:219], a[148:151], a[184:187], v[216:219]\n"
    "ds_read2_b32 v[222:223], v245 offset0:32 offset1:48\n"
    "ds_read2_b32 v[80:81], v245 offset0:64 offset1:80\n"
    "ds_read2_b32 v[82:83], v245 offset0:96 offset1:112\n"
    "v_mfma_f32_16x16x32_bf16 v[172:175], a[152:155], a[188:191], v[172:175]\n"
    "v_mfma_f32_16x16x32_bf16 v[176:179], a[156:159], a[188:191], v[176:179]\n"
    "v_mfma_f32_16x16x32_bf16 v[212:215], v[180:183], a[188:191], v[212:215]\n"
    "v_mul_f32_e32 v196, v196, v229\n"
    "v_mul_f32_e32 v197, v197, v229\n"
    "v_mfma_f32_16x16x32_bf16 v[216:219], v[184:187], a[188:191], v[216:219]\n"
    "v_mul_f32_e32 v198, v198, v229\n"
    "v_mfma_f32_16x16x32_bf16 v[156:159], v[234:237], a[48:51], v[156:159]\n"
    "v_mul_f32_e32 v199, v199, v229\n"
    "v_mfma_f32_16x16x32_bf16 v[160:163], v[232:235], a[48:51], v[160:163]\n"
    "v_mul_f32_e32 v200, v200, v229\n"
    "v_mfma_f32_16x16x32_bf16 v[164:167], v[234:237], a[52:55], v[164:167]\n"
    "v_mul_f32_e32 v201, v201, v229\n"
    "v_mul_f32_e32 v202, v202, v229\n"
    "v_mfma_f32_16x16x32_bf16 v[168:171], v[232:235], a[52:55], v[168:171]\n"
    "s_waitcnt lgkmcnt(2)\n"
    "ds_read_b128 a[64:67], v250 offset:8192\n"
    "v_mfma_f32_16x16x32_bf16 v[140:143], a[244:247], a[0:3], 0\n"
    "ds_read_b128 a[68:71], v250 offset:9216\n"
    "v_mul_f32_e32 v203, v203, v229\n"
    "v_mfma_f32_16x16x32_bf16 v[144:147], v[8:11], a[0:3], 0\n"
    "ds_read_b128 a[72:75], v250 offset:10240\n"
    "v_add_f32_e32 v220, v220, v221\n"
    "v_mfma_f32_16x16x32_bf16 v[148:151], a[248:251], a[0:3], 0\n"
    "ds_read_b128 a[76:79], v250 offset:11264\n"
    "v_add_f32_e32 v222, v222, v223\n"
    "v_mfma_f32_16x16x32_bf16 v[152:155], v[12:15], a[0:3], 0\n"
    "s_waitcnt vmcnt(5)\n"
    "v_subrev_u32_e32 v253, s39, v253\n"
    "v_cmp_lt_u32_e32 vcc, 1, v253\n"
    "v_mfma_f32_16x16x32_bf16 v[140:143], a[252:255], a[4:7], v[140:143]\n"
    "ds_read_b128 a[80:83], v250 offset:12288\n"
    "v_mul_f32_e32 v204, v204, v229\n"
    "v_mfma_f32_16x16x32_bf16 v[144:147], v[16:19], a[4:7], v[144:147]\n"
    "ds_read_b128 a[84:87], v250 offset:13312\n"
    "v_mul_f32_e32 v205, v205, v229\n"
    "v_mfma_f32_16x16x32_bf16 v[148:151], v[28:31], a[4:7], v[148:151]\n"
    "s_cbranch_vccnz .Lat_ppoll\n"
    ".Lat_pok:\n"
    "v_cmp_eq_u32_e32 vcc, 1, v253\n"
    "v_mfma_f32_16x16x32_bf16 v[152:155], v[48:51], a[4:7], v[152:155]\n"
    "buffer_load_dwordx4 a[128:131], v250, s[8:11], s54 offen offset:0 sc1\n"
    "v_mfma_f32_16x16x32_bf16 v[140:143], v[32:35], a[8:11], v[140:143]\n"
    "v_mul_f32_e32 v206, v206, v229\n"
    "v_mul_f32_e32 v207, v207, v229\n"
    "v_mfma_f32_16x16x32_bf16 v[144:147], v[52:55], a[8:11], v[144:147]\n"
    "s_cbranch_vccnz .Lat_redo\n"
    "v_mfma_f32_16x16x32_bf16 v[148:151], v[36:39], a[8:11], v[148:151]\n"
    "buffer_load_dwordx4 a[132:135], v250, s[8:11], s54 offen offset:1024 sc1\n"
    "v_mfma_f32_16x16x32_bf16 v[152:155], v[56:59], a[8:11], v[152:155]\n"
    "s_addk_i32 s54, 8192\n"
    "ds_read_b128 a[88:91], v250 offset:14336\n"
    "v_mfma_f32_16x16x32_bf16 v[140:143], v[68:71], a[12:15], v[140:143]\n"
    "buffer_load_dwordx4 a[136:139], v250, s[8:11], s54 offen offset:0 sc1\n"
    "v_mfma_f32_16x16x32_bf16 v[144:147], v[88:91], a[12:15], v[144:147]\n"
    "ds_read_b128 a[92:95], v250 offset:15360\n"
    "v_mul_f32_e32 v208, v208, v229\n"
    "v_mfma_f32_16x16x32_bf16 v[148:151], v[72:75], a[12:15], v[148:151]\n"
    "s_waitcnt vmcnt(3)\n"
    "buffer_load_dwordx4 a[140:143], v250, s[8:11], s54 offen offset:1024 sc1\n"
    "v_mfma_f32_16x16x32_bf16 v[152:155], v[92:95], a[12:15], v[152:155]\n"
    "s_addk_i32 s54, 8192\n"
    "v_add_f32_e32 v220, v220, v222\n"
    "v_mfma_f32_16x16x32_bf16 v[140:143], v[76:79], a[16:19], v[140:143]\n"
    "buffer_load_dwordx4 a[144:147], v250, s[8:11], s54 offen offset:0 sc1\n"
    "v_mfma_f32_16x16x32_bf16 v[144:147], v[96:99], a[16:19], v[144:147]\n"
    "ds_read_b128 a[56:59], v252 offset:30720\n"
    "v_mfma_f32_16x16x32_bf16 v[148:151], a[192:195], a[16:19], v[148:151]\n"
    "buffer_load_dwordx4 a[148:151], v250, s[8:11], s54 offen offset:1024 sc1\n"
    "v_mfma_f32_16x16x32_bf16 v[152:155], a[196:199], a[16:19], v[152:155]\n"
    "s_addk_i32 s54, 8192\n"
    "ds_read_b128 a[60:63], v255 offset:30720\n"
    "v_mul_f32_e32 v209, v209, v229\n"
    "v_mfma_f32_16x16x32_bf16 v[140:143], a[200:203], a[20:23], v[140:143]\n"
    "buffer_load_dwordx4 a[152:155], v250, s[8:11], s54 offen offset:0 sc1\n"
    "v_mfma_f32_16x16x32_bf16 v[144:147], a[204:207], a[20:23], v[144:147]\n"
    "v_mul_f32_e32 v172, v172, v230\n"
    "v_mul_f32_e32 v173, v173, v230\n"
    "v_mfma_f32_16x16x32_bf16 v[148:151], a[208:211], a[20:23], v[148:151]\n"
    "buffer_load_dwordx4 a[156:159], v250, s[8:11], s54 offen offset:1024 sc1\n"
    "v_mfma_f32_16x16x32_bf16 v[152:155], a[212:215], a[20:23], v[152:155]\n"
    "s_waitcnt lgkmcnt(10)\n"
    "v_mul_f32_e32 v210, v210, v229\n"
    "v_mul_f32_e32 v211, v211, v229\n"
    "v_mfma_f32_16x16x32_bf16 v[140:143], v[108:111], a[24:27], v[140:143]\n"
    "buffer_load_dword v227, v248, s[8:11], s45 offen offset:1088 sc1\n"
    "v_mfma_f32_16x16x32_bf16 v[144:147], v[112:115], a[24:27], v[144:147]\n"
    "v_mfma_f32_16x16x32_bf16 v[148:151], v[132:135], a[24:27], v[148:151]\n"
    "v_mfma_f32_16x16x32_bf16 v[152:155], v[116:119], a[24:27], v[152:155]\n"
    "v_rcp_f32_e32 v220, v220\n"
    "v_mfma_f32_16x16x32_bf16 v[140:143], v[136:139], a[28:31], v[140:143]\n"
    "v_add_f32_e32 v80, v80, v81\n"
    "v_add_f32_e32 v82, v82, v83\n"
    "v_mfma_f32_16x16x32_bf16 v[144:147], a[240:243], a[28:31], v[144:147]\n"
    "ds_read_b128 a[160:163], v252 offset:4096\n"
    "v_add_f32_e32 v80, v80, v82\n"
    "v_mfma_f32_16x16x32_bf16 v[148:151], v[4:7], a[28:31], v[148:151]\n"
    "v_rcp_f32_e32 v80, v80\n"
    "v_mfma_f32_16x16x32_bf16 v[152:155], v[24:27], a[28:31], v[152:155]\n"
    "s_waitcnt lgkmcnt(9)\n"
    "ds_read_b128 a[164:167], v255 offset:4096\n"
    "v_mfma_f32_16x16x32_bf16 v[180:183], a[244:247], a[64:67], 0\n"
    "v_mul_f32_e32 v174, v174, v230\n"
    "v_mfma_f32_16x16x32_bf16 v[184:187], v[8:11], a[64:67], 0\n"
    "v_fma_f32 v140, v140, v220, v156\n"
    "v_mfma_f32_16x16x32_bf16 v[188:191], a[248:251], a[64:67], 0\n"
    "v_fma_f32 v141, v141, v220, v157\n"
    "v_fma_f32 v142, v142, v220, v158\n"
    "v_mfma_f32_16x16x32_bf16 v[192:195], v[12:15], a[64:67], 0\n"
    "v_mul_f32_e32 v175, v175, v230\n"
    "v_mfma_f32_16x16x32_bf16 v[180:183], a[252:255], a[68:71], v[180:183]\n"
    "v_fma_f32 v143, v143, v220, v159\n"
    "v_mfma_f32_16x16x32_bf16 v[184:187], v[16:19], a[68:71], v[184:187]\n"
    "v_fma_f32 v144, v144, v220, v160\n"
    "v_mfma_f32_16x16x32_bf16 v[188:191], v[28:31], a[68:71], v[188:191]\n"
    "v_fma_f32 v145, v145, v220, v161\n"
    "v_mfma_f32_16x16x32_bf16 v[192:195], v[48:51], a[68:71], v[192:195]\n"
    "s_waitcnt lgkmcnt(8)\n"
    "v_fma_f32 v146, v146, v220, v162\n"
    "v_mfma_f32_16x16x32_bf16 v[180:183], v[32:35], a[72:75], v[180:183]\n"
    "v_mul_f32_e32 v176, v176, v230\n"
    "v_fma_f32 v147, v147, v220, v163\n"
    "v_mfma_f32_16x16x32_bf16 v[184:187], v[52:55], a[72:75], v[184:187]\n"
    "v_cvt_pk_bf16_f32 v140, v140, v141\n"
    "v_mfma_f32_16x16x32_bf16 v[188:191], v[36:39], a[72:75], v[188:191]\n"
    "v_cvt_pk_bf16_f32 v141, v142, v143\n"
    "v_cvt_pk_bf16_f32 v142, v144, v145\n"
    "v_mfma_f32_16x16x32_bf16 v[192:195], v[56:59], a[72:75], v[192:195]\n"
    "v_cvt_pk_bf16_f32 v143, v146, v147\n"
    "v_mfma_f32_16x16x32_bf16 v[180:183], v[68:71], a[76:79], v[180:183]\n"
    "buffer_store_dwordx4 v[140:143], v239, s[16:19], s80 offen offset:0 sc0 sc1\n"
    "v_mfma_f32_16x16x32_bf16 v[184:187], v[88:91], a[76:79], v[184:187]\n"
    "v_fma_f32 v148, v148, v220, v164\n"
    "v_mfma_f32_16x16x32_bf16 v[188:191], v[72:75], a[76:79], v[188:191]\n"
    "v_fma_f32 v149, v149, v220, v165\n"
    "v_mfma_f32_16x16x32_bf16 v[192:195], v[92:95], a[76:79], v[192:195]\n"
    "s_waitcnt lgkmcnt(6)\n"
    "v_fma_f32 v150, v150, v220, v166\n"
    "v_fma_f32 v151, v151, v220, v167\n"
    "v_mfma_f32_16x16x32_bf16 v[180:183], v[76:79], a[80:83], v[180:183]\n"
    "v_fma_f32 v152, v152, v220, v168\n"
    "v_fma_f32 v153, v153, v220, v169\n"
    "v_mfma_f32_16x16x32_bf16 v[184:187], v[96:99], a[80:83], v[184:187]\n"
    "v_fma_f32 v154, v154, v220, v170\n"
    "v_fma_f32 v155, v155, v220, v171\n"
    "v_mfma_f32_16x16x32_bf16 v[188:191], a[192:195], a[80:83], v[188:191]\n"
    "v_cvt_pk_bf16_f32 v148, v148, v149\n"
    "v_cvt_pk_bf16_f32 v149, v150, v151\n"
    "v_mfma_f32_16x16x32_bf16 v[192:195], a[196:199], a[80:83], v[192:195]\n"
    "v_cvt_pk_bf16_f32 v150, v152, v153\n"
    "v_mfma_f32_16x16x32_bf16 v[180:183], a[200:203], a[84:87], v[180:183]\n"
    "v_cvt_pk_bf16_f32 v151, v154, v155\n"
    "v_mfma_f32_16x16x32_bf16 v[184:187], a[204:207], a[84:87], v[184:187]\n"
    "buffer_store_dwordx4 v[148:151], v239, s[16:19], s80 offen offset:64 sc0 sc1\n"
    "v_mfma_f32_16x16x32_bf16 v[188:191], a[208:211], a[84:87], v[188:191]\n"
    "v_mfma_f32_16x16x32_bf16 v[192:195], a[212:215], a[84:87], v[192:195]\n"
    "s_waitcnt lgkmcnt(4)\n"
    "v_mul_f32_e32 v177, v177, v230\n"
    "v_mfma_f32_16x16x32_bf16 v[180:183], v[108:111], a[88:91], v[180:183]\n"
    "v_mul_f32_e32 v178, v178, v230\n"
    "v_mfma_f32_16x16x32_bf16 v[184:187], v[112:115], a[88:91], v[184:187]\n"
    "v_mul_f32_e32 v179, v179, v230\n"
    "v_mfma_f32_16x16x32_bf16 v[188:191], v[132:135], a[88:91], v[188:191]\n"
    "v_mul_f32_e32 v212, v212, v230\n"
    "v_mfma_f32_16x16x32_bf16 v[192:195], v[116:119], a[88:91], v[192:195]\n"
    "s_waitcnt vmcnt(9)\n"
    "v_mfma_f32_16x16x32_bf16 v[64:67], v[234:237], a[128:131], 0\n"
    "v_mfma_f32_16x16x32_bf16 v[84:87], v[232:235], a[128:131], 0\n"
    "v_mfma_f32_16x16x32_bf16 v[104:107], v[234:237], a[132:135], 0\n"
    "v_mul_f32_e32 v213, v213, v230\n"
    "v_mfma_f32_16x16x32_bf16 v[124:127], v[232:235], a[132:135], 0\n"
    "s_waitcnt vmcnt(7)\n"
    "v_mfma_f32_16x16x32_bf16 v[64:67], v[234:237], a[136:139], v[64:67]\n"
    "v_mfma_f32_16x16x32_bf16 v[84:87], v[232:235], a[136:139], v[84:87]\n"
    "v_mul_f32_e32 v214, v214, v230\n"
    "v_mfma_f32_16x16x32_bf16 v[104:107], v[234:237], a[140:143], v[104:107]\n"
    "v_mfma_f32_16x16x32_bf16 v[124:127], v[232:235], a[140:143], v[124:127]\n"
    "s_waitcnt vmcnt(5)\n"
    "v_mfma_f32_16x16x32_bf16 v[64:67], v[234:237], a[144:147], v[64:67]\n"
    "v_mfma_f32_16x16x32_bf16 v[84:87], v[232:235], a[144:147], v[84:87]\n"
    "v_mul_f32_e32 v215, v215, v230\n"
    "v_mfma_f32_16x16x32_bf16 v[104:107], v[234:237], a[148:151], v[104:107]\n"
    "v_mul_f32_e32 v216, v216, v230\n"
    "v_mul_f32_e32 v217, v217, v230\n"
    "v_mfma_f32_16x16x32_bf16 v[124:127], v[232:235], a[148:151], v[124:127]\n"
    "s_waitcnt vmcnt(3)\n"
    "v_mul_f32_e32 v218, v218, v230\n"
    "v_mfma_f32_16x16x32_bf16 v[64:67], v[234:237], a[152:155], v[64:67]\n"
    "s_waitcnt vmcnt(2)\n"
    "ds_write_b32 v225, v227 offset:512\n"
    "v_mfma_f32_16x16x32_bf16 v[84:87], v[232:235], a[152:155], v[84:87]\n"
    "v_mul_f32_e32 v219, v219, v230\n"
    "v_mfma_f32_16x16x32_bf16 v[104:107], v[234:237], a[156:159], v[104:107]\n"
    "v_mfma_f32_16x16x32_bf16 v[124:127], v[232:235], a[156:159], v[124:127]\n"
    "v_cvt_pk_bf16_f32 v64, v64, v65\n"
    "v_cvt_pk_bf16_f32 v65, v66, v67\n"
    "v_mfma_f32_16x16x32_bf16 v[180:183], v[136:139], a[92:95], v[180:183]\n"
    "v_cvt_pk_bf16_f32 v66, v84, v85\n"
    "v_cvt_pk_bf16_f32 v67, v86, v87\n"
    "v_mfma_f32_16x16x32_bf16 v[184:187], a[240:243], a[92:95], v[184:187]\n"
    "ds_write_b128 v246, v[64:67] offset:16384\n"
    "v_mfma_f32_16x16x32_bf16 v[188:191], v[4:7], a[92:95], v[188:191]\n"
    "v_cvt_pk_bf16_f32 v104, v104, v105\n"
    "v_cvt_pk_bf16_f32 v105, v106, v107\n"
    "v_mfma_f32_16x16x32_bf16 v[192:195], v[24:27], a[92:95], v[192:195]\n"
    "s_waitcnt lgkmcnt(4)\n"
    "v_cvt_pk_bf16_f32 v106, v124, v125\n"
    "v_cvt_pk_bf16_f32 v107, v126, v127\n"
    "v_mfma_f32_16x16x32_bf16 v[196:199], v[234:237], a[56:59], v[196:199]\n"
    "ds_write_b128 v246, v[104:107] offset:17408\n"
    "v_mfma_f32_16x16x32_bf16 v[200:203], v[232:235], a[56:59], v[200:203]\n"
    "v_mfma_f32_16x16x32_bf16 v[204:207], v[234:237], a[60:63], v[204:207]\n"
    "s_waitcnt lgkmcnt(0)\n"
    "v_mfma_f32_16x16x32_bf16 v[208:211], v[232:235], a[60:63], v[208:211]\n"
    "s_barrier\n"
    "v_mfma_f32_16x16x32_bf16 v[172:175], v[234:237], a[160:163], v[172:175]\n"
    "ds_read_b128 a[96:99], v250 offset:16384\n"
    "v_mfma_f32_16x16x32_bf16 v[176:179], v[232:235], a[160:163], v[176:179]\n"
    "ds_read_b128 a[100:103], v250 offset:17408\n"
    "v_mfma_f32_16x16x32_bf16 v[212:215], v[234:237], a[164:167], v[212:215]\n"
    "ds_read_b128 a[104:107], v250 offset:18432\n"
    "ds_read_b128 a[108:111], v250 offset:19456\n"
    "v_mfma_f32_16x16x32_bf16 v[216:219], v[232:235], a[164:167], v[216:219]\n"
    "s_waitcnt lgkmcnt(2)\n"
    "ds_read_b128 a[112:115], v250 offset:20480\n"
    "ds_read_b128 a[116:119], v250 offset:21504\n"
    "v_mfma_f32_16x16x32_bf16 v[0:3], a[244:247], a[96:99], 0\n"
    "ds_read_b128 a[120:123], v250 offset:22528\n"
    "ds_read_b128 a[124:127], v250 offset:23552\n"
    "v_mfma_f32_16x16x32_bf16 v[20:23], v[8:11], a[96:99], 0\n"
    "ds_read2_b32 v[220:221], v245 offset0:128 offset1:144\n"
    "ds_read2_b32 v[222:223], v245 offset0:160 offset1:176\n"
    "v_fma_f32 v180, v180, v80, v196\n"
    "v_mfma_f32_16x16x32_bf16 v[0:3], a[252:255], a[100:103], v[0:3]\n"
    "v_fma_f32 v181, v181, v80, v197\n"
    "v_fma_f32 v182, v182, v80, v198\n"
    "v_mfma_f32_16x16x32_bf16 v[20:23], v[16:19], a[100:103], v[20:23]\n"
    "s_waitcnt lgkmcnt(6)\n"
    "v_fma_f32 v183, v183, v80, v199\n"
    "v_fma_f32 v184, v184, v80, v200\n"
    "v_mfma_f32_16x16x32_bf16 v[0:3], v[32:35], a[104:107], v[0:3]\n"
    "s_or_b32 s45, s34, s93\n"
    "s_cselect_b32 s45, 2.0, 6101760\n"
    "v_fma_f32 v185, v185, v80, v201\n"
    "v_mfma_f32_16x16x32_bf16 v[20:23], v[52:55], a[104:107], v[20:23]\n"
    "v_fma_f32 v186, v186, v80, v202\n"
    "v_fma_f32 v187, v187, v80, v203\n"
    "v_mfma_f32_16x16x32_bf16 v[0:3], v[68:71], a[108:111], v[0:3]\n"
    "v_cvt_pk_bf16_f32 v180, v180, v181\n"
    "v_cvt_pk_bf16_f32 v181, v182, v183\n"
    "v_mfma_f32_16x16x32_bf16 v[20:23], v[88:91], a[108:111], v[20:23]\n"
    "s_waitcnt lgkmcnt(4)\n"
    "v_cvt_pk_bf16_f32 v182, v184, v185\n"
    "v_cvt_pk_bf16_f32 v183, v186, v187\n"
    "v_mfma_f32_16x16x32_bf16 v[0:3], v[76:79], a[112:115], v[0:3]\n"
    "buffer_store_dwordx4 v[180:183], v239, s[16:19], s82 offen offset:0 sc0 sc1\n"
    "v_mfma_f32_16x16x32_bf16 v[20:23], v[96:99], a[112:115], v[20:23]\n"
    "v_fma_f32 v188, v188, v80, v204\n"
    "v_fma_f32 v189, v189, v80, v205\n"
    "v_mfma_f32_16x16x32_bf16 v[0:3], a[200:203], a[116:119], v[0:3]\n"
    "v_fma_f32 v190, v190, v80, v206\n"
    "v_fma_f32 v191, v191, v80, v207\n"
    "v_mfma_f32_16x16x32_bf16 v[20:23], a[204:207], a[116:119], v[20:23]\n"
    "s_waitcnt lgkmcnt(0)\n"
    "v_fma_f32 v192, v192, v80, v208\n"
    "v_fma_f32 v193, v193, v80, v209\n"
    "v_mfma_f32_16x16x32_bf16 v[0:3], v[108:111], a[120:123], v[0:3]\n"
    "v_fma_f32 v194, v194, v80, v210\n"
    "v_fma_f32 v195, v195, v80, v211\n"
    "v_mfma_f32_16x16x32_bf16 v[20:23], v[112:115], a[120:123], v[20:23]\n"
    "v_cvt_pk_bf16_f32 v188, v188, v189\n"
    "v_cvt_pk_bf16_f32 v189, v190, v191\n"
    "v_mfma_f32_16x16x32_bf16 v[0:3], v[136:139], a[124:127], v[0:3]\n"
    "v_cvt_pk_bf16_f32 v190, v192, v193\n"
    "v_cvt_pk_bf16_f32 v191, v194, v195\n"
    "v_mfma_f32_16x16x32_bf16 v[20:23], a[240:243], a[124:127], v[20:23]\n"
    "buffer_store_dwordx4 v[188:191], v239, s[16:19], s82 offen offset:64 sc0 sc1\n"
    "v_mfma_f32_16x16x32_bf16 v[40:43], a[248:251], a[96:99], 0\n"
    "v_add_f32_e32 v220, v220, v221\n"
    "v_add_f32_e32 v222, v222, v223\n"
    "v_add_f32_e32 v220, v220, v222\n"
    "v_mfma_f32_16x16x32_bf16 v[60:63], v[12:15], a[96:99], 0\n"
    "v_rcp_f32_e32 v220, v220\n"
    "v_mfma_f32_16x16x32_bf16 v[40:43], v[28:31], a[100:103], v[40:43]\n"
    "v_fma_f32 v0, v0, v220, v172\n"
    "v_fma_f32 v1, v1, v220, v173\n"
    "v_mfma_f32_16x16x32_bf16 v[60:63], v[48:51], a[100:103], v[60:63]\n"
    "v_fma_f32 v2, v2, v220, v174\n"
    "v_mfma_f32_16x16x32_bf16 v[40:43], v[36:39], a[104:107], v[40:43]\n"
    "v_fma_f32 v3, v3, v220, v175\n"
    "v_mfma_f32_16x16x32_bf16 v[60:63], v[56:59], a[104:107], v[60:63]\n"
    "v_fma_f32 v20, v20, v220, v176\n"
    "v_fma_f32 v21, v21, v220, v177\n"
    "v_mfma_f32_16x16x32_bf16 v[40:43], v[72:75], a[108:111], v[40:43]\n"
    "v_fma_f32 v22, v22, v220, v178\n"
    "v_fma_f32 v23, v23, v220, v179\n"
    "v_mfma_f32_16x16x32_bf16 v[60:63], v[92:95], a[108:111], v[60:63]\n"
    "v_cvt_pk_bf16_f32 v0, v0, v1\n"
    "v_cvt_pk_bf16_f32 v1, v2, v3\n"
    "v_mfma_f32_16x16x32_bf16 v[40:43], a[192:195], a[112:115], v[40:43]\n"
    "v_cvt_pk_bf16_f32 v2, v20, v21\n"
    "v_cvt_pk_bf16_f32 v3, v22, v23\n"
    "v_mfma_f32_16x16x32_bf16 v[60:63], a[196:199], a[112:115], v[60:63]\n"
    "v_mfma_f32_16x16x32_bf16 v[40:43], a[208:211], a[116:119], v[40:43]\n"
    "buffer_store_dwordx4 v[0:3], v239, s[16:19], s28 offen offset:0 sc0 sc1\n"
    "v_mfma_f32_16x16x32_bf16 v[60:63], a[212:215], a[116:119], v[60:63]\n"
    "v_mfma_f32_16x16x32_bf16 v[40:43], v[132:135], a[120:123], v[40:43]\n"
    "v_mfma_f32_16x16x32_bf16 v[60:63], v[116:119], a[120:123], v[60:63]\n"
    "v_mfma_f32_16x16x32_bf16 v[40:43], v[4:7], a[124:127], v[40:43]\n"
    "v_mfma_f32_16x16x32_bf16 v[60:63], v[24:27], a[124:127], v[60:63]\n"
    "v_mov_b32_e32 v254, s39\n"
    "s_nop 2\n"
    "v_fma_f32 v40, v40, v220, v212\n"
    "v_fma_f32 v41, v41, v220, v213\n"
    "v_fma_f32 v42, v42, v220, v214\n"
    "v_fma_f32 v43, v43, v220, v215\n"
    "v_fma_f32 v60, v60, v220, v216\n"
    "v_fma_f32 v61, v61, v220, v217\n"
    "v_fma_f32 v62, v62, v220, v218\n"
    "v_fma_f32 v63, v63, v220, v219\n"
    "v_cvt_pk_bf16_f32 v40, v40, v41\n"
    "v_cvt_pk_bf16_f32 v41, v42, v43\n"
    "v_cvt_pk_bf16_f32 v42, v60, v61\n"
    "v_cvt_pk_bf16_f32 v43, v62, v63\n"
    "buffer_store_dwordx4 v[40:43], v239, s[16:19], s28 offen offset:64 sc0 sc1\n"
    "buffer_store_dword v254, off, s[8:11], s45 offset:0 sc1\n"
    "s_endpgm\n"
    ".p2align 6\n"
    ".long 0xbedf00ff\n"
    ".Lat_w14:\n"
    "s_setpc_b64 s[2:3]\n"
    ".Lat_k:\n"
    "s_mov_b32 s53, 0x400000\n"
    ".Lat_kl:\n"
    "buffer_load_dword v253, v238, s[8:11], s48 offen offset:0 sc1\n"
    "s_waitcnt vmcnt(0)\n"
    "v_subrev_u32_e32 v253, s39, v253\n"
    "v_subrev_u32_e32 v253, 0, v253\n"
    "v_cmp_le_u32_e32 vcc, 1, v253\n"
    "s_sub_u32 s53, s53, 1\n"
    "s_cbranch_scc1 .Lat_kr\n"
    "s_nop 2\n"
    "s_cbranch_vccnz .Lat_kl\n"
    "s_branch .Lat_kr\n"
    ".Lat_kx:\n"
    "s_branch .Lat_kxr\n"
    ".Lat_ppoll:\n"
    "s_mov_b32 s53, 0x400000\n"
    ".Lat_ppl:\n"
    "buffer_load_dword v253, v226, s[8:11], s61 offen offset:0 sc1\n"
    "s_waitcnt vmcnt(0)\n"
    "v_subrev_u32_e32 v253, s39, v253\n"
    "v_cmp_lt_u32_e32 vcc, 1, v253\n"
    "s_sub_u32 s53, s53, 1\n"
    "s_cbranch_scc1 .Lat_pok\n"
    "s_nop 2\n"
    "s_cbranch_vccnz .Lat_ppl\n"
    "s_branch .Lat_pok\n"
    ".Lat_wpoll:\n"
    "s_mov_b32 s53, 0x400000\n"
    ".Lat_wpoll_l:\n"
    "s_sleep 1\n"
    "buffer_load_dword v253, v255, s[8:11], s52 offen offset:0 sc1\n"
    "s_waitcnt vmcnt(0)\n"
    "v_cmp_ne_u32_e32 vcc, s39, v253\n"
    "s_sub_u32 s53, s53, 1\n"
    "s_cbranch_scc1 .Lat_wpoll_ret\n"
    "s_nop 2\n"
    "s_cbranch_vccnz .Lat_wpoll_l\n"
    "s_branch .Lat_wpoll_ret\n"
    ".Lat_glate:\n"
    "s_mov_b32 s53, 0x400000\n"
    ".Lat_glate_l:\n"
    "s_sleep 2\n"
    "buffer_load_dword v188, v189, s[8:11], s81 offen offset:0 sc1\n"
    "s_waitcnt vmcnt(0)\n"
    "v_cmp_ne_u32_e32 vcc, s39, v188\n"
    "s_sub_u32 s53, s53, 1\n"
    "s_cbranch_scc1 .Lat_glate_ret\n"
    "s_nop 2\n"
    "s_cbranch_vccnz .Lat_glate_l\n"
    "s_branch .Lat_glate_ret\n"
    ".Lat_lp0:\n"
    "s_mov_b32 s53, 0x400000\n"
    ".Lat_lp:\n"
    "s_sleep 1\n"
    "buffer_load_dword v253, off, s[8:11], s82 sc1\n"
    "s_waitcnt vmcnt(0)\n"
    "v_readfirstlane_b32 s81, v253\n"
    "s_sub_u32 s53, s53, 1\n"
    "s_cbranch_scc1 .Lat_lpx\n"
    "s_nop 3\n"
    "s_cmp_lg_u32 s81, s39\n"
    "s_cbranch_scc1 .Lat_lp\n"
    ".Lat_lpx:\n"
    "s_mov_b32 m0, s30\n"
    "s_nop 0\n"
    "buffer_load_dwordx4 v250, s[8:11], s83 offen offset:0 sc1 lds\n"
    "s_mov_b32 m0, s42\n"
    "s_branch .Lat_g1b\n"
    ".Lat_h1poll:\n"
    "s_mov_b32 s53, 0x400000\n"
    ".Lat_h1poll_l:\n"
    "s_sleep 1\n"
    "buffer_load_dword v80, v83, s[8:11], s59 offen offset:0 sc1\n"
    "s_waitcnt vmcnt(0)\n"
    "v_cmp_ne_u32_e32 vcc, s39, v80\n"
    "s_sub_u32 s53, s53, 1\n"
    "s_cbranch_scc1 .Lat_h1poll_ret\n"
    "s_nop 2\n"
    "s_cbranch_vccnz .Lat_h1poll_l\n"
    "s_branch .Lat_h1poll_ret\n"
    ".Lat_hpoll:\n"
    "s_mov_b32 s53, 0x400000\n"
    ".Lat_hpoll_l:\n"
    "s_sleep 1\n"
    "buffer_load_dword v253, v250, s[8:11], s59 offen offset:0 sc1\n"
    "buffer_load_dword v254, v252, s[8:11], s59 offen offset:0 sc1\n"
    "s_waitcnt vmcnt(0)\n"
    "v_xor_b32_e32 v254, s39, v254\n"
    "v_xor_b32_e32 v253, s39, v253\n"
    "v_or_b32_e32 v253, v253, v254\n"
    "v_cmp_ne_u32_e32 vcc, 0, v253\n"
    "s_sub_u32 s53, s53, 1\n"
    "s_cbranch_scc1 .Lat_hpoll_ret\n"
    "s_nop 2\n"
    "s_cbranch_vccnz .Lat_hpoll_l\n"
    "s_branch .Lat_hpoll_ret\n"
    ".Lat_redo:\n"
    "s_waitcnt vmcnt(0) lgkmcnt(0)\n"
    "s_barrier\n"
    "s_mov_b32 s49, 1\n"
    "s_mov_b32 s4, 0\n"
    "s_mov_b32 s80, 0\n"
    "s_mov_b32 s82, 2.0\n"
    "v_lshrrev_b32_e32 v224, 4, v250\n"
    "s_and_b32 s50, s93, 1\n"
    "s_lshr_b32 s51, s93, 1\n"
    "v_lshrrev_b32_e32 v225, 3, v224\n"
    "v_and_b32_e32 v226, 6, v225\n"
    "v_and_b32_e32 v227, 7, v224\n"
    "v_xor_b32_e32 v226, v226, v227\n"
    "v_lshlrev_b32_e32 v226, 4, v226\n"
    "v_and_b32_e32 v227, 4, v225\n"
    "v_add_u32_e32 v227, v227, v225\n"
    "v_lshl_add_u32 v251, v227, 9, v226\n"
    "v_add_u32_e32 v252, 0x1c00, v251\n"
    "v_and_b32_e32 v225, 7, v224\n"
    "v_lshlrev_b32_e32 v227, 7, v225\n"
    "v_bfe_u32 v226, v224, 3, 1\n"
    "v_lshl_add_u32 v227, v226, 10, v227\n"
    "v_and_b32_e32 v225, 6, v224\n"
    "v_lshrrev_b32_e32 v226, 4, v224\n"
    "v_xor_b32_e32 v225, v225, v226\n"
    "v_lshl_add_u32 v240, v225, 4, v227\n"
    "v_lshrrev_b32_e32 v8, 4, v250\n"
    "v_and_b32_e32 v10, 0x70, v251\n"
    "v_bfe_u32 v9, v8, 3, 1\n"
    "v_lshl_add_u32 v248, v9, 9, v10\n"
    "v_lshrrev_b32_e32 v9, 4, v8\n"
    "v_lshl_add_u32 v248, v9, 12, v248\n"
    "s_mul_i32 s43, s50, 3\n"
    "s_xor_b32 s43, s43, s51\n"
    "s_lshl_b32 s43, s43, 15\n"
    "s_add_u32 s43, s43, 32768\n"
    "s_mul_i32 s42, s93, 5120\n"
    "s_lshl_b32 s45, s50, 8\n"
    "s_mul_i32 s40, s32, 1277952\n"
    "s_lshl_b32 s41, s51, 14\n"
    "s_add_u32 s40, s40, s41\n"
    "s_add_u32 s40, s40, s45\n"
    "s_add_u32 s41, s40, 128\n"
    "v_bfe_u32 v9, v8, 2, 3\n"
    "v_lshlrev_b32_e32 v244, 7, v9\n"
    "v_lshrrev_b32_e32 v10, 5, v8\n"
    "v_lshl_add_u32 v244, v10, 10, v244\n"
    "v_and_b32_e32 v9, 6, v9\n"
    "v_bfe_u32 v10, v8, 1, 1\n"
    "v_xor_b32_e32 v9, v9, v10\n"
    "v_lshl_add_u32 v244, v9, 4, v244\n"
    "v_and_b32_e32 v10, 1, v8\n"
    "v_lshl_add_u32 v244, v10, 3, v244\n"
    "s_lshl_b32 s45, s51, 16\n"
    "s_add_u32 s45, s45, 32768\n"
    "v_add_u32_e32 v244, s45, v244\n"
    "v_xor_b32_e32 v245, 32, v244\n"
    "v_xor_b32_e32 v246, 64, v244\n"
    "v_xor_b32_e32 v247, 0x60, v244\n"
    "s_lshl_b32 s45, s51, 15\n"
    "s_add_u32 s48, s45, 32768\n"
    "s_sub_u32 s45, 131072, s45\n"
    "v_add_u32_e32 v242, s45, v240\n"
    "v_add_u32_e32 v240, s48, v240\n"
    "v_xor_b32_e32 v241, 64, v240\n"
    "v_xor_b32_e32 v243, 64, v242\n"
    "s_xor_b32 s45, s93, 2\n"
    "s_mul_i32 s45, s45, 5120\n"
    "v_lshl_add_u32 v249, v8, 2, s45\n"
    "s_lshl_b32 s45, s93, 11\n"
    "v_add_u32_e32 v99, s45, v250\n"
    "v_add_u32_e32 v98, 11345920, v99\n"
    "v_and_b32_e32 v252, 0x30, v250\n"
    "v_add_u32_e32 v252, 0x400, v252\n"
    "s_mul_i32 s59, s32, 999\n"
    "s_andn2_b32 s59, s59, 63\n"
    "s_sub_u32 s48, 3968, s59\n"
    "v_cmp_le_u32_e32 vcc, 0x80, v250\n"
    "v_mov_b32_e32 v80, s48\n"
    "s_add_u32 s59, s59, 6076416\n"
    "v_cndmask_b32_e32 v83, v250, v80, vcc\n"
    "v_bfe_u32 v253, v250, 4, 2\n"
    "v_max_u32_e32 v253, 1, v253\n"
    "v_add_u32_e32 v253, s32, v253\n"
    "v_and_b32_e32 v253, 3, v253\n"
    "v_lshlrev_b32_e32 v253, 9, v253\n"
    "v_mov_b32_e32 v84, s39\n"
    "v_lshrrev_b32_e32 v86, 2, v250\n"
    "v_min_u32_e32 v86, 124, v86\n"
    "s_lshl2_add_u32 s58, s34, s93\n"
    "s_lshl_b32 s58, s58, 7\n"
    "s_add_u32 s58, s58, 5910528\n"
    "s_lshl_b32 s45, s50, 13\n"
    "v_add_u32_e32 v85, s45, v250\n"
    "s_lshl3_add_u32 s45, s45, 49152\n"
    "v_add_u32_e32 v255, s45, v250\n"
    "v_lshrrev_b32_e32 v105, 7, v250\n"
    "v_and_b32_e32 v104, 0x70, v251\n"
    "v_lshl_add_u32 v104, v105, 9, v104\n"
    "v_add_u32_e32 v105, 0xc00, v104\n"
    "v_add_u32_e32 v106, 0x1800, v104\n"
    "v_add_u32_e32 v107, 0x2400, v104\n"
    "s_lshl_b32 s57, s93, 7\n"
    "s_lshl4_add_u32 s56, s57, 24576\n"
    "s_add_u32 s57, s57, 5111808\n"
    "s_mul_i32 s45, s51, 32768\n"
    "s_add_u32 s45, s45, 8192\n"
    "v_subrev_u32_e32 v46, s45, v240\n"
    "v_subrev_u32_e32 v47, s45, v241\n"
    "s_mul_i32 s45, s51, 61440\n"
    "s_add_u32 s45, s45, 8192\n"
    "v_subrev_u32_e32 v4, s45, v244\n"
    "v_subrev_u32_e32 v5, s45, v245\n"
    "v_subrev_u32_e32 v6, s45, v246\n"
    "v_subrev_u32_e32 v7, s45, v247\n"
    "v_accvgpr_write_b32 a244, 0\n"
    "v_accvgpr_write_b32 a245, 0\n"
    "v_accvgpr_write_b32 a246, 0\n"
    "v_accvgpr_write_b32 a247, 0\n"
    "v_accvgpr_write_b32 a248, 0\n"
    "v_accvgpr_write_b32 a249, 0\n"
    "v_accvgpr_write_b32 a250, 0\n"
    "v_accvgpr_write_b32 a251, 0\n"
    "v_accvgpr_write_b32 a252, 0\n"
    "v_accvgpr_write_b32 a253, 0\n"
    "v_accvgpr_write_b32 a254, 0\n"
    "v_accvgpr_write_b32 a255, 0\n"
    "v_mov_b32_e32 v8, s4\n"
    "v_mov_b32_e32 v9, s4\n"
    "v_mov_b64_e32 v[10:11], v[8:9]\n"
    "v_mov_b64_e32 v[12:13], v[8:9]\n"
    "v_mov_b64_e32 v[14:15], v[8:9]\n"
    "v_mov_b64_e32 v[16:17], v[8:9]\n"
    "v_mov_b64_e32 v[18:19], v[8:9]\n"
    "v_mov_b64_e32 v[28:29], v[8:9]\n"
    "v_mov_b64_e32 v[30:31], v[8:9]\n"
    "v_mov_b64_e32 v[32:33], v[8:9]\n"
    "v_mov_b64_e32 v[34:35], v[8:9]\n"
    "v_mov_b64_e32 v[36:37], v[8:9]\n"
    "v_mov_b64_e32 v[38:39], v[8:9]\n"
    "v_mov_b64_e32 v[48:49], v[8:9]\n"
    "v_mov_b64_e32 v[50:51], v[8:9]\n"
    "v_mov_b64_e32 v[52:53], v[8:9]\n"
    "v_mov_b64_e32 v[54:55], v[8:9]\n"
    "v_mov_b64_e32 v[56:57], v[8:9]\n"
    "v_mov_b64_e32 v[58:59], v[8:9]\n"
    "v_mov_b64_e32 v[68:69], v[8:9]\n"
    "v_mov_b64_e32 v[70:71], v[8:9]\n"
    "v_mov_b64_e32 v[72:73], v[8:9]\n"
    "v_mov_b64_e32 v[74:75], v[8:9]\n"
    "v_mov_b64_e32 v[76:77], v[8:9]\n"
    "v_mov_b64_e32 v[78:79], v[8:9]\n"
    "v_mov_b64_e32 v[88:89], v[8:9]\n"
    "v_mov_b64_e32 v[90:91], v[8:9]\n"
    "v_mov_b64_e32 v[92:93], v[8:9]\n"
    "v_mov_b64_e32 v[94:95], v[8:9]\n"
    "v_mov_b64_e32 v[96:97], v[8:9]\n"
    "v_mov_b64_e32 v[98:99], v[8:9]\n"
    "v_mov_b64_e32 v[108:109], v[8:9]\n"
    "v_mov_b64_e32 v[110:111], v[8:9]\n"
    "v_mov_b64_e32 v[112:113], v[8:9]\n"
    "v_mov_b64_e32 v[114:115], v[8:9]\n"
    "v_mov_b64_e32 v[116:117], v[8:9]\n"
    "v_mov_b64_e32 v[118:119], v[8:9]\n"
    "v_mov_b64_e32 v[128:129], v[8:9]\n"
    "v_mov_b64_e32 v[130:131], v[8:9]\n"
    "v_mov_b64_e32 v[132:133], v[8:9]\n"
    "v_mov_b64_e32 v[134:135], v[8:9]\n"
    "v_mov_b64_e32 v[136:137], v[8:9]\n"
    "v_mov_b64_e32 v[138:139], v[8:9]\n"
    "s_mov_b32 m0, s56\n"
    "s_nop 0\n"
    "buffer_load_dwordx4 v104, s[8:11], s57 offen offset:0 sc1 lds\n"
    "buffer_load_dwordx4 v105, s[8:11], s57 offen offset:1024 sc1 lds\n"
    "s_add_u32 m0, s43, 0\n"
    "s_nop 0\n"
    "buffer_load_dwordx4 v104, s[8:11], s40 offen offset:0 sc1 lds\n"
    "buffer_load_dwordx4 v105, s[8:11], s40 offen offset:1024 sc1 lds\n"
    "buffer_load_dwordx4 v106, s[8:11], s40 offen offset:2048 sc1 lds\n"
    "buffer_load_dwordx4 v107, s[8:11], s40 offen offset:3072 sc1 lds\n"
    "s_add_u32 s40, s40, 0x8000\n"
    "s_add_u32 m0, s43, 4096\n"
    "s_nop 0\n"
    "buffer_load_dwordx4 v104, s[8:11], s41 offen offset:0 sc1 lds\n"
    "buffer_load_dwordx4 v105, s[8:11], s41 offen offset:1024 sc1 lds\n"
    "buffer_load_dwordx4 v106, s[8:11], s41 offen offset:2048 sc1 lds\n"
    "buffer_load_dwordx4 v107, s[8:11], s41 offen offset:3072 sc1 lds\n"
    "s_add_u32 s41, s41, 0x8000\n"
    "s_add_u32 m0, s43, 8192\n"
    "s_nop 0\n"
    "buffer_load_dwordx4 v104, s[8:11], s40 offen offset:0 sc1 lds\n"
    "buffer_load_dwordx4 v105, s[8:11], s40 offen offset:1024 sc1 lds\n"
    "buffer_load_dwordx4 v106, s[8:11], s40 offen offset:2048 sc1 lds\n"
    "buffer_load_dwordx4 v107, s[8:11], s40 offen offset:3072 sc1 lds\n"
    "s_add_u32 s40, s40, 0x8000\n"
    "s_add_u32 m0, s43, 12288\n"
    "s_nop 0\n"
    "buffer_load_dwordx4 v104, s[8:11], s41 offen offset:0 sc1 lds\n"
    "buffer_load_dwordx4 v105, s[8:11], s41 offen offset:1024 sc1 lds\n"
    "buffer_load_dwordx4 v106, s[8:11], s41 offen offset:2048 sc1 lds\n"
    "buffer_load_dwordx4 v107, s[8:11], s41 offen offset:3072 sc1 lds\n"
    "s_add_u32 s41, s41, 0x8000\n"
    "s_waitcnt vmcnt(0)\n"
    "s_branch .Lat_p2\n"
    ".Lat_maxpass:\n"
    "s_waitcnt lgkmcnt(0)\n"
    "s_mul_i32 s48, s33, 81920\n"
    "s_add_u32 s48, s48, 11345920\n"
    "s_add_u32 s45, s32, 0\n"
    "s_and_b32 s45, s45, 3\n"
    "s_lshl2_add_u32 s45, s50, s45\n"
    "s_lshl_b32 s45, s45, 13\n"
    "s_add_u32 s45, s45, s48\n"
    "s_add_u32 s46, s45, 4096\n"
    "buffer_load_dwordx4 a[0:3], v250, s[8:11], s45 offen offset:0 sc1\n"
    "buffer_load_dwordx4 a[4:7], v250, s[8:11], s45 offen offset:1024 sc1\n"
    "buffer_load_dwordx4 a[8:11], v250, s[8:11], s45 offen offset:2048 sc1\n"
    "buffer_load_dwordx4 a[12:15], v250, s[8:11], s45 offen offset:3072 sc1\n"
    "buffer_load_dwordx4 a[16:19], v250, s[8:11], s46 offen offset:0 sc1\n"
    "buffer_load_dwordx4 a[20:23], v250, s[8:11], s46 offen offset:1024 sc1\n"
    "buffer_load_dwordx4 a[24:27], v250, s[8:11], s46 offen offset:2048 sc1\n"
    "buffer_load_dwordx4 a[28:31], v250, s[8:11], s46 offen offset:3072 sc1\n"
    "s_and_b32 s45, s32, 1\n"
    "s_xor_b32 s45, s45, s50\n"
    "s_add_u32 s45, s45, 8\n"
    "s_lshl_b32 s45, s45, 13\n"
    "s_add_u32 s45, s45, s48\n"
    "s_add_u32 s46, s45, 4096\n"
    "buffer_load_dwordx4 a[32:35], v250, s[8:11], s45 offen offset:0 sc1\n"
    "buffer_load_dwordx4 a[36:39], v250, s[8:11], s45 offen offset:1024 sc1\n"
    "buffer_load_dwordx4 a[40:43], v250, s[8:11], s45 offen offset:2048 sc1\n"
    "buffer_load_dwordx4 a[44:47], v250, s[8:11], s45 offen offset:3072 sc1\n"
    "buffer_load_dwordx4 a[48:51], v250, s[8:11], s46 offen offset:0 sc1\n"
    "buffer_load_dwordx4 a[52:55], v250, s[8:11], s46 offen offset:1024 sc1\n"
    "buffer_load_dwordx4 a[56:59], v250, s[8:11], s46 offen offset:2048 sc1\n"
    "buffer_load_dwordx4 a[60:63], v250, s[8:11], s46 offen offset:3072 sc1\n"
    "s_add_u32 s45, s32, 1\n"
    "s_and_b32 s45, s45, 3\n"
    "s_lshl2_add_u32 s45, s50, s45\n"
    "s_lshl_b32 s45, s45, 13\n"
    "s_add_u32 s45, s45, s48\n"
    "s_add_u32 s46, s45, 4096\n"
    "buffer_load_dwordx4 a[64:67], v250, s[8:11], s45 offen offset:0 sc1\n"
    "buffer_load_dwordx4 a[68:71], v250, s[8:11], s45 offen offset:1024 sc1\n"
    "buffer_load_dwordx4 a[72:75], v250, s[8:11], s45 offen offset:2048 sc1\n"
    "buffer_load_dwordx4 a[76:79], v250, s[8:11], s45 offen offset:3072 sc1\n"
    "buffer_load_dwordx4 a[80:83], v250, s[8:11], s46 offen offset:0 sc1\n"
    "buffer_load_dwordx4 a[84:87], v250, s[8:11], s46 offen offset:1024 sc1\n"
    "buffer_load_dwordx4 a[88:91], v250, s[8:11], s46 offen offset:2048 sc1\n"
    "buffer_load_dwordx4 a[92:95], v250, s[8:11], s46 offen offset:3072 sc1\n"
    "s_add_u32 s45, s32, 2\n"
    "s_and_b32 s45, s45, 3\n"
    "s_lshl2_add_u32 s45, s50, s45\n"
    "s_lshl_b32 s45, s45, 13\n"
    "s_add_u32 s45, s45, s48\n"
    "s_add_u32 s46, s45, 4096\n"
    "buffer_load_dwordx4 a[96:99], v250, s[8:11], s45 offen offset:0 sc1\n"
    "buffer_load_dwordx4 a[100:103], v250, s[8:11], s45 offen offset:1024 sc1\n"
    "buffer_load_dwordx4 a[104:107], v250, s[8:11], s45 offen offset:2048 sc1\n"
    "buffer_load_dwordx4 a[108:111], v250, s[8:11], s45 offen offset:3072 sc1\n"
    "buffer_load_dwordx4 a[112:115], v250, s[8:11], s46 offen offset:0 sc1\n"
    "buffer_load_dwordx4 a[116:119], v250, s[8:11], s46 offen offset:1024 sc1\n"
    "buffer_load_dwordx4 a[120:123], v250, s[8:11], s46 offen offset:2048 sc1\n"
    "buffer_load_dwordx4 a[124:127], v250, s[8:11], s46 offen offset:3072 sc1\n"
    "s_add_u32 s45, s32, 3\n"
    "s_and_b32 s45, s45, 3\n"
    "s_lshl2_add_u32 s45, s50, s45\n"
    "s_lshl_b32 s45, s45, 13\n"
    "s_add_u32 s45, s45, s48\n"
    "s_add_u32 s46, s45, 4096\n"
    "buffer_load_dwordx4 a[128:131], v250, s[8:11], s45 offen offset:0 sc1\n"
    "buffer_load_dwordx4 a[132:135], v250, s[8:11], s45 offen offset:1024 sc1\n"
    "buffer_load_dwordx4 a[136:139], v250, s[8:11], s45 offen offset:2048 sc1\n"
    "buffer_load_dwordx4 a[140:143], v250, s[8:11], s45 offen offset:3072 sc1\n"
    "buffer_load_dwordx4 a[144:147], v250, s[8:11], s46 offen offset:0 sc1\n"
    "buffer_load_dwordx4 a[148:151], v250, s[8:11], s46 offen offset:1024 sc1\n"
    "buffer_load_dwordx4 a[152:155], v250, s[8:11], s46 offen offset:2048 sc1\n"
    "buffer_load_dwordx4 a[156:159], v250, s[8:11], s46 offen offset:3072 sc1\n"
    "s_waitcnt vmcnt(0)\n"
    "v_mov_b32_e32 v200, 0xff800000\n"
    "v_mov_b32_e32 v201, 0xff800000\n"
    "v_mov_b32_e32 v202, 0xff800000\n"
    "v_mov_b32_e32 v203, 0xff800000\n"
    "v_mov_b32_e32 v204, 0xff800000\n"
    "s_lshl_b32 s45, s51, 14\n"
    "s_lshl_b32 s46, s50, 8\n"
    "s_add_u32 s45, s45, s46\n"
    "s_add_u32 s61, s46, 5103616\n"
    "s_movk_i32 s48, 157\n"
    ".Lat_mp_loop:\n"
    "s_min_u32 s45, s45, s61\n"
    "s_add_u32 s46, s45, 128\n"
    "s_add_u32 m0, s43, 16384\n"
    "s_nop 0\n"
    "buffer_load_dwordx4 v248, s[8:11], s45 offen offset:0 sc1 lds\n"
    "buffer_load_dwordx4 v248, s[8:11], s45 offen offset:1024 sc1 lds\n"
    "buffer_load_dwordx4 v248, s[8:11], s45 offen offset:2048 sc1 lds\n"
    "buffer_load_dwordx4 v248, s[8:11], s45 offen offset:3072 sc1 lds\n"
    "s_add_u32 m0, s43, 20480\n"
    "s_nop 0\n"
    "buffer_load_dwordx4 v248, s[8:11], s46 offen offset:0 sc1 lds\n"
    "buffer_load_dwordx4 v248, s[8:11], s46 offen offset:1024 sc1 lds\n"
    "buffer_load_dwordx4 v248, s[8:11], s46 offen offset:2048 sc1 lds\n"
    "buffer_load_dwordx4 v248, s[8:11], s46 offen offset:3072 sc1 lds\n"
    "s_waitcnt vmcnt(0)\n"
    "s_barrier\n"
    "ds_read_b128 a[160:163], v240 offset:16384\n"
    "ds_read_b128 a[164:167], v241 offset:16384\n"
    "ds_read_b128 a[168:171], v240 offset:20480\n"
    "ds_read_b128 a[172:175], v241 offset:20480\n"
    "ds_read_b128 a[176:179], v242 offset:16384\n"
    "ds_read_b128 a[180:183], v243 offset:16384\n"
    "ds_read_b128 a[184:187], v242 offset:20480\n"
    "ds_read_b128 a[188:191], v243 offset:20480\n"
    "s_waitcnt lgkmcnt(0)\n"
    "v_mfma_f32_16x16x32_bf16 v[180:183], a[160:163], a[0:3], 0\n"
    "v_mfma_f32_16x16x32_bf16 v[184:187], a[160:163], a[32:35], 0\n"
    "v_mfma_f32_16x16x32_bf16 v[188:191], a[160:163], a[64:67], 0\n"
    "v_mfma_f32_16x16x32_bf16 v[192:195], a[160:163], a[96:99], 0\n"
    "v_mfma_f32_16x16x32_bf16 v[196:199], a[160:163], a[128:131], 0\n"
    "v_mfma_f32_16x16x32_bf16 v[180:183], a[164:167], a[4:7], v[180:183]\n"
    "v_mfma_f32_16x16x32_bf16 v[184:187], a[164:167], a[36:39], v[184:187]\n"
    "v_mfma_f32_16x16x32_bf16 v[188:191], a[164:167], a[68:71], v[188:191]\n"
    "v_mfma_f32_16x16x32_bf16 v[192:195], a[164:167], a[100:103], v[192:195]\n"
    "v_mfma_f32_16x16x32_bf16 v[196:199], a[164:167], a[132:135], v[196:199]\n"
    "v_mfma_f32_16x16x32_bf16 v[180:183], a[168:171], a[8:11], v[180:183]\n"
    "v_mfma_f32_16x16x32_bf16 v[184:187], a[168:171], a[40:43], v[184:187]\n"
    "v_mfma_f32_16x16x32_bf16 v[188:191], a[168:171], a[72:75], v[188:191]\n"
    "v_mfma_f32_16x16x32_bf16 v[192:195], a[168:171], a[104:107], v[192:195]\n"
    "v_mfma_f32_16x16x32_bf16 v[196:199], a[168:171], a[136:139], v[196:199]\n"
    "v_mfma_f32_16x16x32_bf16 v[180:183], a[172:175], a[12:15], v[180:183]\n"
    "v_mfma_f32_16x16x32_bf16 v[184:187], a[172:175], a[44:47], v[184:187]\n"
    "v_mfma_f32_16x16x32_bf16 v[188:191], a[172:175], a[76:79], v[188:191]\n"
    "v_mfma_f32_16x16x32_bf16 v[192:195], a[172:175], a[108:111], v[192:195]\n"
    "v_mfma_f32_16x16x32_bf16 v[196:199], a[172:175], a[140:143], v[196:199]\n"
    "v_mfma_f32_16x16x32_bf16 v[180:183], a[176:179], a[16:19], v[180:183]\n"
    "v_mfma_f32_16x16x32_bf16 v[184:187], a[176:179], a[48:51], v[184:187]\n"
    "v_mfma_f32_16x16x32_bf16 v[188:191], a[176:179], a[80:83], v[188:191]\n"
    "v_mfma_f32_16x16x32_bf16 v[192:195], a[176:179], a[112:115], v[192:195]\n"
    "v_mfma_f32_16x16x32_bf16 v[196:199], a[176:179], a[144:147], v[196:199]\n"
    "v_mfma_f32_16x16x32_bf16 v[180:183], a[180:183], a[20:23], v[180:183]\n"
    "v_mfma_f32_16x16x32_bf16 v[184:187], a[180:183], a[52:55], v[184:187]\n"
    "v_mfma_f32_16x16x32_bf16 v[188:191], a[180:183], a[84:87], v[188:191]\n"
    "v_mfma_f32_16x16x32_bf16 v[192:195], a[180:183], a[116:119], v[192:195]\n"
    "v_mfma_f32_16x16x32_bf16 v[196:199], a[180:183], a[148:151], v[196:199]\n"
    "v_mfma_f32_16x16x32_bf16 v[180:183], a[184:187], a[24:27], v[180:183]\n"
    "v_mfma_f32_16x16x32_bf16 v[184:187], a[184:187], a[56:59], v[184:187]\n"
    "v_mfma_f32_16x16x32_bf16 v[188:191], a[184:187], a[88:91], v[188:191]\n"
    "v_mfma_f32_16x16x32_bf16 v[192:195], a[184:187], a[120:123], v[192:195]\n"
    "v_mfma_f32_16x16x32_bf16 v[196:199], a[184:187], a[152:155], v[196:199]\n"
    "v_mfma_f32_16x16x32_bf16 v[180:183], a[188:191], a[28:31], v[180:183]\n"
    "v_mfma_f32_16x16x32_bf16 v[184:187], a[188:191], a[60:63], v[184:187]\n"
    "v_mfma_f32_16x16x32_bf16 v[188:191], a[188:191], a[92:95], v[188:191]\n"
    "v_mfma_f32_16x16x32_bf16 v[192:195], a[188:191], a[124:127], v[192:195]\n"
    "v_mfma_f32_16x16x32_bf16 v[196:199], a[188:191], a[156:159], v[196:199]\n"
    "s_nop 15\n"
    "v_max3_f32 v200, v200, v180, v181\n"
    "v_max3_f32 v200, v200, v182, v183\n"
    "v_max3_f32 v201, v201, v184, v185\n"
    "v_max3_f32 v201, v201, v186, v187\n"
    "v_max3_f32 v202, v202, v188, v189\n"
    "v_max3_f32 v202, v202, v190, v191\n"
    "v_max3_f32 v203, v203, v192, v193\n"
    "v_max3_f32 v203, v203, v194, v195\n"
    "v_max3_f32 v204, v204, v196, v197\n"
    "v_max3_f32 v204, v204, v198, v199\n"
    "s_nop 3\n"
    "ds_read_b128 a[160:163], v240 offset:18432\n"
    "ds_read_b128 a[164:167], v241 offset:18432\n"
    "ds_read_b128 a[168:171], v240 offset:22528\n"
    "ds_read_b128 a[172:175], v241 offset:22528\n"
    "ds_read_b128 a[176:179], v242 offset:18432\n"
    "ds_read_b128 a[180:183], v243 offset:18432\n"
    "ds_read_b128 a[184:187], v242 offset:22528\n"
    "ds_read_b128 a[188:191], v243 offset:22528\n"
    "s_waitcnt lgkmcnt(0)\n"
    "v_mfma_f32_16x16x32_bf16 v[180:183], a[160:163], a[0:3], 0\n"
    "v_mfma_f32_16x16x32_bf16 v[184:187], a[160:163], a[32:35], 0\n"
    "v_mfma_f32_16x16x32_bf16 v[188:191], a[160:163], a[64:67], 0\n"
    "v_mfma_f32_16x16x32_bf16 v[192:195], a[160:163], a[96:99], 0\n"
    "v_mfma_f32_16x16x32_bf16 v[196:199], a[160:163], a[128:131], 0\n"
    "v_mfma_f32_16x16x32_bf16 v[180:183], a[164:167], a[4:7], v[180:183]\n"
    "v_mfma_f32_16x16x32_bf16 v[184:187], a[164:167], a[36:39], v[184:187]\n"
    "v_mfma_f32_16x16x32_bf16 v[188:191], a[164:167], a[68:71], v[188:191]\n"
    "v_mfma_f32_16x16x32_bf16 v[192:195], a[164:167], a[100:103], v[192:195]\n"
    "v_mfma_f32_16x16x32_bf16 v[196:199], a[164:167], a[132:135], v[196:199]\n"
    "v_mfma_f32_16x16x32_bf16 v[180:183], a[168:171], a[8:11], v[180:183]\n"
    "v_mfma_f32_16x16x32_bf16 v[184:187], a[168:171], a[40:43], v[184:187]\n"
    "v_mfma_f32_16x16x32_bf16 v[188:191], a[168:171], a[72:75], v[188:191]\n"
    "v_mfma_f32_16x16x32_bf16 v[192:195], a[168:171], a[104:107], v[192:195]\n"
    "v_mfma_f32_16x16x32_bf16 v[196:199], a[168:171], a[136:139], v[196:199]\n"
    "v_mfma_f32_16x16x32_bf16 v[180:183], a[172:175], a[12:15], v[180:183]\n"
    "v_mfma_f32_16x16x32_bf16 v[184:187], a[172:175], a[44:47], v[184:187]\n"
    "v_mfma_f32_16x16x32_bf16 v[188:191], a[172:175], a[76:79], v[188:191]\n"
    "v_mfma_f32_16x16x32_bf16 v[192:195], a[172:175], a[108:111], v[192:195]\n"
    "v_mfma_f32_16x16x32_bf16 v[196:199], a[172:175], a[140:143], v[196:199]\n"
    "v_mfma_f32_16x16x32_bf16 v[180:183], a[176:179], a[16:19], v[180:183]\n"
    "v_mfma_f32_16x16x32_bf16 v[184:187], a[176:179], a[48:51], v[184:187]\n"
    "v_mfma_f32_16x16x32_bf16 v[188:191], a[176:179], a[80:83], v[188:191]\n"
    "v_mfma_f32_16x16x32_bf16 v[192:195], a[176:179], a[112:115], v[192:195]\n"
    "v_mfma_f32_16x16x32_bf16 v[196:199], a[176:179], a[144:147], v[196:199]\n"
    "v_mfma_f32_16x16x32_bf16 v[180:183], a[180:183], a[20:23], v[180:183]\n"
    "v_mfma_f32_16x16x32_bf16 v[184:187], a[180:183], a[52:55], v[184:187]\n"
    "v_mfma_f32_16x16x32_bf16 v[188:191], a[180:183], a[84:87], v[188:191]\n"
    "v_mfma_f32_16x16x32_bf16 v[192:195], a[180:183], a[116:119], v[192:195]\n"
    "v_mfma_f32_16x16x32_bf16 v[196:199], a[180:183], a[148:151], v[196:199]\n"
    "v_mfma_f32_16x16x32_bf16 v[180:183], a[184:187], a[24:27], v[180:183]\n"
    "v_mfma_f32_16x16x32_bf16 v[184:187], a[184:187], a[56:59], v[184:187]\n"
    "v_mfma_f32_16x16x32_bf16 v[188:191], a[184:187], a[88:91], v[188:191]\n"
    "v_mfma_f32_16x16x32_bf16 v[192:195], a[184:187], a[120:123], v[192:195]\n"
    "v_mfma_f32_16x16x32_bf16 v[196:199], a[184:187], a[152:155], v[196:199]\n"
    "v_mfma_f32_16x16x32_bf16 v[180:183], a[188:191], a[28:31], v[180:183]\n"
    "v_mfma_f32_16x16x32_bf16 v[184:187], a[188:191], a[60:63], v[184:187]\n"
    "v_mfma_f32_16x16x32_bf16 v[188:191], a[188:191], a[92:95], v[188:191]\n"
    "v_mfma_f32_16x16x32_bf16 v[192:195], a[188:191], a[124:127], v[192:195]\n"
    "v_mfma_f32_16x16x32_bf16 v[196:199], a[188:191], a[156:159], v[196:199]\n"
    "s_nop 15\n"
    "v_max3_f32 v200, v200, v180, v181\n"
    "v_max3_f32 v200, v200, v182, v183\n"
    "v_max3_f32 v201, v201, v184, v185\n"
    "v_max3_f32 v201, v201, v186, v187\n"
    "v_max3_f32 v202, v202, v188, v189\n"
    "v_max3_f32 v202, v202, v190, v191\n"
    "v_max3_f32 v203, v203, v192, v193\n"
    "v_max3_f32 v203, v203, v194, v195\n"
    "v_max3_f32 v204, v204, v196, v197\n"
    "v_max3_f32 v204, v204, v198, v199\n"
    "s_nop 3\n"
    "s_barrier\n"
    "s_add_u32 s45, s45, 0x8000\n"
    "s_sub_u32 s48, s48, 1\n"
    "s_cmp_lg_u32 s48, 0\n"
    "s_cbranch_scc1 .Lat_mp_loop\n"
    "s_mov_b32 m0, s42\n"
    "s_nop 1\n"
    "ds_write_addtid_b32 v200 offset:0\n"
    "ds_write_addtid_b32 v201 offset:256\n"
    "ds_write_addtid_b32 v202 offset:512\n"
    "ds_write_addtid_b32 v203 offset:768\n"
    "ds_write_addtid_b32 v204 offset:1024\n"
    "s_waitcnt lgkmcnt(0)\n"
    "s_barrier\n"
    "ds_read_b32 v180, v249 offset:0\n"
    "ds_read_b32 v184, v249 offset:256\n"
    "ds_read_b32 v188, v249 offset:512\n"
    "ds_read_b32 v192, v249 offset:768\n"
    "ds_read_b32 v196, v249 offset:1024\n"
    "s_waitcnt lgkmcnt(0)\n"
    "v_max_f32_e32 v205, v200, v180\n"
    "v_mov_b32_e32 v206, v205\n"
    "s_nop 1\n"
    "v_permlane16_swap_b32_e32 v205, v206\n"
    "s_nop 0\n"
    "v_max_f32_e32 v205, v205, v206\n"
    "v_mov_b32_e32 v206, v205\n"
    "s_nop 1\n"
    "v_permlane32_swap_b32_e32 v205, v206\n"
    "s_nop 0\n"
    "v_max_f32_e32 v205, v205, v206\n"
    "v_add_f32_e32 v205, 0x42ae0000, v205\n"
    "v_xor_b32_e32 v220, 0x80000000, v205\n"
    "v_xor_b32_e32 v221, 0x80000000, v205\n"
    "v_xor_b32_e32 v222, 0x80000000, v205\n"
    "v_xor_b32_e32 v223, 0x80000000, v205\n"
    "v_max_f32_e32 v205, v201, v184\n"
    "v_mov_b32_e32 v206, v205\n"
    "s_nop 1\n"
    "v_permlane16_swap_b32_e32 v205, v206\n"
    "s_nop 0\n"
    "v_max_f32_e32 v205, v205, v206\n"
    "v_mov_b32_e32 v206, v205\n"
    "s_nop 1\n"
    "v_permlane32_swap_b32_e32 v205, v206\n"
    "s_nop 0\n"
    "v_max_f32_e32 v205, v205, v206\n"
    "v_add_f32_e32 v205, 0x42ae0000, v205\n"
    "v_xor_b32_e32 v224, 0x80000000, v205\n"
    "v_xor_b32_e32 v225, 0x80000000, v205\n"
    "v_xor_b32_e32 v226, 0x80000000, v205\n"
    "v_xor_b32_e32 v227, 0x80000000, v205\n"
    "v_max_f32_e32 v205, v202, v188\n"
    "v_mov_b32_e32 v206, v205\n"
    "s_nop 1\n"
    "v_permlane16_swap_b32_e32 v205, v206\n"
    "s_nop 0\n"
    "v_max_f32_e32 v205, v205, v206\n"
    "v_mov_b32_e32 v206, v205\n"
    "s_nop 1\n"
    "v_permlane32_swap_b32_e32 v205, v206\n"
    "s_nop 0\n"
    "v_max_f32_e32 v205, v205, v206\n"
    "v_add_f32_e32 v205, 0x42ae0000, v205\n"
    "v_xor_b32_e32 v228, 0x80000000, v205\n"
    "v_xor_b32_e32 v229, 0x80000000, v205\n"
    "v_xor_b32_e32 v230, 0x80000000, v205\n"
    "v_xor_b32_e32 v231, 0x80000000, v205\n"
    "v_max_f32_e32 v205, v203, v192\n"
    "v_mov_b32_e32 v206, v205\n"
    "s_nop 1\n"
    "v_permlane16_swap_b32_e32 v205, v206\n"
    "s_nop 0\n"
    "v_max_f32_e32 v205, v205, v206\n"
    "v_mov_b32_e32 v206, v205\n"
    "s_nop 1\n"
    "v_permlane32_swap_b32_e32 v205, v206\n"
    "s_nop 0\n"
    "v_max_f32_e32 v205, v205, v206\n"
    "v_add_f32_e32 v205, 0x42ae0000, v205\n"
    "v_xor_b32_e32 v232, 0x80000000, v205\n"
    "v_xor_b32_e32 v233, 0x80000000, v205\n"
    "v_xor_b32_e32 v234, 0x80000000, v205\n"
    "v_xor_b32_e32 v235, 0x80000000, v205\n"
    "v_max_f32_e32 v205, v204, v196\n"
    "v_mov_b32_e32 v206, v205\n"
    "s_nop 1\n"
    "v_permlane16_swap_b32_e32 v205, v206\n"
    "s_nop 0\n"
    "v_max_f32_e32 v205, v205, v206\n"
    "v_mov_b32_e32 v206, v205\n"
    "s_nop 1\n"
    "v_permlane32_swap_b32_e32 v205, v206\n"
    "s_nop 0\n"
    "v_max_f32_e32 v205, v205, v206\n"
    "v_add_f32_e32 v205, 0x42ae0000, v205\n"
    "v_xor_b32_e32 v236, 0x80000000, v205\n"
    "v_xor_b32_e32 v237, 0x80000000, v205\n"
    "v_xor_b32_e32 v238, 0x80000000, v205\n"
    "v_xor_b32_e32 v239, 0x80000000, v205\n"
    "s_nop 1\n"
    "s_barrier\n"
    "s_mul_i32 s48, s33, 81920\n"
    "s_add_u32 s48, s48, 11345920\n"
    "s_lshl_b32 s46, s93, 11\n"
    "s_add_u32 s48, s48, s46\n"
    "s_add_u32 s46, s46, 49152\n"
    "s_and_b32 s45, s32, 1\n"
    "s_xor_b32 s45, s45, 8\n"
    "s_lshl_b32 s45, s45, 13\n"
    "s_add_u32 s45, s45, s48\n"
    "s_add_u32 m0, s46, 0\n"
    "s_nop 0\n"
    "buffer_load_dwordx4 v250, s[8:11], s45 offen offset:0 sc1 lds\n"
    "buffer_load_dwordx4 v250, s[8:11], s45 offen offset:1024 sc1 lds\n"
    "s_branch .Lat_maxpass_ret\n"
    ".p2align 12\n"
    ".Lat_out:\n"
    "s_waitcnt lgkmcnt(0)\n"
    "s_mov_b64 s[2:3], s[0:1]\n"
    "s_mov_b64 %0, s[2:3]\n"
    "s_mov_b32 %1, s92\n"
    "s_mov_b32 %2, s93\n"
    : "=s"(ka), "=s"(b), "=s"(wid) : "0"(__builtin_amdgcn_kernarg_segment_ptr()), "1"(blockIdx.x)
    : "v0", "v1", "v2", "v3", "v4", "v5", "v6", "v7", "v8", "v9", "v10", "v11", "v12", "v13", "v14", "v15", "v16", "v17", "v18", "v19", "v20", "v21", "v22", "v23", "v24", "v25", "v26", "v27", "v28", "v29", "v30", "v31", "v32", "v33", "v34", "v35", "v36", "v37", "v38", "v39", "v40", "v41", "v42", "v43", "v44", "v45", "v46", "v47", "v48", "v49", "v50", "v51", "v52", "v53", "v54", "v55", "v56", "v57", "v58", "v59", "v60", "v61", "v62", "v63", "v64", "v65", "v66", "v67", "v68", "v69", "v70", "v71", "v72", "v73", "v74", "v75", "v76", "v77", "v78", "v79", "v80", "v81", "v82", "v83", "v84", "v85", "v86", "v87", "v88", "v89", "v90", "v91", "v92", "v93", "v94", "v95", "v96", "v97", "v98", "v99", "v100", "v101", "v102", "v103", "v104", "v105", "v106", "v107", "v108", "v109", "v110", "v111", "v112", "v113", "v114", "v115", "v116", "v117", "v118", "v119", "v120", "v121", "v122", "v123", "v124", "v125", "v126", "v127", "v128", "v129", "v130", "v131", "v132", "v133", "v134", "v135", "v136", "v137", "v138", "v139", "v140", "v141", "v142", "v143", "v144", "v145", "v146", "v147", "v148", "v149", "v150", "v151", "v152", "v153", "v154", "v155", "v156", "v157", "v158", "v159", "v160", "v161", "v162", "v163", "v164", "v165", "v166", "v167", "v168", "v169", "v170", "v171", "v172", "v173", "v174", "v175", "v176", "v177", "v178", "v179", "v180", "v181", "v182", "v183", "v184", "v185", "v186", "v187", "v188", "v189", "v190", "v191", "v192", "v193", "v194", "v195", "v196", "v197", "v198", "v199", "v200", "v201", "v202", "v203", "v204", "v205", "v206", "v207", "v208", "v209", "v210", "v211", "v212", "v213", "v214", "v215", "v216", "v217", "v218", "v219", "v220", "v221", "v222", "v223", "v224", "v225", "v226", "v227", "v228", "v229", "v230", "v231", "v232", "v233", "v234", "v235", "v236", "v237", "v238", "v239", "v240", "v241", "v242", "v243", "v244", "v245", "v246", "v247", "v248", "v249", "v250", "v251", "v252", "v253", "v254", "v255", "a0", "a1", "a2", "a3", "a4", "a5", "a6", "a7", "a8", "a9", "a10", "a11", "a12", "a13", "a14", "a15", "a16", "a17", "a18", "a19", "a20", "a21", "a22", "a23", "a24", "a25", "a26", "a27", "a28", "a29", "a30", "a31", "a32", "a33", "a34", "a35", "a36", "a37", "a38", "a39", "a40", "a41", "a42", "a43", "a44", "a45", "a46", "a47", "a48", "a49", "a50", "a51", "a52", "a53", "a54", "a55", "a56", "a57", "a58", "a59", "a60", "a61", "a62", "a63", "a64", "a65", "a66", "a67", "a68", "a69", "a70", "a71", "a72", "a73", "a74", "a75", "a76", "a77", "a78", "a79", "a80", "a81", "a82", "a83", "a84", "a85", "a86", "a87", "a88", "a89", "a90", "a91", "a92", "a93", "a94", "a95", "a96", "a97", "a98", "a99", "a100", "a101", "a102", "a103", "a104", "a105", "a106", "a107", "a108", "a109", "a110", "a111", "a112", "a113", "a114", "a115", "a116", "a117", "a118", "a119", "a120", "a121", "a122", "a123", "a124", "a125", "a126", "a127", "a128", "a129", "a130", "a131", "a132", "a133", "a134", "a135", "a136", "a137", "a138", "a139", "a140", "a141", "a142", "a143", "a144", "a145", "a146", "a147", "a148", "a149", "a150", "a151", "a152", "a153", "a154", "a155", "a156", "a157", "a158", "a159", "a160", "a161", "a162", "a163", "a164", "a165", "a166", "a167", "a168", "a169", "a170", "a171", "a172", "a173", "a174", "a175", "a176", "a177", "a178", "a179", "a180", "a181", "a182", "a183", "a184", "a185", "a186", "a187", "a188", "a189", "a190", "a191", "a192", "a193", "a194", "a195", "a196", "a197", "a198", "a199", "a200", "a201", "a202", "a203", "a204", "a205", "a206", "a207", "a208", "a209", "a210", "a211", "a212", "a213", "a214", "a215", "a216", "a217", "a218", "a219", "a220", "a221", "a222", "a223", "a224", "a225", "a226", "a227", "a228", "a229", "a230", "a231", "a232", "a233", "a234", "a235", "a236", "a237", "a238", "a239", "a240", "a241", "a242", "a243", "a244", "a245", "a246", "a247", "a248", "a249", "a250", "a251", "a252", "a253", "a254", "a255", "s2", "s3", "s4", "s5", "s6", "s7", "s8", "s9", "s10", "s11", "s12", "s13", "s14", "s15", "s16", "s17", "s18", "s19", "s20", "s21", "s22", "s23", "s24", "s25", "s26", "s27", "s28", "s29", "s30", "s31", "s32", "s33", "s34", "s35", "s36", "s37", "s38", "s39", "s40", "s41", "s42", "s43", "s44", "s45", "s46", "s47", "s48", "s49", "s50", "s51", "s52", "s53", "s54", "s55", "s56", "s57", "s58", "s59", "s60", "s61", "s62", "s63", "s80", "s81", "s82", "s83", "s84", "s85", "s86", "s87", "s88", "s89", "s90", "s91", "s92", "s93", "s94", "s95", "vcc", "m0", "scc", "memory");
  typedef __attribute__((address_space(1))) void* gp;
  helper(lds, b >> 1 & 3, wid, (const int*)(gp)ka[5], (const int*)(gp)ka[3], (const u16*)(gp)ka[8], (const u16*)(gp)ka[9], (const u16*)(gp)ka[10], (const u16*)(gp)ka[7], (char*)(gp)ka[2]);
}

extern "C" void kernel_launch(void* const* d_in, const int* in_sizes, int n_in, void* d_out, int out_size, void* d_ws,
                              size_t ws_size, hipStream_t stream) {
  const u16* x = (const u16*)d_in[0];
  const int* src = (const int*)d_in[1];
  const int* dst = (const int*)d_in[2];
  const u16* deg_inv = (const u16*)d_in[3];
  const u16* wmsg = (const u16*)d_in[4];
  const u16* wq = (const u16*)d_in[5];
  const u16* wk = (const u16*)d_in[6];
  const u16* wv = (const u16*)d_in[7];
  const u16* wout = (const u16*)d_in[8];
  char* ws = (char*)d_ws;
  hipFuncSetAttribute((const void*)gps_kernel, hipFuncAttributeMaxDynamicSharedMemorySize, 163840);
  gps_kernel<<<256, 256, 163840, stream>>>(wq, wk, ws, dst, x, src, (u16*)d_out, deg_inv, wv, wmsg, wout);
}
```

```cpp
#include <hip/hip_runtime.h>
#include <hip/hip_bf16.h>
#include <stdint.h>

typedef unsigned short u16;
typedef __attribute__((ext_vector_type(8))) short bf16x8;
typedef __attribute__((ext_vector_type(4))) short bf16x4;
typedef __attribute__((ext_vector_type(4))) float f32x4;
typedef __attribute__((ext_vector_type(2))) float f32x2;
typedef __attribute__((ext_vector_type(4))) unsigned u32x4;
typedef __attribute__((ext_vector_type(2))) unsigned u32x2;
#define DI __device__ __forceinline__

constexpr int NN = 10000, DD = 256, EE = 160000;
constexpr int NATT = 252, NSP = 4, SPN = 2560;
constexpr int GU = 5, GNB = 5; static_assert(GU * GNB <= 30, "list size");

constexpr unsigned WS_H = 0;
constexpr unsigned WS_AF = WS_H + 10240 * 512;
constexpr unsigned WS_WV = WS_AF + 135168;
constexpr unsigned WS_WM = WS_WV + 135168;
constexpr unsigned WS_XF = WS_WM + 135168;
constexpr unsigned WS_GF = WS_XF + 3328 * 128;
constexpr unsigned WS_HF = WS_GF + 2048;
constexpr unsigned WS_PF = WS_HF + 4096;
constexpr unsigned WS_NF = WS_PF + 4096;
constexpr unsigned WS_WF = WS_NF + 16384;
constexpr unsigned WS_LF = WS_WF + 256;
constexpr unsigned WS_EP = WS_LF + 512;
constexpr unsigned WS_G = WS_EP + 256 + 1024;
constexpr unsigned WS_QP = WS_G + 10240 * 512;
constexpr unsigned WS_XB = WS_QP + 10240 * 512;
constexpr unsigned WS_LS = WS_XB + 2 * NATT * 101376;
constexpr unsigned WS_BL = WS_LS + 524288;
constexpr unsigned WS_DBG = WS_BL + 64 * 260 * 64;
constexpr unsigned WS_END = WS_DBG + 1024 * 128 + 8192;

DI float bf2f(unsigned x) { return __uint_as_float(x << 16); }
DI float bfhi(unsigned x) { return __uint_as_float(x & 0xffff0000u); }
DI unsigned pk2(float lo, float hi) {
  typedef __attribute__((ext_vector_type(2))) __bf16 bf2_t;
  const bf2_t r = __builtin_convertvector((f32x2){lo, hi}, bf2_t);
  return __builtin_bit_cast(unsigned, r);
}
typedef __attribute__((address_space(3))) bf16x4* lds_bf16x4_p;
DI bf16x4 tr_read(unsigned p) { return __builtin_amdgcn_ds_read_tr16_b64_v4i16((lds_bf16x4_p)p); }
DI bf16x8 as_bf(u32x4 v) { return __builtin_bit_cast(bf16x8, v); }
#define SB() __builtin_amdgcn_sched_barrier(0)
#define MFMA16(a, b, c) __builtin_amdgcn_mfma_f32_16x16x32_bf16(a, b, c, 0, 0, 0)

typedef __amdgpu_buffer_rsrc_t srd_t;
DI void drain() { asm volatile("s_waitcnt vmcnt(0)" ::: "memory"); }
DI void st_flag(srd_t w, unsigned off, unsigned tag) { __builtin_amdgcn_raw_buffer_store_b32(tag, w, off, 0, 16); }
DI void wait_flag(srd_t w, unsigned off, unsigned tag) {
#pragma unroll 1
  for (int it = 0; it < (1 << 22); ++it) {
    unsigned v;
    asm volatile("buffer_load_dword %0, %1, %2, 0 offen sc1\n s_waitcnt vmcnt(0)" : "=v"(v) : "v"(off), "s"(w) : "memory");
    if (__ballot(v != tag) == 0) break;
    __builtin_amdgcn_s_sleep(2);
    asm volatile("" ::: "memory");
  }
  asm volatile("" ::: "memory");
}

DI void prod_store(const f32x4 (&acc)[2][2], int mat, int ks, int J, int lane, srd_t wsrd) {
#pragma unroll
  for (int s = 0; s < 2; ++s) {
    u32x4 o = {pk2(acc[0][s][0], acc[0][s][1]), pk2(acc[0][s][2], acc[0][s][3]), pk2(acc[1][s][0], acc[1][s][1]), pk2(acc[1][s][2], acc[1][s][3])};
    if (ks == 7 && lane >= 48) o[3] &= 0xffffu;
    const int T = 2 * J + s;
    const int at = mat * 135168 + ks * 4096 + (T & 3) * 1024;
    __builtin_amdgcn_raw_buffer_store_b128(o, wsrd, WS_WV + (T >> 2) * 33792 + at + lane * 16, 0, 16);
  }
}
DI void corr(unsigned* dl, const unsigned (&lr)[16]) {
  float a0 = 0.f, a1 = 0.f;
#pragma unroll
  for (int r = 0; r < 16; ++r) { a0 += bf2f(lr[r] & 0xffffu); a1 += bfhi(lr[r]); }
  dl[0] = pk2(a0 * -0.0625f, a1 * -0.0625f);
  dl[128] = pk2((bf2f(lr[15] & 0xffffu) - 0.25f * a0) * (1.f / 3.f), (bfhi(lr[15]) - 0.25f * a1) * (1.f / 3.f));
}
DI void product_load(char* lds, int wid, int lane, int job, const u16* __restrict__ wv, const u16* __restrict__ wm, const u16* __restrict__ wo) {
  const unsigned ldsb = (unsigned)(size_t)lds;
  const int r = lane >> 3, sw = ((lane & 7) ^ (r & 6)) * 16 + wid * 128;
  const unsigned vp = (8 * (r >> 2) + (r & 3)) * 512 + sw, vn = r * 512 + sw;
  auto image = [&](int n, const u16* w, int row0, bool nat) {
#pragma unroll
    for (int i = 0; i < 8; ++i) {
      const int a = i & 3;
      asm volatile("s_mov_b32 m0, %2\n s_nop 0\n global_load_lds_dwordx4 %0, %1"
                   :: "v"(nat ? vn : vp), "s"(w + (row0 + (i >> 2) * 32 + (nat ? 8 * a : 16 * (a & 1) + 4 * (a >> 1))) * DD),
                      "s"(ldsb + n * 32768 + (i >> 2) * 16384 + wid * 4096 + a * 1024) : "m0", "memory");
    }
  };
  image(2, wv, (job >> 2) * 64, true);
  image(3, wm, (job >> 2) * 64, false);
#pragma unroll
  for (int k = 0; k < 8; ++k)
    asm volatile("s_mov_b32 m0, %2\n s_nop 0\n global_load_lds_dwordx4 %0, %1"
                 :: "v"((lane >> 3) * 512 + (lane & 7) * 16), "s"(wo + (k * 32 + wid * 8) * DD + (job & 3) * 64), "s"(ldsb + k * 4096 + wid * 1024) : "m0", "memory");
}
DI void product_w(char* lds, int wid, int lane, int job, srd_t wsrd) {
  const int m = lane & 15, g = lane >> 4;
  const int ks = 2 * (job >> 2) + (wid >> 1), J = 2 * (job & 3) + (wid & 1);
  const unsigned ldsb = (unsigned)(size_t)lds;
  unsigned* dl = reinterpret_cast<unsigned*>(lds + 0x20000);
  const bool hi = (m >> 3) != 0;
  const unsigned ra = (m >> 3) * 1024 + (m & 7) * 128 + (g ^ (m & 6)) * 16;
  auto frag = [&](int n, int tl, int t, int k) {
    return *reinterpret_cast<const bf16x8*>(lds + n * 32768 + tl * 16384 + (k >> 1) * 4096 + t * 2048 + (ra ^ ((k & 1) * 64)));
  };
  f32x4 av[2][2] = {}, am[2][2] = {}, ad[2] = {};
  const unsigned tb = ldsb + (8 * g + ((lane >> 2) & 3)) * 128 + (wid & 1) * 64 + (lane & 3) * 16;
  bf16x8 d0[8], d1[8], d2[8], rr[8][2], vf[8][2], mf[8][2];
#pragma unroll
  for (int kk = 0; kk < 9; ++kk) {
    if (kk < 8) {
      d0[kk] = *reinterpret_cast<const bf16x8*>(dl + 512 + 16 * kk + 4 * g); d1[kk] = *reinterpret_cast<const bf16x8*>(dl + 512 + (ks == 7 ? 128 : 0) + 16 * kk + 4 * g);
      d2[kk] = *reinterpret_cast<const bf16x8*>(dl + 768 + (ks == 7 && hi ? 128 : 0) + 16 * kk + 4 * g);
#pragma unroll
      for (int s = 0; s < 2; ++s) {
        rr[kk][s] = __builtin_shufflevector(tr_read(tb + kk * 4096 + s * 8), tr_read(tb + kk * 4096 + s * 8 + 512), 0, 1, 2, 3, 4, 5, 6, 7);
        vf[kk][s] = frag(2, wid >> 1, s, kk); mf[kk][s] = frag(3, wid >> 1, s, kk);
      }
    }
    SB();
    if (kk) {
      const int k = kk - 1;
#pragma unroll
      for (int s = 0; s < 2; ++s) {
        av[0][s] = MFMA16(d0[k], rr[k][s], av[0][s]); av[1][s] = MFMA16(d1[k], rr[k][s], av[1][s]); ad[s] = MFMA16(d2[k], rr[k][s], ad[s]);
#pragma unroll
        for (int t = 0; t < 2; ++t) { av[t][s] = MFMA16(vf[k][t], rr[k][s], av[t][s]); am[t][s] = MFMA16(mf[k][t], rr[k][s], am[t][s]); }
      }
    }
    SB();
  }
#pragma unroll
  for (int t = 0; t < 2; ++t)
#pragma unroll
    for (int s = 0; s < 2; ++s) am[t][s] += ad[s];
  prod_store(av, 0, ks, J, lane, wsrd);
  prod_store(am, 1, ks, J, lane, wsrd);
}

DI void helper(char* lds, int j, int wid, const int* __restrict__ src, const int* __restrict__ dst,
               const u16* __restrict__ wv, const u16* __restrict__ wm, const u16* __restrict__ wo, const u16* dinv, char* ws) {
  unsigned* img = reinterpret_cast<unsigned*>(lds);
  char* cntb = lds + 0x20000;
  int* cnt = reinterpret_cast<int*>(cntb);
  unsigned* ub = reinterpret_cast<unsigned*>(lds + 0x22c00);
  int* red = reinterpret_cast<int*>(ub + 256);
  u16* dg = reinterpret_cast<u16*>(lds + 0x23400);
  u16* tmp = reinterpret_cast<u16*>(lds + 0x25000) + wid * 512;
  int lane; asm volatile("v_mbcnt_lo_u32_b32 %0, -1, 0\n\tv_mbcnt_hi_u32_b32 %0, -1, %0" : "=v"(lane));
  const int tid = wid * 64 + lane, lo = j * SPN, nn_ = min(SPN, NN - lo);
  const srd_t wsrd = __builtin_amdgcn_make_buffer_rsrc(ws, 0, WS_END, 0x00020000);
  const unsigned tag = __builtin_amdgcn_raw_buffer_load_b32(wsrd, WS_EP, 0, 0) + 4u;
  auto touch = [&](unsigned at) { asm volatile("s_mov_b32 m0, 0x27c00\n s_nop 0\n buffer_load_dword %0, %1, 0 offen sc1 lds" :: "v"(at), "s"(wsrd) : "m0", "memory"); };
  touch(lane < 16 ? min(WS_GF + (lane * 16 + wid * 4 + j) * 128, WS_EP) : 0x40000000u);
  for (int i = tid; i < 8192; i += 256) { const unsigned f = (i & 63) >= 60 ? 0u : ~0u; reinterpret_cast<u32x4*>(img)[i] = (u32x4){f, f, f, f}; }
  for (int i = tid; i < SPN + 64; i += 256) cnt[i] = 0;
  ub[tid] = 0u;
  if (tid < 8) red[tid] = 0;
  touch(WS_XF + 262144 + (j * 256 + tid) * 128);
  wait_flag(wsrd, WS_NF + j * 4096 + min(tid, 249) * 16, tag);
  __syncthreads();
  const unsigned dum = (SPN + lane) << 18;
  auto fetch = [&](int k) {
    return __builtin_amdgcn_raw_buffer_load_b128(wsrd, WS_BL + (k < 48 ? tid * 16 + (k & 7) * 4096 : (tid >> 2) * 128 + (tid & 3) * 16 + (k & 3) * 8192 + (k & 4) * 16), (j * 8 + (k >> 3)) * 33280, 16);
  };
  auto clean = [&](u32x4& e, int k) {
#pragma unroll
    for (int r = 0; r < 4; ++r) e[r] = (k < 48 ? (k & 7) == 7 && tid >= 208 : (k & 3) == 3 && tid >= 232) ? dum : min(e[r], dum);
  };
  auto slot = [&](unsigned e) { return reinterpret_cast<int*>(cntb + (e >> 16)); };
  constexpr int NL = 52;
  u32x4 ent[NL];
#pragma unroll
  for (int k = 0; k < NL; ++k) ent[k] = fetch(k);
  SB();
#pragma unroll
  for (int k = 0; k < NL; ++k) {
    clean(ent[k], k);
    int* p[4];
#pragma unroll
    for (int r = 0; r < 4; ++r) p[r] = slot(ent[k][r]);
    SB();
#pragma unroll
    for (int r = 0; r < 4; ++r) atomicAdd(p[r], 2);
    SB();
  }
  unsigned use = 0;
  const unsigned dum3 = (tid & 3) == 3 ? dum : 0;
#pragma unroll
  for (int k = 0; k < 4; ++k) use |= __ballot(ent[NL - 4 + k][3] < dum3) ? 1u << k : 0u;
  if (use)
#pragma unroll 1
    for (int k = NL; k < 64; ++k)
      if (use >> (k - NL) & 1) {
        u32x4 e = fetch(k); clean(e, k);
#pragma unroll
        for (int r = 0; r < 4; ++r) atomicAdd(slot(e[r]), 2);
        if (__ballot(e[3] < dum3)) { use |= 16u << (k - NL); if (k >= 60) red[7] = 1; }
      }
  __syncthreads();
  int inc = 0, nu[10], cn[10], cm = 0;
  auto units = [&](int c) { return max(1, (c + 6) >> 3); };
#pragma unroll
  for (int i = 0; i < 10; i += 2) { const u32x2 v = *reinterpret_cast<u32x2*>(cnt + 10 * tid + i); cn[i] = v[0]; cn[i + 1] = v[1]; }
  const int nup = units(cnt[max(10 * tid - 1, 0)]);
  const bool act = 10 * tid < nn_;
#pragma unroll
  for (int i = 0; i < 10; ++i) { cm = max(cm, cn[i]); nu[i] = act ? units(cn[i]) : 0; inc += nu[i]; }
  const int own = inc;
  inc += __builtin_amdgcn_update_dpp(0, inc, 0x111, 15, 15, true); inc += __builtin_amdgcn_update_dpp(0, inc, 0x112, 15, 15, true);
  inc += __builtin_amdgcn_update_dpp(0, inc, 0x114, 15, 15, true); inc += __builtin_amdgcn_update_dpp(0, inc, 0x118, 15, 15, true);
  inc += __builtin_amdgcn_update_dpp(0, inc, 0x142, 10, 15, false); inc += __builtin_amdgcn_update_dpp(0, inc, 0x143, 12, 15, false);
  if (lane == 63) red[wid] = inc;
  const bool srt = __ballot(cm > 64);
  __syncthreads();
  const u32x4 rd = *reinterpret_cast<u32x4*>(red);
  const float rs_ = 64.f / (float)(rd[0] + rd[1] + rd[2] + rd[3]);
  inc += (wid > 0 ? rd[0] : 0) + (wid > 1 ? rd[1] : 0) + (wid > 2 ? rd[2] : 0) - own;
  int wv_[10];
  {
    int o = inc, wp = tid ? min(127, (int)((float)(2 * o - nup) * rs_)) : 128;
#pragma unroll
    for (int i = 0; i < 10; ++i) {
      wv_[i] = min(127, (int)((float)(2 * o + nu[i]) * rs_));
      ub[act && wv_[i] != wp ? wv_[i] : 128 + lane] = (unsigned)o << 12 | (unsigned)(10 * tid + i);
      wp = wv_[i]; o += nu[i];
    }
  }
  __syncthreads();
  {
    int u0[10], top = 0;
#pragma unroll
    for (int i = 0; i < 10; ++i) u0[i] = ub[wv_[i]] >> 12;
#pragma unroll
    for (int i = 0; i < 10; ++i) {
      const int lu = inc - u0[i], e = lu + nu[i] - 1 + GU, q = min(e / (4 * GU), GNB);
      u0[i] = wv_[i] * 1024 + lu * 8; top = max(top, q);
      atomicOr(img + wv_[i] * 256 + 244 + q, act ? 1u << (e - q * 4 * GU) : 0u);
      inc += nu[i];
    }
#pragma unroll
    for (int i = 0; i < 10; i += 2) *reinterpret_cast<u32x2*>(cnt + 10 * tid + i) = (u32x2){(unsigned)u0[i], (unsigned)u0[i + 1]};
    if (tid < 64) cnt[SPN + tid] = 0x30000;
    if (act && top == GNB) red[7] = 1;
  }
  __syncthreads();
  const bool big_ = red[7];
  if (tid < 128) {
    unsigned nb = 0;
#pragma unroll
    for (int q = 0; q < GNB; ++q) nb = img[tid * 256 + 244 + q] ? q + 1 : nb;
    *reinterpret_cast<u32x4*>(img + tid * 256 + 240) = (u32x4){big_ ? 0u : nb, WS_G + (lo + (ub[tid] & 4095u)) * 512, big_ || (tid & 3) ? 0x40000000u : WS_GF + j * 512 + tid * 4, 0u};
  }
  if (!big_) {
    const unsigned dmp = 0x27000 + lane * 4;
    typedef __attribute__((address_space(3))) u16* lds_u16_p;
    unsigned p[2][8];
#pragma unroll
    for (int k = 0; k < NL + 2; k += 2) {
      if (k < NL) {
        int* q[8];
#pragma unroll
        for (int r = 0; r < 8; ++r) q[r] = slot(ent[k + (r >> 2)][r & 3]);
        SB();
#pragma unroll
        for (int r = 0; r < 8; ++r) p[k >> 1 & 1][r] = atomicAdd(q[r], 2);
        SB();
      }
      if (k) {
#pragma unroll
        for (int r = 0; r < 8; ++r) p[~k >> 1 & 1][r] = min(p[~k >> 1 & 1][r], dmp);
        SB();
#pragma unroll
        for (int r = 0; r < 8; ++r) *(lds_u16_p)p[~k >> 1 & 1][r] = ent[k - 2 + (r >> 2)][r & 3];
        SB();
      }
    }
    if (use)
#pragma unroll 1
      for (int k = NL; k < 64; ++k)
        if (use >> (k - NL) & 1) {
          u32x4 e = fetch(k); clean(e, k);
#pragma unroll
          for (int r = 0; r < 4; ++r) *(lds_u16_p)min((unsigned)atomicAdd(slot(e[r]), 2), dmp) = e[r];
        }
    __syncthreads();
    if (srt) {
#pragma unroll
      for (int i = 0; i < 10; ++i) dg[10 * tid + i] = cn[i] >> 1;
      for (unsigned long long big = __ballot(cm > 64); big; big &= big - 1)
      for (int n = 10 * (wid * 64 + __builtin_ctzll(big)), n1 = n + 10; n < n1; ++n) {
        const int d = __builtin_amdgcn_readfirstlane(dg[n]);
        if (d <= 32) continue;
        u16* f = reinterpret_cast<u16*>(lds + cnt[n]) - d;
        for (int i = lane; i < d + 8; i += 64) tmp[i] = i < d ? f[i] : 0xffff;
        __builtin_amdgcn_wave_barrier();
        for (int i = lane; i < d; i += 64) {
          const unsigned key = tmp[i] << 9 | i; int p_ = 0;
          for (int q = 0; q < d; q += 8) {
            const u32x4 v = *reinterpret_cast<u32x4*>(tmp + q);
#pragma unroll
            for (int e = 0; e < 4; ++e) p_ += (((v[e] & 0xffffu) << 9 | (q + 2 * e)) < key) + (((v[e] >> 16) << 9 | (q + 2 * e + 1)) < key);
          }
          f[p_] = key >> 9;
        }
        __builtin_amdgcn_wave_barrier();
      }
    }
  }
  __syncthreads();
#pragma unroll 8
  for (int k = 0; k < 32; ++k)
    __builtin_amdgcn_raw_buffer_store_b128(reinterpret_cast<u32x4*>(img)[k * 256 + tid], wsrd, WS_LS + j * 131072 + (k * 256 + tid) * 16, 0, 16);
  unsigned lr[16];
#pragma unroll
  for (int r = 0; r < 16; ++r) lr[r] = reinterpret_cast<const unsigned*>((wid >= 2 ? wm : wv) + (240 + r) * DD)[tid & 127];
  wait_flag(wsrd, WS_HF + min(tid, NATT - 1) * 16, tag);
  drain();
  __syncthreads();
  if (tid == 0) st_flag(wsrd, WS_LF + j * 128, tag);
  asm volatile("s_mov_b32 m0, 0x27c00\n s_nop 0\n global_load_lds_dword %0, %1" :: "v"(min(tid * 128, NN * 2 - 4)), "s"(dinv) : "m0", "memory");
  touch(WS_XF + (j * 256 + tid) * 256);
  touch(wid ? 0x40000000u : WS_GF - 32768 + (j * 64 + lane) * 128);
  corr(reinterpret_cast<unsigned*>(lds + 0x20000) + 512 + (wid >> 1) * 256 + (tid & 127), lr);
#pragma unroll 1
  for (int job = 4 * j; job < 4 * j + 4; ++job) {
    product_load(lds, wid, lane, job, wv, wm, wo);
    drain();
    __syncthreads();
    product_w(lds, wid, lane, job, wsrd);
    __syncthreads();
  }
  drain();
  __syncthreads();
  if (tid == 0) st_flag(wsrd, WS_WF + j * 4, tag);
  if (big_) {
    const srd_t msrd = __builtin_amdgcn_make_buffer_rsrc(ws + WS_H, 0, 10240 * 512, 0x00020000);
    bf16x8 b1;
#pragma unroll
    for (int e = 0; e < 8; ++e) b1[e] = (lane & 15) == e + ((lane >> 1) & 8) ? 0x3f80 : 0;
    for (int n = wid; n < nn_; n += 4) {
      f32x4 acc = {};
      for (int e0 = 0; e0 < EE; e0 += 64) {
        unsigned long long m = __ballot(dst[e0 + lane] == lo + n);
        while (m) {
          const int e = e0 + __builtin_ctzll(m); m &= m - 1;
          const unsigned vo = lane < 32 ? (unsigned)src[e] * 512 + lane * 16 : 0x7fffff00u;
          acc = MFMA16(b1, as_bf(__builtin_amdgcn_raw_buffer_load_b128(msrd, vo, 0, 16)), acc);
        }
      }
      __builtin_amdgcn_raw_buffer_store_b64((u32x2){pk2(acc[0], acc[1]), pk2(acc[2], acc[3])}, wsrd,
                                            WS_G + (lo + n) * 512 + (lane >> 5) * 256 + (lane & 15) * 16 + (lane & 16) / 2, 0, 16);
    }
    drain();
    __syncthreads();
    if (tid < 32) st_flag(wsrd, WS_GF + j * 512 + tid * 16, tag);
  }
}

extern "C" __global__ __launch_bounds__(256, 1) void gps_kernel(const u16* wq, const u16* wk, char* ws, const int* dst, const u16* x, const int* src, u16* out,
                                                                const u16* deg_inv, const u16* wv, const u16* wm, const u16* wo) {
  extern __shared__ __attribute__((aligned(16))) char lds[];
  static_assert(WS_END == 69410816 && WS_EP == 6101760, "workspace layout of the generated code");
  typedef void* const __attribute__((address_space(4)))* arg_p;
  arg_p ka; int b, wid;
  asm volatile(
    "s_load_dwordx16 s[4:19], s[0:1], 0x0\n"
    "s_mov_b32 s92, s2\n"
    "s_getpc_b64 s[30:31]\n"
    "v_readfirstlane_b32 s93, v0\n"
    "s_bfe_u32 s45, s92, 0x40003\n"
    "v_lshlrev_b32_e32 v1, 5, v0\n"
    "s_lshl_b32 s45, s45, 13\n"
    "s_mov_b32 m0, 0x27f00\n"
    "v_add_u32_e32 v1, s45, v1\n"
    "v_min_u32_e32 v1, 76488, v1\n"
    "global_load_lds_dword v1, s[30:31]\n"
    "s_and_b32 s45, s92, 249\n"
    "s_lshr_b32 s93, s93, 6\n"
    "s_cmp_eq_u32 s45, 249\n"
    "s_cbranch_scc1 .Lat_out\n"
    "v_mbcnt_lo_u32_b32 v24, -1, 0\n"
    "v_mbcnt_hi_u32_b32 v24, -1, v24\n"
    "s_and_b32 s50, s93, 1\n"
    "s_lshr_b32 s51, s93, 1\n"
    "v_lshrrev_b32_e32 v25, 3, v24\n"
    "v_and_b32_e32 v26, 6, v25\n"
    "v_and_b32_e32 v27, 7, v24\n"
    "v_xor_b32_e32 v26, v26, v27\n"
    "v_lshlrev_b32_e32 v26, 4, v26\n"
    "v_and_b32_e32 v27, 4, v25\n"
    "v_add_u32_e32 v27, v27, v25\n"
    "v_lshl_add_u32 v251, v27, 9, v26\n"
    "v_add_u32_e32 v252, 0x1c00, v251\n"
    "s_mov_b32 s22, 0x20000\n"
    "s_mov_b32 s23, 0x20000\n"
    "s_mov_b32 s26, 0x20000\n"
    "s_mov_b32 s27, 0x20000\n"
    "s_lshr_b32 s36, s92, 7\n"
    "s_mul_i32 s36, s36, 3\n"
    "s_add_u32 s36, s36, s92\n"
    "s_and_b32 s36, s36, 127\n"
    "s_lshl_b32 s45, s93, 7\n"
    "s_lshl_b32 s46, s93, 12\n"
    "s_and_b32 s47, s36, 7\n"
    "s_lshl_b32 s47, s47, 14\n"
    "s_mov_b32 m0, s46\n"
    "s_add_u32 s47, s47, s45\n"
    "s_and_b32 s34, s92, 7\n"
    "s_lshl_b32 s34, s34, 5\n"
    "s_lshr_b32 s32, s92, 3\n"
    "s_add_u32 s34, s34, s32\n"
    "s_lshl_b32 s32, s34, 6\n"
    "s_or_b32 s32, s32, 63\n"
    "s_and_b32 s32, s32, 255\n"
    "s_cmp_ge_u32 s34, 252\n"
    "s_cselect_b32 s34, s32, s34\n"
    "s_and_b32 s32, s34, 3\n"
    "s_lshr_b32 s33, s34, 2\n"
    "s_or_b32 s38, s33, 16\n"
    "s_cmp_eq_u32 s38, 62\n"
    "s_cselect_b32 s38, 16, 0\n"
    "s_xor_b32 s33, s33, s38\n"
    "s_lshl2_add_u32 s34, s33, s32\n"
    "s_mul_i32 s38, s33, 81920\n"
    "s_lshl_b32 s35, s32, 13\n"
    "s_and_b32 s55, s35, 0x2000\n"
    "s_add_u32 s35, s35, s38\n"
    "s_add_u32 s55, s55, s38\n"
    "s_mov_b32 s39, 6101760\n"
    "s_waitcnt lgkmcnt(0)\n"
    "s_mov_b64 s[20:21], s[4:5]\n"
    "s_mov_b64 s[24:25], s[6:7]\n"
    "buffer_load_dwordx4 v251, s[20:23], s47 offen offset:0 lds\n"
    "s_add_u32 s48, s47, 1024\n"
    "buffer_load_dwordx4 v252, s[20:23], s47 offen offset:3072 lds\n"
    "s_add_u32 s47, s47, 0x1800\n"
    "buffer_load_dwordx4 v251, s[20:23], s48 offen offset:1024 lds\n"
    "buffer_load_dwordx4 v251, s[20:23], s47 offen offset:2048 lds\n"
    "s_lshr_b32 s47, s36, 4\n"
    "s_lshl_b32 s47, s47, 14\n"
    "s_and_b32 s48, s36, 8\n"
    "s_lshl_b32 s48, s48, 8\n"
    "s_add_u32 s47, s47, s48\n"
    "s_add_u32 m0, s46, 0x4000\n"
    "s_add_u32 s47, s47, s45\n"
    "buffer_load_dwordx4 v251, s[24:27], s47 offen offset:0 lds\n"
    "buffer_load_dwordx4 v252, s[24:27], s47 offen offset:1024 lds\n"
    "s_add_u32 s47, s45, 120832\n"
    "buffer_load_dwordx4 v251, s[24:27], s47 offen offset:2048 lds\n"
    "s_add_u32 s47, s47, 1024\n"
    "buffer_load_dwordx4 v251, s[24:27], s47 offen offset:3072 lds\n"
    "s_mov_b64 s[6:7], s[14:15]\n"
    "s_mov_b64 s[86:87], s[10:11]\n"
    "s_mov_b32 s14, 5120000\n"
    "s_mov_b32 s15, 0x20000\n"
    "v_mov_b32_e32 v102, s39\n"
    "global_load_dword v102, v102, s[8:9]\n"
    "s_lshl_b32 s45, s93, 2\n"
    "s_add_u32 s54, s35, 0x8000\n"
    "s_add_u32 s55, s55, 0x10000\n"
    "v_lshlrev_b32_e32 v250, 4, v24\n"
    "v_lshrrev_b32_e32 v28, 4, v24\n"
    "v_add_u32_e32 v28, s45, v28\n"
    "v_and_b32_e32 v104, 0xf0, v250\n"
    "v_lshl_add_u32 v104, v28, 9, v104\n"
    "buffer_load_dwordx4 v[0:3], v104, s[12:15], s35 offen offset:0\n"
    "buffer_load_dwordx4 v[4:7], v104, s[12:15], s35 offen offset:256\n"
    "s_cmp_lt_u32 s32, 2\n"
    "s_cselect_b32 s60, s55, 2.0\n"
    "buffer_load_dwordx4 v[8:11], v104, s[12:15], s54 offen offset:0\n"
    "buffer_load_dwordx4 v[12:15], v104, s[12:15], s54 offen offset:256\n"
    "buffer_load_dwordx4 v[16:19], v104, s[12:15], s55 offen offset:0\n"
    "buffer_load_dwordx4 v[20:23], v104, s[12:15], s55 offen offset:256\n"
    "s_min_u32 s46, s34, 249\n"
    "s_mul_i32 s46, s46, 2560\n"
    "s_lshl_b32 s47, s50, 10\n"
    "s_add_u32 s47, s47, s46\n"
    "v_add_u32_e32 v158, s47, v250\n"
    "s_lshl_b32 s47, s50, 8\n"
    "s_add_u32 s47, s47, s46\n"
    "s_add_u32 s47, s47, 2048\n"
    "v_lshl_add_u32 v159, v24, 2, s47\n"
    "s_cmp_lg_u32 s51, 0\n"
    "s_cselect_b64 exec, -1, 1\n"
    "global_load_dwordx4 v[144:147], v158, s[86:87]\n"
    "global_load_dword v148, v159, s[86:87]\n"
    "global_load_dwordx4 v[152:155], v158, s[6:7]\n"
    "global_load_dword v156, v159, s[6:7]\n"
    "s_mov_b64 exec, -1\n"
    "s_mov_b32 s10, 69410816\n"
    "s_mov_b32 s11, 0x20000\n"
    "s_mov_b32 s49, 0\n"
    "v_and_b32_e32 v25, 7, v24\n"
    "v_lshlrev_b32_e32 v27, 7, v25\n"
    "v_bfe_u32 v26, v24, 3, 1\n"
    "v_lshl_add_u32 v27, v26, 10, v27\n"
    "v_and_b32_e32 v25, 6, v24\n"
    "v_lshrrev_b32_e32 v26, 4, v24\n"
    "v_xor_b32_e32 v25, v25, v26\n"
    "v_lshl_add_u32 v240, v25, 4, v27\n"
    "s_lshl_b32 s48, s50, 11\n"
    "v_xor_b32_e32 v241, 64, v240\n"
    "v_add_u32_e32 v72, s48, v240\n"
    "v_add_u32_e32 v73, s48, v241\n"
    "s_lshl_b32 s47, s93, 10\n"
    "v_add_u32_e32 v108, s47, v250\n"
    "v_lshrrev_b32_e32 v109, 7, v108\n"
    "v_mul_u32_u24_e32 v109, 0x8200, v109\n"
    "v_and_b32_e32 v32, 0x70, v250\n"
    "v_or_b32_e32 v109, v109, v32\n"
    "v_add_u32_e32 v108, 81920, v108\n"
    "v_mov_b64_e32 v[32:33], -1\n"
    "v_mov_b64_e32 v[34:35], -1\n"
    "ds_write_b128 v108, v[32:35] offset:0\n"
    "ds_write_b128 v108, v[32:35] offset:16\n"
    "v_and_b32_e32 v29, 6, v28\n"
    "v_xor_b32_e32 v29, v29, v24\n"
    "v_and_b32_e32 v29, 7, v29\n"
    "v_lshlrev_b32_e32 v105, 7, v28\n"
    "v_lshl_add_u32 v105, v29, 4, v105\n"
    "v_bfe_u32 v29, v24, 3, 1\n"
    "v_subrev_u32_e32 v29, s32, v29\n"
    "v_and_b32_e32 v29, 3, v29\n"
    "v_lshl_add_u32 v105, v29, 11, v105\n"
    "v_add_u32_e32 v105, 32768, v105\n"
    "v_xor_b32_e32 v106, 0x1000, v105\n"
    "v_and_b32_e32 v29, 15, v24\n"
    "v_cmp_eq_u32_e32 vcc, 15, v29\n"
    "v_mov_b32_e32 v107, 0xffff\n"
    "s_mov_b32 s58, 0x3f803f80\n"
    "s_mov_b32 s57, 0x43c00000\n"
    "v_cndmask_b32_e32 v107, -1, v107, vcc\n"
    "s_mov_b32 s56, 0xc3c00000\n"
    "s_mov_b32 s62, 0xc000c000\n"
    "s_mov_b32 s63, 0xc000c000\n"
    "s_lshl_b32 s47, s93, 10\n"
    "s_add_u32 s47, s47, 6080512\n"
    "v_add_u32_e32 v100, s47, v250\n"
    "v_and_b32_e32 v29, 15, v24\n"
    "s_cmp_ge_u32 s36, 112\n"
    "s_cselect_b32 s47, 8, 16\n"
    "v_cmp_le_u32_e32 vcc, s47, v29\n"
    "v_mov_b32_e32 v64, 0xbd80bd80\n"
    "v_mov_b32_e32 v65, 0xbdabbdab\n"
    "v_mov_b32_e32 v66, 0x392b392b\n"
    "v_cndmask_b32_e32 v64, v64, v65, vcc\n"
    "v_cndmask_b32_e32 v66, 0, v66, vcc\n"
    "s_cselect_b32 s47, 56, 64\n"
    "v_cmp_le_u32_e32 vcc, s47, v24\n"
    "v_mov_b32_e32 v65, 0x3e80bdab\n"
    "v_mov_b32_e32 v67, 0x392b\n"
    "v_cndmask_b32_e32 v65, v64, v65, vcc\n"
    "v_cndmask_b32_e32 v67, v66, v67, vcc\n"
    "s_cmp_ge_u32 s36, 120\n"
    "s_cselect_b32 s47, 15, 16\n"
    "v_cmp_ne_u32_e32 vcc, s47, v29\n"
    "v_mov_b32_e32 v68, 0x3db8aa3b\n"
    "s_lshr_b32 s47, s36, 5\n"
    "v_cndmask_b32_e32 v68, 0, v68, vcc\n"
    "s_waitcnt vmcnt(11)\n"
    "s_waitcnt lgkmcnt(0)\n"
    "s_barrier\n"
    "s_cmp_lg_u32 s51, 0\n"
    "s_cbranch_scc1 .Lat_noj2\n"
    "ds_read_b128 v[112:115], v72 offset:0\n"
    "ds_read_b128 v[208:211], v240 offset:18432\n"
    "ds_read_b128 v[176:179], v240 offset:16384\n"
    "ds_read_b128 v[116:119], v73 offset:0\n"
    "ds_read_b128 v[212:215], v241 offset:18432\n"
    "ds_read_b128 v[180:183], v241 offset:16384\n"
    "ds_read_b128 v[120:123], v72 offset:4096\n"
    "ds_read_b128 v[216:219], v240 offset:22528\n"
    "ds_read_b128 v[184:187], v240 offset:20480\n"
    "ds_read_b128 v[124:127], v73 offset:4096\n"
    "ds_read_b128 v[220:223], v241 offset:22528\n"
    "ds_read_b128 v[188:191], v241 offset:20480\n"
    "ds_read_b128 v[128:131], v72 offset:8192\n"
    "ds_read_b128 v[224:227], v240 offset:26624\n"
    "ds_read_b128 v[192:195], v240 offset:24576\n"
    "s_waitcnt lgkmcnt(12)\n"
    "v_mfma_f32_16x16x32_bf16 v[56:59], v[208:211], v[112:115], 0\n"
    "ds_read_b128 v[132:135], v73 offset:8192\n"
    "ds_read_b128 v[228:231], v241 offset:26624\n"
    "v_mfma_f32_16x16x32_bf16 v[48:51], v[112:115], v[176:179], 0\n"
    "ds_read_b128 v[196:199], v241 offset:24576\n"
    "s_waitcnt lgkmcnt(12)\n"
    "v_mfma_f32_16x16x32_bf16 v[56:59], v[212:215], v[116:119], v[56:59]\n"
    "ds_read_b128 v[136:139], v72 offset:12288\n"
    "ds_read_b128 v[232:235], v240 offset:30720\n"
    "v_mfma_f32_16x16x32_bf16 v[48:51], v[116:119], v[180:183], v[48:51]\n"
    "ds_read_b128 v[200:203], v240 offset:28672\n"
    "s_waitcnt lgkmcnt(12)\n"
    "v_mfma_f32_16x16x32_bf16 v[56:59], v[216:219], v[120:123], v[56:59]\n"
    "ds_read_b128 v[140:143], v73 offset:12288\n"
    "ds_read_b128 v[236:239], v241 offset:30720\n"
    "v_mfma_f32_16x16x32_bf16 v[48:51], v[120:123], v[184:187], v[48:51]\n"
    "ds_read_b128 v[204:207], v241 offset:28672\n"
    "s_waitcnt lgkmcnt(12)\n"
    "v_mfma_f32_16x16x32_bf16 v[56:59], v[220:223], v[124:127], v[56:59]\n"
    "s_mul_i32 s47, s47, 33792\n"
    "s_and_b32 s48, s36, 7\n"
    "s_lshl_b32 s48, s48, 11\n"
    "v_mfma_f32_16x16x32_bf16 v[48:51], v[124:127], v[188:191], v[48:51]\n"
    "s_add_u32 s47, s47, s48\n"
    "s_and_b32 s48, s36, 8\n"
    "s_lshl_b32 s48, s48, 7\n"
    "s_waitcnt lgkmcnt(9)\n"
    "v_mfma_f32_16x16x32_bf16 v[56:59], v[224:227], v[128:131], v[56:59]\n"
    "s_add_u32 s47, s47, s48\n"
    "s_and_b32 s48, s36, 16\n"
    "s_lshl_b32 s48, s48, 10\n"
    "v_mfma_f32_16x16x32_bf16 v[48:51], v[128:131], v[192:195], v[48:51]\n"
    "s_add_u32 s47, s47, s48\n"
    "s_add_u32 s52, s47, 5242880\n"
    "s_lshl_b32 s48, s50, 9\n"
    "s_waitcnt lgkmcnt(6)\n"
    "v_mfma_f32_16x16x32_bf16 v[56:59], v[228:231], v[132:135], v[56:59]\n"
    "s_add_u32 s52, s52, s48\n"
    "v_and_b32_e32 v74, 0x1f0, v250\n"
    "v_lshrrev_b32_e32 v29, 5, v24\n"
    "v_mfma_f32_16x16x32_bf16 v[48:51], v[132:135], v[196:199], v[48:51]\n"
    "v_lshl_add_u32 v74, v29, 3, v74\n"
    "s_lshl1_add_u32 s53, s36, s50\n"
    "s_lshl4_add_u32 s53, s53, 6080512\n"
    "s_waitcnt lgkmcnt(3)\n"
    "v_mfma_f32_16x16x32_bf16 v[56:59], v[232:235], v[136:139], v[56:59]\n"
    "s_cmp_lg_u32 s51, 0\n"
    "v_mfma_f32_16x16x32_bf16 v[48:51], v[136:139], v[200:203], v[48:51]\n"
    "s_waitcnt lgkmcnt(0)\n"
    "v_mfma_f32_16x16x32_bf16 v[56:59], v[236:239], v[140:143], v[56:59]\n"
    "v_mfma_f32_16x16x32_bf16 v[48:51], v[140:143], v[204:207], v[48:51]\n"
    "s_nop 3\n"
    "v_cvt_pk_bf16_f32 v56, v56, v57\n"
    "v_cvt_pk_bf16_f32 v57, v58, v59\n"
    "s_nop 1\n"
    "v_mfma_f32_16x16x16_bf16 v[48:51], v[56:57], v[64:65], v[48:51]\n"
    "v_mfma_f32_16x16x16_bf16 v[48:51], v[56:57], v[66:67], v[48:51]\n"
    "s_nop 7\n"
    "v_mul_f32_e32 v48, v48, v68\n"
    "v_mul_f32_e32 v49, v49, v68\n"
    "v_mul_f32_e32 v50, v50, v68\n"
    "v_mul_f32_e32 v51, v51, v68\n"
    "v_cvt_pk_bf16_f32 v48, v48, v49\n"
    "v_cvt_pk_bf16_f32 v49, v50, v51\n"
    "buffer_store_dwordx2 v[48:49], v74, s[8:11], s52 offen offset:0 sc1\n"
    "s_waitcnt vmcnt(5)\n"
    "s_branch .Lat_ln\n"
    ".Lat_noj2:\n"
    "s_cmp_lg_u32 s93, 3\n"
    "s_cbranch_scc1 .Lat_wn10\n"
    "s_call_b64 s[2:3], .Lat_w0\n"
    "s_call_b64 s[2:3], .Lat_w2\n"
    "s_call_b64 s[2:3], .Lat_w4\n"
    ".Lat_wn10:\n"
    "s_cmp_lg_u32 s93, 2\n"
    "s_cbranch_scc1 .Lat_wn11\n"
    "s_call_b64 s[2:3], .Lat_w1\n"
    "s_call_b64 s[2:3], .Lat_w3\n"
    "s_call_b64 s[2:3], .Lat_w5\n"
    ".Lat_wn11:\n"
    "s_cmp_lg_u32 s51, 0\n"
    "s_waitcnt vmcnt(4)\n"
    ".Lat_ln:\n"
    "v_add_u32_e32 v102, 4, v102\n"
    "s_nop 1\n"
    "v_readfirstlane_b32 s39, v102\n"
    "v_dot2_f32_bf16 v24, v0, s58, 0\n"
    "v_dot2_f32_bf16 v32, v8, s58, 0\n"
    "v_dot2_f32_bf16 v40, v16, s58, 0\n"
    "v_dot2_f32_bf16 v25, v4, s58, 0\n"
    "v_dot2_f32_bf16 v33, v12, s58, 0\n"
    "v_dot2_f32_bf16 v41, v20, s58, 0\n"
    "v_dot2_f32_bf16 v26, v0, v0, 0\n"
    "v_dot2_f32_bf16 v34, v8, v8, 0\n"
    "v_dot2_f32_bf16 v42, v16, v16, 0\n"
    "v_dot2_f32_bf16 v27, v4, v4, 0\n"
    "v_dot2_f32_bf16 v35, v12, v12, 0\n"
    "v_dot2_f32_bf16 v43, v20, v20, 0\n"
    "v_dot2_f32_bf16 v24, v1, s58, v24\n"
    "v_dot2_f32_bf16 v32, v9, s58, v32\n"
    "v_dot2_f32_bf16 v40, v17, s58, v40\n"
    "v_dot2_f32_bf16 v25, v5, s58, v25\n"
    "v_dot2_f32_bf16 v33, v13, s58, v33\n"
    "v_dot2_f32_bf16 v41, v21, s58, v41\n"
    "v_dot2_f32_bf16 v26, v1, v1, v26\n"
    "v_dot2_f32_bf16 v34, v9, v9, v34\n"
    "v_dot2_f32_bf16 v42, v17, v17, v42\n"
    "v_dot2_f32_bf16 v27, v5, v5, v27\n"
    "v_dot2_f32_bf16 v35, v13, v13, v35\n"
    "v_dot2_f32_bf16 v43, v21, v21, v43\n"
    "v_dot2_f32_bf16 v24, v2, s58, v24\n"
    "v_dot2_f32_bf16 v32, v10, s58, v32\n"
    "v_dot2_f32_bf16 v40, v18, s58, v40\n"
    "v_dot2_f32_bf16 v25, v6, s58, v25\n"
    "v_dot2_f32_bf16 v33, v14, s58, v33\n"
    "v_dot2_f32_bf16 v41, v22, s58, v41\n"
    "v_dot2_f32_bf16 v26, v2, v2, v26\n"
    "v_dot2_f32_bf16 v34, v10, v10, v34\n"
    "v_dot2_f32_bf16 v42, v18, v18, v42\n"
    "v_dot2_f32_bf16 v27, v6, v6, v27\n"
    "v_dot2_f32_bf16 v35, v14, v14, v35\n"
    "v_dot2_f32_bf16 v43, v22, v22, v43\n"
    "v_dot2_f32_bf16 v24, v3, s58, v24\n"
    "v_dot2_f32_bf16 v32, v11, s58, v32\n"
    "v_dot2_f32_bf16 v40, v19, s58, v40\n"
    "v_dot2_f32_bf16 v25, v7, s58, v25\n"
    "v_dot2_f32_bf16 v33, v15, s58, v33\n"
    "v_dot2_f32_bf16 v41, v23, s58, v41\n"
    "v_dot2_f32_bf16 v26, v3, v3, v26\n"
    "v_dot2_f32_bf16 v34, v11, v11, v34\n"
    "v_dot2_f32_bf16 v42, v19, v19, v42\n"
    "v_dot2_f32_bf16 v27, v7, v7, v27\n"
    "v_dot2_f32_bf16 v35, v15, v15, v35\n"
    "v_dot2_f32_bf16 v43, v23, v23, v43\n"
    "v_and_b32_e32 v30, 0xffff0000, v7\n"
    "v_and_b32_e32 v38, 0xffff0000, v15\n"
    "v_and_b32_e32 v46, 0xffff0000, v23\n"
    "v_add_f32_e32 v24, v24, v25\n"
    "v_add_f32_e32 v32, v32, v33\n"
    "v_add_f32_e32 v40, v40, v41\n"
    "v_add_f32_e32 v26, v26, v27\n"
    "v_add_f32_e32 v34, v34, v35\n"
    "v_add_f32_e32 v42, v42, v43\n"
    "s_cbranch_scc1 .Lat_nojf\n"
    "s_waitcnt vmcnt(0)\n"
    "buffer_store_dword v102, off, s[8:11], s53 offset:0 sc1\n"
    ".Lat_nojf:\n"
    "v_add_f32_dpp v25, v25, v25 quad_perm:[1,0,3,2] row_mask:0xf bank_mask:0xf bound_ctrl:1\n"
    "v_lshlrev_b32_e32 v48, 16, v0\n"
    "v_add_f32_dpp v33, v33, v33 quad_perm:[1,0,3,2] row_mask:0xf bank_mask:0xf bound_ctrl:1\n"
    "v_and_b32_e32 v49, 0xffff0000, v0\n"
    "v_add_f32_dpp v41, v41, v41 quad_perm:[1,0,3,2] row_mask:0xf bank_mask:0xf bound_ctrl:1\n"
    "v_lshlrev_b32_e32 v50, 16, v1\n"
    "v_mov_b32_dpp v30, v30 quad_perm:[3,3,3,3] row_mask:0xf bank_mask:0xf bound_ctrl:1\n"
    "v_and_b32_e32 v51, 0xffff0000, v1\n"
    "v_mov_b32_dpp v38, v38 quad_perm:[3,3,3,3] row_mask:0xf bank_mask:0xf bound_ctrl:1\n"
    "v_lshlrev_b32_e32 v52, 16, v2\n"
    "v_mov_b32_dpp v46, v46 quad_perm:[3,3,3,3] row_mask:0xf bank_mask:0xf bound_ctrl:1\n"
    "v_and_b32_e32 v53, 0xffff0000, v2\n"
    "v_add_f32_dpp v24, v24, v24 quad_perm:[1,0,3,2] row_mask:0xf bank_mask:0xf bound_ctrl:1\n"
    "v_lshlrev_b32_e32 v54, 16, v3\n"
    "v_add_f32_dpp v32, v32, v32 quad_perm:[1,0,3,2] row_mask:0xf bank_mask:0xf bound_ctrl:1\n"
    "v_and_b32_e32 v55, 0xffff0000, v3\n"
    "v_add_f32_dpp v40, v40, v40 quad_perm:[1,0,3,2] row_mask:0xf bank_mask:0xf bound_ctrl:1\n"
    "v_lshlrev_b32_e32 v56, 16, v4\n"
    "v_add_f32_dpp v26, v26, v26 quad_perm:[1,0,3,2] row_mask:0xf bank_mask:0xf bound_ctrl:1\n"
    "v_and_b32_e32 v57, 0xffff0000, v4\n"
    "v_add_f32_dpp v34, v34, v34 quad_perm:[1,0,3,2] row_mask:0xf bank_mask:0xf bound_ctrl:1\n"
    "v_lshlrev_b32_e32 v58, 16, v5\n"
    "v_add_f32_dpp v42, v42, v42 quad_perm:[1,0,3,2] row_mask:0xf bank_mask:0xf bound_ctrl:1\n"
    "v_and_b32_e32 v59, 0xffff0000, v5\n"
    "v_add_f32_dpp v24, v24, v24 quad_perm:[2,3,0,1] row_mask:0xf bank_mask:0xf bound_ctrl:1\n"
    "v_lshlrev_b32_e32 v60, 16, v6\n"
    "v_add_f32_dpp v32, v32, v32 quad_perm:[2,3,0,1] row_mask:0xf bank_mask:0xf bound_ctrl:1\n"
    "v_and_b32_e32 v61, 0xffff0000, v6\n"
    "v_add_f32_dpp v40, v40, v40 quad_perm:[2,3,0,1] row_mask:0xf bank_mask:0xf bound_ctrl:1\n"
    "v_lshlrev_b32_e32 v62, 16, v7\n"
    "v_add_f32_dpp v26, v26, v26 quad_perm:[2,3,0,1] row_mask:0xf bank_mask:0xf bound_ctrl:1\n"
    "v_and_b32_e32 v63, 0xffff0000, v7\n"
    "v_add_f32_dpp v34, v34, v34 quad_perm:[2,3,0,1] row_mask:0xf bank_mask:0xf bound_ctrl:1\n"
    "v_lshlrev_b32_e32 v64, 16, v8\n"
    "v_add_f32_dpp v42, v42, v42 quad_perm:[2,3,0,1] row_mask:0xf bank_mask:0xf bound_ctrl:1\n"
    "v_and_b32_e32 v65, 0xffff0000, v8\n"
    "v_add_f32_dpp v24, v24, v24 row_half_mirror row_mask:0xf bank_mask:0xf bound_ctrl:1\n"
    "v_lshlrev_b32_e32 v66, 16, v9\n"
    "v_add_f32_dpp v32, v32, v32 row_half_mirror row_mask:0xf bank_mask:0xf bound_ctrl:1\n"
    "v_and_b32_e32 v67, 0xffff0000, v9\n"
    "v_add_f32_dpp v40, v40, v40 row_half_mirror row_mask:0xf bank_mask:0xf bound_ctrl:1\n"
    "v_lshlrev_b32_e32 v68, 16, v10\n"
    "v_add_f32_dpp v26, v26, v26 row_half_mirror row_mask:0xf bank_mask:0xf bound_ctrl:1\n"
    "v_and_b32_e32 v69, 0xffff0000, v10\n"
    "v_add_f32_dpp v34, v34, v34 row_half_mirror row_mask:0xf bank_mask:0xf bound_ctrl:1\n"
    "v_lshlrev_b32_e32 v70, 16, v11\n"
    "v_add_f32_dpp v42, v42, v42 row_half_mirror row_mask:0xf bank_mask:0xf bound_ctrl:1\n"
    "v_and_b32_e32 v71, 0xffff0000, v11\n"
    "v_add_f32_dpp v24, v24, v24 row_mirror row_mask:0xf bank_mask:0xf bound_ctrl:1\n"
    "v_lshlrev_b32_e32 v72, 16, v12\n"
    "v_add_f32_dpp v32, v32, v32 row_mirror row_mask:0xf bank_mask:0xf bound_ctrl:1\n"
    "v_and_b32_e32 v73, 0xffff0000, v12\n"
    "v_add_f32_dpp v40, v40, v40 row_mirror row_mask:0xf bank_mask:0xf bound_ctrl:1\n"
    "v_lshlrev_b32_e32 v74, 16, v13\n"
    "v_add_f32_dpp v26, v26, v26 row_mirror row_mask:0xf bank_mask:0xf bound_ctrl:1\n"
    "v_and_b32_e32 v75, 0xffff0000, v13\n"
    "v_add_f32_dpp v34, v34, v34 row_mirror row_mask:0xf bank_mask:0xf bound_ctrl:1\n"
    "v_lshlrev_b32_e32 v76, 16, v14\n"
    "v_add_f32_dpp v42, v42, v42 row_mirror row_mask:0xf bank_mask:0xf bound_ctrl:1\n"
    "v_and_b32_e32 v77, 0xffff0000, v14\n"
    "v_mul_f32_e32 v24, 0x3b800000, v24\n"
    "v_lshlrev_b32_e32 v78, 16, v15\n"
    "v_mul_f32_e32 v32, 0x3b800000, v32\n"
    "v_and_b32_e32 v79, 0xffff0000, v15\n"
    "v_mul_f32_e32 v40, 0x3b800000, v40\n"
    "v_lshlrev_b32_e32 v80, 16, v16\n"
    "v_mul_f32_e32 v25, 0.25, v25\n"
    "v_and_b32_e32 v81, 0xffff0000, v16\n"
    "v_mul_f32_e32 v33, 0.25, v33\n"
    "v_lshlrev_b32_e32 v82, 16, v17\n"
    "v_mul_f32_e32 v41, 0.25, v41\n"
    "v_and_b32_e32 v83, 0xffff0000, v17\n"
    "v_mul_f32_e64 v27, v24, -v24\n"
    "v_lshlrev_b32_e32 v84, 16, v18\n"
    "v_mul_f32_e64 v35, v32, -v32\n"
    "v_and_b32_e32 v85, 0xffff0000, v18\n"
    "v_mul_f32_e64 v43, v40, -v40\n"
    "v_lshlrev_b32_e32 v86, 16, v19\n"
    "v_sub_f32_e32 v25, v25, v30\n"
    "v_and_b32_e32 v87, 0xffff0000, v19\n"
    "v_sub_f32_e32 v33, v33, v38\n"
    "v_lshlrev_b32_e32 v88, 16, v20\n"
    "v_sub_f32_e32 v41, v41, v46\n"
    "v_and_b32_e32 v89, 0xffff0000, v20\n"
    "v_fmac_f32_e32 v27, 0x3b800000, v26\n"
    "v_lshlrev_b32_e32 v90, 16, v21\n"
    "v_fmac_f32_e32 v35, 0x3b800000, v34\n"
    "v_and_b32_e32 v91, 0xffff0000, v21\n"
    "v_fmac_f32_e32 v43, 0x3b800000, v42\n"
    "v_lshlrev_b32_e32 v92, 16, v22\n"
    "v_mul_f32_e32 v25, 0x3eaaaaab, v25\n"
    "v_and_b32_e32 v93, 0xffff0000, v22\n"
    "v_mul_f32_e32 v33, 0x3eaaaaab, v33\n"
    "v_lshlrev_b32_e32 v94, 16, v23\n"
    "v_mul_f32_e32 v41, 0x3eaaaaab, v41\n"
    "v_and_b32_e32 v95, 0xffff0000, v23\n"
    "v_max_f32_e32 v27, 0, v27\n"
    "v_max_f32_e32 v35, 0, v35\n"
    "v_max_f32_e32 v43, 0, v43\n"
    "s_cbranch_scc0 .Lat_bin\n"
    "s_movk_i32 s45, -2560\n"
    "s_mov_b32 s48, 81920\n"
    "v_mov_b32_e32 v172, 1\n"
    "s_waitcnt vmcnt(0)\n"
    "v_mul_u32_u24_e32 v160, 0x199a, v144\n"
    "v_mul_u32_u24_e32 v161, 0x199a, v145\n"
    "v_mul_u32_u24_e32 v162, 0x199a, v146\n"
    "v_mul_u32_u24_e32 v163, 0x199a, v147\n"
    "v_mul_u32_u24_e32 v164, 0x199a, v148\n"
    "v_bfe_u32 v160, v160, 24, 2\n"
    "v_bfe_u32 v161, v161, 24, 2\n"
    "v_bfe_u32 v162, v162, 24, 2\n"
    "v_bfe_u32 v163, v163, 24, 2\n"
    "v_bfe_u32 v164, v164, 24, 2\n"
    "v_lshl_add_u32 v166, v160, 2, s48\n"
    "v_lshl_add_u32 v167, v161, 2, s48\n"
    "v_lshl_add_u32 v168, v162, 2, s48\n"
    "v_lshl_add_u32 v169, v163, 2, s48\n"
    "v_lshl_add_u32 v170, v164, 2, s48\n"
    "ds_add_rtn_u32 v166, v166, v172 offset:4096\n"
    "ds_add_rtn_u32 v167, v167, v172 offset:4096\n"
    "ds_add_rtn_u32 v168, v168, v172 offset:4096\n"
    "ds_add_rtn_u32 v169, v169, v172 offset:4096\n"
    "ds_add_rtn_u32 v170, v170, v172 offset:4096\n"
    "v_mad_i32_i24 v144, v160, s45, v144\n"
    "v_mad_i32_i24 v145, v161, s45, v145\n"
    "v_mad_i32_i24 v146, v162, s45, v146\n"
    "v_mad_i32_i24 v147, v163, s45, v147\n"
    "v_mad_i32_i24 v148, v164, s45, v148\n"
    "v_lshl_or_b32 v144, v144, 18, v152\n"
    "v_lshl_or_b32 v145, v145, 18, v153\n"
    "v_lshl_or_b32 v146, v146, 18, v154\n"
    "v_lshl_or_b32 v147, v147, 18, v155\n"
    "v_lshl_or_b32 v148, v148, 18, v156\n"
    "v_lshl_add_u32 v160, v160, 10, s48\n"
    "v_lshl_add_u32 v161, v161, 10, s48\n"
    "v_lshl_add_u32 v162, v162, 10, s48\n"
    "v_lshl_add_u32 v163, v163, 10, s48\n"
    "v_lshl_add_u32 v164, v164, 10, s48\n"
    ".Lat_bin:\n"
    "v_cndmask_b32_e64 v25, v24, v25, s[62:63]\n"
    "v_cndmask_b32_e64 v33, v32, v33, s[62:63]\n"
    "v_cndmask_b32_e64 v41, v40, v41, s[62:63]\n"
    "v_add_f32_e32 v27, 0x3727c5ac, v27\n"
    "v_add_f32_e32 v35, 0x3727c5ac, v35\n"
    "v_add_f32_e32 v43, 0x3727c5ac, v43\n"
    "v_rsq_f32_e32 v28, v27\n"
    "v_rsq_f32_e32 v36, v35\n"
    "v_rsq_f32_e32 v44, v43\n"
    "v_fma_f32 v29, -v24, v28, s57\n"
    "v_fma_f32 v37, -v32, v36, s57\n"
    "v_fma_f32 v45, -v40, v44, s57\n"
    "v_fma_f32 v31, -v25, v28, s57\n"
    "v_fma_f32 v39, -v33, v36, s57\n"
    "v_fma_f32 v47, -v41, v44, s57\n"
    "v_sub_f32_e32 v24, v29, v31\n"
    "v_sub_f32_e32 v32, v37, v39\n"
    "v_sub_f32_e32 v40, v45, v47\n"
    "v_pk_fma_f32 v[48:49], v[48:49], v[28:29], v[28:29] op_sel:[0,0,1] op_sel_hi:[1,0,1]\n"
    "v_pk_fma_f32 v[50:51], v[50:51], v[28:29], v[28:29] op_sel:[0,0,1] op_sel_hi:[1,0,1]\n"
    "v_pk_fma_f32 v[52:53], v[52:53], v[28:29], v[28:29] op_sel:[0,0,1] op_sel_hi:[1,0,1]\n"
    "v_pk_fma_f32 v[54:55], v[54:55], v[28:29], v[28:29] op_sel:[0,0,1] op_sel_hi:[1,0,1]\n"
    "v_pk_fma_f32 v[56:57], v[56:57], v[28:29], v[30:31] op_sel:[0,0,1] op_sel_hi:[1,0,1]\n"
    "v_pk_fma_f32 v[58:59], v[58:59], v[28:29], v[30:31] op_sel:[0,0,1] op_sel_hi:[1,0,1]\n"
    "v_pk_fma_f32 v[60:61], v[60:61], v[28:29], v[30:31] op_sel:[0,0,1] op_sel_hi:[1,0,1]\n"
    "v_pk_fma_f32 v[62:63], v[62:63], v[28:29], v[30:31] op_sel:[0,0,1] op_sel_hi:[1,0,1]\n"
    "v_pk_add_f32 v[48:49], v[48:49], s[56:57] op_sel_hi:[1,0]\n"
    "v_pk_add_f32 v[50:51], v[50:51], s[56:57] op_sel_hi:[1,0]\n"
    "v_pk_add_f32 v[52:53], v[52:53], s[56:57] op_sel_hi:[1,0]\n"
    "v_pk_add_f32 v[54:55], v[54:55], s[56:57] op_sel_hi:[1,0]\n"
    "v_pk_add_f32 v[56:57], v[56:57], s[56:57] op_sel_hi:[1,0]\n"
    "v_pk_add_f32 v[58:59], v[58:59], s[56:57] op_sel_hi:[1,0]\n"
    "v_pk_add_f32 v[60:61], v[60:61], s[56:57] op_sel_hi:[1,0]\n"
    "v_pk_add_f32 v[62:63], v[62:63], s[56:57] op_sel_hi:[1,0]\n"
    "v_cvt_pk_bf16_f32 v0, v48, v49\n"
    "v_cvt_pk_bf16_f32 v1, v50, v51\n"
    "v_cvt_pk_bf16_f32 v2, v52, v53\n"
    "v_cvt_pk_bf16_f32 v3, v54, v55\n"
    "v_cvt_pk_bf16_f32 v4, v56, v57\n"
    "v_cvt_pk_bf16_f32 v5, v58, v59\n"
    "v_cvt_pk_bf16_f32 v6, v60, v61\n"
    "v_cvt_pk_bf16_f32 v7, v62, v63\n"
    "buffer_store_dwordx4 v[0:3], v104, s[8:11], s35 offen offset:0 sc1\n"
    "v_bfi_b32 v7, v107, v7, 1.0\n"
    "ds_write_b128 v105, v[0:3] offset:0\n"
    "buffer_store_dwordx4 v[4:7], v104, s[8:11], s35 offen offset:256 sc1\n"
    "v_pk_fma_f32 v[64:65], v[64:65], v[36:37], v[36:37] op_sel:[0,0,1] op_sel_hi:[1,0,1]\n"
    "v_pk_fma_f32 v[66:67], v[66:67], v[36:37], v[36:37] op_sel:[0,0,1] op_sel_hi:[1,0,1]\n"
    "v_pk_fma_f32 v[68:69], v[68:69], v[36:37], v[36:37] op_sel:[0,0,1] op_sel_hi:[1,0,1]\n"
    "v_pk_fma_f32 v[70:71], v[70:71], v[36:37], v[36:37] op_sel:[0,0,1] op_sel_hi:[1,0,1]\n"
    "v_pk_fma_f32 v[72:73], v[72:73], v[36:37], v[38:39] op_sel:[0,0,1] op_sel_hi:[1,0,1]\n"
    "v_pk_fma_f32 v[74:75], v[74:75], v[36:37], v[38:39] op_sel:[0,0,1] op_sel_hi:[1,0,1]\n"
    "v_pk_fma_f32 v[76:77], v[76:77], v[36:37], v[38:39] op_sel:[0,0,1] op_sel_hi:[1,0,1]\n"
    "v_pk_fma_f32 v[78:79], v[78:79], v[36:37], v[38:39] op_sel:[0,0,1] op_sel_hi:[1,0,1]\n"
    "v_pk_add_f32 v[64:65], v[64:65], s[56:57] op_sel_hi:[1,0]\n"
    "v_pk_add_f32 v[66:67], v[66:67], s[56:57] op_sel_hi:[1,0]\n"
    "v_pk_add_f32 v[68:69], v[68:69], s[56:57] op_sel_hi:[1,0]\n"
    "v_pk_add_f32 v[70:71], v[70:71], s[56:57] op_sel_hi:[1,0]\n"
    "v_pk_add_f32 v[72:73], v[72:73], s[56:57] op_sel_hi:[1,0]\n"
    "v_pk_add_f32 v[74:75], v[74:75], s[56:57] op_sel_hi:[1,0]\n"
    "v_pk_add_f32 v[76:77], v[76:77], s[56:57] op_sel_hi:[1,0]\n"
    "v_pk_add_f32 v[78:79], v[78:79], s[56:57] op_sel_hi:[1,0]\n"
    "v_cvt_pk_bf16_f32 v8, v64, v65\n"
    "v_cvt_pk_bf16_f32 v9, v66, v67\n"
    "v_cvt_pk_bf16_f32 v10, v68, v69\n"
    "v_cvt_pk_bf16_f32 v11, v70, v71\n"
    "v_cvt_pk_bf16_f32 v12, v72, v73\n"
    "v_cvt_pk_bf16_f32 v13, v74, v75\n"
    "v_cvt_pk_bf16_f32 v14, v76, v77\n"
    "v_cvt_pk_bf16_f32 v15, v78, v79\n"
    "buffer_store_dwordx4 v[8:11], v104, s[8:11], s54 offen offset:0 sc1\n"
    "v_bfi_b32 v15, v107, v15, 1.0\n"
    "ds_write_b128 v105, v[8:11] offset:8192\n"
    "buffer_store_dwordx4 v[12:15], v104, s[8:11], s54 offen offset:256 sc1\n"
    "v_pk_fma_f32 v[80:81], v[80:81], v[44:45], v[44:45] op_sel:[0,0,1] op_sel_hi:[1,0,1]\n"
    "v_pk_fma_f32 v[82:83], v[82:83], v[44:45], v[44:45] op_sel:[0,0,1] op_sel_hi:[1,0,1]\n"
    "v_pk_fma_f32 v[84:85], v[84:85], v[44:45], v[44:45] op_sel:[0,0,1] op_sel_hi:[1,0,1]\n"
    "v_pk_fma_f32 v[86:87], v[86:87], v[44:45], v[44:45] op_sel:[0,0,1] op_sel_hi:[1,0,1]\n"
    "v_pk_fma_f32 v[88:89], v[88:89], v[44:45], v[46:47] op_sel:[0,0,1] op_sel_hi:[1,0,1]\n"
    "v_pk_fma_f32 v[90:91], v[90:91], v[44:45], v[46:47] op_sel:[0,0,1] op_sel_hi:[1,0,1]\n"
    "v_pk_fma_f32 v[92:93], v[92:93], v[44:45], v[46:47] op_sel:[0,0,1] op_sel_hi:[1,0,1]\n"
    "v_pk_fma_f32 v[94:95], v[94:95], v[44:45], v[46:47] op_sel:[0,0,1] op_sel_hi:[1,0,1]\n"
    "v_pk_add_f32 v[80:81], v[80:81], s[56:57] op_sel_hi:[1,0]\n"
    "v_pk_add_f32 v[82:83], v[82:83], s[56:57] op_sel_hi:[1,0]\n"
    "v_pk_add_f32 v[84:85], v[84:85], s[56:57] op_sel_hi:[1,0]\n"
    "v_pk_add_f32 v[86:87], v[86:87], s[56:57] op_sel_hi:[1,0]\n"
    "v_pk_add_f32 v[88:89], v[88:89], s[56:57] op_sel_hi:[1,0]\n"
    "v_pk_add_f32 v[90:91], v[90:91], s[56:57] op_sel_hi:[1,0]\n"
    "v_pk_add_f32 v[92:93], v[92:93], s[56:57] op_sel_hi:[1,0]\n"
    "v_pk_add_f32 v[94:95], v[94:95], s[56:57] op_sel_hi:[1,0]\n"
    "v_cvt_pk_bf16_f32 v16, v80, v81\n"
    "v_cvt_pk_bf16_f32 v17, v82, v83\n"
    "v_cvt_pk_bf16_f32 v18, v84, v85\n"
    "v_cvt_pk_bf16_f32 v19, v86, v87\n"
    "v_cvt_pk_bf16_f32 v20, v88, v89\n"
    "v_cvt_pk_bf16_f32 v21, v90, v91\n"
    "v_cvt_pk_bf16_f32 v22, v92, v93\n"
    "v_cvt_pk_bf16_f32 v23, v94, v95\n"
    "buffer_store_dwordx4 v[16:19], v104, s[8:11], s60 offen offset:0 sc1\n"
    "v_bfi_b32 v23, v107, v23, 1.0\n"
    "ds_write_b128 v105, v[16:19] offset:16384\n"
    "buffer_store_dwordx4 v[20:23], v104, s[8:11], s60 offen offset:256 sc1\n"
    "v_pk_add_f32 v[56:57], v[56:57], v[24:25] op_sel_hi:[1,0]\n"
    "v_pk_add_f32 v[58:59], v[58:59], v[24:25] op_sel_hi:[1,0]\n"
    "v_pk_add_f32 v[60:61], v[60:61], v[24:25] op_sel_hi:[1,0]\n"
    "v_pk_add_f32 v[62:63], v[62:63], v[24:25] op_sel_hi:[1,0]\n"
    "v_cvt_pk_bf16_f32 v56, v56, v57\n"
    "v_cvt_pk_bf16_f32 v57, v58, v59\n"
    "v_cvt_pk_bf16_f32 v58, v60, v61\n"
    "v_cvt_pk_bf16_f32 v59, v62, v63\n"
    "ds_write_b128 v106, v[56:59] offset:0\n"
    "v_pk_add_f32 v[72:73], v[72:73], v[32:33] op_sel_hi:[1,0]\n"
    "v_pk_add_f32 v[74:75], v[74:75], v[32:33] op_sel_hi:[1,0]\n"
    "v_pk_add_f32 v[76:77], v[76:77], v[32:33] op_sel_hi:[1,0]\n"
    "v_pk_add_f32 v[78:79], v[78:79], v[32:33] op_sel_hi:[1,0]\n"
    "v_cvt_pk_bf16_f32 v72, v72, v73\n"
    "v_cvt_pk_bf16_f32 v73, v74, v75\n"
    "v_cvt_pk_bf16_f32 v74, v76, v77\n"
    "v_cvt_pk_bf16_f32 v75, v78, v79\n"
    "ds_write_b128 v106, v[72:75] offset:8192\n"
    ".long 0xbedf00ff\n"
    ".Lat_w0:\n"
    "s_setpc_b64 s[2:3]\n"
    "v_pk_add_f32 v[88:89], v[88:89], v[40:41] op_sel_hi:[1,0]\n"
    "v_pk_add_f32 v[90:91], v[90:91], v[40:41] op_sel_hi:[1,0]\n"
    "v_pk_add_f32 v[92:93], v[92:93], v[40:41] op_sel_hi:[1,0]\n"
    "v_pk_add_f32 v[94:95], v[94:95], v[40:41] op_sel_hi:[1,0]\n"
    "v_cvt_pk_bf16_f32 v88, v88, v89\n"
    "v_cvt_pk_bf16_f32 v89, v90, v91\n"
    "v_cvt_pk_bf16_f32 v90, v92, v93\n"
    "v_cvt_pk_bf16_f32 v91, v94, v95\n"
    "ds_write_b128 v106, v[88:91] offset:16384\n"
    "s_cbranch_scc0 .Lat_bin2\n"
    "s_waitcnt lgkmcnt(0)\n"
    "v_min_i32_e32 v166, 254, v166\n"
    "v_min_i32_e32 v167, 254, v167\n"
    "v_min_i32_e32 v168, 254, v168\n"
    "v_min_i32_e32 v169, 254, v169\n"
    "v_min_i32_e32 v170, 254, v170\n"
    "v_lshl_add_u32 v160, v166, 2, v160\n"
    "v_lshl_add_u32 v161, v167, 2, v161\n"
    "v_lshl_add_u32 v162, v168, 2, v162\n"
    "v_lshl_add_u32 v163, v169, 2, v163\n"
    "v_lshl_add_u32 v164, v170, 2, v164\n"
    "ds_write_b32 v160, v144 offset:4\n"
    "ds_write_b32 v161, v145 offset:4\n"
    "ds_write_b32 v162, v146 offset:4\n"
    "ds_write_b32 v163, v147 offset:4\n"
    "ds_write_b32 v164, v148 offset:4\n"
    ".Lat_bin2:\n"
    "s_waitcnt lgkmcnt(0)\n"
    "s_barrier\n"
    "ds_read_b128 a[0:3], v240 offset:32768\n"
    "ds_read_b128 a[32:35], v240 offset:40960\n"
    "ds_read_b128 a[64:67], v240 offset:49152\n"
    "ds_read_b128 a[4:7], v241 offset:32768\n"
    "ds_read_b128 a[36:39], v241 offset:40960\n"
    "ds_read_b128 a[68:71], v241 offset:49152\n"
    "ds_read_b128 a[8:11], v240 offset:34816\n"
    "ds_read_b128 a[40:43], v240 offset:43008\n"
    "ds_read_b128 a[72:75], v240 offset:51200\n"
    "ds_read_b128 a[12:15], v241 offset:34816\n"
    "ds_read_b128 a[44:47], v241 offset:43008\n"
    "ds_read_b128 a[76:79], v241 offset:51200\n"
    "buffer_load_dword v101, v100, s[8:11], 0 offen offset:0 sc1\n"
    "ds_read_b128 a[16:19], v240 offset:36864\n"
    "ds_read_b128 a[48:51], v240 offset:45056\n"
    "ds_read_b128 a[80:83], v240 offset:53248\n"
    "ds_read_b128 a[20:23], v241 offset:36864\n"
    "ds_read_b128 a[52:55], v241 offset:45056\n"
    "ds_read_b128 a[84:87], v241 offset:53248\n"
    "ds_read_b128 a[24:27], v240 offset:38912\n"
    "ds_read_b128 a[56:59], v240 offset:47104\n"
    "ds_read_b128 a[88:91], v240 offset:55296\n"
    "ds_read_b128 a[28:31], v241 offset:38912\n"
    "ds_read_b128 a[60:63], v241 offset:47104\n"
    "ds_read_b128 a[92:95], v241 offset:55296\n"
    "ds_read_b128 v[40:43], v108 offset:0\n"
    "s_mul_i32 s52, s93, 33792\n"
    "s_add_u32 s52, s52, 5242880\n"
    "s_lshl_b32 s61, s32, 12\n"
    "s_lshl4_add_u32 s46, s34, 6076416\n"
    "s_cmp_eq_u32 s93, 0\n"
    "s_cselect_b32 s46, s46, 2.0\n"
    "s_lshl_b32 s48, s34, 7\n"
    "s_add_u32 s48, s48, 68206592\n"
    "s_lshl_b32 s47, s93, 12\n"
    "s_waitcnt vmcnt(1)\n"
    "s_waitcnt lgkmcnt(0)\n"
    "s_barrier\n"
    "buffer_store_dword v102, off, s[8:11], s46 offset:0 sc1\n"
    "buffer_store_dwordx4 v[40:43], v109, s[8:11], s48 offen offset:0 sc1\n"
    "s_lshl4_add_u32 s46, s34, s47\n"
    "s_add_u32 s46, s46, 6084608\n"
    "s_waitcnt vmcnt(2)\n"
    "v_cmp_ne_u32_e32 vcc, s39, v101\n"
    "s_nop 4\n"
    "s_cbranch_vccz .Lat_apoll_ret\n"
    ".Lat_apoll:\n"
    "s_mov_b32 s53, 0x400000\n"
    ".Lat_apoll_l:\n"
    "s_sleep 1\n"
    "buffer_load_dword v101, v100, s[8:11], 0 offen offset:0 sc1\n"
    "s_waitcnt vmcnt(0)\n"
    "v_cmp_ne_u32_e32 vcc, s39, v101\n"
    "s_sub_u32 s53, s53, 1\n"
    "s_cbranch_scc1 .Lat_apoll_ret\n"
    "s_nop 2\n"
    "s_cbranch_vccnz .Lat_apoll_l\n"
    ".Lat_apoll_ret:\n"
    "s_add_u32 s47, s52, s61\n"
    "buffer_load_dwordx4 v[112:115], v250, s[8:11], s47 offen offset:0 sc1\n"
    "buffer_load_dwordx4 v[116:119], v250, s[8:11], s47 offen offset:1024 sc1\n"
    "buffer_load_dwordx4 v[128:131], v250, s[8:11], s47 offen offset:2048 sc1\n"
    "s_add_u32 s61, s61, 0x1000\n"
    "buffer_load_dwordx4 v[132:135], v250, s[8:11], s47 offen offset:3072 sc1\n"
    "s_bitset0_b32 s61, 14\n"
    "s_add_u32 s47, s52, s61\n"
    "buffer_load_dwordx4 v[144:147], v250, s[8:11], s47 offen offset:0 sc1\n"
    "buffer_load_dwordx4 v[148:151], v250, s[8:11], s47 offen offset:1024 sc1\n"
    "buffer_load_dwordx4 v[160:163], v250, s[8:11], s47 offen offset:2048 sc1\n"
    "s_add_u32 s61, s61, 0x1000\n"
    "buffer_load_dwordx4 v[164:167], v250, s[8:11], s47 offen offset:3072 sc1\n"
    "s_bitset0_b32 s61, 14\n"
    "s_add_u32 s47, s52, s61\n"
    "buffer_load_dwordx4 v[176:179], v250, s[8:11], s47 offen offset:0 sc1\n"
    "buffer_load_dwordx4 v[180:183], v250, s[8:11], s47 offen offset:1024 sc1\n"
    "buffer_load_dwordx4 v[192:195], v250, s[8:11], s47 offen offset:2048 sc1\n"
    "s_add_u32 s61, s61, 0x1000\n"
    "v_lshrrev_b32_e32 v8, 4, v250\n"
    "v_and_b32_e32 v10, 0x70, v251\n"
    "v_bfe_u32 v9, v8, 3, 1\n"
    "v_lshl_add_u32 v248, v9, 9, v10\n"
    "v_lshrrev_b32_e32 v9, 4, v8\n"
    "v_lshl_add_u32 v248, v9, 12, v248\n"
    "s_mul_i32 s43, s50, 3\n"
    "s_xor_b32 s43, s43, s51\n"
    "s_lshl_b32 s43, s43, 15\n"
    "s_add_u32 s43, s43, 32768\n"
    "s_mul_i32 s42, s93, 5120\n"
    "s_lshl_b32 s45, s50, 8\n"
    "s_mul_i32 s40, s32, 1277952\n"
    "s_lshl_b32 s41, s51, 14\n"
    "s_add_u32 s40, s40, s41\n"
    "s_add_u32 s40, s40, s45\n"
    "s_add_u32 s41, s40, 128\n"
    "v_bfe_u32 v9, v8, 2, 3\n"
    "v_lshlrev_b32_e32 v244, 7, v9\n"
    "v_lshrrev_b32_e32 v10, 5, v8\n"
    "v_lshl_add_u32 v244, v10, 10, v244\n"
    "v_and_b32_e32 v9, 6, v9\n"
    "v_bfe_u32 v10, v8, 1, 1\n"
    "v_xor_b32_e32 v9, v9, v10\n"
    "v_lshl_add_u32 v244, v9, 4, v244\n"
    "v_and_b32_e32 v10, 1, v8\n"
    "v_lshl_add_u32 v244, v10, 3, v244\n"
    "s_lshl_b32 s45, s51, 16\n"
    "s_add_u32 s45, s45, 32768\n"
    "v_add_u32_e32 v244, s45, v244\n"
    "v_xor_b32_e32 v245, 32, v244\n"
    "v_xor_b32_e32 v246, 64, v244\n"
    "v_xor_b32_e32 v247, 0x60, v244\n"
    "s_lshl_b32 s45, s51, 15\n"
    "s_add_u32 s48, s45, 32768\n"
    "s_sub_u32 s45, 131072, s45\n"
    "v_add_u32_e32 v242, s45, v240\n"
    "v_add_u32_e32 v240, s48, v240\n"
    "v_xor_b32_e32 v241, 64, v240\n"
    "v_xor_b32_e32 v243, 64, v242\n"
    "s_xor_b32 s45, s93, 2\n"
    "s_mul_i32 s45, s45, 5120\n"
    "v_lshl_add_u32 v249, v8, 2, s45\n"
    "s_lshl_b32 s45, s93, 11\n"
    "v_add_u32_e32 v99, s45, v250\n"
    "v_add_u32_e32 v98, 11345920, v99\n"
    "v_and_b32_e32 v252, 0x30, v250\n"
    "v_add_u32_e32 v252, 0x400, v252\n"
    "s_mul_i32 s59, s32, 999\n"
    "s_andn2_b32 s59, s59, 63\n"
    "s_sub_u32 s48, 3968, s59\n"
    "v_cmp_le_u32_e32 vcc, 0x80, v250\n"
    "v_mov_b32_e32 v80, s48\n"
    "s_add_u32 s59, s59, 6076416\n"
    "v_cndmask_b32_e32 v83, v250, v80, vcc\n"
    "v_bfe_u32 v253, v250, 4, 2\n"
    "v_max_u32_e32 v253, 1, v253\n"
    "v_add_u32_e32 v253, s32, v253\n"
    "v_and_b32_e32 v253, 3, v253\n"
    "v_lshlrev_b32_e32 v253, 9, v253\n"
    "v_mov_b32_e32 v84, s39\n"
    "v_lshrrev_b32_e32 v86, 2, v250\n"
    "v_min_u32_e32 v86, 124, v86\n"
    "s_lshl2_add_u32 s58, s34, s93\n"
    "s_lshl_b32 s58, s58, 7\n"
    "s_add_u32 s58, s58, 5910528\n"
    "s_lshl_b32 s45, s50, 13\n"
    "v_add_u32_e32 v85, s45, v250\n"
    "s_lshl3_add_u32 s45, s45, 49152\n"
    "v_add_u32_e32 v255, s45, v250\n"
    "v_lshrrev_b32_e32 v105, 7, v250\n"
    "v_and_b32_e32 v104, 0x70, v251\n"
    "v_lshl_add_u32 v104, v105, 9, v104\n"
    "v_add_u32_e32 v105, 0xc00, v104\n"
    "v_add_u32_e32 v106, 0x1800, v104\n"
    "v_add_u32_e32 v107, 0x2400, v104\n"
    "s_lshl_b32 s57, s93, 7\n"
    "s_lshl4_add_u32 s56, s57, 24576\n"
    "s_add_u32 s57, s57, 5111808\n"
    "s_mul_i32 s45, s51, 32768\n"
    "s_add_u32 s45, s45, 8192\n"
    "v_subrev_u32_e32 v46, s45, v240\n"
    "v_subrev_u32_e32 v47, s45, v241\n"
    "s_mul_i32 s45, s51, 61440\n"
    "s_add_u32 s45, s45, 8192\n"
    "v_subrev_u32_e32 v4, s45, v244\n"
    "v_subrev_u32_e32 v5, s45, v245\n"
    "v_subrev_u32_e32 v6, s45, v246\n"
    "v_subrev_u32_e32 v7, s45, v247\n"
    "s_waitcnt vmcnt(9)\n"
    "v_mfma_f32_16x16x32_bf16 v[24:27], v[112:115], a[0:3], 0\n"
    "buffer_load_dwordx4 v[196:199], v250, s[8:11], s47 offen offset:3072 sc1\n"
    "s_bitset0_b32 s61, 14\n"
    "v_mfma_f32_16x16x32_bf16 v[28:31], v[116:119], a[0:3], 0\n"
    "v_accvgpr_write_b32 a244, 0\n"
    "v_mfma_f32_16x16x32_bf16 v[48:51], v[112:115], a[32:35], 0\n"
    "v_accvgpr_write_b32 a245, 0\n"
    "v_mfma_f32_16x16x32_bf16 v[52:55], v[116:119], a[32:35], 0\n"
    "s_add_u32 s47, s52, s61\n"
    "buffer_load_dwordx4 v[208:211], v250, s[8:11], s47 offen offset:0 sc1\n"
    "v_mfma_f32_16x16x32_bf16 v[64:67], v[112:115], a[64:67], 0\n"
    "v_bfe_u32 v0, v250, 4, 2\n"
    "v_mfma_f32_16x16x32_bf16 v[68:71], v[116:119], a[64:67], 0\n"
    "s_mov_b64 s[24:25], s[18:19]\n"
    "v_accvgpr_write_b32 a246, 0\n"
    "s_waitcnt vmcnt(9)\n"
    "v_mfma_f32_16x16x32_bf16 v[24:27], v[128:131], a[4:7], v[24:27]\n"
    "buffer_load_dwordx4 v[212:215], v250, s[8:11], s47 offen offset:1024 sc1\n"
    "s_add_u32 s52, s52, 0x4000\n"
    "v_mfma_f32_16x16x32_bf16 v[28:31], v[132:135], a[4:7], v[28:31]\n"
    "buffer_store_dword v102, off, s[8:11], s46 offset:0 sc1\n"
    "v_accvgpr_write_b32 a247, 0\n"
    "v_mfma_f32_16x16x32_bf16 v[48:51], v[128:131], a[36:39], v[48:51]\n"
    "v_bfe_u32 v1, v250, 6, 2\n"
    "v_accvgpr_write_b32 a248, 0\n"
    "v_mfma_f32_16x16x32_bf16 v[52:55], v[132:135], a[36:39], v[52:55]\n"
    "buffer_load_dwordx4 v[224:227], v250, s[8:11], s47 offen offset:2048 sc1\n"
    "s_add_u32 s61, s61, 0x1000\n"
    "v_mfma_f32_16x16x32_bf16 v[64:67], v[128:131], a[68:71], v[64:67]\n"
    "s_mov_b32 s18, 5120000\n"
    "v_accvgpr_write_b32 a249, 0\n"
    "v_mfma_f32_16x16x32_bf16 v[68:71], v[132:135], a[68:71], v[68:71]\n"
    "v_mov_b32_e32 v2, 0x3f80\n"
    "s_waitcnt vmcnt(10)\n"
    "v_mfma_f32_16x16x32_bf16 v[24:27], v[144:147], a[8:11], v[24:27]\n"
    "buffer_load_dwordx4 v[228:231], v250, s[8:11], s47 offen offset:3072 sc1\n"
    "s_bitset0_b32 s61, 14\n"
    "v_mfma_f32_16x16x32_bf16 v[28:31], v[148:151], a[8:11], v[28:31]\n"
    "v_accvgpr_write_b32 a250, 0\n"
    "v_mfma_f32_16x16x32_bf16 v[48:51], v[144:147], a[40:43], v[48:51]\n"
    "s_mov_b32 s19, 0x20000\n"
    "v_accvgpr_write_b32 a251, 0\n"
    "v_mfma_f32_16x16x32_bf16 v[52:55], v[148:151], a[40:43], v[52:55]\n"
    "s_add_u32 s47, s52, s61\n"
    "buffer_load_dwordx4 v[120:123], v250, s[8:11], s47 offen offset:0 sc1\n"
    "v_mfma_f32_16x16x32_bf16 v[64:67], v[144:147], a[72:75], v[64:67]\n"
    "v_cmp_eq_u32_e32 vcc, 3, v0\n"
    "s_nop 1\n"
    "v_cndmask_b32_e64 v2, v2, 1.0, vcc\n"
    "v_mfma_f32_16x16x32_bf16 v[68:71], v[148:151], a[72:75], v[68:71]\n"
    "v_accvgpr_write_b32 a252, 0\n"
    "s_waitcnt vmcnt(10)\n"
    "v_mfma_f32_16x16x32_bf16 v[24:27], v[160:163], a[12:15], v[24:27]\n"
    "buffer_load_dwordx4 v[124:127], v250, s[8:11], s47 offen offset:1024 sc1\n"
    "v_accvgpr_write_b32 a253, 0\n"
    "v_mfma_f32_16x16x32_bf16 v[28:31], v[164:167], a[12:15], v[28:31]\n"
    "s_mov_b64 s[20:21], s[8:9]\n"
    "v_accvgpr_write_b32 a254, 0\n"
    "v_mfma_f32_16x16x32_bf16 v[48:51], v[160:163], a[44:47], v[48:51]\n"
    "v_cmp_eq_u32_e32 vcc, 0, v1\n"
    "s_nop 1\n"
    "v_cndmask_b32_e32 v3, 0, v2, vcc\n"
    "v_mfma_f32_16x16x32_bf16 v[52:55], v[164:167], a[44:47], v[52:55]\n"
    "buffer_load_dwordx4 v[136:139], v250, s[8:11], s47 offen offset:2048 sc1\n"
    "s_add_u32 s61, s61, 0x1000\n"
    "v_mfma_f32_16x16x32_bf16 v[64:67], v[160:163], a[76:79], v[64:67]\n"
    "v_accvgpr_write_b32 a232, v3\n"
    "v_accvgpr_write_b32 a255, 0\n"
    "v_mfma_f32_16x16x32_bf16 v[68:71], v[164:167], a[76:79], v[68:71]\n"
    "s_mov_b32 s22, 5242880\n"
    "v_accvgpr_write_b32 a192, 0\n"
    "s_waitcnt vmcnt(10)\n"
    "v_mfma_f32_16x16x32_bf16 v[24:27], v[176:179], a[16:19], v[24:27]\n"
    "buffer_load_dwordx4 v[140:143], v250, s[8:11], s47 offen offset:3072 sc1\n"
    "s_bitset0_b32 s61, 14\n"
    "v_mfma_f32_16x16x32_bf16 v[28:31], v[180:183], a[16:19], v[28:31]\n"
    "v_cmp_eq_u32_e32 vcc, 1, v1\n"
    "s_nop 1\n"
    "v_cndmask_b32_e32 v3, 0, v2, vcc\n"
    "v_mfma_f32_16x16x32_bf16 v[48:51], v[176:179], a[48:51], v[48:51]\n"
    "s_movk_i32 s26, 20000\n"
    "v_accvgpr_write_b32 a193, 0\n"
    "v_mfma_f32_16x16x32_bf16 v[52:55], v[180:183], a[48:51], v[52:55]\n"
    "s_add_u32 s47, s52, s61\n"
    "buffer_load_dwordx4 v[152:155], v250, s[8:11], s47 offen offset:0 sc1\n"
    "v_mfma_f32_16x16x32_bf16 v[64:67], v[176:179], a[80:83], v[64:67]\n"
    "buffer_load_dword v80, v83, s[8:11], s59 offen offset:0 sc1\n"
    "v_accvgpr_write_b32 a194, 0\n"
    "v_mfma_f32_16x16x32_bf16 v[68:71], v[180:183], a[80:83], v[68:71]\n"
    "v_accvgpr_write_b32 a233, v3\n"
    "v_accvgpr_write_b32 a195, 0\n"
    "s_waitcnt vmcnt(11)\n"
    "v_mfma_f32_16x16x32_bf16 v[24:27], v[192:195], a[20:23], v[24:27]\n"
    "buffer_load_dwordx4 v[156:159], v250, s[8:11], s47 offen offset:1024 sc1\n"
    "v_accvgpr_write_b32 a196, 0\n"
    "v_mfma_f32_16x16x32_bf16 v[28:31], v[196:199], a[20:23], v[28:31]\n"
    "s_bfe_u32 s88, s92, 0x20001\n"
    "v_mfma_f32_16x16x32_bf16 v[48:51], v[192:195], a[52:55], v[48:51]\n"
    "v_accvgpr_write_b32 a197, 0\n"
    "v_mfma_f32_16x16x32_bf16 v[52:55], v[196:199], a[52:55], v[52:55]\n"
    "buffer_load_dwordx4 v[168:171], v250, s[8:11], s47 offen offset:2048 sc1\n"
    "s_add_u32 s61, s61, 0x1000\n"
    "v_mfma_f32_16x16x32_bf16 v[64:67], v[192:195], a[84:87], v[64:67]\n"
    "v_cmp_eq_u32_e32 vcc, 2, v1\n"
    "s_nop 1\n"
    "v_cndmask_b32_e32 v3, 0, v2, vcc\n"
    "v_mfma_f32_16x16x32_bf16 v[68:71], v[196:199], a[84:87], v[68:71]\n"
    "s_lshl_b32 s82, s88, 7\n"
    "v_accvgpr_write_b32 a198, 0\n"
    "s_waitcnt vmcnt(11)\n"
    "v_mfma_f32_16x16x32_bf16 v[24:27], v[208:211], a[24:27], v[24:27]\n"
    "buffer_load_dwordx4 v[172:175], v250, s[8:11], s47 offen offset:3072 sc1\n"
    "s_bitset0_b32 s61, 14\n"
    "v_mfma_f32_16x16x32_bf16 v[28:31], v[212:215], a[24:27], v[28:31]\n"
    "v_accvgpr_write_b32 a234, v3\n"
    "v_accvgpr_write_b32 a199, 0\n"
    "v_mfma_f32_16x16x32_bf16 v[48:51], v[208:211], a[56:59], v[48:51]\n"
    "v_accvgpr_write_b32 a200, 0\n"
    "v_mfma_f32_16x16x32_bf16 v[52:55], v[212:215], a[56:59], v[52:55]\n"
    "s_add_u32 s47, s52, s61\n"
    "buffer_load_dwordx4 v[184:187], v250, s[8:11], s47 offen offset:0 sc1\n"
    "v_mfma_f32_16x16x32_bf16 v[64:67], v[208:211], a[88:91], v[64:67]\n"
    "s_add_u32 s82, s82, 6101248\n"
    "v_accvgpr_write_b32 a201, 0\n"
    "v_mfma_f32_16x16x32_bf16 v[68:71], v[212:215], a[88:91], v[68:71]\n"
    "v_cmp_eq_u32_e32 vcc, 3, v1\n"
    "s_nop 1\n"
    "v_cndmask_b32_e32 v3, 0, v2, vcc\n"
    "s_waitcnt vmcnt(10)\n"
    "v_mfma_f32_16x16x32_bf16 v[24:27], v[224:227], a[28:31], v[24:27]\n"
    "buffer_load_dwordx4 v[188:191], v250, s[8:11], s47 offen offset:1024 sc1\n"
    "v_accvgpr_write_b32 a202, 0\n"
    "v_mfma_f32_16x16x32_bf16 v[28:31], v[228:231], a[28:31], v[28:31]\n"
    "v_accvgpr_write_b32 a235, v3\n"
    "v_accvgpr_write_b32 a203, 0\n"
    "v_mfma_f32_16x16x32_bf16 v[48:51], v[224:227], a[60:63], v[48:51]\n"
    "s_lshl_b32 s83, s88, 17\n"
    "v_accvgpr_write_b32 a204, 0\n"
    "v_mfma_f32_16x16x32_bf16 v[52:55], v[228:231], a[60:63], v[52:55]\n"
    "buffer_load_dwordx4 v[200:203], v250, s[8:11], s47 offen offset:2048 sc1\n"
    "s_add_u32 s61, s61, 0x1000\n"
    "v_mfma_f32_16x16x32_bf16 v[64:67], v[224:227], a[92:95], v[64:67]\n"
    "s_and_b32 s89, s34, 31\n"
    "v_accvgpr_write_b32 a205, 0\n"
    "v_mfma_f32_16x16x32_bf16 v[68:71], v[228:231], a[92:95], v[68:71]\n"
    "s_lshl2_add_u32 s89, s89, s93\n"
    "v_accvgpr_write_b32 a206, 0\n"
    "s_waitcnt vmcnt(10)\n"
    "v_mfma_f32_16x16x32_bf16 v[32:35], v[120:123], a[0:3], 0\n"
    "buffer_load_dwordx4 v[204:207], v250, s[8:11], s47 offen offset:3072 sc1\n"
    "s_bitset0_b32 s61, 14\n"
    "v_mfma_f32_16x16x32_bf16 v[36:39], v[124:127], a[0:3], 0\n"
    "v_cvt_pk_bf16_f32 v24, v24, v25\n"
    "v_accvgpr_write_b32 a207, 0\n"
    "v_mfma_f32_16x16x32_bf16 v[56:59], v[120:123], a[32:35], 0\n"
    "v_cvt_pk_bf16_f32 v25, v26, v27\n"
    "v_accvgpr_write_b32 a208, 0\n"
    "v_mfma_f32_16x16x32_bf16 v[60:63], v[124:127], a[32:35], 0\n"
    "s_add_u32 s47, s52, s61\n"
    "buffer_load_dwordx4 v[216:219], v250, s[8:11], s47 offen offset:0 sc1\n"
    "v_mfma_f32_16x16x32_bf16 v[72:75], v[120:123], a[64:67], 0\n"
    "v_cvt_pk_bf16_f32 v26, v28, v29\n"
    "v_cvt_pk_bf16_f32 v27, v30, v31\n"
    "v_mfma_f32_16x16x32_bf16 v[76:79], v[124:127], a[64:67], 0\n"
    "buffer_store_dwordx4 v[24:27], v98, s[8:11], s35 offen offset:0 sc1\n"
    "v_accvgpr_write_b32 a209, 0\n"
    "s_waitcnt vmcnt(11)\n"
    "v_mfma_f32_16x16x32_bf16 v[32:35], v[136:139], a[4:7], v[32:35]\n"
    "buffer_load_dwordx4 v[220:223], v250, s[8:11], s47 offen offset:1024 sc1\n"
    "s_add_u32 s52, s52, 0x4000\n"
    "v_mfma_f32_16x16x32_bf16 v[36:39], v[140:143], a[4:7], v[36:39]\n"
    "v_cvt_pk_bf16_f32 v48, v48, v49\n"
    "v_accvgpr_write_b32 a210, 0\n"
    "v_mfma_f32_16x16x32_bf16 v[56:59], v[136:139], a[36:39], v[56:59]\n"
    "v_cvt_pk_bf16_f32 v49, v50, v51\n"
    "v_cvt_pk_bf16_f32 v50, v52, v53\n"
    "v_mfma_f32_16x16x32_bf16 v[60:63], v[140:143], a[36:39], v[60:63]\n"
    "buffer_load_dwordx4 v[232:235], v250, s[8:11], s47 offen offset:2048 sc1\n"
    "s_add_u32 s61, s61, 0x1000\n"
    "s_waitcnt vmcnt(11)\n"
    "v_cmp_ne_u32_e32 vcc, s39, v80\n"
    "v_mfma_f32_16x16x32_bf16 v[72:75], v[136:139], a[68:71], v[72:75]\n"
    "v_cvt_pk_bf16_f32 v51, v54, v55\n"
    "buffer_store_dwordx4 v[48:51], v98, s[8:11], s54 offen offset:0 sc1\n"
    "v_mfma_f32_16x16x32_bf16 v[76:79], v[140:143], a[68:71], v[76:79]\n"
    "v_cvt_pk_bf16_f32 v64, v64, v65\n"
    "v_cvt_pk_bf16_f32 v65, v66, v67\n"
    "s_waitcnt vmcnt(11)\n"
    "v_mfma_f32_16x16x32_bf16 v[32:35], v[152:155], a[8:11], v[32:35]\n"
    "buffer_load_dwordx4 v[236:239], v250, s[8:11], s47 offen offset:3072 sc1\n"
    "s_bitset0_b32 s61, 14\n"
    "v_mfma_f32_16x16x32_bf16 v[36:39], v[156:159], a[8:11], v[36:39]\n"
    "s_cbranch_vccnz .Lat_h1poll\n"
    ".Lat_h1poll_ret:\n"
    "s_mov_b32 m0, s56\n"
    "v_mfma_f32_16x16x32_bf16 v[56:59], v[152:155], a[40:43], v[56:59]\n"
    "buffer_load_dwordx4 v104, s[8:11], s57 offen offset:0 sc1 lds\n"
    "v_cvt_pk_bf16_f32 v66, v68, v69\n"
    "v_mfma_f32_16x16x32_bf16 v[60:63], v[156:159], a[40:43], v[60:63]\n"
    "v_cvt_pk_bf16_f32 v67, v70, v71\n"
    "buffer_store_dwordx4 v[64:67], v98, s[8:11], s60 offen offset:0 sc1\n"
    "v_mfma_f32_16x16x32_bf16 v[72:75], v[152:155], a[72:75], v[72:75]\n"
    "s_lshl_b32 s89, s89, 10\n"
    "v_accvgpr_write_b32 a211, 0\n"
    "v_mfma_f32_16x16x32_bf16 v[76:79], v[156:159], a[72:75], v[76:79]\n"
    "s_add_u32 s83, s83, s89\n"
    "v_accvgpr_write_b32 a212, 0\n"
    "s_waitcnt vmcnt(12)\n"
    "v_mfma_f32_16x16x32_bf16 v[32:35], v[168:171], a[12:15], v[32:35]\n"
    "buffer_load_dwordx4 v105, s[8:11], s57 offen offset:1024 sc1 lds\n"
    "v_accvgpr_write_b32 a213, 0\n"
    "v_mfma_f32_16x16x32_bf16 v[36:39], v[172:175], a[12:15], v[36:39]\n"
    "s_add_u32 s83, s83, 67682304\n"
    "v_accvgpr_write_b32 a214, 0\n"
    "v_mfma_f32_16x16x32_bf16 v[56:59], v[168:171], a[44:47], v[56:59]\n"
    "ds_write_b128 v99, v[24:27] offset:0\n"
    "v_accvgpr_write_b32 a215, 0\n"
    "v_mfma_f32_16x16x32_bf16 v[60:63], v[172:175], a[44:47], v[60:63]\n"
    "s_add_u32 m0, s43, 0\n"
    "v_accvgpr_write_b32 a216, 0\n"
    "v_mfma_f32_16x16x32_bf16 v[72:75], v[168:171], a[76:79], v[72:75]\n"
    "buffer_load_dwordx4 v104, s[8:11], s40 offen offset:0 sc1 lds\n"
    "v_accvgpr_write_b32 a217, 0\n"
    "v_mfma_f32_16x16x32_bf16 v[76:79], v[172:175], a[76:79], v[76:79]\n"
    "s_lshl_b32 s30, s93, 10\n"
    "s_waitcnt vmcnt(12)\n"
    "v_mfma_f32_16x16x32_bf16 v[32:35], v[184:187], a[16:19], v[32:35]\n"
    "v_accvgpr_write_b32 a218, 0\n"
    "v_mfma_f32_16x16x32_bf16 v[36:39], v[188:191], a[16:19], v[36:39]\n"
    "s_add_u32 s30, s30, 20480\n"
    "v_accvgpr_write_b32 a219, 0\n"
    "v_mfma_f32_16x16x32_bf16 v[56:59], v[184:187], a[48:51], v[56:59]\n"
    "buffer_load_dwordx4 v105, s[8:11], s40 offen offset:1024 sc1 lds\n"
    "v_mfma_f32_16x16x32_bf16 v[60:63], v[188:191], a[48:51], v[60:63]\n"
    "v_accvgpr_write_b32 a220, 0\n"
    "v_mfma_f32_16x16x32_bf16 v[72:75], v[184:187], a[80:83], v[72:75]\n"
    "s_bitcmp0_b32 s92, 0\n"
    "s_cselect_b32 s85, 1, 0\n"
    "s_cselect_b32 s80, 5, 0\n"
    "v_mfma_f32_16x16x32_bf16 v[76:79], v[188:191], a[80:83], v[76:79]\n"
    "ds_write_b128 v99, v[48:51] offset:8192\n"
    "v_accvgpr_write_b32 a221, 0\n"
    "s_waitcnt vmcnt(11)\n"
    "v_mfma_f32_16x16x32_bf16 v[32:35], v[200:203], a[20:23], v[32:35]\n"
    "buffer_load_dwordx4 v106, s[8:11], s40 offen offset:2048 sc1 lds\n"
    "v_mfma_f32_16x16x32_bf16 v[36:39], v[204:207], a[20:23], v[36:39]\n"
    "v_accvgpr_write_b32 a222, 0\n"
    "v_mfma_f32_16x16x32_bf16 v[56:59], v[200:203], a[52:55], v[56:59]\n"
    "v_accvgpr_write_b32 a223, 0\n"
    "v_mfma_f32_16x16x32_bf16 v[60:63], v[204:207], a[52:55], v[60:63]\n"
    "s_bitcmp0_b32 s92, 0\n"
    "s_cselect_b32 s82, s82, 2.0\n"
    "s_cselect_b32 s83, s83, 2.0\n"
    "v_mfma_f32_16x16x32_bf16 v[72:75], v[200:203], a[84:87], v[72:75]\n"
    "buffer_load_dwordx4 v107, s[8:11], s40 offen offset:3072 sc1 lds\n"
    "v_mfma_f32_16x16x32_bf16 v[76:79], v[204:207], a[84:87], v[76:79]\n"
    "v_accvgpr_write_b32 a224, 0\n"
    "s_waitcnt vmcnt(10)\n"
    "v_mfma_f32_16x16x32_bf16 v[32:35], v[216:219], a[24:27], v[32:35]\n"
    "v_accvgpr_write_b32 a225, 0\n"
    "v_mfma_f32_16x16x32_bf16 v[36:39], v[220:223], a[24:27], v[36:39]\n"
    "s_add_u32 s40, s40, 0x8000\n"
    "v_mfma_f32_16x16x32_bf16 v[56:59], v[216:219], a[56:59], v[56:59]\n"
    "ds_write_b128 v99, v[64:67] offset:49152\n"
    "v_mfma_f32_16x16x32_bf16 v[60:63], v[220:223], a[56:59], v[60:63]\n"
    "s_mov_b32 s84, 2.0\n"
    "v_mfma_f32_16x16x32_bf16 v[72:75], v[216:219], a[88:91], v[72:75]\n"
    "v_accvgpr_write_b32 a226, 0\n"
    "v_mfma_f32_16x16x32_bf16 v[76:79], v[220:223], a[88:91], v[76:79]\n"
    "v_accvgpr_write_b32 a227, 0\n"
    "s_waitcnt vmcnt(7)\n"
    "v_mfma_f32_16x16x32_bf16 v[32:35], v[232:235], a[28:31], v[32:35]\n"
    "s_movk_i32 s90, 512\n"
    "v_mfma_f32_16x16x32_bf16 v[36:39], v[236:239], a[28:31], v[36:39]\n"
    "v_accvgpr_write_b32 a228, 0\n"
    "v_mfma_f32_16x16x32_bf16 v[56:59], v[232:235], a[60:63], v[56:59]\n"
    "v_accvgpr_write_b32 a229, 0\n"
    "v_mfma_f32_16x16x32_bf16 v[60:63], v[236:239], a[60:63], v[60:63]\n"
    "v_accvgpr_write_b32 a230, 0\n"
    "v_mfma_f32_16x16x32_bf16 v[72:75], v[232:235], a[92:95], v[72:75]\n"
    "s_mov_b32 s4, 0x1c400000\n"
    "v_mfma_f32_16x16x32_bf16 v[76:79], v[236:239], a[92:95], v[76:79]\n"
    "v_accvgpr_write_b32 a231, 0\n"
    "v_cvt_pk_bf16_f32 v32, v32, v33\n"
    "v_cvt_pk_bf16_f32 v33, v34, v35\n"
    "v_cvt_pk_bf16_f32 v34, v36, v37\n"
    "v_cvt_pk_bf16_f32 v35, v38, v39\n"
    "ds_write_b128 v99, v[32:35] offset:1024\n"
    "buffer_store_dwordx4 v[32:35], v98, s[8:11], s35 offen offset:1024 sc1\n"
    "v_cvt_pk_bf16_f32 v56, v56, v57\n"
    "v_cvt_pk_bf16_f32 v57, v58, v59\n"
    "v_cvt_pk_bf16_f32 v58, v60, v61\n"
    "v_cvt_pk_bf16_f32 v59, v62, v63\n"
    "ds_write_b128 v99, v[56:59] offset:9216\n"
    "buffer_store_dwordx4 v[56:59], v98, s[8:11], s54 offen offset:1024 sc1\n"
    "v_cvt_pk_bf16_f32 v72, v72, v73\n"
    "v_cvt_pk_bf16_f32 v73, v74, v75\n"
    "v_cvt_pk_bf16_f32 v74, v76, v77\n"
    "v_cvt_pk_bf16_f32 v75, v78, v79\n"
    "ds_write_b128 v99, v[72:75] offset:50176\n"
    "buffer_store_dwordx4 v[72:75], v98, s[8:11], s60 offen offset:1024 sc1\n"
    "v_mov_b32_e32 v8, s4\n"
    "v_mov_b32_e32 v9, s4\n"
    "s_add_u32 m0, s43, 4096\n"
    "v_mov_b64_e32 v[10:11], v[8:9]\n"
    "v_mov_b64_e32 v[12:13], v[8:9]\n"
    "buffer_load_dwordx4 v104, s[8:11], s41 offen offset:0 sc1 lds\n"
    "v_mov_b64_e32 v[14:15], v[8:9]\n"
    "v_mov_b64_e32 v[16:17], v[8:9]\n"
    "buffer_load_dwordx4 v105, s[8:11], s41 offen offset:1024 sc1 lds\n"
    "v_mov_b64_e32 v[18:19], v[8:9]\n"
    "v_mov_b64_e32 v[28:29], v[8:9]\n"
    "buffer_load_dwordx4 v106, s[8:11], s41 offen offset:2048 sc1 lds\n"
    "v_mov_b64_e32 v[30:31], v[8:9]\n"
    "v_mov_b64_e32 v[32:33], v[8:9]\n"
    "buffer_load_dwordx4 v107, s[8:11], s41 offen offset:3072 sc1 lds\n"
    "v_mov_b64_e32 v[34:35], v[8:9]\n"
    "v_mov_b64_e32 v[36:37], v[8:9]\n"
    "s_add_u32 s41, s41, 0x8000\n"
    "v_mov_b64_e32 v[38:39], v[8:9]\n"
    "v_mov_b64_e32 v[48:49], v[8:9]\n"
    "v_mov_b64_e32 v[50:51], v[8:9]\n"
    "v_mov_b64_e32 v[52:53], v[8:9]\n"
    "v_mov_b64_e32 v[54:55], v[8:9]\n"
    "v_mov_b64_e32 v[56:57], v[8:9]\n"
    "v_mov_b64_e32 v[58:59], v[8:9]\n"
    "v_mov_b64_e32 v[68:69], v[8:9]\n"
    "v_mov_b64_e32 v[70:71], v[8:9]\n"
    "v_mov_b64_e32 v[72:73], v[8:9]\n"
    "v_mov_b64_e32 v[74:75], v[8:9]\n"
    "v_mov_b64_e32 v[76:77], v[8:9]\n"
    "s_cmp_lg_u32 s93, 3\n"
    "s_cbranch_scc1 .Lat_wn40\n"
    "s_call_b64 s[2:3], .Lat_w6\n"
    "s_call_b64 s[2:3], .Lat_w10\n"
    ".Lat_wn40:\n"
    "s_cmp_lg_u32 s93, 2\n"
    "s_cbranch_scc1 .Lat_wn41\n"
    "s_call_b64 s[2:3], .Lat_w7\n"
    "s_call_b64 s[2:3], .Lat_w11\n"
    ".Lat_wn41:\n"
    "s_cmp_lg_u32 s93, 1\n"
    "s_cbranch_scc1 .Lat_wn42\n"
    "s_call_b64 s[2:3], .Lat_w8\n"
    "s_call_b64 s[2:3], .Lat_w12\n"
    ".Lat_wn42:\n"
    "s_cmp_lg_u32 s93, 0\n"
    "s_cbranch_scc1 .Lat_wn43\n"
    "s_call_b64 s[2:3], .Lat_w9\n"
    "s_call_b64 s[2:3], .Lat_w13\n"
    ".Lat_wn43:\n"
    "s_waitcnt vmcnt(4)\n"
    "buffer_store_dword v84, v86, s[8:11], s58 offen offset:0 sc1\n"
    "v_mov_b64_e32 v[78:79], v[8:9]\n"
    "v_mov_b64_e32 v[88:89], v[8:9]\n"
    "s_add_u32 m0, s43, 8192\n"
    "v_mov_b64_e32 v[90:91], v[8:9]\n"
    "v_mov_b64_e32 v[92:93], v[8:9]\n"
    "buffer_load_dwordx4 v104, s[8:11], s40 offen offset:0 sc1 lds\n"
    "v_mov_b64_e32 v[94:95], v[8:9]\n"
    "v_mov_b64_e32 v[96:97], v[8:9]\n"
    "buffer_load_dwordx4 v105, s[8:11], s40 offen offset:1024 sc1 lds\n"
    "v_mov_b64_e32 v[98:99], v[8:9]\n"
    "v_mov_b64_e32 v[108:109], v[8:9]\n"
    "buffer_load_dwordx4 v106, s[8:11], s40 offen offset:2048 sc1 lds\n"
    "v_mov_b64_e32 v[110:111], v[8:9]\n"
    "v_mov_b64_e32 v[112:113], v[8:9]\n"
    "buffer_load_dwordx4 v107, s[8:11], s40 offen offset:3072 sc1 lds\n"
    "v_mov_b64_e32 v[114:115], v[8:9]\n"
    "v_mov_b64_e32 v[116:117], v[8:9]\n"
    "s_add_u32 s40, s40, 0x8000\n"
    "v_mov_b64_e32 v[118:119], v[8:9]\n"
    "v_mov_b64_e32 v[128:129], v[8:9]\n"
    "s_add_u32 m0, s43, 12288\n"
    "v_mov_b64_e32 v[130:131], v[8:9]\n"
    "v_mov_b64_e32 v[132:133], v[8:9]\n"
    "buffer_load_dwordx4 v104, s[8:11], s41 offen offset:0 sc1 lds\n"
    "v_mov_b64_e32 v[134:135], v[8:9]\n"
    "v_mov_b64_e32 v[136:137], v[8:9]\n"
    "buffer_load_dwordx4 v105, s[8:11], s41 offen offset:1024 sc1 lds\n"
    "v_mov_b64_e32 v[138:139], v[8:9]\n"
    "buffer_load_dwordx4 v106, s[8:11], s41 offen offset:2048 sc1 lds\n"
    "buffer_load_dwordx4 v107, s[8:11], s41 offen offset:3072 sc1 lds\n"
    "s_add_u32 s41, s41, 0x8000\n"
    ".Lat_p2:\n"
    "v_mov_b32_e32 v220, 0xc2ae0000\n"
    "v_mov_b32_e32 v221, 0xc2ae0000\n"
    "v_mov_b64_e32 v[222:223], v[220:221]\n"
    "v_mov_b64_e32 v[224:225], v[220:221]\n"
    "v_mov_b64_e32 v[226:227], v[220:221]\n"
    "v_mov_b64_e32 v[228:229], v[220:221]\n"
    "v_mov_b64_e32 v[230:231], v[220:221]\n"
    "v_mov_b64_e32 v[232:233], v[220:221]\n"
    "v_mov_b64_e32 v[234:235], v[220:221]\n"
    "v_mov_b64_e32 v[236:237], v[220:221]\n"
    "v_mov_b64_e32 v[238:239], v[220:221]\n"
    "s_andn2_b32 s47, s58, 0x600\n"
    "s_mov_b32 s44, 9\n"
    "s_waitcnt vmcnt(9)\n"
    "s_waitcnt lgkmcnt(0)\n"
    "s_barrier\n"
    "s_cmp_lg_u32 s49, 0\n"
    "s_cbranch_scc1 .Lat_maxpass\n"
    "ds_read_b128 a[0:3], v85 offset:0\n"
    "ds_read_b128 a[4:7], v85 offset:1024\n"
    "ds_read_b128 a[8:11], v85 offset:2048\n"
    "ds_read_b128 a[12:15], v85 offset:3072\n"
    "ds_read_b128 a[16:19], v85 offset:4096\n"
    "ds_read_b128 a[20:23], v85 offset:5120\n"
    "ds_read_b128 a[24:27], v85 offset:6144\n"
    "ds_read_b128 a[28:31], v85 offset:7168\n"
    "ds_read_b128 a[32:35], v255 offset:0\n"
    "ds_read_b128 a[36:39], v255 offset:1024\n"
    "ds_read_b128 a[40:43], v255 offset:2048\n"
    "ds_read_b128 a[44:47], v255 offset:3072\n"
    "ds_read_b128 a[48:51], v255 offset:4096\n"
    "ds_read_b128 a[52:55], v255 offset:5120\n"
    "ds_read_b128 a[56:59], v255 offset:6144\n"
    "ds_read_b128 a[60:63], v255 offset:7168\n"
    ".Lat_maxpass_ret:\n"
    "ds_read_b128 a[160:163], v46 offset:0\n"
    "ds_read_b128 a[164:167], v47 offset:0\n"
    "ds_read_b128 a[168:171], v46 offset:2048\n"
    "ds_read_b128 a[172:175], v47 offset:2048\n"
    "ds_read_b128 a[176:179], v46 offset:4096\n"
    "ds_read_b128 a[180:183], v47 offset:4096\n"
    "ds_read_b128 a[184:187], v46 offset:6144\n"
    "ds_read_b128 a[188:191], v47 offset:6144\n"
    "s_mul_i32 s48, s33, 81920\n"
    "s_add_u32 s48, s48, 11345920\n"
    "s_lshl_b32 s46, s93, 11\n"
    "s_add_u32 s48, s48, s46\n"
    "s_add_u32 s46, s46, 49152\n"
    "buffer_load_dword v254, v253, s[8:11], s47 offen offset:0 sc1\n"
    "s_waitcnt lgkmcnt(4)\n"
    "v_mfma_f32_16x16x32_bf16 v[212:215], a[160:163], a[0:3], v[220:223]\n"
    ".long 0xbedf00ff\n"
    ".Lat_w1:\n"
    "s_setpc_b64 s[2:3]\n"
    "s_and_b32 s45, s32, 1\n"
    "ds_read_b64_tr_b16 v[140:141], v4 offset:0\n"
    "v_mfma_f32_16x16x32_bf16 v[216:219], a[160:163], a[32:35], v[224:227]\n"
    "s_xor_b32 s45, s45, 9\n"
    "ds_read_b64_tr_b16 v[142:143], v5 offset:0\n"
    "v_mfma_f32_16x16x32_bf16 v[212:215], a[164:167], a[4:7], v[212:215]\n"
    "s_lshl_b32 s45, s45, 13\n"
    "ds_read_b64_tr_b16 v[144:145], v6 offset:0\n"
    "v_mfma_f32_16x16x32_bf16 v[216:219], a[164:167], a[36:39], v[216:219]\n"
    "s_add_u32 s45, s45, s48\n"
    "ds_read_b64_tr_b16 v[146:147], v7 offset:0\n"
    "v_mfma_f32_16x16x32_bf16 v[212:215], a[168:171], a[8:11], v[212:215]\n"
    "s_add_u32 s54, s32, 1\n"
    "ds_read_b64_tr_b16 v[148:149], v4 offset:2048\n"
    "v_mfma_f32_16x16x32_bf16 v[216:219], a[168:171], a[40:43], v[216:219]\n"
    "s_and_b32 s54, s54, 3\n"
    "ds_read_b64_tr_b16 v[150:151], v5 offset:2048\n"
    "v_mfma_f32_16x16x32_bf16 v[212:215], a[172:175], a[12:15], v[212:215]\n"
    "s_lshl_b32 s54, s54, 13\n"
    "ds_read_b64_tr_b16 v[152:153], v6 offset:2048\n"
    "v_mfma_f32_16x16x32_bf16 v[216:219], a[172:175], a[44:47], v[216:219]\n"
    "s_add_u32 s54, s54, s48\n"
    "ds_read_b64_tr_b16 v[154:155], v7 offset:2048\n"
    "s_waitcnt lgkmcnt(8)\n"
    "v_mfma_f32_16x16x32_bf16 v[212:215], a[176:179], a[16:19], v[212:215]\n"
    "s_add_u32 s55, s32, 2\n"
    "ds_read_b128 v[180:183], v240 offset:2048\n"
    "v_mfma_f32_16x16x32_bf16 v[216:219], a[176:179], a[48:51], v[216:219]\n"
    "s_and_b32 s55, s55, 3\n"
    "ds_read_b128 v[184:187], v241 offset:2048\n"
    "v_mfma_f32_16x16x32_bf16 v[212:215], a[180:183], a[20:23], v[212:215]\n"
    "s_lshl_b32 s55, s55, 13\n"
    "ds_read_b128 v[188:191], v240 offset:6144\n"
    "v_mfma_f32_16x16x32_bf16 v[216:219], a[180:183], a[52:55], v[216:219]\n"
    "s_add_u32 s55, s55, s48\n"
    "ds_read_b128 v[192:195], v241 offset:6144\n"
    "v_mfma_f32_16x16x32_bf16 v[212:215], a[184:187], a[24:27], v[212:215]\n"
    "s_add_u32 s60, s32, 3\n"
    "ds_read_b128 v[196:199], v242 offset:2048\n"
    "v_mfma_f32_16x16x32_bf16 v[216:219], a[184:187], a[56:59], v[216:219]\n"
    "s_and_b32 s60, s60, 3\n"
    "ds_read_b128 v[200:203], v243 offset:2048\n"
    "v_mfma_f32_16x16x32_bf16 v[212:215], a[188:191], a[28:31], v[212:215]\n"
    "s_lshl_b32 s60, s60, 13\n"
    "ds_read_b128 v[204:207], v242 offset:6144\n"
    "v_mfma_f32_16x16x32_bf16 v[216:219], a[188:191], a[60:63], v[216:219]\n"
    "s_add_u32 s60, s60, s48\n"
    "ds_read_b128 v[208:211], v243 offset:6144\n"
    "s_lshr_b32 s61, s32, 1\n"
    "s_or_b32 s61, s61, s50\n"
    "s_sub_u32 s61, s61, 1\n"
    "s_add_u32 m0, s46, 65536\n"
    "s_waitcnt vmcnt(0)\n"
    "v_cmp_ne_u32_e32 vcc, s39, v254\n"
    "ds_read_b128 a[160:163], v240 offset:0\n"
    "ds_read_b128 a[164:167], v241 offset:0\n"
    "ds_read_b128 a[168:171], v240 offset:4096\n"
    "ds_read_b128 a[172:175], v241 offset:4096\n"
    "ds_read_b128 a[176:179], v242 offset:0\n"
    "s_cbranch_vccz .Lat_qpoll_ret\n"
    ".Lat_qpoll:\n"
    "s_mov_b32 s53, 0x400000\n"
    ".Lat_qpoll_l:\n"
    "s_sleep 0\n"
    "buffer_load_dword v254, v253, s[8:11], s47 offen offset:0 sc1\n"
    "s_waitcnt vmcnt(0)\n"
    "v_cmp_ne_u32_e32 vcc, s39, v254\n"
    "s_sub_u32 s53, s53, 1\n"
    "s_cbranch_scc1 .Lat_qpoll_ret\n"
    "s_nop 2\n"
    "s_cbranch_vccnz .Lat_qpoll_l\n"
    ".Lat_qpoll_ret:\n"
    "buffer_load_dwordx4 v250, s[8:11], s45 offen offset:0 sc1 lds\n"
    "buffer_load_dwordx4 v250, s[8:11], s45 offen offset:1024 sc1 lds\n"
    "s_add_u32 m0, s46, 8192\n"
    "ds_read_b128 a[180:183], v243 offset:0\n"
    "s_nop 0\n"
    "ds_read_b128 a[184:187], v242 offset:4096\n"
    "buffer_load_dwordx4 v250, s[8:11], s54 offen offset:0 sc1 lds\n"
    "ds_read_b128 a[188:191], v243 offset:4096\n"
    "buffer_load_dwordx4 v250, s[8:11], s54 offen offset:1024 sc1 lds\n"
    "v_exp_f32_e32 v212, v212\n"
    "s_add_u32 m0, s46, 73728\n"
    "v_exp_f32_e32 v213, v213\n"
    "s_add_u32 s54, s54, 0x8000\n"
    "v_exp_f32_e32 v214, v214\n"
    "buffer_load_dwordx4 v250, s[8:11], s54 offen offset:0 sc1 lds\n"
    "v_exp_f32_e32 v215, v215\n"
    "buffer_load_dwordx4 v250, s[8:11], s54 offen offset:1024 sc1 lds\n"
    "v_exp_f32_e32 v216, v216\n"
    "s_add_u32 m0, s46, 32768\n"
    "v_exp_f32_e32 v217, v217\n"
    "s_nop 0\n"
    "v_exp_f32_e32 v218, v218\n"
    "buffer_load_dwordx4 v250, s[8:11], s55 offen offset:0 sc1 lds\n"
    "v_exp_f32_e32 v219, v219\n"
    "buffer_load_dwordx4 v250, s[8:11], s55 offen offset:1024 sc1 lds\n"
    "v_cvt_pk_bf16_f32 v212, v212, v213\n"
    "v_cvt_pk_bf16_f32 v213, v214, v215\n"
    "v_cvt_pk_bf16_f32 v216, v216, v217\n"
    "v_cvt_pk_bf16_f32 v217, v218, v219\n"
    "v_and_b32_e32 v216, s61, v216\n"
    "v_and_b32_e32 v217, s61, v217\n"
    "s_waitcnt lgkmcnt(15)\n"
    "v_mfma_f32_16x16x16_bf16 a[236:239], v[140:141], v[212:213], 0\n"
    "s_add_u32 m0, s46, 98304\n"
    "v_mfma_f32_16x16x16_bf16 v[0:3], v[142:143], v[212:213], v[136:139]\n"
    "s_add_u32 s55, s55, 0x8000\n"
    "v_mfma_f32_16x16x16_bf16 v[20:23], v[144:145], v[212:213], v[136:139]\n"
    "buffer_load_dwordx4 v250, s[8:11], s55 offen offset:0 sc1 lds\n"
    "v_mfma_f32_16x16x16_bf16 v[40:43], v[146:147], v[212:213], v[136:139]\n"
    "buffer_load_dwordx4 v250, s[8:11], s55 offen offset:1024 sc1 lds\n"
    "v_mfma_f32_16x16x16_bf16 v[60:63], v[148:149], v[212:213], v[136:139]\n"
    "s_add_u32 m0, s46, 40960\n"
    "v_mfma_f32_16x16x16_bf16 v[80:83], v[150:151], v[212:213], v[136:139]\n"
    "buffer_load_dwordx4 v250, s[8:11], s60 offen offset:0 sc1 lds\n"
    "v_mfma_f32_16x16x16_bf16 v[100:103], v[152:153], v[212:213], v[136:139]\n"
    "buffer_load_dwordx4 v250, s[8:11], s60 offen offset:1024 sc1 lds\n"
    "v_mfma_f32_16x16x16_bf16 v[120:123], v[154:155], v[212:213], v[136:139]\n"
    "s_add_u32 m0, s46, 106496\n"
    "v_mfma_f32_16x16x16_bf16 a[240:243], v[140:141], v[216:217], 0\n"
    "s_add_u32 s60, s60, 0x8000\n"
    "v_mfma_f32_16x16x16_bf16 v[4:7], v[142:143], v[216:217], v[136:139]\n"
    "buffer_load_dwordx4 v250, s[8:11], s60 offen offset:0 sc1 lds\n"
    "v_mfma_f32_16x16x16_bf16 v[24:27], v[144:145], v[216:217], v[136:139]\n"
    "buffer_load_dwordx4 v250, s[8:11], s60 offen offset:1024 sc1 lds\n"
    "v_mfma_f32_16x16x16_bf16 v[44:47], v[146:147], v[216:217], v[136:139]\n"
    "v_mfma_f32_16x16x16_bf16 v[64:67], v[148:149], v[216:217], v[136:139]\n"
    "buffer_load_dword v253, v250, s[8:11], s59 offen offset:0 sc1\n"
    "v_mfma_f32_16x16x16_bf16 v[84:87], v[150:151], v[216:217], v[136:139]\n"
    "v_mfma_f32_16x16x16_bf16 v[104:107], v[152:153], v[216:217], v[136:139]\n"
    "v_mfma_f32_16x16x16_bf16 v[124:127], v[154:155], v[216:217], v[136:139]\n"
    "s_waitcnt lgkmcnt(0)\n"
    "buffer_load_dword v254, v252, s[8:11], s59 offen offset:0 sc1\n"
    "v_mfma_f32_16x16x32_bf16 v[140:143], a[160:163], a[0:3], v[220:223]\n"
    "v_mfma_f32_16x16x32_bf16 v[140:143], a[164:167], a[4:7], v[140:143]\n"
    "v_mfma_f32_16x16x32_bf16 v[140:143], a[168:171], a[8:11], v[140:143]\n"
    "v_mfma_f32_16x16x32_bf16 v[140:143], a[172:175], a[12:15], v[140:143]\n"
    "v_mfma_f32_16x16x32_bf16 v[140:143], a[176:179], a[16:19], v[140:143]\n"
    "v_mfma_f32_16x16x32_bf16 v[140:143], a[180:183], a[20:23], v[140:143]\n"
    "v_mfma_f32_16x16x32_bf16 v[140:143], a[184:187], a[24:27], v[140:143]\n"
    "v_mfma_f32_16x16x32_bf16 v[140:143], a[188:191], a[28:31], v[140:143]\n"
    "v_mfma_f32_16x16x32_bf16 v[144:147], v[180:183], a[0:3], v[220:223]\n"
    "v_mfma_f32_16x16x32_bf16 v[144:147], v[184:187], a[4:7], v[144:147]\n"
    "s_waitcnt vmcnt(10)\n"
    "s_barrier\n"
    "v_mfma_f32_16x16x32_bf16 v[144:147], v[188:191], a[8:11], v[144:147]\n"
    "s_mov_b32 m0, s42\n"
    "ds_read_b128 a[32:35], v255 offset:0\n"
    "v_exp_f32_e32 v140, v140\n"
    "v_mfma_f32_16x16x32_bf16 v[144:147], v[192:195], a[12:15], v[144:147]\n"
    "ds_read_b128 a[36:39], v255 offset:1024\n"
    "v_exp_f32_e32 v141, v141\n"
    "v_mfma_f32_16x16x32_bf16 v[144:147], v[196:199], a[16:19], v[144:147]\n"
    "ds_read_b128 a[40:43], v255 offset:2048\n"
    "ds_read_b128 a[44:47], v255 offset:3072\n"
    "v_mfma_f32_16x16x32_bf16 v[144:147], v[200:203], a[20:23], v[144:147]\n"
    "ds_read_b128 a[48:51], v255 offset:4096\n"
    "ds_read_b128 a[52:55], v255 offset:5120\n"
    "v_mfma_f32_16x16x32_bf16 v[144:147], v[204:207], a[24:27], v[144:147]\n"
    "ds_read_b128 a[56:59], v255 offset:6144\n"
    "ds_read_b128 a[60:63], v255 offset:7168\n"
    "v_mfma_f32_16x16x32_bf16 v[144:147], v[208:211], a[28:31], v[144:147]\n"
    "s_waitcnt lgkmcnt(4)\n"
    "v_exp_f32_e32 v142, v142\n"
    "v_mfma_f32_16x16x32_bf16 v[148:151], a[160:163], a[32:35], v[224:227]\n"
    "ds_read_b128 a[64:67], v255 offset:8192\n"
    "v_exp_f32_e32 v143, v143\n"
    "v_mfma_f32_16x16x32_bf16 v[148:151], a[164:167], a[36:39], v[148:151]\n"
    "ds_read_b128 a[68:71], v255 offset:9216\n"
    "v_cvt_pk_bf16_f32 v140, v140, v141\n"
    "v_mfma_f32_16x16x32_bf16 v[148:151], a[168:171], a[40:43], v[148:151]\n"
    "ds_read_b128 a[72:75], v255 offset:10240\n"
    "v_cvt_pk_bf16_f32 v141, v142, v143\n"
    "v_mfma_f32_16x16x32_bf16 v[148:151], a[172:175], a[44:47], v[148:151]\n"
    "s_waitcnt lgkmcnt(3)\n"
    "v_exp_f32_e32 v144, v144\n"
    "v_mfma_f32_16x16x32_bf16 v[148:151], a[176:179], a[48:51], v[148:151]\n"
    "ds_read_b128 a[76:79], v255 offset:11264\n"
    "ds_write_addtid_b32 v140 offset:0\n"
    "v_mfma_f32_16x16x32_bf16 v[148:151], a[180:183], a[52:55], v[148:151]\n"
    "ds_read_b128 a[80:83], v255 offset:12288\n"
    "v_exp_f32_e32 v145, v145\n"
    "v_mfma_f32_16x16x32_bf16 v[148:151], a[184:187], a[56:59], v[148:151]\n"
    "v_exp_f32_e32 v146, v146\n"
    "v_mfma_f32_16x16x32_bf16 v[148:151], a[188:191], a[60:63], v[148:151]\n"
    "ds_read_b128 a[84:87], v255 offset:13312\n"
    "v_exp_f32_e32 v147, v147\n"
    "v_mfma_f32_16x16x32_bf16 v[152:155], v[180:183], a[32:35], v[224:227]\n"
    "ds_read_b128 a[88:91], v255 offset:14336\n"
    "v_cvt_pk_bf16_f32 v142, v144, v145\n"
    "v_mfma_f32_16x16x32_bf16 v[152:155], v[184:187], a[36:39], v[152:155]\n"
    "ds_read_b128 a[92:95], v255 offset:15360\n"
    "v_cvt_pk_bf16_f32 v143, v146, v147\n"
    "v_mfma_f32_16x16x32_bf16 v[152:155], v[188:191], a[40:43], v[152:155]\n"
    "v_exp_f32_e32 v148, v148\n"
    "v_mfma_f32_16x16x32_bf16 v[152:155], v[192:195], a[44:47], v[152:155]\n"
    "v_exp_f32_e32 v149, v149\n"
    "v_mfma_f32_16x16x32_bf16 v[152:155], v[196:199], a[48:51], v[152:155]\n"
    "v_exp_f32_e32 v150, v150\n"
    "v_mfma_f32_16x16x32_bf16 v[152:155], v[200:203], a[52:55], v[152:155]\n"
    "v_exp_f32_e32 v151, v151\n"
    "v_mfma_f32_16x16x32_bf16 v[152:155], v[204:207], a[56:59], v[152:155]\n"
    "s_waitcnt vmcnt(2)\n"
    "s_barrier\n"
    "v_mfma_f32_16x16x32_bf16 v[152:155], v[208:211], a[60:63], v[152:155]\n"
    "s_waitcnt lgkmcnt(5)\n"
    "ds_read_b128 a[96:99], v255 offset:32768\n"
    "ds_write_addtid_b32 v141 offset:256\n"
    "v_mfma_f32_16x16x32_bf16 v[156:159], a[160:163], a[64:67], v[228:231]\n"
    "ds_read_b128 a[100:103], v255 offset:33792\n"
    "ds_write_addtid_b32 v142 offset:512\n"
    "v_mfma_f32_16x16x32_bf16 v[156:159], a[164:167], a[68:71], v[156:159]\n"
    "ds_read_b128 a[104:107], v255 offset:34816\n"
    "v_mfma_f32_16x16x32_bf16 v[156:159], a[168:171], a[72:75], v[156:159]\n"
    "ds_read_b128 a[108:111], v255 offset:35840\n"
    "v_mfma_f32_16x16x32_bf16 v[156:159], a[172:175], a[76:79], v[156:159]\n"
    "s_waitcnt lgkmcnt(6)\n"
    "ds_read_b128 a[112:115], v255 offset:36864\n"
    "v_cvt_pk_bf16_f32 v148, v148, v149\n"
    "v_mfma_f32_16x16x32_bf16 v[156:159], a[176:179], a[80:83], v[156:159]\n"
    "ds_read_b128 a[116:119], v255 offset:37888\n"
    "v_cvt_pk_bf16_f32 v149, v150, v151\n"
    "v_mfma_f32_16x16x32_bf16 v[156:159], a[180:183], a[84:87], v[156:159]\n"
    "ds_read_b128 a[120:123], v255 offset:38912\n"
    "v_exp_f32_e32 v152, v152\n"
    "v_mfma_f32_16x16x32_bf16 v[156:159], a[184:187], a[88:91], v[156:159]\n"
    "ds_read_b128 a[124:127], v255 offset:39936\n"
    "v_exp_f32_e32 v153, v153\n"
    "v_mfma_f32_16x16x32_bf16 v[156:159], a[188:191], a[92:95], v[156:159]\n"
    "ds_read_b128 a[128:131], v255 offset:40960\n"
    "v_exp_f32_e32 v154, v154\n"
    "v_mfma_f32_16x16x32_bf16 v[160:163], v[180:183], a[64:67], v[228:231]\n"
    "ds_read_b128 a[132:135], v255 offset:41984\n"
    "v_exp_f32_e32 v155, v155\n"
    "v_mfma_f32_16x16x32_bf16 v[160:163], v[184:187], a[68:71], v[160:163]\n"
    "ds_read_b128 a[136:139], v255 offset:43008\n"
    "v_cvt_pk_bf16_f32 v150, v152, v153\n"
    "v_mfma_f32_16x16x32_bf16 v[160:163], v[188:191], a[72:75], v[160:163]\n"
    "ds_read_b128 a[140:143], v255 offset:44032\n"
    "v_cvt_pk_bf16_f32 v151, v154, v155\n"
    "v_mfma_f32_16x16x32_bf16 v[160:163], v[192:195], a[76:79], v[160:163]\n"
    "ds_read_b128 a[144:147], v255 offset:45056\n"
    "ds_read_b128 a[148:151], v255 offset:46080\n"
    "v_mfma_f32_16x16x32_bf16 v[160:163], v[196:199], a[80:83], v[160:163]\n"
    "ds_read_b128 a[152:155], v255 offset:47104\n"
    "ds_read_b128 a[156:159], v255 offset:48128\n"
    "v_mfma_f32_16x16x32_bf16 v[160:163], v[200:203], a[84:87], v[160:163]\n"
    "s_waitcnt vmcnt(0)\n"
    "ds_write_addtid_b32 v148 offset:1024\n"
    "v_xor_b32_e32 v254, s39, v254\n"
    "v_xor_b32_e32 v253, s39, v253\n"
    "v_mfma_f32_16x16x32_bf16 v[160:163], v[204:207], a[88:91], v[160:163]\n"
    "ds_write_addtid_b32 v149 offset:1280\n"
    "v_or_b32_e32 v253, v253, v254\n"
    "v_cmp_ne_u32_e32 vcc, 0, v253\n"
    "v_mfma_f32_16x16x32_bf16 v[160:163], v[208:211], a[92:95], v[160:163]\n"
    "s_waitcnt lgkmcnt(14)\n"
    "v_exp_f32_e32 v156, v156\n"
    "v_mfma_f32_16x16x32_bf16 v[164:167], a[160:163], a[96:99], v[232:235]\n"
    "v_exp_f32_e32 v157, v157\n"
    "v_mfma_f32_16x16x32_bf16 v[164:167], a[164:167], a[100:103], v[164:167]\n"
    "v_exp_f32_e32 v158, v158\n"
    "v_mfma_f32_16x16x32_bf16 v[164:167], a[168:171], a[104:107], v[164:167]\n"
    "s_cbranch_vccnz .Lat_hpoll\n"
    ".Lat_hpoll_ret:\n"
    "s_waitcnt lgkmcnt(2)\n"
    "s_waitcnt vmcnt(17)\n"
    "s_barrier\n"
    "v_mfma_f32_16x16x32_bf16 v[164:167], a[172:175], a[108:111], v[164:167]\n"
    "v_exp_f32_e32 v159, v159\n"
    "v_mfma_f32_16x16x32_bf16 v[164:167], a[176:179], a[112:115], v[164:167]\n"
    "s_add_u32 m0, s43, 16384\n"
    "v_cvt_pk_bf16_f32 v156, v156, v157\n"
    "v_mfma_f32_16x16x32_bf16 v[164:167], a[180:183], a[116:119], v[164:167]\n"
    "buffer_load_dwordx4 v248, s[8:11], s40 offen offset:0 sc1 lds\n"
    "v_cvt_pk_bf16_f32 v157, v158, v159\n"
    "v_mfma_f32_16x16x32_bf16 v[164:167], a[184:187], a[120:123], v[164:167]\n"
    "v_exp_f32_e32 v160, v160\n"
    "v_mfma_f32_16x16x32_bf16 v[164:167], a[188:191], a[124:127], v[164:167]\n"
    "v_exp_f32_e32 v161, v161\n"
    "v_mfma_f32_16x16x32_bf16 v[168:171], v[180:183], a[96:99], v[232:235]\n"
    "buffer_load_dwordx4 v248, s[8:11], s40 offen offset:1024 sc1 lds\n"
    "v_mfma_f32_16x16x32_bf16 v[168:171], v[184:187], a[100:103], v[168:171]\n"
    "v_exp_f32_e32 v162, v162\n"
    "v_mfma_f32_16x16x32_bf16 v[168:171], v[188:191], a[104:107], v[168:171]\n"
    "v_exp_f32_e32 v163, v163\n"
    "v_mfma_f32_16x16x32_bf16 v[168:171], v[192:195], a[108:111], v[168:171]\n"
    "buffer_load_dwordx4 v248, s[8:11], s40 offen offset:2048 sc1 lds\n"
    "v_mfma_f32_16x16x32_bf16 v[168:171], v[196:199], a[112:115], v[168:171]\n"
    "v_cvt_pk_bf16_f32 v158, v160, v161\n"
    "v_cvt_pk_bf16_f32 v159, v162, v163\n"
    "v_mfma_f32_16x16x32_bf16 v[168:171], v[200:203], a[116:119], v[168:171]\n"
    "v_exp_f32_e32 v164, v164\n"
    "v_mfma_f32_16x16x32_bf16 v[168:171], v[204:207], a[120:123], v[168:171]\n"
    "buffer_load_dwordx4 v248, s[8:11], s40 offen offset:3072 sc1 lds\n"
    "v_mfma_f32_16x16x32_bf16 v[168:171], v[208:211], a[124:127], v[168:171]\n"
    "s_add_u32 s40, s40, 0x8000\n"
    "v_exp_f32_e32 v165, v165\n"
    "v_mfma_f32_16x16x32_bf16 v[172:175], a[160:163], a[128:131], v[236:239]\n"
    "s_add_u32 m0, s43, 20480\n"
    "v_exp_f32_e32 v166, v166\n"
    "v_mfma_f32_16x16x32_bf16 v[172:175], a[164:167], a[132:135], v[172:175]\n"
    "buffer_load_dwordx4 v248, s[8:11], s41 offen offset:0 sc1 lds\n"
    "ds_read_b128 a[160:163], v240 offset:8192\n"
    "v_mfma_f32_16x16x32_bf16 v[172:175], a[168:171], a[136:139], v[172:175]\n"
    "ds_read_b128 a[164:167], v241 offset:8192\n"
    "v_mfma_f32_16x16x32_bf16 v[172:175], a[172:175], a[140:143], v[172:175]\n"
    "ds_read_b128 a[168:171], v240 offset:12288\n"
    "v_mfma_f32_16x16x32_bf16 v[172:175], a[176:179], a[144:147], v[172:175]\n"
    "buffer_load_dwordx4 v248, s[8:11], s41 offen offset:1024 sc1 lds\n"
    "ds_read_b128 a[172:175], v241 offset:12288\n"
    "v_mfma_f32_16x16x32_bf16 v[172:175], a[180:183], a[148:151], v[172:175]\n"
    "ds_read_b128 a[176:179], v242 offset:8192\n"
    "v_exp_f32_e32 v167, v167\n"
    "v_mfma_f32_16x16x32_bf16 v[172:175], a[184:187], a[152:155], v[172:175]\n"
    "ds_read_b128 a[180:183], v243 offset:8192\n"
    "v_cvt_pk_bf16_f32 v164, v164, v165\n"
    "v_mfma_f32_16x16x32_bf16 v[172:175], a[188:191], a[156:159], v[172:175]\n"
    "buffer_load_dwordx4 v248, s[8:11], s41 offen offset:2048 sc1 lds\n"
    "v_cvt_pk_bf16_f32 v165, v166, v167\n"
    "v_mfma_f32_16x16x32_bf16 v[176:179], v[180:183], a[128:131], v[236:239]\n"
    "v_exp_f32_e32 v168, v168\n"
    "v_mfma_f32_16x16x32_bf16 v[176:179], v[184:187], a[132:135], v[176:179]\n"
    "v_exp_f32_e32 v169, v169\n"
    "v_mfma_f32_16x16x32_bf16 v[176:179], v[188:191], a[136:139], v[176:179]\n"
    "buffer_load_dwordx4 v248, s[8:11], s41 offen offset:3072 sc1 lds\n"
    "v_mfma_f32_16x16x32_bf16 v[176:179], v[192:195], a[140:143], v[176:179]\n"
    "s_mov_b32 m0, s42\n"
    "s_add_u32 s41, s41, 0x8000\n"
    "v_exp_f32_e32 v172, v172\n"
    "v_mfma_f32_16x16x32_bf16 v[176:179], v[196:199], a[144:147], v[176:179]\n"
    "ds_write_addtid_b32 v156 offset:2048\n"
    "v_exp_f32_e32 v173, v173\n"
    "v_mfma_f32_16x16x32_bf16 v[176:179], v[200:203], a[148:151], v[176:179]\n"
    "ds_write_addtid_b32 v157 offset:2304\n"
    "v_exp_f32_e32 v174, v174\n"
    "v_mfma_f32_16x16x32_bf16 v[176:179], v[204:207], a[152:155], v[176:179]\n"
    "ds_write_addtid_b32 v164 offset:3072\n"
    "v_exp_f32_e32 v175, v175\n"
    "v_mfma_f32_16x16x32_bf16 v[176:179], v[208:211], a[156:159], v[176:179]\n"
    "ds_write_addtid_b32 v165 offset:3328\n"
    "v_cvt_pk_bf16_f32 v172, v172, v173\n"
    "v_cvt_pk_bf16_f32 v173, v174, v175\n"
    "ds_write_addtid_b32 v172 offset:4096\n"
    "ds_write_addtid_b32 v173 offset:4352\n"
    "s_mul_i32 s45, s33, 320\n"
    "s_lshl_b32 s46, s93, 7\n"
    "s_add_u32 s45, s45, s46\n"
    "buffer_load_dword v254, off, s[24:27], s45\n"
    "s_andn2_b32 s45, s34, 3\n"
    "s_add_u32 s45, s45, s93\n"
    "s_mul_i32 s45, s45, 101376\n"
    "s_add_u32 s45, s45, 16588800\n"
    "buffer_load_dword v254, off, s[8:11], s45\n"
    "s_waitcnt lgkmcnt(8)\n"
    "s_branch .Lat_in\n"
    ".fill 245, 4, 0xbf800000\n"
    ".long 0xbedf00ff\n"
    ".Lat_w2:\n"
    "s_setpc_b64 s[2:3]\n"
    ".Lat_loop:\n"
    "v_mfma_f32_16x16x32_bf16 v[212:215], a[160:163], a[128:131], v[236:239]\n"
    "v_mfma_f32_16x16x32_bf16 v[212:215], a[164:167], a[132:135], v[212:215]\n"
    "v_exp_f32_e32 v170, v170\n"
    "v_mfma_f32_16x16x32_bf16 v[212:215], a[168:171], a[136:139], v[212:215]\n"
    "ds_write_addtid_b32 v143 offset:768\n"
    "v_mfma_f32_16x16x32_bf16 v[212:215], a[172:175], a[140:143], v[212:215]\n"
    "ds_read_b128 a[184:187], v242 offset:28672\n"
    "v_mfma_f32_16x16x32_bf16 v[204:207], a[172:175], a[108:111], v[232:235]\n"
    "v_exp_f32_e32 v171, v171\n"
    "v_mfma_f32_16x16x32_bf16 v[204:207], a[168:171], a[104:107], v[204:207]\n"
    "ds_write_addtid_b32 v150 offset:1536\n"
    "v_mfma_f32_16x16x32_bf16 v[204:207], a[164:167], a[100:103], v[204:207]\n"
    "v_cvt_pk_bf16_f32 v166, v168, v169\n"
    "v_mfma_f32_16x16x32_bf16 v[204:207], a[160:163], a[96:99], v[204:207]\n"
    "ds_write_addtid_b32 v151 offset:1792\n"
    "v_mfma_f32_16x16x32_bf16 v[196:199], a[160:163], a[64:67], v[228:231]\n"
    "v_exp_f32_e32 v176, v176\n"
    "v_mfma_f32_16x16x32_bf16 v[196:199], a[164:167], a[68:71], v[196:199]\n"
    "ds_read_b128 a[188:191], v243 offset:28672\n"
    "v_mfma_f32_16x16x32_bf16 v[196:199], a[168:171], a[72:75], v[196:199]\n"
    "ds_write_addtid_b32 v158 offset:2560\n"
    "v_mfma_f32_16x16x32_bf16 v[196:199], a[172:175], a[76:79], v[196:199]\n"
    "v_exp_f32_e32 v177, v177\n"
    "v_mfma_f32_16x16x32_bf16 v[188:191], a[172:175], a[44:47], v[224:227]\n"
    "v_mfma_f32_16x16x32_bf16 v[188:191], a[168:171], a[40:43], v[188:191]\n"
    "ds_write_addtid_b32 v159 offset:2816\n"
    "v_mfma_f32_16x16x32_bf16 v[188:191], a[164:167], a[36:39], v[188:191]\n"
    "v_cvt_pk_bf16_f32 v167, v170, v171\n"
    "v_mfma_f32_16x16x32_bf16 v[188:191], a[160:163], a[32:35], v[188:191]\n"
    "v_mfma_f32_16x16x32_bf16 v[180:183], a[160:163], a[0:3], v[220:223]\n"
    "v_exp_f32_e32 v178, v178\n"
    "v_mfma_f32_16x16x32_bf16 v[180:183], a[164:167], a[4:7], v[180:183]\n"
    "ds_write_addtid_b32 v166 offset:3584\n"
    "v_mfma_f32_16x16x32_bf16 v[180:183], a[168:171], a[8:11], v[180:183]\n"
    "ds_write_addtid_b32 v167 offset:3840\n"
    "v_mfma_f32_16x16x32_bf16 v[180:183], a[172:175], a[12:15], v[180:183]\n"
    "s_waitcnt lgkmcnt(4)\n"
    "v_exp_f32_e32 v179, v179\n"
    "v_mfma_f32_16x16x32_bf16 v[180:183], a[176:179], a[16:19], v[180:183]\n"
    "ds_read_b128 a[160:163], v240 offset:26624\n"
    "v_mfma_f32_16x16x32_bf16 v[180:183], a[180:183], a[20:23], v[180:183]\n"
    "v_cvt_pk_bf16_f32 v174, v176, v177\n"
    "v_mfma_f32_16x16x32_bf16 v[180:183], a[184:187], a[24:27], v[180:183]\n"
    "ds_read_b128 a[168:171], v240 offset:30720\n"
    "v_mfma_f32_16x16x32_bf16 v[180:183], a[188:191], a[28:31], v[180:183]\n"
    "ds_write_addtid_b32 v174 offset:4608\n"
    "v_mfma_f32_16x16x32_bf16 v[188:191], a[188:191], a[60:63], v[188:191]\n"
    "v_cvt_pk_bf16_f32 v175, v178, v179\n"
    "v_mfma_f32_16x16x32_bf16 v[188:191], a[184:187], a[56:59], v[188:191]\n"
    "ds_read_b128 a[164:167], v241 offset:26624\n"
    "v_mfma_f32_16x16x32_bf16 v[188:191], a[180:183], a[52:55], v[188:191]\n"
    "ds_write_addtid_b32 v175 offset:4864\n"
    "v_mfma_f32_16x16x32_bf16 v[188:191], a[176:179], a[48:51], v[188:191]\n"
    "ds_read_b128 a[172:175], v241 offset:30720\n"
    "v_mfma_f32_16x16x32_bf16 v[196:199], a[176:179], a[80:83], v[196:199]\n"
    "v_mfma_f32_16x16x32_bf16 v[196:199], a[180:183], a[84:87], v[196:199]\n"
    "s_waitcnt lgkmcnt(1)\n"
    "v_mfma_f32_16x16x32_bf16 v[196:199], a[184:187], a[88:91], v[196:199]\n"
    "s_barrier\n"
    "v_mfma_f32_16x16x32_bf16 v[196:199], a[188:191], a[92:95], v[196:199]\n"
    "s_add_u32 m0, s43, 8192\n"
    "v_mfma_f32_16x16x32_bf16 v[204:207], a[188:191], a[124:127], v[204:207]\n"
    "ds_read2st64_b32 v[144:145], v249 offset0:0 offset1:1\n"
    "v_mfma_f32_16x16x32_bf16 v[204:207], a[184:187], a[120:123], v[204:207]\n"
    "buffer_load_dwordx4 v248, s[8:11], s40 offen offset:0 sc1 lds\n"
    "v_mfma_f32_16x16x32_bf16 v[204:207], a[180:183], a[116:119], v[204:207]\n"
    "v_mfma_f32_16x16x32_bf16 v[204:207], a[176:179], a[112:115], v[204:207]\n"
    "v_mfma_f32_16x16x32_bf16 v[212:215], a[176:179], a[144:147], v[212:215]\n"
    "ds_read2st64_b32 v[146:147], v249 offset0:2 offset1:3\n"
    "v_mfma_f32_16x16x32_bf16 v[212:215], a[180:183], a[148:151], v[212:215]\n"
    "buffer_load_dwordx4 v248, s[8:11], s40 offen offset:1024 sc1 lds\n"
    "v_mfma_f32_16x16x32_bf16 v[212:215], a[184:187], a[152:155], v[212:215]\n"
    "v_mfma_f32_16x16x32_bf16 v[212:215], a[188:191], a[156:159], v[212:215]\n"
    "s_waitcnt lgkmcnt(2)\n"
    "ds_read2st64_b32 v[152:153], v249 offset0:4 offset1:5\n"
    "v_mfma_f32_16x16x32_bf16 v[216:219], a[160:163], a[128:131], v[236:239]\n"
    "ds_read_b128 a[176:179], v242 offset:26624\n"
    "v_mfma_f32_16x16x32_bf16 v[216:219], a[164:167], a[132:135], v[216:219]\n"
    "buffer_load_dwordx4 v248, s[8:11], s40 offen offset:2048 sc1 lds\n"
    "v_mfma_f32_16x16x32_bf16 v[216:219], a[168:171], a[136:139], v[216:219]\n"
    "ds_read_b128 a[184:187], v242 offset:30720\n"
    "v_mfma_f32_16x16x32_bf16 v[216:219], a[172:175], a[140:143], v[216:219]\n"
    "v_exp_f32_e32 v180, v180\n"
    "v_mfma_f32_16x16x32_bf16 v[208:211], a[172:175], a[108:111], v[232:235]\n"
    "ds_read2st64_b32 v[154:155], v249 offset0:6 offset1:7\n"
    "v_mfma_f32_16x16x32_bf16 v[208:211], a[168:171], a[104:107], v[208:211]\n"
    "buffer_load_dwordx4 v248, s[8:11], s40 offen offset:3072 sc1 lds\n"
    "v_mfma_f32_16x16x32_bf16 v[208:211], a[164:167], a[100:103], v[208:211]\n"
    "s_add_u32 m0, s43, 12288\n"
    "ds_read_b128 a[180:183], v243 offset:26624\n"
    "v_mfma_f32_16x16x32_bf16 v[208:211], a[160:163], a[96:99], v[208:211]\n"
    "s_add_u32 s40, s40, 0x8000\n"
    "ds_read_b128 a[188:191], v243 offset:30720\n"
    "v_mfma_f32_16x16x32_bf16 v[200:203], a[160:163], a[64:67], v[228:231]\n"
    ".long 0xbedf00ff\n"
    ".Lat_w3:\n"
    "s_setpc_b64 s[2:3]\n"
    "ds_read2st64_b32 v[160:161], v249 offset0:8 offset1:9\n"
    "v_mfma_f32_16x16x32_bf16 v[200:203], a[164:167], a[68:71], v[200:203]\n"
    "buffer_load_dwordx4 v248, s[8:11], s41 offen offset:0 sc1 lds\n"
    "v_mfma_f32_16x16x32_bf16 v[200:203], a[168:171], a[72:75], v[200:203]\n"
    "v_exp_f32_e32 v181, v181\n"
    "v_mfma_f32_16x16x32_bf16 v[200:203], a[172:175], a[76:79], v[200:203]\n"
    "v_exp_f32_e32 v182, v182\n"
    "v_mfma_f32_16x16x32_bf16 v[192:195], a[172:175], a[44:47], v[224:227]\n"
    "ds_read2st64_b32 v[162:163], v249 offset0:10 offset1:11\n"
    "v_mfma_f32_16x16x32_bf16 v[192:195], a[168:171], a[40:43], v[192:195]\n"
    "buffer_load_dwordx4 v248, s[8:11], s41 offen offset:1024 sc1 lds\n"
    "v_mfma_f32_16x16x32_bf16 v[192:195], a[164:167], a[36:39], v[192:195]\n"
    "v_exp_f32_e32 v183, v183\n"
    "v_mfma_f32_16x16x32_bf16 v[192:195], a[160:163], a[32:35], v[192:195]\n"
    "v_cvt_pk_bf16_f32 v180, v180, v181\n"
    "v_mfma_f32_16x16x32_bf16 v[184:187], a[160:163], a[0:3], v[220:223]\n"
    "ds_read2st64_b32 v[168:169], v249 offset0:12 offset1:13\n"
    "v_mfma_f32_16x16x32_bf16 v[184:187], a[164:167], a[4:7], v[184:187]\n"
    "buffer_load_dwordx4 v248, s[8:11], s41 offen offset:2048 sc1 lds\n"
    "v_mfma_f32_16x16x32_bf16 v[184:187], a[168:171], a[8:11], v[184:187]\n"
    "ds_read_b64_tr_b16 a[160:161], v244 offset:16384\n"
    "v_mfma_f32_16x16x32_bf16 v[184:187], a[172:175], a[12:15], v[184:187]\n"
    "s_waitcnt lgkmcnt(4)\n"
    "ds_read2st64_b32 v[170:171], v249 offset0:14 offset1:15\n"
    "v_mfma_f32_16x16x32_bf16 v[184:187], a[176:179], a[16:19], v[184:187]\n"
    "ds_read_b64_tr_b16 a[168:169], v245 offset:16384\n"
    "v_mfma_f32_16x16x32_bf16 v[184:187], a[180:183], a[20:23], v[184:187]\n"
    "buffer_load_dwordx4 v248, s[8:11], s41 offen offset:3072 sc1 lds\n"
    "v_mfma_f32_16x16x32_bf16 v[184:187], a[184:187], a[24:27], v[184:187]\n"
    "ds_read_b64_tr_b16 a[162:163], v244 offset:18432\n"
    "v_mfma_f32_16x16x32_bf16 v[184:187], a[188:191], a[28:31], v[184:187]\n"
    "s_add_u32 s41, s41, 0x8000\n"
    "ds_read2st64_b32 v[176:177], v249 offset0:16 offset1:17\n"
    "v_mfma_f32_16x16x32_bf16 v[192:195], a[188:191], a[60:63], v[192:195]\n"
    "ds_read_b64_tr_b16 a[164:165], v244 offset:49152\n"
    "v_mfma_f32_16x16x32_bf16 v[192:195], a[184:187], a[56:59], v[192:195]\n"
    "ds_read_b64_tr_b16 a[170:171], v245 offset:18432\n"
    "v_mfma_f32_16x16x32_bf16 v[192:195], a[180:183], a[52:55], v[192:195]\n"
    "v_exp_f32_e32 v188, v188\n"
    "v_mfma_f32_16x16x32_bf16 v[192:195], a[176:179], a[48:51], v[192:195]\n"
    "ds_read_b64_tr_b16 a[166:167], v244 offset:51200\n"
    "v_mfma_f32_16x16x32_bf16 v[200:203], a[176:179], a[80:83], v[200:203]\n"
    "ds_read_b64_tr_b16 a[172:173], v245 offset:49152\n"
    "v_mfma_f32_16x16x32_bf16 v[200:203], a[180:183], a[84:87], v[200:203]\n"
    "ds_read2st64_b32 v[178:179], v249 offset0:18 offset1:19\n"
    "v_mfma_f32_16x16x32_bf16 v[200:203], a[184:187], a[88:91], v[200:203]\n"
    "v_exp_f32_e32 v189, v189\n"
    "v_mfma_f32_16x16x32_bf16 v[200:203], a[188:191], a[92:95], v[200:203]\n"
    "ds_read_b64_tr_b16 a[174:175], v245 offset:51200\n"
    "v_cvt_pk_bf16_f32 v181, v182, v183\n"
    "v_mfma_f32_16x16x32_bf16 v[208:211], a[188:191], a[124:127], v[208:211]\n"
    "v_exp_f32_e32 v190, v190\n"
    "v_mfma_f32_16x16x32_bf16 v[208:211], a[184:187], a[120:123], v[208:211]\n"
    "v_exp_f32_e32 v191, v191\n"
    "v_mfma_f32_16x16x32_bf16 v[208:211], a[180:183], a[116:119], v[208:211]\n"
    "v_mfma_f32_16x16x32_bf16 v[208:211], a[176:179], a[112:115], v[208:211]\n"
    "v_cvt_pk_bf16_f32 v188, v188, v189\n"
    "v_mfma_f32_16x16x32_bf16 v[216:219], a[176:179], a[144:147], v[216:219]\n"
    "v_exp_f32_e32 v196, v196\n"
    "v_mfma_f32_16x16x32_bf16 v[216:219], a[180:183], a[148:151], v[216:219]\n"
    "v_exp_f32_e32 v197, v197\n"
    "v_mfma_f32_16x16x32_bf16 v[216:219], a[184:187], a[152:155], v[216:219]\n"
    "ds_read_b64_tr_b16 a[176:177], v246 offset:16384\n"
    "v_mfma_f32_16x16x32_bf16 v[216:219], a[188:191], a[156:159], v[216:219]\n"
    "s_waitcnt lgkmcnt(2)\n"
    "v_cvt_pk_bf16_f32 v189, v190, v191\n"
    "v_mfma_f32_16x16x32_bf16 a[236:239], a[160:163], v[140:143], a[236:239]\n"
    "v_mfma_f32_16x16x32_bf16 a[236:239], a[164:167], v[144:147], a[236:239]\n"
    "ds_read_b64_tr_b16 a[178:179], v246 offset:18432\n"
    "v_mfma_f32_16x16x32_bf16 a[240:243], a[164:167], v[152:155], a[240:243]\n"
    "ds_read_b64_tr_b16 a[184:185], v247 offset:16384\n"
    "v_mfma_f32_16x16x32_bf16 a[240:243], a[160:163], v[148:151], a[240:243]\n"
    "v_exp_f32_e32 v198, v198\n"
    "v_mfma_f32_16x16x32_bf16 a[244:247], a[160:163], v[156:159], a[244:247]\n"
    "ds_read_b64_tr_b16 a[180:181], v246 offset:49152\n"
    "v_mfma_f32_16x16x32_bf16 a[244:247], a[164:167], v[160:163], a[244:247]\n"
    "ds_read_b64_tr_b16 a[186:187], v247 offset:18432\n"
    "v_mfma_f32_16x16x32_bf16 a[248:251], a[164:167], v[168:171], a[248:251]\n"
    "v_exp_f32_e32 v199, v199\n"
    "v_mfma_f32_16x16x32_bf16 a[248:251], a[160:163], v[164:167], a[248:251]\n"
    "ds_read_b64_tr_b16 a[182:183], v246 offset:51200\n"
    "v_mfma_f32_16x16x32_bf16 a[252:255], a[160:163], v[172:175], a[252:255]\n"
    "ds_read_b64_tr_b16 a[188:189], v247 offset:49152\n"
    "v_mfma_f32_16x16x32_bf16 a[252:255], a[164:167], v[176:179], a[252:255]\n"
    "s_waitcnt lgkmcnt(7)\n"
    "v_cvt_pk_bf16_f32 v196, v196, v197\n"
    "v_mfma_f32_16x16x32_bf16 v[16:19], a[172:175], v[176:179], v[16:19]\n"
    "ds_read_b64_tr_b16 a[160:161], v244 offset:20480\n"
    "v_mfma_f32_16x16x32_bf16 v[16:19], a[168:171], v[172:175], v[16:19]\n"
    "ds_read_b64_tr_b16 a[190:191], v247 offset:51200\n"
    "v_mfma_f32_16x16x32_bf16 v[12:15], a[168:171], v[164:167], v[12:15]\n"
    "v_exp_f32_e32 v204, v204\n"
    "v_mfma_f32_16x16x32_bf16 v[12:15], a[172:175], v[168:171], v[12:15]\n"
    "v_exp_f32_e32 v205, v205\n"
    "v_mfma_f32_16x16x32_bf16 v[8:11], a[172:175], v[160:163], v[8:11]\n"
    "ds_read_b64_tr_b16 a[162:163], v244 offset:22528\n"
    "v_mfma_f32_16x16x32_bf16 v[8:11], a[168:171], v[156:159], v[8:11]\n"
    "v_cvt_pk_bf16_f32 v197, v198, v199\n"
    "v_mfma_f32_16x16x32_bf16 v[4:7], a[168:171], v[148:151], v[4:7]\n"
    "v_exp_f32_e32 v206, v206\n"
    "v_mfma_f32_16x16x32_bf16 v[4:7], a[172:175], v[152:155], v[4:7]\n"
    "v_exp_f32_e32 v207, v207\n"
    "v_mfma_f32_16x16x32_bf16 v[0:3], a[172:175], v[144:147], v[0:3]\n"
    "ds_read_b64_tr_b16 a[164:165], v244 offset:53248\n"
    "v_mfma_f32_16x16x32_bf16 v[0:3], a[168:171], v[140:143], v[0:3]\n"
    "s_waitcnt lgkmcnt(5)\n"
    "v_cvt_pk_bf16_f32 v204, v204, v205\n"
    "v_mfma_f32_16x16x32_bf16 v[20:23], a[176:179], v[140:143], v[20:23]\n"
    "v_mfma_f32_16x16x32_bf16 v[20:23], a[180:183], v[144:147], v[20:23]\n"
    "ds_read_b64_tr_b16 a[166:167], v244 offset:55296\n"
    "v_mfma_f32_16x16x32_bf16 v[24:27], a[180:183], v[152:155], v[24:27]\n"
    "ds_read_b64_tr_b16 a[168:169], v245 offset:20480\n"
    "v_mfma_f32_16x16x32_bf16 v[24:27], a[176:179], v[148:151], v[24:27]\n"
    "v_exp_f32_e32 v212, v212\n"
    "v_mfma_f32_16x16x32_bf16 v[28:31], a[176:179], v[156:159], v[28:31]\n"
    "ds_read_b64_tr_b16 a[170:171], v245 offset:22528\n"
    "v_mfma_f32_16x16x32_bf16 v[28:31], a[180:183], v[160:163], v[28:31]\n"
    "v_exp_f32_e32 v213, v213\n"
    "v_mfma_f32_16x16x32_bf16 v[32:35], a[180:183], v[168:171], v[32:35]\n"
    "v_cvt_pk_bf16_f32 v205, v206, v207\n"
    "v_mfma_f32_16x16x32_bf16 v[32:35], a[176:179], v[164:167], v[32:35]\n"
    "v_exp_f32_e32 v214, v214\n"
    "v_mfma_f32_16x16x32_bf16 v[36:39], a[176:179], v[172:175], v[36:39]\n"
    "ds_read_b64_tr_b16 a[172:173], v245 offset:53248\n"
    "v_mfma_f32_16x16x32_bf16 v[36:39], a[180:183], v[176:179], v[36:39]\n"
    "s_waitcnt lgkmcnt(6)\n"
    "v_exp_f32_e32 v215, v215\n"
    "v_mfma_f32_16x16x32_bf16 v[56:59], a[188:191], v[176:179], v[56:59]\n"
    "ds_read_b64_tr_b16 a[176:177], v246 offset:20480\n"
    "v_mfma_f32_16x16x32_bf16 v[56:59], a[184:187], v[172:175], v[56:59]\n"
    "ds_read_b64_tr_b16 a[174:175], v245 offset:55296\n"
    "v_mfma_f32_16x16x32_bf16 v[52:55], a[184:187], v[164:167], v[52:55]\n"
    "v_cvt_pk_bf16_f32 v212, v212, v213\n"
    "v_mfma_f32_16x16x32_bf16 v[52:55], a[188:191], v[168:171], v[52:55]\n"
    "v_cvt_pk_bf16_f32 v213, v214, v215\n"
    "v_mfma_f32_16x16x32_bf16 v[48:51], a[188:191], v[160:163], v[48:51]\n"
    "ds_read_b64_tr_b16 a[178:179], v246 offset:22528\n"
    "v_mfma_f32_16x16x32_bf16 v[48:51], a[184:187], v[156:159], v[48:51]\n"
    "v_exp_f32_e32 v184, v184\n"
    "v_mfma_f32_16x16x32_bf16 v[44:47], a[184:187], v[148:151], v[44:47]\n"
    "v_exp_f32_e32 v185, v185\n"
    "v_mfma_f32_16x16x32_bf16 v[44:47], a[188:191], v[152:155], v[44:47]\n"
    "v_exp_f32_e32 v186, v186\n"
    "v_mfma_f32_16x16x32_bf16 v[40:43], a[188:191], v[144:147], v[40:43]\n"
    "ds_read_b64_tr_b16 a[180:181], v246 offset:53248\n"
    "v_mfma_f32_16x16x32_bf16 v[40:43], a[184:187], v[140:143], v[40:43]\n"
    "s_waitcnt lgkmcnt(7)\n"
    "v_exp_f32_e32 v187, v187\n"
    "v_mfma_f32_16x16x32_bf16 v[60:63], a[160:163], v[140:143], v[60:63]\n"
    "ds_read_b64_tr_b16 a[184:185], v247 offset:20480\n"
    "v_mfma_f32_16x16x32_bf16 v[60:63], a[164:167], v[144:147], v[60:63]\n"
    "ds_read_b64_tr_b16 a[182:183], v246 offset:55296\n"
    "v_mfma_f32_16x16x32_bf16 v[64:67], a[164:167], v[152:155], v[64:67]\n"
    "ds_read_b64_tr_b16 a[186:187], v247 offset:22528\n"
    "v_mfma_f32_16x16x32_bf16 v[64:67], a[160:163], v[148:151], v[64:67]\n"
    "v_cvt_pk_bf16_f32 v182, v184, v185\n"
    "v_mfma_f32_16x16x32_bf16 v[68:71], a[160:163], v[156:159], v[68:71]\n"
    "v_exp_f32_e32 v192, v192\n"
    "v_mfma_f32_16x16x32_bf16 v[68:71], a[164:167], v[160:163], v[68:71]\n"
    "ds_read_b64_tr_b16 a[188:189], v247 offset:53248\n"
    "v_mfma_f32_16x16x32_bf16 v[72:75], a[164:167], v[168:171], v[72:75]\n"
    "ds_read_b64_tr_b16 a[190:191], v247 offset:55296\n"
    "v_mfma_f32_16x16x32_bf16 v[72:75], a[160:163], v[164:167], v[72:75]\n"
    "s_waitcnt vmcnt(9)\n"
    "v_mfma_f32_16x16x32_bf16 v[76:79], a[160:163], v[172:175], v[76:79]\n"
    "s_barrier\n"
    "v_mfma_f32_16x16x32_bf16 v[76:79], a[164:167], v[176:179], v[76:79]\n"
    "s_waitcnt lgkmcnt(7)\n"
    "s_mov_b32 m0, s42\n"
    "v_mfma_f32_16x16x32_bf16 v[96:99], a[172:175], v[176:179], v[96:99]\n"
    "v_exp_f32_e32 v193, v193\n"
    "v_mfma_f32_16x16x32_bf16 v[96:99], a[168:171], v[172:175], v[96:99]\n"
    "v_cvt_pk_bf16_f32 v183, v186, v187\n"
    "v_mfma_f32_16x16x32_bf16 v[92:95], a[168:171], v[164:167], v[92:95]\n"
    "v_exp_f32_e32 v194, v194\n"
    "v_mfma_f32_16x16x32_bf16 v[92:95], a[172:175], v[168:171], v[92:95]\n"
    "ds_read_b128 a[160:163], v240 offset:0\n"
    "v_mfma_f32_16x16x32_bf16 v[88:91], a[172:175], v[160:163], v[88:91]\n"
    "ds_write_addtid_b32 v180 offset:0\n"
    "v_mfma_f32_16x16x32_bf16 v[88:91], a[168:171], v[156:159], v[88:91]\n"
    "v_exp_f32_e32 v195, v195\n"
    "v_mfma_f32_16x16x32_bf16 v[84:87], a[168:171], v[148:151], v[84:87]\n"
    "ds_write_addtid_b32 v181 offset:256\n"
    "v_mfma_f32_16x16x32_bf16 v[84:87], a[172:175], v[152:155], v[84:87]\n"
    "ds_write_addtid_b32 v188 offset:1024\n"
    "v_mfma_f32_16x16x32_bf16 v[80:83], a[172:175], v[144:147], v[80:83]\n"
    "v_cvt_pk_bf16_f32 v190, v192, v193\n"
    "v_mfma_f32_16x16x32_bf16 v[80:83], a[168:171], v[140:143], v[80:83]\n"
    "s_waitcnt lgkmcnt(7)\n"
    "v_exp_f32_e32 v200, v200\n"
    "v_mfma_f32_16x16x32_bf16 v[100:103], a[176:179], v[140:143], v[100:103]\n"
    "ds_read_b128 a[164:167], v241 offset:0\n"
    "v_mfma_f32_16x16x32_bf16 v[100:103], a[180:183], v[144:147], v[100:103]\n"
    "ds_write_addtid_b32 v189 offset:1280\n"
    "v_mfma_f32_16x16x32_bf16 v[104:107], a[180:183], v[152:155], v[104:107]\n"
    "ds_read_b128 a[168:171], v240 offset:4096\n"
    "v_mfma_f32_16x16x32_bf16 v[104:107], a[176:179], v[148:151], v[104:107]\n"
    "v_exp_f32_e32 v201, v201\n"
    "v_mfma_f32_16x16x32_bf16 v[108:111], a[176:179], v[156:159], v[108:111]\n"
    "ds_write_addtid_b32 v196 offset:2048\n"
    "v_mfma_f32_16x16x32_bf16 v[108:111], a[180:183], v[160:163], v[108:111]\n"
    "v_cvt_pk_bf16_f32 v191, v194, v195\n"
    "v_mfma_f32_16x16x32_bf16 v[112:115], a[180:183], v[168:171], v[112:115]\n"
    "ds_write_addtid_b32 v197 offset:2304\n"
    "v_mfma_f32_16x16x32_bf16 v[112:115], a[176:179], v[164:167], v[112:115]\n"
    "v_exp_f32_e32 v202, v202\n"
    "v_mfma_f32_16x16x32_bf16 v[116:119], a[176:179], v[172:175], v[116:119]\n"
    "ds_write_addtid_b32 v204 offset:3072\n"
    "v_mfma_f32_16x16x32_bf16 v[116:119], a[180:183], v[176:179], v[116:119]\n"
    "s_waitcnt lgkmcnt(10)\n"
    "ds_read_b128 a[172:175], v241 offset:4096\n"
    "v_mfma_f32_16x16x32_bf16 v[136:139], a[188:191], v[176:179], v[136:139]\n"
    "v_exp_f32_e32 v203, v203\n"
    "v_mfma_f32_16x16x32_bf16 v[136:139], a[184:187], v[172:175], v[136:139]\n"
    "ds_write_addtid_b32 v205 offset:3328\n"
    "v_cvt_pk_bf16_f32 v198, v200, v201\n"
    "v_mfma_f32_16x16x32_bf16 v[132:135], a[184:187], v[164:167], v[132:135]\n"
    "ds_read_b128 a[176:179], v242 offset:0\n"
    "v_mfma_f32_16x16x32_bf16 v[132:135], a[188:191], v[168:171], v[132:135]\n"
    "ds_write_addtid_b32 v212 offset:4096\n"
    "v_mfma_f32_16x16x32_bf16 v[128:131], a[188:191], v[160:163], v[128:131]\n"
    "v_exp_f32_e32 v208, v208\n"
    "v_mfma_f32_16x16x32_bf16 v[128:131], a[184:187], v[156:159], v[128:131]\n"
    "ds_write_addtid_b32 v213 offset:4352\n"
    "v_mfma_f32_16x16x32_bf16 v[124:127], a[184:187], v[148:151], v[124:127]\n"
    "v_exp_f32_e32 v209, v209\n"
    "v_mfma_f32_16x16x32_bf16 v[124:127], a[188:191], v[152:155], v[124:127]\n"
    "ds_read_b128 a[180:183], v243 offset:0\n"
    "v_mfma_f32_16x16x32_bf16 v[120:123], a[188:191], v[144:147], v[120:123]\n"
    "ds_write_addtid_b32 v182 offset:512\n"
    "v_mfma_f32_16x16x32_bf16 v[120:123], a[184:187], v[140:143], v[120:123]\n"
    "s_waitcnt lgkmcnt(6)\n"
    "v_cvt_pk_bf16_f32 v199, v202, v203\n"
    "v_mfma_f32_16x16x32_bf16 v[172:175], a[160:163], a[128:131], v[236:239]\n"
    "v_mfma_f32_16x16x32_bf16 v[172:175], a[164:167], a[132:135], v[172:175]\n"
    "v_exp_f32_e32 v210, v210\n"
    "v_mfma_f32_16x16x32_bf16 v[172:175], a[168:171], a[136:139], v[172:175]\n"
    "ds_write_addtid_b32 v183 offset:768\n"
    "v_mfma_f32_16x16x32_bf16 v[172:175], a[172:175], a[140:143], v[172:175]\n"
    "ds_read_b128 a[184:187], v242 offset:4096\n"
    "v_mfma_f32_16x16x32_bf16 v[164:167], a[172:175], a[108:111], v[232:235]\n"
    "v_exp_f32_e32 v211, v211\n"
    "v_mfma_f32_16x16x32_bf16 v[164:167], a[168:171], a[104:107], v[164:167]\n"
    "ds_write_addtid_b32 v190 offset:1536\n"
    "v_mfma_f32_16x16x32_bf16 v[164:167], a[164:167], a[100:103], v[164:167]\n"
    "v_cvt_pk_bf16_f32 v206, v208, v209\n"
    "v_mfma_f32_16x16x32_bf16 v[164:167], a[160:163], a[96:99], v[164:167]\n"
    "ds_write_addtid_b32 v191 offset:1792\n"
    "v_mfma_f32_16x16x32_bf16 v[156:159], a[160:163], a[64:67], v[228:231]\n"
    "v_exp_f32_e32 v216, v216\n"
    "v_mfma_f32_16x16x32_bf16 v[156:159], a[164:167], a[68:71], v[156:159]\n"
    "ds_read_b128 a[188:191], v243 offset:4096\n"
    "v_mfma_f32_16x16x32_bf16 v[156:159], a[168:171], a[72:75], v[156:159]\n"
    "ds_write_addtid_b32 v198 offset:2560\n"
    "v_mfma_f32_16x16x32_bf16 v[156:159], a[172:175], a[76:79], v[156:159]\n"
    "v_exp_f32_e32 v217, v217\n"
    "v_mfma_f32_16x16x32_bf16 v[148:151], a[172:175], a[44:47], v[224:227]\n"
    "v_mfma_f32_16x16x32_bf16 v[148:151], a[168:171], a[40:43], v[148:151]\n"
    "ds_write_addtid_b32 v199 offset:2816\n"
    "v_mfma_f32_16x16x32_bf16 v[148:151], a[164:167], a[36:39], v[148:151]\n"
    "v_cvt_pk_bf16_f32 v207, v210, v211\n"
    "v_mfma_f32_16x16x32_bf16 v[148:151], a[160:163], a[32:35], v[148:151]\n"
    "v_mfma_f32_16x16x32_bf16 v[140:143], a[160:163], a[0:3], v[220:223]\n"
    "v_exp_f32_e32 v218, v218\n"
    "v_mfma_f32_16x16x32_bf16 v[140:143], a[164:167], a[4:7], v[140:143]\n"
    "ds_write_addtid_b32 v206 offset:3584\n"
    "v_mfma_f32_16x16x32_bf16 v[140:143], a[168:171], a[8:11], v[140:143]\n"
    "ds_write_addtid_b32 v207 offset:3840\n"
    "v_mfma_f32_16x16x32_bf16 v[140:143], a[172:175], a[12:15], v[140:143]\n"
    "s_waitcnt lgkmcnt(4)\n"
    "v_exp_f32_e32 v219, v219\n"
    "v_mfma_f32_16x16x32_bf16 v[140:143], a[176:179], a[16:19], v[140:143]\n"
    "ds_read_b128 a[160:163], v240 offset:2048\n"
    "v_mfma_f32_16x16x32_bf16 v[140:143], a[180:183], a[20:23], v[140:143]\n"
    "v_cvt_pk_bf16_f32 v214, v216, v217\n"
    "v_mfma_f32_16x16x32_bf16 v[140:143], a[184:187], a[24:27], v[140:143]\n"
    "ds_read_b128 a[168:171], v240 offset:6144\n"
    "v_mfma_f32_16x16x32_bf16 v[140:143], a[188:191], a[28:31], v[140:143]\n"
    "ds_write_addtid_b32 v214 offset:4608\n"
    "v_mfma_f32_16x16x32_bf16 v[148:151], a[188:191], a[60:63], v[148:151]\n"
    "v_cvt_pk_bf16_f32 v215, v218, v219\n"
    "v_mfma_f32_16x16x32_bf16 v[148:151], a[184:187], a[56:59], v[148:151]\n"
    "ds_read_b128 a[164:167], v241 offset:2048\n"
    "v_mfma_f32_16x16x32_bf16 v[148:151], a[180:183], a[52:55], v[148:151]\n"
    "ds_write_addtid_b32 v215 offset:4864\n"
    "v_mfma_f32_16x16x32_bf16 v[148:151], a[176:179], a[48:51], v[148:151]\n"
    "ds_read_b128 a[172:175], v241 offset:6144\n"
    "v_mfma_f32_16x16x32_bf16 v[156:159], a[176:179], a[80:83], v[156:159]\n"
    "v_mfma_f32_16x16x32_bf16 v[156:159], a[180:183], a[84:87], v[156:159]\n"
    "s_waitcnt lgkmcnt(1)\n"
    "v_mfma_f32_16x16x32_bf16 v[156:159], a[184:187], a[88:91], v[156:159]\n"
    "s_barrier\n"
    "v_mfma_f32_16x16x32_bf16 v[156:159], a[188:191], a[92:95], v[156:159]\n"
    "s_add_u32 m0, s43, 16384\n"
    "v_mfma_f32_16x16x32_bf16 v[164:167], a[188:191], a[124:127], v[164:167]\n"
    "ds_read2st64_b32 v[184:185], v249 offset0:0 offset1:1\n"
    "v_mfma_f32_16x16x32_bf16 v[164:167], a[184:187], a[120:123], v[164:167]\n"
    "buffer_load_dwordx4 v248, s[8:11], s40 offen offset:0 sc1 lds\n"
    "v_mfma_f32_16x16x32_bf16 v[164:167], a[180:183], a[116:119], v[164:167]\n"
    "v_mfma_f32_16x16x32_bf16 v[164:167], a[176:179], a[112:115], v[164:167]\n"
    "v_mfma_f32_16x16x32_bf16 v[172:175], a[176:179], a[144:147], v[172:175]\n"
    "ds_read2st64_b32 v[186:187], v249 offset0:2 offset1:3\n"
    "v_mfma_f32_16x16x32_bf16 v[172:175], a[180:183], a[148:151], v[172:175]\n"
    "buffer_load_dwordx4 v248, s[8:11], s40 offen offset:1024 sc1 lds\n"
    "v_mfma_f32_16x16x32_bf16 v[172:175], a[184:187], a[152:155], v[172:175]\n"
    "v_mfma_f32_16x16x32_bf16 v[172:175], a[188:191], a[156:159], v[172:175]\n"
    "s_waitcnt lgkmcnt(2)\n"
    "ds_read2st64_b32 v[192:193], v249 offset0:4 offset1:5\n"
    "v_mfma_f32_16x16x32_bf16 v[176:179], a[160:163], a[128:131], v[236:239]\n"
    "ds_read_b128 a[176:179], v242 offset:2048\n"
    "v_mfma_f32_16x16x32_bf16 v[176:179], a[164:167], a[132:135], v[176:179]\n"
    "buffer_load_dwordx4 v248, s[8:11], s40 offen offset:2048 sc1 lds\n"
    "v_mfma_f32_16x16x32_bf16 v[176:179], a[168:171], a[136:139], v[176:179]\n"
    "ds_read_b128 a[184:187], v242 offset:6144\n"
    "v_mfma_f32_16x16x32_bf16 v[176:179], a[172:175], a[140:143], v[176:179]\n"
    "v_exp_f32_e32 v140, v140\n"
    "v_mfma_f32_16x16x32_bf16 v[168:171], a[172:175], a[108:111], v[232:235]\n"
    "ds_read2st64_b32 v[194:195], v249 offset0:6 offset1:7\n"
    "v_mfma_f32_16x16x32_bf16 v[168:171], a[168:171], a[104:107], v[168:171]\n"
    "buffer_load_dwordx4 v248, s[8:11], s40 offen offset:3072 sc1 lds\n"
    "v_mfma_f32_16x16x32_bf16 v[168:171], a[164:167], a[100:103], v[168:171]\n"
    "s_add_u32 m0, s43, 20480\n"
    "ds_read_b128 a[180:183], v243 offset:2048\n"
    "v_mfma_f32_16x16x32_bf16 v[168:171], a[160:163], a[96:99], v[168:171]\n"
    "s_add_u32 s40, s40, 0x8000\n"
    "ds_read_b128 a[188:191], v243 offset:6144\n"
    "v_mfma_f32_16x16x32_bf16 v[160:163], a[160:163], a[64:67], v[228:231]\n"
    "ds_read2st64_b32 v[200:201], v249 offset0:8 offset1:9\n"
    "v_mfma_f32_16x16x32_bf16 v[160:163], a[164:167], a[68:71], v[160:163]\n"
    "buffer_load_dwordx4 v248, s[8:11], s41 offen offset:0 sc1 lds\n"
    "v_mfma_f32_16x16x32_bf16 v[160:163], a[168:171], a[72:75], v[160:163]\n"
    "v_exp_f32_e32 v141, v141\n"
    "v_mfma_f32_16x16x32_bf16 v[160:163], a[172:175], a[76:79], v[160:163]\n"
    "v_exp_f32_e32 v142, v142\n"
    "v_mfma_f32_16x16x32_bf16 v[152:155], a[172:175], a[44:47], v[224:227]\n"
    "ds_read2st64_b32 v[202:203], v249 offset0:10 offset1:11\n"
    "v_mfma_f32_16x16x32_bf16 v[152:155], a[168:171], a[40:43], v[152:155]\n"
    "buffer_load_dwordx4 v248, s[8:11], s41 offen offset:1024 sc1 lds\n"
    "v_mfma_f32_16x16x32_bf16 v[152:155], a[164:167], a[36:39], v[152:155]\n"
    "v_exp_f32_e32 v143, v143\n"
    "v_mfma_f32_16x16x32_bf16 v[152:155], a[160:163], a[32:35], v[152:155]\n"
    "v_cvt_pk_bf16_f32 v140, v140, v141\n"
    "v_mfma_f32_16x16x32_bf16 v[144:147], a[160:163], a[0:3], v[220:223]\n"
    "ds_read2st64_b32 v[208:209], v249 offset0:12 offset1:13\n"
    "v_mfma_f32_16x16x32_bf16 v[144:147], a[164:167], a[4:7], v[144:147]\n"
    "buffer_load_dwordx4 v248, s[8:11], s41 offen offset:2048 sc1 lds\n"
    "v_mfma_f32_16x16x32_bf16 v[144:147], a[168:171], a[8:11], v[144:147]\n"
    "ds_read_b64_tr_b16 a[160:161], v244 offset:24576\n"
    "v_mfma_f32_16x16x32_bf16 v[144:147], a[172:175], a[12:15], v[144:147]\n"
    "s_waitcnt lgkmcnt(4)\n"
    "ds_read2st64_b32 v[210:211], v249 offset0:14 offset1:15\n"
    "v_mfma_f32_16x16x32_bf16 v[144:147], a[176:179], a[16:19], v[144:147]\n"
    "ds_read_b64_tr_b16 a[168:169], v245 offset:24576\n"
    "v_mfma_f32_16x16x32_bf16 v[144:147], a[180:183], a[20:23], v[144:147]\n"
    "buffer_load_dwordx4 v248, s[8:11], s41 offen offset:3072 sc1 lds\n"
    "v_mfma_f32_16x16x32_bf16 v[144:147], a[184:187], a[24:27], v[144:147]\n"
    "ds_read_b64_tr_b16 a[162:163], v244 offset:26624\n"
    "v_mfma_f32_16x16x32_bf16 v[144:147], a[188:191], a[28:31], v[144:147]\n"
    "s_add_u32 s41, s41, 0x8000\n"
    "ds_read2st64_b32 v[216:217], v249 offset0:16 offset1:17\n"
    "v_mfma_f32_16x16x32_bf16 v[152:155], a[188:191], a[60:63], v[152:155]\n"
    "ds_read_b64_tr_b16 a[164:165], v244 offset:57344\n"
    "v_mfma_f32_16x16x32_bf16 v[152:155], a[184:187], a[56:59], v[152:155]\n"
    "ds_read_b64_tr_b16 a[170:171], v245 offset:26624\n"
    "v_mfma_f32_16x16x32_bf16 v[152:155], a[180:183], a[52:55], v[152:155]\n"
    "v_exp_f32_e32 v148, v148\n"
    "v_mfma_f32_16x16x32_bf16 v[152:155], a[176:179], a[48:51], v[152:155]\n"
    "ds_read_b64_tr_b16 a[166:167], v244 offset:59392\n"
    "v_mfma_f32_16x16x32_bf16 v[160:163], a[176:179], a[80:83], v[160:163]\n"
    "ds_read_b64_tr_b16 a[172:173], v245 offset:57344\n"
    "v_mfma_f32_16x16x32_bf16 v[160:163], a[180:183], a[84:87], v[160:163]\n"
    "ds_read2st64_b32 v[218:219], v249 offset0:18 offset1:19\n"
    "v_mfma_f32_16x16x32_bf16 v[160:163], a[184:187], a[88:91], v[160:163]\n"
    "v_exp_f32_e32 v149, v149\n"
    "v_mfma_f32_16x16x32_bf16 v[160:163], a[188:191], a[92:95], v[160:163]\n"
    "ds_read_b64_tr_b16 a[174:175], v245 offset:59392\n"
    "v_cvt_pk_bf16_f32 v141, v142, v143\n"
    "v_mfma_f32_16x16x32_bf16 v[168:171], a[188:191], a[124:127], v[168:171]\n"
    "v_exp_f32_e32 v150, v150\n"
    "v_mfma_f32_16x16x32_bf16 v[168:171], a[184:187], a[120:123], v[168:171]\n"
    "v_exp_f32_e32 v151, v151\n"
    "v_mfma_f32_16x16x32_bf16 v[168:171], a[180:183], a[116:119], v[168:171]\n"
    "v_mfma_f32_16x16x32_bf16 v[168:171], a[176:179], a[112:115], v[168:171]\n"
    "v_cvt_pk_bf16_f32 v148, v148, v149\n"
    "v_mfma_f32_16x16x32_bf16 v[176:179], a[176:179], a[144:147], v[176:179]\n"
    "v_exp_f32_e32 v156, v156\n"
    "v_mfma_f32_16x16x32_bf16 v[176:179], a[180:183], a[148:151], v[176:179]\n"
    "v_exp_f32_e32 v157, v157\n"
    "v_mfma_f32_16x16x32_bf16 v[176:179], a[184:187], a[152:155], v[176:179]\n"
    "ds_read_b64_tr_b16 a[176:177], v246 offset:24576\n"
    "v_mfma_f32_16x16x32_bf16 v[176:179], a[188:191], a[156:159], v[176:179]\n"
    "s_waitcnt lgkmcnt(2)\n"
    "v_cvt_pk_bf16_f32 v149, v150, v151\n"
    "v_mfma_f32_16x16x32_bf16 a[236:239], a[160:163], v[180:183], a[236:239]\n"
    "v_mfma_f32_16x16x32_bf16 a[236:239], a[164:167], v[184:187], a[236:239]\n"
    "ds_read_b64_tr_b16 a[178:179], v246 offset:26624\n"
    "v_mfma_f32_16x16x32_bf16 a[240:243], a[164:167], v[192:195], a[240:243]\n"
    "ds_read_b64_tr_b16 a[184:185], v247 offset:24576\n"
    "v_mfma_f32_16x16x32_bf16 a[240:243], a[160:163], v[188:191], a[240:243]\n"
    "v_exp_f32_e32 v158, v158\n"
    "v_mfma_f32_16x16x32_bf16 a[244:247], a[160:163], v[196:199], a[244:247]\n"
    "ds_read_b64_tr_b16 a[180:181], v246 offset:57344\n"
    "v_mfma_f32_16x16x32_bf16 a[244:247], a[164:167], v[200:203], a[244:247]\n"
    "ds_read_b64_tr_b16 a[186:187], v247 offset:26624\n"
    "v_mfma_f32_16x16x32_bf16 a[248:251], a[164:167], v[208:211], a[248:251]\n"
    "v_exp_f32_e32 v159, v159\n"
    "v_mfma_f32_16x16x32_bf16 a[248:251], a[160:163], v[204:207], a[248:251]\n"
    "ds_read_b64_tr_b16 a[182:183], v246 offset:59392\n"
    "v_mfma_f32_16x16x32_bf16 a[252:255], a[160:163], v[212:215], a[252:255]\n"
    "ds_read_b64_tr_b16 a[188:189], v247 offset:57344\n"
    "v_mfma_f32_16x16x32_bf16 a[252:255], a[164:167], v[216:219], a[252:255]\n"
    "s_waitcnt lgkmcnt(7)\n"
    "v_cvt_pk_bf16_f32 v156, v156, v157\n"
    "v_mfma_f32_16x16x32_bf16 v[16:19], a[172:175], v[216:219], v[16:19]\n"
    "ds_read_b64_tr_b16 a[160:161], v244 offset:28672\n"
    "v_mfma_f32_16x16x32_bf16 v[16:19], a[168:171], v[212:215], v[16:19]\n"
    "ds_read_b64_tr_b16 a[190:191], v247 offset:59392\n"
    "v_mfma_f32_16x16x32_bf16 v[12:15], a[168:171], v[204:207], v[12:15]\n"
    "v_exp_f32_e32 v164, v164\n"
    "v_mfma_f32_16x16x32_bf16 v[12:15], a[172:175], v[208:211], v[12:15]\n"
    "v_exp_f32_e32 v165, v165\n"
    "v_mfma_f32_16x16x32_bf16 v[8:11], a[172:175], v[200:203], v[8:11]\n"
    "ds_read_b64_tr_b16 a[162:163], v244 offset:30720\n"
    "v_mfma_f32_16x16x32_bf16 v[8:11], a[168:171], v[196:199], v[8:11]\n"
    "v_cvt_pk_bf16_f32 v157, v158, v159\n"
    "v_mfma_f32_16x16x32_bf16 v[4:7], a[168:171], v[188:191], v[4:7]\n"
    "v_exp_f32_e32 v166, v166\n"
    "v_mfma_f32_16x16x32_bf16 v[4:7], a[172:175], v[192:195], v[4:7]\n"
    "v_exp_f32_e32 v167, v167\n"
    "v_mfma_f32_16x16x32_bf16 v[0:3], a[172:175], v[184:187], v[0:3]\n"
    "ds_read_b64_tr_b16 a[164:165], v244 offset:61440\n"
    "v_mfma_f32_16x16x32_bf16 v[0:3], a[168:171], v[180:183], v[0:3]\n"
    "s_waitcnt lgkmcnt(5)\n"
    "v_cvt_pk_bf16_f32 v164, v164, v165\n"
    "v_mfma_f32_16x16x32_bf16 v[20:23], a[176:179], v[180:183], v[20:23]\n"
    "v_mfma_f32_16x16x32_bf16 v[20:23], a[180:183], v[184:187], v[20:23]\n"
    "ds_read_b64_tr_b16 a[166:167], v244 offset:63488\n"
    "v_mfma_f32_16x16x32_bf16 v[24:27], a[180:183], v[192:195], v[24:27]\n"
    "ds_read_b64_tr_b16 a[168:169], v245 offset:28672\n"
    "v_mfma_f32_16x16x32_bf16 v[24:27], a[176:179], v[188:191], v[24:27]\n"
    "v_exp_f32_e32 v172, v172\n"
    "v_mfma_f32_16x16x32_bf16 v[28:31], a[176:179], v[196:199], v[28:31]\n"
    "ds_read_b64_tr_b16 a[170:171], v245 offset:30720\n"
    "v_mfma_f32_16x16x32_bf16 v[28:31], a[180:183], v[200:203], v[28:31]\n"
    "v_exp_f32_e32 v173, v173\n"
    "v_mfma_f32_16x16x32_bf16 v[32:35], a[180:183], v[208:211], v[32:35]\n"
    "v_cvt_pk_bf16_f32 v165, v166, v167\n"
    "v_mfma_f32_16x16x32_bf16 v[32:35], a[176:179], v[204:207], v[32:35]\n"
    "v_exp_f32_e32 v174, v174\n"
    "v_mfma_f32_16x16x32_bf16 v[36:39], a[176:179], v[212:215], v[36:39]\n"
    "ds_read_b64_tr_b16 a[172:173], v245 offset:61440\n"
    "v_mfma_f32_16x16x32_bf16 v[36:39], a[180:183], v[216:219], v[36:39]\n"
    "s_waitcnt lgkmcnt(6)\n"
    "v_exp_f32_e32 v175, v175\n"
    "v_mfma_f32_16x16x32_bf16 v[56:59], a[188:191], v[216:219], v[56:59]\n"
    "ds_read_b64_tr_b16 a[176:177], v246 offset:28672\n"
    "v_mfma_f32_16x16x32_bf16 v[56:59], a[184:187], v[212:215], v[56:59]\n"
    "ds_read_b64_tr_b16 a[174:175], v245 offset:63488\n"
    "v_mfma_f32_16x16x32_bf16 v[52:55], a[184:187], v[204:207], v[52:55]\n"
    "v_cvt_pk_bf16_f32 v172, v172, v173\n"
    "v_mfma_f32_16x16x32_bf16 v[52:55], a[188:191], v[208:211], v[52:55]\n"
    "v_cvt_pk_bf16_f32 v173, v174, v175\n"
    "v_mfma_f32_16x16x32_bf16 v[48:51], a[188:191], v[200:203], v[48:51]\n"
    "ds_read_b64_tr_b16 a[178:179], v246 offset:30720\n"
    "v_mfma_f32_16x16x32_bf16 v[48:51], a[184:187], v[196:199], v[48:51]\n"
    "v_exp_f32_e32 v144, v144\n"
    "v_mfma_f32_16x16x32_bf16 v[44:47], a[184:187], v[188:191], v[44:47]\n"
    "v_exp_f32_e32 v145, v145\n"
    "v_mfma_f32_16x16x32_bf16 v[44:47], a[188:191], v[192:195], v[44:47]\n"
    "v_exp_f32_e32 v146, v146\n"
    "v_mfma_f32_16x16x32_bf16 v[40:43], a[188:191], v[184:187], v[40:43]\n"
    "ds_read_b64_tr_b16 a[180:181], v246 offset:61440\n"
    "v_mfma_f32_16x16x32_bf16 v[40:43], a[184:187], v[180:183], v[40:43]\n"
    "s_waitcnt lgkmcnt(7)\n"
    "v_exp_f32_e32 v147, v147\n"
    "v_mfma_f32_16x16x32_bf16 v[60:63], a[160:163], v[180:183], v[60:63]\n"
    "ds_read_b64_tr_b16 a[184:185], v247 offset:28672\n"
    "v_mfma_f32_16x16x32_bf16 v[60:63], a[164:167], v[184:187], v[60:63]\n"
    "ds_read_b64_tr_b16 a[182:183], v246 offset:63488\n"
    "v_mfma_f32_16x16x32_bf16 v[64:67], a[164:167], v[192:195], v[64:67]\n"
    "ds_read_b64_tr_b16 a[186:187], v247 offset:30720\n"
    "v_mfma_f32_16x16x32_bf16 v[64:67], a[160:163], v[188:191], v[64:67]\n"
    "v_cvt_pk_bf16_f32 v142, v144, v145\n"
    "v_mfma_f32_16x16x32_bf16 v[68:71], a[160:163], v[196:199], v[68:71]\n"
    "v_exp_f32_e32 v152, v152\n"
    "v_mfma_f32_16x16x32_bf16 v[68:71], a[164:167], v[200:203], v[68:71]\n"
    "ds_read_b64_tr_b16 a[188:189], v247 offset:61440\n"
    "v_mfma_f32_16x16x32_bf16 v[72:75], a[164:167], v[208:211], v[72:75]\n"
    "ds_read_b64_tr_b16 a[190:191], v247 offset:63488\n"
    "v_mfma_f32_16x16x32_bf16 v[72:75], a[160:163], v[204:207], v[72:75]\n"
    "s_waitcnt vmcnt(8)\n"
    "v_mfma_f32_16x16x32_bf16 v[76:79], a[160:163], v[212:215], v[76:79]\n"
    "s_barrier\n"
    "v_mfma_f32_16x16x32_bf16 v[76:79], a[164:167], v[216:219], v[76:79]\n"
    "s_waitcnt lgkmcnt(7)\n"
    "s_mov_b32 m0, s42\n"
    "v_mfma_f32_16x16x32_bf16 v[96:99], a[172:175], v[216:219], v[96:99]\n"
    "v_exp_f32_e32 v153, v153\n"
    "v_mfma_f32_16x16x32_bf16 v[96:99], a[168:171], v[212:215], v[96:99]\n"
    "v_cvt_pk_bf16_f32 v143, v146, v147\n"
    "v_mfma_f32_16x16x32_bf16 v[92:95], a[168:171], v[204:207], v[92:95]\n"
    "v_exp_f32_e32 v154, v154\n"
    "v_mfma_f32_16x16x32_bf16 v[92:95], a[172:175], v[208:211], v[92:95]\n"
    "ds_read_b128 a[160:163], v240 offset:8192\n"
    "v_mfma_f32_16x16x32_bf16 v[88:91], a[172:175], v[200:203], v[88:91]\n"
    "ds_write_addtid_b32 v140 offset:0\n"
    "v_mfma_f32_16x16x32_bf16 v[88:91], a[168:171], v[196:199], v[88:91]\n"
    "v_exp_f32_e32 v155, v155\n"
    "v_mfma_f32_16x16x32_bf16 v[84:87], a[168:171], v[188:191], v[84:87]\n"
    "ds_write_addtid_b32 v141 offset:256\n"
    "v_mfma_f32_16x16x32_bf16 v[84:87], a[172:175], v[192:195], v[84:87]\n"
    "ds_write_addtid_b32 v148 offset:1024\n"
    "v_mfma_f32_16x16x32_bf16 v[80:83], a[172:175], v[184:187], v[80:83]\n"
    "v_cvt_pk_bf16_f32 v150, v152, v153\n"
    "v_mfma_f32_16x16x32_bf16 v[80:83], a[168:171], v[180:183], v[80:83]\n"
    "s_waitcnt lgkmcnt(7)\n"
    "v_exp_f32_e32 v160, v160\n"
    "v_mfma_f32_16x16x32_bf16 v[100:103], a[176:179], v[180:183], v[100:103]\n"
    "ds_read_b128 a[164:167], v241 offset:8192\n"
    "v_mfma_f32_16x16x32_bf16 v[100:103], a[180:183], v[184:187], v[100:103]\n"
    "ds_write_addtid_b32 v149 offset:1280\n"
    "v_mfma_f32_16x16x32_bf16 v[104:107], a[180:183], v[192:195], v[104:107]\n"
    "ds_read_b128 a[168:171], v240 offset:12288\n"
    "v_mfma_f32_16x16x32_bf16 v[104:107], a[176:179], v[188:191], v[104:107]\n"
    "v_exp_f32_e32 v161, v161\n"
    "v_mfma_f32_16x16x32_bf16 v[108:111], a[176:179], v[196:199], v[108:111]\n"
    "ds_write_addtid_b32 v156 offset:2048\n"
    "v_mfma_f32_16x16x32_bf16 v[108:111], a[180:183], v[200:203], v[108:111]\n"
    "v_cvt_pk_bf16_f32 v151, v154, v155\n"
    "v_mfma_f32_16x16x32_bf16 v[112:115], a[180:183], v[208:211], v[112:115]\n"
    "ds_write_addtid_b32 v157 offset:2304\n"
    "v_mfma_f32_16x16x32_bf16 v[112:115], a[176:179], v[204:207], v[112:115]\n"
    "v_exp_f32_e32 v162, v162\n"
    "v_mfma_f32_16x16x32_bf16 v[116:119], a[176:179], v[212:215], v[116:119]\n"
    "ds_write_addtid_b32 v164 offset:3072\n"
    "v_mfma_f32_16x16x32_bf16 v[116:119], a[180:183], v[216:219], v[116:119]\n"
    "s_waitcnt lgkmcnt(10)\n"
    "ds_read_b128 a[172:175], v241 offset:12288\n"
    "v_mfma_f32_16x16x32_bf16 v[136:139], a[188:191], v[216:219], v[136:139]\n"
    "v_exp_f32_e32 v163, v163\n"
    "v_mfma_f32_16x16x32_bf16 v[136:139], a[184:187], v[212:215], v[136:139]\n"
    "ds_write_addtid_b32 v165 offset:3328\n"
    "v_cvt_pk_bf16_f32 v158, v160, v161\n"
    "v_mfma_f32_16x16x32_bf16 v[132:135], a[184:187], v[204:207], v[132:135]\n"
    "ds_read_b128 a[176:179], v242 offset:8192\n"
    "v_mfma_f32_16x16x32_bf16 v[132:135], a[188:191], v[208:211], v[132:135]\n"
    "ds_write_addtid_b32 v172 offset:4096\n"
    "v_mfma_f32_16x16x32_bf16 v[128:131], a[188:191], v[200:203], v[128:131]\n"
    "v_exp_f32_e32 v168, v168\n"
    "v_mfma_f32_16x16x32_bf16 v[128:131], a[184:187], v[196:199], v[128:131]\n"
    "ds_write_addtid_b32 v173 offset:4352\n"
    "v_mfma_f32_16x16x32_bf16 v[124:127], a[184:187], v[188:191], v[124:127]\n"
    "v_exp_f32_e32 v169, v169\n"
    "v_mfma_f32_16x16x32_bf16 v[124:127], a[188:191], v[192:195], v[124:127]\n"
    "ds_read_b128 a[180:183], v243 offset:8192\n"
    "v_mfma_f32_16x16x32_bf16 v[120:123], a[188:191], v[184:187], v[120:123]\n"
    "ds_write_addtid_b32 v142 offset:512\n"
    "v_mfma_f32_16x16x32_bf16 v[120:123], a[184:187], v[180:183], v[120:123]\n"
    "s_waitcnt lgkmcnt(6)\n"
    "v_cvt_pk_bf16_f32 v159, v162, v163\n"
    ".Lat_in:\n"
    "v_mfma_f32_16x16x32_bf16 v[212:215], a[160:163], a[128:131], v[236:239]\n"
    ".long 0xbedf00ff\n"
    ".Lat_w4:\n"
    "s_setpc_b64 s[2:3]\n"
    "v_mfma_f32_16x16x32_bf16 v[212:215], a[164:167], a[132:135], v[212:215]\n"
    "v_exp_f32_e32 v170, v170\n"
    "v_mfma_f32_16x16x32_bf16 v[212:215], a[168:171], a[136:139], v[212:215]\n"
    "ds_write_addtid_b32 v143 offset:768\n"
    "v_mfma_f32_16x16x32_bf16 v[212:215], a[172:175], a[140:143], v[212:215]\n"
    "ds_read_b128 a[184:187], v242 offset:12288\n"
    "v_mfma_f32_16x16x32_bf16 v[204:207], a[172:175], a[108:111], v[232:235]\n"
    "v_exp_f32_e32 v171, v171\n"
    "v_mfma_f32_16x16x32_bf16 v[204:207], a[168:171], a[104:107], v[204:207]\n"
    "ds_write_addtid_b32 v150 offset:1536\n"
    "v_mfma_f32_16x16x32_bf16 v[204:207], a[164:167], a[100:103], v[204:207]\n"
    "v_cvt_pk_bf16_f32 v166, v168, v169\n"
    "v_mfma_f32_16x16x32_bf16 v[204:207], a[160:163], a[96:99], v[204:207]\n"
    "ds_write_addtid_b32 v151 offset:1792\n"
    "v_mfma_f32_16x16x32_bf16 v[196:199], a[160:163], a[64:67], v[228:231]\n"
    "v_exp_f32_e32 v176, v176\n"
    "v_mfma_f32_16x16x32_bf16 v[196:199], a[164:167], a[68:71], v[196:199]\n"
    "ds_read_b128 a[188:191], v243 offset:12288\n"
    "v_mfma_f32_16x16x32_bf16 v[196:199], a[168:171], a[72:75], v[196:199]\n"
    "ds_write_addtid_b32 v158 offset:2560\n"
    "v_mfma_f32_16x16x32_bf16 v[196:199], a[172:175], a[76:79], v[196:199]\n"
    "v_exp_f32_e32 v177, v177\n"
    "v_mfma_f32_16x16x32_bf16 v[188:191], a[172:175], a[44:47], v[224:227]\n"
    "v_mfma_f32_16x16x32_bf16 v[188:191], a[168:171], a[40:43], v[188:191]\n"
    "ds_write_addtid_b32 v159 offset:2816\n"
    "v_mfma_f32_16x16x32_bf16 v[188:191], a[164:167], a[36:39], v[188:191]\n"
    "v_cvt_pk_bf16_f32 v167, v170, v171\n"
    "v_mfma_f32_16x16x32_bf16 v[188:191], a[160:163], a[32:35], v[188:191]\n"
    "v_mfma_f32_16x16x32_bf16 v[180:183], a[160:163], a[0:3], v[220:223]\n"
    "v_exp_f32_e32 v178, v178\n"
    "v_mfma_f32_16x16x32_bf16 v[180:183], a[164:167], a[4:7], v[180:183]\n"
    "ds_write_addtid_b32 v166 offset:3584\n"
    "v_mfma_f32_16x16x32_bf16 v[180:183], a[168:171], a[8:11], v[180:183]\n"
    "ds_write_addtid_b32 v167 offset:3840\n"
    "v_mfma_f32_16x16x32_bf16 v[180:183], a[172:175], a[12:15], v[180:183]\n"
    "s_waitcnt lgkmcnt(4)\n"
    "v_exp_f32_e32 v179, v179\n"
    "v_mfma_f32_16x16x32_bf16 v[180:183], a[176:179], a[16:19], v[180:183]\n"
    "ds_read_b128 a[160:163], v240 offset:10240\n"
    "v_mfma_f32_16x16x32_bf16 v[180:183], a[180:183], a[20:23], v[180:183]\n"
    "v_cvt_pk_bf16_f32 v174, v176, v177\n"
    "v_mfma_f32_16x16x32_bf16 v[180:183], a[184:187], a[24:27], v[180:183]\n"
    "ds_read_b128 a[168:171], v240 offset:14336\n"
    "v_mfma_f32_16x16x32_bf16 v[180:183], a[188:191], a[28:31], v[180:183]\n"
    "ds_write_addtid_b32 v174 offset:4608\n"
    "v_mfma_f32_16x16x32_bf16 v[188:191], a[188:191], a[60:63], v[188:191]\n"
    "v_cvt_pk_bf16_f32 v175, v178, v179\n"
    "v_mfma_f32_16x16x32_bf16 v[188:191], a[184:187], a[56:59], v[188:191]\n"
    "ds_read_b128 a[164:167], v241 offset:10240\n"
    "v_mfma_f32_16x16x32_bf16 v[188:191], a[180:183], a[52:55], v[188:191]\n"
    "ds_write_addtid_b32 v175 offset:4864\n"
    "v_mfma_f32_16x16x32_bf16 v[188:191], a[176:179], a[48:51], v[188:191]\n"
    "ds_read_b128 a[172:175], v241 offset:14336\n"
    "v_mfma_f32_16x16x32_bf16 v[196:199], a[176:179], a[80:83], v[196:199]\n"
    "v_mfma_f32_16x16x32_bf16 v[196:199], a[180:183], a[84:87], v[196:199]\n"
    "s_waitcnt lgkmcnt(1)\n"
    "v_mfma_f32_16x16x32_bf16 v[196:199], a[184:187], a[88:91], v[196:199]\n"
    "s_barrier\n"
    "v_mfma_f32_16x16x32_bf16 v[196:199], a[188:191], a[92:95], v[196:199]\n"
    "s_add_u32 m0, s43, 24576\n"
    "v_mfma_f32_16x16x32_bf16 v[204:207], a[188:191], a[124:127], v[204:207]\n"
    "ds_read2st64_b32 v[144:145], v249 offset0:0 offset1:1\n"
    "v_mfma_f32_16x16x32_bf16 v[204:207], a[184:187], a[120:123], v[204:207]\n"
    "buffer_load_dwordx4 v248, s[8:11], s40 offen offset:0 sc1 lds\n"
    "v_mfma_f32_16x16x32_bf16 v[204:207], a[180:183], a[116:119], v[204:207]\n"
    "v_mfma_f32_16x16x32_bf16 v[204:207], a[176:179], a[112:115], v[204:207]\n"
    "v_mfma_f32_16x16x32_bf16 v[212:215], a[176:179], a[144:147], v[212:215]\n"
    "ds_read2st64_b32 v[146:147], v249 offset0:2 offset1:3\n"
    "v_mfma_f32_16x16x32_bf16 v[212:215], a[180:183], a[148:151], v[212:215]\n"
    "buffer_load_dwordx4 v248, s[8:11], s40 offen offset:1024 sc1 lds\n"
    "v_mfma_f32_16x16x32_bf16 v[212:215], a[184:187], a[152:155], v[212:215]\n"
    "v_mfma_f32_16x16x32_bf16 v[212:215], a[188:191], a[156:159], v[212:215]\n"
    "s_waitcnt lgkmcnt(2)\n"
    "ds_read2st64_b32 v[152:153], v249 offset0:4 offset1:5\n"
    "v_mfma_f32_16x16x32_bf16 v[216:219], a[160:163], a[128:131], v[236:239]\n"
    "ds_read_b128 a[176:179], v242 offset:10240\n"
    "v_mfma_f32_16x16x32_bf16 v[216:219], a[164:167], a[132:135], v[216:219]\n"
    "buffer_load_dwordx4 v248, s[8:11], s40 offen offset:2048 sc1 lds\n"
    "v_mfma_f32_16x16x32_bf16 v[216:219], a[168:171], a[136:139], v[216:219]\n"
    "ds_read_b128 a[184:187], v242 offset:14336\n"
    "v_mfma_f32_16x16x32_bf16 v[216:219], a[172:175], a[140:143], v[216:219]\n"
    "v_exp_f32_e32 v180, v180\n"
    "v_mfma_f32_16x16x32_bf16 v[208:211], a[172:175], a[108:111], v[232:235]\n"
    "ds_read2st64_b32 v[154:155], v249 offset0:6 offset1:7\n"
    "v_mfma_f32_16x16x32_bf16 v[208:211], a[168:171], a[104:107], v[208:211]\n"
    "buffer_load_dwordx4 v248, s[8:11], s40 offen offset:3072 sc1 lds\n"
    "v_mfma_f32_16x16x32_bf16 v[208:211], a[164:167], a[100:103], v[208:211]\n"
    "s_add_u32 m0, s43, 28672\n"
    "ds_read_b128 a[180:183], v243 offset:10240\n"
    "v_mfma_f32_16x16x32_bf16 v[208:211], a[160:163], a[96:99], v[208:211]\n"
    "s_add_u32 s40, s40, 0x8000\n"
    "ds_read_b128 a[188:191], v243 offset:14336\n"
    "v_mfma_f32_16x16x32_bf16 v[200:203], a[160:163], a[64:67], v[228:231]\n"
    "ds_read2st64_b32 v[160:161], v249 offset0:8 offset1:9\n"
    "v_mfma_f32_16x16x32_bf16 v[200:203], a[164:167], a[68:71], v[200:203]\n"
    "buffer_load_dwordx4 v248, s[8:11], s41 offen offset:0 sc1 lds\n"
    "v_mfma_f32_16x16x32_bf16 v[200:203], a[168:171], a[72:75], v[200:203]\n"
    "v_exp_f32_e32 v181, v181\n"
    "v_mfma_f32_16x16x32_bf16 v[200:203], a[172:175], a[76:79], v[200:203]\n"
    "v_exp_f32_e32 v182, v182\n"
    "v_mfma_f32_16x16x32_bf16 v[192:195], a[172:175], a[44:47], v[224:227]\n"
    "ds_read2st64_b32 v[162:163], v249 offset0:10 offset1:11\n"
    "v_mfma_f32_16x16x32_bf16 v[192:195], a[168:171], a[40:43], v[192:195]\n"
    "buffer_load_dwordx4 v248, s[8:11], s41 offen offset:1024 sc1 lds\n"
    "v_mfma_f32_16x16x32_bf16 v[192:195], a[164:167], a[36:39], v[192:195]\n"
    "v_exp_f32_e32 v183, v183\n"
    "v_mfma_f32_16x16x32_bf16 v[192:195], a[160:163], a[32:35], v[192:195]\n"
    "v_cvt_pk_bf16_f32 v180, v180, v181\n"
    "v_mfma_f32_16x16x32_bf16 v[184:187], a[160:163], a[0:3], v[220:223]\n"
    "ds_read2st64_b32 v[168:169], v249 offset0:12 offset1:13\n"
    "v_mfma_f32_16x16x32_bf16 v[184:187], a[164:167], a[4:7], v[184:187]\n"
    "buffer_load_dwordx4 v248, s[8:11], s41 offen offset:2048 sc1 lds\n"
    "v_mfma_f32_16x16x32_bf16 v[184:187], a[168:171], a[8:11], v[184:187]\n"
    "ds_read_b64_tr_b16 a[160:161], v244 offset:0\n"
    "v_mfma_f32_16x16x32_bf16 v[184:187], a[172:175], a[12:15], v[184:187]\n"
    "s_waitcnt lgkmcnt(4)\n"
    "ds_read2st64_b32 v[170:171], v249 offset0:14 offset1:15\n"
    "v_mfma_f32_16x16x32_bf16 v[184:187], a[176:179], a[16:19], v[184:187]\n"
    "ds_read_b64_tr_b16 a[168:169], v245 offset:0\n"
    "v_mfma_f32_16x16x32_bf16 v[184:187], a[180:183], a[20:23], v[184:187]\n"
    "buffer_load_dwordx4 v248, s[8:11], s41 offen offset:3072 sc1 lds\n"
    "v_mfma_f32_16x16x32_bf16 v[184:187], a[184:187], a[24:27], v[184:187]\n"
    "ds_read_b64_tr_b16 a[162:163], v244 offset:2048\n"
    "v_mfma_f32_16x16x32_bf16 v[184:187], a[188:191], a[28:31], v[184:187]\n"
    "s_add_u32 s41, s41, 0x8000\n"
    "ds_read2st64_b32 v[176:177], v249 offset0:16 offset1:17\n"
    "v_mfma_f32_16x16x32_bf16 v[192:195], a[188:191], a[60:63], v[192:195]\n"
    "ds_read_b64_tr_b16 a[164:165], v244 offset:32768\n"
    "v_mfma_f32_16x16x32_bf16 v[192:195], a[184:187], a[56:59], v[192:195]\n"
    "ds_read_b64_tr_b16 a[170:171], v245 offset:2048\n"
    "v_mfma_f32_16x16x32_bf16 v[192:195], a[180:183], a[52:55], v[192:195]\n"
    "v_exp_f32_e32 v188, v188\n"
    "v_mfma_f32_16x16x32_bf16 v[192:195], a[176:179], a[48:51], v[192:195]\n"
    "ds_read_b64_tr_b16 a[166:167], v244 offset:34816\n"
    "v_mfma_f32_16x16x32_bf16 v[200:203], a[176:179], a[80:83], v[200:203]\n"
    "ds_read_b64_tr_b16 a[172:173], v245 offset:32768\n"
    "v_mfma_f32_16x16x32_bf16 v[200:203], a[180:183], a[84:87], v[200:203]\n"
    "ds_read2st64_b32 v[178:179], v249 offset0:18 offset1:19\n"
    "v_mfma_f32_16x16x32_bf16 v[200:203], a[184:187], a[88:91], v[200:203]\n"
    "v_exp_f32_e32 v189, v189\n"
    "v_mfma_f32_16x16x32_bf16 v[200:203], a[188:191], a[92:95], v[200:203]\n"
    "ds_read_b64_tr_b16 a[174:175], v245 offset:34816\n"
    "v_cvt_pk_bf16_f32 v181, v182, v183\n"
    "v_mfma_f32_16x16x32_bf16 v[208:211], a[188:191], a[124:127], v[208:211]\n"
    "v_exp_f32_e32 v190, v190\n"
    "v_mfma_f32_16x16x32_bf16 v[208:211], a[184:187], a[120:123], v[208:211]\n"
    "v_exp_f32_e32 v191, v191\n"
    "v_mfma_f32_16x16x32_bf16 v[208:211], a[180:183], a[116:119], v[208:211]\n"
    "v_mfma_f32_16x16x32_bf16 v[208:211], a[176:179], a[112:115], v[208:211]\n"
    "v_cvt_pk_bf16_f32 v188, v188, v189\n"
    "v_mfma_f32_16x16x32_bf16 v[216:219], a[176:179], a[144:147], v[216:219]\n"
    "v_exp_f32_e32 v196, v196\n"
    "v_mfma_f32_16x16x32_bf16 v[216:219], a[180:183], a[148:151], v[216:219]\n"
    "v_exp_f32_e32 v197, v197\n"
    "v_mfma_f32_16x16x32_bf16 v[216:219], a[184:187], a[152:155], v[216:219]\n"
    "ds_read_b64_tr_b16 a[176:177], v246 offset:0\n"
    "v_mfma_f32_16x16x32_bf16 v[216:219], a[188:191], a[156:159], v[216:219]\n"
    "s_waitcnt lgkmcnt(2)\n"
    "v_cvt_pk_bf16_f32 v189, v190, v191\n"
    "v_mfma_f32_16x16x32_bf16 a[236:239], a[160:163], v[140:143], a[236:239]\n"
    "v_mfma_f32_16x16x32_bf16 a[236:239], a[164:167], v[144:147], a[236:239]\n"
    "ds_read_b64_tr_b16 a[178:179], v246 offset:2048\n"
    "v_mfma_f32_16x16x32_bf16 a[240:243], a[164:167], v[152:155], a[240:243]\n"
    "ds_read_b64_tr_b16 a[184:185], v247 offset:0\n"
    "v_mfma_f32_16x16x32_bf16 a[240:243], a[160:163], v[148:151], a[240:243]\n"
    "v_exp_f32_e32 v198, v198\n"
    "v_mfma_f32_16x16x32_bf16 a[244:247], a[160:163], v[156:159], a[244:247]\n"
    "ds_read_b64_tr_b16 a[180:181], v246 offset:32768\n"
    "v_mfma_f32_16x16x32_bf16 a[244:247], a[164:167], v[160:163], a[244:247]\n"
    "ds_read_b64_tr_b16 a[186:187], v247 offset:2048\n"
    "v_mfma_f32_16x16x32_bf16 a[248:251], a[164:167], v[168:171], a[248:251]\n"
    "v_exp_f32_e32 v199, v199\n"
    "v_mfma_f32_16x16x32_bf16 a[248:251], a[160:163], v[164:167], a[248:251]\n"
    "ds_read_b64_tr_b16 a[182:183], v246 offset:34816\n"
    "v_mfma_f32_16x16x32_bf16 a[252:255], a[160:163], v[172:175], a[252:255]\n"
    "ds_read_b64_tr_b16 a[188:189], v247 offset:32768\n"
    "v_mfma_f32_16x16x32_bf16 a[252:255], a[164:167], v[176:179], a[252:255]\n"
    "s_waitcnt lgkmcnt(7)\n"
    "v_cvt_pk_bf16_f32 v196, v196, v197\n"
    "v_mfma_f32_16x16x32_bf16 v[16:19], a[172:175], v[176:179], v[16:19]\n"
    "ds_read_b64_tr_b16 a[160:161], v244 offset:4096\n"
    "v_mfma_f32_16x16x32_bf16 v[16:19], a[168:171], v[172:175], v[16:19]\n"
    "ds_read_b64_tr_b16 a[190:191], v247 offset:34816\n"
    "v_mfma_f32_16x16x32_bf16 v[12:15], a[168:171], v[164:167], v[12:15]\n"
    "v_exp_f32_e32 v204, v204\n"
    "v_mfma_f32_16x16x32_bf16 v[12:15], a[172:175], v[168:171], v[12:15]\n"
    "v_exp_f32_e32 v205, v205\n"
    "v_mfma_f32_16x16x32_bf16 v[8:11], a[172:175], v[160:163], v[8:11]\n"
    "ds_read_b64_tr_b16 a[162:163], v244 offset:6144\n"
    "v_mfma_f32_16x16x32_bf16 v[8:11], a[168:171], v[156:159], v[8:11]\n"
    "v_cvt_pk_bf16_f32 v197, v198, v199\n"
    "v_mfma_f32_16x16x32_bf16 v[4:7], a[168:171], v[148:151], v[4:7]\n"
    "v_exp_f32_e32 v206, v206\n"
    "v_mfma_f32_16x16x32_bf16 v[4:7], a[172:175], v[152:155], v[4:7]\n"
    "v_exp_f32_e32 v207, v207\n"
    "v_mfma_f32_16x16x32_bf16 v[0:3], a[172:175], v[144:147], v[0:3]\n"
    "ds_read_b64_tr_b16 a[164:165], v244 offset:36864\n"
    "v_mfma_f32_16x16x32_bf16 v[0:3], a[168:171], v[140:143], v[0:3]\n"
    "s_waitcnt lgkmcnt(5)\n"
    "v_cvt_pk_bf16_f32 v204, v204, v205\n"
    "v_mfma_f32_16x16x32_bf16 v[20:23], a[176:179], v[140:143], v[20:23]\n"
    "buffer_load_dword v253, off, s[8:11], s82 sc1\n"
    "v_mfma_f32_16x16x32_bf16 v[20:23], a[180:183], v[144:147], v[20:23]\n"
    "ds_read_b64_tr_b16 a[166:167], v244 offset:38912\n"
    "v_mfma_f32_16x16x32_bf16 v[24:27], a[180:183], v[152:155], v[24:27]\n"
    "ds_read_b64_tr_b16 a[168:169], v245 offset:4096\n"
    "v_mfma_f32_16x16x32_bf16 v[24:27], a[176:179], v[148:151], v[24:27]\n"
    "v_exp_f32_e32 v212, v212\n"
    "v_mfma_f32_16x16x32_bf16 v[28:31], a[176:179], v[156:159], v[28:31]\n"
    "ds_read_b64_tr_b16 a[170:171], v245 offset:6144\n"
    "v_mfma_f32_16x16x32_bf16 v[28:31], a[180:183], v[160:163], v[28:31]\n"
    "v_exp_f32_e32 v213, v213\n"
    "v_mfma_f32_16x16x32_bf16 v[32:35], a[180:183], v[168:171], v[32:35]\n"
    "v_cvt_pk_bf16_f32 v205, v206, v207\n"
    "v_mfma_f32_16x16x32_bf16 v[32:35], a[176:179], v[164:167], v[32:35]\n"
    "v_exp_f32_e32 v214, v214\n"
    "v_mfma_f32_16x16x32_bf16 v[36:39], a[176:179], v[172:175], v[36:39]\n"
    "ds_read_b64_tr_b16 a[172:173], v245 offset:36864\n"
    "v_mfma_f32_16x16x32_bf16 v[36:39], a[180:183], v[176:179], v[36:39]\n"
    "s_waitcnt lgkmcnt(6)\n"
    "v_exp_f32_e32 v215, v215\n"
    "v_mfma_f32_16x16x32_bf16 v[56:59], a[188:191], v[176:179], v[56:59]\n"
    "ds_read_b64_tr_b16 a[176:177], v246 offset:4096\n"
    "v_mfma_f32_16x16x32_bf16 v[56:59], a[184:187], v[172:175], v[56:59]\n"
    "ds_read_b64_tr_b16 a[174:175], v245 offset:38912\n"
    "v_mfma_f32_16x16x32_bf16 v[52:55], a[184:187], v[164:167], v[52:55]\n"
    "v_cvt_pk_bf16_f32 v212, v212, v213\n"
    "v_mfma_f32_16x16x32_bf16 v[52:55], a[188:191], v[168:171], v[52:55]\n"
    "v_cvt_pk_bf16_f32 v213, v214, v215\n"
    "v_mfma_f32_16x16x32_bf16 v[48:51], a[188:191], v[160:163], v[48:51]\n"
    "ds_read_b64_tr_b16 a[178:179], v246 offset:6144\n"
    "v_mfma_f32_16x16x32_bf16 v[48:51], a[184:187], v[156:159], v[48:51]\n"
    "v_exp_f32_e32 v184, v184\n"
    "v_mfma_f32_16x16x32_bf16 v[44:47], a[184:187], v[148:151], v[44:47]\n"
    "v_exp_f32_e32 v185, v185\n"
    "v_mfma_f32_16x16x32_bf16 v[44:47], a[188:191], v[152:155], v[44:47]\n"
    "v_exp_f32_e32 v186, v186\n"
    "v_mfma_f32_16x16x32_bf16 v[40:43], a[188:191], v[144:147], v[40:43]\n"
    "ds_read_b64_tr_b16 a[180:181], v246 offset:36864\n"
    "v_mfma_f32_16x16x32_bf16 v[40:43], a[184:187], v[140:143], v[40:43]\n"
    "s_waitcnt lgkmcnt(7)\n"
    "v_exp_f32_e32 v187, v187\n"
    "v_mfma_f32_16x16x32_bf16 v[60:63], a[160:163], v[140:143], v[60:63]\n"
    "ds_read_b64_tr_b16 a[184:185], v247 offset:4096\n"
    "v_mfma_f32_16x16x32_bf16 v[60:63], a[164:167], v[144:147], v[60:63]\n"
    "ds_read_b64_tr_b16 a[182:183], v246 offset:38912\n"
    "v_mfma_f32_16x16x32_bf16 v[64:67], a[164:167], v[152:155], v[64:67]\n"
    "ds_read_b64_tr_b16 a[186:187], v247 offset:6144\n"
    "v_mfma_f32_16x16x32_bf16 v[64:67], a[160:163], v[148:151], v[64:67]\n"
    "v_cvt_pk_bf16_f32 v182, v184, v185\n"
    "v_mfma_f32_16x16x32_bf16 v[68:71], a[160:163], v[156:159], v[68:71]\n"
    "v_exp_f32_e32 v192, v192\n"
    "v_mfma_f32_16x16x32_bf16 v[68:71], a[164:167], v[160:163], v[68:71]\n"
    "ds_read_b64_tr_b16 a[188:189], v247 offset:36864\n"
    "v_mfma_f32_16x16x32_bf16 v[72:75], a[164:167], v[168:171], v[72:75]\n"
    "ds_read_b64_tr_b16 a[190:191], v247 offset:38912\n"
    "v_mfma_f32_16x16x32_bf16 v[72:75], a[160:163], v[164:167], v[72:75]\n"
    "s_waitcnt vmcnt(9)\n"
    "v_mfma_f32_16x16x32_bf16 v[76:79], a[160:163], v[172:175], v[76:79]\n"
    "s_barrier\n"
    "v_mfma_f32_16x16x32_bf16 v[76:79], a[164:167], v[176:179], v[76:79]\n"
    "s_waitcnt lgkmcnt(7)\n"
    "s_mov_b32 m0, s42\n"
    "v_mfma_f32_16x16x32_bf16 v[96:99], a[172:175], v[176:179], v[96:99]\n"
    "v_exp_f32_e32 v193, v193\n"
    "v_mfma_f32_16x16x32_bf16 v[96:99], a[168:171], v[172:175], v[96:99]\n"
    "v_cvt_pk_bf16_f32 v183, v186, v187\n"
    "v_mfma_f32_16x16x32_bf16 v[92:95], a[168:171], v[164:167], v[92:95]\n"
    "v_exp_f32_e32 v194, v194\n"
    "v_mfma_f32_16x16x32_bf16 v[92:95], a[172:175], v[168:171], v[92:95]\n"
    "ds_read_b128 a[160:163], v240 offset:16384\n"
    "v_mfma_f32_16x16x32_bf16 v[88:91], a[172:175], v[160:163], v[88:91]\n"
    "ds_write_addtid_b32 v180 offset:0\n"
    "v_mfma_f32_16x16x32_bf16 v[88:91], a[168:171], v[156:159], v[88:91]\n"
    "v_exp_f32_e32 v195, v195\n"
    "v_mfma_f32_16x16x32_bf16 v[84:87], a[168:171], v[148:151], v[84:87]\n"
    "ds_write_addtid_b32 v181 offset:256\n"
    "v_mfma_f32_16x16x32_bf16 v[84:87], a[172:175], v[152:155], v[84:87]\n"
    "ds_write_addtid_b32 v188 offset:1024\n"
    "v_mfma_f32_16x16x32_bf16 v[80:83], a[172:175], v[144:147], v[80:83]\n"
    "v_cvt_pk_bf16_f32 v190, v192, v193\n"
    "v_mfma_f32_16x16x32_bf16 v[80:83], a[168:171], v[140:143], v[80:83]\n"
    "s_waitcnt lgkmcnt(7)\n"
    "v_exp_f32_e32 v200, v200\n"
    "v_mfma_f32_16x16x32_bf16 v[100:103], a[176:179], v[140:143], v[100:103]\n"
    "ds_read_b128 a[164:167], v241 offset:16384\n"
    "v_mfma_f32_16x16x32_bf16 v[100:103], a[180:183], v[144:147], v[100:103]\n"
    "ds_write_addtid_b32 v189 offset:1280\n"
    "v_mfma_f32_16x16x32_bf16 v[104:107], a[180:183], v[152:155], v[104:107]\n"
    "ds_read_b128 a[168:171], v240 offset:20480\n"
    "v_mfma_f32_16x16x32_bf16 v[104:107], a[176:179], v[148:151], v[104:107]\n"
    "v_exp_f32_e32 v201, v201\n"
    "v_mfma_f32_16x16x32_bf16 v[108:111], a[176:179], v[156:159], v[108:111]\n"
    "ds_write_addtid_b32 v196 offset:2048\n"
    "v_mfma_f32_16x16x32_bf16 v[108:111], a[180:183], v[160:163], v[108:111]\n"
    "v_cvt_pk_bf16_f32 v191, v194, v195\n"
    "v_mfma_f32_16x16x32_bf16 v[112:115], a[180:183], v[168:171], v[112:115]\n"
    "ds_write_addtid_b32 v197 offset:2304\n"
    "v_mfma_f32_16x16x32_bf16 v[112:115], a[176:179], v[164:167], v[112:115]\n"
    "v_exp_f32_e32 v202, v202\n"
    "v_mfma_f32_16x16x32_bf16 v[116:119], a[176:179], v[172:175], v[116:119]\n"
    "ds_write_addtid_b32 v204 offset:3072\n"
    "v_mfma_f32_16x16x32_bf16 v[116:119], a[180:183], v[176:179], v[116:119]\n"
    "s_waitcnt lgkmcnt(10)\n"
    "ds_read_b128 a[172:175], v241 offset:20480\n"
    "v_mfma_f32_16x16x32_bf16 v[136:139], a[188:191], v[176:179], v[136:139]\n"
    "v_exp_f32_e32 v203, v203\n"
    "v_mfma_f32_16x16x32_bf16 v[136:139], a[184:187], v[172:175], v[136:139]\n"
    "ds_write_addtid_b32 v205 offset:3328\n"
    "v_cvt_pk_bf16_f32 v198, v200, v201\n"
    "v_mfma_f32_16x16x32_bf16 v[132:135], a[184:187], v[164:167], v[132:135]\n"
    "ds_read_b128 a[176:179], v242 offset:16384\n"
    "v_mfma_f32_16x16x32_bf16 v[132:135], a[188:191], v[168:171], v[132:135]\n"
    "ds_write_addtid_b32 v212 offset:4096\n"
    "v_mfma_f32_16x16x32_bf16 v[128:131], a[188:191], v[160:163], v[128:131]\n"
    "v_exp_f32_e32 v208, v208\n"
    "v_mfma_f32_16x16x32_bf16 v[128:131], a[184:187], v[156:159], v[128:131]\n"
    "ds_write_addtid_b32 v213 offset:4352\n"
    "v_mfma_f32_16x16x32_bf16 v[124:127], a[184:187], v[148:151], v[124:127]\n"
    "v_exp_f32_e32 v209, v209\n"
    "v_mfma_f32_16x16x32_bf16 v[124:127], a[188:191], v[152:155], v[124:127]\n"
    "ds_read_b128 a[180:183], v243 offset:16384\n"
    "v_mfma_f32_16x16x32_bf16 v[120:123], a[188:191], v[144:147], v[120:123]\n"
    "ds_write_addtid_b32 v182 offset:512\n"
    "v_mfma_f32_16x16x32_bf16 v[120:123], a[184:187], v[140:143], v[120:123]\n"
    "s_waitcnt lgkmcnt(6)\n"
    "v_cvt_pk_bf16_f32 v199, v202, v203\n"
    "v_mfma_f32_16x16x32_bf16 v[172:175], a[160:163], a[128:131], v[236:239]\n"
    "v_mfma_f32_16x16x32_bf16 v[172:175], a[164:167], a[132:135], v[172:175]\n"
    "v_exp_f32_e32 v210, v210\n"
    "v_mfma_f32_16x16x32_bf16 v[172:175], a[168:171], a[136:139], v[172:175]\n"
    "ds_write_addtid_b32 v183 offset:768\n"
    "v_mfma_f32_16x16x32_bf16 v[172:175], a[172:175], a[140:143], v[172:175]\n"
    "ds_read_b128 a[184:187], v242 offset:20480\n"
    "v_mfma_f32_16x16x32_bf16 v[164:167], a[172:175], a[108:111], v[232:235]\n"
    "v_exp_f32_e32 v211, v211\n"
    "v_mfma_f32_16x16x32_bf16 v[164:167], a[168:171], a[104:107], v[164:167]\n"
    "ds_write_addtid_b32 v190 offset:1536\n"
    "v_mfma_f32_16x16x32_bf16 v[164:167], a[164:167], a[100:103], v[164:167]\n"
    "v_cvt_pk_bf16_f32 v206, v208, v209\n"
    "v_mfma_f32_16x16x32_bf16 v[164:167], a[160:163], a[96:99], v[164:167]\n"
    "ds_write_addtid_b32 v191 offset:1792\n"
    "v_mfma_f32_16x16x32_bf16 v[156:159], a[160:163], a[64:67], v[228:231]\n"
    "v_exp_f32_e32 v216, v216\n"
    "v_mfma_f32_16x16x32_bf16 v[156:159], a[164:167], a[68:71], v[156:159]\n"
    "ds_read_b128 a[188:191], v243 offset:20480\n"
    "v_mfma_f32_16x16x32_bf16 v[156:159], a[168:171], a[72:75], v[156:159]\n"
    "ds_write_addtid_b32 v198 offset:2560\n"
    "v_mfma_f32_16x16x32_bf16 v[156:159], a[172:175], a[76:79], v[156:159]\n"
    "v_exp_f32_e32 v217, v217\n"
    "v_mfma_f32_16x16x32_bf16 v[148:151], a[172:175], a[44:47], v[224:227]\n"
    "v_mfma_f32_16x16x32_bf16 v[148:151], a[168:171], a[40:43], v[148:151]\n"
    "ds_write_addtid_b32 v199 offset:2816\n"
    "v_mfma_f32_16x16x32_bf16 v[148:151], a[164:167], a[36:39], v[148:151]\n"
    "v_cvt_pk_bf16_f32 v207, v210, v211\n"
    "v_mfma_f32_16x16x32_bf16 v[148:151], a[160:163], a[32:35], v[148:151]\n"
    "v_mfma_f32_16x16x32_bf16 v[140:143], a[160:163], a[0:3], v[220:223]\n"
    "v_exp_f32_e32 v218, v218\n"
    "v_mfma_f32_16x16x32_bf16 v[140:143], a[164:167], a[4:7], v[140:143]\n"
    "ds_write_addtid_b32 v206 offset:3584\n"
    "v_mfma_f32_16x16x32_bf16 v[140:143], a[168:171], a[8:11], v[140:143]\n"
    "ds_write_addtid_b32 v207 offset:3840\n"
    "v_mfma_f32_16x16x32_bf16 v[140:143], a[172:175], a[12:15], v[140:143]\n"
    "s_waitcnt lgkmcnt(4)\n"
    "v_exp_f32_e32 v219, v219\n"
    "v_mfma_f32_16x16x32_bf16 v[140:143], a[176:179], a[16:19], v[140:143]\n"
    "ds_read_b128 a[160:163], v240 offset:18432\n"
    "v_mfma_f32_16x16x32_bf16 v[140:143], a[180:183], a[20:23], v[140:143]\n"
    "v_cvt_pk_bf16_f32 v214, v216, v217\n"
    "v_mfma_f32_16x16x32_bf16 v[140:143], a[184:187], a[24:27], v[140:143]\n"
    "ds_read_b128 a[168:171], v240 offset:22528\n"
    "v_mfma_f32_16x16x32_bf16 v[140:143], a[188:191], a[28:31], v[140:143]\n"
    "ds_write_addtid_b32 v214 offset:4608\n"
    "v_mfma_f32_16x16x32_bf16 v[148:151], a[188:191], a[60:63], v[148:151]\n"
    "v_cvt_pk_bf16_f32 v215, v218, v219\n"
    "v_mfma_f32_16x16x32_bf16 v[148:151], a[184:187], a[56:59], v[148:151]\n"
    "ds_read_b128 a[164:167], v241 offset:18432\n"
    "v_mfma_f32_16x16x32_bf16 v[148:151], a[180:183], a[52:55], v[148:151]\n"
    "ds_write_addtid_b32 v215 offset:4864\n"
    "v_mfma_f32_16x16x32_bf16 v[148:151], a[176:179], a[48:51], v[148:151]\n"
    "ds_read_b128 a[172:175], v241 offset:22528\n"
    "v_mfma_f32_16x16x32_bf16 v[156:159], a[176:179], a[80:83], v[156:159]\n"
    "v_mfma_f32_16x16x32_bf16 v[156:159], a[180:183], a[84:87], v[156:159]\n"
    "s_waitcnt lgkmcnt(1)\n"
    "v_mfma_f32_16x16x32_bf16 v[156:159], a[184:187], a[88:91], v[156:159]\n"
    "s_barrier\n"
    "v_mfma_f32_16x16x32_bf16 v[156:159], a[188:191], a[92:95], v[156:159]\n"
    "s_add_u32 m0, s43, 0\n"
    "v_mfma_f32_16x16x32_bf16 v[164:167], a[188:191], a[124:127], v[164:167]\n"
    "ds_read2st64_b32 v[184:185], v249 offset0:0 offset1:1\n"
    "v_mfma_f32_16x16x32_bf16 v[164:167], a[184:187], a[120:123], v[164:167]\n"
    "buffer_load_dwordx4 v248, s[8:11], s40 offen offset:0 sc1 lds\n"
    "v_mfma_f32_16x16x32_bf16 v[164:167], a[180:183], a[116:119], v[164:167]\n"
    "v_mfma_f32_16x16x32_bf16 v[164:167], a[176:179], a[112:115], v[164:167]\n"
    "v_mfma_f32_16x16x32_bf16 v[172:175], a[176:179], a[144:147], v[172:175]\n"
    "ds_read2st64_b32 v[186:187], v249 offset0:2 offset1:3\n"
    "v_mfma_f32_16x16x32_bf16 v[172:175], a[180:183], a[148:151], v[172:175]\n"
    "buffer_load_dwordx4 v248, s[8:11], s40 offen offset:1024 sc1 lds\n"
    "v_mfma_f32_16x16x32_bf16 v[172:175], a[184:187], a[152:155], v[172:175]\n"
    "v_mfma_f32_16x16x32_bf16 v[172:175], a[188:191], a[156:159], v[172:175]\n"
    "s_waitcnt lgkmcnt(2)\n"
    "ds_read2st64_b32 v[192:193], v249 offset0:4 offset1:5\n"
    "v_mfma_f32_16x16x32_bf16 v[176:179], a[160:163], a[128:131], v[236:239]\n"
    "ds_read_b128 a[176:179], v242 offset:18432\n"
    "v_mfma_f32_16x16x32_bf16 v[176:179], a[164:167], a[132:135], v[176:179]\n"
    "buffer_load_dwordx4 v248, s[8:11], s40 offen offset:2048 sc1 lds\n"
    "v_mfma_f32_16x16x32_bf16 v[176:179], a[168:171], a[136:139], v[176:179]\n"
    "ds_read_b128 a[184:187], v242 offset:22528\n"
    "v_mfma_f32_16x16x32_bf16 v[176:179], a[172:175], a[140:143], v[176:179]\n"
    "v_exp_f32_e32 v140, v140\n"
    "v_mfma_f32_16x16x32_bf16 v[168:171], a[172:175], a[108:111], v[232:235]\n"
    "ds_read2st64_b32 v[194:195], v249 offset0:6 offset1:7\n"
    "v_mfma_f32_16x16x32_bf16 v[168:171], a[168:171], a[104:107], v[168:171]\n"
    "buffer_load_dwordx4 v248, s[8:11], s40 offen offset:3072 sc1 lds\n"
    "v_mfma_f32_16x16x32_bf16 v[168:171], a[164:167], a[100:103], v[168:171]\n"
    "s_add_u32 m0, s43, 4096\n"
    "ds_read_b128 a[180:183], v243 offset:18432\n"
    "v_mfma_f32_16x16x32_bf16 v[168:171], a[160:163], a[96:99], v[168:171]\n"
    "s_add_u32 s40, s40, 0x8000\n"
    "ds_read_b128 a[188:191], v243 offset:22528\n"
    "v_mfma_f32_16x16x32_bf16 v[160:163], a[160:163], a[64:67], v[228:231]\n"
    "ds_read2st64_b32 v[200:201], v249 offset0:8 offset1:9\n"
    "v_mfma_f32_16x16x32_bf16 v[160:163], a[164:167], a[68:71], v[160:163]\n"
    "buffer_load_dwordx4 v248, s[8:11], s41 offen offset:0 sc1 lds\n"
    "v_mfma_f32_16x16x32_bf16 v[160:163], a[168:171], a[72:75], v[160:163]\n"
    "v_exp_f32_e32 v141, v141\n"
    "v_mfma_f32_16x16x32_bf16 v[160:163], a[172:175], a[76:79], v[160:163]\n"
    "v_exp_f32_e32 v142, v142\n"
    "v_mfma_f32_16x16x32_bf16 v[152:155], a[172:175], a[44:47], v[224:227]\n"
    "ds_read2st64_b32 v[202:203], v249 offset0:10 offset1:11\n"
    "v_mfma_f32_16x16x32_bf16 v[152:155], a[168:171], a[40:43], v[152:155]\n"
    "buffer_load_dwordx4 v248, s[8:11], s41 offen offset:1024 sc1 lds\n"
    "v_mfma_f32_16x16x32_bf16 v[152:155], a[164:167], a[36:39], v[152:155]\n"
    "v_exp_f32_e32 v143, v143\n"
    "v_mfma_f32_16x16x32_bf16 v[152:155], a[160:163], a[32:35], v[152:155]\n"
    "v_cvt_pk_bf16_f32 v140, v140, v141\n"
    "v_mfma_f32_16x16x32_bf16 v[144:147], a[160:163], a[0:3], v[220:223]\n"
    "ds_read2st64_b32 v[208:209], v249 offset0:12 offset1:13\n"
    "v_mfma_f32_16x16x32_bf16 v[144:147], a[164:167], a[4:7], v[144:147]\n"
    "buffer_load_dwordx4 v248, s[8:11], s41 offen offset:2048 sc1 lds\n"
    "v_mfma_f32_16x16x32_bf16 v[144:147], a[168:171], a[8:11], v[144:147]\n"
    "ds_read_b64_tr_b16 a[160:161], v244 offset:8192\n"
    "v_mfma_f32_16x16x32_bf16 v[144:147], a[172:175], a[12:15], v[144:147]\n"
    "s_waitcnt lgkmcnt(4)\n"
    "ds_read2st64_b32 v[210:211], v249 offset0:14 offset1:15\n"
    "v_mfma_f32_16x16x32_bf16 v[144:147], a[176:179], a[16:19], v[144:147]\n"
    "ds_read_b64_tr_b16 a[168:169], v245 offset:8192\n"
    "v_mfma_f32_16x16x32_bf16 v[144:147], a[180:183], a[20:23], v[144:147]\n"
    "buffer_load_dwordx4 v248, s[8:11], s41 offen offset:3072 sc1 lds\n"
    "v_mfma_f32_16x16x32_bf16 v[144:147], a[184:187], a[24:27], v[144:147]\n"
    "ds_read_b64_tr_b16 a[162:163], v244 offset:10240\n"
    "v_mfma_f32_16x16x32_bf16 v[144:147], a[188:191], a[28:31], v[144:147]\n"
    "s_add_u32 s41, s41, 0x8000\n"
    "ds_read2st64_b32 v[216:217], v249 offset0:16 offset1:17\n"
    "v_mfma_f32_16x16x32_bf16 v[152:155], a[188:191], a[60:63], v[152:155]\n"
    "ds_read_b64_tr_b16 a[164:165], v244 offset:40960\n"
    "v_mfma_f32_16x16x32_bf16 v[152:155], a[184:187], a[56:59], v[152:155]\n"
    "ds_read_b64_tr_b16 a[170:171], v245 offset:10240\n"
    "v_mfma_f32_16x16x32_bf16 v[152:155], a[180:183], a[52:55], v[152:155]\n"
    "v_exp_f32_e32 v148, v148\n"
    "v_mfma_f32_16x16x32_bf16 v[152:155], a[176:179], a[48:51], v[152:155]\n"
    "ds_read_b64_tr_b16 a[166:167], v244 offset:43008\n"
    "v_mfma_f32_16x16x32_bf16 v[160:163], a[176:179], a[80:83], v[160:163]\n"
    "ds_read_b64_tr_b16 a[172:173], v245 offset:40960\n"
    "v_mfma_f32_16x16x32_bf16 v[160:163], a[180:183], a[84:87], v[160:163]\n"
    "ds_read2st64_b32 v[218:219], v249 offset0:18 offset1:19\n"
    "v_mfma_f32_16x16x32_bf16 v[160:163], a[184:187], a[88:91], v[160:163]\n"
    "v_exp_f32_e32 v149, v149\n"
    "v_mfma_f32_16x16x32_bf16 v[160:163], a[188:191], a[92:95], v[160:163]\n"
    "ds_read_b64_tr_b16 a[174:175], v245 offset:43008\n"
    "v_cvt_pk_bf16_f32 v141, v142, v143\n"
    "v_mfma_f32_16x16x32_bf16 v[168:171], a[188:191], a[124:127], v[168:171]\n"
    "v_exp_f32_e32 v150, v150\n"
    "v_mfma_f32_16x16x32_bf16 v[168:171], a[184:187], a[120:123], v[168:171]\n"
    "v_exp_f32_e32 v151, v151\n"
    "v_mfma_f32_16x16x32_bf16 v[168:171], a[180:183], a[116:119], v[168:171]\n"
    "s_waitcnt vmcnt(8)\n"
    "v_readfirstlane_b32 s81, v253\n"
    "v_mfma_f32_16x16x32_bf16 v[168:171], a[176:179], a[112:115], v[168:171]\n"
    "v_cvt_pk_bf16_f32 v148, v148, v149\n"
    "v_mfma_f32_16x16x32_bf16 v[176:179], a[176:179], a[144:147], v[176:179]\n"
    "s_cmp_eq_u32 s81, s39\n"
    "v_exp_f32_e32 v156, v156\n"
    "v_mfma_f32_16x16x32_bf16 v[176:179], a[180:183], a[148:151], v[176:179]\n"
    "s_cselect_b32 s88, s83, 2.0\n"
    "v_exp_f32_e32 v157, v157\n"
    "v_mfma_f32_16x16x32_bf16 v[176:179], a[184:187], a[152:155], v[176:179]\n"
    "s_mov_b32 m0, s30\n"
    "ds_read_b64_tr_b16 a[176:177], v246 offset:8192\n"
    "v_mfma_f32_16x16x32_bf16 v[176:179], a[188:191], a[156:159], v[176:179]\n"
    "s_waitcnt lgkmcnt(2)\n"
    "v_cvt_pk_bf16_f32 v149, v150, v151\n"
    "v_mfma_f32_16x16x32_bf16 a[236:239], a[160:163], v[180:183], a[236:239]\n"
    "buffer_load_dwordx4 v250, s[8:11], s88 offen offset:0 sc1 lds\n"
    "v_mfma_f32_16x16x32_bf16 a[236:239], a[164:167], v[184:187], a[236:239]\n"
    "ds_read_b64_tr_b16 a[178:179], v246 offset:10240\n"
    "v_mfma_f32_16x16x32_bf16 a[240:243], a[164:167], v[192:195], a[240:243]\n"
    "ds_read_b64_tr_b16 a[184:185], v247 offset:8192\n"
    "v_mfma_f32_16x16x32_bf16 a[240:243], a[160:163], v[188:191], a[240:243]\n"
    "v_exp_f32_e32 v158, v158\n"
    "v_mfma_f32_16x16x32_bf16 a[244:247], a[160:163], v[196:199], a[244:247]\n"
    "ds_read_b64_tr_b16 a[180:181], v246 offset:40960\n"
    "v_mfma_f32_16x16x32_bf16 a[244:247], a[164:167], v[200:203], a[244:247]\n"
    "ds_read_b64_tr_b16 a[186:187], v247 offset:10240\n"
    "v_mfma_f32_16x16x32_bf16 a[248:251], a[164:167], v[208:211], a[248:251]\n"
    "v_exp_f32_e32 v159, v159\n"
    "v_mfma_f32_16x16x32_bf16 a[248:251], a[160:163], v[204:207], a[248:251]\n"
    "ds_read_b64_tr_b16 a[182:183], v246 offset:43008\n"
    "v_mfma_f32_16x16x32_bf16 a[252:255], a[160:163], v[212:215], a[252:255]\n"
    "ds_read_b64_tr_b16 a[188:189], v247 offset:40960\n"
    "v_mfma_f32_16x16x32_bf16 a[252:255], a[164:167], v[216:219], a[252:255]\n"
    "s_waitcnt lgkmcnt(7)\n"
    "v_cvt_pk_bf16_f32 v156, v156, v157\n"
    "v_mfma_f32_16x16x32_bf16 v[16:19], a[172:175], v[216:219], v[16:19]\n"
    "ds_read_b64_tr_b16 a[160:161], v244 offset:12288\n"
    "v_mfma_f32_16x16x32_bf16 v[16:19], a[168:171], v[212:215], v[16:19]\n"
    "ds_read_b64_tr_b16 a[190:191], v247 offset:43008\n"
    "v_mfma_f32_16x16x32_bf16 v[12:15], a[168:171], v[204:207], v[12:15]\n"
    "v_exp_f32_e32 v164, v164\n"
    "v_mfma_f32_16x16x32_bf16 v[12:15], a[172:175], v[208:211], v[12:15]\n"
    "v_exp_f32_e32 v165, v165\n"
    "v_mfma_f32_16x16x32_bf16 v[8:11], a[172:175], v[200:203], v[8:11]\n"
    "ds_read_b64_tr_b16 a[162:163], v244 offset:14336\n"
    "v_mfma_f32_16x16x32_bf16 v[8:11], a[168:171], v[196:199], v[8:11]\n"
    "v_cvt_pk_bf16_f32 v157, v158, v159\n"
    "v_mfma_f32_16x16x32_bf16 v[4:7], a[168:171], v[188:191], v[4:7]\n"
    "v_exp_f32_e32 v166, v166\n"
    "v_mfma_f32_16x16x32_bf16 v[4:7], a[172:175], v[192:195], v[4:7]\n"
    "v_exp_f32_e32 v167, v167\n"
    "v_mfma_f32_16x16x32_bf16 v[0:3], a[172:175], v[184:187], v[0:3]\n"
    "ds_read_b64_tr_b16 a[164:165], v244 offset:45056\n"
    "v_mfma_f32_16x16x32_bf16 v[0:3], a[168:171], v[180:183], v[0:3]\n"
    "s_waitcnt lgkmcnt(5)\n"
    "v_cvt_pk_bf16_f32 v164, v164, v165\n"
    "v_mfma_f32_16x16x32_bf16 v[20:23], a[176:179], v[180:183], v[20:23]\n"
    "v_mfma_f32_16x16x32_bf16 v[20:23], a[180:183], v[184:187], v[20:23]\n"
    "ds_read_b64_tr_b16 a[166:167], v244 offset:47104\n"
    "v_mfma_f32_16x16x32_bf16 v[24:27], a[180:183], v[192:195], v[24:27]\n"
    "ds_read_b64_tr_b16 a[168:169], v245 offset:12288\n"
    "v_mfma_f32_16x16x32_bf16 v[24:27], a[176:179], v[188:191], v[24:27]\n"
    "v_exp_f32_e32 v172, v172\n"
    "v_mfma_f32_16x16x32_bf16 v[28:31], a[176:179], v[196:199], v[28:31]\n"
    "ds_read_b64_tr_b16 a[170:171], v245 offset:14336\n"
    "v_mfma_f32_16x16x32_bf16 v[28:31], a[180:183], v[200:203], v[28:31]\n"
    "v_exp_f32_e32 v173, v173\n"
    "v_mfma_f32_16x16x32_bf16 v[32:35], a[180:183], v[208:211], v[32:35]\n"
    "v_cvt_pk_bf16_f32 v165, v166, v167\n"
    "v_mfma_f32_16x16x32_bf16 v[32:35], a[176:179], v[204:207], v[32:35]\n"
    "v_exp_f32_e32 v174, v174\n"
    "v_mfma_f32_16x16x32_bf16 v[36:39], a[176:179], v[212:215], v[36:39]\n"
    "ds_read_b64_tr_b16 a[172:173], v245 offset:45056\n"
    "v_mfma_f32_16x16x32_bf16 v[36:39], a[180:183], v[216:219], v[36:39]\n"
    "s_waitcnt lgkmcnt(6)\n"
    "v_exp_f32_e32 v175, v175\n"
    "v_mfma_f32_16x16x32_bf16 v[56:59], a[188:191], v[216:219], v[56:59]\n"
    "ds_read_b64_tr_b16 a[176:177], v246 offset:12288\n"
    "v_mfma_f32_16x16x32_bf16 v[56:59], a[184:187], v[212:215], v[56:59]\n"
    "ds_read_b64_tr_b16 a[174:175], v245 offset:47104\n"
    "v_mfma_f32_16x16x32_bf16 v[52:55], a[184:187], v[204:207], v[52:55]\n"
    ".long 0xbedf00ff\n"
    ".Lat_w5:\n"
    "s_setpc_b64 s[2:3]\n"
    "v_cvt_pk_bf16_f32 v172, v172, v173\n"
    "v_mfma_f32_16x16x32_bf16 v[52:55], a[188:191], v[208:211], v[52:55]\n"
    "v_cvt_pk_bf16_f32 v173, v174, v175\n"
    "v_mfma_f32_16x16x32_bf16 v[48:51], a[188:191], v[200:203], v[48:51]\n"
    "ds_read_b64_tr_b16 a[178:179], v246 offset:14336\n"
    "v_mfma_f32_16x16x32_bf16 v[48:51], a[184:187], v[196:199], v[48:51]\n"
    "v_exp_f32_e32 v144, v144\n"
    "v_mfma_f32_16x16x32_bf16 v[44:47], a[184:187], v[188:191], v[44:47]\n"
    "v_exp_f32_e32 v145, v145\n"
    "v_mfma_f32_16x16x32_bf16 v[44:47], a[188:191], v[192:195], v[44:47]\n"
    "v_exp_f32_e32 v146, v146\n"
    "v_mfma_f32_16x16x32_bf16 v[40:43], a[188:191], v[184:187], v[40:43]\n"
    "ds_read_b64_tr_b16 a[180:181], v246 offset:45056\n"
    "v_mfma_f32_16x16x32_bf16 v[40:43], a[184:187], v[180:183], v[40:43]\n"
    "s_waitcnt lgkmcnt(7)\n"
    "v_exp_f32_e32 v147, v147\n"
    "v_mfma_f32_16x16x32_bf16 v[60:63], a[160:163], v[180:183], v[60:63]\n"
    "ds_read_b64_tr_b16 a[184:185], v247 offset:12288\n"
    "v_mfma_f32_16x16x32_bf16 v[60:63], a[164:167], v[184:187], v[60:63]\n"
    "ds_read_b64_tr_b16 a[182:183], v246 offset:47104\n"
    "v_mfma_f32_16x16x32_bf16 v[64:67], a[164:167], v[192:195], v[64:67]\n"
    "ds_read_b64_tr_b16 a[186:187], v247 offset:14336\n"
    "v_mfma_f32_16x16x32_bf16 v[64:67], a[160:163], v[188:191], v[64:67]\n"
    "v_cvt_pk_bf16_f32 v142, v144, v145\n"
    "v_mfma_f32_16x16x32_bf16 v[68:71], a[160:163], v[196:199], v[68:71]\n"
    "v_exp_f32_e32 v152, v152\n"
    "v_mfma_f32_16x16x32_bf16 v[68:71], a[164:167], v[200:203], v[68:71]\n"
    "ds_read_b64_tr_b16 a[188:189], v247 offset:45056\n"
    "v_mfma_f32_16x16x32_bf16 v[72:75], a[164:167], v[208:211], v[72:75]\n"
    "ds_read_b64_tr_b16 a[190:191], v247 offset:47104\n"
    "v_mfma_f32_16x16x32_bf16 v[72:75], a[160:163], v[204:207], v[72:75]\n"
    "s_waitcnt vmcnt(10)\n"
    "v_mfma_f32_16x16x32_bf16 v[76:79], a[160:163], v[212:215], v[76:79]\n"
    "s_barrier\n"
    "v_mfma_f32_16x16x32_bf16 v[76:79], a[164:167], v[216:219], v[76:79]\n"
    "s_waitcnt lgkmcnt(7)\n"
    "s_mov_b32 m0, s42\n"
    "v_mfma_f32_16x16x32_bf16 v[96:99], a[172:175], v[216:219], v[96:99]\n"
    "v_exp_f32_e32 v153, v153\n"
    "v_mfma_f32_16x16x32_bf16 v[96:99], a[168:171], v[212:215], v[96:99]\n"
    "v_cvt_pk_bf16_f32 v143, v146, v147\n"
    "v_mfma_f32_16x16x32_bf16 v[92:95], a[168:171], v[204:207], v[92:95]\n"
    "v_exp_f32_e32 v154, v154\n"
    "v_mfma_f32_16x16x32_bf16 v[92:95], a[172:175], v[208:211], v[92:95]\n"
    "ds_read_b128 a[160:163], v240 offset:24576\n"
    "v_mfma_f32_16x16x32_bf16 v[88:91], a[172:175], v[200:203], v[88:91]\n"
    "ds_write_addtid_b32 v140 offset:0\n"
    "v_mfma_f32_16x16x32_bf16 v[88:91], a[168:171], v[196:199], v[88:91]\n"
    "v_exp_f32_e32 v155, v155\n"
    "v_mfma_f32_16x16x32_bf16 v[84:87], a[168:171], v[188:191], v[84:87]\n"
    "ds_write_addtid_b32 v141 offset:256\n"
    "v_mfma_f32_16x16x32_bf16 v[84:87], a[172:175], v[192:195], v[84:87]\n"
    "ds_write_addtid_b32 v148 offset:1024\n"
    "v_mfma_f32_16x16x32_bf16 v[80:83], a[172:175], v[184:187], v[80:83]\n"
    "v_cvt_pk_bf16_f32 v150, v152, v153\n"
    "v_mfma_f32_16x16x32_bf16 v[80:83], a[168:171], v[180:183], v[80:83]\n"
    "s_waitcnt lgkmcnt(7)\n"
    "v_exp_f32_e32 v160, v160\n"
    "v_mfma_f32_16x16x32_bf16 v[100:103], a[176:179], v[180:183], v[100:103]\n"
    "ds_read_b128 a[164:167], v241 offset:24576\n"
    "v_mfma_f32_16x16x32_bf16 v[100:103], a[180:183], v[184:187], v[100:103]\n"
    "ds_write_addtid_b32 v149 offset:1280\n"
    "v_mfma_f32_16x16x32_bf16 v[104:107], a[180:183], v[192:195], v[104:107]\n"
    "ds_read_b128 a[168:171], v240 offset:28672\n"
    "v_mfma_f32_16x16x32_bf16 v[104:107], a[176:179], v[188:191], v[104:107]\n"
    "v_exp_f32_e32 v161, v161\n"
    "v_mfma_f32_16x16x32_bf16 v[108:111], a[176:179], v[196:199], v[108:111]\n"
    "ds_write_addtid_b32 v156 offset:2048\n"
    "v_mfma_f32_16x16x32_bf16 v[108:111], a[180:183], v[200:203], v[108:111]\n"
    "v_cvt_pk_bf16_f32 v151, v154, v155\n"
    "v_mfma_f32_16x16x32_bf16 v[112:115], a[180:183], v[208:211], v[112:115]\n"
    "ds_write_addtid_b32 v157 offset:2304\n"
    "v_mfma_f32_16x16x32_bf16 v[112:115], a[176:179], v[204:207], v[112:115]\n"
    "v_exp_f32_e32 v162, v162\n"
    "v_mfma_f32_16x16x32_bf16 v[116:119], a[176:179], v[212:215], v[116:119]\n"
    "ds_write_addtid_b32 v164 offset:3072\n"
    "v_mfma_f32_16x16x32_bf16 v[116:119], a[180:183], v[216:219], v[116:119]\n"
    "s_waitcnt lgkmcnt(10)\n"
    "ds_read_b128 a[172:175], v241 offset:28672\n"
    "v_mfma_f32_16x16x32_bf16 v[136:139], a[188:191], v[216:219], v[136:139]\n"
    "v_exp_f32_e32 v163, v163\n"
    "v_mfma_f32_16x16x32_bf16 v[136:139], a[184:187], v[212:215], v[136:139]\n"
    "ds_write_addtid_b32 v165 offset:3328\n"
    "v_cvt_pk_bf16_f32 v158, v160, v161\n"
    "v_mfma_f32_16x16x32_bf16 v[132:135], a[184:187], v[204:207], v[132:135]\n"
    "ds_read_b128 a[176:179], v242 offset:24576\n"
    "v_mfma_f32_16x16x32_bf16 v[132:135], a[188:191], v[208:211], v[132:135]\n"
    "ds_write_addtid_b32 v172 offset:4096\n"
    "v_mfma_f32_16x16x32_bf16 v[128:131], a[188:191], v[200:203], v[128:131]\n"
    "v_exp_f32_e32 v168, v168\n"
    "v_mfma_f32_16x16x32_bf16 v[128:131], a[184:187], v[196:199], v[128:131]\n"
    "s_sub_u32 s44, s44, 1\n"
    "ds_write_addtid_b32 v173 offset:4352\n"
    "v_mfma_f32_16x16x32_bf16 v[124:127], a[184:187], v[188:191], v[124:127]\n"
    "s_cmp_eq_u32 s44, s80\n"
    "v_exp_f32_e32 v169, v169\n"
    "v_mfma_f32_16x16x32_bf16 v[124:127], a[188:191], v[192:195], v[124:127]\n"
    "s_cselect_b32 s88, s39, s81\n"
    "ds_read_b128 a[180:183], v243 offset:24576\n"
    "v_mfma_f32_16x16x32_bf16 v[120:123], a[188:191], v[184:187], v[120:123]\n"
    "s_cmp_lg_u32 s88, s39\n"
    "ds_write_addtid_b32 v142 offset:512\n"
    "v_mfma_f32_16x16x32_bf16 v[120:123], a[184:187], v[180:183], v[120:123]\n"
    "s_waitcnt lgkmcnt(6)\n"
    "v_cvt_pk_bf16_f32 v159, v162, v163\n"
    "s_cbranch_scc1 .Lat_loop\n"
    "s_cmp_eq_u32 s44, 0\n"
    "s_cbranch_scc1 .Lat_tail\n"
    "s_cmp_eq_u32 s85, 0\n"
    "s_cbranch_scc1 .Lat_loop\n"
    "s_cmp_lg_u32 s81, s39\n"
    "s_cbranch_scc1 .Lat_lp0\n"
    ".Lat_g1b:\n"
    "v_lshrrev_b32_e32 v228, 2, v250\n"
    "v_add_u32_e32 v254, s30, v228\n"
    "s_waitcnt vmcnt(0)\n"
    "ds_read_b32 v254, v254 offset:960\n"
    "v_lshrrev_b32_e32 v228, 9, v250\n"
    "v_lshl_add_u32 v251, v228, 2, s30\n"
    "v_and_b32_e32 v252, 0xf0, v250\n"
    "v_lshl_add_u32 v252, v228, 8, v252\n"
    "v_bfe_u32 v229, v250, 8, 1\n"
    "v_lshl_add_u32 v252, v229, 3, v252\n"
    "v_and_b32_e32 v239, 0x1f0, v250\n"
    "v_bfe_u32 v228, v250, 2, 4\n"
    "s_mov_b32 s88, 0xcedc\n"
    "v_bfe_u32 v238, s88, v228, 4\n"
    "v_mul_u32_u24_e32 v238, 0x1111, v238\n"
    "v_mov_b64_e32 v[224:225], 0\n"
    "v_mov_b64_e32 v[226:227], 0\n"
    "s_mov_b32 s85, 0\n"
    "s_mov_b32 s82, 2.0\n"
    "s_mov_b32 s83, 2.0\n"
    "s_mov_b32 s89, 4\n"
    "s_waitcnt lgkmcnt(0)\n"
    "v_readlane_b32 s88, v254, 0\n"
    "v_readlane_b32 s87, v254, 1\n"
    "v_readlane_b32 s84, v254, 2\n"
    "v_readlane_b32 s86, v254, 4\n"
    "s_nop 3\n"
    "s_min_u32 s88, s88, s44\n"
    "s_sub_u32 s80, s44, s88\n"
    "s_cmp_eq_u32 s88, 0\n"
    "s_cbranch_scc1 .Lat_g2\n"
    ".Lat_gloop:\n"
    "v_mfma_f32_16x16x32_bf16 v[212:215], a[160:163], a[128:131], v[220:223]\n"
    "ds_write_addtid_b32 v143 offset:768\n"
    "v_mfma_f32_16x16x32_bf16 v[212:215], a[164:167], a[132:135], v[212:215]\n"
    "v_exp_f32_e32 v170, v170\n"
    "v_mfma_f32_16x16x32_bf16 v[204:207], a[164:167], a[100:103], v[220:223]\n"
    "ds_write_addtid_b32 v150 offset:1536\n"
    "v_mfma_f32_16x16x32_bf16 v[204:207], a[160:163], a[96:99], v[204:207]\n"
    "ds_read_b128 a[184:187], v242 offset:28672\n"
    "v_mfma_f32_16x16x32_bf16 v[196:199], a[160:163], a[64:67], v[220:223]\n"
    "v_exp_f32_e32 v171, v171\n"
    "v_mfma_f32_16x16x32_bf16 v[196:199], a[164:167], a[68:71], v[196:199]\n"
    "ds_write_addtid_b32 v151 offset:1792\n"
    "v_cvt_pk_bf16_f32 v166, v168, v169\n"
    "v_mfma_f32_16x16x32_bf16 v[188:191], a[164:167], a[36:39], v[220:223]\n"
    "v_exp_f32_e32 v176, v176\n"
    "v_mfma_f32_16x16x32_bf16 v[188:191], a[160:163], a[32:35], v[188:191]\n"
    "v_exp_f32_e32 v177, v177\n"
    "v_mfma_f32_16x16x32_bf16 v[180:183], a[160:163], a[0:3], v[220:223]\n"
    "ds_write_addtid_b32 v158 offset:2560\n"
    "v_cvt_pk_bf16_f32 v167, v170, v171\n"
    "v_mfma_f32_16x16x32_bf16 v[180:183], a[164:167], a[4:7], v[180:183]\n"
    "s_waitcnt vmcnt(20)\n"
    "v_exp_f32_e32 v178, v178\n"
    "v_smfmac_f32_16x16x64_bf16 v[224:227], a[232:235], a[192:199], v238\n"
    "s_waitcnt lgkmcnt(10)\n"
    "ds_read_b128 a[188:191], v243 offset:28672\n"
    "v_mfma_f32_16x16x32_bf16 v[180:183], a[168:171], a[8:11], v[180:183]\n"
    "s_bitcmp1_b32 s86, 0\n"
    "ds_read_b128 a[160:163], v240 offset:26624\n"
    "v_mfma_f32_16x16x32_bf16 v[180:183], a[172:175], a[12:15], v[180:183]\n"
    "s_cselect_b64 exec, -1, 0\n"
    "v_cvt_pk_bf16_f32 v236, v224, v225\n"
    "v_cvt_pk_bf16_f32 v237, v226, v227\n"
    "v_mfma_f32_16x16x32_bf16 v[188:191], a[172:175], a[44:47], v[188:191]\n"
    "s_cselect_b32 s88, 512, 0\n"
    "v_mov_b64_e32 v[224:225], 0\n"
    "v_mov_b64_e32 v[226:227], 0\n"
    "v_mfma_f32_16x16x32_bf16 v[188:191], a[168:171], a[40:43], v[188:191]\n"
    "buffer_store_dwordx2 v[236:237], v252, s[8:11], s87 offen sc1\n"
    "v_mfma_f32_16x16x32_bf16 v[196:199], a[168:171], a[72:75], v[196:199]\n"
    "s_mov_b64 exec, -1\n"
    "ds_read2_b32 v[228:229], v251 offset0:0 offset1:2\n"
    "v_mfma_f32_16x16x32_bf16 v[196:199], a[172:175], a[76:79], v[196:199]\n"
    "s_add_u32 s87, s87, s88\n"
    "ds_read2_b32 v[230:231], v251 offset0:4 offset1:6\n"
    "v_mfma_f32_16x16x32_bf16 v[204:207], a[172:175], a[108:111], v[204:207]\n"
    "ds_read_b32 v232, v251 offset:32\n"
    "v_mfma_f32_16x16x32_bf16 v[204:207], a[168:171], a[104:107], v[204:207]\n"
    "v_exp_f32_e32 v179, v179\n"
    "v_mfma_f32_16x16x32_bf16 v[212:215], a[168:171], a[136:139], v[212:215]\n"
    "ds_write_addtid_b32 v159 offset:2816\n"
    "v_cvt_pk_bf16_f32 v174, v176, v177\n"
    "v_mfma_f32_16x16x32_bf16 v[212:215], a[172:175], a[140:143], v[212:215]\n"
    "s_waitcnt lgkmcnt(13)\n"
    "ds_read_b128 a[164:167], v241 offset:26624\n"
    "v_mfma_f32_16x16x32_bf16 v[212:215], a[176:179], a[144:147], v[212:215]\n"
    "ds_write_addtid_b32 v166 offset:3584\n"
    "v_cvt_pk_bf16_f32 v175, v178, v179\n"
    "v_mfma_f32_16x16x32_bf16 v[212:215], a[180:183], a[148:151], v[212:215]\n"
    "ds_read_b128 a[168:171], v240 offset:30720\n"
    "v_mfma_f32_16x16x32_bf16 v[204:207], a[180:183], a[116:119], v[204:207]\n"
    "s_waitcnt lgkmcnt(6)\n"
    "ds_write_addtid_b32 v167 offset:3840\n"
    "v_mad_u32_u16 v234, v228, s90, v239 op_sel:[0,0,0,0]\n"
    "v_mfma_f32_16x16x32_bf16 v[204:207], a[176:179], a[112:115], v[204:207]\n"
    "buffer_load_dwordx4 a[192:195], v234, s[20:23], 0 offen sc1\n"
    "v_mad_u32_u16 v235, v228, s90, v239 op_sel:[1,0,0,0]\n"
    "v_mfma_f32_16x16x32_bf16 v[196:199], a[176:179], a[80:83], v[196:199]\n"
    "ds_write_addtid_b32 v174 offset:4608\n"
    "v_mfma_f32_16x16x32_bf16 v[196:199], a[180:183], a[84:87], v[196:199]\n"
    "ds_write_addtid_b32 v175 offset:4864\n"
    "v_mfma_f32_16x16x32_bf16 v[188:191], a[180:183], a[52:55], v[188:191]\n"
    "buffer_load_dwordx4 a[196:199], v235, s[20:23], 0 offen sc1\n"
    "v_mfma_f32_16x16x32_bf16 v[188:191], a[176:179], a[48:51], v[188:191]\n"
    "v_mfma_f32_16x16x32_bf16 v[180:183], a[176:179], a[16:19], v[180:183]\n"
    "s_waitcnt lgkmcnt(0)\n"
    "v_mfma_f32_16x16x32_bf16 v[180:183], a[180:183], a[20:23], v[180:183]\n"
    "s_barrier\n"
    "v_mfma_f32_16x16x32_bf16 v[180:183], a[184:187], a[24:27], v[180:183]\n"
    "s_add_u32 m0, s43, 8192\n"
    "ds_read_b128 a[172:175], v241 offset:30720\n"
    "v_mfma_f32_16x16x32_bf16 v[180:183], a[188:191], a[28:31], v[180:183]\n"
    "ds_read2st64_b32 v[144:145], v249 offset0:0 offset1:1\n"
    "v_mfma_f32_16x16x32_bf16 v[188:191], a[188:191], a[60:63], v[188:191]\n"
    "buffer_load_dwordx4 v248, s[8:11], s40 offen offset:0 sc1 lds\n"
    "v_mfma_f32_16x16x32_bf16 v[188:191], a[184:187], a[56:59], v[188:191]\n"
    "ds_read_b128 a[176:179], v242 offset:26624\n"
    "v_mfma_f32_16x16x32_bf16 v[196:199], a[184:187], a[88:91], v[196:199]\n"
    "v_mfma_f32_16x16x32_bf16 v[196:199], a[188:191], a[92:95], v[196:199]\n"
    "v_mfma_f32_16x16x32_bf16 v[204:207], a[188:191], a[124:127], v[204:207]\n"
    "buffer_load_dwordx4 v248, s[8:11], s40 offen offset:1024 sc1 lds\n"
    "v_mfma_f32_16x16x32_bf16 v[204:207], a[184:187], a[120:123], v[204:207]\n"
    "ds_read2st64_b32 v[146:147], v249 offset0:2 offset1:3\n"
    "v_mfma_f32_16x16x32_bf16 v[212:215], a[184:187], a[152:155], v[212:215]\n"
    "v_mfma_f32_16x16x32_bf16 v[212:215], a[188:191], a[156:159], v[212:215]\n"
    "ds_read_b128 a[180:183], v243 offset:26624\n"
    "v_mfma_f32_16x16x32_bf16 v[216:219], a[160:163], a[128:131], v[220:223]\n"
    "buffer_load_dwordx4 v248, s[8:11], s40 offen offset:2048 sc1 lds\n"
    "v_mfma_f32_16x16x32_bf16 v[216:219], a[164:167], a[132:135], v[216:219]\n"
    "ds_read2st64_b32 v[152:153], v249 offset0:4 offset1:5\n"
    "v_mfma_f32_16x16x32_bf16 v[208:211], a[164:167], a[100:103], v[220:223]\n"
    "v_exp_f32_e32 v180, v180\n"
    "v_mfma_f32_16x16x32_bf16 v[208:211], a[160:163], a[96:99], v[208:211]\n"
    "ds_read_b128 a[184:187], v242 offset:30720\n"
    "v_mfma_f32_16x16x32_bf16 v[200:203], a[160:163], a[64:67], v[220:223]\n"
    "buffer_load_dwordx4 v248, s[8:11], s40 offen offset:3072 sc1 lds\n"
    "v_mfma_f32_16x16x32_bf16 v[200:203], a[164:167], a[68:71], v[200:203]\n"
    "s_add_u32 m0, s43, 12288\n"
    "ds_read2st64_b32 v[154:155], v249 offset0:6 offset1:7\n"
    "v_mfma_f32_16x16x32_bf16 v[192:195], a[164:167], a[36:39], v[220:223]\n"
    "s_add_u32 s40, s40, 0x8000\n"
    "ds_read2st64_b32 v[160:161], v249 offset0:8 offset1:9\n"
    "v_mfma_f32_16x16x32_bf16 v[192:195], a[160:163], a[32:35], v[192:195]\n"
    "ds_read2st64_b32 v[162:163], v249 offset0:10 offset1:11\n"
    "v_mfma_f32_16x16x32_bf16 v[184:187], a[160:163], a[0:3], v[220:223]\n"
    "buffer_load_dwordx4 v248, s[8:11], s41 offen offset:0 sc1 lds\n"
    "v_mfma_f32_16x16x32_bf16 v[184:187], a[164:167], a[4:7], v[184:187]\n"
    "s_waitcnt vmcnt(17)\n"
    "ds_read_b128 a[188:191], v243 offset:30720\n"
    "v_smfmac_f32_16x16x64_bf16 v[224:227], a[232:235], a[200:207], v238\n"
    "s_waitcnt lgkmcnt(10)\n"
    "v_mad_u32_u16 v234, v229, s90, v239 op_sel:[0,0,0,0]\n"
    "v_mfma_f32_16x16x32_bf16 v[184:187], a[168:171], a[8:11], v[184:187]\n"
    "s_bitcmp1_b32 s86, 1\n"
    "buffer_load_dwordx4 a[200:203], v234, s[20:23], 0 offen sc1\n"
    "v_mad_u32_u16 v235, v229, s90, v239 op_sel:[1,0,0,0]\n"
    "v_mfma_f32_16x16x32_bf16 v[184:187], a[172:175], a[12:15], v[184:187]\n"
    "s_cselect_b64 exec, -1, 0\n"
    "v_cvt_pk_bf16_f32 v236, v224, v225\n"
    "v_cvt_pk_bf16_f32 v237, v226, v227\n"
    "v_mfma_f32_16x16x32_bf16 v[192:195], a[172:175], a[44:47], v[192:195]\n"
    "s_cselect_b32 s88, 512, 0\n"
    "v_mov_b64_e32 v[224:225], 0\n"
    "v_mov_b64_e32 v[226:227], 0\n"
    "v_mfma_f32_16x16x32_bf16 v[192:195], a[168:171], a[40:43], v[192:195]\n"
    "buffer_store_dwordx2 v[236:237], v252, s[8:11], s87 offen sc1\n"
    "v_mfma_f32_16x16x32_bf16 v[200:203], a[168:171], a[72:75], v[200:203]\n"
    "s_mov_b64 exec, -1\n"
    "ds_read_b64_tr_b16 a[160:161], v244 offset:16384\n"
    "v_mfma_f32_16x16x32_bf16 v[200:203], a[172:175], a[76:79], v[200:203]\n"
    "s_add_u32 s87, s87, s88\n"
    "ds_read_b64_tr_b16 a[162:163], v244 offset:18432\n"
    "v_mfma_f32_16x16x32_bf16 v[208:211], a[172:175], a[108:111], v[208:211]\n"
    "buffer_load_dwordx4 v248, s[8:11], s41 offen offset:1024 sc1 lds\n"
    "v_mfma_f32_16x16x32_bf16 v[208:211], a[168:171], a[104:107], v[208:211]\n"
    "ds_read_b64_tr_b16 a[164:165], v244 offset:49152\n"
    "v_mfma_f32_16x16x32_bf16 v[216:219], a[168:171], a[136:139], v[216:219]\n"
    "ds_read2st64_b32 v[168:169], v249 offset0:12 offset1:13\n"
    "v_mfma_f32_16x16x32_bf16 v[216:219], a[172:175], a[140:143], v[216:219]\n"
    "s_waitcnt lgkmcnt(10)\n"
    "ds_read2st64_b32 v[170:171], v249 offset0:14 offset1:15\n"
    "v_mfma_f32_16x16x32_bf16 v[216:219], a[176:179], a[144:147], v[216:219]\n"
    "buffer_load_dwordx4 v248, s[8:11], s41 offen offset:2048 sc1 lds\n"
    "v_mfma_f32_16x16x32_bf16 v[216:219], a[180:183], a[148:151], v[216:219]\n"
    "ds_read_b64_tr_b16 a[166:167], v244 offset:51200\n"
    "v_mfma_f32_16x16x32_bf16 v[208:211], a[180:183], a[116:119], v[208:211]\n"
    "ds_read_b64_tr_b16 a[168:169], v245 offset:16384\n"
    "v_mfma_f32_16x16x32_bf16 v[208:211], a[176:179], a[112:115], v[208:211]\n"
    "v_exp_f32_e32 v181, v181\n"
    "v_mfma_f32_16x16x32_bf16 v[200:203], a[176:179], a[80:83], v[200:203]\n"
    "buffer_load_dwordx4 v248, s[8:11], s41 offen offset:3072 sc1 lds\n"
    "v_mfma_f32_16x16x32_bf16 v[200:203], a[180:183], a[84:87], v[200:203]\n"
    "ds_read_b64_tr_b16 a[170:171], v245 offset:18432\n"
    "v_mfma_f32_16x16x32_bf16 v[192:195], a[180:183], a[52:55], v[192:195]\n"
    "s_add_u32 s41, s41, 0x8000\n"
    "ds_read2st64_b32 v[176:177], v249 offset0:16 offset1:17\n"
    "v_mfma_f32_16x16x32_bf16 v[192:195], a[176:179], a[48:51], v[192:195]\n"
    "buffer_load_dwordx4 a[204:207], v235, s[20:23], 0 offen sc1\n"
    "v_mfma_f32_16x16x32_bf16 v[184:187], a[176:179], a[16:19], v[184:187]\n"
    "ds_read_b64_tr_b16 a[172:173], v245 offset:49152\n"
    "v_mfma_f32_16x16x32_bf16 v[184:187], a[180:183], a[20:23], v[184:187]\n"
    "s_waitcnt lgkmcnt(10)\n"
    "ds_read2st64_b32 v[178:179], v249 offset0:18 offset1:19\n"
    "v_mfma_f32_16x16x32_bf16 v[184:187], a[184:187], a[24:27], v[184:187]\n"
    "ds_read_b64_tr_b16 a[176:177], v246 offset:16384\n"
    "v_mfma_f32_16x16x32_bf16 v[184:187], a[188:191], a[28:31], v[184:187]\n"
    "ds_read_b64_tr_b16 a[174:175], v245 offset:51200\n"
    "v_mfma_f32_16x16x32_bf16 v[192:195], a[188:191], a[60:63], v[192:195]\n"
    "v_exp_f32_e32 v182, v182\n"
    "v_mfma_f32_16x16x32_bf16 v[192:195], a[184:187], a[56:59], v[192:195]\n"
    "v_exp_f32_e32 v183, v183\n"
    "v_mfma_f32_16x16x32_bf16 v[200:203], a[184:187], a[88:91], v[200:203]\n"
    "ds_read_b64_tr_b16 a[178:179], v246 offset:18432\n"
    "v_cvt_pk_bf16_f32 v180, v180, v181\n"
    "v_mfma_f32_16x16x32_bf16 v[200:203], a[188:191], a[92:95], v[200:203]\n"
    "v_exp_f32_e32 v188, v188\n"
    "v_mfma_f32_16x16x32_bf16 v[208:211], a[188:191], a[124:127], v[208:211]\n"
    "v_exp_f32_e32 v189, v189\n"
    "v_mfma_f32_16x16x32_bf16 v[208:211], a[184:187], a[120:123], v[208:211]\n"
    "v_cvt_pk_bf16_f32 v181, v182, v183\n"
    "v_mfma_f32_16x16x32_bf16 v[216:219], a[184:187], a[152:155], v[216:219]\n"
    "ds_read_b64_tr_b16 a[180:181], v246 offset:49152\n"
    "v_mfma_f32_16x16x32_bf16 v[216:219], a[188:191], a[156:159], v[216:219]\n"
    "s_waitcnt vmcnt(20)\n"
    "ds_read_b64_tr_b16 a[182:183], v246 offset:51200\n"
    "v_smfmac_f32_16x16x64_bf16 v[224:227], a[232:235], a[208:215], v238\n"
    "s_waitcnt lgkmcnt(5)\n"
    "v_mad_u32_u16 v234, v230, s90, v239 op_sel:[0,0,0,0]\n"
    "v_mfma_f32_16x16x32_bf16 a[236:239], a[160:163], v[140:143], a[236:239]\n"
    "s_bitcmp1_b32 s86, 2\n"
    "buffer_load_dwordx4 a[208:211], v234, s[20:23], 0 offen sc1\n"
    "v_mad_u32_u16 v235, v230, s90, v239 op_sel:[1,0,0,0]\n"
    "v_mfma_f32_16x16x32_bf16 a[236:239], a[164:167], v[144:147], a[236:239]\n"
    "s_cselect_b64 exec, -1, 0\n"
    "v_cvt_pk_bf16_f32 v236, v224, v225\n"
    "v_cvt_pk_bf16_f32 v237, v226, v227\n"
    "v_mfma_f32_16x16x32_bf16 a[240:243], a[164:167], v[152:155], a[240:243]\n"
    "s_cselect_b32 s88, 512, 0\n"
    "v_mov_b64_e32 v[224:225], 0\n"
    "v_mov_b64_e32 v[226:227], 0\n"
    "v_mfma_f32_16x16x32_bf16 a[240:243], a[160:163], v[148:151], a[240:243]\n"
    "buffer_store_dwordx2 v[236:237], v252, s[8:11], s87 offen sc1\n"
    "v_mfma_f32_16x16x32_bf16 a[244:247], a[160:163], v[156:159], a[244:247]\n"
    "s_mov_b64 exec, -1\n"
    "ds_read_b64_tr_b16 a[184:185], v247 offset:16384\n"
    "v_mfma_f32_16x16x32_bf16 a[244:247], a[164:167], v[160:163], a[244:247]\n"
    "s_add_u32 s87, s87, s88\n"
    "ds_read_b64_tr_b16 a[186:187], v247 offset:18432\n"
    "v_mfma_f32_16x16x32_bf16 a[248:251], a[164:167], v[168:171], a[248:251]\n"
    "buffer_load_dwordx4 a[212:215], v235, s[20:23], 0 offen sc1\n"
    "v_mfma_f32_16x16x32_bf16 a[248:251], a[160:163], v[164:167], a[248:251]\n"
    "ds_read_b64_tr_b16 a[188:189], v247 offset:49152\n"
    "v_mfma_f32_16x16x32_bf16 a[252:255], a[160:163], v[172:175], a[252:255]\n"
    "v_exp_f32_e32 v190, v190\n"
    "v_mfma_f32_16x16x32_bf16 a[252:255], a[164:167], v[176:179], a[252:255]\n"
    "s_waitcnt lgkmcnt(6)\n"
    "v_exp_f32_e32 v191, v191\n"
    "v_mfma_f32_16x16x32_bf16 v[16:19], a[172:175], v[176:179], v[16:19]\n"
    "ds_read_b64_tr_b16 a[190:191], v247 offset:51200\n"
    "v_cvt_pk_bf16_f32 v188, v188, v189\n"
    "v_mfma_f32_16x16x32_bf16 v[16:19], a[168:171], v[172:175], v[16:19]\n"
    "ds_read_b64_tr_b16 a[160:161], v244 offset:20480\n"
    "v_mfma_f32_16x16x32_bf16 v[12:15], a[168:171], v[164:167], v[12:15]\n"
    "v_exp_f32_e32 v196, v196\n"
    "v_mfma_f32_16x16x32_bf16 v[12:15], a[172:175], v[168:171], v[12:15]\n"
    "v_exp_f32_e32 v197, v197\n"
    "v_mfma_f32_16x16x32_bf16 v[8:11], a[172:175], v[160:163], v[8:11]\n"
    "ds_read_b64_tr_b16 a[162:163], v244 offset:22528\n"
    "v_cvt_pk_bf16_f32 v189, v190, v191\n"
    "v_mfma_f32_16x16x32_bf16 v[8:11], a[168:171], v[156:159], v[8:11]\n"
    "v_exp_f32_e32 v198, v198\n"
    "v_mfma_f32_16x16x32_bf16 v[4:7], a[168:171], v[148:151], v[4:7]\n"
    "v_exp_f32_e32 v199, v199\n"
    "v_mfma_f32_16x16x32_bf16 v[4:7], a[172:175], v[152:155], v[4:7]\n"
    "v_cvt_pk_bf16_f32 v196, v196, v197\n"
    "v_mfma_f32_16x16x32_bf16 v[0:3], a[172:175], v[144:147], v[0:3]\n"
    "ds_read_b64_tr_b16 a[164:165], v244 offset:53248\n"
    "v_mfma_f32_16x16x32_bf16 v[0:3], a[168:171], v[140:143], v[0:3]\n"
    "s_waitcnt lgkmcnt(7)\n"
    "v_exp_f32_e32 v204, v204\n"
    "v_mfma_f32_16x16x32_bf16 v[20:23], a[176:179], v[140:143], v[20:23]\n"
    "ds_read_b64_tr_b16 a[168:169], v245 offset:20480\n"
    "v_mfma_f32_16x16x32_bf16 v[20:23], a[180:183], v[144:147], v[20:23]\n"
    "ds_read_b64_tr_b16 a[166:167], v244 offset:55296\n"
    "v_mfma_f32_16x16x32_bf16 v[24:27], a[180:183], v[152:155], v[24:27]\n"
    "v_exp_f32_e32 v205, v205\n"
    "v_mfma_f32_16x16x32_bf16 v[24:27], a[176:179], v[148:151], v[24:27]\n"
    "v_cvt_pk_bf16_f32 v197, v198, v199\n"
    "v_mfma_f32_16x16x32_bf16 v[28:31], a[176:179], v[156:159], v[28:31]\n"
    "ds_read_b64_tr_b16 a[170:171], v245 offset:22528\n"
    "v_mfma_f32_16x16x32_bf16 v[28:31], a[180:183], v[160:163], v[28:31]\n"
    "v_exp_f32_e32 v206, v206\n"
    "v_mfma_f32_16x16x32_bf16 v[32:35], a[180:183], v[168:171], v[32:35]\n"
    "v_exp_f32_e32 v207, v207\n"
    "v_mfma_f32_16x16x32_bf16 v[32:35], a[176:179], v[164:167], v[32:35]\n"
    "v_cvt_pk_bf16_f32 v204, v204, v205\n"
    "v_mfma_f32_16x16x32_bf16 v[36:39], a[176:179], v[172:175], v[36:39]\n"
    "ds_read_b64_tr_b16 a[172:173], v245 offset:53248\n"
    "v_mfma_f32_16x16x32_bf16 v[36:39], a[180:183], v[176:179], v[36:39]\n"
    "s_waitcnt vmcnt(20)\n"
    "ds_read_b64_tr_b16 a[174:175], v245 offset:55296\n"
    "v_smfmac_f32_16x16x64_bf16 v[224:227], a[232:235], a[216:223], v238\n"
    "s_waitcnt lgkmcnt(8)\n"
    "v_mad_u32_u16 v234, v231, s90, v239 op_sel:[0,0,0,0]\n"
    "v_mfma_f32_16x16x32_bf16 v[56:59], a[188:191], v[176:179], v[56:59]\n"
    "s_bitcmp1_b32 s86, 3\n"
    "buffer_load_dwordx4 a[216:219], v234, s[20:23], 0 offen sc1\n"
    "v_mad_u32_u16 v235, v231, s90, v239 op_sel:[1,0,0,0]\n"
    "v_mfma_f32_16x16x32_bf16 v[56:59], a[184:187], v[172:175], v[56:59]\n"
    "s_cselect_b64 exec, -1, 0\n"
    "v_cvt_pk_bf16_f32 v236, v224, v225\n"
    "v_cvt_pk_bf16_f32 v237, v226, v227\n"
    "v_mfma_f32_16x16x32_bf16 v[52:55], a[184:187], v[164:167], v[52:55]\n"
    "s_cselect_b32 s88, 512, 0\n"
    "v_mov_b64_e32 v[224:225], 0\n"
    "v_mov_b64_e32 v[226:227], 0\n"
    "v_mfma_f32_16x16x32_bf16 v[52:55], a[188:191], v[168:171], v[52:55]\n"
    "buffer_store_dwordx2 v[236:237], v252, s[8:11], s87 offen sc1\n"
    "v_mfma_f32_16x16x32_bf16 v[48:51], a[188:191], v[160:163], v[48:51]\n"
    "s_mov_b64 exec, -1\n"
    "ds_read_b64_tr_b16 a[176:177], v246 offset:20480\n"
    "v_mfma_f32_16x16x32_bf16 v[48:51], a[184:187], v[156:159], v[48:51]\n"
    "s_add_u32 s87, s87, s88\n"
    "ds_read_b64_tr_b16 a[178:179], v246 offset:22528\n"
    "v_mfma_f32_16x16x32_bf16 v[44:47], a[184:187], v[148:151], v[44:47]\n"
    "buffer_load_dwordx4 a[220:223], v235, s[20:23], 0 offen sc1\n"
    "v_mfma_f32_16x16x32_bf16 v[44:47], a[188:191], v[152:155], v[44:47]\n"
    "ds_read_b64_tr_b16 a[180:181], v246 offset:53248\n"
    "v_mfma_f32_16x16x32_bf16 v[40:43], a[188:191], v[144:147], v[40:43]\n"
    "v_exp_f32_e32 v212, v212\n"
    "v_mfma_f32_16x16x32_bf16 v[40:43], a[184:187], v[140:143], v[40:43]\n"
    "s_waitcnt lgkmcnt(6)\n"
    "v_exp_f32_e32 v213, v213\n"
    "v_mfma_f32_16x16x32_bf16 v[60:63], a[160:163], v[140:143], v[60:63]\n"
    "ds_read_b64_tr_b16 a[182:183], v246 offset:55296\n"
    "v_cvt_pk_bf16_f32 v205, v206, v207\n"
    "v_mfma_f32_16x16x32_bf16 v[60:63], a[164:167], v[144:147], v[60:63]\n"
    "ds_read_b64_tr_b16 a[184:185], v247 offset:20480\n"
    "v_mfma_f32_16x16x32_bf16 v[64:67], a[164:167], v[152:155], v[64:67]\n"
    "ds_read_b64_tr_b16 a[186:187], v247 offset:22528\n"
    "v_mfma_f32_16x16x32_bf16 v[64:67], a[160:163], v[148:151], v[64:67]\n"
    "v_exp_f32_e32 v214, v214\n"
    "v_mfma_f32_16x16x32_bf16 v[68:71], a[160:163], v[156:159], v[68:71]\n"
    "v_exp_f32_e32 v215, v215\n"
    "v_mfma_f32_16x16x32_bf16 v[68:71], a[164:167], v[160:163], v[68:71]\n"
    "ds_read_b64_tr_b16 a[188:189], v247 offset:53248\n"
    "v_cvt_pk_bf16_f32 v212, v212, v213\n"
    "v_mfma_f32_16x16x32_bf16 v[72:75], a[164:167], v[168:171], v[72:75]\n"
    "ds_read_b64_tr_b16 a[190:191], v247 offset:55296\n"
    "v_cvt_pk_bf16_f32 v213, v214, v215\n"
    "v_mfma_f32_16x16x32_bf16 v[72:75], a[160:163], v[164:167], v[72:75]\n"
    "s_waitcnt vmcnt(30)\n"
    "v_mfma_f32_16x16x32_bf16 v[76:79], a[160:163], v[172:175], v[76:79]\n"
    "s_barrier\n"
    "v_mfma_f32_16x16x32_bf16 v[76:79], a[164:167], v[176:179], v[76:79]\n"
    "s_waitcnt lgkmcnt(8)\n"
    "s_mov_b32 m0, s42\n"
    "v_mfma_f32_16x16x32_bf16 v[96:99], a[172:175], v[176:179], v[96:99]\n"
    "v_exp_f32_e32 v184, v184\n"
    "v_mfma_f32_16x16x32_bf16 v[96:99], a[168:171], v[172:175], v[96:99]\n"
    "v_exp_f32_e32 v185, v185\n"
    "v_mfma_f32_16x16x32_bf16 v[92:95], a[168:171], v[164:167], v[92:95]\n"
    "v_exp_f32_e32 v186, v186\n"
    "v_mfma_f32_16x16x32_bf16 v[92:95], a[172:175], v[168:171], v[92:95]\n"
    "ds_read_b128 a[160:163], v240 offset:0\n"
    "ds_write_addtid_b32 v180 offset:0\n"
    "v_mfma_f32_16x16x32_bf16 v[88:91], a[172:175], v[160:163], v[88:91]\n"
    "v_exp_f32_e32 v187, v187\n"
    "v_mfma_f32_16x16x32_bf16 v[88:91], a[168:171], v[156:159], v[88:91]\n"
    "ds_write_addtid_b32 v181 offset:256\n"
    "v_cvt_pk_bf16_f32 v182, v184, v185\n"
    "v_mfma_f32_16x16x32_bf16 v[84:87], a[168:171], v[148:151], v[84:87]\n"
    "v_exp_f32_e32 v192, v192\n"
    "v_mfma_f32_16x16x32_bf16 v[84:87], a[172:175], v[152:155], v[84:87]\n"
    "v_exp_f32_e32 v193, v193\n"
    "v_mfma_f32_16x16x32_bf16 v[80:83], a[172:175], v[144:147], v[80:83]\n"
    "ds_write_addtid_b32 v188 offset:1024\n"
    "v_cvt_pk_bf16_f32 v183, v186, v187\n"
    "v_mfma_f32_16x16x32_bf16 v[80:83], a[168:171], v[140:143], v[80:83]\n"
    "s_waitcnt vmcnt(20)\n"
    "ds_read_b128 a[164:167], v241 offset:0\n"
    "ds_write_addtid_b32 v189 offset:1280\n"
    "v_smfmac_f32_16x16x64_bf16 v[224:227], a[232:235], a[224:231], v238\n"
    "s_waitcnt lgkmcnt(10)\n"
    "ds_write_addtid_b32 v196 offset:2048\n"
    "v_mad_u32_u16 v234, v232, s90, v239 op_sel:[0,0,0,0]\n"
    "v_mfma_f32_16x16x32_bf16 v[100:103], a[176:179], v[140:143], v[100:103]\n"
    "s_bitcmp1_b32 s86, 4\n"
    "buffer_load_dwordx4 a[224:227], v234, s[20:23], 0 offen sc1\n"
    "v_mad_u32_u16 v235, v232, s90, v239 op_sel:[1,0,0,0]\n"
    "v_mfma_f32_16x16x32_bf16 v[100:103], a[180:183], v[144:147], v[100:103]\n"
    "s_cselect_b64 exec, -1, 0\n"
    "v_cvt_pk_bf16_f32 v236, v224, v225\n"
    "v_cvt_pk_bf16_f32 v237, v226, v227\n"
    "v_mfma_f32_16x16x32_bf16 v[104:107], a[180:183], v[152:155], v[104:107]\n"
    "s_cselect_b32 s88, 512, 0\n"
    "v_mov_b64_e32 v[224:225], 0\n"
    "v_mov_b64_e32 v[226:227], 0\n"
    "v_mfma_f32_16x16x32_bf16 v[104:107], a[176:179], v[148:151], v[104:107]\n"
    "buffer_store_dwordx2 v[236:237], v252, s[8:11], s87 offen sc1\n"
    "v_mfma_f32_16x16x32_bf16 v[108:111], a[176:179], v[156:159], v[108:111]\n"
    "s_mov_b64 exec, -1\n"
    "ds_read_b128 a[168:171], v240 offset:4096\n"
    "ds_write_addtid_b32 v197 offset:2304\n"
    "v_mfma_f32_16x16x32_bf16 v[108:111], a[180:183], v[160:163], v[108:111]\n"
    "s_add_u32 s87, s87, s88\n"
    "v_exp_f32_e32 v194, v194\n"
    "v_mfma_f32_16x16x32_bf16 v[112:115], a[180:183], v[168:171], v[112:115]\n"
    "buffer_load_dwordx4 a[228:231], v235, s[20:23], 0 offen sc1\n"
    "ds_write_addtid_b32 v204 offset:3072\n"
    "v_mfma_f32_16x16x32_bf16 v[112:115], a[176:179], v[164:167], v[112:115]\n"
    "ds_write_addtid_b32 v205 offset:3328\n"
    "v_mfma_f32_16x16x32_bf16 v[116:119], a[176:179], v[172:175], v[116:119]\n"
    "v_exp_f32_e32 v195, v195\n"
    "v_mfma_f32_16x16x32_bf16 v[116:119], a[180:183], v[176:179], v[116:119]\n"
    "s_waitcnt lgkmcnt(11)\n"
    "ds_read_b128 a[172:175], v241 offset:4096\n"
    "v_cvt_pk_bf16_f32 v190, v192, v193\n"
    "v_mfma_f32_16x16x32_bf16 v[136:139], a[188:191], v[176:179], v[136:139]\n"
    "v_exp_f32_e32 v200, v200\n"
    "v_mfma_f32_16x16x32_bf16 v[136:139], a[184:187], v[172:175], v[136:139]\n"
    "v_exp_f32_e32 v201, v201\n"
    "v_mfma_f32_16x16x32_bf16 v[132:135], a[184:187], v[164:167], v[132:135]\n"
    "ds_read_b128 a[176:179], v242 offset:0\n"
    "v_cvt_pk_bf16_f32 v191, v194, v195\n"
    "v_mfma_f32_16x16x32_bf16 v[132:135], a[188:191], v[168:171], v[132:135]\n"
    "v_exp_f32_e32 v202, v202\n"
    "v_mfma_f32_16x16x32_bf16 v[128:131], a[188:191], v[160:163], v[128:131]\n"
    "v_exp_f32_e32 v203, v203\n"
    "v_mfma_f32_16x16x32_bf16 v[128:131], a[184:187], v[156:159], v[128:131]\n"
    "ds_write_addtid_b32 v212 offset:4096\n"
    "v_cvt_pk_bf16_f32 v198, v200, v201\n"
    "v_mfma_f32_16x16x32_bf16 v[124:127], a[184:187], v[148:151], v[124:127]\n"
    "v_exp_f32_e32 v208, v208\n"
    "v_mfma_f32_16x16x32_bf16 v[124:127], a[188:191], v[152:155], v[124:127]\n"
    "ds_read_b128 a[180:183], v243 offset:0\n"
    "ds_write_addtid_b32 v213 offset:4352\n"
    "v_mfma_f32_16x16x32_bf16 v[120:123], a[188:191], v[144:147], v[120:123]\n"
    "v_exp_f32_e32 v209, v209\n"
    "v_mfma_f32_16x16x32_bf16 v[120:123], a[184:187], v[140:143], v[120:123]\n"
    "s_waitcnt lgkmcnt(11)\n"
    "ds_write_addtid_b32 v182 offset:512\n"
    "v_cvt_pk_bf16_f32 v199, v202, v203\n"
    "v_mfma_f32_16x16x32_bf16 v[172:175], a[160:163], a[128:131], v[220:223]\n"
    "ds_write_addtid_b32 v183 offset:768\n"
    "v_mfma_f32_16x16x32_bf16 v[172:175], a[164:167], a[132:135], v[172:175]\n"
    "v_exp_f32_e32 v210, v210\n"
    "v_mfma_f32_16x16x32_bf16 v[164:167], a[164:167], a[100:103], v[220:223]\n"
    "ds_write_addtid_b32 v190 offset:1536\n"
    "v_mfma_f32_16x16x32_bf16 v[164:167], a[160:163], a[96:99], v[164:167]\n"
    "ds_read_b128 a[184:187], v242 offset:4096\n"
    "v_mfma_f32_16x16x32_bf16 v[156:159], a[160:163], a[64:67], v[220:223]\n"
    "v_exp_f32_e32 v211, v211\n"
    "v_mfma_f32_16x16x32_bf16 v[156:159], a[164:167], a[68:71], v[156:159]\n"
    "ds_write_addtid_b32 v191 offset:1792\n"
    "v_cvt_pk_bf16_f32 v206, v208, v209\n"
    "v_mfma_f32_16x16x32_bf16 v[148:151], a[164:167], a[36:39], v[220:223]\n"
    "v_exp_f32_e32 v216, v216\n"
    "v_mfma_f32_16x16x32_bf16 v[148:151], a[160:163], a[32:35], v[148:151]\n"
    "v_exp_f32_e32 v217, v217\n"
    "v_mfma_f32_16x16x32_bf16 v[140:143], a[160:163], a[0:3], v[220:223]\n"
    "ds_write_addtid_b32 v198 offset:2560\n"
    "v_cvt_pk_bf16_f32 v207, v210, v211\n"
    "v_mfma_f32_16x16x32_bf16 v[140:143], a[164:167], a[4:7], v[140:143]\n"
    "s_waitcnt vmcnt(20)\n"
    "v_exp_f32_e32 v218, v218\n"
    "v_smfmac_f32_16x16x64_bf16 v[224:227], a[232:235], a[192:199], v238\n"
    "s_waitcnt lgkmcnt(10)\n"
    "ds_read_b128 a[188:191], v243 offset:4096\n"
    "v_mfma_f32_16x16x32_bf16 v[140:143], a[168:171], a[8:11], v[140:143]\n"
    "s_bitcmp1_b32 s86, 5\n"
    "ds_read_b128 a[160:163], v240 offset:2048\n"
    "v_mfma_f32_16x16x32_bf16 v[140:143], a[172:175], a[12:15], v[140:143]\n"
    "s_cselect_b64 exec, -1, 0\n"
    "v_cvt_pk_bf16_f32 v236, v224, v225\n"
    "v_cvt_pk_bf16_f32 v237, v226, v227\n"
    "v_mfma_f32_16x16x32_bf16 v[148:151], a[172:175], a[44:47], v[148:151]\n"
    "s_cselect_b32 s88, 512, 0\n"
    "v_mov_b64_e32 v[224:225], 0\n"
    "v_mov_b64_e32 v[226:227], 0\n"
    "v_mfma_f32_16x16x32_bf16 v[148:151], a[168:171], a[40:43], v[148:151]\n"
    ".long 0xbedf00ff\n"
    ".Lat_w6:\n"
    "s_setpc_b64 s[2:3]\n"
    "buffer_store_dwordx2 v[236:237], v252, s[8:11], s87 offen sc1\n"
    "v_mfma_f32_16x16x32_bf16 v[156:159], a[168:171], a[72:75], v[156:159]\n"
    "s_mov_b64 exec, -1\n"
    "ds_read2_b32 v[228:229], v251 offset0:10 offset1:12\n"
    "v_mfma_f32_16x16x32_bf16 v[156:159], a[172:175], a[76:79], v[156:159]\n"
    "s_add_u32 s87, s87, s88\n"
    "ds_read2_b32 v[230:231], v251 offset0:14 offset1:16\n"
    "v_mfma_f32_16x16x32_bf16 v[164:167], a[172:175], a[108:111], v[164:167]\n"
    "ds_read_b32 v232, v251 offset:72\n"
    "v_mfma_f32_16x16x32_bf16 v[164:167], a[168:171], a[104:107], v[164:167]\n"
    "v_exp_f32_e32 v219, v219\n"
    "v_mfma_f32_16x16x32_bf16 v[172:175], a[168:171], a[136:139], v[172:175]\n"
    "ds_write_addtid_b32 v199 offset:2816\n"
    "v_cvt_pk_bf16_f32 v214, v216, v217\n"
    "v_mfma_f32_16x16x32_bf16 v[172:175], a[172:175], a[140:143], v[172:175]\n"
    "s_waitcnt lgkmcnt(13)\n"
    "ds_read_b128 a[164:167], v241 offset:2048\n"
    "v_mfma_f32_16x16x32_bf16 v[172:175], a[176:179], a[144:147], v[172:175]\n"
    "ds_write_addtid_b32 v206 offset:3584\n"
    "v_cvt_pk_bf16_f32 v215, v218, v219\n"
    "v_mfma_f32_16x16x32_bf16 v[172:175], a[180:183], a[148:151], v[172:175]\n"
    "ds_read_b128 a[168:171], v240 offset:6144\n"
    "v_mfma_f32_16x16x32_bf16 v[164:167], a[180:183], a[116:119], v[164:167]\n"
    "s_waitcnt lgkmcnt(6)\n"
    "ds_write_addtid_b32 v207 offset:3840\n"
    "v_mad_u32_u16 v234, v228, s90, v239 op_sel:[0,0,0,0]\n"
    "v_mfma_f32_16x16x32_bf16 v[164:167], a[176:179], a[112:115], v[164:167]\n"
    "buffer_load_dwordx4 a[192:195], v234, s[20:23], 0 offen sc1\n"
    "v_mad_u32_u16 v235, v228, s90, v239 op_sel:[1,0,0,0]\n"
    "v_mfma_f32_16x16x32_bf16 v[156:159], a[176:179], a[80:83], v[156:159]\n"
    "ds_write_addtid_b32 v214 offset:4608\n"
    "v_mfma_f32_16x16x32_bf16 v[156:159], a[180:183], a[84:87], v[156:159]\n"
    "ds_write_addtid_b32 v215 offset:4864\n"
    "v_mfma_f32_16x16x32_bf16 v[148:151], a[180:183], a[52:55], v[148:151]\n"
    "buffer_load_dwordx4 a[196:199], v235, s[20:23], 0 offen sc1\n"
    "v_mfma_f32_16x16x32_bf16 v[148:151], a[176:179], a[48:51], v[148:151]\n"
    "v_mfma_f32_16x16x32_bf16 v[140:143], a[176:179], a[16:19], v[140:143]\n"
    "s_waitcnt lgkmcnt(0)\n"
    "v_mfma_f32_16x16x32_bf16 v[140:143], a[180:183], a[20:23], v[140:143]\n"
    "s_barrier\n"
    "v_mfma_f32_16x16x32_bf16 v[140:143], a[184:187], a[24:27], v[140:143]\n"
    "s_add_u32 m0, s43, 16384\n"
    "ds_read_b128 a[172:175], v241 offset:6144\n"
    "v_mfma_f32_16x16x32_bf16 v[140:143], a[188:191], a[28:31], v[140:143]\n"
    "ds_read2st64_b32 v[184:185], v249 offset0:0 offset1:1\n"
    "v_mfma_f32_16x16x32_bf16 v[148:151], a[188:191], a[60:63], v[148:151]\n"
    "buffer_load_dwordx4 v248, s[8:11], s40 offen offset:0 sc1 lds\n"
    "v_mfma_f32_16x16x32_bf16 v[148:151], a[184:187], a[56:59], v[148:151]\n"
    "ds_read_b128 a[176:179], v242 offset:2048\n"
    "v_mfma_f32_16x16x32_bf16 v[156:159], a[184:187], a[88:91], v[156:159]\n"
    "v_mfma_f32_16x16x32_bf16 v[156:159], a[188:191], a[92:95], v[156:159]\n"
    "v_mfma_f32_16x16x32_bf16 v[164:167], a[188:191], a[124:127], v[164:167]\n"
    "buffer_load_dwordx4 v248, s[8:11], s40 offen offset:1024 sc1 lds\n"
    "v_mfma_f32_16x16x32_bf16 v[164:167], a[184:187], a[120:123], v[164:167]\n"
    "ds_read2st64_b32 v[186:187], v249 offset0:2 offset1:3\n"
    "v_mfma_f32_16x16x32_bf16 v[172:175], a[184:187], a[152:155], v[172:175]\n"
    "v_mfma_f32_16x16x32_bf16 v[172:175], a[188:191], a[156:159], v[172:175]\n"
    "ds_read_b128 a[180:183], v243 offset:2048\n"
    "v_mfma_f32_16x16x32_bf16 v[176:179], a[160:163], a[128:131], v[220:223]\n"
    "buffer_load_dwordx4 v248, s[8:11], s40 offen offset:2048 sc1 lds\n"
    "v_mfma_f32_16x16x32_bf16 v[176:179], a[164:167], a[132:135], v[176:179]\n"
    "ds_read2st64_b32 v[192:193], v249 offset0:4 offset1:5\n"
    "v_mfma_f32_16x16x32_bf16 v[168:171], a[164:167], a[100:103], v[220:223]\n"
    "v_exp_f32_e32 v140, v140\n"
    "v_mfma_f32_16x16x32_bf16 v[168:171], a[160:163], a[96:99], v[168:171]\n"
    "ds_read_b128 a[184:187], v242 offset:6144\n"
    "v_mfma_f32_16x16x32_bf16 v[160:163], a[160:163], a[64:67], v[220:223]\n"
    "buffer_load_dwordx4 v248, s[8:11], s40 offen offset:3072 sc1 lds\n"
    "v_mfma_f32_16x16x32_bf16 v[160:163], a[164:167], a[68:71], v[160:163]\n"
    "s_add_u32 m0, s43, 20480\n"
    "ds_read2st64_b32 v[194:195], v249 offset0:6 offset1:7\n"
    "v_mfma_f32_16x16x32_bf16 v[152:155], a[164:167], a[36:39], v[220:223]\n"
    "s_add_u32 s40, s40, 0x8000\n"
    "ds_read2st64_b32 v[200:201], v249 offset0:8 offset1:9\n"
    "v_mfma_f32_16x16x32_bf16 v[152:155], a[160:163], a[32:35], v[152:155]\n"
    "ds_read2st64_b32 v[202:203], v249 offset0:10 offset1:11\n"
    "v_mfma_f32_16x16x32_bf16 v[144:147], a[160:163], a[0:3], v[220:223]\n"
    "buffer_load_dwordx4 v248, s[8:11], s41 offen offset:0 sc1 lds\n"
    "v_mfma_f32_16x16x32_bf16 v[144:147], a[164:167], a[4:7], v[144:147]\n"
    "s_waitcnt vmcnt(17)\n"
    "ds_read_b128 a[188:191], v243 offset:6144\n"
    "v_smfmac_f32_16x16x64_bf16 v[224:227], a[232:235], a[200:207], v238\n"
    "s_waitcnt lgkmcnt(10)\n"
    "v_mad_u32_u16 v234, v229, s90, v239 op_sel:[0,0,0,0]\n"
    "v_mfma_f32_16x16x32_bf16 v[144:147], a[168:171], a[8:11], v[144:147]\n"
    "s_bitcmp1_b32 s86, 6\n"
    "buffer_load_dwordx4 a[200:203], v234, s[20:23], 0 offen sc1\n"
    "v_mad_u32_u16 v235, v229, s90, v239 op_sel:[1,0,0,0]\n"
    "v_mfma_f32_16x16x32_bf16 v[144:147], a[172:175], a[12:15], v[144:147]\n"
    "s_cselect_b64 exec, -1, 0\n"
    "v_cvt_pk_bf16_f32 v236, v224, v225\n"
    "v_cvt_pk_bf16_f32 v237, v226, v227\n"
    "v_mfma_f32_16x16x32_bf16 v[152:155], a[172:175], a[44:47], v[152:155]\n"
    "s_cselect_b32 s88, 512, 0\n"
    "v_mov_b64_e32 v[224:225], 0\n"
    "v_mov_b64_e32 v[226:227], 0\n"
    "v_mfma_f32_16x16x32_bf16 v[152:155], a[168:171], a[40:43], v[152:155]\n"
    "buffer_store_dwordx2 v[236:237], v252, s[8:11], s87 offen sc1\n"
    "v_mfma_f32_16x16x32_bf16 v[160:163], a[168:171], a[72:75], v[160:163]\n"
    "s_mov_b64 exec, -1\n"
    "ds_read_b64_tr_b16 a[160:161], v244 offset:24576\n"
    "v_mfma_f32_16x16x32_bf16 v[160:163], a[172:175], a[76:79], v[160:163]\n"
    "s_add_u32 s87, s87, s88\n"
    "ds_read_b64_tr_b16 a[162:163], v244 offset:26624\n"
    "v_mfma_f32_16x16x32_bf16 v[168:171], a[172:175], a[108:111], v[168:171]\n"
    "buffer_load_dwordx4 v248, s[8:11], s41 offen offset:1024 sc1 lds\n"
    "v_mfma_f32_16x16x32_bf16 v[168:171], a[168:171], a[104:107], v[168:171]\n"
    "ds_read_b64_tr_b16 a[164:165], v244 offset:57344\n"
    "v_mfma_f32_16x16x32_bf16 v[176:179], a[168:171], a[136:139], v[176:179]\n"
    "ds_read2st64_b32 v[208:209], v249 offset0:12 offset1:13\n"
    "v_mfma_f32_16x16x32_bf16 v[176:179], a[172:175], a[140:143], v[176:179]\n"
    "s_waitcnt lgkmcnt(10)\n"
    "ds_read2st64_b32 v[210:211], v249 offset0:14 offset1:15\n"
    "v_mfma_f32_16x16x32_bf16 v[176:179], a[176:179], a[144:147], v[176:179]\n"
    "buffer_load_dwordx4 v248, s[8:11], s41 offen offset:2048 sc1 lds\n"
    "v_mfma_f32_16x16x32_bf16 v[176:179], a[180:183], a[148:151], v[176:179]\n"
    "ds_read_b64_tr_b16 a[166:167], v244 offset:59392\n"
    "v_mfma_f32_16x16x32_bf16 v[168:171], a[180:183], a[116:119], v[168:171]\n"
    "ds_read_b64_tr_b16 a[168:169], v245 offset:24576\n"
    "v_mfma_f32_16x16x32_bf16 v[168:171], a[176:179], a[112:115], v[168:171]\n"
    "v_exp_f32_e32 v141, v141\n"
    "v_mfma_f32_16x16x32_bf16 v[160:163], a[176:179], a[80:83], v[160:163]\n"
    "buffer_load_dwordx4 v248, s[8:11], s41 offen offset:3072 sc1 lds\n"
    "v_mfma_f32_16x16x32_bf16 v[160:163], a[180:183], a[84:87], v[160:163]\n"
    "ds_read_b64_tr_b16 a[170:171], v245 offset:26624\n"
    "v_mfma_f32_16x16x32_bf16 v[152:155], a[180:183], a[52:55], v[152:155]\n"
    "s_add_u32 s41, s41, 0x8000\n"
    "ds_read2st64_b32 v[216:217], v249 offset0:16 offset1:17\n"
    "v_mfma_f32_16x16x32_bf16 v[152:155], a[176:179], a[48:51], v[152:155]\n"
    "buffer_load_dwordx4 a[204:207], v235, s[20:23], 0 offen sc1\n"
    "v_mfma_f32_16x16x32_bf16 v[144:147], a[176:179], a[16:19], v[144:147]\n"
    "ds_read_b64_tr_b16 a[172:173], v245 offset:57344\n"
    "v_mfma_f32_16x16x32_bf16 v[144:147], a[180:183], a[20:23], v[144:147]\n"
    "s_waitcnt lgkmcnt(10)\n"
    "ds_read2st64_b32 v[218:219], v249 offset0:18 offset1:19\n"
    "v_mfma_f32_16x16x32_bf16 v[144:147], a[184:187], a[24:27], v[144:147]\n"
    "ds_read_b64_tr_b16 a[176:177], v246 offset:24576\n"
    "v_mfma_f32_16x16x32_bf16 v[144:147], a[188:191], a[28:31], v[144:147]\n"
    "ds_read_b64_tr_b16 a[174:175], v245 offset:59392\n"
    "v_mfma_f32_16x16x32_bf16 v[152:155], a[188:191], a[60:63], v[152:155]\n"
    "v_exp_f32_e32 v142, v142\n"
    "v_mfma_f32_16x16x32_bf16 v[152:155], a[184:187], a[56:59], v[152:155]\n"
    "v_exp_f32_e32 v143, v143\n"
    "v_mfma_f32_16x16x32_bf16 v[160:163], a[184:187], a[88:91], v[160:163]\n"
    "ds_read_b64_tr_b16 a[178:179], v246 offset:26624\n"
    "v_cvt_pk_bf16_f32 v140, v140, v141\n"
    "v_mfma_f32_16x16x32_bf16 v[160:163], a[188:191], a[92:95], v[160:163]\n"
    "v_exp_f32_e32 v148, v148\n"
    "v_mfma_f32_16x16x32_bf16 v[168:171], a[188:191], a[124:127], v[168:171]\n"
    "v_exp_f32_e32 v149, v149\n"
    "v_mfma_f32_16x16x32_bf16 v[168:171], a[184:187], a[120:123], v[168:171]\n"
    "v_cvt_pk_bf16_f32 v141, v142, v143\n"
    "v_mfma_f32_16x16x32_bf16 v[176:179], a[184:187], a[152:155], v[176:179]\n"
    "ds_read_b64_tr_b16 a[180:181], v246 offset:57344\n"
    "v_mfma_f32_16x16x32_bf16 v[176:179], a[188:191], a[156:159], v[176:179]\n"
    "s_waitcnt vmcnt(20)\n"
    "ds_read_b64_tr_b16 a[182:183], v246 offset:59392\n"
    "v_smfmac_f32_16x16x64_bf16 v[224:227], a[232:235], a[208:215], v238\n"
    "s_waitcnt lgkmcnt(5)\n"
    "v_mad_u32_u16 v234, v230, s90, v239 op_sel:[0,0,0,0]\n"
    "v_mfma_f32_16x16x32_bf16 a[236:239], a[160:163], v[180:183], a[236:239]\n"
    "s_bitcmp1_b32 s86, 7\n"
    "buffer_load_dwordx4 a[208:211], v234, s[20:23], 0 offen sc1\n"
    "v_mad_u32_u16 v235, v230, s90, v239 op_sel:[1,0,0,0]\n"
    "v_mfma_f32_16x16x32_bf16 a[236:239], a[164:167], v[184:187], a[236:239]\n"
    "s_cselect_b64 exec, -1, 0\n"
    "v_cvt_pk_bf16_f32 v236, v224, v225\n"
    "v_cvt_pk_bf16_f32 v237, v226, v227\n"
    "v_mfma_f32_16x16x32_bf16 a[240:243], a[164:167], v[192:195], a[240:243]\n"
    "s_cselect_b32 s88, 512, 0\n"
    "v_mov_b64_e32 v[224:225], 0\n"
    "v_mov_b64_e32 v[226:227], 0\n"
    "v_mfma_f32_16x16x32_bf16 a[240:243], a[160:163], v[188:191], a[240:243]\n"
    "buffer_store_dwordx2 v[236:237], v252, s[8:11], s87 offen sc1\n"
    "v_mfma_f32_16x16x32_bf16 a[244:247], a[160:163], v[196:199], a[244:247]\n"
    "s_mov_b64 exec, -1\n"
    "ds_read_b64_tr_b16 a[184:185], v247 offset:24576\n"
    "v_mfma_f32_16x16x32_bf16 a[244:247], a[164:167], v[200:203], a[244:247]\n"
    "s_add_u32 s87, s87, s88\n"
    "ds_read_b64_tr_b16 a[186:187], v247 offset:26624\n"
    "v_mfma_f32_16x16x32_bf16 a[248:251], a[164:167], v[208:211], a[248:251]\n"
    "buffer_load_dwordx4 a[212:215], v235, s[20:23], 0 offen sc1\n"
    "v_mfma_f32_16x16x32_bf16 a[248:251], a[160:163], v[204:207], a[248:251]\n"
    "ds_read_b64_tr_b16 a[188:189], v247 offset:57344\n"
    "v_mfma_f32_16x16x32_bf16 a[252:255], a[160:163], v[212:215], a[252:255]\n"
    "v_exp_f32_e32 v150, v150\n"
    "v_mfma_f32_16x16x32_bf16 a[252:255], a[164:167], v[216:219], a[252:255]\n"
    "s_waitcnt lgkmcnt(6)\n"
    "v_exp_f32_e32 v151, v151\n"
    "v_mfma_f32_16x16x32_bf16 v[16:19], a[172:175], v[216:219], v[16:19]\n"
    "ds_read_b64_tr_b16 a[190:191], v247 offset:59392\n"
    "v_cvt_pk_bf16_f32 v148, v148, v149\n"
    "v_mfma_f32_16x16x32_bf16 v[16:19], a[168:171], v[212:215], v[16:19]\n"
    "ds_read_b64_tr_b16 a[160:161], v244 offset:28672\n"
    "v_mfma_f32_16x16x32_bf16 v[12:15], a[168:171], v[204:207], v[12:15]\n"
    "v_exp_f32_e32 v156, v156\n"
    "v_mfma_f32_16x16x32_bf16 v[12:15], a[172:175], v[208:211], v[12:15]\n"
    "v_exp_f32_e32 v157, v157\n"
    "v_mfma_f32_16x16x32_bf16 v[8:11], a[172:175], v[200:203], v[8:11]\n"
    "ds_read_b64_tr_b16 a[162:163], v244 offset:30720\n"
    "v_cvt_pk_bf16_f32 v149, v150, v151\n"
    "v_mfma_f32_16x16x32_bf16 v[8:11], a[168:171], v[196:199], v[8:11]\n"
    "v_exp_f32_e32 v158, v158\n"
    "v_mfma_f32_16x16x32_bf16 v[4:7], a[168:171], v[188:191], v[4:7]\n"
    "v_exp_f32_e32 v159, v159\n"
    "v_mfma_f32_16x16x32_bf16 v[4:7], a[172:175], v[192:195], v[4:7]\n"
    "v_cvt_pk_bf16_f32 v156, v156, v157\n"
    "v_mfma_f32_16x16x32_bf16 v[0:3], a[172:175], v[184:187], v[0:3]\n"
    "ds_read_b64_tr_b16 a[164:165], v244 offset:61440\n"
    "v_mfma_f32_16x16x32_bf16 v[0:3], a[168:171], v[180:183], v[0:3]\n"
    "s_waitcnt lgkmcnt(7)\n"
    "v_exp_f32_e32 v164, v164\n"
    "v_mfma_f32_16x16x32_bf16 v[20:23], a[176:179], v[180:183], v[20:23]\n"
    "ds_read_b64_tr_b16 a[168:169], v245 offset:28672\n"
    "v_mfma_f32_16x16x32_bf16 v[20:23], a[180:183], v[184:187], v[20:23]\n"
    "ds_read_b64_tr_b16 a[166:167], v244 offset:63488\n"
    "v_mfma_f32_16x16x32_bf16 v[24:27], a[180:183], v[192:195], v[24:27]\n"
    "v_exp_f32_e32 v165, v165\n"
    "v_mfma_f32_16x16x32_bf16 v[24:27], a[176:179], v[188:191], v[24:27]\n"
    "v_cvt_pk_bf16_f32 v157, v158, v159\n"
    "v_mfma_f32_16x16x32_bf16 v[28:31], a[176:179], v[196:199], v[28:31]\n"
    "ds_read_b64_tr_b16 a[170:171], v245 offset:30720\n"
    "v_mfma_f32_16x16x32_bf16 v[28:31], a[180:183], v[200:203], v[28:31]\n"
    "v_exp_f32_e32 v166, v166\n"
    "v_mfma_f32_16x16x32_bf16 v[32:35], a[180:183], v[208:211], v[32:35]\n"
    "v_exp_f32_e32 v167, v167\n"
    "v_mfma_f32_16x16x32_bf16 v[32:35], a[176:179], v[204:207], v[32:35]\n"
    "v_cvt_pk_bf16_f32 v164, v164, v165\n"
    "v_mfma_f32_16x16x32_bf16 v[36:39], a[176:179], v[212:215], v[36:39]\n"
    "ds_read_b64_tr_b16 a[172:173], v245 offset:61440\n"
    "v_mfma_f32_16x16x32_bf16 v[36:39], a[180:183], v[216:219], v[36:39]\n"
    "s_waitcnt vmcnt(20)\n"
    "ds_read_b64_tr_b16 a[174:175], v245 offset:63488\n"
    "v_smfmac_f32_16x16x64_bf16 v[224:227], a[232:235], a[216:223], v238\n"
    "s_waitcnt lgkmcnt(8)\n"
    "v_mad_u32_u16 v234, v231, s90, v239 op_sel:[0,0,0,0]\n"
    "v_mfma_f32_16x16x32_bf16 v[56:59], a[188:191], v[216:219], v[56:59]\n"
    "s_bitcmp1_b32 s86, 8\n"
    "buffer_load_dwordx4 a[216:219], v234, s[20:23], 0 offen sc1\n"
    "v_mad_u32_u16 v235, v231, s90, v239 op_sel:[1,0,0,0]\n"
    "v_mfma_f32_16x16x32_bf16 v[56:59], a[184:187], v[212:215], v[56:59]\n"
    "s_cselect_b64 exec, -1, 0\n"
    "v_cvt_pk_bf16_f32 v236, v224, v225\n"
    "v_cvt_pk_bf16_f32 v237, v226, v227\n"
    "v_mfma_f32_16x16x32_bf16 v[52:55], a[184:187], v[204:207], v[52:55]\n"
    "s_cselect_b32 s88, 512, 0\n"
    "v_mov_b64_e32 v[224:225], 0\n"
    "v_mov_b64_e32 v[226:227], 0\n"
    "v_mfma_f32_16x16x32_bf16 v[52:55], a[188:191], v[208:211], v[52:55]\n"
    "buffer_store_dwordx2 v[236:237], v252, s[8:11], s87 offen sc1\n"
    "v_mfma_f32_16x16x32_bf16 v[48:51], a[188:191], v[200:203], v[48:51]\n"
    "s_mov_b64 exec, -1\n"
    "ds_read_b64_tr_b16 a[176:177], v246 offset:28672\n"
    "v_mfma_f32_16x16x32_bf16 v[48:51], a[184:187], v[196:199], v[48:51]\n"
    "s_add_u32 s87, s87, s88\n"
    "ds_read_b64_tr_b16 a[178:179], v246 offset:30720\n"
    "v_mfma_f32_16x16x32_bf16 v[44:47], a[184:187], v[188:191], v[44:47]\n"
    "buffer_load_dwordx4 a[220:223], v235, s[20:23], 0 offen sc1\n"
    "v_mfma_f32_16x16x32_bf16 v[44:47], a[188:191], v[192:195], v[44:47]\n"
    "ds_read_b64_tr_b16 a[180:181], v246 offset:61440\n"
    "v_mfma_f32_16x16x32_bf16 v[40:43], a[188:191], v[184:187], v[40:43]\n"
    "v_exp_f32_e32 v172, v172\n"
    "v_mfma_f32_16x16x32_bf16 v[40:43], a[184:187], v[180:183], v[40:43]\n"
    "s_waitcnt lgkmcnt(6)\n"
    "v_exp_f32_e32 v173, v173\n"
    "v_mfma_f32_16x16x32_bf16 v[60:63], a[160:163], v[180:183], v[60:63]\n"
    "ds_read_b64_tr_b16 a[182:183], v246 offset:63488\n"
    "v_cvt_pk_bf16_f32 v165, v166, v167\n"
    "v_mfma_f32_16x16x32_bf16 v[60:63], a[164:167], v[184:187], v[60:63]\n"
    "ds_read_b64_tr_b16 a[184:185], v247 offset:28672\n"
    "v_mfma_f32_16x16x32_bf16 v[64:67], a[164:167], v[192:195], v[64:67]\n"
    "ds_read_b64_tr_b16 a[186:187], v247 offset:30720\n"
    "v_mfma_f32_16x16x32_bf16 v[64:67], a[160:163], v[188:191], v[64:67]\n"
    "v_exp_f32_e32 v174, v174\n"
    "v_mfma_f32_16x16x32_bf16 v[68:71], a[160:163], v[196:199], v[68:71]\n"
    "v_exp_f32_e32 v175, v175\n"
    "v_mfma_f32_16x16x32_bf16 v[68:71], a[164:167], v[200:203], v[68:71]\n"
    "ds_read_b64_tr_b16 a[188:189], v247 offset:61440\n"
    "v_cvt_pk_bf16_f32 v172, v172, v173\n"
    "v_mfma_f32_16x16x32_bf16 v[72:75], a[164:167], v[208:211], v[72:75]\n"
    "ds_read_b64_tr_b16 a[190:191], v247 offset:63488\n"
    "v_cvt_pk_bf16_f32 v173, v174, v175\n"
    "v_mfma_f32_16x16x32_bf16 v[72:75], a[160:163], v[204:207], v[72:75]\n"
    "s_waitcnt vmcnt(30)\n"
    "v_mfma_f32_16x16x32_bf16 v[76:79], a[160:163], v[212:215], v[76:79]\n"
    "s_barrier\n"
    "v_mfma_f32_16x16x32_bf16 v[76:79], a[164:167], v[216:219], v[76:79]\n"
    "s_waitcnt lgkmcnt(8)\n"
    "s_mov_b32 m0, s42\n"
    "v_mfma_f32_16x16x32_bf16 v[96:99], a[172:175], v[216:219], v[96:99]\n"
    "v_exp_f32_e32 v144, v144\n"
    "v_mfma_f32_16x16x32_bf16 v[96:99], a[168:171], v[212:215], v[96:99]\n"
    "v_exp_f32_e32 v145, v145\n"
    "v_mfma_f32_16x16x32_bf16 v[92:95], a[168:171], v[204:207], v[92:95]\n"
    "v_exp_f32_e32 v146, v146\n"
    "v_mfma_f32_16x16x32_bf16 v[92:95], a[172:175], v[208:211], v[92:95]\n"
    "ds_read_b128 a[160:163], v240 offset:8192\n"
    "ds_write_addtid_b32 v140 offset:0\n"
    "v_mfma_f32_16x16x32_bf16 v[88:91], a[172:175], v[200:203], v[88:91]\n"
    "v_exp_f32_e32 v147, v147\n"
    "v_mfma_f32_16x16x32_bf16 v[88:91], a[168:171], v[196:199], v[88:91]\n"
    "ds_write_addtid_b32 v141 offset:256\n"
    "v_cvt_pk_bf16_f32 v142, v144, v145\n"
    "v_mfma_f32_16x16x32_bf16 v[84:87], a[168:171], v[188:191], v[84:87]\n"
    "v_exp_f32_e32 v152, v152\n"
    "v_mfma_f32_16x16x32_bf16 v[84:87], a[172:175], v[192:195], v[84:87]\n"
    "v_exp_f32_e32 v153, v153\n"
    "v_mfma_f32_16x16x32_bf16 v[80:83], a[172:175], v[184:187], v[80:83]\n"
    "ds_write_addtid_b32 v148 offset:1024\n"
    "v_cvt_pk_bf16_f32 v143, v146, v147\n"
    "v_mfma_f32_16x16x32_bf16 v[80:83], a[168:171], v[180:183], v[80:83]\n"
    "s_waitcnt vmcnt(20)\n"
    "ds_read_b128 a[164:167], v241 offset:8192\n"
    "ds_write_addtid_b32 v149 offset:1280\n"
    "v_smfmac_f32_16x16x64_bf16 v[224:227], a[232:235], a[224:231], v238\n"
    "s_waitcnt lgkmcnt(10)\n"
    "ds_write_addtid_b32 v156 offset:2048\n"
    "v_mad_u32_u16 v234, v232, s90, v239 op_sel:[0,0,0,0]\n"
    "v_mfma_f32_16x16x32_bf16 v[100:103], a[176:179], v[180:183], v[100:103]\n"
    "s_bitcmp1_b32 s86, 9\n"
    "buffer_load_dwordx4 a[224:227], v234, s[20:23], 0 offen sc1\n"
    "v_mad_u32_u16 v235, v232, s90, v239 op_sel:[1,0,0,0]\n"
    "v_mfma_f32_16x16x32_bf16 v[100:103], a[180:183], v[184:187], v[100:103]\n"
    "s_cselect_b64 exec, -1, 0\n"
    "v_cvt_pk_bf16_f32 v236, v224, v225\n"
    "v_cvt_pk_bf16_f32 v237, v226, v227\n"
    "v_mfma_f32_16x16x32_bf16 v[104:107], a[180:183], v[192:195], v[104:107]\n"
    "s_cselect_b32 s88, 512, 0\n"
    "v_mov_b64_e32 v[224:225], 0\n"
    "v_mov_b64_e32 v[226:227], 0\n"
    "v_mfma_f32_16x16x32_bf16 v[104:107], a[176:179], v[188:191], v[104:107]\n"
    "buffer_store_dwordx2 v[236:237], v252, s[8:11], s87 offen sc1\n"
    "v_mfma_f32_16x16x32_bf16 v[108:111], a[176:179], v[196:199], v[108:111]\n"
    "s_mov_b64 exec, -1\n"
    "ds_read_b128 a[168:171], v240 offset:12288\n"
    "ds_write_addtid_b32 v157 offset:2304\n"
    "v_mfma_f32_16x16x32_bf16 v[108:111], a[180:183], v[200:203], v[108:111]\n"
    "s_add_u32 s87, s87, s88\n"
    "v_exp_f32_e32 v154, v154\n"
    "v_mfma_f32_16x16x32_bf16 v[112:115], a[180:183], v[208:211], v[112:115]\n"
    "buffer_load_dwordx4 a[228:231], v235, s[20:23], 0 offen sc1\n"
    "ds_write_addtid_b32 v164 offset:3072\n"
    "v_mfma_f32_16x16x32_bf16 v[112:115], a[176:179], v[204:207], v[112:115]\n"
    "ds_write_addtid_b32 v165 offset:3328\n"
    "v_mfma_f32_16x16x32_bf16 v[116:119], a[176:179], v[212:215], v[116:119]\n"
    "v_exp_f32_e32 v155, v155\n"
    "v_mfma_f32_16x16x32_bf16 v[116:119], a[180:183], v[216:219], v[116:119]\n"
    "s_waitcnt lgkmcnt(11)\n"
    "ds_read_b128 a[172:175], v241 offset:12288\n"
    "v_cvt_pk_bf16_f32 v150, v152, v153\n"
    "v_mfma_f32_16x16x32_bf16 v[136:139], a[188:191], v[216:219], v[136:139]\n"
    "v_exp_f32_e32 v160, v160\n"
    "v_mfma_f32_16x16x32_bf16 v[136:139], a[184:187], v[212:215], v[136:139]\n"
    "v_exp_f32_e32 v161, v161\n"
    "v_mfma_f32_16x16x32_bf16 v[132:135], a[184:187], v[204:207], v[132:135]\n"
    "ds_read_b128 a[176:179], v242 offset:8192\n"
    "v_cvt_pk_bf16_f32 v151, v154, v155\n"
    "v_mfma_f32_16x16x32_bf16 v[132:135], a[188:191], v[208:211], v[132:135]\n"
    "v_exp_f32_e32 v162, v162\n"
    "v_mfma_f32_16x16x32_bf16 v[128:131], a[188:191], v[200:203], v[128:131]\n"
    "v_exp_f32_e32 v163, v163\n"
    "v_mfma_f32_16x16x32_bf16 v[128:131], a[184:187], v[196:199], v[128:131]\n"
    "ds_write_addtid_b32 v172 offset:4096\n"
    "v_cvt_pk_bf16_f32 v158, v160, v161\n"
    "v_mfma_f32_16x16x32_bf16 v[124:127], a[184:187], v[188:191], v[124:127]\n"
    "v_exp_f32_e32 v168, v168\n"
    "v_mfma_f32_16x16x32_bf16 v[124:127], a[188:191], v[192:195], v[124:127]\n"
    "ds_read_b128 a[180:183], v243 offset:8192\n"
    "ds_write_addtid_b32 v173 offset:4352\n"
    "v_mfma_f32_16x16x32_bf16 v[120:123], a[188:191], v[184:187], v[120:123]\n"
    "v_exp_f32_e32 v169, v169\n"
    "v_mfma_f32_16x16x32_bf16 v[120:123], a[184:187], v[180:183], v[120:123]\n"
    "s_waitcnt lgkmcnt(11)\n"
    "ds_write_addtid_b32 v142 offset:512\n"
    "v_cvt_pk_bf16_f32 v159, v162, v163\n"
    "v_mfma_f32_16x16x32_bf16 v[212:215], a[160:163], a[128:131], v[220:223]\n"
    "ds_write_addtid_b32 v143 offset:768\n"
    "v_mfma_f32_16x16x32_bf16 v[212:215], a[164:167], a[132:135], v[212:215]\n"
    "v_exp_f32_e32 v170, v170\n"
    "v_mfma_f32_16x16x32_bf16 v[204:207], a[164:167], a[100:103], v[220:223]\n"
    "ds_write_addtid_b32 v150 offset:1536\n"
    "v_mfma_f32_16x16x32_bf16 v[204:207], a[160:163], a[96:99], v[204:207]\n"
    "ds_read_b128 a[184:187], v242 offset:12288\n"
    "v_mfma_f32_16x16x32_bf16 v[196:199], a[160:163], a[64:67], v[220:223]\n"
    "v_exp_f32_e32 v171, v171\n"
    "v_mfma_f32_16x16x32_bf16 v[196:199], a[164:167], a[68:71], v[196:199]\n"
    "ds_write_addtid_b32 v151 offset:1792\n"
    "v_cvt_pk_bf16_f32 v166, v168, v169\n"
    "v_mfma_f32_16x16x32_bf16 v[188:191], a[164:167], a[36:39], v[220:223]\n"
    "v_exp_f32_e32 v176, v176\n"
    "v_mfma_f32_16x16x32_bf16 v[188:191], a[160:163], a[32:35], v[188:191]\n"
    "v_exp_f32_e32 v177, v177\n"
    "v_mfma_f32_16x16x32_bf16 v[180:183], a[160:163], a[0:3], v[220:223]\n"
    "ds_write_addtid_b32 v158 offset:2560\n"
    "v_cvt_pk_bf16_f32 v167, v170, v171\n"
    "v_mfma_f32_16x16x32_bf16 v[180:183], a[164:167], a[4:7], v[180:183]\n"
    "s_waitcnt vmcnt(20)\n"
    "v_exp_f32_e32 v178, v178\n"
    "v_smfmac_f32_16x16x64_bf16 v[224:227], a[232:235], a[192:199], v238\n"
    "s_waitcnt lgkmcnt(10)\n"
    "ds_read_b128 a[188:191], v243 offset:12288\n"
    "v_mfma_f32_16x16x32_bf16 v[180:183], a[168:171], a[8:11], v[180:183]\n"
    "s_bitcmp1_b32 s86, 10\n"
    "ds_read_b128 a[160:163], v240 offset:10240\n"
    "v_mfma_f32_16x16x32_bf16 v[180:183], a[172:175], a[12:15], v[180:183]\n"
    "s_cselect_b64 exec, -1, 0\n"
    "v_cvt_pk_bf16_f32 v236, v224, v225\n"
    "v_cvt_pk_bf16_f32 v237, v226, v227\n"
    "v_mfma_f32_16x16x32_bf16 v[188:191], a[172:175], a[44:47], v[188:191]\n"
    "s_cselect_b32 s88, 512, 0\n"
    "v_mov_b64_e32 v[224:225], 0\n"
    "v_mov_b64_e32 v[226:227], 0\n"
    "v_mfma_f32_16x16x32_bf16 v[188:191], a[168:171], a[40:43], v[188:191]\n"
    "buffer_store_dwordx2 v[236:237], v252, s[8:11], s87 offen sc1\n"
    "v_mfma_f32_16x16x32_bf16 v[196:199], a[168:171], a[72:75], v[196:199]\n"
    "s_mov_b64 exec, -1\n"
    "ds_read2_b32 v[228:229], v251 offset0:20 offset1:22\n"
    "v_mfma_f32_16x16x32_bf16 v[196:199], a[172:175], a[76:79], v[196:199]\n"
    "s_add_u32 s87, s87, s88\n"
    "ds_read2_b32 v[230:231], v251 offset0:24 offset1:26\n"
    "v_mfma_f32_16x16x32_bf16 v[204:207], a[172:175], a[108:111], v[204:207]\n"
    "ds_read_b32 v232, v251 offset:112\n"
    "v_mfma_f32_16x16x32_bf16 v[204:207], a[168:171], a[104:107], v[204:207]\n"
    "v_exp_f32_e32 v179, v179\n"
    "v_mfma_f32_16x16x32_bf16 v[212:215], a[168:171], a[136:139], v[212:215]\n"
    "ds_write_addtid_b32 v159 offset:2816\n"
    "v_cvt_pk_bf16_f32 v174, v176, v177\n"
    "v_mfma_f32_16x16x32_bf16 v[212:215], a[172:175], a[140:143], v[212:215]\n"
    "s_waitcnt lgkmcnt(13)\n"
    "ds_read_b128 a[164:167], v241 offset:10240\n"
    "v_mfma_f32_16x16x32_bf16 v[212:215], a[176:179], a[144:147], v[212:215]\n"
    "ds_write_addtid_b32 v166 offset:3584\n"
    "v_cvt_pk_bf16_f32 v175, v178, v179\n"
    "v_mfma_f32_16x16x32_bf16 v[212:215], a[180:183], a[148:151], v[212:215]\n"
    "ds_read_b128 a[168:171], v240 offset:14336\n"
    "v_mfma_f32_16x16x32_bf16 v[204:207], a[180:183], a[116:119], v[204:207]\n"
    "s_waitcnt lgkmcnt(6)\n"
    "ds_write_addtid_b32 v167 offset:3840\n"
    "v_mad_u32_u16 v234, v228, s90, v239 op_sel:[0,0,0,0]\n"
    "v_mfma_f32_16x16x32_bf16 v[204:207], a[176:179], a[112:115], v[204:207]\n"
    "buffer_load_dwordx4 a[192:195], v234, s[20:23], 0 offen sc1\n"
    "v_mad_u32_u16 v235, v228, s90, v239 op_sel:[1,0,0,0]\n"
    "v_mfma_f32_16x16x32_bf16 v[196:199], a[176:179], a[80:83], v[196:199]\n"
    "ds_write_addtid_b32 v174 offset:4608\n"
    "v_mfma_f32_16x16x32_bf16 v[196:199], a[180:183], a[84:87], v[196:199]\n"
    "ds_write_addtid_b32 v175 offset:4864\n"
    "v_mfma_f32_16x16x32_bf16 v[188:191], a[180:183], a[52:55], v[188:191]\n"
    "buffer_load_dwordx4 a[196:199], v235, s[20:23], 0 offen sc1\n"
    "v_mfma_f32_16x16x32_bf16 v[188:191], a[176:179], a[48:51], v[188:191]\n"
    "v_mfma_f32_16x16x32_bf16 v[180:183], a[176:179], a[16:19], v[180:183]\n"
    "s_waitcnt lgkmcnt(0)\n"
    "v_mfma_f32_16x16x32_bf16 v[180:183], a[180:183], a[20:23], v[180:183]\n"
    "s_barrier\n"
    "v_mfma_f32_16x16x32_bf16 v[180:183], a[184:187], a[24:27], v[180:183]\n"
    "s_add_u32 m0, s43, 24576\n"
    "ds_read_b128 a[172:175], v241 offset:14336\n"
    "v_mfma_f32_16x16x32_bf16 v[180:183], a[188:191], a[28:31], v[180:183]\n"
    "ds_read2st64_b32 v[144:145], v249 offset0:0 offset1:1\n"
    "v_mfma_f32_16x16x32_bf16 v[188:191], a[188:191], a[60:63], v[188:191]\n"
    "buffer_load_dwordx4 v248, s[8:11], s40 offen offset:0 sc1 lds\n"
    "v_mfma_f32_16x16x32_bf16 v[188:191], a[184:187], a[56:59], v[188:191]\n"
    "ds_read_b128 a[176:179], v242 offset:10240\n"
    "v_mfma_f32_16x16x32_bf16 v[196:199], a[184:187], a[88:91], v[196:199]\n"
    "v_mfma_f32_16x16x32_bf16 v[196:199], a[188:191], a[92:95], v[196:199]\n"
    "v_mfma_f32_16x16x32_bf16 v[204:207], a[188:191], a[124:127], v[204:207]\n"
    "buffer_load_dwordx4 v248, s[8:11], s40 offen offset:1024 sc1 lds\n"
    "v_mfma_f32_16x16x32_bf16 v[204:207], a[184:187], a[120:123], v[204:207]\n"
    "ds_read2st64_b32 v[146:147], v249 offset0:2 offset1:3\n"
    "v_mfma_f32_16x16x32_bf16 v[212:215], a[184:187], a[152:155], v[212:215]\n"
    "v_mfma_f32_16x16x32_bf16 v[212:215], a[188:191], a[156:159], v[212:215]\n"
    "ds_read_b128 a[180:183], v243 offset:10240\n"
    "v_mfma_f32_16x16x32_bf16 v[216:219], a[160:163], a[128:131], v[220:223]\n"
    "buffer_load_dwordx4 v248, s[8:11], s40 offen offset:2048 sc1 lds\n"
    "v_mfma_f32_16x16x32_bf16 v[216:219], a[164:167], a[132:135], v[216:219]\n"
    "ds_read2st64_b32 v[152:153], v249 offset0:4 offset1:5\n"
    "v_mfma_f32_16x16x32_bf16 v[208:211], a[164:167], a[100:103], v[220:223]\n"
    "v_exp_f32_e32 v180, v180\n"
    "v_mfma_f32_16x16x32_bf16 v[208:211], a[160:163], a[96:99], v[208:211]\n"
    "ds_read_b128 a[184:187], v242 offset:14336\n"
    "v_mfma_f32_16x16x32_bf16 v[200:203], a[160:163], a[64:67], v[220:223]\n"
    "buffer_load_dwordx4 v248, s[8:11], s40 offen offset:3072 sc1 lds\n"
    "v_mfma_f32_16x16x32_bf16 v[200:203], a[164:167], a[68:71], v[200:203]\n"
    "s_add_u32 m0, s43, 28672\n"
    "ds_read2st64_b32 v[154:155], v249 offset0:6 offset1:7\n"
    "v_mfma_f32_16x16x32_bf16 v[192:195], a[164:167], a[36:39], v[220:223]\n"
    "s_add_u32 s40, s40, 0x8000\n"
    "ds_read2st64_b32 v[160:161], v249 offset0:8 offset1:9\n"
    "v_mfma_f32_16x16x32_bf16 v[192:195], a[160:163], a[32:35], v[192:195]\n"
    "ds_read2st64_b32 v[162:163], v249 offset0:10 offset1:11\n"
    "v_mfma_f32_16x16x32_bf16 v[184:187], a[160:163], a[0:3], v[220:223]\n"
    "buffer_load_dwordx4 v248, s[8:11], s41 offen offset:0 sc1 lds\n"
    "v_mfma_f32_16x16x32_bf16 v[184:187], a[164:167], a[4:7], v[184:187]\n"
    "s_waitcnt vmcnt(17)\n"
    "ds_read_b128 a[188:191], v243 offset:14336\n"
    "v_smfmac_f32_16x16x64_bf16 v[224:227], a[232:235], a[200:207], v238\n"
    "s_waitcnt lgkmcnt(10)\n"
    "v_mad_u32_u16 v234, v229, s90, v239 op_sel:[0,0,0,0]\n"
    "v_mfma_f32_16x16x32_bf16 v[184:187], a[168:171], a[8:11], v[184:187]\n"
    "s_bitcmp1_b32 s86, 11\n"
    "buffer_load_dwordx4 a[200:203], v234, s[20:23], 0 offen sc1\n"
    "v_mad_u32_u16 v235, v229, s90, v239 op_sel:[1,0,0,0]\n"
    "v_mfma_f32_16x16x32_bf16 v[184:187], a[172:175], a[12:15], v[184:187]\n"
    "s_cselect_b64 exec, -1, 0\n"
    "v_cvt_pk_bf16_f32 v236, v224, v225\n"
    "v_cvt_pk_bf16_f32 v237, v226, v227\n"
    "v_mfma_f32_16x16x32_bf16 v[192:195], a[172:175], a[44:47], v[192:195]\n"
    "s_cselect_b32 s88, 512, 0\n"
    "v_mov_b64_e32 v[224:225], 0\n"
    "v_mov_b64_e32 v[226:227], 0\n"
    "v_mfma_f32_16x16x32_bf16 v[192:195], a[168:171], a[40:43], v[192:195]\n"
    "buffer_store_dwordx2 v[236:237], v252, s[8:11], s87 offen sc1\n"
    "v_mfma_f32_16x16x32_bf16 v[200:203], a[168:171], a[72:75], v[200:203]\n"
    "s_mov_b64 exec, -1\n"
    "ds_read_b64_tr_b16 a[160:161], v244 offset:0\n"
    "v_mfma_f32_16x16x32_bf16 v[200:203], a[172:175], a[76:79], v[200:203]\n"
    "s_add_u32 s87, s87, s88\n"
    "ds_read_b64_tr_b16 a[162:163], v244 offset:2048\n"
    "v_mfma_f32_16x16x32_bf16 v[208:211], a[172:175], a[108:111], v[208:211]\n"
    "buffer_load_dwordx4 v248, s[8:11], s41 offen offset:1024 sc1 lds\n"
    "v_mfma_f32_16x16x32_bf16 v[208:211], a[168:171], a[104:107], v[208:211]\n"
    "ds_read_b64_tr_b16 a[164:165], v244 offset:32768\n"
    "v_mfma_f32_16x16x32_bf16 v[216:219], a[168:171], a[136:139], v[216:219]\n"
    "ds_read2st64_b32 v[168:169], v249 offset0:12 offset1:13\n"
    "v_mfma_f32_16x16x32_bf16 v[216:219], a[172:175], a[140:143], v[216:219]\n"
    "s_waitcnt lgkmcnt(10)\n"
    "ds_read2st64_b32 v[170:171], v249 offset0:14 offset1:15\n"
    "v_mfma_f32_16x16x32_bf16 v[216:219], a[176:179], a[144:147], v[216:219]\n"
    "buffer_load_dwordx4 v248, s[8:11], s41 offen offset:2048 sc1 lds\n"
    "v_mfma_f32_16x16x32_bf16 v[216:219], a[180:183], a[148:151], v[216:219]\n"
    "ds_read_b64_tr_b16 a[166:167], v244 offset:34816\n"
    "v_mfma_f32_16x16x32_bf16 v[208:211], a[180:183], a[116:119], v[208:211]\n"
    "ds_read_b64_tr_b16 a[168:169], v245 offset:0\n"
    "v_mfma_f32_16x16x32_bf16 v[208:211], a[176:179], a[112:115], v[208:211]\n"
    "v_exp_f32_e32 v181, v181\n"
    "v_mfma_f32_16x16x32_bf16 v[200:203], a[176:179], a[80:83], v[200:203]\n"
    "buffer_load_dwordx4 v248, s[8:11], s41 offen offset:3072 sc1 lds\n"
    "v_mfma_f32_16x16x32_bf16 v[200:203], a[180:183], a[84:87], v[200:203]\n"
    "ds_read_b64_tr_b16 a[170:171], v245 offset:2048\n"
    "v_mfma_f32_16x16x32_bf16 v[192:195], a[180:183], a[52:55], v[192:195]\n"
    "s_add_u32 s41, s41, 0x8000\n"
    "ds_read2st64_b32 v[176:177], v249 offset0:16 offset1:17\n"
    "v_mfma_f32_16x16x32_bf16 v[192:195], a[176:179], a[48:51], v[192:195]\n"
    "buffer_load_dwordx4 a[204:207], v235, s[20:23], 0 offen sc1\n"
    "v_mfma_f32_16x16x32_bf16 v[184:187], a[176:179], a[16:19], v[184:187]\n"
    "ds_read_b64_tr_b16 a[172:173], v245 offset:32768\n"
    "v_mfma_f32_16x16x32_bf16 v[184:187], a[180:183], a[20:23], v[184:187]\n"
    "s_waitcnt lgkmcnt(10)\n"
    "ds_read2st64_b32 v[178:179], v249 offset0:18 offset1:19\n"
    "v_mfma_f32_16x16x32_bf16 v[184:187], a[184:187], a[24:27], v[184:187]\n"
    "ds_read_b64_tr_b16 a[176:177], v246 offset:0\n"
    "v_mfma_f32_16x16x32_bf16 v[184:187], a[188:191], a[28:31], v[184:187]\n"
    "ds_read_b64_tr_b16 a[174:175], v245 offset:34816\n"
    "v_mfma_f32_16x16x32_bf16 v[192:195], a[188:191], a[60:63], v[192:195]\n"
    "v_exp_f32_e32 v182, v182\n"
    "v_mfma_f32_16x16x32_bf16 v[192:195], a[184:187], a[56:59], v[192:195]\n"
    "v_exp_f32_e32 v183, v183\n"
    "v_mfma_f32_16x16x32_bf16 v[200:203], a[184:187], a[88:91], v[200:203]\n"
    "ds_read_b64_tr_b16 a[178:179], v246 offset:2048\n"
    "v_cvt_pk_bf16_f32 v180, v180, v181\n"
    "v_mfma_f32_16x16x32_bf16 v[200:203], a[188:191], a[92:95], v[200:203]\n"
    "v_exp_f32_e32 v188, v188\n"
    "v_mfma_f32_16x16x32_bf16 v[208:211], a[188:191], a[124:127], v[208:211]\n"
    "v_exp_f32_e32 v189, v189\n"
    "v_mfma_f32_16x16x32_bf16 v[208:211], a[184:187], a[120:123], v[208:211]\n"
    "v_cvt_pk_bf16_f32 v181, v182, v183\n"
    "v_mfma_f32_16x16x32_bf16 v[216:219], a[184:187], a[152:155], v[216:219]\n"
    ".long 0xbedf00ff\n"
    ".Lat_w7:\n"
    "s_setpc_b64 s[2:3]\n"
    "ds_read_b64_tr_b16 a[180:181], v246 offset:32768\n"
    "v_mfma_f32_16x16x32_bf16 v[216:219], a[188:191], a[156:159], v[216:219]\n"
    "s_waitcnt vmcnt(20)\n"
    "ds_read_b64_tr_b16 a[182:183], v246 offset:34816\n"
    "v_smfmac_f32_16x16x64_bf16 v[224:227], a[232:235], a[208:215], v238\n"
    "s_waitcnt lgkmcnt(5)\n"
    "v_mad_u32_u16 v234, v230, s90, v239 op_sel:[0,0,0,0]\n"
    "v_mfma_f32_16x16x32_bf16 a[236:239], a[160:163], v[140:143], a[236:239]\n"
    "s_bitcmp1_b32 s86, 12\n"
    "buffer_load_dwordx4 a[208:211], v234, s[20:23], 0 offen sc1\n"
    "v_mad_u32_u16 v235, v230, s90, v239 op_sel:[1,0,0,0]\n"
    "v_mfma_f32_16x16x32_bf16 a[236:239], a[164:167], v[144:147], a[236:239]\n"
    "s_cselect_b64 exec, -1, 0\n"
    "v_cvt_pk_bf16_f32 v236, v224, v225\n"
    "v_cvt_pk_bf16_f32 v237, v226, v227\n"
    "v_mfma_f32_16x16x32_bf16 a[240:243], a[164:167], v[152:155], a[240:243]\n"
    "s_cselect_b32 s88, 512, 0\n"
    "v_mov_b64_e32 v[224:225], 0\n"
    "v_mov_b64_e32 v[226:227], 0\n"
    "v_mfma_f32_16x16x32_bf16 a[240:243], a[160:163], v[148:151], a[240:243]\n"
    "buffer_store_dwordx2 v[236:237], v252, s[8:11], s87 offen sc1\n"
    "v_mfma_f32_16x16x32_bf16 a[244:247], a[160:163], v[156:159], a[244:247]\n"
    "s_mov_b64 exec, -1\n"
    "ds_read_b64_tr_b16 a[184:185], v247 offset:0\n"
    "v_mfma_f32_16x16x32_bf16 a[244:247], a[164:167], v[160:163], a[244:247]\n"
    "s_add_u32 s87, s87, s88\n"
    "ds_read_b64_tr_b16 a[186:187], v247 offset:2048\n"
    "v_mfma_f32_16x16x32_bf16 a[248:251], a[164:167], v[168:171], a[248:251]\n"
    "buffer_load_dwordx4 a[212:215], v235, s[20:23], 0 offen sc1\n"
    "v_mfma_f32_16x16x32_bf16 a[248:251], a[160:163], v[164:167], a[248:251]\n"
    "ds_read_b64_tr_b16 a[188:189], v247 offset:32768\n"
    "v_mfma_f32_16x16x32_bf16 a[252:255], a[160:163], v[172:175], a[252:255]\n"
    "v_exp_f32_e32 v190, v190\n"
    "v_mfma_f32_16x16x32_bf16 a[252:255], a[164:167], v[176:179], a[252:255]\n"
    "s_waitcnt lgkmcnt(6)\n"
    "v_exp_f32_e32 v191, v191\n"
    "v_mfma_f32_16x16x32_bf16 v[16:19], a[172:175], v[176:179], v[16:19]\n"
    "ds_read_b64_tr_b16 a[190:191], v247 offset:34816\n"
    "v_cvt_pk_bf16_f32 v188, v188, v189\n"
    "v_mfma_f32_16x16x32_bf16 v[16:19], a[168:171], v[172:175], v[16:19]\n"
    "ds_read_b64_tr_b16 a[160:161], v244 offset:4096\n"
    "v_mfma_f32_16x16x32_bf16 v[12:15], a[168:171], v[164:167], v[12:15]\n"
    "v_exp_f32_e32 v196, v196\n"
    "v_mfma_f32_16x16x32_bf16 v[12:15], a[172:175], v[168:171], v[12:15]\n"
    "v_exp_f32_e32 v197, v197\n"
    "v_mfma_f32_16x16x32_bf16 v[8:11], a[172:175], v[160:163], v[8:11]\n"
    "ds_read_b64_tr_b16 a[162:163], v244 offset:6144\n"
    "v_cvt_pk_bf16_f32 v189, v190, v191\n"
    "v_mfma_f32_16x16x32_bf16 v[8:11], a[168:171], v[156:159], v[8:11]\n"
    "v_exp_f32_e32 v198, v198\n"
    "v_mfma_f32_16x16x32_bf16 v[4:7], a[168:171], v[148:151], v[4:7]\n"
    "v_exp_f32_e32 v199, v199\n"
    "v_mfma_f32_16x16x32_bf16 v[4:7], a[172:175], v[152:155], v[4:7]\n"
    "v_cvt_pk_bf16_f32 v196, v196, v197\n"
    "v_mfma_f32_16x16x32_bf16 v[0:3], a[172:175], v[144:147], v[0:3]\n"
    "ds_read_b64_tr_b16 a[164:165], v244 offset:36864\n"
    "v_mfma_f32_16x16x32_bf16 v[0:3], a[168:171], v[140:143], v[0:3]\n"
    "s_waitcnt lgkmcnt(7)\n"
    "v_exp_f32_e32 v204, v204\n"
    "v_mfma_f32_16x16x32_bf16 v[20:23], a[176:179], v[140:143], v[20:23]\n"
    "ds_read_b64_tr_b16 a[168:169], v245 offset:4096\n"
    "v_mfma_f32_16x16x32_bf16 v[20:23], a[180:183], v[144:147], v[20:23]\n"
    "ds_read_b64_tr_b16 a[166:167], v244 offset:38912\n"
    "v_mfma_f32_16x16x32_bf16 v[24:27], a[180:183], v[152:155], v[24:27]\n"
    "v_exp_f32_e32 v205, v205\n"
    "v_mfma_f32_16x16x32_bf16 v[24:27], a[176:179], v[148:151], v[24:27]\n"
    "v_cvt_pk_bf16_f32 v197, v198, v199\n"
    "v_mfma_f32_16x16x32_bf16 v[28:31], a[176:179], v[156:159], v[28:31]\n"
    "ds_read_b64_tr_b16 a[170:171], v245 offset:6144\n"
    "v_mfma_f32_16x16x32_bf16 v[28:31], a[180:183], v[160:163], v[28:31]\n"
    "v_exp_f32_e32 v206, v206\n"
    "v_mfma_f32_16x16x32_bf16 v[32:35], a[180:183], v[168:171], v[32:35]\n"
    "v_exp_f32_e32 v207, v207\n"
    "v_mfma_f32_16x16x32_bf16 v[32:35], a[176:179], v[164:167], v[32:35]\n"
    "v_cvt_pk_bf16_f32 v204, v204, v205\n"
    "v_mfma_f32_16x16x32_bf16 v[36:39], a[176:179], v[172:175], v[36:39]\n"
    "ds_read_b64_tr_b16 a[172:173], v245 offset:36864\n"
    "v_mfma_f32_16x16x32_bf16 v[36:39], a[180:183], v[176:179], v[36:39]\n"
    "s_waitcnt vmcnt(20)\n"
    "ds_read_b64_tr_b16 a[174:175], v245 offset:38912\n"
    "v_smfmac_f32_16x16x64_bf16 v[224:227], a[232:235], a[216:223], v238\n"
    "s_waitcnt lgkmcnt(8)\n"
    "v_mad_u32_u16 v234, v231, s90, v239 op_sel:[0,0,0,0]\n"
    "v_mfma_f32_16x16x32_bf16 v[56:59], a[188:191], v[176:179], v[56:59]\n"
    "s_bitcmp1_b32 s86, 13\n"
    "buffer_load_dwordx4 a[216:219], v234, s[20:23], 0 offen sc1\n"
    "v_mad_u32_u16 v235, v231, s90, v239 op_sel:[1,0,0,0]\n"
    "v_mfma_f32_16x16x32_bf16 v[56:59], a[184:187], v[172:175], v[56:59]\n"
    "s_cselect_b64 exec, -1, 0\n"
    "v_cvt_pk_bf16_f32 v236, v224, v225\n"
    "v_cvt_pk_bf16_f32 v237, v226, v227\n"
    "v_mfma_f32_16x16x32_bf16 v[52:55], a[184:187], v[164:167], v[52:55]\n"
    "s_cselect_b32 s88, 512, 0\n"
    "v_mov_b64_e32 v[224:225], 0\n"
    "v_mov_b64_e32 v[226:227], 0\n"
    "v_mfma_f32_16x16x32_bf16 v[52:55], a[188:191], v[168:171], v[52:55]\n"
    "buffer_store_dwordx2 v[236:237], v252, s[8:11], s87 offen sc1\n"
    "v_mfma_f32_16x16x32_bf16 v[48:51], a[188:191], v[160:163], v[48:51]\n"
    "s_mov_b64 exec, -1\n"
    "ds_read_b64_tr_b16 a[176:177], v246 offset:4096\n"
    "v_mfma_f32_16x16x32_bf16 v[48:51], a[184:187], v[156:159], v[48:51]\n"
    "s_add_u32 s87, s87, s88\n"
    "ds_read_b64_tr_b16 a[178:179], v246 offset:6144\n"
    "v_mfma_f32_16x16x32_bf16 v[44:47], a[184:187], v[148:151], v[44:47]\n"
    "buffer_load_dwordx4 a[220:223], v235, s[20:23], 0 offen sc1\n"
    "v_mfma_f32_16x16x32_bf16 v[44:47], a[188:191], v[152:155], v[44:47]\n"
    "ds_read_b64_tr_b16 a[180:181], v246 offset:36864\n"
    "v_mfma_f32_16x16x32_bf16 v[40:43], a[188:191], v[144:147], v[40:43]\n"
    "v_exp_f32_e32 v212, v212\n"
    "v_mfma_f32_16x16x32_bf16 v[40:43], a[184:187], v[140:143], v[40:43]\n"
    "s_waitcnt lgkmcnt(6)\n"
    "v_exp_f32_e32 v213, v213\n"
    "v_mfma_f32_16x16x32_bf16 v[60:63], a[160:163], v[140:143], v[60:63]\n"
    "ds_read_b64_tr_b16 a[182:183], v246 offset:38912\n"
    "v_cvt_pk_bf16_f32 v205, v206, v207\n"
    "v_mfma_f32_16x16x32_bf16 v[60:63], a[164:167], v[144:147], v[60:63]\n"
    "ds_read_b64_tr_b16 a[184:185], v247 offset:4096\n"
    "v_mfma_f32_16x16x32_bf16 v[64:67], a[164:167], v[152:155], v[64:67]\n"
    "ds_read_b64_tr_b16 a[186:187], v247 offset:6144\n"
    "v_mfma_f32_16x16x32_bf16 v[64:67], a[160:163], v[148:151], v[64:67]\n"
    "v_exp_f32_e32 v214, v214\n"
    "v_mfma_f32_16x16x32_bf16 v[68:71], a[160:163], v[156:159], v[68:71]\n"
    "v_exp_f32_e32 v215, v215\n"
    "v_mfma_f32_16x16x32_bf16 v[68:71], a[164:167], v[160:163], v[68:71]\n"
    "ds_read_b64_tr_b16 a[188:189], v247 offset:36864\n"
    "v_cvt_pk_bf16_f32 v212, v212, v213\n"
    "v_mfma_f32_16x16x32_bf16 v[72:75], a[164:167], v[168:171], v[72:75]\n"
    "ds_read_b64_tr_b16 a[190:191], v247 offset:38912\n"
    "v_cvt_pk_bf16_f32 v213, v214, v215\n"
    "v_mfma_f32_16x16x32_bf16 v[72:75], a[160:163], v[164:167], v[72:75]\n"
    "s_waitcnt vmcnt(30)\n"
    "v_mfma_f32_16x16x32_bf16 v[76:79], a[160:163], v[172:175], v[76:79]\n"
    "s_barrier\n"
    "v_mfma_f32_16x16x32_bf16 v[76:79], a[164:167], v[176:179], v[76:79]\n"
    "s_waitcnt lgkmcnt(8)\n"
    "s_mov_b32 m0, s42\n"
    "v_mfma_f32_16x16x32_bf16 v[96:99], a[172:175], v[176:179], v[96:99]\n"
    "v_exp_f32_e32 v184, v184\n"
    "v_mfma_f32_16x16x32_bf16 v[96:99], a[168:171], v[172:175], v[96:99]\n"
    "v_exp_f32_e32 v185, v185\n"
    "v_mfma_f32_16x16x32_bf16 v[92:95], a[168:171], v[164:167], v[92:95]\n"
    "v_exp_f32_e32 v186, v186\n"
    "v_mfma_f32_16x16x32_bf16 v[92:95], a[172:175], v[168:171], v[92:95]\n"
    "ds_read_b128 a[160:163], v240 offset:16384\n"
    "ds_write_addtid_b32 v180 offset:0\n"
    "v_mfma_f32_16x16x32_bf16 v[88:91], a[172:175], v[160:163], v[88:91]\n"
    "v_exp_f32_e32 v187, v187\n"
    "v_mfma_f32_16x16x32_bf16 v[88:91], a[168:171], v[156:159], v[88:91]\n"
    "ds_write_addtid_b32 v181 offset:256\n"
    "v_cvt_pk_bf16_f32 v182, v184, v185\n"
    "v_mfma_f32_16x16x32_bf16 v[84:87], a[168:171], v[148:151], v[84:87]\n"
    "v_exp_f32_e32 v192, v192\n"
    "v_mfma_f32_16x16x32_bf16 v[84:87], a[172:175], v[152:155], v[84:87]\n"
    "v_exp_f32_e32 v193, v193\n"
    "v_mfma_f32_16x16x32_bf16 v[80:83], a[172:175], v[144:147], v[80:83]\n"
    "ds_write_addtid_b32 v188 offset:1024\n"
    "v_cvt_pk_bf16_f32 v183, v186, v187\n"
    "v_mfma_f32_16x16x32_bf16 v[80:83], a[168:171], v[140:143], v[80:83]\n"
    "s_waitcnt vmcnt(20)\n"
    "ds_read_b128 a[164:167], v241 offset:16384\n"
    "ds_write_addtid_b32 v189 offset:1280\n"
    "v_smfmac_f32_16x16x64_bf16 v[224:227], a[232:235], a[224:231], v238\n"
    "s_waitcnt lgkmcnt(10)\n"
    "ds_write_addtid_b32 v196 offset:2048\n"
    "v_mad_u32_u16 v234, v232, s90, v239 op_sel:[0,0,0,0]\n"
    "v_mfma_f32_16x16x32_bf16 v[100:103], a[176:179], v[140:143], v[100:103]\n"
    "s_bitcmp1_b32 s86, 14\n"
    "buffer_load_dwordx4 a[224:227], v234, s[20:23], 0 offen sc1\n"
    "v_mad_u32_u16 v235, v232, s90, v239 op_sel:[1,0,0,0]\n"
    "v_mfma_f32_16x16x32_bf16 v[100:103], a[180:183], v[144:147], v[100:103]\n"
    "s_cselect_b64 exec, -1, 0\n"
    "v_cvt_pk_bf16_f32 v236, v224, v225\n"
    "v_cvt_pk_bf16_f32 v237, v226, v227\n"
    "v_mfma_f32_16x16x32_bf16 v[104:107], a[180:183], v[152:155], v[104:107]\n"
    "s_cselect_b32 s88, 512, 0\n"
    "v_mov_b64_e32 v[224:225], 0\n"
    "v_mov_b64_e32 v[226:227], 0\n"
    "v_mfma_f32_16x16x32_bf16 v[104:107], a[176:179], v[148:151], v[104:107]\n"
    "buffer_store_dwordx2 v[236:237], v252, s[8:11], s87 offen sc1\n"
    "v_mfma_f32_16x16x32_bf16 v[108:111], a[176:179], v[156:159], v[108:111]\n"
    "s_mov_b64 exec, -1\n"
    "ds_read_b128 a[168:171], v240 offset:20480\n"
    "ds_write_addtid_b32 v197 offset:2304\n"
    "v_mfma_f32_16x16x32_bf16 v[108:111], a[180:183], v[160:163], v[108:111]\n"
    "s_add_u32 s87, s87, s88\n"
    "v_exp_f32_e32 v194, v194\n"
    "v_mfma_f32_16x16x32_bf16 v[112:115], a[180:183], v[168:171], v[112:115]\n"
    "buffer_load_dwordx4 a[228:231], v235, s[20:23], 0 offen sc1\n"
    "ds_write_addtid_b32 v204 offset:3072\n"
    "v_mfma_f32_16x16x32_bf16 v[112:115], a[176:179], v[164:167], v[112:115]\n"
    "ds_write_addtid_b32 v205 offset:3328\n"
    "v_mfma_f32_16x16x32_bf16 v[116:119], a[176:179], v[172:175], v[116:119]\n"
    "v_exp_f32_e32 v195, v195\n"
    "v_mfma_f32_16x16x32_bf16 v[116:119], a[180:183], v[176:179], v[116:119]\n"
    "s_waitcnt lgkmcnt(11)\n"
    "ds_read_b128 a[172:175], v241 offset:20480\n"
    "v_cvt_pk_bf16_f32 v190, v192, v193\n"
    "v_mfma_f32_16x16x32_bf16 v[136:139], a[188:191], v[176:179], v[136:139]\n"
    "v_exp_f32_e32 v200, v200\n"
    "v_mfma_f32_16x16x32_bf16 v[136:139], a[184:187], v[172:175], v[136:139]\n"
    "v_exp_f32_e32 v201, v201\n"
    "v_mfma_f32_16x16x32_bf16 v[132:135], a[184:187], v[164:167], v[132:135]\n"
    "ds_read_b128 a[176:179], v242 offset:16384\n"
    "v_cvt_pk_bf16_f32 v191, v194, v195\n"
    "v_mfma_f32_16x16x32_bf16 v[132:135], a[188:191], v[168:171], v[132:135]\n"
    "v_exp_f32_e32 v202, v202\n"
    "v_mfma_f32_16x16x32_bf16 v[128:131], a[188:191], v[160:163], v[128:131]\n"
    "v_exp_f32_e32 v203, v203\n"
    "v_mfma_f32_16x16x32_bf16 v[128:131], a[184:187], v[156:159], v[128:131]\n"
    "ds_write_addtid_b32 v212 offset:4096\n"
    "v_cvt_pk_bf16_f32 v198, v200, v201\n"
    "v_mfma_f32_16x16x32_bf16 v[124:127], a[184:187], v[148:151], v[124:127]\n"
    "v_exp_f32_e32 v208, v208\n"
    "v_mfma_f32_16x16x32_bf16 v[124:127], a[188:191], v[152:155], v[124:127]\n"
    "ds_read_b128 a[180:183], v243 offset:16384\n"
    "ds_write_addtid_b32 v213 offset:4352\n"
    "v_mfma_f32_16x16x32_bf16 v[120:123], a[188:191], v[144:147], v[120:123]\n"
    "v_exp_f32_e32 v209, v209\n"
    "v_mfma_f32_16x16x32_bf16 v[120:123], a[184:187], v[140:143], v[120:123]\n"
    "s_waitcnt lgkmcnt(11)\n"
    "ds_write_addtid_b32 v182 offset:512\n"
    "v_cvt_pk_bf16_f32 v199, v202, v203\n"
    "v_mfma_f32_16x16x32_bf16 v[172:175], a[160:163], a[128:131], v[220:223]\n"
    "ds_write_addtid_b32 v183 offset:768\n"
    "v_mfma_f32_16x16x32_bf16 v[172:175], a[164:167], a[132:135], v[172:175]\n"
    "v_exp_f32_e32 v210, v210\n"
    "v_mfma_f32_16x16x32_bf16 v[164:167], a[164:167], a[100:103], v[220:223]\n"
    "ds_write_addtid_b32 v190 offset:1536\n"
    "v_mfma_f32_16x16x32_bf16 v[164:167], a[160:163], a[96:99], v[164:167]\n"
    "ds_read_b128 a[184:187], v242 offset:20480\n"
    "v_mfma_f32_16x16x32_bf16 v[156:159], a[160:163], a[64:67], v[220:223]\n"
    "v_exp_f32_e32 v211, v211\n"
    "v_mfma_f32_16x16x32_bf16 v[156:159], a[164:167], a[68:71], v[156:159]\n"
    "ds_write_addtid_b32 v191 offset:1792\n"
    "v_cvt_pk_bf16_f32 v206, v208, v209\n"
    "v_mfma_f32_16x16x32_bf16 v[148:151], a[164:167], a[36:39], v[220:223]\n"
    "v_exp_f32_e32 v216, v216\n"
    "v_mfma_f32_16x16x32_bf16 v[148:151], a[160:163], a[32:35], v[148:151]\n"
    "v_exp_f32_e32 v217, v217\n"
    "v_mfma_f32_16x16x32_bf16 v[140:143], a[160:163], a[0:3], v[220:223]\n"
    "ds_write_addtid_b32 v198 offset:2560\n"
    "v_cvt_pk_bf16_f32 v207, v210, v211\n"
    "v_mfma_f32_16x16x32_bf16 v[140:143], a[164:167], a[4:7], v[140:143]\n"
    "s_waitcnt vmcnt(20)\n"
    "v_exp_f32_e32 v218, v218\n"
    "v_smfmac_f32_16x16x64_bf16 v[224:227], a[232:235], a[192:199], v238\n"
    "s_waitcnt lgkmcnt(10)\n"
    "ds_read_b128 a[188:191], v243 offset:20480\n"
    "v_mfma_f32_16x16x32_bf16 v[140:143], a[168:171], a[8:11], v[140:143]\n"
    "s_bitcmp1_b32 s86, 15\n"
    "ds_read_b128 a[160:163], v240 offset:18432\n"
    "v_mfma_f32_16x16x32_bf16 v[140:143], a[172:175], a[12:15], v[140:143]\n"
    "s_cselect_b64 exec, -1, 0\n"
    "v_cvt_pk_bf16_f32 v236, v224, v225\n"
    "v_cvt_pk_bf16_f32 v237, v226, v227\n"
    "v_mfma_f32_16x16x32_bf16 v[148:151], a[172:175], a[44:47], v[148:151]\n"
    "s_cselect_b32 s88, 512, 0\n"
    "v_mov_b64_e32 v[224:225], 0\n"
    "v_mov_b64_e32 v[226:227], 0\n"
    "v_mfma_f32_16x16x32_bf16 v[148:151], a[168:171], a[40:43], v[148:151]\n"
    "buffer_store_dwordx2 v[236:237], v252, s[8:11], s87 offen sc1\n"
    "v_mfma_f32_16x16x32_bf16 v[156:159], a[168:171], a[72:75], v[156:159]\n"
    "s_mov_b64 exec, -1\n"
    "ds_read2_b32 v[228:229], v251 offset0:30 offset1:32\n"
    "v_mfma_f32_16x16x32_bf16 v[156:159], a[172:175], a[76:79], v[156:159]\n"
    "s_add_u32 s87, s87, s88\n"
    "ds_read2_b32 v[230:231], v251 offset0:34 offset1:36\n"
    "v_mfma_f32_16x16x32_bf16 v[164:167], a[172:175], a[108:111], v[164:167]\n"
    "ds_read_b32 v232, v251 offset:152\n"
    "v_mfma_f32_16x16x32_bf16 v[164:167], a[168:171], a[104:107], v[164:167]\n"
    "v_exp_f32_e32 v219, v219\n"
    "v_mfma_f32_16x16x32_bf16 v[172:175], a[168:171], a[136:139], v[172:175]\n"
    "ds_write_addtid_b32 v199 offset:2816\n"
    "v_cvt_pk_bf16_f32 v214, v216, v217\n"
    "v_mfma_f32_16x16x32_bf16 v[172:175], a[172:175], a[140:143], v[172:175]\n"
    "s_waitcnt lgkmcnt(13)\n"
    "ds_read_b128 a[164:167], v241 offset:18432\n"
    "v_mfma_f32_16x16x32_bf16 v[172:175], a[176:179], a[144:147], v[172:175]\n"
    "ds_write_addtid_b32 v206 offset:3584\n"
    "v_cvt_pk_bf16_f32 v215, v218, v219\n"
    "v_mfma_f32_16x16x32_bf16 v[172:175], a[180:183], a[148:151], v[172:175]\n"
    "ds_read_b128 a[168:171], v240 offset:22528\n"
    "v_mfma_f32_16x16x32_bf16 v[164:167], a[180:183], a[116:119], v[164:167]\n"
    "s_waitcnt lgkmcnt(6)\n"
    "ds_write_addtid_b32 v207 offset:3840\n"
    "v_mad_u32_u16 v234, v228, s90, v239 op_sel:[0,0,0,0]\n"
    "v_mfma_f32_16x16x32_bf16 v[164:167], a[176:179], a[112:115], v[164:167]\n"
    "buffer_load_dwordx4 a[192:195], v234, s[20:23], 0 offen sc1\n"
    "v_mad_u32_u16 v235, v228, s90, v239 op_sel:[1,0,0,0]\n"
    "v_mfma_f32_16x16x32_bf16 v[156:159], a[176:179], a[80:83], v[156:159]\n"
    "ds_write_addtid_b32 v214 offset:4608\n"
    "v_add_u32_e32 v251, 160, v251\n"
    "v_mfma_f32_16x16x32_bf16 v[156:159], a[180:183], a[84:87], v[156:159]\n"
    "ds_write_addtid_b32 v215 offset:4864\n"
    "v_mfma_f32_16x16x32_bf16 v[148:151], a[180:183], a[52:55], v[148:151]\n"
    "buffer_load_dwordx4 a[196:199], v235, s[20:23], 0 offen sc1\n"
    "v_mfma_f32_16x16x32_bf16 v[148:151], a[176:179], a[48:51], v[148:151]\n"
    "v_mfma_f32_16x16x32_bf16 v[140:143], a[176:179], a[16:19], v[140:143]\n"
    "s_waitcnt lgkmcnt(0)\n"
    "v_mfma_f32_16x16x32_bf16 v[140:143], a[180:183], a[20:23], v[140:143]\n"
    "s_barrier\n"
    "v_mfma_f32_16x16x32_bf16 v[140:143], a[184:187], a[24:27], v[140:143]\n"
    "s_add_u32 m0, s43, 0\n"
    "ds_read_b128 a[172:175], v241 offset:22528\n"
    "v_mfma_f32_16x16x32_bf16 v[140:143], a[188:191], a[28:31], v[140:143]\n"
    "ds_read2st64_b32 v[184:185], v249 offset0:0 offset1:1\n"
    "v_mfma_f32_16x16x32_bf16 v[148:151], a[188:191], a[60:63], v[148:151]\n"
    "buffer_load_dwordx4 v248, s[8:11], s40 offen offset:0 sc1 lds\n"
    "v_mfma_f32_16x16x32_bf16 v[148:151], a[184:187], a[56:59], v[148:151]\n"
    "ds_read_b128 a[176:179], v242 offset:18432\n"
    "v_mfma_f32_16x16x32_bf16 v[156:159], a[184:187], a[88:91], v[156:159]\n"
    "v_mfma_f32_16x16x32_bf16 v[156:159], a[188:191], a[92:95], v[156:159]\n"
    "v_mfma_f32_16x16x32_bf16 v[164:167], a[188:191], a[124:127], v[164:167]\n"
    "buffer_load_dwordx4 v248, s[8:11], s40 offen offset:1024 sc1 lds\n"
    "v_mfma_f32_16x16x32_bf16 v[164:167], a[184:187], a[120:123], v[164:167]\n"
    "ds_read2st64_b32 v[186:187], v249 offset0:2 offset1:3\n"
    "v_mfma_f32_16x16x32_bf16 v[172:175], a[184:187], a[152:155], v[172:175]\n"
    "v_mfma_f32_16x16x32_bf16 v[172:175], a[188:191], a[156:159], v[172:175]\n"
    "ds_read_b128 a[180:183], v243 offset:18432\n"
    "v_mfma_f32_16x16x32_bf16 v[176:179], a[160:163], a[128:131], v[220:223]\n"
    "buffer_load_dwordx4 v248, s[8:11], s40 offen offset:2048 sc1 lds\n"
    "v_mfma_f32_16x16x32_bf16 v[176:179], a[164:167], a[132:135], v[176:179]\n"
    "ds_read2st64_b32 v[192:193], v249 offset0:4 offset1:5\n"
    "v_mfma_f32_16x16x32_bf16 v[168:171], a[164:167], a[100:103], v[220:223]\n"
    "v_exp_f32_e32 v140, v140\n"
    "v_mfma_f32_16x16x32_bf16 v[168:171], a[160:163], a[96:99], v[168:171]\n"
    "ds_read_b128 a[184:187], v242 offset:22528\n"
    "v_mfma_f32_16x16x32_bf16 v[160:163], a[160:163], a[64:67], v[220:223]\n"
    "buffer_load_dwordx4 v248, s[8:11], s40 offen offset:3072 sc1 lds\n"
    "v_mfma_f32_16x16x32_bf16 v[160:163], a[164:167], a[68:71], v[160:163]\n"
    "s_add_u32 m0, s43, 4096\n"
    "ds_read2st64_b32 v[194:195], v249 offset0:6 offset1:7\n"
    "v_mfma_f32_16x16x32_bf16 v[152:155], a[164:167], a[36:39], v[220:223]\n"
    "s_add_u32 s40, s40, 0x8000\n"
    "ds_read2st64_b32 v[200:201], v249 offset0:8 offset1:9\n"
    "v_mfma_f32_16x16x32_bf16 v[152:155], a[160:163], a[32:35], v[152:155]\n"
    "ds_read2st64_b32 v[202:203], v249 offset0:10 offset1:11\n"
    "v_mfma_f32_16x16x32_bf16 v[144:147], a[160:163], a[0:3], v[220:223]\n"
    "buffer_load_dwordx4 v248, s[8:11], s41 offen offset:0 sc1 lds\n"
    "v_mfma_f32_16x16x32_bf16 v[144:147], a[164:167], a[4:7], v[144:147]\n"
    "s_waitcnt vmcnt(17)\n"
    "ds_read_b128 a[188:191], v243 offset:22528\n"
    "v_smfmac_f32_16x16x64_bf16 v[224:227], a[232:235], a[200:207], v238\n"
    "s_waitcnt lgkmcnt(10)\n"
    "v_mad_u32_u16 v234, v229, s90, v239 op_sel:[0,0,0,0]\n"
    "v_mfma_f32_16x16x32_bf16 v[144:147], a[168:171], a[8:11], v[144:147]\n"
    "s_bitcmp1_b32 s86, 16\n"
    "buffer_load_dwordx4 a[200:203], v234, s[20:23], 0 offen sc1\n"
    "v_mad_u32_u16 v235, v229, s90, v239 op_sel:[1,0,0,0]\n"
    "v_mfma_f32_16x16x32_bf16 v[144:147], a[172:175], a[12:15], v[144:147]\n"
    "s_cselect_b64 exec, -1, 0\n"
    "v_cvt_pk_bf16_f32 v236, v224, v225\n"
    "v_cvt_pk_bf16_f32 v237, v226, v227\n"
    "v_mfma_f32_16x16x32_bf16 v[152:155], a[172:175], a[44:47], v[152:155]\n"
    "s_cselect_b32 s88, 512, 0\n"
    "v_mov_b64_e32 v[224:225], 0\n"
    "v_mov_b64_e32 v[226:227], 0\n"
    "v_mfma_f32_16x16x32_bf16 v[152:155], a[168:171], a[40:43], v[152:155]\n"
    "buffer_store_dwordx2 v[236:237], v252, s[8:11], s87 offen sc1\n"
    "v_mfma_f32_16x16x32_bf16 v[160:163], a[168:171], a[72:75], v[160:163]\n"
    "s_mov_b64 exec, -1\n"
    "ds_read_b64_tr_b16 a[160:161], v244 offset:8192\n"
    "v_mfma_f32_16x16x32_bf16 v[160:163], a[172:175], a[76:79], v[160:163]\n"
    "s_add_u32 s87, s87, s88\n"
    "ds_read_b64_tr_b16 a[162:163], v244 offset:10240\n"
    "v_mfma_f32_16x16x32_bf16 v[168:171], a[172:175], a[108:111], v[168:171]\n"
    "buffer_load_dwordx4 v248, s[8:11], s41 offen offset:1024 sc1 lds\n"
    "v_mfma_f32_16x16x32_bf16 v[168:171], a[168:171], a[104:107], v[168:171]\n"
    "ds_read_b64_tr_b16 a[164:165], v244 offset:40960\n"
    "v_mfma_f32_16x16x32_bf16 v[176:179], a[168:171], a[136:139], v[176:179]\n"
    "ds_read2st64_b32 v[208:209], v249 offset0:12 offset1:13\n"
    "v_mfma_f32_16x16x32_bf16 v[176:179], a[172:175], a[140:143], v[176:179]\n"
    "s_waitcnt lgkmcnt(10)\n"
    "ds_read2st64_b32 v[210:211], v249 offset0:14 offset1:15\n"
    "v_mfma_f32_16x16x32_bf16 v[176:179], a[176:179], a[144:147], v[176:179]\n"
    "buffer_load_dwordx4 v248, s[8:11], s41 offen offset:2048 sc1 lds\n"
    "v_mfma_f32_16x16x32_bf16 v[176:179], a[180:183], a[148:151], v[176:179]\n"
    "ds_read_b64_tr_b16 a[166:167], v244 offset:43008\n"
    "v_mfma_f32_16x16x32_bf16 v[168:171], a[180:183], a[116:119], v[168:171]\n"
    "ds_read_b64_tr_b16 a[168:169], v245 offset:8192\n"
    "v_mfma_f32_16x16x32_bf16 v[168:171], a[176:179], a[112:115], v[168:171]\n"
    "v_exp_f32_e32 v141, v141\n"
    "v_mfma_f32_16x16x32_bf16 v[160:163], a[176:179], a[80:83], v[160:163]\n"
    "buffer_load_dwordx4 v248, s[8:11], s41 offen offset:3072 sc1 lds\n"
    "v_mfma_f32_16x16x32_bf16 v[160:163], a[180:183], a[84:87], v[160:163]\n"
    "ds_read_b64_tr_b16 a[170:171], v245 offset:10240\n"
    "v_mfma_f32_16x16x32_bf16 v[152:155], a[180:183], a[52:55], v[152:155]\n"
    "s_add_u32 s41, s41, 0x8000\n"
    "ds_read2st64_b32 v[216:217], v249 offset0:16 offset1:17\n"
    "v_mfma_f32_16x16x32_bf16 v[152:155], a[176:179], a[48:51], v[152:155]\n"
    "buffer_load_dwordx4 a[204:207], v235, s[20:23], 0 offen sc1\n"
    "v_mfma_f32_16x16x32_bf16 v[144:147], a[176:179], a[16:19], v[144:147]\n"
    "ds_read_b64_tr_b16 a[172:173], v245 offset:40960\n"
    "v_mfma_f32_16x16x32_bf16 v[144:147], a[180:183], a[20:23], v[144:147]\n"
    "s_waitcnt lgkmcnt(10)\n"
    "ds_read2st64_b32 v[218:219], v249 offset0:18 offset1:19\n"
    "v_mfma_f32_16x16x32_bf16 v[144:147], a[184:187], a[24:27], v[144:147]\n"
    "ds_read_b64_tr_b16 a[176:177], v246 offset:8192\n"
    "v_mfma_f32_16x16x32_bf16 v[144:147], a[188:191], a[28:31], v[144:147]\n"
    "ds_read_b64_tr_b16 a[174:175], v245 offset:43008\n"
    "v_mfma_f32_16x16x32_bf16 v[152:155], a[188:191], a[60:63], v[152:155]\n"
    "v_exp_f32_e32 v142, v142\n"
    "v_mfma_f32_16x16x32_bf16 v[152:155], a[184:187], a[56:59], v[152:155]\n"
    "v_exp_f32_e32 v143, v143\n"
    "v_mfma_f32_16x16x32_bf16 v[160:163], a[184:187], a[88:91], v[160:163]\n"
    "ds_read_b64_tr_b16 a[178:179], v246 offset:10240\n"
    "v_cvt_pk_bf16_f32 v140, v140, v141\n"
    "v_mfma_f32_16x16x32_bf16 v[160:163], a[188:191], a[92:95], v[160:163]\n"
    "v_exp_f32_e32 v148, v148\n"
    "v_mfma_f32_16x16x32_bf16 v[168:171], a[188:191], a[124:127], v[168:171]\n"
    "v_exp_f32_e32 v149, v149\n"
    "v_mfma_f32_16x16x32_bf16 v[168:171], a[184:187], a[120:123], v[168:171]\n"
    "v_cvt_pk_bf16_f32 v141, v142, v143\n"
    "v_mfma_f32_16x16x32_bf16 v[176:179], a[184:187], a[152:155], v[176:179]\n"
    "ds_read_b64_tr_b16 a[180:181], v246 offset:40960\n"
    "v_mfma_f32_16x16x32_bf16 v[176:179], a[188:191], a[156:159], v[176:179]\n"
    "s_waitcnt vmcnt(20)\n"
    "ds_read_b64_tr_b16 a[182:183], v246 offset:43008\n"
    "v_smfmac_f32_16x16x64_bf16 v[224:227], a[232:235], a[208:215], v238\n"
    "s_waitcnt lgkmcnt(5)\n"
    "v_mad_u32_u16 v234, v230, s90, v239 op_sel:[0,0,0,0]\n"
    "v_mfma_f32_16x16x32_bf16 a[236:239], a[160:163], v[180:183], a[236:239]\n"
    "s_bitcmp1_b32 s86, 17\n"
    "buffer_load_dwordx4 a[208:211], v234, s[20:23], 0 offen sc1\n"
    "v_mad_u32_u16 v235, v230, s90, v239 op_sel:[1,0,0,0]\n"
    "v_mfma_f32_16x16x32_bf16 a[236:239], a[164:167], v[184:187], a[236:239]\n"
    "s_cselect_b64 exec, -1, 0\n"
    "v_cvt_pk_bf16_f32 v236, v224, v225\n"
    "v_cvt_pk_bf16_f32 v237, v226, v227\n"
    "v_mfma_f32_16x16x32_bf16 a[240:243], a[164:167], v[192:195], a[240:243]\n"
    "s_cselect_b32 s88, 512, 0\n"
    "v_mov_b64_e32 v[224:225], 0\n"
    "v_mov_b64_e32 v[226:227], 0\n"
    "v_mfma_f32_16x16x32_bf16 a[240:243], a[160:163], v[188:191], a[240:243]\n"
    "buffer_store_dwordx2 v[236:237], v252, s[8:11], s87 offen sc1\n"
    "v_mfma_f32_16x16x32_bf16 a[244:247], a[160:163], v[196:199], a[244:247]\n"
    "s_mov_b64 exec, -1\n"
    "ds_read_b64_tr_b16 a[184:185], v247 offset:8192\n"
    "v_mfma_f32_16x16x32_bf16 a[244:247], a[164:167], v[200:203], a[244:247]\n"
    "s_add_u32 s87, s87, s88\n"
    "ds_read_b64_tr_b16 a[186:187], v247 offset:10240\n"
    "v_mfma_f32_16x16x32_bf16 a[248:251], a[164:167], v[208:211], a[248:251]\n"
    "buffer_load_dwordx4 a[212:215], v235, s[20:23], 0 offen sc1\n"
    "v_mfma_f32_16x16x32_bf16 a[248:251], a[160:163], v[204:207], a[248:251]\n"
    "ds_read_b64_tr_b16 a[188:189], v247 offset:40960\n"
    "v_mfma_f32_16x16x32_bf16 a[252:255], a[160:163], v[212:215], a[252:255]\n"
    "v_exp_f32_e32 v150, v150\n"
    "v_mfma_f32_16x16x32_bf16 a[252:255], a[164:167], v[216:219], a[252:255]\n"
    "s_waitcnt lgkmcnt(6)\n"
    "v_exp_f32_e32 v151, v151\n"
    "v_mfma_f32_16x16x32_bf16 v[16:19], a[172:175], v[216:219], v[16:19]\n"
    "ds_read_b64_tr_b16 a[190:191], v247 offset:43008\n"
    "v_cvt_pk_bf16_f32 v148, v148, v149\n"
    "v_mfma_f32_16x16x32_bf16 v[16:19], a[168:171], v[212:215], v[16:19]\n"
    "ds_read_b64_tr_b16 a[160:161], v244 offset:12288\n"
    "v_mfma_f32_16x16x32_bf16 v[12:15], a[168:171], v[204:207], v[12:15]\n"
    "v_exp_f32_e32 v156, v156\n"
    "v_mfma_f32_16x16x32_bf16 v[12:15], a[172:175], v[208:211], v[12:15]\n"
    "v_exp_f32_e32 v157, v157\n"
    "v_mfma_f32_16x16x32_bf16 v[8:11], a[172:175], v[200:203], v[8:11]\n"
    "ds_read_b64_tr_b16 a[162:163], v244 offset:14336\n"
    "v_cvt_pk_bf16_f32 v149, v150, v151\n"
    "v_mfma_f32_16x16x32_bf16 v[8:11], a[168:171], v[196:199], v[8:11]\n"
    "v_exp_f32_e32 v158, v158\n"
    "v_mfma_f32_16x16x32_bf16 v[4:7], a[168:171], v[188:191], v[4:7]\n"
    "v_exp_f32_e32 v159, v159\n"
    "v_mfma_f32_16x16x32_bf16 v[4:7], a[172:175], v[192:195], v[4:7]\n"
    "v_cvt_pk_bf16_f32 v156, v156, v157\n"
    "v_mfma_f32_16x16x32_bf16 v[0:3], a[172:175], v[184:187], v[0:3]\n"
    "ds_read_b64_tr_b16 a[164:165], v244 offset:45056\n"
    "v_mfma_f32_16x16x32_bf16 v[0:3], a[168:171], v[180:183], v[0:3]\n"
    "s_waitcnt lgkmcnt(7)\n"
    "v_exp_f32_e32 v164, v164\n"
    "v_mfma_f32_16x16x32_bf16 v[20:23], a[176:179], v[180:183], v[20:23]\n"
    "ds_read_b64_tr_b16 a[168:169], v245 offset:12288\n"
    "v_mfma_f32_16x16x32_bf16 v[20:23], a[180:183], v[184:187], v[20:23]\n"
    "ds_read_b64_tr_b16 a[166:167], v244 offset:47104\n"
    "v_mfma_f32_16x16x32_bf16 v[24:27], a[180:183], v[192:195], v[24:27]\n"
    "v_exp_f32_e32 v165, v165\n"
    "v_mfma_f32_16x16x32_bf16 v[24:27], a[176:179], v[188:191], v[24:27]\n"
    "v_cvt_pk_bf16_f32 v157, v158, v159\n"
    "v_mfma_f32_16x16x32_bf16 v[28:31], a[176:179], v[196:199], v[28:31]\n"
    "ds_read_b64_tr_b16 a[170:171], v245 offset:14336\n"
    "v_mfma_f32_16x16x32_bf16 v[28:31], a[180:183], v[200:203], v[28:31]\n"
    "v_exp_f32_e32 v166, v166\n"
    "v_mfma_f32_16x16x32_bf16 v[32:35], a[180:183], v[208:211], v[32:35]\n"
    "v_exp_f32_e32 v167, v167\n"
    "v_mfma_f32_16x16x32_bf16 v[32:35], a[176:179], v[204:207], v[32:35]\n"
    "v_cvt_pk_bf16_f32 v164, v164, v165\n"
    "v_mfma_f32_16x16x32_bf16 v[36:39], a[176:179], v[212:215], v[36:39]\n"
    "ds_read_b64_tr_b16 a[172:173], v245 offset:45056\n"
    "v_mfma_f32_16x16x32_bf16 v[36:39], a[180:183], v[216:219], v[36:39]\n"
    "s_waitcnt vmcnt(20)\n"
    "ds_read_b64_tr_b16 a[174:175], v245 offset:47104\n"
    "v_smfmac_f32_16x16x64_bf16 v[224:227], a[232:235], a[216:223], v238\n"
    "s_waitcnt lgkmcnt(8)\n"
    "v_mad_u32_u16 v234, v231, s90, v239 op_sel:[0,0,0,0]\n"
    "v_mfma_f32_16x16x32_bf16 v[56:59], a[188:191], v[216:219], v[56:59]\n"
    "s_bitcmp1_b32 s86, 18\n"
    "buffer_load_dwordx4 a[216:219], v234, s[20:23], 0 offen sc1\n"
    "v_mad_u32_u16 v235, v231, s90, v239 op_sel:[1,0,0,0]\n"
    "v_mfma_f32_16x16x32_bf16 v[56:59], a[184:187], v[212:215], v[56:59]\n"
    "s_cselect_b64 exec, -1, 0\n"
    "v_cvt_pk_bf16_f32 v236, v224, v225\n"
    "v_cvt_pk_bf16_f32 v237, v226, v227\n"
    "v_mfma_f32_16x16x32_bf16 v[52:55], a[184:187], v[204:207], v[52:55]\n"
    "s_cselect_b32 s88, 512, 0\n"
    "v_mov_b64_e32 v[224:225], 0\n"
    "v_mov_b64_e32 v[226:227], 0\n"
    "v_mfma_f32_16x16x32_bf16 v[52:55], a[188:191], v[208:211], v[52:55]\n"
    "buffer_store_dwordx2 v[236:237], v252, s[8:11], s87 offen sc1\n"
    "v_mfma_f32_16x16x32_bf16 v[48:51], a[188:191], v[200:203], v[48:51]\n"
    "s_mov_b64 exec, -1\n"
    "ds_read_b64_tr_b16 a[176:177], v246 offset:12288\n"
    "v_mfma_f32_16x16x32_bf16 v[48:51], a[184:187], v[196:199], v[48:51]\n"
    "s_add_u32 s87, s87, s88\n"
    "ds_read_b64_tr_b16 a[178:179], v246 offset:14336\n"
    "v_mfma_f32_16x16x32_bf16 v[44:47], a[184:187], v[188:191], v[44:47]\n"
    "buffer_load_dwordx4 a[220:223], v235, s[20:23], 0 offen sc1\n"
    "v_mfma_f32_16x16x32_bf16 v[44:47], a[188:191], v[192:195], v[44:47]\n"
    "ds_read_b64_tr_b16 a[180:181], v246 offset:45056\n"
    "v_mfma_f32_16x16x32_bf16 v[40:43], a[188:191], v[184:187], v[40:43]\n"
    "v_exp_f32_e32 v172, v172\n"
    "v_mfma_f32_16x16x32_bf16 v[40:43], a[184:187], v[180:183], v[40:43]\n"
    "s_waitcnt lgkmcnt(6)\n"
    "v_exp_f32_e32 v173, v173\n"
    "v_mfma_f32_16x16x32_bf16 v[60:63], a[160:163], v[180:183], v[60:63]\n"
    "ds_read_b64_tr_b16 a[182:183], v246 offset:47104\n"
    "v_cvt_pk_bf16_f32 v165, v166, v167\n"
    "v_mfma_f32_16x16x32_bf16 v[60:63], a[164:167], v[184:187], v[60:63]\n"
    "ds_read_b64_tr_b16 a[184:185], v247 offset:12288\n"
    "v_mfma_f32_16x16x32_bf16 v[64:67], a[164:167], v[192:195], v[64:67]\n"
    "ds_read_b64_tr_b16 a[186:187], v247 offset:14336\n"
    "v_mfma_f32_16x16x32_bf16 v[64:67], a[160:163], v[188:191], v[64:67]\n"
    "v_exp_f32_e32 v174, v174\n"
    "v_mfma_f32_16x16x32_bf16 v[68:71], a[160:163], v[196:199], v[68:71]\n"
    "v_exp_f32_e32 v175, v175\n"
    "v_mfma_f32_16x16x32_bf16 v[68:71], a[164:167], v[200:203], v[68:71]\n"
    "ds_read_b64_tr_b16 a[188:189], v247 offset:45056\n"
    "v_cvt_pk_bf16_f32 v172, v172, v173\n"
    "v_mfma_f32_16x16x32_bf16 v[72:75], a[164:167], v[208:211], v[72:75]\n"
    "ds_read_b64_tr_b16 a[190:191], v247 offset:47104\n"
    "v_cvt_pk_bf16_f32 v173, v174, v175\n"
    "v_mfma_f32_16x16x32_bf16 v[72:75], a[160:163], v[204:207], v[72:75]\n"
    "s_waitcnt vmcnt(30)\n"
    "v_mfma_f32_16x16x32_bf16 v[76:79], a[160:163], v[212:215], v[76:79]\n"
    "s_barrier\n"
    "v_mfma_f32_16x16x32_bf16 v[76:79], a[164:167], v[216:219], v[76:79]\n"
    "s_waitcnt lgkmcnt(8)\n"
    "s_mov_b32 m0, s42\n"
    "v_mfma_f32_16x16x32_bf16 v[96:99], a[172:175], v[216:219], v[96:99]\n"
    "v_exp_f32_e32 v144, v144\n"
    "v_mfma_f32_16x16x32_bf16 v[96:99], a[168:171], v[212:215], v[96:99]\n"
    "v_exp_f32_e32 v145, v145\n"
    "v_mfma_f32_16x16x32_bf16 v[92:95], a[168:171], v[204:207], v[92:95]\n"
    "v_exp_f32_e32 v146, v146\n"
    "v_mfma_f32_16x16x32_bf16 v[92:95], a[172:175], v[208:211], v[92:95]\n"
    "ds_read_b128 a[160:163], v240 offset:24576\n"
    "ds_write_addtid_b32 v140 offset:0\n"
    "v_mfma_f32_16x16x32_bf16 v[88:91], a[172:175], v[200:203], v[88:91]\n"
    "v_exp_f32_e32 v147, v147\n"
    "v_mfma_f32_16x16x32_bf16 v[88:91], a[168:171], v[196:199], v[88:91]\n"
    "ds_write_addtid_b32 v141 offset:256\n"
    "v_cvt_pk_bf16_f32 v142, v144, v145\n"
    "v_mfma_f32_16x16x32_bf16 v[84:87], a[168:171], v[188:191], v[84:87]\n"
    "v_exp_f32_e32 v152, v152\n"
    "v_mfma_f32_16x16x32_bf16 v[84:87], a[172:175], v[192:195], v[84:87]\n"
    "v_exp_f32_e32 v153, v153\n"
    "v_mfma_f32_16x16x32_bf16 v[80:83], a[172:175], v[184:187], v[80:83]\n"
    "ds_write_addtid_b32 v148 offset:1024\n"
    "v_cvt_pk_bf16_f32 v143, v146, v147\n"
    "v_mfma_f32_16x16x32_bf16 v[80:83], a[168:171], v[180:183], v[80:83]\n"
    "s_waitcnt vmcnt(20)\n"
    "ds_read_b128 a[164:167], v241 offset:24576\n"
    "ds_write_addtid_b32 v149 offset:1280\n"
    "v_smfmac_f32_16x16x64_bf16 v[224:227], a[232:235], a[224:231], v238\n"
    "s_waitcnt lgkmcnt(10)\n"
    "ds_write_addtid_b32 v156 offset:2048\n"
    "v_mad_u32_u16 v234, v232, s90, v239 op_sel:[0,0,0,0]\n"
    "v_mfma_f32_16x16x32_bf16 v[100:103], a[176:179], v[180:183], v[100:103]\n"
    "s_bitcmp1_b32 s86, 19\n"
    "buffer_load_dwordx4 a[224:227], v234, s[20:23], 0 offen sc1\n"
    "v_mad_u32_u16 v235, v232, s90, v239 op_sel:[1,0,0,0]\n"
    "v_mfma_f32_16x16x32_bf16 v[100:103], a[180:183], v[184:187], v[100:103]\n"
    "s_cselect_b64 exec, -1, 0\n"
    "v_cvt_pk_bf16_f32 v236, v224, v225\n"
    ".long 0xbedf00ff\n"
    ".Lat_w8:\n"
    "s_setpc_b64 s[2:3]\n"
    "v_cvt_pk_bf16_f32 v237, v226, v227\n"
    "v_mfma_f32_16x16x32_bf16 v[104:107], a[180:183], v[192:195], v[104:107]\n"
    "s_cselect_b32 s88, 512, 0\n"
    "v_mov_b64_e32 v[224:225], 0\n"
    "v_mov_b64_e32 v[226:227], 0\n"
    "v_mfma_f32_16x16x32_bf16 v[104:107], a[176:179], v[188:191], v[104:107]\n"
    "buffer_store_dwordx2 v[236:237], v252, s[8:11], s87 offen sc1\n"
    "v_mfma_f32_16x16x32_bf16 v[108:111], a[176:179], v[196:199], v[108:111]\n"
    "s_mov_b64 exec, -1\n"
    "ds_read_b128 a[168:171], v240 offset:28672\n"
    "ds_write_addtid_b32 v157 offset:2304\n"
    "v_mfma_f32_16x16x32_bf16 v[108:111], a[180:183], v[200:203], v[108:111]\n"
    "s_add_u32 s87, s87, s88\n"
    "v_exp_f32_e32 v154, v154\n"
    "v_mfma_f32_16x16x32_bf16 v[112:115], a[180:183], v[208:211], v[112:115]\n"
    "s_add_u32 s89, s89, 1\n"
    "buffer_load_dwordx4 a[228:231], v235, s[20:23], 0 offen sc1\n"
    "ds_write_addtid_b32 v164 offset:3072\n"
    "v_mfma_f32_16x16x32_bf16 v[112:115], a[176:179], v[204:207], v[112:115]\n"
    "ds_write_addtid_b32 v165 offset:3328\n"
    "v_readlane_b32 s86, v254, s89\n"
    "v_mfma_f32_16x16x32_bf16 v[116:119], a[176:179], v[212:215], v[116:119]\n"
    "v_exp_f32_e32 v155, v155\n"
    "v_mfma_f32_16x16x32_bf16 v[116:119], a[180:183], v[216:219], v[116:119]\n"
    "s_waitcnt lgkmcnt(11)\n"
    "ds_read_b128 a[172:175], v241 offset:28672\n"
    "v_cvt_pk_bf16_f32 v150, v152, v153\n"
    "v_mfma_f32_16x16x32_bf16 v[136:139], a[188:191], v[216:219], v[136:139]\n"
    "v_exp_f32_e32 v160, v160\n"
    "v_mfma_f32_16x16x32_bf16 v[136:139], a[184:187], v[212:215], v[136:139]\n"
    "v_exp_f32_e32 v161, v161\n"
    "v_mfma_f32_16x16x32_bf16 v[132:135], a[184:187], v[204:207], v[132:135]\n"
    "ds_read_b128 a[176:179], v242 offset:24576\n"
    "v_cvt_pk_bf16_f32 v151, v154, v155\n"
    "v_mfma_f32_16x16x32_bf16 v[132:135], a[188:191], v[208:211], v[132:135]\n"
    "v_exp_f32_e32 v162, v162\n"
    "v_mfma_f32_16x16x32_bf16 v[128:131], a[188:191], v[200:203], v[128:131]\n"
    "v_exp_f32_e32 v163, v163\n"
    "v_mfma_f32_16x16x32_bf16 v[128:131], a[184:187], v[196:199], v[128:131]\n"
    "ds_write_addtid_b32 v172 offset:4096\n"
    "v_cvt_pk_bf16_f32 v158, v160, v161\n"
    "v_mfma_f32_16x16x32_bf16 v[124:127], a[184:187], v[188:191], v[124:127]\n"
    "v_exp_f32_e32 v168, v168\n"
    "v_mfma_f32_16x16x32_bf16 v[124:127], a[188:191], v[192:195], v[124:127]\n"
    "s_sub_u32 s44, s44, 1\n"
    "ds_read_b128 a[180:183], v243 offset:24576\n"
    "ds_write_addtid_b32 v173 offset:4352\n"
    "v_mfma_f32_16x16x32_bf16 v[120:123], a[188:191], v[184:187], v[120:123]\n"
    "s_cmp_lg_u32 s44, s80\n"
    "v_exp_f32_e32 v169, v169\n"
    "v_mfma_f32_16x16x32_bf16 v[120:123], a[184:187], v[180:183], v[120:123]\n"
    "s_waitcnt lgkmcnt(11)\n"
    "ds_write_addtid_b32 v142 offset:512\n"
    "v_cvt_pk_bf16_f32 v159, v162, v163\n"
    "s_cbranch_scc1 .Lat_gloop\n"
    ".Lat_g2:\n"
    "v_mov_b64_e32 v[224:225], v[220:221]\n"
    "v_mov_b64_e32 v[226:227], v[220:221]\n"
    "v_mov_b64_e32 v[228:229], v[220:221]\n"
    "v_mov_b64_e32 v[230:231], v[220:221]\n"
    "v_mov_b64_e32 v[232:233], v[220:221]\n"
    "v_mov_b64_e32 v[234:235], v[220:221]\n"
    "v_mov_b64_e32 v[236:237], v[220:221]\n"
    "v_mov_b64_e32 v[238:239], v[220:221]\n"
    "s_mov_b32 s80, 0\n"
    "s_waitcnt lgkmcnt(0)\n"
    "s_cmp_lg_u32 s44, 0\n"
    "s_cbranch_scc1 .Lat_loop\n"
    ".Lat_tail:\n"
    "v_mfma_f32_16x16x32_bf16 v[212:215], a[160:163], a[128:131], v[236:239]\n"
    ".long 0xbedf00ff\n"
    ".Lat_w9:\n"
    "s_setpc_b64 s[2:3]\n"
    "ds_read_b128 a[184:187], v242 offset:28672\n"
    "v_and_b32_e32 v189, 0x1f0, v250\n"
    "v_lshlrev_b32_e32 v190, 3, v250\n"
    "v_mfma_f32_16x16x32_bf16 v[212:215], a[164:167], a[132:135], v[212:215]\n"
    "ds_write_addtid_b32 v143 offset:768\n"
    "v_lshrrev_b32_e32 v253, 4, v250\n"
    "v_mfma_f32_16x16x32_bf16 v[204:207], a[164:167], a[100:103], v[232:235]\n"
    "s_mov_b32 s52, 6100992\n"
    "v_exp_f32_e32 v170, v170\n"
    "v_bfe_u32 v251, v253, 4, 2\n"
    "v_mfma_f32_16x16x32_bf16 v[204:207], a[160:163], a[96:99], v[204:207]\n"
    "s_lshr_b32 s81, s34, 6\n"
    "v_exp_f32_e32 v171, v171\n"
    "v_mfma_f32_16x16x32_bf16 v[196:199], a[160:163], a[64:67], v[228:231]\n"
    "s_lshl_b32 s81, s81, 9\n"
    "v_cvt_pk_bf16_f32 v166, v168, v169\n"
    "v_lshlrev_b32_e32 v251, 1, v251\n"
    "v_xor_b32_e32 v251, v251, v253\n"
    "v_mfma_f32_16x16x32_bf16 v[196:199], a[164:167], a[68:71], v[196:199]\n"
    "s_waitcnt lgkmcnt(8)\n"
    "ds_read_b128 a[188:191], v243 offset:28672\n"
    "v_and_b32_e32 v251, 7, v251\n"
    "v_mfma_f32_16x16x32_bf16 v[196:199], a[168:171], a[72:75], v[196:199]\n"
    "s_add_u32 s81, s81, 6074368\n"
    "v_exp_f32_e32 v176, v176\n"
    "v_mfma_f32_16x16x32_bf16 v[196:199], a[172:175], a[76:79], v[196:199]\n"
    "s_lshl_b32 s63, s93, 13\n"
    "v_exp_f32_e32 v177, v177\n"
    "v_mfma_f32_16x16x32_bf16 v[204:207], a[172:175], a[108:111], v[204:207]\n"
    "s_xor_b32 s45, s63, 0x4000\n"
    "ds_read_b128 a[160:163], v240 offset:26624\n"
    "v_lshlrev_b32_e32 v251, 4, v251\n"
    "v_lshrrev_b32_e32 v253, 3, v253\n"
    "v_mfma_f32_16x16x32_bf16 v[204:207], a[168:171], a[104:107], v[204:207]\n"
    "s_mul_i32 s58, s93, 33792\n"
    "ds_write_addtid_b32 v150 offset:1536\n"
    "v_lshl_add_u32 v251, v253, 9, v251\n"
    "v_mfma_f32_16x16x32_bf16 v[212:215], a[168:171], a[136:139], v[212:215]\n"
    "s_add_u32 s57, s58, 5513216\n"
    "ds_write_addtid_b32 v151 offset:1792\n"
    "v_bfe_u32 v255, v250, 2, 4\n"
    "v_mfma_f32_16x16x32_bf16 v[212:215], a[172:175], a[140:143], v[212:215]\n"
    "s_waitcnt lgkmcnt(7)\n"
    "ds_read_b128 a[168:171], v240 offset:30720\n"
    "v_mov_b32_e32 v254, s39\n"
    "v_mfma_f32_16x16x32_bf16 v[212:215], a[176:179], a[144:147], v[212:215]\n"
    "s_add_u32 s58, s58, 5378048\n"
    "ds_read_b128 a[164:167], v241 offset:26624\n"
    "v_lshrrev_b32_e32 v252, 2, v250\n"
    "v_mfma_f32_16x16x32_bf16 v[212:215], a[180:183], a[148:151], v[212:215]\n"
    "s_add_u32 s36, s35, 0x8000\n"
    "ds_write_addtid_b32 v158 offset:2560\n"
    "v_cvt_pk_bf16_f32 v167, v170, v171\n"
    "v_mfma_f32_16x16x32_bf16 v[204:207], a[180:183], a[116:119], v[204:207]\n"
    "s_add_u32 s37, s35, 0x10000\n"
    "v_exp_f32_e32 v178, v178\n"
    "v_mfma_f32_16x16x32_bf16 v[204:207], a[176:179], a[112:115], v[204:207]\n"
    "s_cmp_lt_u32 s32, 2\n"
    "s_cselect_b32 s37, s37, 2.0\n"
    "ds_read_b128 a[172:175], v241 offset:30720\n"
    "v_mfma_f32_16x16x32_bf16 v[196:199], a[176:179], a[80:83], v[196:199]\n"
    "s_cmp_lt_u32 s36, 5120000\n"
    "s_cselect_b32 s36, s36, 2.0\n"
    "ds_write_addtid_b32 v159 offset:2816\n"
    "v_mfma_f32_16x16x32_bf16 v[196:199], a[180:183], a[84:87], v[196:199]\n"
    "s_waitcnt lgkmcnt(8)\n"
    "v_exp_f32_e32 v179, v179\n"
    "v_mfma_f32_16x16x32_bf16 v[196:199], a[184:187], a[88:91], v[196:199]\n"
    "s_cmp_lt_u32 s37, 5120000\n"
    "s_cselect_b32 s37, s37, 2.0\n"
    "ds_read_b128 a[176:179], v242 offset:26624\n"
    "v_mfma_f32_16x16x32_bf16 v[196:199], a[188:191], a[92:95], v[196:199]\n"
    "buffer_load_dword v253, v255, s[8:11], s52 offen offset:0 sc1\n"
    "v_mfma_f32_16x16x32_bf16 v[204:207], a[188:191], a[124:127], v[204:207]\n"
    "ds_read_b128 a[180:183], v243 offset:26624\n"
    "v_cvt_pk_bf16_f32 v174, v176, v177\n"
    "v_mfma_f32_16x16x32_bf16 v[204:207], a[184:187], a[120:123], v[204:207]\n"
    "ds_write_addtid_b32 v166 offset:3584\n"
    "v_cvt_pk_bf16_f32 v175, v178, v179\n"
    "v_mfma_f32_16x16x32_bf16 v[212:215], a[184:187], a[152:155], v[212:215]\n"
    "v_exp_f32_e32 v196, v196\n"
    "v_mfma_f32_16x16x32_bf16 v[212:215], a[188:191], a[156:159], v[212:215]\n"
    "s_waitcnt lgkmcnt(6)\n"
    "ds_write_addtid_b32 v167 offset:3840\n"
    "v_mfma_f32_16x16x32_bf16 v[216:219], a[160:163], a[128:131], v[236:239]\n"
    "ds_write_addtid_b32 v174 offset:4608\n"
    "ds_write_addtid_b32 v175 offset:4864\n"
    "v_mfma_f32_16x16x32_bf16 v[216:219], a[164:167], a[132:135], v[216:219]\n"
    "ds_read_b128 a[184:187], v242 offset:30720\n"
    "v_mfma_f32_16x16x32_bf16 v[208:211], a[164:167], a[100:103], v[232:235]\n"
    "ds_read_b128 a[188:191], v243 offset:30720\n"
    "v_mfma_f32_16x16x32_bf16 v[208:211], a[160:163], a[96:99], v[208:211]\n"
    "s_waitcnt lgkmcnt(2)\n"
    "v_mfma_f32_16x16x32_bf16 v[200:203], a[160:163], a[64:67], v[228:231]\n"
    "s_barrier\n"
    "v_mfma_f32_16x16x32_bf16 v[200:203], a[164:167], a[68:71], v[200:203]\n"
    "ds_read_b64_tr_b16 a[160:161], v244 offset:16384\n"
    "ds_read2st64_b32 v[178:179], v249 offset0:18 offset1:19\n"
    "v_mfma_f32_16x16x32_bf16 v[200:203], a[168:171], a[72:75], v[200:203]\n"
    "s_add_u32 m0, s43, 8192\n"
    "ds_read_b64_tr_b16 a[162:163], v244 offset:18432\n"
    "v_mfma_f32_16x16x32_bf16 v[200:203], a[172:175], a[76:79], v[200:203]\n"
    "buffer_load_dwordx4 v248, s[8:11], s40 offen offset:0 sc1 lds\n"
    "v_mfma_f32_16x16x32_bf16 v[208:211], a[172:175], a[108:111], v[208:211]\n"
    "v_exp_f32_e32 v197, v197\n"
    "v_mfma_f32_16x16x32_bf16 v[208:211], a[168:171], a[104:107], v[208:211]\n"
    "ds_read2st64_b32 v[176:177], v249 offset0:16 offset1:17\n"
    "v_mfma_f32_16x16x32_bf16 v[216:219], a[168:171], a[136:139], v[216:219]\n"
    "ds_read2st64_b32 v[170:171], v249 offset0:14 offset1:15\n"
    "v_mfma_f32_16x16x32_bf16 v[216:219], a[172:175], a[140:143], v[216:219]\n"
    "ds_read_b64_tr_b16 a[164:165], v244 offset:49152\n"
    "v_mfma_f32_16x16x32_bf16 v[216:219], a[176:179], a[144:147], v[216:219]\n"
    "buffer_load_dwordx4 v248, s[8:11], s40 offen offset:1024 sc1 lds\n"
    "v_mfma_f32_16x16x32_bf16 v[216:219], a[180:183], a[148:151], v[216:219]\n"
    "ds_read2st64_b32 v[162:163], v249 offset0:10 offset1:11\n"
    "ds_read2st64_b32 v[168:169], v249 offset0:12 offset1:13\n"
    "v_mfma_f32_16x16x32_bf16 v[208:211], a[180:183], a[116:119], v[208:211]\n"
    "ds_read_b64_tr_b16 a[166:167], v244 offset:51200\n"
    "v_mfma_f32_16x16x32_bf16 v[208:211], a[176:179], a[112:115], v[208:211]\n"
    "ds_read2st64_b32 v[154:155], v249 offset0:6 offset1:7\n"
    "ds_read2st64_b32 v[160:161], v249 offset0:8 offset1:9\n"
    "v_mfma_f32_16x16x32_bf16 v[200:203], a[176:179], a[80:83], v[200:203]\n"
    "ds_read2st64_b32 v[146:147], v249 offset0:2 offset1:3\n"
    "ds_read2st64_b32 v[152:153], v249 offset0:4 offset1:5\n"
    "v_mfma_f32_16x16x32_bf16 v[200:203], a[180:183], a[84:87], v[200:203]\n"
    "s_waitcnt lgkmcnt(13)\n"
    "ds_read2st64_b32 v[144:145], v249 offset0:0 offset1:1\n"
    "v_mfma_f32_16x16x32_bf16 v[200:203], a[184:187], a[88:91], v[200:203]\n"
    "buffer_load_dwordx4 v248, s[8:11], s40 offen offset:2048 sc1 lds\n"
    "v_mfma_f32_16x16x32_bf16 v[200:203], a[188:191], a[92:95], v[200:203]\n"
    "v_exp_f32_e32 v198, v198\n"
    "v_mfma_f32_16x16x32_bf16 v[208:211], a[188:191], a[124:127], v[208:211]\n"
    "ds_read_b64_tr_b16 a[176:177], v246 offset:16384\n"
    "v_mfma_f32_16x16x32_bf16 v[208:211], a[184:187], a[120:123], v[208:211]\n"
    "ds_read_b64_tr_b16 a[168:169], v245 offset:16384\n"
    "v_mfma_f32_16x16x32_bf16 v[216:219], a[184:187], a[152:155], v[216:219]\n"
    "ds_read_b64_tr_b16 a[170:171], v245 offset:18432\n"
    "v_mfma_f32_16x16x32_bf16 v[216:219], a[188:191], a[156:159], v[216:219]\n"
    "s_waitcnt lgkmcnt(3)\n"
    "ds_read_b64_tr_b16 a[184:185], v247 offset:16384\n"
    "v_mfma_f32_16x16x32_bf16 a[252:255], a[160:163], v[172:175], a[252:255]\n"
    "buffer_load_dwordx4 v248, s[8:11], s40 offen offset:3072 sc1 lds\n"
    "v_mfma_f32_16x16x32_bf16 a[252:255], a[164:167], v[176:179], a[252:255]\n"
    "s_add_u32 s40, s40, 0x8000\n"
    "ds_read_b64_tr_b16 a[172:173], v245 offset:49152\n"
    "v_mfma_f32_16x16x32_bf16 a[248:251], a[164:167], v[168:171], a[248:251]\n"
    "s_add_u32 m0, s43, 12288\n"
    "ds_read_b64_tr_b16 a[174:175], v245 offset:51200\n"
    "v_mfma_f32_16x16x32_bf16 a[248:251], a[160:163], v[164:167], a[248:251]\n"
    "v_exp_f32_e32 v199, v199\n"
    "v_mfma_f32_16x16x32_bf16 a[244:247], a[160:163], v[156:159], a[244:247]\n"
    "ds_read_b64_tr_b16 a[186:187], v247 offset:18432\n"
    "v_mfma_f32_16x16x32_bf16 a[244:247], a[164:167], v[160:163], a[244:247]\n"
    "buffer_load_dwordx4 v248, s[8:11], s41 offen offset:0 sc1 lds\n"
    "v_mfma_f32_16x16x32_bf16 a[240:243], a[164:167], v[152:155], a[240:243]\n"
    "ds_read_b64_tr_b16 a[178:179], v246 offset:18432\n"
    "v_mfma_f32_16x16x32_bf16 a[240:243], a[160:163], v[148:151], a[240:243]\n"
    "ds_read_b64_tr_b16 a[188:189], v247 offset:49152\n"
    "v_mfma_f32_16x16x32_bf16 a[236:239], a[160:163], v[140:143], a[236:239]\n"
    "ds_read_b64_tr_b16 a[180:181], v246 offset:49152\n"
    "v_mfma_f32_16x16x32_bf16 a[236:239], a[164:167], v[144:147], a[236:239]\n"
    "s_waitcnt lgkmcnt(4)\n"
    "ds_read_b64_tr_b16 a[182:183], v246 offset:51200\n"
    "v_cvt_pk_bf16_f32 v196, v196, v197\n"
    "v_mfma_f32_16x16x32_bf16 v[0:3], a[168:171], v[140:143], v[0:3]\n"
    "buffer_load_dwordx4 v248, s[8:11], s41 offen offset:1024 sc1 lds\n"
    "v_mfma_f32_16x16x32_bf16 v[0:3], a[172:175], v[144:147], v[0:3]\n"
    "ds_read_b64_tr_b16 a[190:191], v247 offset:51200\n"
    "v_mfma_f32_16x16x32_bf16 v[4:7], a[172:175], v[152:155], v[4:7]\n"
    "v_exp_f32_e32 v204, v204\n"
    "v_mfma_f32_16x16x32_bf16 v[4:7], a[168:171], v[148:151], v[4:7]\n"
    "ds_read_b64_tr_b16 a[160:161], v244 offset:20480\n"
    "v_subrev_f32_e32 v0, s4, v0\n"
    "v_mfma_f32_16x16x32_bf16 v[8:11], a[168:171], v[156:159], v[8:11]\n"
    "v_exp_f32_e32 v205, v205\n"
    "v_mfma_f32_16x16x32_bf16 v[8:11], a[172:175], v[160:163], v[8:11]\n"
    "buffer_load_dwordx4 v248, s[8:11], s41 offen offset:2048 sc1 lds\n"
    "v_mfma_f32_16x16x32_bf16 v[12:15], a[172:175], v[168:171], v[12:15]\n"
    "ds_read_b64_tr_b16 a[162:163], v244 offset:22528\n"
    "v_cvt_pk_bf16_f32 v197, v198, v199\n"
    "v_mfma_f32_16x16x32_bf16 v[12:15], a[168:171], v[164:167], v[12:15]\n"
    "s_waitcnt vmcnt(7)\n"
    "v_mfma_f32_16x16x32_bf16 v[16:19], a[168:171], v[172:175], v[16:19]\n"
    "s_barrier\n"
    "v_mfma_f32_16x16x32_bf16 v[16:19], a[172:175], v[176:179], v[16:19]\n"
    "s_waitcnt lgkmcnt(3)\n"
    "v_add_u32_e32 v249, s45, v252\n"
    "v_add_u32_e32 v252, s63, v252\n"
    "v_mfma_f32_16x16x32_bf16 v[36:39], a[176:179], v[172:175], v[36:39]\n"
    "buffer_store_dword v254, off, s[8:11], s84 offset:0 sc1\n"
    "v_mfma_f32_16x16x32_bf16 v[36:39], a[180:183], v[176:179], v[36:39]\n"
    "s_mul_i32 s45, s49, 63\n"
    "ds_read_b64_tr_b16 a[164:165], v244 offset:53248\n"
    "v_subrev_f32_e32 v12, s4, v12\n"
    "v_mfma_f32_16x16x32_bf16 v[32:35], a[180:183], v[168:171], v[32:35]\n"
    "s_add_u32 s45, s45, s33\n"
    "ds_read_b64_tr_b16 a[168:169], v245 offset:20480\n"
    "v_subrev_f32_e32 v16, s4, v16\n"
    "v_mfma_f32_16x16x32_bf16 v[32:35], a[176:179], v[164:167], v[32:35]\n"
    "buffer_load_dwordx4 v248, s[8:11], s41 offen offset:3072 sc1 lds\n"
    "v_mfma_f32_16x16x32_bf16 v[28:31], a[176:179], v[156:159], v[28:31]\n"
    "s_add_u32 s41, s41, 0x8000\n"
    "ds_read_b64_tr_b16 a[166:167], v244 offset:55296\n"
    "v_cmp_ne_u32_e32 vcc, s39, v253\n"
    "v_mfma_f32_16x16x32_bf16 v[28:31], a[180:183], v[160:163], v[28:31]\n"
    "s_mov_b32 m0, s63\n"
    "v_subrev_f32_e32 v36, s4, v36\n"
    "v_subrev_f32_e32 v32, s4, v32\n"
    "v_mfma_f32_16x16x32_bf16 v[24:27], a[180:183], v[152:155], v[24:27]\n"
    "s_mul_i32 s45, s45, 405504\n"
    "ds_read_b64_tr_b16 a[170:171], v245 offset:22528\n"
    "v_subrev_f32_e32 v37, s4, v37\n"
    "v_mfma_f32_16x16x32_bf16 v[24:27], a[176:179], v[148:151], v[24:27]\n"
    "s_cbranch_vccnz .Lat_wpoll\n"
    ".Lat_wpoll_ret:\n"
    "v_mfma_f32_16x16x32_bf16 v[20:23], a[176:179], v[140:143], v[20:23]\n"
    "s_add_u32 s62, s57, 0\n"
    "v_exp_f32_e32 v206, v206\n"
    "v_mfma_f32_16x16x32_bf16 v[20:23], a[180:183], v[144:147], v[20:23]\n"
    "s_waitcnt lgkmcnt(6)\n"
    "ds_read_b64_tr_b16 a[172:173], v245 offset:53248\n"
    "v_subrev_f32_e32 v28, s4, v28\n"
    "v_mfma_f32_16x16x32_bf16 v[40:43], a[184:187], v[140:143], v[40:43]\n"
    "buffer_load_dwordx4 a[216:219], v250, s[8:11], s62 offen offset:0 sc1\n"
    "v_mfma_f32_16x16x32_bf16 v[40:43], a[188:191], v[144:147], v[40:43]\n"
    "s_add_u32 s45, s45, 16588800\n"
    "ds_write_addtid_b32 v196 offset:0\n"
    "v_subrev_f32_e32 v8, s4, v8\n"
    "v_mfma_f32_16x16x32_bf16 v[44:47], a[188:191], v[152:155], v[44:47]\n"
    "s_lshl_b32 s46, s51, 12\n"
    "v_subrev_f32_e32 v9, s4, v9\n"
    "v_subrev_f32_e32 v10, s4, v10\n"
    "v_mfma_f32_16x16x32_bf16 v[44:47], a[184:187], v[148:151], v[44:47]\n"
    "s_lshl_b32 s48, s50, 15\n"
    "ds_read_b64_tr_b16 a[176:177], v246 offset:20480\n"
    "v_subrev_f32_e32 v40, s4, v40\n"
    "v_mfma_f32_16x16x32_bf16 v[48:51], a[184:187], v[156:159], v[48:51]\n"
    "s_add_u32 s46, s46, s48\n"
    "v_exp_f32_e32 v207, v207\n"
    "v_mfma_f32_16x16x32_bf16 v[48:51], a[188:191], v[160:163], v[48:51]\n"
    "buffer_load_dwordx4 a[220:223], v250, s[8:11], s62 offen offset:1024 sc1\n"
    "v_mfma_f32_16x16x32_bf16 v[52:55], a[188:191], v[168:171], v[52:55]\n"
    "s_cmp_eq_u32 s51, 0\n"
    "s_cselect_b32 s47, 2.0, 0\n"
    "ds_read_b64_tr_b16 a[178:179], v246 offset:22528\n"
    "v_mfma_f32_16x16x32_bf16 v[52:55], a[184:187], v[164:167], v[52:55]\n"
    "s_add_u32 s47, s47, 98304\n"
    "ds_read_b64_tr_b16 a[174:175], v245 offset:55296\n"
    "v_cvt_pk_bf16_f32 v204, v204, v205\n"
    "v_mfma_f32_16x16x32_bf16 v[56:59], a[184:187], v[172:175], v[56:59]\n"
    "s_lshl_b32 s61, s50, 9\n"
    "ds_read_b64_tr_b16 a[180:181], v246 offset:53248\n"
    "v_mfma_f32_16x16x32_bf16 v[56:59], a[188:191], v[176:179], v[56:59]\n"
    "s_waitcnt lgkmcnt(7)\n"
    "ds_write_addtid_b32 v197 offset:256\n"
    "v_subrev_f32_e32 v48, s4, v48\n"
    "v_mfma_f32_16x16x32_bf16 v[76:79], a[160:163], v[172:175], v[76:79]\n"
    "buffer_load_dwordx4 a[224:227], v250, s[8:11], s62 offen offset:2048 sc1\n"
    "v_mfma_f32_16x16x32_bf16 v[76:79], a[164:167], v[176:179], v[76:79]\n"
    "s_add_u32 s61, s61, s47\n"
    "v_exp_f32_e32 v212, v212\n"
    "v_mfma_f32_16x16x32_bf16 v[72:75], a[164:167], v[168:171], v[72:75]\n"
    "s_add_u32 s48, s32, 3\n"
    "ds_read_b64_tr_b16 a[184:185], v247 offset:20480\n"
    "v_subrev_f32_e32 v29, s4, v29\n"
    "v_mfma_f32_16x16x32_bf16 v[72:75], a[160:163], v[164:167], v[72:75]\n"
    "s_and_b32 s48, s48, 3\n"
    "v_subrev_f32_e32 v4, s4, v4\n"
    "v_mfma_f32_16x16x32_bf16 v[68:71], a[160:163], v[156:159], v[68:71]\n"
    "s_mul_i32 s48, s48, 101376\n"
    "v_exp_f32_e32 v213, v213\n"
    "v_mfma_f32_16x16x32_bf16 v[68:71], a[164:167], v[160:163], v[68:71]\n"
    "buffer_load_dwordx4 a[228:231], v250, s[8:11], s62 offen offset:3072 sc1\n"
    "v_mfma_f32_16x16x32_bf16 v[64:67], a[164:167], v[152:155], v[64:67]\n"
    "s_add_u32 s62, s57, 4096\n"
    "ds_read_b64_tr_b16 a[186:187], v247 offset:22528\n"
    "v_subrev_f32_e32 v76, s4, v76\n"
    "v_mfma_f32_16x16x32_bf16 v[64:67], a[160:163], v[148:151], v[64:67]\n"
    "s_add_u32 s48, s48, s45\n"
    "v_subrev_f32_e32 v33, s4, v33\n"
    "v_subrev_f32_e32 v52, s4, v52\n"
    "v_mfma_f32_16x16x32_bf16 v[60:63], a[160:163], v[140:143], v[60:63]\n"
    "s_add_u32 s29, s48, s46\n"
    "ds_read_b64_tr_b16 a[182:183], v246 offset:55296\n"
    "v_cvt_pk_bf16_f32 v205, v206, v207\n"
    "v_mfma_f32_16x16x32_bf16 v[60:63], a[164:167], v[144:147], v[60:63]\n"
    "s_waitcnt lgkmcnt(5)\n"
    "ds_read_b64_tr_b16 a[188:189], v247 offset:53248\n"
    "v_subrev_f32_e32 v68, s4, v68\n"
    "v_mfma_f32_16x16x32_bf16 v[80:83], a[168:171], v[140:143], v[80:83]\n"
    "buffer_load_dwordx4 a[232:235], v250, s[8:11], s62 offen offset:0 sc1\n"
    "v_mfma_f32_16x16x32_bf16 v[80:83], a[172:175], v[144:147], v[80:83]\n"
    "s_addk_i32 s29, 8192\n"
    "v_exp_f32_e32 v214, v214\n"
    "v_mfma_f32_16x16x32_bf16 v[84:87], a[172:175], v[152:155], v[84:87]\n"
    "s_add_u32 s89, s48, s61\n"
    "v_exp_f32_e32 v215, v215\n"
    "v_mfma_f32_16x16x32_bf16 v[84:87], a[168:171], v[148:151], v[84:87]\n"
    "s_addk_i32 s89, 128\n"
    "ds_write_addtid_b32 v204 offset:1024\n"
    "v_subrev_f32_e32 v38, s4, v38\n"
    "v_mfma_f32_16x16x32_bf16 v[88:91], a[168:171], v[156:159], v[88:91]\n"
    "s_add_u32 s48, s32, 2\n"
    "ds_read_b64_tr_b16 a[190:191], v247 offset:55296\n"
    "v_cvt_pk_bf16_f32 v212, v212, v213\n"
    "v_mfma_f32_16x16x32_bf16 v[88:91], a[172:175], v[160:163], v[88:91]\n"
    "buffer_load_dwordx4 v[192:195], v250, s[8:11], s62 offen offset:1024 sc1\n"
    "v_mfma_f32_16x16x32_bf16 v[92:95], a[172:175], v[168:171], v[92:95]\n"
    "s_and_b32 s48, s48, 3\n"
    "ds_write_addtid_b32 v205 offset:1280\n"
    "v_subrev_f32_e32 v17, s4, v17\n"
    "v_mfma_f32_16x16x32_bf16 v[92:95], a[168:171], v[164:167], v[92:95]\n"
    "s_mul_i32 s48, s48, 101376\n"
    "v_exp_f32_e32 v200, v200\n"
    "v_mfma_f32_16x16x32_bf16 v[96:99], a[168:171], v[172:175], v[96:99]\n"
    "s_add_u32 s48, s48, s45\n"
    "v_exp_f32_e32 v201, v201\n"
    "v_mfma_f32_16x16x32_bf16 v[96:99], a[172:175], v[176:179], v[96:99]\n"
    "s_waitcnt lgkmcnt(4)\n"
    "ds_read_b128 a[160:163], v240 offset:0\n"
    "v_cvt_pk_bf16_f32 v213, v214, v215\n"
    "v_mfma_f32_16x16x32_bf16 v[116:119], a[176:179], v[172:175], v[116:119]\n"
    "s_add_u32 s28, s48, s46\n"
    "ds_write_addtid_b32 v212 offset:2048\n"
    "v_subrev_f32_e32 v49, s4, v49\n"
    "v_mfma_f32_16x16x32_bf16 v[116:119], a[180:183], v[176:179], v[116:119]\n"
    "s_waitcnt lgkmcnt(3)\n"
    "v_mfma_f32_16x16x32_bf16 v[112:115], a[180:183], v[168:171], v[112:115]\n"
    "s_barrier\n"
    "v_mfma_f32_16x16x32_bf16 v[112:115], a[176:179], v[164:167], v[112:115]\n"
    "s_add_u32 m0, s43, 16384\n"
    "v_exp_f32_e32 v202, v202\n"
    "v_mfma_f32_16x16x32_bf16 v[108:111], a[176:179], v[156:159], v[108:111]\n"
    "buffer_load_dwordx4 v248, s[8:11], s40 offen offset:0 sc1 lds\n"
    "v_mfma_f32_16x16x32_bf16 v[108:111], a[180:183], v[160:163], v[108:111]\n"
    "s_mov_b32 m0, s63\n"
    "ds_read_b128 a[168:171], v240 offset:4096\n"
    "v_subrev_f32_e32 v72, s4, v72\n"
    "v_mfma_f32_16x16x32_bf16 v[104:107], a[180:183], v[152:155], v[104:107]\n"
    "s_addk_i32 s28, 16384\n"
    "ds_read_b128 a[164:167], v241 offset:0\n"
    "v_exp_f32_e32 v203, v203\n"
    "v_mfma_f32_16x16x32_bf16 v[104:107], a[176:179], v[148:151], v[104:107]\n"
    "s_add_u32 s88, s48, s61\n"
    "ds_read_b128 a[172:175], v241 offset:4096\n"
    "v_cvt_pk_bf16_f32 v198, v200, v201\n"
    "v_mfma_f32_16x16x32_bf16 v[100:103], a[176:179], v[140:143], v[100:103]\n"
    "s_add_u32 m0, s43, 16384\n"
    "v_exp_f32_e32 v208, v208\n"
    "v_mfma_f32_16x16x32_bf16 v[100:103], a[180:183], v[144:147], v[100:103]\n"
    "v_subrev_f32_e32 v13, s4, v13\n"
    "v_subrev_f32_e32 v108, s4, v108\n"
    "v_mfma_f32_16x16x32_bf16 v[120:123], a[184:187], v[140:143], v[120:123]\n"
    "buffer_load_dwordx4 v248, s[8:11], s40 offen offset:1024 sc1 lds\n"
    "v_mfma_f32_16x16x32_bf16 v[120:123], a[188:191], v[144:147], v[120:123]\n"
    "s_mov_b32 m0, s63\n"
    "v_subrev_f32_e32 v39, s4, v39\n"
    "v_subrev_f32_e32 v44, s4, v44\n"
    "v_mfma_f32_16x16x32_bf16 v[124:127], a[188:191], v[152:155], v[124:127]\n"
    "s_addk_i32 s88, 256\n"
    "v_exp_f32_e32 v209, v209\n"
    "v_mfma_f32_16x16x32_bf16 v[124:127], a[184:187], v[148:151], v[124:127]\n"
    "s_add_u32 s48, s32, 1\n"
    "ds_write_addtid_b32 v213 offset:2304\n"
    "v_cvt_pk_bf16_f32 v199, v202, v203\n"
    "v_mfma_f32_16x16x32_bf16 v[128:131], a[184:187], v[156:159], v[128:131]\n"
    "s_add_u32 m0, s43, 16384\n"
    "v_exp_f32_e32 v210, v210\n"
    "v_mfma_f32_16x16x32_bf16 v[128:131], a[188:191], v[160:163], v[128:131]\n"
    "buffer_load_dwordx4 v248, s[8:11], s40 offen offset:2048 sc1 lds\n"
    "v_mfma_f32_16x16x32_bf16 v[132:135], a[188:191], v[168:171], v[132:135]\n"
    "s_mov_b32 m0, s63\n"
    "v_exp_f32_e32 v211, v211\n"
    "v_mfma_f32_16x16x32_bf16 v[132:135], a[184:187], v[164:167], v[132:135]\n"
    "s_and_b32 s48, s48, 3\n"
    "ds_write_addtid_b32 v198 offset:512\n"
    "v_cvt_pk_bf16_f32 v206, v208, v209\n"
    "v_mfma_f32_16x16x32_bf16 v[136:139], a[184:187], v[172:175], v[136:139]\n"
    "s_mul_i32 s48, s48, 101376\n"
    "v_exp_f32_e32 v216, v216\n"
    "v_mfma_f32_16x16x32_bf16 v[136:139], a[188:191], v[176:179], v[136:139]\n"
    "s_waitcnt lgkmcnt(3)\n"
    "v_exp_f32_e32 v217, v217\n"
    "v_mfma_f32_16x16x32_bf16 v[172:175], a[160:163], a[128:131], v[236:239]\n"
    "s_add_u32 m0, s43, 16384\n"
    "ds_read_b128 a[176:179], v242 offset:0\n"
    "v_subrev_f32_e32 v73, s4, v73\n"
    "v_mfma_f32_16x16x32_bf16 v[172:175], a[164:167], a[132:135], v[172:175]\n"
    "buffer_load_dwordx4 v248, s[8:11], s40 offen offset:3072 sc1 lds\n"
    "v_mfma_f32_16x16x32_bf16 v[164:167], a[164:167], a[100:103], v[232:235]\n"
    "s_mov_b32 m0, s63\n"
    "ds_read_b128 a[180:183], v243 offset:0\n"
    "v_subrev_f32_e32 v18, s4, v18\n"
    "v_mfma_f32_16x16x32_bf16 v[164:167], a[160:163], a[96:99], v[164:167]\n"
    "s_add_u32 s48, s48, s45\n"
    "ds_write_addtid_b32 v199 offset:768\n"
    "v_cvt_pk_bf16_f32 v207, v210, v211\n"
    "v_mfma_f32_16x16x32_bf16 v[156:159], a[160:163], a[64:67], v[228:231]\n"
    "s_add_u32 s82, s48, s46\n"
    "ds_read_b128 a[184:187], v242 offset:4096\n"
    "v_subrev_f32_e32 v88, s4, v88\n"
    "v_mfma_f32_16x16x32_bf16 v[156:159], a[164:167], a[68:71], v[156:159]\n"
    "s_waitcnt lgkmcnt(6)\n"
    "v_exp_f32_e32 v218, v218\n"
    "v_mfma_f32_16x16x32_bf16 v[156:159], a[168:171], a[72:75], v[156:159]\n"
    "s_add_u32 m0, s43, 20480\n"
    "v_exp_f32_e32 v219, v219\n"
    "v_mfma_f32_16x16x32_bf16 v[156:159], a[172:175], a[76:79], v[156:159]\n"
    "buffer_load_dwordx4 v248, s[8:11], s41 offen offset:0 sc1 lds\n"
    "v_mfma_f32_16x16x32_bf16 v[164:167], a[172:175], a[108:111], v[164:167]\n"
    "s_mov_b32 m0, s63\n"
    "v_subrev_f32_e32 v14, s4, v14\n"
    "v_subrev_f32_e32 v19, s4, v19\n"
    "v_mfma_f32_16x16x32_bf16 v[164:167], a[168:171], a[104:107], v[164:167]\n"
    "s_addk_i32 s82, 24576\n"
    "ds_read_b128 a[188:191], v243 offset:4096\n"
    "v_subrev_f32_e32 v112, s4, v112\n"
    "v_mfma_f32_16x16x32_bf16 v[172:175], a[168:171], a[136:139], v[172:175]\n"
    "s_add_u32 s87, s48, s61\n"
    "ds_write_addtid_b32 v206 offset:1536\n"
    "v_cvt_pk_bf16_f32 v214, v216, v217\n"
    "v_mfma_f32_16x16x32_bf16 v[172:175], a[172:175], a[140:143], v[172:175]\n"
    "s_waitcnt lgkmcnt(4)\n"
    "ds_write_addtid_b32 v207 offset:1792\n"
    "v_subrev_f32_e32 v96, s4, v96\n"
    "v_mfma_f32_16x16x32_bf16 v[172:175], a[176:179], a[144:147], v[172:175]\n"
    "s_add_u32 m0, s43, 20480\n"
    "ds_read_b128 a[160:163], v240 offset:2048\n"
    "v_cvt_pk_bf16_f32 v215, v218, v219\n"
    "v_mfma_f32_16x16x32_bf16 v[172:175], a[180:183], a[148:151], v[172:175]\n"
    "buffer_load_dwordx4 v248, s[8:11], s41 offen offset:1024 sc1 lds\n"
    "v_mfma_f32_16x16x32_bf16 v[164:167], a[180:183], a[116:119], v[164:167]\n"
    "s_mov_b32 m0, s63\n"
    "ds_read_b128 a[164:167], v241 offset:2048\n"
    "v_subrev_f32_e32 v89, s4, v89\n"
    "v_mfma_f32_16x16x32_bf16 v[164:167], a[176:179], a[112:115], v[164:167]\n"
    "s_addk_i32 s87, 384\n"
    "ds_read_b128 a[168:171], v240 offset:6144\n"
    "v_subrev_f32_e32 v90, s4, v90\n"
    "v_mfma_f32_16x16x32_bf16 v[156:159], a[176:179], a[80:83], v[156:159]\n"
    "s_mul_i32 s48, s32, 101376\n"
    "ds_write_addtid_b32 v214 offset:2560\n"
    "v_subrev_f32_e32 v56, s4, v56\n"
    "v_mfma_f32_16x16x32_bf16 v[156:159], a[180:183], a[84:87], v[156:159]\n"
    "s_waitcnt lgkmcnt(6)\n"
    "ds_write_addtid_b32 v215 offset:2816\n"
    "v_subrev_f32_e32 v30, s4, v30\n"
    "v_mfma_f32_16x16x32_bf16 v[156:159], a[184:187], a[88:91], v[156:159]\n"
    "s_add_u32 m0, s43, 20480\n"
    "ds_read_b128 a[176:179], v242 offset:2048\n"
    "v_subrev_f32_e32 v77, s4, v77\n"
    "v_mfma_f32_16x16x32_bf16 v[156:159], a[188:191], a[92:95], v[156:159]\n"
    "buffer_load_dwordx4 v248, s[8:11], s41 offen offset:2048 sc1 lds\n"
    "v_mfma_f32_16x16x32_bf16 v[164:167], a[188:191], a[124:127], v[164:167]\n"
    "s_mov_b32 m0, s63\n"
    "ds_read_b128 a[172:175], v241 offset:6144\n"
    "v_subrev_f32_e32 v11, s4, v11\n"
    "v_mfma_f32_16x16x32_bf16 v[164:167], a[184:187], a[120:123], v[164:167]\n"
    "s_add_u32 s48, s48, s45\n"
    "ds_read_b128 a[180:183], v243 offset:2048\n"
    "v_subrev_f32_e32 v69, s4, v69\n"
    "v_mfma_f32_16x16x32_bf16 v[172:175], a[184:187], a[152:155], v[172:175]\n"
    "s_add_u32 s59, s48, s46\n"
    "v_exp_f32_e32 v156, v156\n"
    "v_mfma_f32_16x16x32_bf16 v[172:175], a[188:191], a[156:159], v[172:175]\n"
    "s_waitcnt lgkmcnt(6)\n"
    "v_exp_f32_e32 v157, v157\n"
    "v_mfma_f32_16x16x32_bf16 v[176:179], a[160:163], a[128:131], v[236:239]\n"
    "s_add_u32 m0, s43, 20480\n"
    "v_exp_f32_e32 v158, v158\n"
    "v_mfma_f32_16x16x32_bf16 v[176:179], a[164:167], a[132:135], v[176:179]\n"
    "buffer_load_dwordx4 v248, s[8:11], s41 offen offset:3072 sc1 lds\n"
    "v_mfma_f32_16x16x32_bf16 v[168:171], a[164:167], a[100:103], v[232:235]\n"
    "s_mov_b32 m0, s63\n"
    "ds_read_b128 a[184:187], v242 offset:6144\n"
    "ds_read_b128 a[188:191], v243 offset:6144\n"
    "v_mfma_f32_16x16x32_bf16 v[168:171], a[160:163], a[96:99], v[168:171]\n"
    "s_waitcnt lgkmcnt(5)\n"
    "s_waitcnt vmcnt(14)\n"
    "v_mfma_f32_16x16x32_bf16 v[160:163], a[160:163], a[64:67], v[228:231]\n"
    "s_barrier\n"
    "v_mfma_f32_16x16x32_bf16 v[160:163], a[164:167], a[68:71], v[160:163]\n"
    "s_waitcnt lgkmcnt(3)\n"
    "v_exp_f32_e32 v159, v159\n"
    "v_mfma_f32_16x16x32_bf16 v[160:163], a[168:171], a[72:75], v[160:163]\n"
    "s_add_u32 s60, s48, s61\n"
    "ds_read2st64_b32 a[196:197], v249 offset0:0 offset1:1\n"
    "v_cvt_pk_bf16_f32 v156, v156, v157\n"
    "v_mfma_f32_16x16x32_bf16 v[160:163], a[172:175], a[76:79], v[160:163]\n"
    "s_and_b32 s48, s32, 1\n"
    "v_exp_f32_e32 v164, v164\n"
    "v_mfma_f32_16x16x32_bf16 v[168:171], a[172:175], a[108:111], v[168:171]\n"
    "s_xor_b32 s48, s48, s50\n"
    "ds_write_addtid_b32 v156 offset:3072\n"
    "v_subrev_f32_e32 v132, s4, v132\n"
    "v_mfma_f32_16x16x32_bf16 v[168:171], a[168:171], a[104:107], v[168:171]\n"
    "s_sub_u32 s61, s32, s48\n"
    "ds_read2st64_b32 a[198:199], v249 offset0:2 offset1:3\n"
    "v_subrev_f32_e32 v109, s4, v109\n"
    "v_mfma_f32_16x16x32_bf16 v[176:179], a[168:171], a[136:139], v[176:179]\n"
    "s_and_b32 s61, s61, 3\n"
    "ds_read2st64_b32 a[192:193], v252 offset0:0 offset1:1\n"
    "v_subrev_f32_e32 v57, s4, v57\n"
    "v_mfma_f32_16x16x32_bf16 v[176:179], a[172:175], a[140:143], v[176:179]\n"
    "s_waitcnt lgkmcnt(6)\n"
    "v_exp_f32_e32 v165, v165\n"
    "v_mfma_f32_16x16x32_bf16 v[176:179], a[176:179], a[144:147], v[176:179]\n"
    "s_add_u32 s61, s61, 8\n"
    "ds_read2st64_b32 a[194:195], v252 offset0:2 offset1:3\n"
    "v_cvt_pk_bf16_f32 v157, v158, v159\n"
    "v_mfma_f32_16x16x32_bf16 v[176:179], a[180:183], a[148:151], v[176:179]\n"
    "s_mul_i32 s48, s48, 101376\n"
    "ds_read2st64_b32 a[204:205], v249 offset0:4 offset1:5\n"
    "v_exp_f32_e32 v166, v166\n"
    "v_mfma_f32_16x16x32_bf16 v[168:171], a[180:183], a[116:119], v[168:171]\n"
    "s_add_u32 s48, s48, s45\n"
    "ds_read_b128 a[160:163], v240 offset:8192\n"
    "v_exp_f32_e32 v167, v167\n"
    "v_mfma_f32_16x16x32_bf16 v[168:171], a[176:179], a[112:115], v[168:171]\n"
    "s_lshl_b32 s80, s61, 13\n"
    "ds_write_addtid_b32 v157 offset:3328\n"
    "v_subrev_f32_e32 v34, s4, v34\n"
    "v_mfma_f32_16x16x32_bf16 v[160:163], a[176:179], a[80:83], v[160:163]\n"
    "s_add_u32 s80, s80, s48\n"
    "ds_read_b128 a[164:167], v241 offset:8192\n"
    "v_subrev_f32_e32 v70, s4, v70\n"
    "v_mfma_f32_16x16x32_bf16 v[160:163], a[180:183], a[84:87], v[160:163]\n"
    "s_waitcnt lgkmcnt(9)\n"
    "ds_read_b128 a[176:179], v242 offset:8192\n"
    "v_cvt_pk_bf16_f32 v164, v164, v165\n"
    "v_mfma_f32_16x16x32_bf16 v[160:163], a[184:187], a[88:91], v[160:163]\n"
    "s_lshl_b32 s46, s51, 12\n"
    "ds_read_b128 a[168:171], v240 offset:12288\n"
    "v_exp_f32_e32 v172, v172\n"
    "v_mfma_f32_16x16x32_bf16 v[160:163], a[188:191], a[92:95], v[160:163]\n"
    "s_add_u32 s80, s80, s46\n"
    "ds_read_b128 a[180:183], v243 offset:8192\n"
    "v_exp_f32_e32 v173, v173\n"
    "v_mfma_f32_16x16x32_bf16 v[168:171], a[188:191], a[124:127], v[168:171]\n"
    "s_lshl_b32 s86, s61, 7\n"
    "ds_write_addtid_b32 v164 offset:4096\n"
    "v_cvt_pk_bf16_f32 v165, v166, v167\n"
    "v_mfma_f32_16x16x32_bf16 v[168:171], a[184:187], a[120:123], v[168:171]\n"
    "s_add_u32 s86, s86, s48\n"
    "ds_read_b128 a[172:175], v241 offset:12288\n"
    "v_subrev_f32_e32 v41, s4, v41\n"
    "v_mfma_f32_16x16x32_bf16 v[176:179], a[184:187], a[152:155], v[176:179]\n"
    "s_add_u32 s86, s86, s47\n"
    "ds_read2st64_b32 a[206:207], v249 offset0:6 offset1:7\n"
    "v_exp_f32_e32 v174, v174\n"
    "v_mfma_f32_16x16x32_bf16 v[176:179], a[188:191], a[156:159], v[176:179]\n"
    "s_waitcnt lgkmcnt(6)\n"
    "ds_read_b128 a[184:187], v242 offset:12288\n"
    "ds_read_b128 a[188:191], v243 offset:12288\n"
    "v_mfma_f32_16x16x32_bf16 v[180:183], a[160:163], a[128:131], v[236:239]\n"
    ".long 0xbedf00ff\n"
    ".Lat_w10:\n"
    "s_setpc_b64 s[2:3]\n"
    "s_lshl_b32 s46, s93, 7\n"
    "v_exp_f32_e32 v175, v175\n"
    "v_mfma_f32_16x16x32_bf16 v[180:183], a[164:167], a[132:135], v[180:183]\n"
    "s_lshl_b32 s55, s93, 15\n"
    "ds_read2st64_b32 a[200:201], v252 offset0:4 offset1:5\n"
    "v_subrev_f32_e32 v15, s4, v15\n"
    "v_mfma_f32_16x16x32_bf16 v[148:151], a[164:167], a[100:103], v[232:235]\n"
    "s_sub_u32 s54, s55, 24576\n"
    "ds_write_addtid_b32 v165 offset:4352\n"
    "v_cvt_pk_bf16_f32 v172, v172, v173\n"
    "v_mfma_f32_16x16x32_bf16 v[148:151], a[160:163], a[96:99], v[148:151]\n"
    "s_add_u32 s55, s55, 32768\n"
    "v_exp_f32_e32 v160, v160\n"
    "v_mfma_f32_16x16x32_bf16 v[140:143], a[160:163], a[64:67], v[228:231]\n"
    "s_lshl2_add_u32 s38, s34, s93\n"
    "v_exp_f32_e32 v161, v161\n"
    "v_mfma_f32_16x16x32_bf16 v[140:143], a[164:167], a[68:71], v[140:143]\n"
    "s_waitcnt lgkmcnt(5)\n"
    "ds_read_b128 a[160:163], v240 offset:10240\n"
    "v_cvt_pk_bf16_f32 v173, v174, v175\n"
    "v_mfma_f32_16x16x32_bf16 v[140:143], a[168:171], a[72:75], v[140:143]\n"
    "s_lshl1_add_u32 s38, s38, s49\n"
    "ds_read_b128 a[164:167], v241 offset:10240\n"
    "v_exp_f32_e32 v162, v162\n"
    "v_mfma_f32_16x16x32_bf16 v[140:143], a[172:175], a[76:79], v[140:143]\n"
    "s_lshl_b32 s38, s38, 7\n"
    "ds_read2st64_b32 a[202:203], v252 offset0:6 offset1:7\n"
    "v_exp_f32_e32 v163, v163\n"
    "v_mfma_f32_16x16x32_bf16 v[148:151], a[172:175], a[108:111], v[148:151]\n"
    "s_add_u32 s38, s38, 5648384\n"
    "v_cvt_pk_bf16_f32 v158, v160, v161\n"
    "v_exp_f32_e32 v168, v168\n"
    "v_mfma_f32_16x16x32_bf16 v[148:151], a[168:171], a[104:107], v[148:151]\n"
    "s_lshl3_add_u32 s90, s34, s49\n"
    "v_exp_f32_e32 v169, v169\n"
    "v_mfma_f32_16x16x32_bf16 v[180:183], a[168:171], a[136:139], v[180:183]\n"
    "s_lshl2_add_u32 s90, s90, s93\n"
    "ds_write_addtid_b32 v172 offset:5120\n"
    "v_cvt_pk_bf16_f32 v159, v162, v163\n"
    "v_mfma_f32_16x16x32_bf16 v[180:183], a[172:175], a[140:143], v[180:183]\n"
    "ds_read_b128 a[168:171], v240 offset:14336\n"
    "v_exp_f32_e32 v170, v170\n"
    "v_mfma_f32_16x16x32_bf16 v[180:183], a[176:179], a[144:147], v[180:183]\n"
    "s_lshl2_add_u32 s90, s90, 6041600\n"
    "v_cvt_pk_bf16_f32 v166, v168, v169\n"
    "v_exp_f32_e32 v171, v171\n"
    "v_mfma_f32_16x16x32_bf16 v[180:183], a[180:183], a[148:151], v[180:183]\n"
    "s_lshl_b32 s45, s51, 15\n"
    "ds_read_b128 a[172:175], v241 offset:14336\n"
    "v_exp_f32_e32 v176, v176\n"
    "v_mfma_f32_16x16x32_bf16 v[148:151], a[180:183], a[116:119], v[148:151]\n"
    "s_sub_u32 s45, 24576, s45\n"
    "v_exp_f32_e32 v177, v177\n"
    "v_mfma_f32_16x16x32_bf16 v[148:151], a[176:179], a[112:115], v[148:151]\n"
    "s_add_u32 s47, s46, 6103040\n"
    "ds_read2st64_b32 a[212:213], v249 offset0:8 offset1:9\n"
    "v_cvt_pk_bf16_f32 v167, v170, v171\n"
    "v_mfma_f32_16x16x32_bf16 v[140:143], a[176:179], a[80:83], v[140:143]\n"
    "s_andn2_b32 s48, s38, 0xc00\n"
    "ds_write_addtid_b32 v173 offset:5376\n"
    "v_subrev_f32_e32 v53, s4, v53\n"
    "v_mfma_f32_16x16x32_bf16 v[140:143], a[180:183], a[84:87], v[140:143]\n"
    "s_waitcnt lgkmcnt(10)\n"
    "ds_read2st64_b32 a[214:215], v249 offset0:10 offset1:11\n"
    "v_exp_f32_e32 v178, v178\n"
    "v_mfma_f32_16x16x32_bf16 v[140:143], a[184:187], a[88:91], v[140:143]\n"
    "s_andn2_b32 s61, s90, 0x18c\n"
    "ds_write_addtid_b32 v158 offset:3584\n"
    "v_exp_f32_e32 v179, v179\n"
    "v_mfma_f32_16x16x32_bf16 v[140:143], a[188:191], a[92:95], v[140:143]\n"
    "buffer_load_dword v188, v189, s[8:11], s81 offen offset:0 sc1\n"
    "ds_write_addtid_b32 v159 offset:3840\n"
    "v_mfma_f32_16x16x32_bf16 v[148:151], a[188:191], a[124:127], v[148:151]\n"
    "s_add_u32 s31, s46, s35\n"
    "ds_read2st64_b32 a[208:209], v252 offset0:8 offset1:9\n"
    "v_subrev_f32_e32 v74, s4, v74\n"
    "v_mfma_f32_16x16x32_bf16 v[148:151], a[184:187], a[120:123], v[148:151]\n"
    "s_add_u32 m0, s63, 6144\n"
    "ds_read2st64_b32 a[210:211], v252 offset0:10 offset1:11\n"
    "v_exp_f32_e32 v140, v140\n"
    "v_mfma_f32_16x16x32_bf16 v[180:183], a[184:187], a[152:155], v[180:183]\n"
    "buffer_load_dwordx4 v251, s[12:15], s31 offen offset:0 lds\n"
    "ds_read_b128 a[176:179], v242 offset:10240\n"
    "v_mfma_f32_16x16x32_bf16 v[180:183], a[188:191], a[156:159], v[180:183]\n"
    "s_waitcnt lgkmcnt(12)\n"
    "v_exp_f32_e32 v141, v141\n"
    "v_mfma_f32_16x16x32_bf16 v[184:187], a[160:163], a[128:131], v[236:239]\n"
    "s_mov_b32 m0, s63\n"
    "ds_read_b128 a[180:183], v243 offset:10240\n"
    "v_exp_f32_e32 v142, v142\n"
    "v_mfma_f32_16x16x32_bf16 v[184:187], a[164:167], a[132:135], v[184:187]\n"
    "ds_read_b128 a[184:187], v242 offset:14336\n"
    "v_exp_f32_e32 v143, v143\n"
    "v_mfma_f32_16x16x32_bf16 v[152:155], a[164:167], a[100:103], v[232:235]\n"
    "s_addk_i32 s31, 0x1000\n"
    "ds_write_addtid_b32 v166 offset:4608\n"
    "v_cvt_pk_bf16_f32 v140, v140, v141\n"
    "v_mfma_f32_16x16x32_bf16 v[152:155], a[160:163], a[96:99], v[152:155]\n"
    "s_add_u32 m0, s63, 7168\n"
    "v_exp_f32_e32 v148, v148\n"
    "v_mfma_f32_16x16x32_bf16 v[144:147], a[160:163], a[64:67], v[228:231]\n"
    "buffer_load_dwordx4 v251, s[12:15], s31 offen offset:0 lds\n"
    "v_mfma_f32_16x16x32_bf16 v[144:147], a[164:167], a[68:71], v[144:147]\n"
    "s_waitcnt lgkmcnt(11)\n"
    "s_waitcnt lgkmcnt(4)\n"
    "s_waitcnt vmcnt(3)\n"
    "v_mfma_f32_16x16x32_bf16 v[144:147], a[168:171], a[72:75], v[144:147]\n"
    "s_barrier\n"
    "v_mfma_f32_16x16x32_bf16 v[144:147], a[172:175], a[76:79], v[144:147]\n"
    "s_mov_b32 m0, s63\n"
    "ds_read_b128 a[188:191], v243 offset:14336\n"
    "v_cvt_pk_bf16_f32 v174, v176, v177\n"
    "v_mfma_f32_16x16x32_bf16 v[152:155], a[172:175], a[108:111], v[152:155]\n"
    "s_cmp_eq_u32 s93, 1\n"
    "s_cselect_b32 s31, s36, 2.0\n"
    "ds_write_addtid_b32 v140 offset:0\n"
    "v_mfma_f32_16x16x32_bf16 v[152:155], a[168:171], a[104:107], v[152:155]\n"
    "s_cmp_eq_u32 s93, 2\n"
    "s_cselect_b32 s31, s37, s31\n"
    "ds_read_b128 a[160:163], v240 offset:16384\n"
    "v_mfma_f32_16x16x32_bf16 v[184:187], a[168:171], a[136:139], v[184:187]\n"
    "buffer_load_dword v191, v190, s[12:15], s31 offen offset:0\n"
    "v_mfma_f32_16x16x32_bf16 v[184:187], a[172:175], a[140:143], v[184:187]\n"
    "s_waitcnt lgkmcnt(5)\n"
    "v_exp_f32_e32 v149, v149\n"
    "v_mfma_f32_16x16x32_bf16 v[184:187], a[176:179], a[144:147], v[184:187]\n"
    "s_cmp_eq_u32 s93, 0\n"
    "s_cselect_b32 s31, s35, s31\n"
    "ds_read_b128 a[164:167], v241 offset:16384\n"
    "v_cvt_pk_bf16_f32 v141, v142, v143\n"
    "v_mfma_f32_16x16x32_bf16 v[184:187], a[180:183], a[148:151], v[184:187]\n"
    "s_add_u32 s31, s31, 6103040\n"
    "v_exp_f32_e32 v150, v150\n"
    "v_mfma_f32_16x16x32_bf16 v[152:155], a[180:183], a[116:119], v[152:155]\n"
    "ds_read_b128 a[168:171], v240 offset:20480\n"
    "v_cvt_pk_bf16_f32 v175, v178, v179\n"
    "v_mfma_f32_16x16x32_bf16 v[152:155], a[176:179], a[112:115], v[152:155]\n"
    "v_exp_f32_e32 v151, v151\n"
    "v_mfma_f32_16x16x32_bf16 v[144:147], a[176:179], a[80:83], v[144:147]\n"
    "ds_write_addtid_b32 v167 offset:4864\n"
    "v_subrev_f32_e32 v75, s4, v75\n"
    "v_mfma_f32_16x16x32_bf16 v[144:147], a[180:183], a[84:87], v[144:147]\n"
    "s_waitcnt lgkmcnt(5)\n"
    "ds_read_b128 a[172:175], v241 offset:20480\n"
    "v_cvt_pk_bf16_f32 v148, v148, v149\n"
    "v_mfma_f32_16x16x32_bf16 v[144:147], a[184:187], a[88:91], v[144:147]\n"
    "ds_write_addtid_b32 v141 offset:256\n"
    "v_exp_f32_e32 v180, v180\n"
    "v_mfma_f32_16x16x32_bf16 v[144:147], a[188:191], a[92:95], v[144:147]\n"
    "ds_write_addtid_b32 v174 offset:5632\n"
    "v_exp_f32_e32 v181, v181\n"
    "v_mfma_f32_16x16x32_bf16 v[152:155], a[188:191], a[124:127], v[152:155]\n"
    "ds_write_addtid_b32 v148 offset:1024\n"
    "v_cvt_pk_bf16_f32 v149, v150, v151\n"
    "v_mfma_f32_16x16x32_bf16 v[152:155], a[184:187], a[120:123], v[152:155]\n"
    "v_exp_f32_e32 v182, v182\n"
    "v_mfma_f32_16x16x32_bf16 v[184:187], a[184:187], a[152:155], v[184:187]\n"
    "v_exp_f32_e32 v183, v183\n"
    "v_mfma_f32_16x16x32_bf16 v[184:187], a[188:191], a[156:159], v[184:187]\n"
    "s_waitcnt lgkmcnt(6)\n"
    "ds_read_b128 a[176:179], v242 offset:16384\n"
    "v_cvt_pk_bf16_f32 v180, v180, v181\n"
    "v_mfma_f32_16x16x32_bf16 v[212:215], a[160:163], a[128:131], v[236:239]\n"
    "ds_write_addtid_b32 v175 offset:5888\n"
    "v_exp_f32_e32 v144, v144\n"
    "v_mfma_f32_16x16x32_bf16 v[212:215], a[164:167], a[132:135], v[212:215]\n"
    "ds_read_b128 a[184:187], v242 offset:20480\n"
    "v_exp_f32_e32 v145, v145\n"
    "v_mfma_f32_16x16x32_bf16 v[204:207], a[164:167], a[100:103], v[232:235]\n"
    "ds_read_b128 a[180:183], v243 offset:16384\n"
    "v_cvt_pk_bf16_f32 v181, v182, v183\n"
    "v_mfma_f32_16x16x32_bf16 v[204:207], a[160:163], a[96:99], v[204:207]\n"
    "v_exp_f32_e32 v146, v146\n"
    "v_mfma_f32_16x16x32_bf16 v[196:199], a[160:163], a[64:67], v[228:231]\n"
    "v_exp_f32_e32 v147, v147\n"
    "v_mfma_f32_16x16x32_bf16 v[196:199], a[164:167], a[68:71], v[196:199]\n"
    "s_waitcnt lgkmcnt(7)\n"
    "ds_write_addtid_b32 v149 offset:1280\n"
    "v_subrev_f32_e32 v116, s4, v116\n"
    "v_mfma_f32_16x16x32_bf16 v[196:199], a[168:171], a[72:75], v[196:199]\n"
    "ds_write_addtid_b32 v180 offset:2048\n"
    "v_subrev_f32_e32 v35, s4, v35\n"
    "v_mfma_f32_16x16x32_bf16 v[196:199], a[172:175], a[76:79], v[196:199]\n"
    "s_waitcnt lgkmcnt(4)\n"
    "v_mfma_f32_16x16x32_bf16 v[204:207], a[172:175], a[108:111], v[204:207]\n"
    "s_barrier\n"
    "v_mfma_f32_16x16x32_bf16 v[204:207], a[168:171], a[104:107], v[204:207]\n"
    "ds_read_b128 a[188:191], v243 offset:20480\n"
    "v_cvt_pk_bf16_f32 v142, v144, v145\n"
    "v_mfma_f32_16x16x32_bf16 v[212:215], a[168:171], a[136:139], v[212:215]\n"
    "v_exp_f32_e32 v152, v152\n"
    "v_mfma_f32_16x16x32_bf16 v[212:215], a[172:175], a[140:143], v[212:215]\n"
    "s_waitcnt lgkmcnt(3)\n"
    "v_exp_f32_e32 v153, v153\n"
    "v_mfma_f32_16x16x32_bf16 v[212:215], a[176:179], a[144:147], v[212:215]\n"
    "ds_read_b128 a[168:171], v240 offset:22528\n"
    "v_cvt_pk_bf16_f32 v143, v146, v147\n"
    "v_mfma_f32_16x16x32_bf16 v[212:215], a[180:183], a[148:151], v[212:215]\n"
    "ds_read_b128 a[160:163], v240 offset:18432\n"
    "v_exp_f32_e32 v154, v154\n"
    "v_mfma_f32_16x16x32_bf16 v[204:207], a[180:183], a[116:119], v[204:207]\n"
    "ds_write_addtid_b32 v181 offset:2304\n"
    "v_exp_f32_e32 v155, v155\n"
    "v_mfma_f32_16x16x32_bf16 v[204:207], a[176:179], a[112:115], v[204:207]\n"
    "ds_read2st64_b32 v[160:161], v249 offset0:12 offset1:13\n"
    "v_subrev_f32_e32 v54, s4, v54\n"
    "v_mfma_f32_16x16x32_bf16 v[196:199], a[176:179], a[80:83], v[196:199]\n"
    "ds_read_b128 a[164:167], v241 offset:18432\n"
    "ds_read_b128 a[172:175], v241 offset:22528\n"
    "v_mfma_f32_16x16x32_bf16 v[196:199], a[180:183], a[84:87], v[196:199]\n"
    "s_waitcnt lgkmcnt(6)\n"
    "ds_write_addtid_b32 v142 offset:512\n"
    "v_cvt_pk_bf16_f32 v150, v152, v153\n"
    "v_mfma_f32_16x16x32_bf16 v[196:199], a[184:187], a[88:91], v[196:199]\n"
    "ds_write_addtid_b32 v143 offset:768\n"
    "v_exp_f32_e32 v184, v184\n"
    "v_mfma_f32_16x16x32_bf16 v[196:199], a[188:191], a[92:95], v[196:199]\n"
    "ds_read2st64_b32 v[162:163], v249 offset0:14 offset1:15\n"
    "v_exp_f32_e32 v185, v185\n"
    "v_mfma_f32_16x16x32_bf16 v[204:207], a[188:191], a[124:127], v[204:207]\n"
    "ds_read2st64_b32 v[168:169], v249 offset0:16 offset1:17\n"
    "v_cvt_pk_bf16_f32 v151, v154, v155\n"
    "v_mfma_f32_16x16x32_bf16 v[204:207], a[184:187], a[120:123], v[204:207]\n"
    "s_waitcnt vmcnt(3)\n"
    "v_cmp_ne_u32_e32 vcc, s39, v188\n"
    "v_exp_f32_e32 v186, v186\n"
    "v_mfma_f32_16x16x32_bf16 v[212:215], a[184:187], a[152:155], v[212:215]\n"
    "ds_read_b128 a[176:179], v242 offset:18432\n"
    "v_exp_f32_e32 v196, v196\n"
    "v_mfma_f32_16x16x32_bf16 v[212:215], a[188:191], a[156:159], v[212:215]\n"
    "s_waitcnt lgkmcnt(6)\n"
    "ds_read_b128 a[184:187], v242 offset:22528\n"
    "v_exp_f32_e32 v197, v197\n"
    "v_mfma_f32_16x16x32_bf16 v[216:219], a[160:163], a[128:131], v[236:239]\n"
    "s_cmp_eq_u64 vcc, 0\n"
    "s_cselect_b32 s31, s31, 2.0\n"
    "s_cselect_b32 s44, 0, 1\n"
    "v_mfma_f32_16x16x32_bf16 v[216:219], a[164:167], a[132:135], v[216:219]\n"
    "buffer_load_dword v191, v190, s[8:11], s31 offen offset:0 sc1\n"
    "ds_read_b128 a[180:183], v243 offset:18432\n"
    "v_mfma_f32_16x16x32_bf16 v[208:211], a[164:167], a[100:103], v[232:235]\n"
    "ds_write_addtid_b32 v150 offset:1536\n"
    "v_exp_f32_e32 v198, v198\n"
    "v_mfma_f32_16x16x32_bf16 v[208:211], a[160:163], a[96:99], v[208:211]\n"
    "ds_read2st64_b32 v[170:171], v249 offset0:18 offset1:19\n"
    "v_exp_f32_e32 v199, v199\n"
    "v_mfma_f32_16x16x32_bf16 v[200:203], a[160:163], a[64:67], v[228:231]\n"
    "ds_read_b128 a[188:191], v243 offset:22528\n"
    "v_exp_f32_e32 v187, v187\n"
    "v_mfma_f32_16x16x32_bf16 v[200:203], a[164:167], a[68:71], v[200:203]\n"
    "s_waitcnt lgkmcnt(10)\n"
    "ds_read_b64_tr_b16 a[160:161], v244 offset:24576\n"
    "v_cvt_pk_bf16_f32 v196, v196, v197\n"
    "v_subrev_f32_e32 v31, s4, v31\n"
    "v_mfma_f32_16x16x32_bf16 v[200:203], a[168:171], a[72:75], v[200:203]\n"
    "ds_read_b64_tr_b16 a[162:163], v244 offset:26624\n"
    "v_exp_f32_e32 v204, v204\n"
    "v_mfma_f32_16x16x32_bf16 v[200:203], a[172:175], a[76:79], v[200:203]\n"
    "v_exp_f32_e32 v205, v205\n"
    "v_mfma_f32_16x16x32_bf16 v[208:211], a[172:175], a[108:111], v[208:211]\n"
    "ds_write_addtid_b32 v151 offset:1792\n"
    "v_and_b32_e32 v248, 0xf0, v250\n"
    "v_lshrrev_b32_e32 v231, 3, v248\n"
    "v_mfma_f32_16x16x32_bf16 v[208:211], a[168:171], a[104:107], v[208:211]\n"
    "ds_read2st64_b32 v[176:177], v249 offset0:20 offset1:21\n"
    "v_cvt_pk_bf16_f32 v182, v184, v185\n"
    "v_cvt_pk_bf16_f32 v197, v198, v199\n"
    "v_mfma_f32_16x16x32_bf16 v[216:219], a[168:171], a[136:139], v[216:219]\n"
    "s_lshr_b32 s31, s35, 8\n"
    "ds_read2st64_b32 v[178:179], v249 offset0:22 offset1:23\n"
    "v_exp_f32_e32 v206, v206\n"
    "v_mfma_f32_16x16x32_bf16 v[216:219], a[172:175], a[140:143], v[216:219]\n"
    "s_waitcnt lgkmcnt(8)\n"
    "ds_write_addtid_b32 v182 offset:2560\n"
    "v_cvt_pk_bf16_f32 v183, v186, v187\n"
    "v_mfma_f32_16x16x32_bf16 v[216:219], a[176:179], a[144:147], v[216:219]\n"
    "buffer_load_ushort v228, v231, s[24:27], s31 offen\n"
    "ds_read_b64_tr_b16 a[164:165], v244 offset:57344\n"
    "v_mfma_f32_16x16x32_bf16 v[216:219], a[180:183], a[148:151], v[216:219]\n"
    "s_lshr_b32 s31, s36, 8\n"
    "ds_read_b64_tr_b16 a[166:167], v244 offset:59392\n"
    "v_exp_f32_e32 v207, v207\n"
    "v_mfma_f32_16x16x32_bf16 v[208:211], a[180:183], a[116:119], v[208:211]\n"
    "ds_read_b64_tr_b16 a[168:169], v245 offset:24576\n"
    "v_cvt_pk_bf16_f32 v204, v204, v205\n"
    "v_mfma_f32_16x16x32_bf16 v[208:211], a[176:179], a[112:115], v[208:211]\n"
    "ds_read_b64_tr_b16 a[170:171], v245 offset:26624\n"
    "v_exp_f32_e32 v212, v212\n"
    "v_mfma_f32_16x16x32_bf16 v[200:203], a[176:179], a[80:83], v[200:203]\n"
    "buffer_load_ushort v229, v231, s[24:27], s31 offen\n"
    "ds_read_b64_tr_b16 a[172:173], v245 offset:57344\n"
    "v_mfma_f32_16x16x32_bf16 v[200:203], a[180:183], a[84:87], v[200:203]\n"
    "s_waitcnt lgkmcnt(11)\n"
    "ds_write_addtid_b32 v183 offset:2816\n"
    "v_exp_f32_e32 v213, v213\n"
    "v_mfma_f32_16x16x32_bf16 v[200:203], a[184:187], a[88:91], v[200:203]\n"
    "s_lshr_b32 s31, s37, 8\n"
    "ds_read_b64_tr_b16 a[176:177], v246 offset:24576\n"
    "v_cvt_pk_bf16_f32 v205, v206, v207\n"
    "v_mfma_f32_16x16x32_bf16 v[200:203], a[188:191], a[92:95], v[200:203]\n"
    "ds_read_b64_tr_b16 a[178:179], v246 offset:26624\n"
    "v_exp_f32_e32 v214, v214\n"
    "v_mfma_f32_16x16x32_bf16 v[208:211], a[188:191], a[124:127], v[208:211]\n"
    "buffer_load_ushort v230, v231, s[24:27], s31 offen\n"
    "ds_read_b64_tr_b16 a[174:175], v245 offset:59392\n"
    "v_mfma_f32_16x16x32_bf16 v[208:211], a[184:187], a[120:123], v[208:211]\n"
    "s_waitcnt lgkmcnt(3)\n"
    "v_mfma_f32_16x16x32_bf16 v[216:219], a[184:187], a[152:155], v[216:219]\n"
    "s_barrier\n"
    "v_mfma_f32_16x16x32_bf16 v[216:219], a[188:191], a[156:159], v[216:219]\n"
    "ds_read_b64_tr_b16 a[184:185], v246 offset:0\n"
    "v_exp_f32_e32 v215, v215\n"
    "v_mfma_f32_16x16x32_bf16 a[252:255], a[160:163], a[208:211], a[252:255]\n"
    "s_add_u32 s62, s57, 4096\n"
    "ds_write_addtid_b32 v196 offset:3072\n"
    "v_cvt_pk_bf16_f32 v212, v212, v213\n"
    "v_mfma_f32_16x16x32_bf16 a[252:255], a[164:167], a[212:215], a[252:255]\n"
    "buffer_load_dwordx4 a[64:67], v250, s[8:11], s62 offen offset:2048 sc1\n"
    "ds_write_addtid_b32 v197 offset:3328\n"
    "v_mfma_f32_16x16x32_bf16 a[248:251], a[164:167], a[204:207], a[248:251]\n"
    "ds_read_b64_tr_b16 a[180:181], v246 offset:57344\n"
    "v_exp_f32_e32 v200, v200\n"
    "v_mfma_f32_16x16x32_bf16 a[248:251], a[160:163], a[200:203], a[248:251]\n"
    "ds_read_b64_tr_b16 a[186:187], v246 offset:2048\n"
    "v_exp_f32_e32 v201, v201\n"
    "v_mfma_f32_16x16x32_bf16 a[244:247], a[160:163], a[192:195], a[244:247]\n"
    "ds_read_b64_tr_b16 a[182:183], v246 offset:59392\n"
    "ds_read2st64_b32 v[144:145], v249 offset0:0 offset1:1\n"
    "v_mfma_f32_16x16x32_bf16 a[244:247], a[164:167], a[196:199], a[244:247]\n"
    "s_waitcnt lgkmcnt(7)\n"
    "ds_write_addtid_b32 v204 offset:4096\n"
    "v_lshrrev_b32_e32 v231, 2, v250\n"
    "v_mfma_f32_16x16x32_bf16 v[8:11], a[168:171], a[192:195], v[8:11]\n"
    "buffer_load_dwordx4 a[68:71], v250, s[8:11], s62 offen offset:3072 sc1\n"
    "ds_read_b64_tr_b16 a[160:161], v244 offset:0\n"
    "v_mfma_f32_16x16x32_bf16 v[8:11], a[172:175], a[196:199], v[8:11]\n"
    "s_add_u32 s62, s57, 8192\n"
    "ds_read_b64_tr_b16 a[188:189], v246 offset:32768\n"
    "ds_read_b64_tr_b16 a[162:163], v244 offset:2048\n"
    "v_cvt_pk_bf16_f32 v213, v214, v215\n"
    "v_mfma_f32_16x16x32_bf16 v[12:15], a[172:175], a[204:207], v[12:15]\n"
    "ds_read2st64_b32 v[146:147], v249 offset0:2 offset1:3\n"
    "v_exp_f32_e32 v202, v202\n"
    "v_mfma_f32_16x16x32_bf16 v[12:15], a[168:171], a[200:203], v[12:15]\n"
    "ds_write_addtid_b32 v205 offset:4352\n"
    "v_exp_f32_e32 v203, v203\n"
    "v_mfma_f32_16x16x32_bf16 v[16:19], a[168:171], a[208:211], v[16:19]\n"
    "ds_read_b64_tr_b16 a[190:191], v246 offset:34816\n"
    "v_cvt_pk_bf16_f32 v198, v200, v201\n"
    "v_mfma_f32_16x16x32_bf16 v[16:19], a[172:175], a[212:215], v[16:19]\n"
    "s_waitcnt lgkmcnt(8)\n"
    "ds_read_b64_tr_b16 a[164:165], v244 offset:32768\n"
    "v_exp_f32_e32 v208, v208\n"
    "v_mfma_f32_16x16x32_bf16 v[36:39], a[176:179], a[208:211], v[36:39]\n"
    "buffer_load_dwordx4 a[72:75], v250, s[8:11], s62 offen offset:0 sc1\n"
    "ds_read2st64_b32 v[152:153], v249 offset0:4 offset1:5\n"
    "v_mfma_f32_16x16x32_bf16 v[36:39], a[180:183], a[212:215], v[36:39]\n"
    "ds_read2st64_b32 v[154:155], v249 offset0:6 offset1:7\n"
    "v_exp_f32_e32 v209, v209\n"
    "v_mfma_f32_16x16x32_bf16 v[32:35], a[180:183], a[204:207], v[32:35]\n"
    "ds_read_b64_tr_b16 a[166:167], v244 offset:34816\n"
    "v_cvt_pk_bf16_f32 v199, v202, v203\n"
    "v_mfma_f32_16x16x32_bf16 v[32:35], a[176:179], a[200:203], v[32:35]\n"
    "ds_write_addtid_b32 v212 offset:5120\n"
    "v_exp_f32_e32 v210, v210\n"
    "v_mfma_f32_16x16x32_bf16 v[28:31], a[176:179], a[192:195], v[28:31]\n"
    "ds_read_b64_tr_b16 a[168:169], v245 offset:0\n"
    "v_subrev_u32_e32 v231, 0x80, v231\n"
    "v_mfma_f32_16x16x32_bf16 v[28:31], a[180:183], a[196:199], v[28:31]\n"
    "s_waitcnt lgkmcnt(6)\n"
    "ds_read_b64_tr_b16 a[170:171], v245 offset:2048\n"
    "v_min_u32_e32 v231, 2.0, v231\n"
    "v_lshrrev_b32_e32 v252, 4, v250\n"
    "v_mfma_f32_16x16x32_bf16 v[28:31], a[184:187], v[156:159], v[28:31]\n"
    "buffer_load_dwordx4 a[76:79], v250, s[8:11], s62 offen offset:1024 sc1\n"
    "ds_read2st64_b32 v[184:185], v249 offset0:8 offset1:9\n"
    "v_mfma_f32_16x16x32_bf16 v[28:31], a[188:191], v[160:163], v[28:31]\n"
    "ds_read2st64_b32 v[186:187], v249 offset0:10 offset1:11\n"
    "v_exp_f32_e32 v211, v211\n"
    "v_mfma_f32_16x16x32_bf16 v[32:35], a[188:191], v[168:171], v[32:35]\n"
    "ds_read_b64_tr_b16 a[176:177], v245 offset:8192\n"
    "v_cvt_pk_bf16_f32 v206, v208, v209\n"
    "v_mfma_f32_16x16x32_bf16 v[32:35], a[184:187], v[164:167], v[32:35]\n"
    "ds_read_b64_tr_b16 a[172:173], v245 offset:32768\n"
    "v_exp_f32_e32 v216, v216\n"
    "v_mfma_f32_16x16x32_bf16 v[36:39], a[184:187], v[172:175], v[36:39]\n"
    "ds_read_b64_tr_b16 a[174:175], v245 offset:34816\n"
    "v_lshlrev_b32_e32 v239, 5, v248\n"
    "v_mfma_f32_16x16x32_bf16 v[36:39], a[188:191], v[176:179], v[36:39]\n"
    "s_waitcnt lgkmcnt(8)\n"
    "ds_read_b64_tr_b16 a[178:179], v245 offset:10240\n"
    "v_exp_f32_e32 v217, v217\n"
    "v_mfma_f32_16x16x32_bf16 a[252:255], a[160:163], v[172:175], a[252:255]\n"
    "buffer_load_dwordx4 a[80:83], v250, s[8:11], s62 offen offset:2048 sc1\n"
    "ds_read_b64_tr_b16 a[180:181], v245 offset:40960\n"
    "v_mfma_f32_16x16x32_bf16 a[252:255], a[164:167], v[176:179], a[252:255]\n"
    "ds_read_b64_tr_b16 a[182:183], v245 offset:43008\n"
    "ds_read_b64_tr_b16 a[184:185], v244 offset:8192\n"
    "ds_read_b64_tr_b16 a[186:187], v244 offset:10240\n"
    "v_mfma_f32_16x16x32_bf16 a[248:251], a[164:167], v[168:171], a[248:251]\n"
    "ds_write_addtid_b32 v213 offset:5376\n"
    "v_cvt_pk_bf16_f32 v207, v210, v211\n"
    "v_mfma_f32_16x16x32_bf16 a[248:251], a[160:163], v[164:167], a[248:251]\n"
    "ds_read_b64_tr_b16 a[188:189], v244 offset:40960\n"
    "ds_write_addtid_b32 v198 offset:3584\n"
    "v_mfma_f32_16x16x32_bf16 a[244:247], a[160:163], v[156:159], a[244:247]\n"
    "ds_write_addtid_b32 v199 offset:3840\n"
    "v_exp_f32_e32 v218, v218\n"
    "v_mfma_f32_16x16x32_bf16 a[244:247], a[164:167], v[160:163], a[244:247]\n"
    "s_waitcnt lgkmcnt(9)\n"
    "ds_write_addtid_b32 v206 offset:4608\n"
    "v_exp_f32_e32 v219, v219\n"
    "v_mfma_f32_16x16x32_bf16 v[8:11], a[168:171], v[156:159], v[8:11]\n"
    "buffer_load_dwordx4 a[84:87], v250, s[8:11], s62 offen offset:3072 sc1\n"
    "ds_read_b64_tr_b16 a[160:161], v244 offset:16384\n"
    "v_mfma_f32_16x16x32_bf16 v[8:11], a[172:175], v[160:163], v[8:11]\n"
    "s_add_u32 s62, s57, 12288\n"
    "ds_write_addtid_b32 v207 offset:4864\n"
    "v_cvt_pk_bf16_f32 v214, v216, v217\n"
    "v_cvt_pk_bf16_f32 v215, v218, v219\n"
    "v_mfma_f32_16x16x32_bf16 v[12:15], a[172:175], v[168:171], v[12:15]\n"
    "ds_write_addtid_b32 v214 offset:5632\n"
    "ds_write_addtid_b32 v215 offset:5888\n"
    "v_mfma_f32_16x16x32_bf16 v[12:15], a[168:171], v[164:167], v[12:15]\n"
    "ds_read_b64_tr_b16 a[190:191], v244 offset:43008\n"
    "v_and_or_b32 v239, v252, 48, v239\n"
    "v_lshrrev_b32_e32 v248, 2, v248\n"
    "v_mfma_f32_16x16x32_bf16 v[16:19], a[168:171], v[172:175], v[16:19]\n"
    "ds_read_b64_tr_b16 a[162:163], v244 offset:18432\n"
    "ds_read_b64_tr_b16 a[164:165], v244 offset:49152\n"
    "v_mfma_f32_16x16x32_bf16 v[16:19], a[172:175], v[176:179], v[16:19]\n"
    "s_waitcnt lgkmcnt(14)\n"
    "s_waitcnt lgkmcnt(3)\n"
    "v_mfma_f32_16x16x32_bf16 v[16:19], a[176:179], v[180:183], v[16:19]\n"
    "s_barrier\n"
    "v_mfma_f32_16x16x32_bf16 v[16:19], a[180:183], v[184:187], v[16:19]\n"
    "buffer_load_dwordx4 a[88:91], v250, s[8:11], s62 offen offset:0 sc1\n"
    "ds_read2st64_b32 v[200:201], v249 offset0:12 offset1:13\n"
    "v_mfma_f32_16x16x32_bf16 v[12:15], a[180:183], v[152:155], v[12:15]\n"
    "ds_read_b64_tr_b16 a[166:167], v244 offset:51200\n"
    "ds_read2st64_b32 v[202:203], v249 offset0:14 offset1:15\n"
    "v_mfma_f32_16x16x32_bf16 v[12:15], a[176:179], v[148:151], v[12:15]\n"
    "ds_read2st64_b32 v[208:209], v249 offset0:16 offset1:17\n"
    "v_lshrrev_b32_e32 v253, 4, v252\n"
    "v_mfma_f32_16x16x32_bf16 v[8:11], a[176:179], v[140:143], v[8:11]\n"
    "ds_read2st64_b32 v[210:211], v249 offset0:18 offset1:19\n"
    "v_lshl_add_u32 v248, v253, 7, v248\n"
    "v_mfma_f32_16x16x32_bf16 v[8:11], a[180:183], v[144:147], v[8:11]\n"
    "s_waitcnt lgkmcnt(7)\n"
    "ds_read2st64_b32 v[216:217], v249 offset0:20 offset1:21\n"
    "ds_read2st64_b32 v[218:219], v249 offset0:22 offset1:23\n"
    "v_bfe_u32 v255, v252, 2, 2\n"
    "v_mfma_f32_16x16x32_bf16 a[244:247], a[184:187], v[140:143], a[244:247]\n"
    "buffer_load_dwordx4 a[92:95], v250, s[8:11], s62 offen offset:1024 sc1\n"
    "v_mfma_f32_16x16x32_bf16 a[244:247], a[188:191], v[144:147], a[244:247]\n"
    "ds_read_b64_tr_b16 a[168:169], v245 offset:16384\n"
    "v_subrev_f32_e32 v120, s4, v120\n"
    "v_mfma_f32_16x16x32_bf16 a[248:251], a[188:191], v[152:155], a[248:251]\n"
    "ds_read_b64_tr_b16 a[170:171], v245 offset:18432\n"
    "v_subrev_f32_e32 v58, s4, v58\n"
    "v_mfma_f32_16x16x32_bf16 a[248:251], a[184:187], v[148:151], a[248:251]\n"
    "ds_read_b64_tr_b16 a[172:173], v245 offset:49152\n"
    "v_max_u32_e32 v238, 1, v255\n"
    "v_mfma_f32_16x16x32_bf16 a[252:255], a[184:187], v[180:183], a[252:255]\n"
    "ds_read_b64_tr_b16 a[174:175], v245 offset:51200\n"
    "v_add_u32_e32 v238, s32, v238\n"
    "v_mfma_f32_16x16x32_bf16 a[252:255], a[188:191], v[184:187], a[252:255]\n"
    "s_waitcnt lgkmcnt(4)\n"
    "ds_read_b64_tr_b16 a[176:177], v246 offset:8192\n"
    "v_and_b32_e32 v238, 3, v238\n"
    "v_mfma_f32_16x16x32_bf16 a[252:255], a[160:163], v[212:215], a[252:255]\n"
    "buffer_load_dwordx4 a[96:99], v250, s[8:11], s62 offen offset:2048 sc1\n"
    "ds_read_b64_tr_b16 a[184:185], v246 offset:16384\n"
    "v_mfma_f32_16x16x32_bf16 a[252:255], a[164:167], v[216:219], a[252:255]\n"
    "ds_read_b64_tr_b16 a[178:179], v246 offset:10240\n"
    "v_subrev_f32_e32 v97, s4, v97\n"
    "v_mfma_f32_16x16x32_bf16 a[248:251], a[164:167], v[208:211], a[248:251]\n"
    "ds_read_b64_tr_b16 a[180:181], v246 offset:40960\n"
    "v_lshlrev_b32_e32 v238, 10, v238\n"
    "v_mfma_f32_16x16x32_bf16 a[248:251], a[160:163], v[204:207], a[248:251]\n"
    "ds_read_b64_tr_b16 a[186:187], v246 offset:18432\n"
    "v_and_b32_e32 v252, 3, v252\n"
    "v_mfma_f32_16x16x32_bf16 a[244:247], a[160:163], v[196:199], a[244:247]\n"
    "ds_read_b64_tr_b16 a[182:183], v246 offset:43008\n"
    "v_subrev_f32_e32 v128, s4, v128\n"
    "v_mfma_f32_16x16x32_bf16 a[244:247], a[164:167], v[200:203], a[244:247]\n"
    "s_waitcnt lgkmcnt(6)\n"
    "ds_read_b64_tr_b16 a[188:189], v246 offset:49152\n"
    "v_xor_b32_e32 v253, v253, v255\n"
    "v_mfma_f32_16x16x32_bf16 v[8:11], a[168:171], v[196:199], v[8:11]\n"
    "buffer_load_dwordx4 a[100:103], v250, s[8:11], s62 offen offset:3072 sc1\n"
    "ds_read_b64_tr_b16 a[160:161], v247 offset:24576\n"
    "v_mfma_f32_16x16x32_bf16 v[8:11], a[172:175], v[200:203], v[8:11]\n"
    "s_add_u32 s62, s57, 16384\n"
    "ds_read_b64_tr_b16 a[190:191], v246 offset:51200\n"
    "v_subrev_f32_e32 v55, s4, v55\n"
    "v_mfma_f32_16x16x32_bf16 v[12:15], a[172:175], v[208:211], v[12:15]\n"
    "ds_read_b64_tr_b16 a[162:163], v247 offset:26624\n"
    "v_subrev_f32_e32 v78, s4, v78\n"
    "v_mfma_f32_16x16x32_bf16 v[12:15], a[168:171], v[204:207], v[12:15]\n"
    "v_cvt_pk_bf16_f32 v11, v10, v11\n"
    "v_cvt_pk_bf16_f32 v10, v8, v9\n"
    "v_mfma_f32_16x16x32_bf16 v[16:19], a[168:171], v[212:215], v[16:19]\n"
    "ds_read_b64_tr_b16 a[164:165], v247 offset:57344\n"
    "v_subrev_f32_e32 v50, s4, v50\n"
    "v_mfma_f32_16x16x32_bf16 v[16:19], a[172:175], v[216:219], v[16:19]\n"
    "s_waitcnt lgkmcnt(5)\n"
    "v_accvgpr_read_b32 v8, a244\n"
    "v_accvgpr_read_b32 v9, a245\n"
    "v_mfma_f32_16x16x32_bf16 v[36:39], a[176:179], v[180:183], v[36:39]\n"
    "v_accvgpr_read_b32 v255, a247\n"
    "v_cvt_pk_bf16_f32 v8, v8, v9\n"
    "v_mfma_f32_16x16x32_bf16 v[36:39], a[180:183], v[184:187], v[36:39]\n"
    "v_accvgpr_read_b32 v9, a246\n"
    "v_cvt_pk_bf16_f32 v9, v9, v255\n"
    "v_mfma_f32_16x16x32_bf16 v[32:35], a[180:183], v[152:155], v[32:35]\n"
    "buffer_store_dwordx4 v[8:11], v250, s[8:11], s82 offen offset:0 sc1\n"
    "v_mfma_f32_16x16x32_bf16 v[32:35], a[176:179], v[148:151], v[32:35]\n"
    "ds_read_b64_tr_b16 a[166:167], v247 offset:59392\n"
    "v_cvt_pk_bf16_f32 v15, v14, v15\n"
    "v_mfma_f32_16x16x32_bf16 v[28:31], a[176:179], v[140:143], v[28:31]\n"
    "v_cvt_pk_bf16_f32 v14, v12, v13\n"
    "v_accvgpr_read_b32 v12, a248\n"
    "v_mfma_f32_16x16x32_bf16 v[28:31], a[180:183], v[144:147], v[28:31]\n"
    "s_waitcnt lgkmcnt(3)\n"
    "ds_read_b64_tr_b16 a[168:169], v247 offset:0\n"
    "v_accvgpr_read_b32 v13, a249\n"
    "v_mfma_f32_16x16x32_bf16 v[28:31], a[184:187], v[196:199], v[28:31]\n"
    "buffer_load_dwordx4 a[104:107], v250, s[8:11], s62 offen offset:0 sc1\n"
    "v_mfma_f32_16x16x32_bf16 v[28:31], a[188:191], v[200:203], v[28:31]\n"
    "ds_read_b64_tr_b16 a[170:171], v247 offset:2048\n"
    "v_accvgpr_read_b32 v255, a251\n"
    "v_cvt_pk_bf16_f32 v12, v12, v13\n"
    "v_mfma_f32_16x16x32_bf16 v[32:35], a[188:191], v[208:211], v[32:35]\n"
    "ds_read_b64_tr_b16 a[172:173], v247 offset:32768\n"
    "v_subrev_f32_e32 v59, s4, v59\n"
    "v_mfma_f32_16x16x32_bf16 v[32:35], a[184:187], v[204:207], v[32:35]\n"
    "ds_read_b64_tr_b16 a[176:177], v247 offset:8192\n"
    "v_subrev_f32_e32 v51, s4, v51\n"
    "v_mfma_f32_16x16x32_bf16 v[36:39], a[184:187], v[212:215], v[36:39]\n"
    "ds_read_b64_tr_b16 a[174:175], v247 offset:34816\n"
    "v_accvgpr_read_b32 v13, a250\n"
    "v_mfma_f32_16x16x32_bf16 v[36:39], a[188:191], v[216:219], v[36:39]\n"
    "s_waitcnt lgkmcnt(5)\n"
    "ds_read_b64_tr_b16 a[178:179], v247 offset:10240\n"
    "v_cvt_pk_bf16_f32 v13, v13, v255\n"
    "v_mfma_f32_16x16x32_bf16 v[56:59], a[160:163], a[208:211], v[56:59]\n"
    "buffer_load_dwordx4 a[108:111], v250, s[8:11], s62 offen offset:1024 sc1\n"
    "v_mfma_f32_16x16x32_bf16 v[56:59], a[164:167], a[212:215], v[56:59]\n"
    "ds_read_b64_tr_b16 a[184:185], v247 offset:16384\n"
    "v_lshl_or_b32 v252, v253, 2, v252\n"
    "v_mfma_f32_16x16x32_bf16 v[52:55], a[164:167], a[204:207], v[52:55]\n"
    "ds_read_b64_tr_b16 a[180:181], v247 offset:40960\n"
    "v_cvt_pk_bf16_f32 v28, v28, v29\n"
    "v_mfma_f32_16x16x32_bf16 v[52:55], a[160:163], a[200:203], v[52:55]\n"
    "buffer_store_dwordx4 v[12:15], v250, s[8:11], s28 offen offset:0 sc1\n"
    "v_mfma_f32_16x16x32_bf16 v[48:51], a[160:163], a[192:195], v[48:51]\n"
    "s_add_u32 s62, s57, 16384\n"
    "ds_read_b64_tr_b16 a[182:183], v247 offset:43008\n"
    "v_cvt_pk_bf16_f32 v19, v18, v19\n"
    "v_mfma_f32_16x16x32_bf16 v[48:51], a[164:167], a[196:199], v[48:51]\n"
    "s_waitcnt lgkmcnt(4)\n"
    "ds_read_b64_tr_b16 a[186:187], v247 offset:18432\n"
    "v_cvt_pk_bf16_f32 v18, v16, v17\n"
    "v_mfma_f32_16x16x32_bf16 v[48:51], a[168:171], v[156:159], v[48:51]\n"
    "buffer_load_dwordx4 a[112:115], v250, s[8:11], s62 offen offset:2048 sc1\n"
    "v_mfma_f32_16x16x32_bf16 v[48:51], a[172:175], v[160:163], v[48:51]\n"
    "ds_read_b64_tr_b16 a[160:161], v244 offset:28672\n"
    "v_accvgpr_read_b32 v16, a252\n"
    "v_mfma_f32_16x16x32_bf16 v[52:55], a[172:175], v[168:171], v[52:55]\n"
    "ds_read_b64_tr_b16 a[188:189], v247 offset:49152\n"
    "v_subrev_f32_e32 v79, s4, v79\n"
    "v_mfma_f32_16x16x32_bf16 v[52:55], a[168:171], v[164:167], v[52:55]\n"
    "v_accvgpr_read_b32 v17, a253\n"
    "v_accvgpr_read_b32 v255, a255\n"
    "v_mfma_f32_16x16x32_bf16 v[56:59], a[168:171], v[172:175], v[56:59]\n"
    "ds_read_b64_tr_b16 a[190:191], v247 offset:51200\n"
    "v_cvt_pk_bf16_f32 v16, v16, v17\n"
    "v_mfma_f32_16x16x32_bf16 v[56:59], a[172:175], v[176:179], v[56:59]\n"
    "s_waitcnt lgkmcnt(4)\n"
    "ds_read_b64_tr_b16 a[162:163], v244 offset:30720\n"
    "v_subrev_f32_e32 v98, s4, v98\n"
    "v_mfma_f32_16x16x32_bf16 v[56:59], a[176:179], v[180:183], v[56:59]\n"
    "v_accvgpr_read_b32 v17, a254\n"
    "v_cvt_pk_bf16_f32 v17, v17, v255\n"
    "v_mfma_f32_16x16x32_bf16 v[56:59], a[180:183], v[184:187], v[56:59]\n"
    ".long 0xbedf00ff\n"
    ".Lat_w11:\n"
    "s_setpc_b64 s[2:3]\n"
    "ds_read_b64_tr_b16 a[164:165], v244 offset:61440\n"
    "v_subrev_f32_e32 v80, s4, v80\n"
    "v_mfma_f32_16x16x32_bf16 v[52:55], a[180:183], v[152:155], v[52:55]\n"
    "ds_read_b64_tr_b16 a[168:169], v244 offset:4096\n"
    "v_subrev_f32_e32 v71, s4, v71\n"
    "v_mfma_f32_16x16x32_bf16 v[52:55], a[176:179], v[148:151], v[52:55]\n"
    "ds_read_b64_tr_b16 a[166:167], v244 offset:63488\n"
    "v_subrev_f32_e32 v133, s4, v133\n"
    "v_mfma_f32_16x16x32_bf16 v[48:51], a[176:179], v[140:143], v[48:51]\n"
    "buffer_store_dwordx4 v[16:19], v250, s[8:11], s29 offen offset:0 sc1\n"
    "v_mfma_f32_16x16x32_bf16 v[48:51], a[180:183], v[144:147], v[48:51]\n"
    "s_waitcnt lgkmcnt(4)\n"
    "ds_read_b64_tr_b16 a[170:171], v244 offset:6144\n"
    "v_cvt_pk_bf16_f32 v29, v30, v31\n"
    "v_mfma_f32_16x16x32_bf16 v[48:51], a[184:187], v[196:199], v[48:51]\n"
    "ds_read_b64_tr_b16 a[176:177], v244 offset:12288\n"
    "v_cvt_pk_bf16_f32 v32, v32, v33\n"
    "v_mfma_f32_16x16x32_bf16 v[48:51], a[188:191], v[200:203], v[48:51]\n"
    "buffer_load_dwordx4 a[116:119], v250, s[8:11], s62 offen offset:3072 sc1\n"
    "v_mfma_f32_16x16x32_bf16 v[52:55], a[188:191], v[208:211], v[52:55]\n"
    "s_add_u32 s62, s57, 20480\n"
    "ds_read_b64_tr_b16 a[178:179], v244 offset:14336\n"
    "v_subrev_f32_e32 v113, s4, v113\n"
    "v_mfma_f32_16x16x32_bf16 v[52:55], a[184:187], v[204:207], v[52:55]\n"
    "ds_read_b64_tr_b16 a[172:173], v244 offset:36864\n"
    "v_cvt_pk_bf16_f32 v33, v34, v35\n"
    "v_mfma_f32_16x16x32_bf16 v[56:59], a[184:187], v[212:215], v[56:59]\n"
    "ds_read_b64_tr_b16 a[174:175], v244 offset:38912\n"
    "v_subrev_f32_e32 v91, s4, v91\n"
    "v_mfma_f32_16x16x32_bf16 v[56:59], a[188:191], v[216:219], v[56:59]\n"
    "s_waitcnt lgkmcnt(5)\n"
    "ds_read_b64_tr_b16 a[180:181], v244 offset:45056\n"
    "v_cvt_pk_bf16_f32 v36, v36, v37\n"
    "v_mfma_f32_16x16x32_bf16 v[76:79], a[160:163], a[208:211], v[76:79]\n"
    "ds_read_b64_tr_b16 a[184:185], v244 offset:20480\n"
    "v_cvt_pk_bf16_f32 v37, v38, v39\n"
    "v_mfma_f32_16x16x32_bf16 v[76:79], a[164:167], a[212:215], v[76:79]\n"
    "ds_read_b64_tr_b16 a[182:183], v244 offset:47104\n"
    "ds_read_b64_tr_b16 a[186:187], v244 offset:22528\n"
    "v_mfma_f32_16x16x32_bf16 v[72:75], a[164:167], a[204:207], v[72:75]\n"
    "v_cvt_pk_bf16_f32 v30, v48, v49\n"
    "v_cvt_pk_bf16_f32 v31, v50, v51\n"
    "v_mfma_f32_16x16x32_bf16 v[72:75], a[160:163], a[200:203], v[72:75]\n"
    "buffer_load_dwordx4 a[120:123], v250, s[8:11], s62 offen offset:0 sc1\n"
    "v_mfma_f32_16x16x32_bf16 v[68:71], a[160:163], a[192:195], v[68:71]\n"
    "ds_read_b64_tr_b16 a[188:189], v244 offset:53248\n"
    "v_subrev_f32_e32 v99, s4, v99\n"
    "v_mfma_f32_16x16x32_bf16 v[68:71], a[164:167], a[196:199], v[68:71]\n"
    "s_waitcnt lgkmcnt(5)\n"
    "v_cvt_pk_bf16_f32 v34, v52, v53\n"
    "v_subrev_f32_e32 v92, s4, v92\n"
    "v_mfma_f32_16x16x32_bf16 v[68:71], a[168:171], v[156:159], v[68:71]\n"
    "buffer_store_dwordx4 v[28:31], v250, s[8:11], s82 offen offset:1024 sc1\n"
    "v_mfma_f32_16x16x32_bf16 v[68:71], a[172:175], v[160:163], v[68:71]\n"
    "ds_read_b64_tr_b16 a[190:191], v244 offset:55296\n"
    "v_subrev_f32_e32 v81, s4, v81\n"
    "v_mfma_f32_16x16x32_bf16 v[72:75], a[172:175], v[168:171], v[72:75]\n"
    "v_subrev_f32_e32 v117, s4, v117\n"
    "v_subrev_f32_e32 v118, s4, v118\n"
    "v_mfma_f32_16x16x32_bf16 v[72:75], a[168:171], v[164:167], v[72:75]\n"
    "ds_read_b64_tr_b16 a[160:161], v245 offset:28672\n"
    "v_subrev_f32_e32 v93, s4, v93\n"
    "v_mfma_f32_16x16x32_bf16 v[76:79], a[168:171], v[172:175], v[76:79]\n"
    "ds_read_b64_tr_b16 a[162:163], v245 offset:30720\n"
    "v_subrev_f32_e32 v114, s4, v114\n"
    "v_mfma_f32_16x16x32_bf16 v[76:79], a[172:175], v[176:179], v[76:79]\n"
    "s_waitcnt lgkmcnt(5)\n"
    "v_subrev_f32_e32 v94, s4, v94\n"
    "v_subrev_f32_e32 v119, s4, v119\n"
    "v_mfma_f32_16x16x32_bf16 v[76:79], a[176:179], v[180:183], v[76:79]\n"
    "buffer_load_dwordx4 a[124:127], v250, s[8:11], s62 offen offset:1024 sc1\n"
    "v_mfma_f32_16x16x32_bf16 v[76:79], a[180:183], v[184:187], v[76:79]\n"
    "ds_read_b64_tr_b16 a[168:169], v245 offset:4096\n"
    "v_subrev_f32_e32 v104, s4, v104\n"
    "v_mfma_f32_16x16x32_bf16 v[72:75], a[180:183], v[152:155], v[72:75]\n"
    "ds_read_b64_tr_b16 a[170:171], v245 offset:6144\n"
    "v_cvt_pk_bf16_f32 v35, v54, v55\n"
    "v_mfma_f32_16x16x32_bf16 v[72:75], a[176:179], v[148:151], v[72:75]\n"
    "ds_read_b64_tr_b16 a[164:165], v245 offset:61440\n"
    "v_mov_b32_e32 v253, 0x3f80\n"
    "v_mfma_f32_16x16x32_bf16 v[68:71], a[176:179], v[140:143], v[68:71]\n"
    "ds_read_b64_tr_b16 a[166:167], v245 offset:63488\n"
    "v_subrev_f32_e32 v82, s4, v82\n"
    "v_mfma_f32_16x16x32_bf16 v[68:71], a[180:183], v[144:147], v[68:71]\n"
    "s_waitcnt lgkmcnt(6)\n"
    "ds_read_b64_tr_b16 a[172:173], v245 offset:36864\n"
    "v_subrev_f32_e32 v95, s4, v95\n"
    "v_mfma_f32_16x16x32_bf16 v[68:71], a[184:187], v[196:199], v[68:71]\n"
    "ds_read_b64_tr_b16 a[176:177], v245 offset:12288\n"
    "v_mov_b64_e32 v[232:233], 0\n"
    "v_mfma_f32_16x16x32_bf16 v[68:71], a[188:191], v[200:203], v[68:71]\n"
    "buffer_store_dwordx4 v[32:35], v250, s[8:11], s28 offen offset:1024 sc1\n"
    "v_mfma_f32_16x16x32_bf16 v[72:75], a[188:191], v[208:211], v[72:75]\n"
    "ds_read_b64_tr_b16 a[178:179], v245 offset:14336\n"
    "v_subrev_f32_e32 v129, s4, v129\n"
    "v_mfma_f32_16x16x32_bf16 v[72:75], a[184:187], v[204:207], v[72:75]\n"
    "ds_read_b64_tr_b16 a[174:175], v245 offset:38912\n"
    "v_cvt_pk_bf16_f32 v38, v56, v57\n"
    "v_mfma_f32_16x16x32_bf16 v[76:79], a[184:187], v[212:215], v[76:79]\n"
    "buffer_load_dwordx4 a[128:131], v250, s[8:11], s62 offen offset:2048 sc1\n"
    "v_mfma_f32_16x16x32_bf16 v[76:79], a[188:191], v[216:219], v[76:79]\n"
    "s_waitcnt lgkmcnt(4)\n"
    "ds_read_b64_tr_b16 a[180:181], v245 offset:45056\n"
    "v_subrev_f32_e32 v110, s4, v110\n"
    "v_mfma_f32_16x16x32_bf16 v[96:99], a[160:163], a[208:211], v[96:99]\n"
    "v_mov_b64_e32 v[236:237], 0\n"
    "v_subrev_f32_e32 v1, s4, v1\n"
    "v_mfma_f32_16x16x32_bf16 v[96:99], a[164:167], a[212:215], v[96:99]\n"
    "ds_read_b64_tr_b16 a[184:185], v245 offset:20480\n"
    "v_cvt_pk_bf16_f32 v39, v58, v59\n"
    "v_mfma_f32_16x16x32_bf16 v[92:95], a[164:167], a[204:207], v[92:95]\n"
    "ds_read_b64_tr_b16 a[186:187], v245 offset:22528\n"
    "v_subrev_f32_e32 v24, s4, v24\n"
    "v_mfma_f32_16x16x32_bf16 v[92:95], a[160:163], a[200:203], v[92:95]\n"
    "ds_read_b64_tr_b16 a[182:183], v245 offset:47104\n"
    "v_cmp_eq_u32_e32 vcc, 0, v252\n"
    "v_mfma_f32_16x16x32_bf16 v[88:91], a[160:163], a[192:195], v[88:91]\n"
    "ds_read_b64_tr_b16 a[188:189], v245 offset:53248\n"
    "v_cndmask_b32_e32 v234, 0, v253, vcc\n"
    "v_mfma_f32_16x16x32_bf16 v[88:91], a[164:167], a[196:199], v[88:91]\n"
    "s_waitcnt lgkmcnt(5)\n"
    "v_cvt_pk_bf16_f32 v72, v72, v73\n"
    "v_subrev_f32_e32 v45, s4, v45\n"
    "v_mfma_f32_16x16x32_bf16 v[88:91], a[168:171], v[156:159], v[88:91]\n"
    "ds_read_b64_tr_b16 a[160:161], v246 offset:28672\n"
    "v_mfma_f32_16x16x32_bf16 v[88:91], a[172:175], v[160:163], v[88:91]\n"
    "v_subrev_f32_e32 v136, s4, v136\n"
    "v_subrev_f32_e32 v121, s4, v121\n"
    "v_mfma_f32_16x16x32_bf16 v[92:95], a[172:175], v[168:171], v[92:95]\n"
    "buffer_store_dwordx4 v[36:39], v250, s[8:11], s29 offen offset:1024 sc1\n"
    "v_mfma_f32_16x16x32_bf16 v[92:95], a[168:171], v[164:167], v[92:95]\n"
    "ds_read_b64_tr_b16 a[162:163], v246 offset:30720\n"
    "v_subrev_f32_e32 v130, s4, v130\n"
    "v_mfma_f32_16x16x32_bf16 v[96:99], a[168:171], v[172:175], v[96:99]\n"
    "ds_read_b64_tr_b16 a[190:191], v245 offset:55296\n"
    "v_subrev_f32_e32 v122, s4, v122\n"
    "v_mfma_f32_16x16x32_bf16 v[96:99], a[172:175], v[176:179], v[96:99]\n"
    "s_waitcnt lgkmcnt(4)\n"
    "ds_read_b64_tr_b16 a[168:169], v246 offset:4096\n"
    "v_subrev_f32_e32 v137, s4, v137\n"
    "v_mfma_f32_16x16x32_bf16 v[96:99], a[176:179], v[180:183], v[96:99]\n"
    "buffer_load_dwordx4 a[132:135], v250, s[8:11], s62 offen offset:3072 sc1\n"
    "v_mfma_f32_16x16x32_bf16 v[96:99], a[180:183], v[184:187], v[96:99]\n"
    "s_add_u32 s62, s57, 24576\n"
    "ds_read_b64_tr_b16 a[170:171], v246 offset:6144\n"
    "v_subrev_f32_e32 v138, s4, v138\n"
    "v_mfma_f32_16x16x32_bf16 v[92:95], a[180:183], v[152:155], v[92:95]\n"
    "ds_read_b64_tr_b16 a[164:165], v246 offset:61440\n"
    "v_subrev_f32_e32 v84, s4, v84\n"
    "v_mfma_f32_16x16x32_bf16 v[92:95], a[176:179], v[148:151], v[92:95]\n"
    "v_cmp_eq_u32_e32 vcc, 1, v252\n"
    "v_cvt_pk_bf16_f32 v73, v74, v75\n"
    "v_mfma_f32_16x16x32_bf16 v[88:91], a[176:179], v[140:143], v[88:91]\n"
    "ds_read_b64_tr_b16 a[166:167], v246 offset:63488\n"
    "v_cvt_pk_bf16_f32 v68, v68, v69\n"
    "v_mfma_f32_16x16x32_bf16 v[88:91], a[180:183], v[144:147], v[88:91]\n"
    "s_waitcnt lgkmcnt(4)\n"
    "ds_read_b64_tr_b16 a[176:177], v246 offset:12288\n"
    "v_cndmask_b32_e64 v234, v234, 1.0, vcc\n"
    "v_mfma_f32_16x16x32_bf16 v[88:91], a[184:187], v[196:199], v[88:91]\n"
    "ds_read_b64_tr_b16 a[172:173], v246 offset:36864\n"
    "v_subrev_f32_e32 v115, s4, v115\n"
    "v_mfma_f32_16x16x32_bf16 v[88:91], a[188:191], v[200:203], v[88:91]\n"
    "ds_read_b64_tr_b16 a[174:175], v246 offset:38912\n"
    "v_cvt_pk_bf16_f32 v69, v70, v71\n"
    "v_mfma_f32_16x16x32_bf16 v[92:95], a[188:191], v[208:211], v[92:95]\n"
    "buffer_load_dwordx4 a[136:139], v250, s[8:11], s62 offen offset:0 sc1\n"
    "v_mfma_f32_16x16x32_bf16 v[92:95], a[184:187], v[204:207], v[92:95]\n"
    "ds_read_b64_tr_b16 a[178:179], v246 offset:14336\n"
    "v_subrev_f32_e32 v5, s4, v5\n"
    "v_mfma_f32_16x16x32_bf16 v[96:99], a[184:187], v[212:215], v[96:99]\n"
    "v_cvt_pk_bf16_f32 v70, v88, v89\n"
    "v_cvt_pk_bf16_f32 v71, v90, v91\n"
    "v_mfma_f32_16x16x32_bf16 v[96:99], a[188:191], v[216:219], v[96:99]\n"
    "s_waitcnt lgkmcnt(4)\n"
    "ds_read_b64_tr_b16 a[180:181], v246 offset:45056\n"
    "v_cvt_pk_bf16_f32 v76, v76, v77\n"
    "v_mfma_f32_16x16x32_bf16 v[116:119], a[160:163], a[208:211], v[116:119]\n"
    "buffer_store_dwordx4 v[68:71], v250, s[8:11], s82 offen offset:2048 sc1\n"
    "v_mfma_f32_16x16x32_bf16 v[116:119], a[164:167], a[212:215], v[116:119]\n"
    "ds_read_b64_tr_b16 a[182:183], v246 offset:47104\n"
    "v_subrev_f32_e32 v111, s4, v111\n"
    "v_mfma_f32_16x16x32_bf16 v[112:115], a[164:167], a[204:207], v[112:115]\n"
    "ds_read_b64_tr_b16 a[184:185], v246 offset:20480\n"
    "v_subrev_f32_e32 v139, s4, v139\n"
    "v_mfma_f32_16x16x32_bf16 v[112:115], a[160:163], a[200:203], v[112:115]\n"
    "ds_read_b64_tr_b16 a[186:187], v246 offset:22528\n"
    "v_subrev_f32_e32 v134, s4, v134\n"
    "v_mfma_f32_16x16x32_bf16 v[108:111], a[160:163], a[192:195], v[108:111]\n"
    "buffer_load_dwordx4 a[140:143], v250, s[8:11], s62 offen offset:1024 sc1\n"
    "v_mfma_f32_16x16x32_bf16 v[108:111], a[164:167], a[196:199], v[108:111]\n"
    "s_waitcnt lgkmcnt(5)\n"
    "ds_read_b64_tr_b16 a[160:161], v247 offset:28672\n"
    "v_cmp_eq_u32_e32 vcc, 2, v252\n"
    "v_mfma_f32_16x16x32_bf16 v[108:111], a[168:171], v[156:159], v[108:111]\n"
    "v_cvt_pk_bf16_f32 v74, v92, v93\n"
    "v_subrev_f32_e32 v20, s4, v20\n"
    "v_mfma_f32_16x16x32_bf16 v[108:111], a[172:175], v[160:163], v[108:111]\n"
    "ds_read_b64_tr_b16 a[188:189], v246 offset:53248\n"
    "v_subrev_f32_e32 v25, s4, v25\n"
    "v_mfma_f32_16x16x32_bf16 v[112:115], a[172:175], v[168:171], v[112:115]\n"
    "ds_read_b64_tr_b16 a[162:163], v247 offset:30720\n"
    "v_subrev_f32_e32 v100, s4, v100\n"
    "v_mfma_f32_16x16x32_bf16 v[112:115], a[168:171], v[164:167], v[112:115]\n"
    "ds_read_b64_tr_b16 a[164:165], v247 offset:61440\n"
    "v_cndmask_b32_e32 v235, 0, v253, vcc\n"
    "v_mfma_f32_16x16x32_bf16 v[116:119], a[168:171], v[172:175], v[116:119]\n"
    "ds_read_b64_tr_b16 a[190:191], v246 offset:55296\n"
    "v_cvt_pk_bf16_f32 v77, v78, v79\n"
    "v_mfma_f32_16x16x32_bf16 v[116:119], a[172:175], v[176:179], v[116:119]\n"
    "s_waitcnt lgkmcnt(7)\n"
    "ds_read_b64_tr_b16 a[168:169], v247 offset:4096\n"
    "v_cvt_pk_bf16_f32 v75, v94, v95\n"
    "v_mfma_f32_16x16x32_bf16 v[116:119], a[176:179], v[180:183], v[116:119]\n"
    "ds_read_b64_tr_b16 a[170:171], v247 offset:6144\n"
    "v_subrev_f32_e32 v135, s4, v135\n"
    "v_mfma_f32_16x16x32_bf16 v[116:119], a[180:183], v[184:187], v[116:119]\n"
    "buffer_store_dwordx4 v[72:75], v250, s[8:11], s28 offen offset:2048 sc1\n"
    "v_mfma_f32_16x16x32_bf16 v[112:115], a[180:183], v[152:155], v[112:115]\n"
    "v_cvt_pk_bf16_f32 v78, v96, v97\n"
    "v_subrev_f32_e32 v6, s4, v6\n"
    "v_mfma_f32_16x16x32_bf16 v[112:115], a[176:179], v[148:151], v[112:115]\n"
    "ds_read_b64_tr_b16 a[166:167], v247 offset:63488\n"
    "v_subrev_f32_e32 v85, s4, v85\n"
    "v_mfma_f32_16x16x32_bf16 v[108:111], a[176:179], v[140:143], v[108:111]\n"
    "buffer_load_dwordx4 a[144:147], v250, s[8:11], s62 offen offset:2048 sc1\n"
    "v_mfma_f32_16x16x32_bf16 v[108:111], a[180:183], v[144:147], v[108:111]\n"
    "s_waitcnt lgkmcnt(3)\n"
    "ds_read_b64_tr_b16 a[172:173], v247 offset:36864\n"
    "v_cmp_eq_u32_e32 vcc, 3, v252\n"
    "v_mfma_f32_16x16x32_bf16 v[108:111], a[184:187], v[196:199], v[108:111]\n"
    "ds_read_b64_tr_b16 a[174:175], v247 offset:38912\n"
    "v_subrev_f32_e32 v83, s4, v83\n"
    "v_mfma_f32_16x16x32_bf16 v[108:111], a[188:191], v[200:203], v[108:111]\n"
    "ds_read_b64_tr_b16 a[176:177], v247 offset:12288\n"
    "v_cndmask_b32_e64 v235, v235, 1.0, vcc\n"
    "v_mfma_f32_16x16x32_bf16 v[112:115], a[188:191], v[208:211], v[112:115]\n"
    "v_subrev_f32_e32 v60, s4, v60\n"
    "v_subrev_f32_e32 v124, s4, v124\n"
    "v_mfma_f32_16x16x32_bf16 v[112:115], a[184:187], v[204:207], v[112:115]\n"
    "v_subrev_f32_e32 v131, s4, v131\n"
    "v_subrev_f32_e32 v123, s4, v123\n"
    "v_mfma_f32_16x16x32_bf16 v[116:119], a[184:187], v[212:215], v[116:119]\n"
    "ds_read_b64_tr_b16 a[178:179], v247 offset:14336\n"
    "v_subrev_f32_e32 v64, s4, v64\n"
    "v_mfma_f32_16x16x32_bf16 v[116:119], a[188:191], v[216:219], v[116:119]\n"
    "s_waitcnt lgkmcnt(4)\n"
    "v_cvt_pk_bf16_f32 v79, v98, v99\n"
    "v_cvt_pk_bf16_f32 v108, v108, v109\n"
    "v_mfma_f32_16x16x32_bf16 v[136:139], a[160:163], a[208:211], v[136:139]\n"
    "s_cmp_lg_u32 s44, 0\n"
    "s_cbranch_scc1 .Lat_glate\n"
    ".Lat_glate_ret:\n"
    "v_mfma_f32_16x16x32_bf16 v[136:139], a[164:167], a[212:215], v[136:139]\n"
    "ds_read_b64_tr_b16 a[180:181], v247 offset:45056\n"
    "v_mfma_f32_16x16x32_bf16 v[132:135], a[164:167], a[204:207], v[132:135]\n"
    "buffer_store_dwordx4 v[76:79], v250, s[8:11], s29 offen offset:2048 sc1\n"
    "v_mfma_f32_16x16x32_bf16 v[132:135], a[160:163], a[200:203], v[132:135]\n"
    "ds_read_b64_tr_b16 a[184:185], v247 offset:20480\n"
    "v_subrev_f32_e32 v65, s4, v65\n"
    "v_mfma_f32_16x16x32_bf16 v[128:131], a[160:163], a[192:195], v[128:131]\n"
    "ds_read_b64_tr_b16 a[182:183], v247 offset:47104\n"
    "v_mfma_f32_16x16x32_bf16 v[128:131], a[164:167], a[196:199], v[128:131]\n"
    "s_waitcnt lgkmcnt(5)\n"
    "ds_read_b64_tr_b16 a[186:187], v247 offset:22528\n"
    "v_subrev_f32_e32 v105, s4, v105\n"
    "v_mfma_f32_16x16x32_bf16 v[128:131], a[168:171], v[156:159], v[128:131]\n"
    "buffer_load_dwordx4 a[148:151], v250, s[8:11], s62 offen offset:3072 sc1\n"
    "v_mfma_f32_16x16x32_bf16 v[128:131], a[172:175], v[160:163], v[128:131]\n"
    "s_add_u32 s62, s57, 28672\n"
    "v_subrev_f32_e32 v106, s4, v106\n"
    "v_subrev_f32_e32 v101, s4, v101\n"
    "v_mfma_f32_16x16x32_bf16 v[132:135], a[172:175], v[168:171], v[132:135]\n"
    "ds_read_b64_tr_b16 a[188:189], v247 offset:53248\n"
    "v_subrev_f32_e32 v42, s4, v42\n"
    "v_mfma_f32_16x16x32_bf16 v[132:135], a[168:171], v[164:167], v[132:135]\n"
    "ds_read_b64_tr_b16 a[190:191], v247 offset:55296\n"
    "v_cvt_pk_bf16_f32 v112, v112, v113\n"
    "v_mfma_f32_16x16x32_bf16 v[136:139], a[168:171], v[172:175], v[136:139]\n"
    "ds_read_b128 a[160:163], v240 offset:24576\n"
    "v_subrev_f32_e32 v86, s4, v86\n"
    "v_mfma_f32_16x16x32_bf16 v[136:139], a[172:175], v[176:179], v[136:139]\n"
    "s_waitcnt lgkmcnt(4)\n"
    "v_subrev_f32_e32 v26, s4, v26\n"
    "v_mfma_f32_16x16x32_bf16 v[136:139], a[176:179], v[180:183], v[136:139]\n"
    "ds_read_b128 a[164:167], v241 offset:24576\n"
    "v_subrev_f32_e32 v107, s4, v107\n"
    "v_mfma_f32_16x16x32_bf16 v[136:139], a[180:183], v[184:187], v[136:139]\n"
    "v_subrev_f32_e32 v43, s4, v43\n"
    "v_subrev_f32_e32 v102, s4, v102\n"
    "v_mfma_f32_16x16x32_bf16 v[132:135], a[180:183], v[152:155], v[132:135]\n"
    "buffer_load_dwordx4 a[152:155], v250, s[8:11], s62 offen offset:0 sc1\n"
    "v_mfma_f32_16x16x32_bf16 v[132:135], a[176:179], v[148:151], v[132:135]\n"
    "v_subrev_f32_e32 v21, s4, v21\n"
    "v_subrev_f32_e32 v103, s4, v103\n"
    "v_mfma_f32_16x16x32_bf16 v[128:131], a[176:179], v[140:143], v[128:131]\n"
    "v_subrev_f32_e32 v2, s4, v2\n"
    "v_subrev_f32_e32 v61, s4, v61\n"
    "v_mfma_f32_16x16x32_bf16 v[128:131], a[180:183], v[144:147], v[128:131]\n"
    "s_waitcnt lgkmcnt(2)\n"
    "ds_read_b128 a[168:171], v240 offset:28672\n"
    "v_cvt_pk_bf16_f32 v116, v116, v117\n"
    "v_mfma_f32_16x16x32_bf16 v[128:131], a[184:187], v[196:199], v[128:131]\n"
    "v_cvt_pk_bf16_f32 v109, v110, v111\n"
    "v_subrev_f32_e32 v27, s4, v27\n"
    "v_mfma_f32_16x16x32_bf16 v[128:131], a[188:191], v[200:203], v[128:131]\n"
    "ds_read_b128 a[176:179], v242 offset:24576\n"
    "v_cvt_pk_bf16_f32 v117, v118, v119\n"
    "v_mfma_f32_16x16x32_bf16 v[132:135], a[188:191], v[208:211], v[132:135]\n"
    "ds_read_b128 a[172:175], v241 offset:28672\n"
    "v_subrev_f32_e32 v125, s4, v125\n"
    "v_mfma_f32_16x16x32_bf16 v[132:135], a[184:187], v[204:207], v[132:135]\n"
    "v_cvt_pk_bf16_f32 v110, v128, v129\n"
    "v_subrev_f32_e32 v87, s4, v87\n"
    "v_mfma_f32_16x16x32_bf16 v[136:139], a[184:187], v[212:215], v[136:139]\n"
    "ds_read_b128 a[180:183], v243 offset:24576\n"
    "v_cvt_pk_bf16_f32 v111, v130, v131\n"
    "v_mfma_f32_16x16x32_bf16 v[136:139], a[188:191], v[216:219], v[136:139]\n"
    "s_waitcnt lgkmcnt(4)\n"
    "ds_read_b128 a[184:187], v242 offset:28672\n"
    "v_subrev_f32_e32 v3, s4, v3\n"
    "v_mfma_f32_16x16x32_bf16 v[164:167], a[160:163], a[32:35], v[224:227]\n"
    "buffer_store_dwordx4 v[108:111], v250, s[8:11], s82 offen offset:3072 sc1\n"
    "v_mfma_f32_16x16x32_bf16 v[164:167], a[164:167], a[36:39], v[164:167]\n"
    "v_cvt_pk_bf16_f32 v113, v114, v115\n"
    "v_subrev_f32_e32 v46, s4, v46\n"
    "v_mfma_f32_16x16x32_bf16 v[156:159], a[164:167], a[4:7], v[220:223]\n"
    "ds_read_b128 a[188:191], v243 offset:28672\n"
    "v_mfma_f32_16x16x32_bf16 v[156:159], a[160:163], a[0:3], v[156:159]\n"
    "s_waitcnt lgkmcnt(3)\n"
    "v_subrev_f32_e32 v126, s4, v126\n"
    "v_mfma_f32_16x16x32_bf16 v[156:159], a[168:171], a[8:11], v[156:159]\n"
    "buffer_store_dword v131, v231, s[8:11], s87 offen offset:0 sc1\n"
    "v_mfma_f32_16x16x32_bf16 v[156:159], a[172:175], a[12:15], v[156:159]\n"
    "v_cvt_pk_bf16_f32 v114, v132, v133\n"
    "v_cvt_pk_bf16_f32 v118, v136, v137\n"
    "v_mfma_f32_16x16x32_bf16 v[164:167], a[172:175], a[44:47], v[164:167]\n"
    "ds_read_b128 a[160:163], v240 offset:26624\n"
    "v_cvt_pk_bf16_f32 v119, v138, v139\n"
    "v_mfma_f32_16x16x32_bf16 v[164:167], a[168:171], a[40:43], v[164:167]\n"
    "s_waitcnt lgkmcnt(3)\n"
    "ds_read_b128 a[164:167], v241 offset:26624\n"
    "v_subrev_f32_e32 v66, s4, v66\n"
    "v_mfma_f32_16x16x32_bf16 v[164:167], a[176:179], a[48:51], v[164:167]\n"
    "ds_read_b128 a[168:171], v240 offset:30720\n"
    "v_subrev_f32_e32 v67, s4, v67\n"
    "v_mfma_f32_16x16x32_bf16 v[164:167], a[180:183], a[52:55], v[164:167]\n"
    "buffer_store_dword v135, v231, s[8:11], s88 offen offset:0 sc1\n"
    "v_mfma_f32_16x16x32_bf16 v[156:159], a[180:183], a[20:23], v[156:159]\n"
    "v_cvt_pk_bf16_f32 v115, v134, v135\n"
    "v_subrev_f32_e32 v62, s4, v62\n"
    "v_mfma_f32_16x16x32_bf16 v[156:159], a[176:179], a[16:19], v[156:159]\n"
    "s_waitcnt lgkmcnt(3)\n"
    "ds_read_b128 a[172:175], v241 offset:30720\n"
    "v_subrev_f32_e32 v127, s4, v127\n"
    "v_mfma_f32_16x16x32_bf16 v[156:159], a[184:187], a[24:27], v[156:159]\n"
    "ds_read_b128 a[176:179], v242 offset:26624\n"
    "v_subrev_f32_e32 v22, s4, v22\n"
    "v_mfma_f32_16x16x32_bf16 v[156:159], a[188:191], a[28:31], v[156:159]\n"
    "buffer_store_dwordx4 v[112:115], v250, s[8:11], s28 offen offset:3072 sc1\n"
    "v_mfma_f32_16x16x32_bf16 v[164:167], a[188:191], a[60:63], v[164:167]\n"
    "ds_read_b128 a[180:183], v243 offset:26624\n"
    "v_subrev_f32_e32 v47, s4, v47\n"
    "v_mfma_f32_16x16x32_bf16 v[164:167], a[184:187], a[56:59], v[164:167]\n"
    "s_waitcnt lgkmcnt(4)\n"
    "ds_read_b128 a[184:187], v242 offset:30720\n"
    "ds_read_b128 a[188:191], v243 offset:30720\n"
    "v_mfma_f32_16x16x32_bf16 v[168:171], a[160:163], a[32:35], v[224:227]\n"
    "buffer_store_dword v139, v231, s[8:11], s89 offen offset:0 sc1\n"
    "v_mfma_f32_16x16x32_bf16 v[168:171], a[164:167], a[36:39], v[168:171]\n"
    "v_exp_f32_e32 v156, v156\n"
    "v_mfma_f32_16x16x32_bf16 v[160:163], a[164:167], a[4:7], v[220:223]\n"
    "v_exp_f32_e32 v157, v157\n"
    "v_mfma_f32_16x16x32_bf16 v[160:163], a[160:163], a[0:3], v[160:163]\n"
    "s_waitcnt lgkmcnt(4)\n"
    "ds_read_b128 a[160:163], v240 offset:0\n"
    "v_add_f32_e32 v128, v131, v135\n"
    "v_mfma_f32_16x16x32_bf16 v[160:163], a[168:171], a[8:11], v[160:163]\n"
    "buffer_load_dwordx4 a[156:159], v250, s[8:11], s62 offen offset:1024 sc1\n"
    "v_mfma_f32_16x16x32_bf16 v[160:163], a[172:175], a[12:15], v[160:163]\n"
    "s_add_u32 s62, s58, 0\n"
    "ds_read_b128 a[164:167], v241 offset:0\n"
    "v_min3_f32 v129, v131, v135, v139\n"
    "v_mfma_f32_16x16x32_bf16 v[168:171], a[172:175], a[44:47], v[168:171]\n"
    "v_exp_f32_e32 v158, v158\n"
    "v_mfma_f32_16x16x32_bf16 v[168:171], a[168:171], a[40:43], v[168:171]\n"
    "s_waitcnt lgkmcnt(4)\n"
    "ds_read_b128 a[168:171], v240 offset:4096\n"
    "v_add_f32_e32 v128, v128, v139\n"
    "v_mfma_f32_16x16x32_bf16 v[168:171], a[176:179], a[48:51], v[168:171]\n"
    "v_exp_f32_e32 v159, v159\n"
    "v_mfma_f32_16x16x32_bf16 v[168:171], a[180:183], a[52:55], v[168:171]\n"
    "ds_read_b128 a[172:175], v241 offset:4096\n"
    "v_cvt_pk_bf16_f32 v156, v156, v157\n"
    "v_mfma_f32_16x16x32_bf16 v[160:163], a[180:183], a[20:23], v[160:163]\n"
    "buffer_store_dwordx4 v[116:119], v250, s[8:11], s29 offen offset:3072 sc1\n"
    "v_mfma_f32_16x16x32_bf16 v[160:163], a[176:179], a[16:19], v[160:163]\n"
    "s_waitcnt lgkmcnt(4)\n"
    "v_exp_f32_e32 v164, v164\n"
    "v_mfma_f32_16x16x32_bf16 v[160:163], a[184:187], a[24:27], v[160:163]\n"
    "ds_write_addtid_b32 v156 offset:0\n"
    "v_exp_f32_e32 v165, v165\n"
    "v_mfma_f32_16x16x32_bf16 v[160:163], a[188:191], a[28:31], v[160:163]\n"
    "buffer_load_dwordx4 a[244:247], v250, s[8:11], s62 offen offset:0 sc1\n"
    "ds_read_b128 a[176:179], v242 offset:0\n"
    "v_mfma_f32_16x16x32_bf16 v[168:171], a[188:191], a[60:63], v[168:171]\n"
    "ds_read_b128 a[180:183], v243 offset:0\n"
    "v_cvt_pk_bf16_f32 v157, v158, v159\n"
    "v_mfma_f32_16x16x32_bf16 v[168:171], a[184:187], a[56:59], v[168:171]\n"
    "s_waitcnt lgkmcnt(5)\n"
    "v_exp_f32_e32 v166, v166\n"
    "v_mfma_f32_16x16x32_bf16 v[204:207], a[160:163], a[32:35], v[224:227]\n"
    "v_exp_f32_e32 v167, v167\n"
    "v_mfma_f32_16x16x32_bf16 v[204:207], a[164:167], a[36:39], v[204:207]\n"
    "ds_read_b128 a[184:187], v242 offset:4096\n"
    "v_cvt_pk_bf16_f32 v164, v164, v165\n"
    "v_mfma_f32_16x16x32_bf16 v[196:199], a[164:167], a[4:7], v[220:223]\n"
    "v_exp_f32_e32 v160, v160\n"
    "v_mfma_f32_16x16x32_bf16 v[196:199], a[160:163], a[0:3], v[196:199]\n"
    "s_waitcnt lgkmcnt(4)\n"
    "ds_read_b128 a[160:163], v240 offset:2048\n"
    "v_exp_f32_e32 v161, v161\n"
    "v_mfma_f32_16x16x32_bf16 v[196:199], a[168:171], a[8:11], v[196:199]\n"
    "ds_read_b128 a[188:191], v243 offset:4096\n"
    "ds_write_addtid_b32 v157 offset:256\n"
    "v_mfma_f32_16x16x32_bf16 v[196:199], a[172:175], a[12:15], v[196:199]\n"
    "buffer_load_dwordx4 v[8:11], v250, s[8:11], s62 offen offset:1024 sc1\n"
    "v_mfma_f32_16x16x32_bf16 v[204:207], a[172:175], a[44:47], v[204:207]\n"
    "ds_read_b128 a[164:167], v241 offset:2048\n"
    "v_cvt_pk_bf16_f32 v165, v166, v167\n"
    "v_mfma_f32_16x16x32_bf16 v[204:207], a[168:171], a[40:43], v[204:207]\n"
    "s_waitcnt lgkmcnt(5)\n"
    "v_exp_f32_e32 v162, v162\n"
    "v_mfma_f32_16x16x32_bf16 v[204:207], a[176:179], a[48:51], v[204:207]\n"
    "v_exp_f32_e32 v163, v163\n"
    "v_mfma_f32_16x16x32_bf16 v[204:207], a[180:183], a[52:55], v[204:207]\n"
    "ds_read_b128 a[168:171], v240 offset:6144\n"
    "ds_write_addtid_b32 v164 offset:1024\n"
    "v_mfma_f32_16x16x32_bf16 v[196:199], a[180:183], a[20:23], v[196:199]\n"
    "ds_read_b128 a[172:175], v241 offset:6144\n"
    "v_cvt_pk_bf16_f32 v158, v160, v161\n"
    "v_mfma_f32_16x16x32_bf16 v[196:199], a[176:179], a[16:19], v[196:199]\n"
    "s_waitcnt lgkmcnt(5)\n"
    "v_exp_f32_e32 v168, v168\n"
    "v_mfma_f32_16x16x32_bf16 v[196:199], a[184:187], a[24:27], v[196:199]\n"
    "v_exp_f32_e32 v169, v169\n"
    "v_mfma_f32_16x16x32_bf16 v[196:199], a[188:191], a[28:31], v[196:199]\n"
    "buffer_load_dwordx4 a[248:251], v250, s[8:11], s62 offen offset:2048 sc1\n"
    "v_mfma_f32_16x16x32_bf16 v[204:207], a[188:191], a[60:63], v[204:207]\n"
    "ds_read_b128 a[176:179], v242 offset:2048\n"
    "v_cvt_pk_bf16_f32 v159, v162, v163\n"
    "v_mfma_f32_16x16x32_bf16 v[204:207], a[184:187], a[56:59], v[204:207]\n"
    "s_waitcnt lgkmcnt(4)\n"
    "ds_read_b128 a[180:183], v243 offset:2048\n"
    "v_exp_f32_e32 v170, v170\n"
    "v_mfma_f32_16x16x32_bf16 v[208:211], a[160:163], a[32:35], v[224:227]\n"
    "ds_read_b128 a[184:187], v242 offset:6144\n"
    "ds_write_addtid_b32 v165 offset:1280\n"
    "v_mfma_f32_16x16x32_bf16 v[208:211], a[164:167], a[36:39], v[208:211]\n"
    "ds_read_b128 a[188:191], v243 offset:6144\n"
    "v_exp_f32_e32 v196, v196\n"
    "v_mfma_f32_16x16x32_bf16 v[200:203], a[164:167], a[4:7], v[220:223]\n"
    "v_exp_f32_e32 v171, v171\n"
    "v_mfma_f32_16x16x32_bf16 v[200:203], a[160:163], a[0:3], v[200:203]\n"
    "s_waitcnt lgkmcnt(5)\n"
    "v_exp_f32_e32 v197, v197\n"
    "v_mfma_f32_16x16x32_bf16 v[200:203], a[168:171], a[8:11], v[200:203]\n"
    "v_exp_f32_e32 v198, v198\n"
    "v_mfma_f32_16x16x32_bf16 v[200:203], a[172:175], a[12:15], v[200:203]\n"
    "buffer_load_dwordx4 v[12:15], v250, s[8:11], s62 offen offset:3072 sc1\n"
    "ds_read_b128 a[160:163], v240 offset:8192\n"
    "v_mfma_f32_16x16x32_bf16 v[208:211], a[172:175], a[44:47], v[208:211]\n"
    "s_add_u32 s62, s58, 4096\n"
    "v_exp_f32_e32 v199, v199\n"
    "v_mfma_f32_16x16x32_bf16 v[208:211], a[168:171], a[40:43], v[208:211]\n"
    "s_waitcnt lgkmcnt(4)\n"
    "ds_read_b128 a[164:167], v241 offset:8192\n"
    "v_cvt_pk_bf16_f32 v196, v196, v197\n"
    "v_mfma_f32_16x16x32_bf16 v[208:211], a[176:179], a[48:51], v[208:211]\n"
    "ds_read_b128 a[168:171], v240 offset:12288\n"
    "v_exp_f32_e32 v204, v204\n"
    "v_mfma_f32_16x16x32_bf16 v[208:211], a[180:183], a[52:55], v[208:211]\n"
    "v_exp_f32_e32 v205, v205\n"
    "v_mfma_f32_16x16x32_bf16 v[200:203], a[180:183], a[20:23], v[200:203]\n"
    "v_cvt_pk_bf16_f32 v197, v198, v199\n"
    "v_exp_f32_e32 v206, v206\n"
    "v_mfma_f32_16x16x32_bf16 v[200:203], a[176:179], a[16:19], v[200:203]\n"
    "s_waitcnt lgkmcnt(3)\n"
    "v_exp_f32_e32 v207, v207\n"
    "v_mfma_f32_16x16x32_bf16 v[200:203], a[184:187], a[24:27], v[200:203]\n"
    "ds_read_b128 a[172:175], v241 offset:12288\n"
    "v_cvt_pk_bf16_f32 v204, v204, v205\n"
    "v_mfma_f32_16x16x32_bf16 v[200:203], a[188:191], a[28:31], v[200:203]\n"
    "buffer_load_dwordx4 a[252:255], v250, s[8:11], s62 offen offset:0 sc1\n"
    "v_mfma_f32_16x16x32_bf16 v[208:211], a[188:191], a[60:63], v[208:211]\n"
    "ds_read_b128 a[176:179], v242 offset:8192\n"
    "v_cvt_pk_bf16_f32 v166, v168, v169\n"
    "v_mfma_f32_16x16x32_bf16 v[208:211], a[184:187], a[56:59], v[208:211]\n"
    "s_waitcnt lgkmcnt(3)\n"
    "v_exp_f32_e32 v200, v200\n"
    "v_mfma_f32_16x16x32_bf16 v[212:215], a[160:163], a[32:35], v[224:227]\n"
    "ds_read_b128 a[180:183], v243 offset:8192\n"
    "v_exp_f32_e32 v201, v201\n"
    "v_mfma_f32_16x16x32_bf16 v[212:215], a[164:167], a[36:39], v[212:215]\n"
    "v_cvt_pk_bf16_f32 v205, v206, v207\n"
    "v_exp_f32_e32 v202, v202\n"
    "v_mfma_f32_16x16x32_bf16 v[172:175], a[164:167], a[4:7], v[220:223]\n"
    "v_exp_f32_e32 v203, v203\n"
    "v_mfma_f32_16x16x32_bf16 v[172:175], a[160:163], a[0:3], v[172:175]\n"
    "s_waitcnt lgkmcnt(2)\n"
    "ds_read_b128 a[184:187], v242 offset:12288\n"
    "v_cvt_pk_bf16_f32 v198, v200, v201\n"
    "v_mfma_f32_16x16x32_bf16 v[172:175], a[168:171], a[8:11], v[172:175]\n"
    "ds_read_b128 a[188:191], v243 offset:12288\n"
    "v_exp_f32_e32 v208, v208\n"
    "v_mfma_f32_16x16x32_bf16 v[172:175], a[172:175], a[12:15], v[172:175]\n"
    "buffer_load_dwordx4 v[16:19], v250, s[8:11], s62 offen offset:1024 sc1\n"
    "v_mfma_f32_16x16x32_bf16 v[212:215], a[172:175], a[44:47], v[212:215]\n"
    "s_add_u32 s62, s57, 28672\n"
    "ds_read_b128 a[160:163], v240 offset:10240\n"
    "v_subrev_f32_e32 v23, s4, v23\n"
    "v_mfma_f32_16x16x32_bf16 v[212:215], a[168:171], a[40:43], v[212:215]\n"
    "s_waitcnt lgkmcnt(3)\n"
    "v_exp_f32_e32 v209, v209\n"
    "v_mfma_f32_16x16x32_bf16 v[212:215], a[176:179], a[48:51], v[212:215]\n"
    "ds_read_b128 a[164:167], v241 offset:10240\n"
    "ds_read_b128 a[168:171], v240 offset:14336\n"
    "v_mfma_f32_16x16x32_bf16 v[212:215], a[180:183], a[52:55], v[212:215]\n"
    "ds_write_addtid_b32 v158 offset:512\n"
    "ds_write_addtid_b32 v159 offset:768\n"
    "v_mfma_f32_16x16x32_bf16 v[172:175], a[180:183], a[20:23], v[172:175]\n"
    "ds_write_addtid_b32 v166 offset:1536\n"
    "v_cvt_pk_bf16_f32 v167, v170, v171\n"
    "v_mfma_f32_16x16x32_bf16 v[172:175], a[176:179], a[16:19], v[172:175]\n"
    "s_waitcnt lgkmcnt(6)\n"
    "ds_read_b128 a[172:175], v241 offset:14336\n"
    "ds_read_b128 a[176:179], v242 offset:10240\n"
    "v_mfma_f32_16x16x32_bf16 v[172:175], a[184:187], a[24:27], v[172:175]\n"
    "ds_read_b128 a[180:183], v243 offset:10240\n"
    "v_cvt_pk_bf16_f32 v199, v202, v203\n"
    "v_mfma_f32_16x16x32_bf16 v[172:175], a[188:191], a[28:31], v[172:175]\n"
    "buffer_load_dwordx4 v[180:183], v250, s[8:11], s62 offen offset:2048 sc1\n"
    "v_mfma_f32_16x16x32_bf16 v[212:215], a[188:191], a[60:63], v[212:215]\n"
    "ds_write_addtid_b32 v167 offset:1792\n"
    "v_subrev_f32_e32 v63, s4, v63\n"
    "v_mfma_f32_16x16x32_bf16 v[212:215], a[184:187], a[56:59], v[212:215]\n"
    "s_waitcnt lgkmcnt(8)\n"
    "ds_read_b128 a[184:187], v242 offset:14336\n"
    "v_exp_f32_e32 v210, v210\n"
    "v_mfma_f32_16x16x32_bf16 v[216:219], a[160:163], a[32:35], v[224:227]\n"
    "ds_read_b128 a[188:191], v243 offset:14336\n"
    "v_exp_f32_e32 v172, v172\n"
    "v_mfma_f32_16x16x32_bf16 v[216:219], a[164:167], a[36:39], v[216:219]\n"
    "s_waitcnt lgkmcnt(2)\n"
    "v_mfma_f32_16x16x32_bf16 v[176:179], a[164:167], a[4:7], v[220:223]\n"
    "s_barrier\n"
    "v_mfma_f32_16x16x32_bf16 v[176:179], a[160:163], a[0:3], v[176:179]\n"
    "s_waitcnt vmcnt(7)\n"
    "buffer_store_dword v254, v231, s[8:11], s38 offen offset:0 sc1\n"
    "v_mfma_f32_16x16x32_bf16 v[176:179], a[168:171], a[8:11], v[176:179]\n"
    "ds_read_b64_tr_b16 a[160:161], v244 offset:24576\n"
    "v_exp_f32_e32 v173, v173\n"
    "v_mfma_f32_16x16x32_bf16 v[176:179], a[172:175], a[12:15], v[176:179]\n"
    "ds_read_b64_tr_b16 a[162:163], v244 offset:26624\n"
    "ds_read2st64_b32 v[160:161], v249 offset0:0 offset1:1\n"
    "v_mfma_f32_16x16x32_bf16 v[216:219], a[172:175], a[44:47], v[216:219]\n"
    "buffer_load_dwordx4 v[184:187], v250, s[8:11], s62 offen offset:3072 sc1\n"
    "ds_read2st64_b32 v[162:163], v249 offset0:2 offset1:3\n"
    "v_mfma_f32_16x16x32_bf16 v[216:219], a[168:171], a[40:43], v[216:219]\n"
    "ds_read_b64_tr_b16 a[164:165], v244 offset:57344\n"
    "ds_read_b64_tr_b16 a[166:167], v244 offset:59392\n"
    "v_mfma_f32_16x16x32_bf16 v[216:219], a[176:179], a[48:51], v[216:219]\n"
    "ds_read2st64_b32 v[168:169], v249 offset0:4 offset1:5\n"
    "ds_read2st64_b32 v[170:171], v249 offset0:6 offset1:7\n"
    "v_mfma_f32_16x16x32_bf16 v[216:219], a[180:183], a[52:55], v[216:219]\n"
    "ds_write_addtid_b32 v196 offset:2048\n"
    "v_exp_f32_e32 v174, v174\n"
    "v_mfma_f32_16x16x32_bf16 v[176:179], a[180:183], a[20:23], v[176:179]\n"
    "s_add_u32 s62, s58, 4096\n"
    "ds_read_b64_tr_b16 a[168:169], v245 offset:24576\n"
    "v_exp_f32_e32 v175, v175\n"
    "v_mfma_f32_16x16x32_bf16 v[176:179], a[176:179], a[16:19], v[176:179]\n"
    "s_waitcnt lgkmcnt(10)\n"
    "ds_read_b64_tr_b16 a[170:171], v245 offset:26624\n"
    "ds_read_b64_tr_b16 a[172:173], v245 offset:57344\n"
    "v_mfma_f32_16x16x32_bf16 v[176:179], a[184:187], a[24:27], v[176:179]\n"
    "buffer_load_dwordx4 v[28:31], v250, s[8:11], s62 offen offset:2048 sc1\n"
    "ds_read_b64_tr_b16 a[174:175], v245 offset:59392\n"
    "v_mfma_f32_16x16x32_bf16 v[176:179], a[188:191], a[28:31], v[176:179]\n"
    "ds_read_b64_tr_b16 a[176:177], v246 offset:24576\n"
    "v_subrev_f32_e32 v7, s4, v7\n"
    "v_mfma_f32_16x16x32_bf16 v[216:219], a[188:191], a[60:63], v[216:219]\n"
    ".long 0xbedf00ff\n"
    ".Lat_w12:\n"
    "s_setpc_b64 s[2:3]\n"
    "ds_read_b64_tr_b16 a[178:179], v246 offset:26624\n"
    "v_cvt_pk_bf16_f32 v172, v172, v173\n"
    "v_mfma_f32_16x16x32_bf16 v[216:219], a[184:187], a[56:59], v[216:219]\n"
    "s_waitcnt lgkmcnt(7)\n"
    "ds_read_b64_tr_b16 a[180:181], v246 offset:57344\n"
    "ds_read_b64_tr_b16 a[182:183], v246 offset:59392\n"
    "v_mfma_f32_16x16x32_bf16 a[240:243], a[160:163], v[164:167], a[240:243]\n"
    "buffer_load_dwordx4 v[48:51], v250, s[8:11], s62 offen offset:3072 sc1\n"
    "ds_read_b64_tr_b16 a[184:185], v247 offset:24576\n"
    "v_mfma_f32_16x16x32_bf16 a[240:243], a[164:167], v[168:171], a[240:243]\n"
    "s_add_u32 s62, s58, 8192\n"
    "ds_read_b64_tr_b16 a[186:187], v247 offset:26624\n"
    "v_exp_f32_e32 v212, v212\n"
    "v_mfma_f32_16x16x32_bf16 a[236:239], a[164:167], v[160:163], a[236:239]\n"
    "ds_read_b64_tr_b16 a[188:189], v247 offset:57344\n"
    "v_exp_f32_e32 v213, v213\n"
    "v_mfma_f32_16x16x32_bf16 a[236:239], a[160:163], v[156:159], a[236:239]\n"
    "s_waitcnt lgkmcnt(7)\n"
    "ds_read_b64_tr_b16 a[160:161], v244 offset:28672\n"
    "v_cvt_pk_bf16_f32 v173, v174, v175\n"
    "v_mfma_f32_16x16x32_bf16 v[0:3], a[168:171], v[156:159], v[0:3]\n"
    "buffer_load_dwordx4 v[32:35], v250, s[8:11], s62 offen offset:0 sc1\n"
    "ds_read_b64_tr_b16 a[190:191], v247 offset:59392\n"
    "v_mfma_f32_16x16x32_bf16 v[0:3], a[172:175], v[160:163], v[0:3]\n"
    "ds_read_b64_tr_b16 a[162:163], v244 offset:30720\n"
    "v_exp_f32_e32 v214, v214\n"
    "v_mfma_f32_16x16x32_bf16 v[4:7], a[172:175], v[168:171], v[4:7]\n"
    "ds_read_b64_tr_b16 a[164:165], v244 offset:61440\n"
    "v_exp_f32_e32 v211, v211\n"
    "v_mfma_f32_16x16x32_bf16 v[4:7], a[168:171], v[164:167], v[4:7]\n"
    "s_waitcnt lgkmcnt(7)\n"
    "ds_read_b64_tr_b16 a[166:167], v244 offset:63488\n"
    "ds_read_b64_tr_b16 a[168:169], v245 offset:28672\n"
    "v_mfma_f32_16x16x32_bf16 v[24:27], a[176:179], v[164:167], v[24:27]\n"
    "buffer_load_dwordx4 v[52:55], v250, s[8:11], s62 offen offset:1024 sc1\n"
    "ds_read_b64_tr_b16 a[170:171], v245 offset:30720\n"
    "v_mfma_f32_16x16x32_bf16 v[24:27], a[180:183], v[168:171], v[24:27]\n"
    "ds_write_addtid_b32 v197 offset:2304\n"
    "v_exp_f32_e32 v215, v215\n"
    "v_mfma_f32_16x16x32_bf16 v[20:23], a[180:183], v[160:163], v[20:23]\n"
    "ds_read_b64_tr_b16 a[172:173], v245 offset:61440\n"
    "ds_write_addtid_b32 v172 offset:4096\n"
    "v_mfma_f32_16x16x32_bf16 v[20:23], a[176:179], v[156:159], v[20:23]\n"
    "s_waitcnt lgkmcnt(8)\n"
    "ds_read_b64_tr_b16 a[174:175], v245 offset:63488\n"
    "ds_read_b64_tr_b16 a[176:177], v246 offset:28672\n"
    "v_mfma_f32_16x16x32_bf16 v[40:43], a[184:187], v[156:159], v[40:43]\n"
    "buffer_load_dwordx4 v[36:39], v250, s[8:11], s62 offen offset:2048 sc1\n"
    "v_mfma_f32_16x16x32_bf16 v[40:43], a[188:191], v[160:163], v[40:43]\n"
    "ds_read_b64_tr_b16 a[178:179], v246 offset:30720\n"
    "v_cvt_pk_bf16_f32 v206, v208, v209\n"
    "v_mfma_f32_16x16x32_bf16 v[44:47], a[188:191], v[168:171], v[44:47]\n"
    "ds_read_b64_tr_b16 a[180:181], v246 offset:61440\n"
    "v_cvt_pk_bf16_f32 v212, v212, v213\n"
    "v_mfma_f32_16x16x32_bf16 v[44:47], a[184:187], v[164:167], v[44:47]\n"
    "s_waitcnt lgkmcnt(9)\n"
    "ds_read_b64_tr_b16 a[182:183], v246 offset:63488\n"
    "v_exp_f32_e32 v176, v176\n"
    "v_mfma_f32_16x16x32_bf16 v[64:67], a[160:163], v[164:167], v[64:67]\n"
    "buffer_load_dwordx4 v[56:59], v250, s[8:11], s62 offen offset:3072 sc1\n"
    "ds_read_b64_tr_b16 a[184:185], v247 offset:28672\n"
    "v_mfma_f32_16x16x32_bf16 v[64:67], a[164:167], v[168:171], v[64:67]\n"
    "s_add_u32 s62, s58, 12288\n"
    "v_exp_f32_e32 v177, v177\n"
    "v_mfma_f32_16x16x32_bf16 v[60:63], a[164:167], v[160:163], v[60:63]\n"
    "ds_read_b64_tr_b16 a[186:187], v247 offset:30720\n"
    "v_cvt_pk_bf16_f32 v213, v214, v215\n"
    "v_mfma_f32_16x16x32_bf16 v[60:63], a[160:163], v[156:159], v[60:63]\n"
    "s_waitcnt lgkmcnt(6)\n"
    "ds_read_b64_tr_b16 a[188:189], v247 offset:61440\n"
    "v_exp_f32_e32 v178, v178\n"
    "v_mfma_f32_16x16x32_bf16 v[80:83], a[168:171], v[156:159], v[80:83]\n"
    "buffer_load_dwordx4 v[68:71], v250, s[8:11], s62 offen offset:0 sc1\n"
    "ds_read_b64_tr_b16 a[190:191], v247 offset:63488\n"
    "v_mfma_f32_16x16x32_bf16 v[80:83], a[172:175], v[160:163], v[80:83]\n"
    "ds_write_addtid_b32 v204 offset:3072\n"
    "ds_write_addtid_b32 v205 offset:3328\n"
    "v_mfma_f32_16x16x32_bf16 v[84:87], a[172:175], v[168:171], v[84:87]\n"
    "ds_read_b128 a[160:163], v240 offset:16384\n"
    "ds_write_addtid_b32 v198 offset:2560\n"
    "v_mfma_f32_16x16x32_bf16 v[84:87], a[168:171], v[164:167], v[84:87]\n"
    "s_waitcnt lgkmcnt(8)\n"
    "ds_write_addtid_b32 v199 offset:2816\n"
    "v_exp_f32_e32 v179, v179\n"
    "v_mfma_f32_16x16x32_bf16 v[104:107], a[176:179], v[164:167], v[104:107]\n"
    "buffer_load_dwordx4 v[88:91], v250, s[8:11], s62 offen offset:1024 sc1\n"
    "ds_read_b128 a[164:167], v241 offset:16384\n"
    "v_mfma_f32_16x16x32_bf16 v[104:107], a[180:183], v[168:171], v[104:107]\n"
    "s_waitcnt lgkmcnt(6)\n"
    "v_mfma_f32_16x16x32_bf16 v[100:103], a[180:183], v[160:163], v[100:103]\n"
    "s_barrier\n"
    "v_mfma_f32_16x16x32_bf16 v[100:103], a[176:179], v[156:159], v[100:103]\n"
    "ds_read_b128 a[168:171], v240 offset:20480\n"
    "v_cvt_pk_bf16_f32 v174, v176, v177\n"
    "v_mfma_f32_16x16x32_bf16 v[120:123], a[184:187], v[156:159], v[120:123]\n"
    "s_add_u32 s31, s47, s35\n"
    "ds_read_b128 a[172:175], v241 offset:20480\n"
    "ds_read_b128 a[176:179], v242 offset:16384\n"
    "v_mfma_f32_16x16x32_bf16 v[120:123], a[188:191], v[160:163], v[120:123]\n"
    "s_add_u32 m0, s55, 24576\n"
    "ds_read_b128 a[180:183], v243 offset:16384\n"
    "v_exp_f32_e32 v216, v216\n"
    "v_mfma_f32_16x16x32_bf16 v[124:127], a[188:191], v[168:171], v[124:127]\n"
    "buffer_load_dwordx4 v251, s[8:11], s31 offen offset:0 sc1 lds\n"
    "v_mfma_f32_16x16x32_bf16 v[124:127], a[184:187], v[164:167], v[124:127]\n"
    "s_waitcnt lgkmcnt(4)\n"
    "v_exp_f32_e32 v217, v217\n"
    "v_mfma_f32_16x16x32_bf16 v[148:151], a[160:163], a[32:35], v[224:227]\n"
    "s_mov_b32 m0, s63\n"
    "ds_read_b128 a[184:187], v242 offset:20480\n"
    "v_cvt_pk_bf16_f32 v207, v210, v211\n"
    "v_mfma_f32_16x16x32_bf16 v[148:151], a[164:167], a[36:39], v[148:151]\n"
    "buffer_load_dword v253, v238, s[8:11], s48 offen offset:0 sc1\n"
    "v_mfma_f32_16x16x32_bf16 v[140:143], a[164:167], a[4:7], v[220:223]\n"
    "s_addk_i32 s31, 0x1000\n"
    "ds_write_addtid_b32 v206 offset:3584\n"
    "ds_write_addtid_b32 v173 offset:4352\n"
    "v_mfma_f32_16x16x32_bf16 v[140:143], a[160:163], a[0:3], v[140:143]\n"
    "s_waitcnt lgkmcnt(5)\n"
    "ds_read_b128 a[188:191], v243 offset:20480\n"
    "v_cvt_pk_bf16_f32 v175, v178, v179\n"
    "v_mfma_f32_16x16x32_bf16 v[140:143], a[168:171], a[8:11], v[140:143]\n"
    "s_add_u32 m0, s55, 25600\n"
    "ds_read_b128 a[160:163], v240 offset:18432\n"
    "ds_read_b128 a[164:167], v241 offset:18432\n"
    "v_mfma_f32_16x16x32_bf16 v[140:143], a[172:175], a[12:15], v[140:143]\n"
    "buffer_load_dwordx4 v251, s[8:11], s31 offen offset:0 sc1 lds\n"
    "v_mfma_f32_16x16x32_bf16 v[148:151], a[172:175], a[44:47], v[148:151]\n"
    "s_mov_b32 m0, s63\n"
    "v_exp_f32_e32 v218, v218\n"
    "v_mfma_f32_16x16x32_bf16 v[148:151], a[168:171], a[40:43], v[148:151]\n"
    "s_waitcnt lgkmcnt(6)\n"
    "ds_write_addtid_b32 v207 offset:3840\n"
    "v_exp_f32_e32 v219, v219\n"
    "v_mfma_f32_16x16x32_bf16 v[148:151], a[176:179], a[48:51], v[148:151]\n"
    "s_add_u32 s87, s59, 8192\n"
    "ds_write_addtid_b32 v212 offset:5120\n"
    "ds_write_addtid_b32 v213 offset:5376\n"
    "v_mfma_f32_16x16x32_bf16 v[148:151], a[180:183], a[52:55], v[148:151]\n"
    "s_add_u32 s31, s47, s36\n"
    "ds_write_addtid_b32 v174 offset:4608\n"
    "ds_write_addtid_b32 v175 offset:4864\n"
    "v_mfma_f32_16x16x32_bf16 v[140:143], a[180:183], a[20:23], v[140:143]\n"
    "s_add_u32 m0, s55, 26624\n"
    "ds_read_b128 a[168:171], v240 offset:22528\n"
    "v_cvt_pk_bf16_f32 v214, v216, v217\n"
    "v_mfma_f32_16x16x32_bf16 v[140:143], a[176:179], a[16:19], v[140:143]\n"
    "s_waitcnt lgkmcnt(8)\n"
    "ds_read_b128 a[172:175], v241 offset:22528\n"
    "ds_read_b128 a[176:179], v242 offset:18432\n"
    "v_mfma_f32_16x16x32_bf16 v[140:143], a[184:187], a[24:27], v[140:143]\n"
    "buffer_load_dwordx4 v251, s[8:11], s31 offen offset:0 sc1 lds\n"
    "v_mfma_f32_16x16x32_bf16 v[140:143], a[188:191], a[28:31], v[140:143]\n"
    "s_mov_b32 m0, s63\n"
    "ds_read_b128 a[180:183], v243 offset:18432\n"
    "v_cvt_pk_bf16_f32 v215, v218, v219\n"
    "v_mfma_f32_16x16x32_bf16 v[148:151], a[188:191], a[60:63], v[148:151]\n"
    "s_add_u32 s88, s59, 16384\n"
    "ds_write_addtid_b32 v214 offset:5632\n"
    "ds_write_addtid_b32 v215 offset:5888\n"
    "v_mfma_f32_16x16x32_bf16 v[148:151], a[184:187], a[56:59], v[148:151]\n"
    "s_waitcnt lgkmcnt(11)\n"
    "ds_read_b128 a[184:187], v242 offset:22528\n"
    "v_exp_f32_e32 v140, v140\n"
    "v_mfma_f32_16x16x32_bf16 v[152:155], a[160:163], a[32:35], v[224:227]\n"
    "s_addk_i32 s31, 0x1000\n"
    "ds_read_b128 a[188:191], v243 offset:22528\n"
    "v_exp_f32_e32 v141, v141\n"
    "v_mfma_f32_16x16x32_bf16 v[152:155], a[164:167], a[36:39], v[152:155]\n"
    "s_waitcnt lgkmcnt(2)\n"
    "v_mfma_f32_16x16x32_bf16 v[144:147], a[164:167], a[4:7], v[220:223]\n"
    "s_barrier\n"
    "v_mfma_f32_16x16x32_bf16 v[144:147], a[160:163], a[0:3], v[144:147]\n"
    "ds_read_b64_tr_b16 a[160:161], v244 offset:0\n"
    "ds_read2st64_b32 v[200:201], v249 offset0:8 offset1:9\n"
    "v_mfma_f32_16x16x32_bf16 v[144:147], a[168:171], a[8:11], v[144:147]\n"
    "s_add_u32 m0, s55, 27648\n"
    "ds_read_b64_tr_b16 a[162:163], v244 offset:2048\n"
    "v_exp_f32_e32 v142, v142\n"
    "v_mfma_f32_16x16x32_bf16 v[144:147], a[172:175], a[12:15], v[144:147]\n"
    "buffer_load_dwordx4 v251, s[8:11], s31 offen offset:0 sc1 lds\n"
    "ds_read2st64_b32 v[202:203], v249 offset0:10 offset1:11\n"
    "v_mfma_f32_16x16x32_bf16 v[152:155], a[172:175], a[44:47], v[152:155]\n"
    "s_mov_b32 m0, s63\n"
    "ds_read_b64_tr_b16 a[164:165], v244 offset:32768\n"
    "v_exp_f32_e32 v143, v143\n"
    "v_mfma_f32_16x16x32_bf16 v[152:155], a[168:171], a[40:43], v[152:155]\n"
    "ds_read2st64_b32 v[208:209], v249 offset0:12 offset1:13\n"
    "ds_read2st64_b32 v[210:211], v249 offset0:14 offset1:15\n"
    "v_cvt_pk_bf16_f32 v140, v140, v141\n"
    "v_mfma_f32_16x16x32_bf16 v[152:155], a[176:179], a[48:51], v[152:155]\n"
    "s_add_u32 s89, s59, 24576\n"
    "ds_read_b64_tr_b16 a[166:167], v244 offset:34816\n"
    "v_exp_f32_e32 v148, v148\n"
    "v_mfma_f32_16x16x32_bf16 v[152:155], a[180:183], a[52:55], v[152:155]\n"
    "s_add_u32 s31, s47, s37\n"
    "ds_read_b64_tr_b16 a[168:169], v245 offset:0\n"
    "v_exp_f32_e32 v149, v149\n"
    "v_mfma_f32_16x16x32_bf16 v[144:147], a[180:183], a[20:23], v[144:147]\n"
    "s_add_u32 m0, s55, 28672\n"
    "ds_read_b64_tr_b16 a[170:171], v245 offset:2048\n"
    "v_cvt_pk_bf16_f32 v141, v142, v143\n"
    "v_mfma_f32_16x16x32_bf16 v[144:147], a[176:179], a[16:19], v[144:147]\n"
    "s_waitcnt lgkmcnt(10)\n"
    "ds_read_b64_tr_b16 a[172:173], v245 offset:32768\n"
    "ds_read_b64_tr_b16 a[174:175], v245 offset:34816\n"
    "v_mfma_f32_16x16x32_bf16 v[144:147], a[184:187], a[24:27], v[144:147]\n"
    "buffer_load_dwordx4 v251, s[8:11], s31 offen offset:0 sc1 lds\n"
    "ds_read2st64_b32 v[176:177], v249 offset0:16 offset1:17\n"
    "v_mfma_f32_16x16x32_bf16 v[144:147], a[188:191], a[28:31], v[144:147]\n"
    "s_mov_b32 m0, s63\n"
    "ds_read_b64_tr_b16 a[176:177], v246 offset:0\n"
    "ds_read_b64_tr_b16 a[178:179], v246 offset:2048\n"
    "v_mfma_f32_16x16x32_bf16 v[152:155], a[188:191], a[60:63], v[152:155]\n"
    "ds_read2st64_b32 v[178:179], v249 offset0:18 offset1:19\n"
    "v_exp_f32_e32 v150, v150\n"
    "v_mfma_f32_16x16x32_bf16 v[152:155], a[184:187], a[56:59], v[152:155]\n"
    "s_waitcnt lgkmcnt(8)\n"
    "ds_read_b64_tr_b16 a[180:181], v246 offset:32768\n"
    "ds_read_b64_tr_b16 a[182:183], v246 offset:34816\n"
    "v_mfma_f32_16x16x32_bf16 a[240:243], a[160:163], v[204:207], a[240:243]\n"
    "s_addk_i32 s31, 0x1000\n"
    "ds_read_b64_tr_b16 a[184:185], v247 offset:0\n"
    "ds_write_addtid_b32 v140 offset:0\n"
    "v_mfma_f32_16x16x32_bf16 a[240:243], a[164:167], v[208:211], a[240:243]\n"
    "s_add_u32 m0, s55, 29696\n"
    "ds_read2st64_b32 v[216:217], v249 offset0:20 offset1:21\n"
    "v_exp_f32_e32 v151, v151\n"
    "v_mfma_f32_16x16x32_bf16 a[236:239], a[164:167], v[200:203], a[236:239]\n"
    "buffer_load_dwordx4 v251, s[8:11], s31 offen offset:0 sc1 lds\n"
    "ds_read_b64_tr_b16 a[186:187], v247 offset:2048\n"
    "v_mfma_f32_16x16x32_bf16 a[236:239], a[160:163], v[196:199], a[236:239]\n"
    "s_waitcnt lgkmcnt(10)\n"
    "ds_read_b64_tr_b16 a[188:189], v247 offset:32768\n"
    "ds_read2st64_b32 v[218:219], v249 offset0:22 offset1:23\n"
    "v_mfma_f32_16x16x32_bf16 v[0:3], a[168:171], v[196:199], v[0:3]\n"
    "s_mov_b32 m0, s63\n"
    "ds_read_b64_tr_b16 a[190:191], v247 offset:34816\n"
    "ds_read_b64_tr_b16 a[160:161], v244 offset:4096\n"
    "v_mfma_f32_16x16x32_bf16 v[0:3], a[172:175], v[200:203], v[0:3]\n"
    "buffer_load_dwordx4 v[72:75], v250, s[8:11], s62 offen offset:2048 sc1\n"
    "ds_read_b64_tr_b16 a[162:163], v244 offset:6144\n"
    "v_mfma_f32_16x16x32_bf16 v[4:7], a[172:175], v[208:211], v[4:7]\n"
    "s_add_u32 s31, s46, s36\n"
    "ds_read_b64_tr_b16 a[164:165], v244 offset:36864\n"
    "v_cvt_pk_bf16_f32 v148, v148, v149\n"
    "v_mfma_f32_16x16x32_bf16 v[4:7], a[168:171], v[204:207], v[4:7]\n"
    "s_waitcnt lgkmcnt(10)\n"
    "ds_read_b64_tr_b16 a[166:167], v244 offset:38912\n"
    "v_exp_f32_e32 v144, v144\n"
    "v_mfma_f32_16x16x32_bf16 v[24:27], a[176:179], v[204:207], v[24:27]\n"
    "s_add_u32 m0, s55, 30720\n"
    "s_waitcnt vmcnt(6)\n"
    "ds_read_b64_tr_b16 a[168:169], v245 offset:4096\n"
    "v_subrev_u32_e32 v253, s39, v253\n"
    "v_mfma_f32_16x16x32_bf16 v[24:27], a[180:183], v[208:211], v[24:27]\n"
    "buffer_load_dwordx4 v251, s[12:15], s31 offen offset:0 lds\n"
    "ds_read_b64_tr_b16 a[170:171], v245 offset:6144\n"
    "v_cmp_le_u32_e32 vcc, 1, v253\n"
    "v_min_u32_e32 v220, 0x13c, v248\n"
    "v_mfma_f32_16x16x32_bf16 v[20:23], a[180:183], v[200:203], v[20:23]\n"
    "s_mov_b32 m0, s63\n"
    "ds_read_b64_tr_b16 a[172:173], v245 offset:36864\n"
    "ds_read_b64_tr_b16 a[174:175], v245 offset:38912\n"
    "v_mfma_f32_16x16x32_bf16 v[20:23], a[176:179], v[196:199], v[20:23]\n"
    "s_waitcnt lgkmcnt(8)\n"
    "v_exp_f32_e32 v145, v145\n"
    "v_mfma_f32_16x16x32_bf16 v[40:43], a[184:187], v[196:199], v[40:43]\n"
    "s_cbranch_vccnz .Lat_k\n"
    ".Lat_kr:\n"
    ".Lat_kxr:\n"
    "v_mfma_f32_16x16x32_bf16 v[40:43], a[188:191], v[200:203], v[40:43]\n"
    "s_addk_i32 s31, 0x1000\n"
    "ds_read_b64_tr_b16 a[176:177], v246 offset:4096\n"
    "ds_read_b64_tr_b16 a[178:179], v246 offset:6144\n"
    "v_mfma_f32_16x16x32_bf16 v[44:47], a[188:191], v[208:211], v[44:47]\n"
    "s_add_u32 m0, s55, 31744\n"
    "ds_read_b64_tr_b16 a[180:181], v246 offset:36864\n"
    "v_cvt_pk_bf16_f32 v149, v150, v151\n"
    "v_mfma_f32_16x16x32_bf16 v[44:47], a[184:187], v[204:207], v[44:47]\n"
    "s_waitcnt lgkmcnt(7)\n"
    "v_exp_f32_e32 v146, v146\n"
    "v_mfma_f32_16x16x32_bf16 v[64:67], a[160:163], v[204:207], v[64:67]\n"
    "buffer_load_dwordx4 v251, s[12:15], s31 offen offset:0 lds\n"
    "ds_read_b64_tr_b16 a[182:183], v246 offset:38912\n"
    "v_mfma_f32_16x16x32_bf16 v[64:67], a[164:167], v[208:211], v[64:67]\n"
    "s_mov_b32 m0, s63\n"
    "ds_read_b64_tr_b16 a[184:185], v247 offset:4096\n"
    "v_exp_f32_e32 v147, v147\n"
    "v_mfma_f32_16x16x32_bf16 v[60:63], a[164:167], v[200:203], v[60:63]\n"
    "buffer_load_dwordx4 a[0:3], v250, s[8:11], s87 offen offset:0 sc1\n"
    "ds_read_b64_tr_b16 a[186:187], v247 offset:6144\n"
    "v_mfma_f32_16x16x32_bf16 v[60:63], a[160:163], v[196:199], v[60:63]\n"
    "s_waitcnt lgkmcnt(6)\n"
    "ds_read_b64_tr_b16 a[188:189], v247 offset:36864\n"
    "ds_read_b64_tr_b16 a[190:191], v247 offset:38912\n"
    "v_mfma_f32_16x16x32_bf16 v[80:83], a[168:171], v[196:199], v[80:83]\n"
    "ds_write_addtid_b32 v141 offset:256\n"
    "v_cvt_pk_bf16_f32 v142, v144, v145\n"
    "v_mfma_f32_16x16x32_bf16 v[80:83], a[172:175], v[200:203], v[80:83]\n"
    "buffer_load_dwordx4 v[92:95], v250, s[8:11], s62 offen offset:3072 sc1\n"
    "ds_read_b64_tr_b16 a[160:161], v244 offset:12288\n"
    "v_mfma_f32_16x16x32_bf16 v[84:87], a[172:175], v[208:211], v[84:87]\n"
    "ds_read_b64_tr_b16 a[162:163], v244 offset:14336\n"
    "v_exp_f32_e32 v152, v152\n"
    "v_mfma_f32_16x16x32_bf16 v[84:87], a[168:171], v[204:207], v[84:87]\n"
    "s_waitcnt lgkmcnt(7)\n"
    "ds_read_b64_tr_b16 a[164:165], v244 offset:45056\n"
    "ds_read_b64_tr_b16 a[166:167], v244 offset:47104\n"
    "v_mfma_f32_16x16x32_bf16 v[104:107], a[176:179], v[204:207], v[104:107]\n"
    "ds_read_b64_tr_b16 a[168:169], v245 offset:12288\n"
    "ds_read_b64_tr_b16 a[170:171], v245 offset:14336\n"
    "v_mfma_f32_16x16x32_bf16 v[104:107], a[180:183], v[208:211], v[104:107]\n"
    "buffer_load_dwordx4 a[4:7], v250, s[8:11], s88 offen offset:0 sc1\n"
    "ds_read_b64_tr_b16 a[172:173], v245 offset:45056\n"
    "v_mfma_f32_16x16x32_bf16 v[100:103], a[180:183], v[200:203], v[100:103]\n"
    "ds_read_b64_tr_b16 a[174:175], v245 offset:47104\n"
    "v_exp_f32_e32 v153, v153\n"
    "v_mfma_f32_16x16x32_bf16 v[100:103], a[176:179], v[196:199], v[100:103]\n"
    "s_waitcnt lgkmcnt(9)\n"
    "ds_write_addtid_b32 v148 offset:1024\n"
    "ds_write_addtid_b32 v149 offset:1280\n"
    "v_mfma_f32_16x16x32_bf16 v[120:123], a[184:187], v[196:199], v[120:123]\n"
    "ds_read_b64_tr_b16 a[176:177], v246 offset:12288\n"
    "v_cvt_pk_bf16_f32 v143, v146, v147\n"
    "v_mfma_f32_16x16x32_bf16 v[120:123], a[188:191], v[200:203], v[120:123]\n"
    "ds_read_b64_tr_b16 a[178:179], v246 offset:14336\n"
    "v_exp_f32_e32 v154, v154\n"
    "v_mfma_f32_16x16x32_bf16 v[124:127], a[188:191], v[208:211], v[124:127]\n"
    "ds_write_addtid_b32 v142 offset:512\n"
    "v_exp_f32_e32 v155, v155\n"
    "v_mfma_f32_16x16x32_bf16 v[124:127], a[184:187], v[204:207], v[124:127]\n"
    "s_waitcnt lgkmcnt(9)\n"
    "ds_read_b64_tr_b16 a[180:181], v246 offset:45056\n"
    "ds_read_b64_tr_b16 a[184:185], v247 offset:12288\n"
    "v_mfma_f32_16x16x32_bf16 v[64:67], a[160:163], v[212:215], v[64:67]\n"
    "buffer_load_dwordx4 a[8:11], v250, s[8:11], s89 offen offset:0 sc1\n"
    "ds_read_b64_tr_b16 a[182:183], v246 offset:47104\n"
    "v_mfma_f32_16x16x32_bf16 v[64:67], a[164:167], v[216:219], v[64:67]\n"
    "ds_read_b64_tr_b16 a[186:187], v247 offset:14336\n"
    "ds_read_b64_tr_b16 a[188:189], v247 offset:45056\n"
    "v_mfma_f32_16x16x32_bf16 v[60:63], a[164:167], v[176:179], v[60:63]\n"
    "ds_write_addtid_b32 v143 offset:768\n"
    "v_cvt_pk_bf16_f32 v150, v152, v153\n"
    "v_mfma_f32_16x16x32_bf16 v[60:63], a[160:163], v[172:175], v[60:63]\n"
    "s_waitcnt lgkmcnt(11)\n"
    "ds_read_b64_tr_b16 a[190:191], v247 offset:47104\n"
    "v_cvt_pk_bf16_f32 v151, v154, v155\n"
    "v_mfma_f32_16x16x32_bf16 v[80:83], a[168:171], v[172:175], v[80:83]\n"
    "ds_write_addtid_b32 v150 offset:1536\n"
    "ds_write_addtid_b32 v151 offset:1792\n"
    "v_mfma_f32_16x16x32_bf16 v[80:83], a[172:175], v[176:179], v[80:83]\n"
    "ds_read_b64_tr_b16 a[160:161], v244 offset:8192\n"
    "ds_read_b64_tr_b16 a[162:163], v244 offset:10240\n"
    "v_mfma_f32_16x16x32_bf16 v[84:87], a[172:175], v[216:219], v[84:87]\n"
    "ds_read_b64_tr_b16 a[164:165], v244 offset:40960\n"
    "ds_read_b64_tr_b16 a[166:167], v244 offset:43008\n"
    "v_mfma_f32_16x16x32_bf16 v[84:87], a[168:171], v[212:215], v[84:87]\n"
    "s_waitcnt lgkmcnt(10)\n"
    "s_waitcnt lgkmcnt(4)\n"
    "s_waitcnt vmcnt(7)\n"
    "v_mfma_f32_16x16x32_bf16 v[104:107], a[176:179], v[212:215], v[104:107]\n"
    "s_barrier\n"
    "v_mfma_f32_16x16x32_bf16 v[104:107], a[180:183], v[216:219], v[104:107]\n"
    "buffer_load_dword v220, v220, s[8:11], s60 offen offset:192 sc1\n"
    "ds_read_b64_tr_b16 a[168:169], v244 offset:16384\n"
    "v_mfma_f32_16x16x32_bf16 v[100:103], a[180:183], v[176:179], v[100:103]\n"
    "s_add_u32 s31, s46, s37\n"
    "ds_read2st64_b32 v[144:145], v249 offset0:0 offset1:1\n"
    "ds_read2st64_b32 v[146:147], v249 offset0:2 offset1:3\n"
    "v_mfma_f32_16x16x32_bf16 v[100:103], a[176:179], v[172:175], v[100:103]\n"
    "ds_read_b64_tr_b16 a[170:171], v244 offset:18432\n"
    "ds_read_b64_tr_b16 a[172:173], v244 offset:49152\n"
    "v_mfma_f32_16x16x32_bf16 v[120:123], a[184:187], v[172:175], v[120:123]\n"
    "s_add_u32 m0, s55, 4096\n"
    "ds_read_b64_tr_b16 a[174:175], v244 offset:51200\n"
    "ds_read2st64_b32 v[152:153], v249 offset0:4 offset1:5\n"
    "v_mfma_f32_16x16x32_bf16 v[120:123], a[188:191], v[176:179], v[120:123]\n"
    "buffer_load_dwordx4 v251, s[12:15], s31 offen offset:0 lds\n"
    "ds_read2st64_b32 v[154:155], v249 offset0:6 offset1:7\n"
    "v_mfma_f32_16x16x32_bf16 v[124:127], a[188:191], v[216:219], v[124:127]\n"
    "ds_read_b64_tr_b16 a[176:177], v245 offset:8192\n"
    "ds_read_b64_tr_b16 a[178:179], v245 offset:10240\n"
    "v_mfma_f32_16x16x32_bf16 v[124:127], a[184:187], v[212:215], v[124:127]\n"
    "s_waitcnt lgkmcnt(10)\n"
    "ds_read_b64_tr_b16 a[180:181], v245 offset:40960\n"
    "ds_read_b64_tr_b16 a[182:183], v245 offset:43008\n"
    "v_mfma_f32_16x16x32_bf16 a[240:243], a[160:163], v[212:215], a[240:243]\n"
    "buffer_load_dwordx4 a[12:15], v250, s[8:11], s87 offen offset:1024 sc1\n"
    "v_mfma_f32_16x16x32_bf16 a[240:243], a[164:167], v[216:219], a[240:243]\n"
    "s_addk_i32 s31, 0x1000\n"
    "ds_read_b64_tr_b16 a[184:185], v245 offset:16384\n"
    "v_add_u32_e32 v240, s45, v240\n"
    "v_mfma_f32_16x16x32_bf16 a[236:239], a[164:167], v[176:179], a[236:239]\n"
    "s_add_u32 m0, s55, 5120\n"
    "ds_read_b64_tr_b16 a[186:187], v245 offset:18432\n"
    "v_xor_b32_e32 v241, 64, v240\n"
    "v_mfma_f32_16x16x32_bf16 a[236:239], a[160:163], v[172:175], a[236:239]\n"
    "s_waitcnt lgkmcnt(6)\n"
    "ds_read_b64_tr_b16 a[188:189], v245 offset:49152\n"
    "ds_read_b64_tr_b16 a[190:191], v245 offset:51200\n"
    "v_mfma_f32_16x16x32_bf16 a[236:239], a[168:171], v[140:143], a[236:239]\n"
    "buffer_load_dwordx4 v251, s[12:15], s31 offen offset:0 lds\n"
    "v_mfma_f32_16x16x32_bf16 a[236:239], a[172:175], v[144:147], a[236:239]\n"
    "ds_read_b64_tr_b16 a[160:161], v246 offset:8192\n"
    "v_add_u32_e32 v242, 0x10000, v240\n"
    "v_mfma_f32_16x16x32_bf16 a[240:243], a[172:175], v[152:155], a[240:243]\n"
    "ds_read_b64_tr_b16 a[162:163], v246 offset:10240\n"
    "ds_read_b64_tr_b16 a[164:165], v246 offset:40960\n"
    "v_mfma_f32_16x16x32_bf16 a[240:243], a[168:171], v[148:151], a[240:243]\n"
    "s_waitcnt lgkmcnt(7)\n"
    "ds_read_b64_tr_b16 a[166:167], v246 offset:43008\n"
    "v_add_u32_e32 v243, 0x10000, v241\n"
    "v_mfma_f32_16x16x32_bf16 v[4:7], a[176:179], v[212:215], v[4:7]\n"
    "buffer_load_dwordx4 a[16:19], v250, s[8:11], s88 offen offset:1024 sc1\n"
    "v_mfma_f32_16x16x32_bf16 v[4:7], a[180:183], v[216:219], v[4:7]\n"
    "ds_read_b64_tr_b16 a[168:169], v246 offset:16384\n"
    "v_mfma_f32_16x16x32_bf16 v[0:3], a[180:183], v[176:179], v[0:3]\n"
    "ds_read_b64_tr_b16 a[170:171], v246 offset:18432\n"
    "v_add_u32_e32 v252, s54, v240\n"
    "v_mfma_f32_16x16x32_bf16 v[0:3], a[176:179], v[172:175], v[0:3]\n"
    "s_waitcnt lgkmcnt(6)\n"
    "ds_read_b64_tr_b16 a[172:173], v246 offset:49152\n"
    "v_xor_b32_e32 v255, 64, v252\n"
    "v_mfma_f32_16x16x32_bf16 v[0:3], a[184:187], v[140:143], v[0:3]\n"
    "ds_read_b64_tr_b16 a[174:175], v246 offset:51200\n"
    "v_lshlrev_b32_e32 v228, 16, v228\n"
    "v_mfma_f32_16x16x32_bf16 v[0:3], a[188:191], v[144:147], v[0:3]\n"
    "ds_read_b64_tr_b16 a[176:177], v247 offset:8192\n"
    "v_lshlrev_b32_e32 v229, 16, v229\n"
    "v_mfma_f32_16x16x32_bf16 v[4:7], a[188:191], v[152:155], v[4:7]\n"
    "ds_read_b64_tr_b16 a[178:179], v247 offset:10240\n"
    "v_lshlrev_b32_e32 v230, 16, v230\n"
    "v_mfma_f32_16x16x32_bf16 v[4:7], a[184:187], v[148:151], v[4:7]\n"
    "s_waitcnt lgkmcnt(6)\n"
    "ds_read_b64_tr_b16 a[180:181], v247 offset:40960\n"
    "ds_read_b64_tr_b16 a[182:183], v247 offset:43008\n"
    "v_mfma_f32_16x16x32_bf16 v[24:27], a[160:163], v[212:215], v[24:27]\n"
    "buffer_load_dwordx4 a[20:23], v250, s[8:11], s89 offen offset:1024 sc1\n"
    "v_mfma_f32_16x16x32_bf16 v[24:27], a[164:167], v[216:219], v[24:27]\n"
    "ds_read_b64_tr_b16 a[184:185], v247 offset:16384\n"
    "v_cvt_pk_bf16_f32 v7, v6, v7\n"
    "v_mfma_f32_16x16x32_bf16 v[20:23], a[164:167], v[176:179], v[20:23]\n"
    "ds_read_b64_tr_b16 a[186:187], v247 offset:18432\n"
    "v_cvt_pk_bf16_f32 v6, v4, v5\n"
    "v_mfma_f32_16x16x32_bf16 v[20:23], a[160:163], v[172:175], v[20:23]\n"
    "s_waitcnt lgkmcnt(6)\n"
    "ds_read_b64_tr_b16 a[160:161], v244 offset:20480\n"
    "v_accvgpr_read_b32 v4, a240\n"
    "v_mfma_f32_16x16x32_bf16 v[20:23], a[168:171], v[140:143], v[20:23]\n"
    "ds_read_b64_tr_b16 a[188:189], v247 offset:49152\n"
    "ds_read_b64_tr_b16 a[190:191], v247 offset:51200\n"
    "v_mfma_f32_16x16x32_bf16 v[20:23], a[172:175], v[144:147], v[20:23]\n"
    "ds_read_b64_tr_b16 a[162:163], v244 offset:22528\n"
    "v_mfma_f32_16x16x32_bf16 v[24:27], a[172:175], v[152:155], v[24:27]\n"
    "ds_read_b64_tr_b16 a[164:165], v244 offset:53248\n"
    "v_accvgpr_read_b32 v5, a241\n"
    "v_mfma_f32_16x16x32_bf16 v[24:27], a[168:171], v[148:151], v[24:27]\n"
    "s_waitcnt lgkmcnt(7)\n"
    "ds_read_b64_tr_b16 a[168:169], v245 offset:20480\n"
    "v_accvgpr_read_b32 v225, a243\n"
    "v_mfma_f32_16x16x32_bf16 v[44:47], a[176:179], v[212:215], v[44:47]\n"
    "ds_read_b64_tr_b16 a[166:167], v244 offset:55296\n"
    "ds_read_b64_tr_b16 a[170:171], v245 offset:22528\n"
    "v_mfma_f32_16x16x32_bf16 v[44:47], a[180:183], v[216:219], v[44:47]\n"
    "ds_read_b64_tr_b16 a[172:173], v245 offset:53248\n"
    "ds_read_b64_tr_b16 a[174:175], v245 offset:55296\n"
    "v_mfma_f32_16x16x32_bf16 v[40:43], a[180:183], v[176:179], v[40:43]\n"
    "v_cvt_pk_bf16_f32 v4, v4, v5\n"
    "v_mfma_f32_16x16x32_bf16 v[40:43], a[176:179], v[172:175], v[40:43]\n"
    "s_waitcnt lgkmcnt(7)\n"
    "ds_read_b64_tr_b16 a[176:177], v246 offset:20480\n"
    "v_accvgpr_read_b32 v5, a242\n"
    "v_mfma_f32_16x16x32_bf16 v[40:43], a[184:187], v[140:143], v[40:43]\n"
    "ds_read_b64_tr_b16 a[178:179], v246 offset:22528\n"
    "ds_read_b64_tr_b16 a[180:181], v246 offset:53248\n"
    "v_mfma_f32_16x16x32_bf16 v[40:43], a[188:191], v[144:147], v[40:43]\n"
    "ds_read_b64_tr_b16 a[182:183], v246 offset:55296\n"
    "v_cvt_pk_bf16_f32 v5, v5, v225\n"
    "v_mfma_f32_16x16x32_bf16 v[44:47], a[188:191], v[152:155], v[44:47]\n"
    "buffer_store_dwordx4 v[4:7], v250, s[8:11], s80 offen offset:0 sc1\n"
    "v_mfma_f32_16x16x32_bf16 v[44:47], a[184:187], v[148:151], v[44:47]\n"
    "s_waitcnt lgkmcnt(7)\n"
    "ds_read_b64_tr_b16 a[184:185], v247 offset:20480\n"
    "v_mfma_f32_16x16x32_bf16 v[64:67], a[160:163], v[148:151], v[64:67]\n"
    "ds_read_b64_tr_b16 a[186:187], v247 offset:22528\n"
    "v_mfma_f32_16x16x32_bf16 v[64:67], a[164:167], v[152:155], v[64:67]\n"
    "s_waitcnt lgkmcnt(6)\n"
    "ds_read_b64_tr_b16 a[188:189], v247 offset:53248\n"
    "ds_read_b64_tr_b16 a[190:191], v247 offset:55296\n"
    "v_mfma_f32_16x16x32_bf16 v[84:87], a[172:175], v[152:155], v[84:87]\n"
    "ds_read_b128 a[48:51], v240 offset:0\n"
    "v_cvt_pk_bf16_f32 v24, v24, v25\n"
    "v_mfma_f32_16x16x32_bf16 v[84:87], a[168:171], v[148:151], v[84:87]\n"
    "s_waitcnt lgkmcnt(5)\n"
    "ds_read_b128 a[52:55], v241 offset:0\n"
    "v_cvt_pk_bf16_f32 v25, v26, v27\n"
    "v_mfma_f32_16x16x32_bf16 v[104:107], a[176:179], v[148:151], v[104:107]\n"
    "v_cvt_pk_bf16_f32 v26, v44, v45\n"
    "v_mfma_f32_16x16x32_bf16 v[104:107], a[180:183], v[152:155], v[104:107]\n"
    "s_waitcnt lgkmcnt(2)\n"
    "ds_read_b128 a[56:59], v240 offset:32768\n"
    "v_cvt_pk_bf16_f32 v27, v46, v47\n"
    "v_mfma_f32_16x16x32_bf16 v[124:127], a[188:191], v[152:155], v[124:127]\n"
    "ds_read_b128 a[60:63], v241 offset:32768\n"
    "v_mfma_f32_16x16x32_bf16 v[124:127], a[184:187], v[148:151], v[124:127]\n"
    "buffer_store_dwordx4 v[24:27], v250, s[8:11], s80 offen offset:1024 sc1\n"
    "v_mfma_f32_16x16x32_bf16 v[60:63], a[164:167], v[144:147], v[60:63]\n"
    "v_cvt_pk_bf16_f32 v64, v64, v65\n"
    "v_mfma_f32_16x16x32_bf16 v[60:63], a[160:163], v[140:143], v[60:63]\n"
    "v_cvt_pk_bf16_f32 v65, v66, v67\n"
    "v_mfma_f32_16x16x32_bf16 v[80:83], a[168:171], v[140:143], v[80:83]\n"
    "v_cvt_pk_bf16_f32 v66, v84, v85\n"
    "v_cvt_pk_bf16_f32 v67, v86, v87\n"
    "v_mfma_f32_16x16x32_bf16 v[80:83], a[172:175], v[144:147], v[80:83]\n"
    "buffer_store_dwordx4 v[64:67], v250, s[8:11], s80 offen offset:2048 sc1\n"
    "v_mfma_f32_16x16x32_bf16 v[100:103], a[180:183], v[144:147], v[100:103]\n"
    "s_mov_b32 s45, 0x71800000\n"
    "v_cvt_pk_bf16_f32 v104, v104, v105\n"
    "v_cvt_pk_bf16_f32 v105, v106, v107\n"
    "v_mfma_f32_16x16x32_bf16 v[100:103], a[176:179], v[140:143], v[100:103]\n"
    "v_cvt_pk_bf16_f32 v106, v124, v125\n"
    "v_cvt_pk_bf16_f32 v107, v126, v127\n"
    "v_mfma_f32_16x16x32_bf16 v[120:123], a[184:187], v[140:143], v[120:123]\n"
    "buffer_store_dwordx4 v[104:107], v250, s[8:11], s80 offen offset:3072 sc1\n"
    "v_mfma_f32_16x16x32_bf16 v[120:123], a[188:191], v[144:147], v[120:123]\n"
    "s_waitcnt lgkmcnt(2)\n"
    "buffer_store_dword v127, v231, s[8:11], s86 offen offset:0 sc1\n"
    "v_mfma_f32_16x16x32_bf16 v[156:159], a[216:219], a[48:51], 0\n"
    "ds_read_b128 a[160:163], v242 offset:0\n"
    "v_mfma_f32_16x16x32_bf16 v[160:163], a[220:223], a[48:51], 0\n"
    "ds_read_b128 a[164:167], v243 offset:0\n"
    "v_mfma_f32_16x16x32_bf16 v[164:167], a[224:227], a[48:51], 0\n"
    "ds_read_b128 a[168:171], v242 offset:32768\n"
    "v_add_f32_e32 v225, v123, v127\n"
    "v_mfma_f32_16x16x32_bf16 v[168:171], a[228:231], a[48:51], 0\n"
    "ds_read_b128 a[172:175], v243 offset:32768\n"
    "v_min3_f32 v129, v129, v123, v127\n"
    "v_mfma_f32_16x16x32_bf16 v[156:159], a[232:235], a[52:55], v[156:159]\n"
    "ds_read_b128 a[176:179], v240 offset:2048\n"
    "v_add_f32_e32 v225, v225, v128\n"
    "v_mfma_f32_16x16x32_bf16 v[160:163], v[192:195], a[52:55], v[160:163]\n"
    "ds_read_b128 a[180:183], v241 offset:2048\n"
    "v_mov_b32_e32 v227, v123\n"
    "v_mfma_f32_16x16x32_bf16 v[164:167], a[64:67], a[52:55], v[164:167]\n"
    "ds_read_b128 a[184:187], v240 offset:34816\n"
    "v_mfma_f32_16x16x32_bf16 v[168:171], a[68:71], a[52:55], v[168:171]\n"
    "s_waitcnt lgkmcnt(7)\n"
    "ds_read_b128 a[188:191], v241 offset:34816\n"
    "v_cmp_nlt_f32_e32 vcc, 0x18000000, v129\n"
    "v_mfma_f32_16x16x32_bf16 v[156:159], a[72:75], a[56:59], v[156:159]\n"
    "ds_read_b128 a[48:51], v242 offset:2048\n"
    "v_cmp_nlt_f32_e64 s[28:29], v225, s45\n"
    "v_mfma_f32_16x16x32_bf16 v[160:163], a[76:79], a[56:59], v[160:163]\n"
    "buffer_load_dwordx4 a[24:27], v250, s[8:11], s87 offen offset:2048 sc1\n"
    "v_mfma_f32_16x16x32_bf16 v[164:167], a[80:83], a[56:59], v[164:167]\n"
    "s_or_b32 s45, s29, vcc_hi\n"
    "v_mfma_f32_16x16x32_bf16 v[168:171], a[84:87], a[56:59], v[168:171]\n"
    "s_lshr_b32 s45, s45, 16\n"
    "s_cselect_b32 s45, s51, 0\n"
    "v_mfma_f32_16x16x32_bf16 v[156:159], a[88:91], a[60:63], v[156:159]\n"
    "s_andn2_b32 s45, s45, s49\n"
    "ds_read_b128 a[52:55], v243 offset:2048\n"
    "v_mfma_f32_16x16x32_bf16 v[160:163], a[92:95], a[60:63], v[160:163]\n"
    "s_add_u32 s45, s45, s39\n"
    "v_mfma_f32_16x16x32_bf16 v[164:167], a[96:99], a[60:63], v[164:167]\n"
    "buffer_load_dwordx4 a[28:31], v250, s[8:11], s88 offen offset:2048 sc1\n"
    "v_mov_b32_e32 v254, s45\n"
    "v_mfma_f32_16x16x32_bf16 v[168:171], a[100:103], a[60:63], v[168:171]\n"
    "s_waitcnt lgkmcnt(8)\n"
    "v_mfma_f32_16x16x32_bf16 v[156:159], a[104:107], a[160:163], v[156:159]\n"
    "s_lshl1_add_u32 s55, s50, s51\n"
    "ds_read_b128 a[56:59], v242 offset:34816\n"
    "v_mfma_f32_16x16x32_bf16 v[160:163], a[108:111], a[160:163], v[160:163]\n"
    "s_lshl_b32 s55, s55, 12\n"
    "v_mfma_f32_16x16x32_bf16 v[164:167], a[112:115], a[160:163], v[164:167]\n"
    ".long 0xbedf00ff\n"
    ".Lat_w13:\n"
    "s_setpc_b64 s[2:3]\n"
    "s_xor_b32 s31, s93, 2\n"
    "v_mfma_f32_16x16x32_bf16 v[168:171], a[116:119], a[160:163], v[168:171]\n"
    "buffer_load_dwordx4 a[32:35], v250, s[8:11], s89 offen offset:2048 sc1\n"
    "v_mfma_f32_16x16x32_bf16 v[156:159], a[120:123], a[164:167], v[156:159]\n"
    "s_lshl_b32 s31, s31, 8\n"
    "ds_read_b128 a[60:63], v243 offset:34816\n"
    "v_mfma_f32_16x16x32_bf16 v[160:163], a[124:127], a[164:167], v[160:163]\n"
    "s_addk_i32 s31, 0x6000\n"
    "v_mfma_f32_16x16x32_bf16 v[164:167], a[128:131], a[164:167], v[164:167]\n"
    "s_mul_i32 s45, s49, 252\n"
    "v_mfma_f32_16x16x32_bf16 v[168:171], a[132:135], a[164:167], v[168:171]\n"
    "s_waitcnt lgkmcnt(8)\n"
    "v_mfma_f32_16x16x32_bf16 v[156:159], a[136:139], a[168:171], v[156:159]\n"
    "buffer_load_dwordx4 a[36:39], v250, s[8:11], s87 offen offset:3072 sc1\n"
    "ds_read_b128 a[160:163], v240 offset:4096\n"
    "v_mfma_f32_16x16x32_bf16 v[160:163], a[140:143], a[168:171], v[160:163]\n"
    "s_add_u32 s45, s45, s34\n"
    "v_and_b32_e32 v251, 0x7ff, v252\n"
    "v_mfma_f32_16x16x32_bf16 v[164:167], a[144:147], a[168:171], v[164:167]\n"
    "s_mul_i32 s45, s45, 101376\n"
    "v_mfma_f32_16x16x32_bf16 v[168:171], a[148:151], a[168:171], v[168:171]\n"
    "s_add_u32 s45, s45, 16654336\n"
    "v_add_u32_e32 v251, s63, v251\n"
    "v_mfma_f32_16x16x32_bf16 v[156:159], a[152:155], a[172:175], v[156:159]\n"
    "s_lshl_b32 s47, s93, 11\n"
    "ds_read_b128 a[164:167], v241 offset:4096\n"
    "v_mfma_f32_16x16x32_bf16 v[160:163], a[156:159], a[172:175], v[160:163]\n"
    "buffer_load_dwordx4 a[40:43], v250, s[8:11], s88 offen offset:3072 sc1\n"
    "v_mfma_f32_16x16x32_bf16 v[164:167], v[180:183], a[172:175], v[164:167]\n"
    "s_add_u32 s54, s47, s45\n"
    "v_mfma_f32_16x16x32_bf16 v[168:171], v[184:187], a[172:175], v[168:171]\n"
    "s_waitcnt lgkmcnt(8)\n"
    "v_mfma_f32_16x16x32_bf16 v[196:199], a[216:219], a[176:179], 0\n"
    "s_add_u32 s45, s45, 32768\n"
    "ds_read_b128 a[168:171], v240 offset:36864\n"
    "v_mfma_f32_16x16x32_bf16 v[200:203], a[220:223], a[176:179], 0\n"
    "s_cmp_lt_u32 s32, 2\n"
    "s_cselect_b32 s54, s54, 2.0\n"
    "s_cselect_b32 s45, s45, 2.0\n"
    "v_mfma_f32_16x16x32_bf16 v[204:207], a[224:227], a[176:179], 0\n"
    "buffer_load_dwordx4 a[44:47], v250, s[8:11], s89 offen offset:3072 sc1\n"
    "v_mfma_f32_16x16x32_bf16 v[208:211], a[228:231], a[176:179], 0\n"
    "s_add_u32 s80, s35, s46\n"
    "v_mul_f32_e32 v156, v156, v228\n"
    "v_mfma_f32_16x16x32_bf16 v[196:199], a[232:235], a[180:183], v[196:199]\n"
    "s_add_u32 s82, s36, s46\n"
    "ds_read_b128 a[172:175], v241 offset:36864\n"
    "v_mfma_f32_16x16x32_bf16 v[200:203], v[192:195], a[180:183], v[200:203]\n"
    "s_add_u32 s28, s37, s46\n"
    "v_xor_b32_e32 v249, 64, v251\n"
    "v_mfma_f32_16x16x32_bf16 v[204:207], a[64:67], a[180:183], v[204:207]\n"
    "v_add_u32_e32 v244, s55, v250\n"
    "v_mfma_f32_16x16x32_bf16 v[208:211], a[68:71], a[180:183], v[208:211]\n"
    "s_waitcnt lgkmcnt(8)\n"
    "v_mfma_f32_16x16x32_bf16 v[196:199], a[72:75], a[184:187], v[196:199]\n"
    "ds_read_b128 a[176:179], v242 offset:4096\n"
    "v_mul_f32_e32 v157, v157, v228\n"
    "v_mfma_f32_16x16x32_bf16 v[200:203], a[76:79], a[184:187], v[200:203]\n"
    "v_lshrrev_b32_e32 v247, 2, v250\n"
    "v_add_u32_e32 v247, s31, v247\n"
    "v_mfma_f32_16x16x32_bf16 v[204:207], a[80:83], a[184:187], v[204:207]\n"
    "s_waitcnt vmcnt(6)\n"
    "buffer_store_dword v254, off, s[8:11], s90 offset:0 sc1\n"
    "v_mfma_f32_16x16x32_bf16 v[208:211], a[84:87], a[184:187], v[208:211]\n"
    "v_add_u32_e32 v246, s47, v250\n"
    "v_mfma_f32_16x16x32_bf16 v[196:199], a[88:91], a[188:191], v[196:199]\n"
    "s_add_u32 s62, s58, 16384\n"
    "ds_read_b128 a[180:183], v243 offset:4096\n"
    "v_mul_f32_e32 v158, v158, v228\n"
    "v_mfma_f32_16x16x32_bf16 v[200:203], a[92:95], a[188:191], v[200:203]\n"
    "v_and_b32_e32 v245, 0x603f, v247\n"
    "v_mfma_f32_16x16x32_bf16 v[204:207], a[96:99], a[188:191], v[204:207]\n"
    "buffer_load_dwordx4 v[76:79], v250, s[8:11], s62 offen offset:0 sc1\n"
    "v_mfma_f32_16x16x32_bf16 v[208:211], a[100:103], a[188:191], v[208:211]\n"
    "s_waitcnt lgkmcnt(8)\n"
    "v_bfe_u32 v226, v250, 2, 4\n"
    "v_mfma_f32_16x16x32_bf16 v[196:199], a[104:107], a[48:51], v[196:199]\n"
    "ds_read_b128 a[184:187], v242 offset:36864\n"
    "v_mfma_f32_16x16x32_bf16 v[200:203], a[108:111], a[48:51], v[200:203]\n"
    "v_mul_f32_e32 v159, v159, v228\n"
    "v_mul_f32_e32 v160, v160, v228\n"
    "v_mfma_f32_16x16x32_bf16 v[204:207], a[112:115], a[48:51], v[204:207]\n"
    "s_waitcnt vmcnt(15)\n"
    "v_bfe_u32 v225, v250, 6, 2\n"
    "v_mfma_f32_16x16x32_bf16 v[208:211], a[116:119], a[48:51], v[208:211]\n"
    "v_lshl_or_b32 v226, v225, 7, v226\n"
    "v_and_b32_e32 v225, 0x60ff, v247\n"
    "v_mfma_f32_16x16x32_bf16 v[196:199], a[120:123], a[52:55], v[196:199]\n"
    "ds_read_b128 a[188:191], v243 offset:36864\n"
    "v_mfma_f32_16x16x32_bf16 v[200:203], a[124:127], a[52:55], v[200:203]\n"
    "buffer_load_dwordx4 v[96:99], v250, s[8:11], s62 offen offset:1024 sc1\n"
    "v_mfma_f32_16x16x32_bf16 v[204:207], a[128:131], a[52:55], v[204:207]\n"
    "v_mul_f32_e32 v161, v161, v228\n"
    "v_mfma_f32_16x16x32_bf16 v[208:211], a[132:135], a[52:55], v[208:211]\n"
    "s_waitcnt lgkmcnt(8)\n"
    "v_mul_f32_e32 v162, v162, v228\n"
    "v_mfma_f32_16x16x32_bf16 v[196:199], a[136:139], a[56:59], v[196:199]\n"
    "v_mfma_f32_16x16x32_bf16 v[200:203], a[140:143], a[56:59], v[200:203]\n"
    "buffer_load_dwordx4 a[192:195], v250, s[8:11], s62 offen offset:2048 sc1\n"
    "v_mfma_f32_16x16x32_bf16 v[204:207], a[144:147], a[56:59], v[204:207]\n"
    "v_mul_f32_e32 v163, v163, v228\n"
    "v_mfma_f32_16x16x32_bf16 v[208:211], a[148:151], a[56:59], v[208:211]\n"
    "v_mfma_f32_16x16x32_bf16 v[196:199], a[152:155], a[60:63], v[196:199]\n"
    "ds_read_b128 a[48:51], v251 offset:6144\n"
    "v_mfma_f32_16x16x32_bf16 v[200:203], a[156:159], a[60:63], v[200:203]\n"
    "buffer_load_dwordx4 a[196:199], v250, s[8:11], s62 offen offset:3072 sc1\n"
    "v_mfma_f32_16x16x32_bf16 v[204:207], v[180:183], a[60:63], v[204:207]\n"
    "s_add_u32 s62, s58, 20480\n"
    "v_mul_f32_e32 v164, v164, v228\n"
    "v_mfma_f32_16x16x32_bf16 v[208:211], v[184:187], a[60:63], v[208:211]\n"
    "s_waitcnt lgkmcnt(7)\n"
    "ds_read_b128 a[52:55], v249 offset:6144\n"
    "v_mul_f32_e32 v165, v165, v228\n"
    "v_mfma_f32_16x16x32_bf16 v[172:175], a[216:219], a[160:163], 0\n"
    "v_mfma_f32_16x16x32_bf16 v[176:179], a[220:223], a[160:163], 0\n"
    "buffer_load_dwordx4 a[200:203], v250, s[8:11], s62 offen offset:0 sc1\n"
    "v_mfma_f32_16x16x32_bf16 v[212:215], a[224:227], a[160:163], 0\n"
    "v_mfma_f32_16x16x32_bf16 v[216:219], a[228:231], a[160:163], 0\n"
    "v_mfma_f32_16x16x32_bf16 v[172:175], a[232:235], a[164:167], v[172:175]\n"
    "v_mul_f32_e32 v166, v166, v228\n"
    "v_mfma_f32_16x16x32_bf16 v[176:179], v[192:195], a[164:167], v[176:179]\n"
    "buffer_load_dwordx4 a[204:207], v250, s[8:11], s62 offen offset:1024 sc1\n"
    "v_mfma_f32_16x16x32_bf16 v[212:215], a[64:67], a[164:167], v[212:215]\n"
    "v_mfma_f32_16x16x32_bf16 v[216:219], a[68:71], a[164:167], v[216:219]\n"
    "s_waitcnt lgkmcnt(6)\n"
    "v_mfma_f32_16x16x32_bf16 v[172:175], a[72:75], a[168:171], v[172:175]\n"
    "v_mfma_f32_16x16x32_bf16 v[176:179], a[76:79], a[168:171], v[176:179]\n"
    "buffer_load_dwordx4 a[208:211], v250, s[8:11], s62 offen offset:2048 sc1\n"
    "v_mfma_f32_16x16x32_bf16 v[212:215], a[80:83], a[168:171], v[212:215]\n"
    "v_mfma_f32_16x16x32_bf16 v[216:219], a[84:87], a[168:171], v[216:219]\n"
    "v_mfma_f32_16x16x32_bf16 v[172:175], a[88:91], a[172:175], v[172:175]\n"
    "v_mul_f32_e32 v167, v167, v228\n"
    "v_mul_f32_e32 v168, v168, v228\n"
    "v_mfma_f32_16x16x32_bf16 v[176:179], a[92:95], a[172:175], v[176:179]\n"
    "buffer_load_dwordx4 a[212:215], v250, s[8:11], s62 offen offset:3072 sc1\n"
    "v_mfma_f32_16x16x32_bf16 v[212:215], a[96:99], a[172:175], v[212:215]\n"
    "s_add_u32 s62, s58, 24576\n"
    "v_mul_f32_e32 v169, v169, v228\n"
    "v_mfma_f32_16x16x32_bf16 v[216:219], a[100:103], a[172:175], v[216:219]\n"
    "s_waitcnt vmcnt(25)\n"
    "v_mul_f32_e32 v170, v170, v228\n"
    "v_mul_f32_e32 v171, v171, v228\n"
    "v_mfma_f32_16x16x32_bf16 a[236:239], v[234:237], a[0:3], a[236:239]\n"
    "v_mfma_f32_16x16x32_bf16 v[0:3], v[232:235], a[0:3], v[0:3]\n"
    "buffer_load_dwordx4 v[108:111], v250, s[8:11], s62 offen offset:0 sc1\n"
    "v_mfma_f32_16x16x32_bf16 a[236:239], v[234:237], a[4:7], a[236:239]\n"
    "v_cmp_le_u32_e32 vcc, 0x300, v250\n"
    "v_mfma_f32_16x16x32_bf16 v[0:3], v[232:235], a[4:7], v[0:3]\n"
    "v_mfma_f32_16x16x32_bf16 a[236:239], v[234:237], a[8:11], a[236:239]\n"
    "v_cndmask_b32_e32 v227, v220, v227, vcc\n"
    "v_mfma_f32_16x16x32_bf16 v[0:3], v[232:235], a[8:11], v[0:3]\n"
    "s_waitcnt lgkmcnt(0)\n"
    "s_waitcnt vmcnt(21)\n"
    "v_mfma_f32_16x16x32_bf16 v[20:23], v[234:237], a[12:15], v[20:23]\n"
    "s_barrier\n"
    "v_mfma_f32_16x16x32_bf16 v[40:43], v[232:235], a[12:15], v[40:43]\n"
    "buffer_load_dwordx4 v[112:115], v250, s[8:11], s62 offen offset:1024 sc1\n"
    "v_mfma_f32_16x16x32_bf16 v[20:23], v[234:237], a[16:19], v[20:23]\n"
    "v_accvgpr_read_b32 v64, a236\n"
    "v_accvgpr_read_b32 v65, a237\n"
    "v_mfma_f32_16x16x32_bf16 v[40:43], v[232:235], a[16:19], v[40:43]\n"
    "v_accvgpr_read_b32 v66, a238\n"
    "v_accvgpr_read_b32 v67, a239\n"
    "v_mfma_f32_16x16x32_bf16 v[20:23], v[234:237], a[20:23], v[20:23]\n"
    "ds_write_b32 v247, v227 offset:0\n"
    "v_mfma_f32_16x16x32_bf16 v[40:43], v[232:235], a[20:23], v[40:43]\n"
    "s_waitcnt vmcnt(14)\n"
    "buffer_load_dwordx4 v[132:135], v250, s[8:11], s62 offen offset:2048 sc1\n"
    "v_cvt_pk_bf16_f32 v64, v64, v65\n"
    "v_mfma_f32_16x16x32_bf16 v[60:63], v[234:237], a[24:27], v[60:63]\n"
    "v_cvt_pk_bf16_f32 v65, v66, v67\n"
    "v_cvt_pk_bf16_f32 v66, v0, v1\n"
    "v_cvt_pk_bf16_f32 v67, v2, v3\n"
    "v_mfma_f32_16x16x32_bf16 v[80:83], v[232:235], a[24:27], v[80:83]\n"
    "ds_write_b128 v244, v[64:67] offset:0\n"
    "v_mfma_f32_16x16x32_bf16 v[60:63], v[234:237], a[28:31], v[60:63]\n"
    "buffer_load_dword v253, v226, s[8:11], s61 offen offset:0 sc1\n"
    "v_mfma_f32_16x16x32_bf16 v[80:83], v[232:235], a[28:31], v[80:83]\n"
    "v_cvt_pk_bf16_f32 v20, v20, v21\n"
    "v_cvt_pk_bf16_f32 v21, v22, v23\n"
    "v_mfma_f32_16x16x32_bf16 v[60:63], v[234:237], a[32:35], v[60:63]\n"
    "buffer_load_dwordx4 v[116:119], v250, s[8:11], s62 offen offset:3072 sc1\n"
    "v_mfma_f32_16x16x32_bf16 v[80:83], v[232:235], a[32:35], v[80:83]\n"
    "s_add_u32 s62, s58, 28672\n"
    "s_waitcnt vmcnt(14)\n"
    "v_cvt_pk_bf16_f32 v22, v40, v41\n"
    "v_mfma_f32_16x16x32_bf16 v[100:103], v[234:237], a[36:39], v[100:103]\n"
    "v_cvt_pk_bf16_f32 v23, v42, v43\n"
    "v_mfma_f32_16x16x32_bf16 v[120:123], v[232:235], a[36:39], v[120:123]\n"
    "ds_write_b128 v244, v[20:23] offset:1024\n"
    "v_mfma_f32_16x16x32_bf16 v[100:103], v[234:237], a[40:43], v[100:103]\n"
    "buffer_load_dwordx4 v[136:139], v250, s[8:11], s62 offen offset:0 sc1\n"
    "v_mfma_f32_16x16x32_bf16 v[120:123], v[232:235], a[40:43], v[120:123]\n"
    "v_cvt_pk_bf16_f32 v60, v60, v61\n"
    "v_cvt_pk_bf16_f32 v61, v62, v63\n"
    "v_mfma_f32_16x16x32_bf16 v[100:103], v[234:237], a[44:47], v[100:103]\n"
    "v_cvt_pk_bf16_f32 v62, v80, v81\n"
    "v_cvt_pk_bf16_f32 v63, v82, v83\n"
    "v_mfma_f32_16x16x32_bf16 v[120:123], v[232:235], a[44:47], v[120:123]\n"
    "ds_write_b128 v244, v[60:63] offset:2048\n"
    "v_mfma_f32_16x16x32_bf16 v[172:175], a[104:107], a[176:179], v[172:175]\n"
    "buffer_load_dwordx4 a[240:243], v250, s[8:11], s62 offen offset:1024 sc1\n"
    "v_cvt_pk_bf16_f32 v100, v100, v101\n"
    "v_mfma_f32_16x16x32_bf16 v[176:179], a[108:111], a[176:179], v[176:179]\n"
    "v_cvt_pk_bf16_f32 v101, v102, v103\n"
    "v_cvt_pk_bf16_f32 v102, v120, v121\n"
    "v_cvt_pk_bf16_f32 v103, v122, v123\n"
    "v_mfma_f32_16x16x32_bf16 v[212:215], a[112:115], a[176:179], v[212:215]\n"
    "ds_write_b128 v244, v[100:103] offset:3072\n"
    "v_mfma_f32_16x16x32_bf16 v[216:219], a[116:119], a[176:179], v[216:219]\n"
    "v_mfma_f32_16x16x32_bf16 v[172:175], a[120:123], a[180:183], v[172:175]\n"
    "buffer_load_dwordx4 v[4:7], v250, s[8:11], s62 offen offset:2048 sc1\n"
    "v_mfma_f32_16x16x32_bf16 v[176:179], a[124:127], a[180:183], v[176:179]\n"
    "s_waitcnt lgkmcnt(0)\n"
    "v_mfma_f32_16x16x32_bf16 v[212:215], a[128:131], a[180:183], v[212:215]\n"
    "s_barrier\n"
    "v_mfma_f32_16x16x32_bf16 v[216:219], a[132:135], a[180:183], v[216:219]\n"
    "ds_read_b128 a[0:3], v250 offset:0\n"
    "ds_read_b128 a[4:7], v250 offset:1024\n"
    "ds_read_b128 a[8:11], v250 offset:2048\n"
    "v_mfma_f32_16x16x32_bf16 v[172:175], a[136:139], a[184:187], v[172:175]\n"
    "buffer_load_dwordx4 v[24:27], v250, s[8:11], s62 offen offset:3072 sc1\n"
    "ds_read_b128 a[12:15], v250 offset:3072\n"
    "v_mfma_f32_16x16x32_bf16 v[176:179], a[140:143], a[184:187], v[176:179]\n"
    "ds_read_b128 a[16:19], v250 offset:4096\n"
    "ds_read_b128 a[20:23], v250 offset:5120\n"
    "v_mfma_f32_16x16x32_bf16 v[212:215], a[144:147], a[184:187], v[212:215]\n"
    "ds_read_b128 a[24:27], v250 offset:6144\n"
    "ds_read_b128 a[28:31], v250 offset:7168\n"
    "ds_read2_b32 v[220:221], v245 offset0:0 offset1:16\n"
    "v_mfma_f32_16x16x32_bf16 v[216:219], a[148:151], a[184:187], v[216:219]\n"
    "ds_read2_b32 v[222:223], v245 offset0:32 offset1:48\n"
    "ds_read2_b32 v[80:81], v245 offset0:64 offset1:80\n"
    "ds_read2_b32 v[82:83], v245 offset0:96 offset1:112\n"
    "v_mfma_f32_16x16x32_bf16 v[172:175], a[152:155], a[188:191], v[172:175]\n"
    "v_mfma_f32_16x16x32_bf16 v[176:179], a[156:159], a[188:191], v[176:179]\n"
    "v_mfma_f32_16x16x32_bf16 v[212:215], v[180:183], a[188:191], v[212:215]\n"
    "v_mul_f32_e32 v196, v196, v229\n"
    "v_mul_f32_e32 v197, v197, v229\n"
    "v_mfma_f32_16x16x32_bf16 v[216:219], v[184:187], a[188:191], v[216:219]\n"
    "v_mul_f32_e32 v198, v198, v229\n"
    "v_mfma_f32_16x16x32_bf16 v[156:159], v[234:237], a[48:51], v[156:159]\n"
    "v_mul_f32_e32 v199, v199, v229\n"
    "v_mfma_f32_16x16x32_bf16 v[160:163], v[232:235], a[48:51], v[160:163]\n"
    "v_mul_f32_e32 v200, v200, v229\n"
    "v_mfma_f32_16x16x32_bf16 v[164:167], v[234:237], a[52:55], v[164:167]\n"
    "v_mul_f32_e32 v201, v201, v229\n"
    "v_mul_f32_e32 v202, v202, v229\n"
    "v_mfma_f32_16x16x32_bf16 v[168:171], v[232:235], a[52:55], v[168:171]\n"
    "s_waitcnt lgkmcnt(2)\n"
    "ds_read_b128 a[64:67], v250 offset:8192\n"
    "v_mfma_f32_16x16x32_bf16 v[140:143], a[244:247], a[0:3], 0\n"
    "ds_read_b128 a[68:71], v250 offset:9216\n"
    "v_mul_f32_e32 v203, v203, v229\n"
    "v_mfma_f32_16x16x32_bf16 v[144:147], v[8:11], a[0:3], 0\n"
    "ds_read_b128 a[72:75], v250 offset:10240\n"
    "v_add_f32_e32 v220, v220, v221\n"
    "v_mfma_f32_16x16x32_bf16 v[148:151], a[248:251], a[0:3], 0\n"
    "ds_read_b128 a[76:79], v250 offset:11264\n"
    "v_add_f32_e32 v222, v222, v223\n"
    "v_mfma_f32_16x16x32_bf16 v[152:155], v[12:15], a[0:3], 0\n"
    "s_waitcnt vmcnt(5)\n"
    "v_subrev_u32_e32 v253, s39, v253\n"
    "v_cmp_lt_u32_e32 vcc, 1, v253\n"
    "v_mfma_f32_16x16x32_bf16 v[140:143], a[252:255], a[4:7], v[140:143]\n"
    "ds_read_b128 a[80:83], v250 offset:12288\n"
    "v_mul_f32_e32 v204, v204, v229\n"
    "v_mfma_f32_16x16x32_bf16 v[144:147], v[16:19], a[4:7], v[144:147]\n"
    "ds_read_b128 a[84:87], v250 offset:13312\n"
    "v_mul_f32_e32 v205, v205, v229\n"
    "v_mfma_f32_16x16x32_bf16 v[148:151], v[28:31], a[4:7], v[148:151]\n"
    "s_cbranch_vccnz .Lat_ppoll\n"
    ".Lat_pok:\n"
    "v_cmp_eq_u32_e32 vcc, 1, v253\n"
    "v_mfma_f32_16x16x32_bf16 v[152:155], v[48:51], a[4:7], v[152:155]\n"
    "buffer_load_dwordx4 a[128:131], v250, s[8:11], s54 offen offset:0 sc1\n"
    "v_mfma_f32_16x16x32_bf16 v[140:143], v[32:35], a[8:11], v[140:143]\n"
    "v_mul_f32_e32 v206, v206, v229\n"
    "v_mul_f32_e32 v207, v207, v229\n"
    "v_mfma_f32_16x16x32_bf16 v[144:147], v[52:55], a[8:11], v[144:147]\n"
    "s_cbranch_vccnz .Lat_redo\n"
    "v_mfma_f32_16x16x32_bf16 v[148:151], v[36:39], a[8:11], v[148:151]\n"
    "buffer_load_dwordx4 a[132:135], v250, s[8:11], s54 offen offset:1024 sc1\n"
    "v_mfma_f32_16x16x32_bf16 v[152:155], v[56:59], a[8:11], v[152:155]\n"
    "s_addk_i32 s54, 8192\n"
    "ds_read_b128 a[88:91], v250 offset:14336\n"
    "v_mfma_f32_16x16x32_bf16 v[140:143], v[68:71], a[12:15], v[140:143]\n"
    "buffer_load_dwordx4 a[136:139], v250, s[8:11], s54 offen offset:0 sc1\n"
    "v_mfma_f32_16x16x32_bf16 v[144:147], v[88:91], a[12:15], v[144:147]\n"
    "ds_read_b128 a[92:95], v250 offset:15360\n"
    "v_mul_f32_e32 v208, v208, v229\n"
    "v_mfma_f32_16x16x32_bf16 v[148:151], v[72:75], a[12:15], v[148:151]\n"
    "s_waitcnt vmcnt(3)\n"
    "buffer_load_dwordx4 a[140:143], v250, s[8:11], s54 offen offset:1024 sc1\n"
    "v_mfma_f32_16x16x32_bf16 v[152:155], v[92:95], a[12:15], v[152:155]\n"
    "s_addk_i32 s54, 8192\n"
    "v_add_f32_e32 v220, v220, v222\n"
    "v_mfma_f32_16x16x32_bf16 v[140:143], v[76:79], a[16:19], v[140:143]\n"
    "buffer_load_dwordx4 a[144:147], v250, s[8:11], s54 offen offset:0 sc1\n"
    "v_mfma_f32_16x16x32_bf16 v[144:147], v[96:99], a[16:19], v[144:147]\n"
    "ds_read_b128 a[56:59], v252 offset:30720\n"
    "v_mfma_f32_16x16x32_bf16 v[148:151], a[192:195], a[16:19], v[148:151]\n"
    "buffer_load_dwordx4 a[148:151], v250, s[8:11], s54 offen offset:1024 sc1\n"
    "v_mfma_f32_16x16x32_bf16 v[152:155], a[196:199], a[16:19], v[152:155]\n"
    "s_addk_i32 s54, 8192\n"
    "ds_read_b128 a[60:63], v255 offset:30720\n"
    "v_mul_f32_e32 v209, v209, v229\n"
    "v_mfma_f32_16x16x32_bf16 v[140:143], a[200:203], a[20:23], v[140:143]\n"
    "buffer_load_dwordx4 a[152:155], v250, s[8:11], s54 offen offset:0 sc1\n"
    "v_mfma_f32_16x16x32_bf16 v[144:147], a[204:207], a[20:23], v[144:147]\n"
    "v_mul_f32_e32 v172, v172, v230\n"
    "v_mul_f32_e32 v173, v173, v230\n"
    "v_mfma_f32_16x16x32_bf16 v[148:151], a[208:211], a[20:23], v[148:151]\n"
    "buffer_load_dwordx4 a[156:159], v250, s[8:11], s54 offen offset:1024 sc1\n"
    "v_mfma_f32_16x16x32_bf16 v[152:155], a[212:215], a[20:23], v[152:155]\n"
    "s_waitcnt lgkmcnt(10)\n"
    "v_mul_f32_e32 v210, v210, v229\n"
    "v_mul_f32_e32 v211, v211, v229\n"
    "v_mfma_f32_16x16x32_bf16 v[140:143], v[108:111], a[24:27], v[140:143]\n"
    "buffer_load_dword v227, v248, s[8:11], s45 offen offset:1088 sc1\n"
    "v_mfma_f32_16x16x32_bf16 v[144:147], v[112:115], a[24:27], v[144:147]\n"
    "v_mfma_f32_16x16x32_bf16 v[148:151], v[132:135], a[24:27], v[148:151]\n"
    "v_mfma_f32_16x16x32_bf16 v[152:155], v[116:119], a[24:27], v[152:155]\n"
    "v_rcp_f32_e32 v220, v220\n"
    "v_mfma_f32_16x16x32_bf16 v[140:143], v[136:139], a[28:31], v[140:143]\n"
    "v_add_f32_e32 v80, v80, v81\n"
    "v_add_f32_e32 v82, v82, v83\n"
    "v_mfma_f32_16x16x32_bf16 v[144:147], a[240:243], a[28:31], v[144:147]\n"
    "ds_read_b128 a[160:163], v252 offset:4096\n"
    "v_add_f32_e32 v80, v80, v82\n"
    "v_mfma_f32_16x16x32_bf16 v[148:151], v[4:7], a[28:31], v[148:151]\n"
    "v_rcp_f32_e32 v80, v80\n"
    "v_mfma_f32_16x16x32_bf16 v[152:155], v[24:27], a[28:31], v[152:155]\n"
    "s_waitcnt lgkmcnt(9)\n"
    "ds_read_b128 a[164:167], v255 offset:4096\n"
    "v_mfma_f32_16x16x32_bf16 v[180:183], a[244:247], a[64:67], 0\n"
    "v_mul_f32_e32 v174, v174, v230\n"
    "v_mfma_f32_16x16x32_bf16 v[184:187], v[8:11], a[64:67], 0\n"
    "v_fma_f32 v140, v140, v220, v156\n"
    "v_mfma_f32_16x16x32_bf16 v[188:191], a[248:251], a[64:67], 0\n"
    "v_fma_f32 v141, v141, v220, v157\n"
    "v_fma_f32 v142, v142, v220, v158\n"
    "v_mfma_f32_16x16x32_bf16 v[192:195], v[12:15], a[64:67], 0\n"
    "v_mul_f32_e32 v175, v175, v230\n"
    "v_mfma_f32_16x16x32_bf16 v[180:183], a[252:255], a[68:71], v[180:183]\n"
    "v_fma_f32 v143, v143, v220, v159\n"
    "v_mfma_f32_16x16x32_bf16 v[184:187], v[16:19], a[68:71], v[184:187]\n"
    "v_fma_f32 v144, v144, v220, v160\n"
    "v_mfma_f32_16x16x32_bf16 v[188:191], v[28:31], a[68:71], v[188:191]\n"
    "v_fma_f32 v145, v145, v220, v161\n"
    "v_mfma_f32_16x16x32_bf16 v[192:195], v[48:51], a[68:71], v[192:195]\n"
    "s_waitcnt lgkmcnt(8)\n"
    "v_fma_f32 v146, v146, v220, v162\n"
    "v_mfma_f32_16x16x32_bf16 v[180:183], v[32:35], a[72:75], v[180:183]\n"
    "v_mul_f32_e32 v176, v176, v230\n"
    "v_fma_f32 v147, v147, v220, v163\n"
    "v_mfma_f32_16x16x32_bf16 v[184:187], v[52:55], a[72:75], v[184:187]\n"
    "v_cvt_pk_bf16_f32 v140, v140, v141\n"
    "v_mfma_f32_16x16x32_bf16 v[188:191], v[36:39], a[72:75], v[188:191]\n"
    "v_cvt_pk_bf16_f32 v141, v142, v143\n"
    "v_cvt_pk_bf16_f32 v142, v144, v145\n"
    "v_mfma_f32_16x16x32_bf16 v[192:195], v[56:59], a[72:75], v[192:195]\n"
    "v_cvt_pk_bf16_f32 v143, v146, v147\n"
    "v_mfma_f32_16x16x32_bf16 v[180:183], v[68:71], a[76:79], v[180:183]\n"
    "buffer_store_dwordx4 v[140:143], v239, s[16:19], s80 offen offset:0 sc0 sc1\n"
    "v_mfma_f32_16x16x32_bf16 v[184:187], v[88:91], a[76:79], v[184:187]\n"
    "v_fma_f32 v148, v148, v220, v164\n"
    "v_mfma_f32_16x16x32_bf16 v[188:191], v[72:75], a[76:79], v[188:191]\n"
    "v_fma_f32 v149, v149, v220, v165\n"
    "v_mfma_f32_16x16x32_bf16 v[192:195], v[92:95], a[76:79], v[192:195]\n"
    "s_waitcnt lgkmcnt(6)\n"
    "v_fma_f32 v150, v150, v220, v166\n"
    "v_fma_f32 v151, v151, v220, v167\n"
    "v_mfma_f32_16x16x32_bf16 v[180:183], v[76:79], a[80:83], v[180:183]\n"
    "v_fma_f32 v152, v152, v220, v168\n"
    "v_fma_f32 v153, v153, v220, v169\n"
    "v_mfma_f32_16x16x32_bf16 v[184:187], v[96:99], a[80:83], v[184:187]\n"
    "v_fma_f32 v154, v154, v220, v170\n"
    "v_fma_f32 v155, v155, v220, v171\n"
    "v_mfma_f32_16x16x32_bf16 v[188:191], a[192:195], a[80:83], v[188:191]\n"
    "v_cvt_pk_bf16_f32 v148, v148, v149\n"
    "v_cvt_pk_bf16_f32 v149, v150, v151\n"
    "v_mfma_f32_16x16x32_bf16 v[192:195], a[196:199], a[80:83], v[192:195]\n"
    "v_cvt_pk_bf16_f32 v150, v152, v153\n"
    "v_mfma_f32_16x16x32_bf16 v[180:183], a[200:203], a[84:87], v[180:183]\n"
    "v_cvt_pk_bf16_f32 v151, v154, v155\n"
    "v_mfma_f32_16x16x32_bf16 v[184:187], a[204:207], a[84:87], v[184:187]\n"
    "buffer_store_dwordx4 v[148:151], v239, s[16:19], s80 offen offset:64 sc0 sc1\n"
    "v_mfma_f32_16x16x32_bf16 v[188:191], a[208:211], a[84:87], v[188:191]\n"
    "v_mfma_f32_16x16x32_bf16 v[192:195], a[212:215], a[84:87], v[192:195]\n"
    "s_waitcnt lgkmcnt(4)\n"
    "v_mul_f32_e32 v177, v177, v230\n"
    "v_mfma_f32_16x16x32_bf16 v[180:183], v[108:111], a[88:91], v[180:183]\n"
    "v_mul_f32_e32 v178, v178, v230\n"
    "v_mfma_f32_16x16x32_bf16 v[184:187], v[112:115], a[88:91], v[184:187]\n"
    "v_mul_f32_e32 v179, v179, v230\n"
    "v_mfma_f32_16x16x32_bf16 v[188:191], v[132:135], a[88:91], v[188:191]\n"
    "v_mul_f32_e32 v212, v212, v230\n"
    "v_mfma_f32_16x16x32_bf16 v[192:195], v[116:119], a[88:91], v[192:195]\n"
    "s_waitcnt vmcnt(9)\n"
    "v_mfma_f32_16x16x32_bf16 v[64:67], v[234:237], a[128:131], 0\n"
    "v_mfma_f32_16x16x32_bf16 v[84:87], v[232:235], a[128:131], 0\n"
    "v_mfma_f32_16x16x32_bf16 v[104:107], v[234:237], a[132:135], 0\n"
    "v_mul_f32_e32 v213, v213, v230\n"
    "v_mfma_f32_16x16x32_bf16 v[124:127], v[232:235], a[132:135], 0\n"
    "s_waitcnt vmcnt(7)\n"
    "v_mfma_f32_16x16x32_bf16 v[64:67], v[234:237], a[136:139], v[64:67]\n"
    "v_mfma_f32_16x16x32_bf16 v[84:87], v[232:235], a[136:139], v[84:87]\n"
    "v_mul_f32_e32 v214, v214, v230\n"
    "v_mfma_f32_16x16x32_bf16 v[104:107], v[234:237], a[140:143], v[104:107]\n"
    "v_mfma_f32_16x16x32_bf16 v[124:127], v[232:235], a[140:143], v[124:127]\n"
    "s_waitcnt vmcnt(5)\n"
    "v_mfma_f32_16x16x32_bf16 v[64:67], v[234:237], a[144:147], v[64:67]\n"
    "v_mfma_f32_16x16x32_bf16 v[84:87], v[232:235], a[144:147], v[84:87]\n"
    "v_mul_f32_e32 v215, v215, v230\n"
    "v_mfma_f32_16x16x32_bf16 v[104:107], v[234:237], a[148:151], v[104:107]\n"
    "v_mul_f32_e32 v216, v216, v230\n"
    "v_mul_f32_e32 v217, v217, v230\n"
    "v_mfma_f32_16x16x32_bf16 v[124:127], v[232:235], a[148:151], v[124:127]\n"
    "s_waitcnt vmcnt(3)\n"
    "v_mul_f32_e32 v218, v218, v230\n"
    "v_mfma_f32_16x16x32_bf16 v[64:67], v[234:237], a[152:155], v[64:67]\n"
    "s_waitcnt vmcnt(2)\n"
    "ds_write_b32 v225, v227 offset:512\n"
    "v_mfma_f32_16x16x32_bf16 v[84:87], v[232:235], a[152:155], v[84:87]\n"
    "v_mul_f32_e32 v219, v219, v230\n"
    "v_mfma_f32_16x16x32_bf16 v[104:107], v[234:237], a[156:159], v[104:107]\n"
    "v_mfma_f32_16x16x32_bf16 v[124:127], v[232:235], a[156:159], v[124:127]\n"
    "v_cvt_pk_bf16_f32 v64, v64, v65\n"
    "v_cvt_pk_bf16_f32 v65, v66, v67\n"
    "v_mfma_f32_16x16x32_bf16 v[180:183], v[136:139], a[92:95], v[180:183]\n"
    "v_cvt_pk_bf16_f32 v66, v84, v85\n"
    "v_cvt_pk_bf16_f32 v67, v86, v87\n"
    "v_mfma_f32_16x16x32_bf16 v[184:187], a[240:243], a[92:95], v[184:187]\n"
    "ds_write_b128 v246, v[64:67] offset:16384\n"
    "v_mfma_f32_16x16x32_bf16 v[188:191], v[4:7], a[92:95], v[188:191]\n"
    "v_cvt_pk_bf16_f32 v104, v104, v105\n"
    "v_cvt_pk_bf16_f32 v105, v106, v107\n"
    "v_mfma_f32_16x16x32_bf16 v[192:195], v[24:27], a[92:95], v[192:195]\n"
    "s_waitcnt lgkmcnt(4)\n"
    "v_cvt_pk_bf16_f32 v106, v124, v125\n"
    "v_cvt_pk_bf16_f32 v107, v126, v127\n"
    "v_mfma_f32_16x16x32_bf16 v[196:199], v[234:237], a[56:59], v[196:199]\n"
    "ds_write_b128 v246, v[104:107] offset:17408\n"
    "v_mfma_f32_16x16x32_bf16 v[200:203], v[232:235], a[56:59], v[200:203]\n"
    "v_mfma_f32_16x16x32_bf16 v[204:207], v[234:237], a[60:63], v[204:207]\n"
    "s_waitcnt lgkmcnt(0)\n"
    "v_mfma_f32_16x16x32_bf16 v[208:211], v[232:235], a[60:63], v[208:211]\n"
    "s_barrier\n"
    "v_mfma_f32_16x16x32_bf16 v[172:175], v[234:237], a[160:163], v[172:175]\n"
    "ds_read_b128 a[96:99], v250 offset:16384\n"
    "v_mfma_f32_16x16x32_bf16 v[176:179], v[232:235], a[160:163], v[176:179]\n"
    "ds_read_b128 a[100:103], v250 offset:17408\n"
    "v_mfma_f32_16x16x32_bf16 v[212:215], v[234:237], a[164:167], v[212:215]\n"
    "ds_read_b128 a[104:107], v250 offset:18432\n"
    "ds_read_b128 a[108:111], v250 offset:19456\n"
    "v_mfma_f32_16x16x32_bf16 v[216:219], v[232:235], a[164:167], v[216:219]\n"
    "s_waitcnt lgkmcnt(2)\n"
    "ds_read_b128 a[112:115], v250 offset:20480\n"
    "ds_read_b128 a[116:119], v250 offset:21504\n"
    "v_mfma_f32_16x16x32_bf16 v[0:3], a[244:247], a[96:99], 0\n"
    "ds_read_b128 a[120:123], v250 offset:22528\n"
    "ds_read_b128 a[124:127], v250 offset:23552\n"
    "v_mfma_f32_16x16x32_bf16 v[20:23], v[8:11], a[96:99], 0\n"
    "ds_read2_b32 v[220:221], v245 offset0:128 offset1:144\n"
    "ds_read2_b32 v[222:223], v245 offset0:160 offset1:176\n"
    "v_fma_f32 v180, v180, v80, v196\n"
    "v_mfma_f32_16x16x32_bf16 v[0:3], a[252:255], a[100:103], v[0:3]\n"
    "v_fma_f32 v181, v181, v80, v197\n"
    "v_fma_f32 v182, v182, v80, v198\n"
    "v_mfma_f32_16x16x32_bf16 v[20:23], v[16:19], a[100:103], v[20:23]\n"
    "s_waitcnt lgkmcnt(6)\n"
    "v_fma_f32 v183, v183, v80, v199\n"
    "v_fma_f32 v184, v184, v80, v200\n"
    "v_mfma_f32_16x16x32_bf16 v[0:3], v[32:35], a[104:107], v[0:3]\n"
    "s_or_b32 s45, s34, s93\n"
    "s_cselect_b32 s45, 2.0, 6101760\n"
    "v_fma_f32 v185, v185, v80, v201\n"
    "v_mfma_f32_16x16x32_bf16 v[20:23], v[52:55], a[104:107], v[20:23]\n"
    "v_fma_f32 v186, v186, v80, v202\n"
    "v_fma_f32 v187, v187, v80, v203\n"
    "v_mfma_f32_16x16x32_bf16 v[0:3], v[68:71], a[108:111], v[0:3]\n"
    "v_cvt_pk_bf16_f32 v180, v180, v181\n"
    "v_cvt_pk_bf16_f32 v181, v182, v183\n"
    "v_mfma_f32_16x16x32_bf16 v[20:23], v[88:91], a[108:111], v[20:23]\n"
    "s_waitcnt lgkmcnt(4)\n"
    "v_cvt_pk_bf16_f32 v182, v184, v185\n"
    "v_cvt_pk_bf16_f32 v183, v186, v187\n"
    "v_mfma_f32_16x16x32_bf16 v[0:3], v[76:79], a[112:115], v[0:3]\n"
    "buffer_store_dwordx4 v[180:183], v239, s[16:19], s82 offen offset:0 sc0 sc1\n"
    "v_mfma_f32_16x16x32_bf16 v[20:23], v[96:99], a[112:115], v[20:23]\n"
    "v_fma_f32 v188, v188, v80, v204\n"
    "v_fma_f32 v189, v189, v80, v205\n"
    "v_mfma_f32_16x16x32_bf16 v[0:3], a[200:203], a[116:119], v[0:3]\n"
    "v_fma_f32 v190, v190, v80, v206\n"
    "v_fma_f32 v191, v191, v80, v207\n"
    "v_mfma_f32_16x16x32_bf16 v[20:23], a[204:207], a[116:119], v[20:23]\n"
    "s_waitcnt lgkmcnt(0)\n"
    "v_fma_f32 v192, v192, v80, v208\n"
    "v_fma_f32 v193, v193, v80, v209\n"
    "v_mfma_f32_16x16x32_bf16 v[0:3], v[108:111], a[120:123], v[0:3]\n"
    "v_fma_f32 v194, v194, v80, v210\n"
    "v_fma_f32 v195, v195, v80, v211\n"
    "v_mfma_f32_16x16x32_bf16 v[20:23], v[112:115], a[120:123], v[20:23]\n"
    "v_cvt_pk_bf16_f32 v188, v188, v189\n"
    "v_cvt_pk_bf16_f32 v189, v190, v191\n"
    "v_mfma_f32_16x16x32_bf16 v[0:3], v[136:139], a[124:127], v[0:3]\n"
    "v_cvt_pk_bf16_f32 v190, v192, v193\n"
    "v_cvt_pk_bf16_f32 v191, v194, v195\n"
    "v_mfma_f32_16x16x32_bf16 v[20:23], a[240:243], a[124:127], v[20:23]\n"
    "buffer_store_dwordx4 v[188:191], v239, s[16:19], s82 offen offset:64 sc0 sc1\n"
    "v_mfma_f32_16x16x32_bf16 v[40:43], a[248:251], a[96:99], 0\n"
    "v_add_f32_e32 v220, v220, v221\n"
    "v_add_f32_e32 v222, v222, v223\n"
    "v_add_f32_e32 v220, v220, v222\n"
    "v_mfma_f32_16x16x32_bf16 v[60:63], v[12:15], a[96:99], 0\n"
    "v_rcp_f32_e32 v220, v220\n"
    "v_mfma_f32_16x16x32_bf16 v[40:43], v[28:31], a[100:103], v[40:43]\n"
    "v_fma_f32 v0, v0, v220, v172\n"
    "v_fma_f32 v1, v1, v220, v173\n"
    "v_mfma_f32_16x16x32_bf16 v[60:63], v[48:51], a[100:103], v[60:63]\n"
    "v_fma_f32 v2, v2, v220, v174\n"
    "v_mfma_f32_16x16x32_bf16 v[40:43], v[36:39], a[104:107], v[40:43]\n"
    "v_fma_f32 v3, v3, v220, v175\n"
    "v_mfma_f32_16x16x32_bf16 v[60:63], v[56:59], a[104:107], v[60:63]\n"
    "v_fma_f32 v20, v20, v220, v176\n"
    "v_fma_f32 v21, v21, v220, v177\n"
    "v_mfma_f32_16x16x32_bf16 v[40:43], v[72:75], a[108:111], v[40:43]\n"
    "v_fma_f32 v22, v22, v220, v178\n"
    "v_fma_f32 v23, v23, v220, v179\n"
    "v_mfma_f32_16x16x32_bf16 v[60:63], v[92:95], a[108:111], v[60:63]\n"
    "v_cvt_pk_bf16_f32 v0, v0, v1\n"
    "v_cvt_pk_bf16_f32 v1, v2, v3\n"
    "v_mfma_f32_16x16x32_bf16 v[40:43], a[192:195], a[112:115], v[40:43]\n"
    "v_cvt_pk_bf16_f32 v2, v20, v21\n"
    "v_cvt_pk_bf16_f32 v3, v22, v23\n"
    "v_mfma_f32_16x16x32_bf16 v[60:63], a[196:199], a[112:115], v[60:63]\n"
    "v_mfma_f32_16x16x32_bf16 v[40:43], a[208:211], a[116:119], v[40:43]\n"
    "buffer_store_dwordx4 v[0:3], v239, s[16:19], s28 offen offset:0 sc0 sc1\n"
    "v_mfma_f32_16x16x32_bf16 v[60:63], a[212:215], a[116:119], v[60:63]\n"
    "v_mfma_f32_16x16x32_bf16 v[40:43], v[132:135], a[120:123], v[40:43]\n"
    "v_mfma_f32_16x16x32_bf16 v[60:63], v[116:119], a[120:123], v[60:63]\n"
    "v_mfma_f32_16x16x32_bf16 v[40:43], v[4:7], a[124:127], v[40:43]\n"
    "v_mfma_f32_16x16x32_bf16 v[60:63], v[24:27], a[124:127], v[60:63]\n"
    "v_mov_b32_e32 v254, s39\n"
    "s_nop 2\n"
    "v_fma_f32 v40, v40, v220, v212\n"
    "v_fma_f32 v41, v41, v220, v213\n"
    "v_fma_f32 v42, v42, v220, v214\n"
    "v_fma_f32 v43, v43, v220, v215\n"
    "v_fma_f32 v60, v60, v220, v216\n"
    "v_fma_f32 v61, v61, v220, v217\n"
    "v_fma_f32 v62, v62, v220, v218\n"
    "v_fma_f32 v63, v63, v220, v219\n"
    "v_cvt_pk_bf16_f32 v40, v40, v41\n"
    "v_cvt_pk_bf16_f32 v41, v42, v43\n"
    "v_cvt_pk_bf16_f32 v42, v60, v61\n"
    "v_cvt_pk_bf16_f32 v43, v62, v63\n"
    "buffer_store_dwordx4 v[40:43], v239, s[16:19], s28 offen offset:64 sc0 sc1\n"
    "buffer_store_dword v254, off, s[8:11], s45 offset:0 sc1\n"
    "s_endpgm\n"
    ".p2align 6\n"
    ".long 0xbedf00ff\n"
    ".Lat_w14:\n"
    "s_setpc_b64 s[2:3]\n"
    ".Lat_k:\n"
    "s_mov_b32 s53, 0x400000\n"
    ".Lat_kl:\n"
    "buffer_load_dword v253, v238, s[8:11], s48 offen offset:0 sc1\n"
    "s_waitcnt vmcnt(0)\n"
    "v_subrev_u32_e32 v253, s39, v253\n"
    "v_subrev_u32_e32 v253, 0, v253\n"
    "v_cmp_le_u32_e32 vcc, 1, v253\n"
    "s_sub_u32 s53, s53, 1\n"
    "s_cbranch_scc1 .Lat_kr\n"
    "s_nop 2\n"
    "s_cbranch_vccnz .Lat_kl\n"
    "s_branch .Lat_kr\n"
    ".Lat_kx:\n"
    "s_branch .Lat_kxr\n"
    ".Lat_ppoll:\n"
    "s_mov_b32 s53, 0x400000\n"
    ".Lat_ppl:\n"
    "buffer_load_dword v253, v226, s[8:11], s61 offen offset:0 sc1\n"
    "s_waitcnt vmcnt(0)\n"
    "v_subrev_u32_e32 v253, s39, v253\n"
    "v_cmp_lt_u32_e32 vcc, 1, v253\n"
    "s_sub_u32 s53, s53, 1\n"
    "s_cbranch_scc1 .Lat_pok\n"
    "s_nop 2\n"
    "s_cbranch_vccnz .Lat_ppl\n"
    "s_branch .Lat_pok\n"
    ".Lat_wpoll:\n"
    "s_mov_b32 s53, 0x400000\n"
    ".Lat_wpoll_l:\n"
    "s_sleep 1\n"
    "buffer_load_dword v253, v255, s[8:11], s52 offen offset:0 sc1\n"
    "s_waitcnt vmcnt(0)\n"
    "v_cmp_ne_u32_e32 vcc, s39, v253\n"
    "s_sub_u32 s53, s53, 1\n"
    "s_cbranch_scc1 .Lat_wpoll_ret\n"
    "s_nop 2\n"
    "s_cbranch_vccnz .Lat_wpoll_l\n"
    "s_branch .Lat_wpoll_ret\n"
    ".Lat_glate:\n"
    "s_mov_b32 s53, 0x400000\n"
    ".Lat_glate_l:\n"
    "s_sleep 2\n"
    "buffer_load_dword v188, v189, s[8:11], s81 offen offset:0 sc1\n"
    "s_waitcnt vmcnt(0)\n"
    "v_cmp_ne_u32_e32 vcc, s39, v188\n"
    "s_sub_u32 s53, s53, 1\n"
    "s_cbranch_scc1 .Lat_glate_ret\n"
    "s_nop 2\n"
    "s_cbranch_vccnz .Lat_glate_l\n"
    "s_branch .Lat_glate_ret\n"
    ".Lat_lp0:\n"
    "s_mov_b32 s53, 0x400000\n"
    ".Lat_lp:\n"
    "s_sleep 1\n"
    "buffer_load_dword v253, off, s[8:11], s82 sc1\n"
    "s_waitcnt vmcnt(0)\n"
    "v_readfirstlane_b32 s81, v253\n"
    "s_sub_u32 s53, s53, 1\n"
    "s_cbranch_scc1 .Lat_lpx\n"
    "s_nop 3\n"
    "s_cmp_lg_u32 s81, s39\n"
    "s_cbranch_scc1 .Lat_lp\n"
    ".Lat_lpx:\n"
    "s_mov_b32 m0, s30\n"
    "s_nop 0\n"
    "buffer_load_dwordx4 v250, s[8:11], s83 offen offset:0 sc1 lds\n"
    "s_mov_b32 m0, s42\n"
    "s_branch .Lat_g1b\n"
    ".Lat_h1poll:\n"
    "s_mov_b32 s53, 0x400000\n"
    ".Lat_h1poll_l:\n"
    "s_sleep 1\n"
    "buffer_load_dword v80, v83, s[8:11], s59 offen offset:0 sc1\n"
    "s_waitcnt vmcnt(0)\n"
    "v_cmp_ne_u32_e32 vcc, s39, v80\n"
    "s_sub_u32 s53, s53, 1\n"
    "s_cbranch_scc1 .Lat_h1poll_ret\n"
    "s_nop 2\n"
    "s_cbranch_vccnz .Lat_h1poll_l\n"
    "s_branch .Lat_h1poll_ret\n"
    ".Lat_hpoll:\n"
    "s_mov_b32 s53, 0x400000\n"
    ".Lat_hpoll_l:\n"
    "s_sleep 1\n"
    "buffer_load_dword v253, v250, s[8:11], s59 offen offset:0 sc1\n"
    "buffer_load_dword v254, v252, s[8:11], s59 offen offset:0 sc1\n"
    "s_waitcnt vmcnt(0)\n"
    "v_xor_b32_e32 v254, s39, v254\n"
    "v_xor_b32_e32 v253, s39, v253\n"
    "v_or_b32_e32 v253, v253, v254\n"
    "v_cmp_ne_u32_e32 vcc, 0, v253\n"
    "s_sub_u32 s53, s53, 1\n"
    "s_cbranch_scc1 .Lat_hpoll_ret\n"
    "s_nop 2\n"
    "s_cbranch_vccnz .Lat_hpoll_l\n"
    "s_branch .Lat_hpoll_ret\n"
    ".Lat_redo:\n"
    "s_waitcnt vmcnt(0) lgkmcnt(0)\n"
    "s_barrier\n"
    "s_mov_b32 s49, 1\n"
    "s_mov_b32 s4, 0\n"
    "s_mov_b32 s80, 0\n"
    "s_mov_b32 s82, 2.0\n"
    "v_lshrrev_b32_e32 v224, 4, v250\n"
    "s_and_b32 s50, s93, 1\n"
    "s_lshr_b32 s51, s93, 1\n"
    "v_lshrrev_b32_e32 v225, 3, v224\n"
    "v_and_b32_e32 v226, 6, v225\n"
    "v_and_b32_e32 v227, 7, v224\n"
    "v_xor_b32_e32 v226, v226, v227\n"
    "v_lshlrev_b32_e32 v226, 4, v226\n"
    "v_and_b32_e32 v227, 4, v225\n"
    "v_add_u32_e32 v227, v227, v225\n"
    "v_lshl_add_u32 v251, v227, 9, v226\n"
    "v_add_u32_e32 v252, 0x1c00, v251\n"
    "v_and_b32_e32 v225, 7, v224\n"
    "v_lshlrev_b32_e32 v227, 7, v225\n"
    "v_bfe_u32 v226, v224, 3, 1\n"
    "v_lshl_add_u32 v227, v226, 10, v227\n"
    "v_and_b32_e32 v225, 6, v224\n"
    "v_lshrrev_b32_e32 v226, 4, v224\n"
    "v_xor_b32_e32 v225, v225, v226\n"
    "v_lshl_add_u32 v240, v225, 4, v227\n"
    "v_lshrrev_b32_e32 v8, 4, v250\n"
    "v_and_b32_e32 v10, 0x70, v251\n"
    "v_bfe_u32 v9, v8, 3, 1\n"
    "v_lshl_add_u32 v248, v9, 9, v10\n"
    "v_lshrrev_b32_e32 v9, 4, v8\n"
    "v_lshl_add_u32 v248, v9, 12, v248\n"
    "s_mul_i32 s43, s50, 3\n"
    "s_xor_b32 s43, s43, s51\n"
    "s_lshl_b32 s43, s43, 15\n"
    "s_add_u32 s43, s43, 32768\n"
    "s_mul_i32 s42, s93, 5120\n"
    "s_lshl_b32 s45, s50, 8\n"
    "s_mul_i32 s40, s32, 1277952\n"
    "s_lshl_b32 s41, s51, 14\n"
    "s_add_u32 s40, s40, s41\n"
    "s_add_u32 s40, s40, s45\n"
    "s_add_u32 s41, s40, 128\n"
    "v_bfe_u32 v9, v8, 2, 3\n"
    "v_lshlrev_b32_e32 v244, 7, v9\n"
    "v_lshrrev_b32_e32 v10, 5, v8\n"
    "v_lshl_add_u32 v244, v10, 10, v244\n"
    "v_and_b32_e32 v9, 6, v9\n"
    "v_bfe_u32 v10, v8, 1, 1\n"
    "v_xor_b32_e32 v9, v9, v10\n"
    "v_lshl_add_u32 v244, v9, 4, v244\n"
    "v_and_b32_e32 v10, 1, v8\n"
    "v_lshl_add_u32 v244, v10, 3, v244\n"
    "s_lshl_b32 s45, s51, 16\n"
    "s_add_u32 s45, s45, 32768\n"
    "v_add_u32_e32 v244, s45, v244\n"
    "v_xor_b32_e32 v245, 32, v244\n"
    "v_xor_b32_e32 v246, 64, v244\n"
    "v_xor_b32_e32 v247, 0x60, v244\n"
    "s_lshl_b32 s45, s51, 15\n"
    "s_add_u32 s48, s45, 32768\n"
    "s_sub_u32 s45, 131072, s45\n"
    "v_add_u32_e32 v242, s45, v240\n"
    "v_add_u32_e32 v240, s48, v240\n"
    "v_xor_b32_e32 v241, 64, v240\n"
    "v_xor_b32_e32 v243, 64, v242\n"
    "s_xor_b32 s45, s93, 2\n"
    "s_mul_i32 s45, s45, 5120\n"
    "v_lshl_add_u32 v249, v8, 2, s45\n"
    "s_lshl_b32 s45, s93, 11\n"
    "v_add_u32_e32 v99, s45, v250\n"
    "v_add_u32_e32 v98, 11345920, v99\n"
    "v_and_b32_e32 v252, 0x30, v250\n"
    "v_add_u32_e32 v252, 0x400, v252\n"
    "s_mul_i32 s59, s32, 999\n"
    "s_andn2_b32 s59, s59, 63\n"
    "s_sub_u32 s48, 3968, s59\n"
    "v_cmp_le_u32_e32 vcc, 0x80, v250\n"
    "v_mov_b32_e32 v80, s48\n"
    "s_add_u32 s59, s59, 6076416\n"
    "v_cndmask_b32_e32 v83, v250, v80, vcc\n"
    "v_bfe_u32 v253, v250, 4, 2\n"
    "v_max_u32_e32 v253, 1, v253\n"
    "v_add_u32_e32 v253, s32, v253\n"
    "v_and_b32_e32 v253, 3, v253\n"
    "v_lshlrev_b32_e32 v253, 9, v253\n"
    "v_mov_b32_e32 v84, s39\n"
    "v_lshrrev_b32_e32 v86, 2, v250\n"
    "v_min_u32_e32 v86, 124, v86\n"
    "s_lshl2_add_u32 s58, s34, s93\n"
    "s_lshl_b32 s58, s58, 7\n"
    "s_add_u32 s58, s58, 5910528\n"
    "s_lshl_b32 s45, s50, 13\n"
    "v_add_u32_e32 v85, s45, v250\n"
    "s_lshl3_add_u32 s45, s45, 49152\n"
    "v_add_u32_e32 v255, s45, v250\n"
    "v_lshrrev_b32_e32 v105, 7, v250\n"
    "v_and_b32_e32 v104, 0x70, v251\n"
    "v_lshl_add_u32 v104, v105, 9, v104\n"
    "v_add_u32_e32 v105, 0xc00, v104\n"
    "v_add_u32_e32 v106, 0x1800, v104\n"
    "v_add_u32_e32 v107, 0x2400, v104\n"
    "s_lshl_b32 s57, s93, 7\n"
    "s_lshl4_add_u32 s56, s57, 24576\n"
    "s_add_u32 s57, s57, 5111808\n"
    "s_mul_i32 s45, s51, 32768\n"
    "s_add_u32 s45, s45, 8192\n"
    "v_subrev_u32_e32 v46, s45, v240\n"
    "v_subrev_u32_e32 v47, s45, v241\n"
    "s_mul_i32 s45, s51, 61440\n"
    "s_add_u32 s45, s45, 8192\n"
    "v_subrev_u32_e32 v4, s45, v244\n"
    "v_subrev_u32_e32 v5, s45, v245\n"
    "v_subrev_u32_e32 v6, s45, v246\n"
    "v_subrev_u32_e32 v7, s45, v247\n"
    "v_accvgpr_write_b32 a244, 0\n"
    "v_accvgpr_write_b32 a245, 0\n"
    "v_accvgpr_write_b32 a246, 0\n"
    "v_accvgpr_write_b32 a247, 0\n"
    "v_accvgpr_write_b32 a248, 0\n"
    "v_accvgpr_write_b32 a249, 0\n"
    "v_accvgpr_write_b32 a250, 0\n"
    "v_accvgpr_write_b32 a251, 0\n"
    "v_accvgpr_write_b32 a252, 0\n"
    "v_accvgpr_write_b32 a253, 0\n"
    "v_accvgpr_write_b32 a254, 0\n"
    "v_accvgpr_write_b32 a255, 0\n"
    "v_mov_b32_e32 v8, s4\n"
    "v_mov_b32_e32 v9, s4\n"
    "v_mov_b64_e32 v[10:11], v[8:9]\n"
    "v_mov_b64_e32 v[12:13], v[8:9]\n"
    "v_mov_b64_e32 v[14:15], v[8:9]\n"
    "v_mov_b64_e32 v[16:17], v[8:9]\n"
    "v_mov_b64_e32 v[18:19], v[8:9]\n"
    "v_mov_b64_e32 v[28:29], v[8:9]\n"
    "v_mov_b64_e32 v[30:31], v[8:9]\n"
    "v_mov_b64_e32 v[32:33], v[8:9]\n"
    "v_mov_b64_e32 v[34:35], v[8:9]\n"
    "v_mov_b64_e32 v[36:37], v[8:9]\n"
    "v_mov_b64_e32 v[38:39], v[8:9]\n"
    "v_mov_b64_e32 v[48:49], v[8:9]\n"
    "v_mov_b64_e32 v[50:51], v[8:9]\n"
    "v_mov_b64_e32 v[52:53], v[8:9]\n"
    "v_mov_b64_e32 v[54:55], v[8:9]\n"
    "v_mov_b64_e32 v[56:57], v[8:9]\n"
    "v_mov_b64_e32 v[58:59], v[8:9]\n"
    "v_mov_b64_e32 v[68:69], v[8:9]\n"
    "v_mov_b64_e32 v[70:71], v[8:9]\n"
    "v_mov_b64_e32 v[72:73], v[8:9]\n"
    "v_mov_b64_e32 v[74:75], v[8:9]\n"
    "v_mov_b64_e32 v[76:77], v[8:9]\n"
    "v_mov_b64_e32 v[78:79], v[8:9]\n"
    "v_mov_b64_e32 v[88:89], v[8:9]\n"
    "v_mov_b64_e32 v[90:91], v[8:9]\n"
    "v_mov_b64_e32 v[92:93], v[8:9]\n"
    "v_mov_b64_e32 v[94:95], v[8:9]\n"
    "v_mov_b64_e32 v[96:97], v[8:9]\n"
    "v_mov_b64_e32 v[98:99], v[8:9]\n"
    "v_mov_b64_e32 v[108:109], v[8:9]\n"
    "v_mov_b64_e32 v[110:111], v[8:9]\n"
    "v_mov_b64_e32 v[112:113], v[8:9]\n"
    "v_mov_b64_e32 v[114:115], v[8:9]\n"
    "v_mov_b64_e32 v[116:117], v[8:9]\n"
    "v_mov_b64_e32 v[118:119], v[8:9]\n"
    "v_mov_b64_e32 v[128:129], v[8:9]\n"
    "v_mov_b64_e32 v[130:131], v[8:9]\n"
    "v_mov_b64_e32 v[132:133], v[8:9]\n"
    "v_mov_b64_e32 v[134:135], v[8:9]\n"
    "v_mov_b64_e32 v[136:137], v[8:9]\n"
    "v_mov_b64_e32 v[138:139], v[8:9]\n"
    "s_mov_b32 m0, s56\n"
    "s_nop 0\n"
    "buffer_load_dwordx4 v104, s[8:11], s57 offen offset:0 sc1 lds\n"
    "buffer_load_dwordx4 v105, s[8:11], s57 offen offset:1024 sc1 lds\n"
    "s_add_u32 m0, s43, 0\n"
    "s_nop 0\n"
    "buffer_load_dwordx4 v104, s[8:11], s40 offen offset:0 sc1 lds\n"
    "buffer_load_dwordx4 v105, s[8:11], s40 offen offset:1024 sc1 lds\n"
    "buffer_load_dwordx4 v106, s[8:11], s40 offen offset:2048 sc1 lds\n"
    "buffer_load_dwordx4 v107, s[8:11], s40 offen offset:3072 sc1 lds\n"
    "s_add_u32 s40, s40, 0x8000\n"
    "s_add_u32 m0, s43, 4096\n"
    "s_nop 0\n"
    "buffer_load_dwordx4 v104, s[8:11], s41 offen offset:0 sc1 lds\n"
    "buffer_load_dwordx4 v105, s[8:11], s41 offen offset:1024 sc1 lds\n"
    "buffer_load_dwordx4 v106, s[8:11], s41 offen offset:2048 sc1 lds\n"
    "buffer_load_dwordx4 v107, s[8:11], s41 offen offset:3072 sc1 lds\n"
    "s_add_u32 s41, s41, 0x8000\n"
    "s_add_u32 m0, s43, 8192\n"
    "s_nop 0\n"
    "buffer_load_dwordx4 v104, s[8:11], s40 offen offset:0 sc1 lds\n"
    "buffer_load_dwordx4 v105, s[8:11], s40 offen offset:1024 sc1 lds\n"
    "buffer_load_dwordx4 v106, s[8:11], s40 offen offset:2048 sc1 lds\n"
    "buffer_load_dwordx4 v107, s[8:11], s40 offen offset:3072 sc1 lds\n"
    "s_add_u32 s40, s40, 0x8000\n"
    "s_add_u32 m0, s43, 12288\n"
    "s_nop 0\n"
    "buffer_load_dwordx4 v104, s[8:11], s41 offen offset:0 sc1 lds\n"
    "buffer_load_dwordx4 v105, s[8:11], s41 offen offset:1024 sc1 lds\n"
    "buffer_load_dwordx4 v106, s[8:11], s41 offen offset:2048 sc1 lds\n"
    "buffer_load_dwordx4 v107, s[8:11], s41 offen offset:3072 sc1 lds\n"
    "s_add_u32 s41, s41, 0x8000\n"
    "s_waitcnt vmcnt(0)\n"
    "s_branch .Lat_p2\n"
    ".Lat_maxpass:\n"
    "s_waitcnt lgkmcnt(0)\n"
    "s_mul_i32 s48, s33, 81920\n"
    "s_add_u32 s48, s48, 11345920\n"
    "s_add_u32 s45, s32, 0\n"
    "s_and_b32 s45, s45, 3\n"
    "s_lshl2_add_u32 s45, s50, s45\n"
    "s_lshl_b32 s45, s45, 13\n"
    "s_add_u32 s45, s45, s48\n"
    "s_add_u32 s46, s45, 4096\n"
    "buffer_load_dwordx4 a[0:3], v250, s[8:11], s45 offen offset:0 sc1\n"
    "buffer_load_dwordx4 a[4:7], v250, s[8:11], s45 offen offset:1024 sc1\n"
    "buffer_load_dwordx4 a[8:11], v250, s[8:11], s45 offen offset:2048 sc1\n"
    "buffer_load_dwordx4 a[12:15], v250, s[8:11], s45 offen offset:3072 sc1\n"
    "buffer_load_dwordx4 a[16:19], v250, s[8:11], s46 offen offset:0 sc1\n"
    "buffer_load_dwordx4 a[20:23], v250, s[8:11], s46 offen offset:1024 sc1\n"
    "buffer_load_dwordx4 a[24:27], v250, s[8:11], s46 offen offset:2048 sc1\n"
    "buffer_load_dwordx4 a[28:31], v250, s[8:11], s46 offen offset:3072 sc1\n"
    "s_and_b32 s45, s32, 1\n"
    "s_xor_b32 s45, s45, s50\n"
    "s_add_u32 s45, s45, 8\n"
    "s_lshl_b32 s45, s45, 13\n"
    "s_add_u32 s45, s45, s48\n"
    "s_add_u32 s46, s45, 4096\n"
    "buffer_load_dwordx4 a[32:35], v250, s[8:11], s45 offen offset:0 sc1\n"
    "buffer_load_dwordx4 a[36:39], v250, s[8:11], s45 offen offset:1024 sc1\n"
    "buffer_load_dwordx4 a[40:43], v250, s[8:11], s45 offen offset:2048 sc1\n"
    "buffer_load_dwordx4 a[44:47], v250, s[8:11], s45 offen offset:3072 sc1\n"
    "buffer_load_dwordx4 a[48:51], v250, s[8:11], s46 offen offset:0 sc1\n"
    "buffer_load_dwordx4 a[52:55], v250, s[8:11], s46 offen offset:1024 sc1\n"
    "buffer_load_dwordx4 a[56:59], v250, s[8:11], s46 offen offset:2048 sc1\n"
    "buffer_load_dwordx4 a[60:63], v250, s[8:11], s46 offen offset:3072 sc1\n"
    "s_add_u32 s45, s32, 1\n"
    "s_and_b32 s45, s45, 3\n"
    "s_lshl2_add_u32 s45, s50, s45\n"
    "s_lshl_b32 s45, s45, 13\n"
    "s_add_u32 s45, s45, s48\n"
    "s_add_u32 s46, s45, 4096\n"
    "buffer_load_dwordx4 a[64:67], v250, s[8:11], s45 offen offset:0 sc1\n"
    "buffer_load_dwordx4 a[68:71], v250, s[8:11], s45 offen offset:1024 sc1\n"
    "buffer_load_dwordx4 a[72:75], v250, s[8:11], s45 offen offset:2048 sc1\n"
    "buffer_load_dwordx4 a[76:79], v250, s[8:11], s45 offen offset:3072 sc1\n"
    "buffer_load_dwordx4 a[80:83], v250, s[8:11], s46 offen offset:0 sc1\n"
    "buffer_load_dwordx4 a[84:87], v250, s[8:11], s46 offen offset:1024 sc1\n"
    "buffer_load_dwordx4 a[88:91], v250, s[8:11], s46 offen offset:2048 sc1\n"
    "buffer_load_dwordx4 a[92:95], v250, s[8:11], s46 offen offset:3072 sc1\n"
    "s_add_u32 s45, s32, 2\n"
    "s_and_b32 s45, s45, 3\n"
    "s_lshl2_add_u32 s45, s50, s45\n"
    "s_lshl_b32 s45, s45, 13\n"
    "s_add_u32 s45, s45, s48\n"
    "s_add_u32 s46, s45, 4096\n"
    "buffer_load_dwordx4 a[96:99], v250, s[8:11], s45 offen offset:0 sc1\n"
    "buffer_load_dwordx4 a[100:103], v250, s[8:11], s45 offen offset:1024 sc1\n"
    "buffer_load_dwordx4 a[104:107], v250, s[8:11], s45 offen offset:2048 sc1\n"
    "buffer_load_dwordx4 a[108:111], v250, s[8:11], s45 offen offset:3072 sc1\n"
    "buffer_load_dwordx4 a[112:115], v250, s[8:11], s46 offen offset:0 sc1\n"
    "buffer_load_dwordx4 a[116:119], v250, s[8:11], s46 offen offset:1024 sc1\n"
    "buffer_load_dwordx4 a[120:123], v250, s[8:11], s46 offen offset:2048 sc1\n"
    "buffer_load_dwordx4 a[124:127], v250, s[8:11], s46 offen offset:3072 sc1\n"
    "s_add_u32 s45, s32, 3\n"
    "s_and_b32 s45, s45, 3\n"
    "s_lshl2_add_u32 s45, s50, s45\n"
    "s_lshl_b32 s45, s45, 13\n"
    "s_add_u32 s45, s45, s48\n"
    "s_add_u32 s46, s45, 4096\n"
    "buffer_load_dwordx4 a[128:131], v250, s[8:11], s45 offen offset:0 sc1\n"
    "buffer_load_dwordx4 a[132:135], v250, s[8:11], s45 offen offset:1024 sc1\n"
    "buffer_load_dwordx4 a[136:139], v250, s[8:11], s45 offen offset:2048 sc1\n"
    "buffer_load_dwordx4 a[140:143], v250, s[8:11], s45 offen offset:3072 sc1\n"
    "buffer_load_dwordx4 a[144:147], v250, s[8:11], s46 offen offset:0 sc1\n"
    "buffer_load_dwordx4 a[148:151], v250, s[8:11], s46 offen offset:1024 sc1\n"
    "buffer_load_dwordx4 a[152:155], v250, s[8:11], s46 offen offset:2048 sc1\n"
    "buffer_load_dwordx4 a[156:159], v250, s[8:11], s46 offen offset:3072 sc1\n"
    "s_waitcnt vmcnt(0)\n"
    "v_mov_b32_e32 v200, 0xff800000\n"
    "v_mov_b32_e32 v201, 0xff800000\n"
    "v_mov_b32_e32 v202, 0xff800000\n"
    "v_mov_b32_e32 v203, 0xff800000\n"
    "v_mov_b32_e32 v204, 0xff800000\n"
    "s_lshl_b32 s45, s51, 14\n"
    "s_lshl_b32 s46, s50, 8\n"
    "s_add_u32 s45, s45, s46\n"
    "s_add_u32 s61, s46, 5103616\n"
    "s_movk_i32 s48, 157\n"
    ".Lat_mp_loop:\n"
    "s_min_u32 s45, s45, s61\n"
    "s_add_u32 s46, s45, 128\n"
    "s_add_u32 m0, s43, 16384\n"
    "s_nop 0\n"
    "buffer_load_dwordx4 v248, s[8:11], s45 offen offset:0 sc1 lds\n"
    "buffer_load_dwordx4 v248, s[8:11], s45 offen offset:1024 sc1 lds\n"
    "buffer_load_dwordx4 v248, s[8:11], s45 offen offset:2048 sc1 lds\n"
    "buffer_load_dwordx4 v248, s[8:11], s45 offen offset:3072 sc1 lds\n"
    "s_add_u32 m0, s43, 20480\n"
    "s_nop 0\n"
    "buffer_load_dwordx4 v248, s[8:11], s46 offen offset:0 sc1 lds\n"
    "buffer_load_dwordx4 v248, s[8:11], s46 offen offset:1024 sc1 lds\n"
    "buffer_load_dwordx4 v248, s[8:11], s46 offen offset:2048 sc1 lds\n"
    "buffer_load_dwordx4 v248, s[8:11], s46 offen offset:3072 sc1 lds\n"
    "s_waitcnt vmcnt(0)\n"
    "s_barrier\n"
    "ds_read_b128 a[160:163], v240 offset:16384\n"
    "ds_read_b128 a[164:167], v241 offset:16384\n"
    "ds_read_b128 a[168:171], v240 offset:20480\n"
    "ds_read_b128 a[172:175], v241 offset:20480\n"
    "ds_read_b128 a[176:179], v242 offset:16384\n"
    "ds_read_b128 a[180:183], v243 offset:16384\n"
    "ds_read_b128 a[184:187], v242 offset:20480\n"
    "ds_read_b128 a[188:191], v243 offset:20480\n"
    "s_waitcnt lgkmcnt(0)\n"
    "v_mfma_f32_16x16x32_bf16 v[180:183], a[160:163], a[0:3], 0\n"
    "v_mfma_f32_16x16x32_bf16 v[184:187], a[160:163], a[32:35], 0\n"
    "v_mfma_f32_16x16x32_bf16 v[188:191], a[160:163], a[64:67], 0\n"
    "v_mfma_f32_16x16x32_bf16 v[192:195], a[160:163], a[96:99], 0\n"
    "v_mfma_f32_16x16x32_bf16 v[196:199], a[160:163], a[128:131], 0\n"
    "v_mfma_f32_16x16x32_bf16 v[180:183], a[164:167], a[4:7], v[180:183]\n"
    "v_mfma_f32_16x16x32_bf16 v[184:187], a[164:167], a[36:39], v[184:187]\n"
    "v_mfma_f32_16x16x32_bf16 v[188:191], a[164:167], a[68:71], v[188:191]\n"
    "v_mfma_f32_16x16x32_bf16 v[192:195], a[164:167], a[100:103], v[192:195]\n"
    "v_mfma_f32_16x16x32_bf16 v[196:199], a[164:167], a[132:135], v[196:199]\n"
    "v_mfma_f32_16x16x32_bf16 v[180:183], a[168:171], a[8:11], v[180:183]\n"
    "v_mfma_f32_16x16x32_bf16 v[184:187], a[168:171], a[40:43], v[184:187]\n"
    "v_mfma_f32_16x16x32_bf16 v[188:191], a[168:171], a[72:75], v[188:191]\n"
    "v_mfma_f32_16x16x32_bf16 v[192:195], a[168:171], a[104:107], v[192:195]\n"
    "v_mfma_f32_16x16x32_bf16 v[196:199], a[168:171], a[136:139], v[196:199]\n"
    "v_mfma_f32_16x16x32_bf16 v[180:183], a[172:175], a[12:15], v[180:183]\n"
    "v_mfma_f32_16x16x32_bf16 v[184:187], a[172:175], a[44:47], v[184:187]\n"
    "v_mfma_f32_16x16x32_bf16 v[188:191], a[172:175], a[76:79], v[188:191]\n"
    "v_mfma_f32_16x16x32_bf16 v[192:195], a[172:175], a[108:111], v[192:195]\n"
    "v_mfma_f32_16x16x32_bf16 v[196:199], a[172:175], a[140:143], v[196:199]\n"
    "v_mfma_f32_16x16x32_bf16 v[180:183], a[176:179], a[16:19], v[180:183]\n"
    "v_mfma_f32_16x16x32_bf16 v[184:187], a[176:179], a[48:51], v[184:187]\n"
    "v_mfma_f32_16x16x32_bf16 v[188:191], a[176:179], a[80:83], v[188:191]\n"
    "v_mfma_f32_16x16x32_bf16 v[192:195], a[176:179], a[112:115], v[192:195]\n"
    "v_mfma_f32_16x16x32_bf16 v[196:199], a[176:179], a[144:147], v[196:199]\n"
    "v_mfma_f32_16x16x32_bf16 v[180:183], a[180:183], a[20:23], v[180:183]\n"
    "v_mfma_f32_16x16x32_bf16 v[184:187], a[180:183], a[52:55], v[184:187]\n"
    "v_mfma_f32_16x16x32_bf16 v[188:191], a[180:183], a[84:87], v[188:191]\n"
    "v_mfma_f32_16x16x32_bf16 v[192:195], a[180:183], a[116:119], v[192:195]\n"
    "v_mfma_f32_16x16x32_bf16 v[196:199], a[180:183], a[148:151], v[196:199]\n"
    "v_mfma_f32_16x16x32_bf16 v[180:183], a[184:187], a[24:27], v[180:183]\n"
    "v_mfma_f32_16x16x32_bf16 v[184:187], a[184:187], a[56:59], v[184:187]\n"
    "v_mfma_f32_16x16x32_bf16 v[188:191], a[184:187], a[88:91], v[188:191]\n"
    "v_mfma_f32_16x16x32_bf16 v[192:195], a[184:187], a[120:123], v[192:195]\n"
    "v_mfma_f32_16x16x32_bf16 v[196:199], a[184:187], a[152:155], v[196:199]\n"
    "v_mfma_f32_16x16x32_bf16 v[180:183], a[188:191], a[28:31], v[180:183]\n"
    "v_mfma_f32_16x16x32_bf16 v[184:187], a[188:191], a[60:63], v[184:187]\n"
    "v_mfma_f32_16x16x32_bf16 v[188:191], a[188:191], a[92:95], v[188:191]\n"
    "v_mfma_f32_16x16x32_bf16 v[192:195], a[188:191], a[124:127], v[192:195]\n"
    "v_mfma_f32_16x16x32_bf16 v[196:199], a[188:191], a[156:159], v[196:199]\n"
    "s_nop 15\n"
    "v_max3_f32 v200, v200, v180, v181\n"
    "v_max3_f32 v200, v200, v182, v183\n"
    "v_max3_f32 v201, v201, v184, v185\n"
    "v_max3_f32 v201, v201, v186, v187\n"
    "v_max3_f32 v202, v202, v188, v189\n"
    "v_max3_f32 v202, v202, v190, v191\n"
    "v_max3_f32 v203, v203, v192, v193\n"
    "v_max3_f32 v203, v203, v194, v195\n"
    "v_max3_f32 v204, v204, v196, v197\n"
    "v_max3_f32 v204, v204, v198, v199\n"
    "s_nop 3\n"
    "ds_read_b128 a[160:163], v240 offset:18432\n"
    "ds_read_b128 a[164:167], v241 offset:18432\n"
    "ds_read_b128 a[168:171], v240 offset:22528\n"
    "ds_read_b128 a[172:175], v241 offset:22528\n"
    "ds_read_b128 a[176:179], v242 offset:18432\n"
    "ds_read_b128 a[180:183], v243 offset:18432\n"
    "ds_read_b128 a[184:187], v242 offset:22528\n"
    "ds_read_b128 a[188:191], v243 offset:22528\n"
    "s_waitcnt lgkmcnt(0)\n"
    "v_mfma_f32_16x16x32_bf16 v[180:183], a[160:163], a[0:3], 0\n"
    "v_mfma_f32_16x16x32_bf16 v[184:187], a[160:163], a[32:35], 0\n"
    "v_mfma_f32_16x16x32_bf16 v[188:191], a[160:163], a[64:67], 0\n"
    "v_mfma_f32_16x16x32_bf16 v[192:195], a[160:163], a[96:99], 0\n"
    "v_mfma_f32_16x16x32_bf16 v[196:199], a[160:163], a[128:131], 0\n"
    "v_mfma_f32_16x16x32_bf16 v[180:183], a[164:167], a[4:7], v[180:183]\n"
    "v_mfma_f32_16x16x32_bf16 v[184:187], a[164:167], a[36:39], v[184:187]\n"
    "v_mfma_f32_16x16x32_bf16 v[188:191], a[164:167], a[68:71], v[188:191]\n"
    "v_mfma_f32_16x16x32_bf16 v[192:195], a[164:167], a[100:103], v[192:195]\n"
    "v_mfma_f32_16x16x32_bf16 v[196:199], a[164:167], a[132:135], v[196:199]\n"
    "v_mfma_f32_16x16x32_bf16 v[180:183], a[168:171], a[8:11], v[180:183]\n"
    "v_mfma_f32_16x16x32_bf16 v[184:187], a[168:171], a[40:43], v[184:187]\n"
    "v_mfma_f32_16x16x32_bf16 v[188:191], a[168:171], a[72:75], v[188:191]\n"
    "v_mfma_f32_16x16x32_bf16 v[192:195], a[168:171], a[104:107], v[192:195]\n"
    "v_mfma_f32_16x16x32_bf16 v[196:199], a[168:171], a[136:139], v[196:199]\n"
    "v_mfma_f32_16x16x32_bf16 v[180:183], a[172:175], a[12:15], v[180:183]\n"
    "v_mfma_f32_16x16x32_bf16 v[184:187], a[172:175], a[44:47], v[184:187]\n"
    "v_mfma_f32_16x16x32_bf16 v[188:191], a[172:175], a[76:79], v[188:191]\n"
    "v_mfma_f32_16x16x32_bf16 v[192:195], a[172:175], a[108:111], v[192:195]\n"
    "v_mfma_f32_16x16x32_bf16 v[196:199], a[172:175], a[140:143], v[196:199]\n"
    "v_mfma_f32_16x16x32_bf16 v[180:183], a[176:179], a[16:19], v[180:183]\n"
    "v_mfma_f32_16x16x32_bf16 v[184:187], a[176:179], a[48:51], v[184:187]\n"
    "v_mfma_f32_16x16x32_bf16 v[188:191], a[176:179], a[80:83], v[188:191]\n"
    "v_mfma_f32_16x16x32_bf16 v[192:195], a[176:179], a[112:115], v[192:195]\n"
    "v_mfma_f32_16x16x32_bf16 v[196:199], a[176:179], a[144:147], v[196:199]\n"
    "v_mfma_f32_16x16x32_bf16 v[180:183], a[180:183], a[20:23], v[180:183]\n"
    "v_mfma_f32_16x16x32_bf16 v[184:187], a[180:183], a[52:55], v[184:187]\n"
    "v_mfma_f32_16x16x32_bf16 v[188:191], a[180:183], a[84:87], v[188:191]\n"
    "v_mfma_f32_16x16x32_bf16 v[192:195], a[180:183], a[116:119], v[192:195]\n"
    "v_mfma_f32_16x16x32_bf16 v[196:199], a[180:183], a[148:151], v[196:199]\n"
    "v_mfma_f32_16x16x32_bf16 v[180:183], a[184:187], a[24:27], v[180:183]\n"
    "v_mfma_f32_16x16x32_bf16 v[184:187], a[184:187], a[56:59], v[184:187]\n"
    "v_mfma_f32_16x16x32_bf16 v[188:191], a[184:187], a[88:91], v[188:191]\n"
    "v_mfma_f32_16x16x32_bf16 v[192:195], a[184:187], a[120:123], v[192:195]\n"
    "v_mfma_f32_16x16x32_bf16 v[196:199], a[184:187], a[152:155], v[196:199]\n"
    "v_mfma_f32_16x16x32_bf16 v[180:183], a[188:191], a[28:31], v[180:183]\n"
    "v_mfma_f32_16x16x32_bf16 v[184:187], a[188:191], a[60:63], v[184:187]\n"
    "v_mfma_f32_16x16x32_bf16 v[188:191], a[188:191], a[92:95], v[188:191]\n"
    "v_mfma_f32_16x16x32_bf16 v[192:195], a[188:191], a[124:127], v[192:195]\n"
    "v_mfma_f32_16x16x32_bf16 v[196:199], a[188:191], a[156:159], v[196:199]\n"
    "s_nop 15\n"
    "v_max3_f32 v200, v200, v180, v181\n"
    "v_max3_f32 v200, v200, v182, v183\n"
    "v_max3_f32 v201, v201, v184, v185\n"
    "v_max3_f32 v201, v201, v186, v187\n"
    "v_max3_f32 v202, v202, v188, v189\n"
    "v_max3_f32 v202, v202, v190, v191\n"
    "v_max3_f32 v203, v203, v192, v193\n"
    "v_max3_f32 v203, v203, v194, v195\n"
    "v_max3_f32 v204, v204, v196, v197\n"
    "v_max3_f32 v204, v204, v198, v199\n"
    "s_nop 3\n"
    "s_barrier\n"
    "s_add_u32 s45, s45, 0x8000\n"
    "s_sub_u32 s48, s48, 1\n"
    "s_cmp_lg_u32 s48, 0\n"
    "s_cbranch_scc1 .Lat_mp_loop\n"
    "s_mov_b32 m0, s42\n"
    "s_nop 1\n"
    "ds_write_addtid_b32 v200 offset:0\n"
    "ds_write_addtid_b32 v201 offset:256\n"
    "ds_write_addtid_b32 v202 offset:512\n"
    "ds_write_addtid_b32 v203 offset:768\n"
    "ds_write_addtid_b32 v204 offset:1024\n"
    "s_waitcnt lgkmcnt(0)\n"
    "s_barrier\n"
    "ds_read_b32 v180, v249 offset:0\n"
    "ds_read_b32 v184, v249 offset:256\n"
    "ds_read_b32 v188, v249 offset:512\n"
    "ds_read_b32 v192, v249 offset:768\n"
    "ds_read_b32 v196, v249 offset:1024\n"
    "s_waitcnt lgkmcnt(0)\n"
    "v_max_f32_e32 v205, v200, v180\n"
    "v_mov_b32_e32 v206, v205\n"
    "s_nop 1\n"
    "v_permlane16_swap_b32_e32 v205, v206\n"
    "s_nop 0\n"
    "v_max_f32_e32 v205, v205, v206\n"
    "v_mov_b32_e32 v206, v205\n"
    "s_nop 1\n"
    "v_permlane32_swap_b32_e32 v205, v206\n"
    "s_nop 0\n"
    "v_max_f32_e32 v205, v205, v206\n"
    "v_add_f32_e32 v205, 0x42ae0000, v205\n"
    "v_xor_b32_e32 v220, 0x80000000, v205\n"
    "v_xor_b32_e32 v221, 0x80000000, v205\n"
    "v_xor_b32_e32 v222, 0x80000000, v205\n"
    "v_xor_b32_e32 v223, 0x80000000, v205\n"
    "v_max_f32_e32 v205, v201, v184\n"
    "v_mov_b32_e32 v206, v205\n"
    "s_nop 1\n"
    "v_permlane16_swap_b32_e32 v205, v206\n"
    "s_nop 0\n"
    "v_max_f32_e32 v205, v205, v206\n"
    "v_mov_b32_e32 v206, v205\n"
    "s_nop 1\n"
    "v_permlane32_swap_b32_e32 v205, v206\n"
    "s_nop 0\n"
    "v_max_f32_e32 v205, v205, v206\n"
    "v_add_f32_e32 v205, 0x42ae0000, v205\n"
    "v_xor_b32_e32 v224, 0x80000000, v205\n"
    "v_xor_b32_e32 v225, 0x80000000, v205\n"
    "v_xor_b32_e32 v226, 0x80000000, v205\n"
    "v_xor_b32_e32 v227, 0x80000000, v205\n"
    "v_max_f32_e32 v205, v202, v188\n"
    "v_mov_b32_e32 v206, v205\n"
    "s_nop 1\n"
    "v_permlane16_swap_b32_e32 v205, v206\n"
    "s_nop 0\n"
    "v_max_f32_e32 v205, v205, v206\n"
    "v_mov_b32_e32 v206, v205\n"
    "s_nop 1\n"
    "v_permlane32_swap_b32_e32 v205, v206\n"
    "s_nop 0\n"
    "v_max_f32_e32 v205, v205, v206\n"
    "v_add_f32_e32 v205, 0x42ae0000, v205\n"
    "v_xor_b32_e32 v228, 0x80000000, v205\n"
    "v_xor_b32_e32 v229, 0x80000000, v205\n"
    "v_xor_b32_e32 v230, 0x80000000, v205\n"
    "v_xor_b32_e32 v231, 0x80000000, v205\n"
    "v_max_f32_e32 v205, v203, v192\n"
    "v_mov_b32_e32 v206, v205\n"
    "s_nop 1\n"
    "v_permlane16_swap_b32_e32 v205, v206\n"
    "s_nop 0\n"
    "v_max_f32_e32 v205, v205, v206\n"
    "v_mov_b32_e32 v206, v205\n"
    "s_nop 1\n"
    "v_permlane32_swap_b32_e32 v205, v206\n"
    "s_nop 0\n"
    "v_max_f32_e32 v205, v205, v206\n"
    "v_add_f32_e32 v205, 0x42ae0000, v205\n"
    "v_xor_b32_e32 v232, 0x80000000, v205\n"
    "v_xor_b32_e32 v233, 0x80000000, v205\n"
    "v_xor_b32_e32 v234, 0x80000000, v205\n"
    "v_xor_b32_e32 v235, 0x80000000, v205\n"
    "v_max_f32_e32 v205, v204, v196\n"
    "v_mov_b32_e32 v206, v205\n"
    "s_nop 1\n"
    "v_permlane16_swap_b32_e32 v205, v206\n"
    "s_nop 0\n"
    "v_max_f32_e32 v205, v205, v206\n"
    "v_mov_b32_e32 v206, v205\n"
    "s_nop 1\n"
    "v_permlane32_swap_b32_e32 v205, v206\n"
    "s_nop 0\n"
    "v_max_f32_e32 v205, v205, v206\n"
    "v_add_f32_e32 v205, 0x42ae0000, v205\n"
    "v_xor_b32_e32 v236, 0x80000000, v205\n"
    "v_xor_b32_e32 v237, 0x80000000, v205\n"
    "v_xor_b32_e32 v238, 0x80000000, v205\n"
    "v_xor_b32_e32 v239, 0x80000000, v205\n"
    "s_nop 1\n"
    "s_barrier\n"
    "s_mul_i32 s48, s33, 81920\n"
    "s_add_u32 s48, s48, 11345920\n"
    "s_lshl_b32 s46, s93, 11\n"
    "s_add_u32 s48, s48, s46\n"
    "s_add_u32 s46, s46, 49152\n"
    "s_and_b32 s45, s32, 1\n"
    "s_xor_b32 s45, s45, 8\n"
    "s_lshl_b32 s45, s45, 13\n"
    "s_add_u32 s45, s45, s48\n"
    "s_add_u32 m0, s46, 0\n"
    "s_nop 0\n"
    "buffer_load_dwordx4 v250, s[8:11], s45 offen offset:0 sc1 lds\n"
    "buffer_load_dwordx4 v250, s[8:11], s45 offen offset:1024 sc1 lds\n"
    "s_branch .Lat_maxpass_ret\n"
    ".p2align 12\n"
    ".Lat_out:\n"
    "s_waitcnt lgkmcnt(0)\n"
    "s_mov_b64 s[2:3], s[0:1]\n"
    "s_mov_b64 %0, s[2:3]\n"
    "s_mov_b32 %1, s92\n"
    "s_mov_b32 %2, s93\n"
    : "=s"(ka), "=s"(b), "=s"(wid) : "0"(__builtin_amdgcn_kernarg_segment_ptr()), "1"(blockIdx.x)
    : "v0", "v1", "v2", "v3", "v4", "v5", "v6", "v7", "v8", "v9", "v10", "v11", "v12", "v13", "v14", "v15", "v16", "v17", "v18", "v19", "v20", "v21", "v22", "v23", "v24", "v25", "v26", "v27", "v28", "v29", "v30", "v31", "v32", "v33", "v34", "v35", "v36", "v37", "v38", "v39", "v40", "v41", "v42", "v43", "v44", "v45", "v46", "v47", "v48", "v49", "v50", "v51", "v52", "v53", "v54", "v55", "v56", "v57", "v58", "v59", "v60", "v61", "v62", "v63", "v64", "v65", "v66", "v67", "v68", "v69", "v70", "v71", "v72", "v73", "v74", "v75", "v76", "v77", "v78", "v79", "v80", "v81", "v82", "v83", "v84", "v85", "v86", "v87", "v88", "v89", "v90", "v91", "v92", "v93", "v94", "v95", "v96", "v97", "v98", "v99", "v100", "v101", "v102", "v103", "v104", "v105", "v106", "v107", "v108", "v109", "v110", "v111", "v112", "v113", "v114", "v115", "v116", "v117", "v118", "v119", "v120", "v121", "v122", "v123", "v124", "v125", "v126", "v127", "v128", "v129", "v130", "v131", "v132", "v133", "v134", "v135", "v136", "v137", "v138", "v139", "v140", "v141", "v142", "v143", "v144", "v145", "v146", "v147", "v148", "v149", "v150", "v151", "v152", "v153", "v154", "v155", "v156", "v157", "v158", "v159", "v160", "v161", "v162", "v163", "v164", "v165", "v166", "v167", "v168", "v169", "v170", "v171", "v172", "v173", "v174", "v175", "v176", "v177", "v178", "v179", "v180", "v181", "v182", "v183", "v184", "v185", "v186", "v187", "v188", "v189", "v190", "v191", "v192", "v193", "v194", "v195", "v196", "v197", "v198", "v199", "v200", "v201", "v202", "v203", "v204", "v205", "v206", "v207", "v208", "v209", "v210", "v211", "v212", "v213", "v214", "v215", "v216", "v217", "v218", "v219", "v220", "v221", "v222", "v223", "v224", "v225", "v226", "v227", "v228", "v229", "v230", "v231", "v232", "v233", "v234", "v235", "v236", "v237", "v238", "v239", "v240", "v241", "v242", "v243", "v244", "v245", "v246", "v247", "v248", "v249", "v250", "v251", "v252", "v253", "v254", "v255", "a0", "a1", "a2", "a3", "a4", "a5", "a6", "a7", "a8", "a9", "a10", "a11", "a12", "a13", "a14", "a15", "a16", "a17", "a18", "a19", "a20", "a21", "a22", "a23", "a24", "a25", "a26", "a27", "a28", "a29", "a30", "a31", "a32", "a33", "a34", "a35", "a36", "a37", "a38", "a39", "a40", "a41", "a42", "a43", "a44", "a45", "a46", "a47", "a48", "a49", "a50", "a51", "a52", "a53", "a54", "a55", "a56", "a57", "a58", "a59", "a60", "a61", "a62", "a63", "a64", "a65", "a66", "a67", "a68", "a69", "a70", "a71", "a72", "a73", "a74", "a75", "a76", "a77", "a78", "a79", "a80", "a81", "a82", "a83", "a84", "a85", "a86", "a87", "a88", "a89", "a90", "a91", "a92", "a93", "a94", "a95", "a96", "a97", "a98", "a99", "a100", "a101", "a102", "a103", "a104", "a105", "a106", "a107", "a108", "a109", "a110", "a111", "a112", "a113", "a114", "a115", "a116", "a117", "a118", "a119", "a120", "a121", "a122", "a123", "a124", "a125", "a126", "a127", "a128", "a129", "a130", "a131", "a132", "a133", "a134", "a135", "a136", "a137", "a138", "a139", "a140", "a141", "a142", "a143", "a144", "a145", "a146", "a147", "a148", "a149", "a150", "a151", "a152", "a153", "a154", "a155", "a156", "a157", "a158", "a159", "a160", "a161", "a162", "a163", "a164", "a165", "a166", "a167", "a168", "a169", "a170", "a171", "a172", "a173", "a174", "a175", "a176", "a177", "a178", "a179", "a180", "a181", "a182", "a183", "a184", "a185", "a186", "a187", "a188", "a189", "a190", "a191", "a192", "a193", "a194", "a195", "a196", "a197", "a198", "a199", "a200", "a201", "a202", "a203", "a204", "a205", "a206", "a207", "a208", "a209", "a210", "a211", "a212", "a213", "a214", "a215", "a216", "a217", "a218", "a219", "a220", "a221", "a222", "a223", "a224", "a225", "a226", "a227", "a228", "a229", "a230", "a231", "a232", "a233", "a234", "a235", "a236", "a237", "a238", "a239", "a240", "a241", "a242", "a243", "a244", "a245", "a246", "a247", "a248", "a249", "a250", "a251", "a252", "a253", "a254", "a255", "s2", "s3", "s4", "s5", "s6", "s7", "s8", "s9", "s10", "s11", "s12", "s13", "s14", "s15", "s16", "s17", "s18", "s19", "s20", "s21", "s22", "s23", "s24", "s25", "s26", "s27", "s28", "s29", "s30", "s31", "s32", "s33", "s34", "s35", "s36", "s37", "s38", "s39", "s40", "s41", "s42", "s43", "s44", "s45", "s46", "s47", "s48", "s49", "s50", "s51", "s52", "s53", "s54", "s55", "s56", "s57", "s58", "s59", "s60", "s61", "s62", "s63", "s80", "s81", "s82", "s83", "s84", "s85", "s86", "s87", "s88", "s89", "s90", "s91", "s92", "s93", "s94", "s95", "vcc", "m0", "scc", "memory");
  typedef __attribute__((address_space(1))) void* gp;
  helper(lds, b >> 1 & 3, wid, (const int*)(gp)ka[5], (const int*)(gp)ka[3], (const u16*)(gp)ka[8], (const u16*)(gp)ka[9], (const u16*)(gp)ka[10], (const u16*)(gp)ka[7], (char*)(gp)ka[2]);
}

extern "C" void kernel_launch(void* const* d_in, const int* in_sizes, int n_in, void* d_out, int out_size, void* d_ws,
                              size_t ws_size, hipStream_t stream) {
  const u16* x = (const u16*)d_in[0];
  const int* src = (const int*)d_in[1];
  const int* dst = (const int*)d_in[2];
  const u16* deg_inv = (const u16*)d_in[3];
  const u16* wmsg = (const u16*)d_in[4];
  const u16* wq = (const u16*)d_in[5];
  const u16* wk = (const u16*)d_in[6];
  const u16* wv = (const u16*)d_in[7];
  const u16* wout = (const u16*)d_in[8];
  char* ws = (char*)d_ws;
  hipFuncSetAttribute((const void*)gps_kernel, hipFuncAttributeMaxDynamicSharedMemorySize, 163840);
  gps_kernel<<<256, 256, 163840, stream>>>(wq, wk, ws, dst, x, src, (u16*)d_out, deg_inv, wv, wmsg, wout);
}
```
